```python
import torch

def _softmax(x, axis=-1):
    return torch.softmax(x, dim=axis)
def _gelu(x):
    return torch.nn.functional.gelu(x, approximate="tanh")
def _silu(x):
    return torch.nn.functional.silu(x)
def _sigmoid(x):
    return torch.sigmoid(x)
def _layernorm(x, axis=-1, eps=1e-5):
    m = x.mean(dim=axis, keepdim=True)
    v = x.var(dim=axis, keepdim=True, unbiased=False)
    return (x - m) / torch.sqrt(v + eps)
def _scatter_add(tgt, idx, src, axis=0):
    return tgt.clone().index_add_(axis, idx, src)
def _segment_sum(src, seg, n):
    out = torch.zeros((n,) + src.shape[1:], dtype=src.dtype)
    return out.index_add_(0, seg, src)
def _pad(x, pw):
    flat = tuple(v for pair in reversed(pw) for v in pair)
    return torch.nn.functional.pad(x, flat)

kernel_name = 'interleave_layers__sliding_block__moe_arch_mod__prompt'

def setup_inputs():

    _w = (lambda a: (lambda t: t.to(torch.bfloat16) if t.is_floating_point() else t)(torch.as_tensor(a)))
    def _A_setup():
        B, S, D = 1, 2048, 1024
        return {
            "x": _w((lambda *s: torch.randn(*s, dtype=torch.bfloat16) * 0.05)(B, S, D)),
            "wqkv": _w((lambda *s: torch.randn(*s, dtype=torch.bfloat16) * 0.05)(D, 3 * D)),
            "wo": _w((lambda *s: torch.randn(*s, dtype=torch.bfloat16) * 0.05)(D, D)),
        }
    _A_in = _A_setup()

    def _B_setup():
        B, S, D, F, C = 1, 2048, 1024, 3584, 1
        return {
            "x": _w((lambda *s: torch.randn(*s, dtype=torch.bfloat16) * 0.05)(B, S, D)),
            "w_r": _w((lambda *s: torch.randn(*s, dtype=torch.bfloat16) * 1.6)(D)),
            "w_gate": _w((lambda *s: torch.randn(*s, dtype=torch.bfloat16) * 0.1)(D, F)),
            "w_up": _w((lambda *s: torch.randn(*s, dtype=torch.bfloat16) * 0.1)(D, F)),
            "w_down": _w((lambda *s: torch.randn(*s, dtype=torch.bfloat16) * 0.1)(F, D)),
        }
    _B_in = _B_setup()

    return {
        "A_x": _A_in["x"],
        "A_wqkv": _A_in["wqkv"],
        "A_wo": _A_in["wo"],
        "B_x": _B_in["x"],
        "B_w_r": _B_in["w_r"],
        "B_w_gate": _B_in["w_gate"],
        "B_w_up": _B_in["w_up"],
        "B_w_down": _B_in["w_down"],
    }

def reference(A_x, A_wqkv, A_wo, B_x, B_w_r, B_w_gate, B_w_up, B_w_down):
    def _A_ref(x, wqkv, wo):
        B, S, D = x.shape
        H = 32; Dh = D // H; W = 256
        qkv = (lambda x, *d: x.permute(*d))((x @ wqkv).reshape(B, S, 3, H, Dh), 2, 0, 3, 1, 4)
        q, k, v = qkv[0], qkv[1], qkv[2]
        scores = torch.einsum("bhqd,bhkd->bhqk", q, k) * (Dh ** -0.5)
        idx = torch.arange(S)
        mask = (idx[:, None] - idx[None, :] >= 0) & (idx[:, None] - idx[None, :] < W)
        scores = torch.where(mask[None, None], scores, -1e9)
        p = _softmax(scores, axis=-1)
        o = torch.einsum("bhqk,bhkd->bhqd", p, v)
        return (lambda x, *d: x.permute(*d))(o, 0, 2, 1, 3).reshape(B, S, D) @ wo

    def _B_ref(x, w_r, w_gate, w_up, w_down):
        B, S, D = x.shape
        C = 1
        r = x @ w_r
        idx = torch.argsort(-r, axis=-1)[:, :C]
        idx_e = (lambda x, n, axis: torch.repeat_interleave(x, n, dim=axis))(idx.reshape(B, C, 1), D, axis=2)
        x_sel = (lambda x, i, axis: torch.take_along_dim(x, i, dim=axis))(x, idx_e, axis=1)
        g = _silu(x_sel @ w_gate)
        u = x_sel @ w_up
        y = (g * u) @ w_down
        r_sel = _sigmoid((lambda x, i, axis: torch.take_along_dim(x, i, dim=axis))(r, idx, axis=1))
        y = y * r_sel.reshape(B, C, 1)
        b_off = (torch.arange(B) * S).reshape(B, 1)
        flat_idx = (idx + b_off).reshape(B * C)
        zero = (x * 0.0).reshape(B * S, D)
        upd = _scatter_add(zero, flat_idx, y.reshape(B * C, D), axis=0)
        return x + upd.reshape(B, S, D)

    x = A_x
    x = x + _A_ref(_layernorm(x, axis=-1), A_wqkv, A_wo)
    x = x + _B_ref(_layernorm(x, axis=-1), B_w_r, B_w_gate, B_w_up, B_w_down)
    x = x + _A_ref(_layernorm(x, axis=-1), A_wqkv, A_wo)
    x = x + _B_ref(_layernorm(x, axis=-1), B_w_r, B_w_gate, B_w_up, B_w_down)
    return x
```

```cpp
#define TCH 10,4,0x3F,26,0xC0,4,1
#include <hip/hip_runtime.h>
#include <stdint.h>
#include <utility>

typedef __bf16 bf16;
typedef __bf16 bf16x8 __attribute__((ext_vector_type(8)));
typedef __bf16 bf16x2 __attribute__((ext_vector_type(2)));
typedef float f32x4 __attribute__((ext_vector_type(4)));
typedef float f32x2 __attribute__((ext_vector_type(2)));
typedef short s16x4 __attribute__((ext_vector_type(4)));
typedef unsigned int u32x4 __attribute__((ext_vector_type(4)));
typedef unsigned int u32x2 __attribute__((ext_vector_type(2)));
#define LDS_AS __attribute__((address_space(3)))
#define GAS __attribute__((address_space(1)))
typedef GAS unsigned gu32;
typedef GAS unsigned long long gu64;
#define RLX_AGENT __ATOMIC_RELAXED, __HIP_MEMORY_SCOPE_AGENT
typedef __amdgpu_buffer_rsrc_t rsrc_t;

constexpr int F_ = 3584;
constexpr int SC1 = 16;

constexpr int RX = 4;
constexpr unsigned F_EP = 0, F_TMO = 128;
constexpr unsigned F_XN = 4096;
constexpr unsigned F_X1 = 196608;
constexpr unsigned F_O = 32768;
constexpr unsigned F_KV = 65536;
static_assert(F_XN + 8 * RX * 256 <= F_O && F_O + 2 * 64 * 256 <= F_KV, "flag block");
constexpr unsigned OFF_KEY = 131072;
constexpr unsigned OFF_AB = 2u << 20;
constexpr unsigned OFF_XNEW = 262144;
constexpr unsigned OFF_CS = OFF_AB + 65536;
constexpr unsigned OFF_FX = 393216;
constexpr unsigned OFF_CA = 524288;
constexpr unsigned OFF_CB = 655360;
constexpr unsigned OFF_STAT = 1u << 20;
constexpr unsigned OFF_PART = 3u << 20;
constexpr unsigned OFF_STAMP = 6u << 20;
constexpr unsigned OFF_XN = 8u << 20;
constexpr unsigned OFF_O = 16u << 20;
constexpr unsigned OFF_KV = 32u << 20;
constexpr unsigned OFF_WI = 52u << 20;
constexpr unsigned WS_BYTES = 58u << 20;
constexpr unsigned POISON = 0xFFFFFFFFu;

constexpr int L_ABW = 16640, L_CSP = 17408, L_CSL = 31872, L_FXL = 32256, L_K = 0, L_V = 32768, L_RING = 65536, L_XCH = 0, L_DN = 32768, L_RDY = 31744, L_QC = 31808, LDS_BYTES = 163840;
constexpr int STAGE = 24576;
#define SLOT(t) ((t) & 3)

template <typename F, int... I> __device__ __forceinline__ void static_for_impl(F&& f, std::integer_sequence<int, I...>) {
  (f(std::integral_constant<int, I>{}), ...);
}
template <int N, typename F> __device__ __forceinline__ void static_for(F&& f) { static_for_impl(f, std::make_integer_sequence<int, N>{}); }
__device__ __forceinline__ unsigned pack2(float a, float b) {
  f32x2 v = {a, b};
  return __builtin_bit_cast(unsigned, __builtin_convertvector(v, bf16x2));
}
__device__ __forceinline__ float asf(unsigned u) { return __builtin_bit_cast(float, u); }
__device__ __forceinline__ float lo16(unsigned u) { return __builtin_bit_cast(float, u << 16); }
__device__ __forceinline__ float hi16(unsigned u) { return __builtin_bit_cast(float, u & 0xffff0000u); }
__device__ __forceinline__ u32x4 pack8(const f32x4 a, const f32x4 b) {
  u32x4 o;
  o[0] = pack2(a[0], a[1]); o[1] = pack2(a[2], a[3]); o[2] = pack2(b[0], b[1]); o[3] = pack2(b[2], b[3]);
  return o;
}
__device__ __forceinline__ f32x4 mfma16(u32x4 a, u32x4 b, f32x4 c) {
  return __builtin_amdgcn_mfma_f32_16x16x32_bf16(__builtin_bit_cast(bf16x8, a), __builtin_bit_cast(bf16x8, b), c, 0, 0, 0);
}
__device__ __forceinline__ f32x4 mfma16k16(s16x4 a, s16x4 b, f32x4 c) { return __builtin_amdgcn_mfma_f32_16x16x16bf16_1k(a, b, c, 0, 0, 0); }
template <int CTRL> __device__ __forceinline__ float dppf(float v) {
  return __builtin_bit_cast(float, __builtin_amdgcn_update_dpp(0, __builtin_bit_cast(int, v), CTRL, 0xf, 0xf, false));
}
template <int CTRL> __device__ __forceinline__ unsigned dppu(unsigned v) {
  return (unsigned)__builtin_amdgcn_update_dpp(0, (int)v, CTRL, 0xf, 0xf, false);
}
__device__ __forceinline__ float add_x16(float v) {
  auto r = __builtin_amdgcn_permlane16_swap(__builtin_bit_cast(unsigned, v), __builtin_bit_cast(unsigned, v), false, false);
  return __builtin_bit_cast(float, (unsigned)r[0]) + __builtin_bit_cast(float, (unsigned)r[1]);
}
__device__ __forceinline__ float add_x32(float v) {
  auto r = __builtin_amdgcn_permlane32_swap(__builtin_bit_cast(unsigned, v), __builtin_bit_cast(unsigned, v), false, false);
  return __builtin_bit_cast(float, (unsigned)r[0]) + __builtin_bit_cast(float, (unsigned)r[1]);
}
__device__ __forceinline__ float max_x16(float v) {
  auto r = __builtin_amdgcn_permlane16_swap(__builtin_bit_cast(unsigned, v), __builtin_bit_cast(unsigned, v), false, false);
  return fmaxf(__builtin_bit_cast(float, (unsigned)r[0]), __builtin_bit_cast(float, (unsigned)r[1]));
}
__device__ __forceinline__ float max_x32(float v) {
  auto r = __builtin_amdgcn_permlane32_swap(__builtin_bit_cast(unsigned, v), __builtin_bit_cast(unsigned, v), false, false);
  return fmaxf(__builtin_bit_cast(float, (unsigned)r[0]), __builtin_bit_cast(float, (unsigned)r[1]));
}
__device__ __forceinline__ unsigned umax_x16(unsigned v) {
  auto r = __builtin_amdgcn_permlane16_swap(v, v, false, false);
  return max((unsigned)r[0], (unsigned)r[1]);
}
__device__ __forceinline__ unsigned umax_x32(unsigned v) {
  auto r = __builtin_amdgcn_permlane32_swap(v, v, false, false);
  return max((unsigned)r[0], (unsigned)r[1]);
}
__device__ __forceinline__ float sum_hi8(float v) {
  v += dppf<0x128>(v);
  v = add_x16(v);
  return add_x32(v);
}
__device__ __forceinline__ float sum64(float v) {
  v += dppf<0xB1>(v);
  v += dppf<0x4E>(v);
  v += dppf<0x124>(v);
  v += dppf<0x128>(v);
  v = add_x16(v);
  return add_x32(v);
}

#define BAR() asm volatile("s_waitcnt lgkmcnt(0)\n\ts_barrier" ::: "memory")
#define DRAIN() asm volatile("s_waitcnt vmcnt(0)" ::: "memory")
#define SB() __builtin_amdgcn_sched_barrier(0)
template <int N> __device__ __forceinline__ void wait_vm() { asm volatile("s_waitcnt vmcnt(%0)" ::"n"(N) : "memory"); }

__device__ __forceinline__ void dma16(rsrc_t rs, unsigned voff, unsigned soff, unsigned lds) {
  asm volatile("s_mov_b32 m0, %0\n\ts_nop 0\n\tbuffer_load_dwordx4 %1, %2, %3 offen lds" ::"s"(lds), "v"(voff), "s"(rs), "s"(soff) : "memory");
}
template <int OFF> __device__ __forceinline__ void dma16s(rsrc_t rs, unsigned voff, unsigned soff, unsigned lds) {
  asm volatile("s_mov_b32 m0, %0\n\ts_nop 0\n\tbuffer_load_dwordx4 %1, %2, %3 offen offset:%4 sc1 lds" ::"s"(lds), "v"(voff), "s"(rs), "s"(soff), "n"(OFF) : "memory");
}
__device__ __forceinline__ float rbf1(float x) { return __builtin_bit_cast(float, pack2(0.f, x)); }
__device__ __forceinline__ void vswap(unsigned& a, unsigned& b) { asm volatile("v_swap_b32 %0, %1" : "+v"(a), "+v"(b)); }
__device__ __forceinline__ void vswap(float& a, float& b) { asm volatile("v_swap_b32 %0, %1" : "+v"(a), "+v"(b)); }
template <typename V, int N> __device__ __forceinline__ void vswapn(V& a, V& b) {
#pragma unroll
  for (int i = 0; i < N; ++i) { auto x = a[i], y = b[i]; vswap(x, y); a[i] = x; b[i] = y; }
}
__device__ __forceinline__ u32x4 ld16(rsrc_t rs, unsigned voff, unsigned soff) { return __builtin_amdgcn_raw_buffer_load_b128(rs, voff, soff, SC1); }
template <int OFF> __device__ __forceinline__ void ld16h(u32x4& d, rsrc_t rs, unsigned voff, unsigned soff) {
  asm volatile("buffer_load_dwordx4 %0, %1, %2, %3 offen offset:%4 sc1" : "=v"(d) : "v"(voff), "s"(rs), "s"(soff), "n"(OFF) : "memory");
}
template <int I, int OFF> __device__ __forceinline__ void ld16w(u32x4& d, rsrc_t rs, unsigned voff, unsigned soff) {
  if constexpr (I == 0) asm volatile("buffer_load_dwordx4 %0, %1, %2, %3 offen offset:%4 sc1" : "={v[128:131]}"(d) : "v"(voff), "s"(rs), "s"(soff), "n"(OFF) : "memory");
  else if constexpr (I == 1) asm volatile("buffer_load_dwordx4 %0, %1, %2, %3 offen offset:%4 sc1" : "={v[132:135]}"(d) : "v"(voff), "s"(rs), "s"(soff), "n"(OFF) : "memory");
  else if constexpr (I == 2) asm volatile("buffer_load_dwordx4 %0, %1, %2, %3 offen offset:%4 sc1" : "={v[136:139]}"(d) : "v"(voff), "s"(rs), "s"(soff), "n"(OFF) : "memory");
  else if constexpr (I == 3) asm volatile("buffer_load_dwordx4 %0, %1, %2, %3 offen offset:%4 sc1" : "={v[140:143]}"(d) : "v"(voff), "s"(rs), "s"(soff), "n"(OFF) : "memory");
  else if constexpr (I == 4) asm volatile("buffer_load_dwordx4 %0, %1, %2, %3 offen offset:%4 sc1" : "={v[144:147]}"(d) : "v"(voff), "s"(rs), "s"(soff), "n"(OFF) : "memory");
  else if constexpr (I == 5) asm volatile("buffer_load_dwordx4 %0, %1, %2, %3 offen offset:%4 sc1" : "={v[148:151]}"(d) : "v"(voff), "s"(rs), "s"(soff), "n"(OFF) : "memory");
  else if constexpr (I == 6) asm volatile("buffer_load_dwordx4 %0, %1, %2, %3 offen offset:%4 sc1" : "={v[152:155]}"(d) : "v"(voff), "s"(rs), "s"(soff), "n"(OFF) : "memory");
  else if constexpr (I == 7) asm volatile("buffer_load_dwordx4 %0, %1, %2, %3 offen offset:%4 sc1" : "={v[156:159]}"(d) : "v"(voff), "s"(rs), "s"(soff), "n"(OFF) : "memory");
  else if constexpr (I == 8) asm volatile("buffer_load_dwordx4 %0, %1, %2, %3 offen offset:%4 sc1" : "={v[160:163]}"(d) : "v"(voff), "s"(rs), "s"(soff), "n"(OFF) : "memory");
  else if constexpr (I == 9) asm volatile("buffer_load_dwordx4 %0, %1, %2, %3 offen offset:%4 sc1" : "={v[164:167]}"(d) : "v"(voff), "s"(rs), "s"(soff), "n"(OFF) : "memory");
  else if constexpr (I == 10) asm volatile("buffer_load_dwordx4 %0, %1, %2, %3 offen offset:%4 sc1" : "={v[168:171]}"(d) : "v"(voff), "s"(rs), "s"(soff), "n"(OFF) : "memory");
  else if constexpr (I == 11) asm volatile("buffer_load_dwordx4 %0, %1, %2, %3 offen offset:%4 sc1" : "={v[172:175]}"(d) : "v"(voff), "s"(rs), "s"(soff), "n"(OFF) : "memory");
}
template <int K> __device__ __forceinline__ void pin_w(u32x4& a, u32x4& b) {
  if constexpr (K == 0) asm volatile("" : "+{v[128:131]}"(a), "+{v[132:135]}"(b)::"memory");
  else if constexpr (K == 1) asm volatile("" : "+{v[136:139]}"(a), "+{v[140:143]}"(b)::"memory");
  else if constexpr (K == 2) asm volatile("" : "+{v[144:147]}"(a), "+{v[148:151]}"(b)::"memory");
  else if constexpr (K == 3) asm volatile("" : "+{v[152:155]}"(a), "+{v[156:159]}"(b)::"memory");
  else if constexpr (K == 4) asm volatile("" : "+{v[160:163]}"(a), "+{v[164:167]}"(b)::"memory");
  else if constexpr (K == 5) asm volatile("" : "+{v[168:171]}"(a), "+{v[172:175]}"(b)::"memory");
}
__device__ __forceinline__ void ld8h(u32x2& d, rsrc_t rs, unsigned voff, unsigned soff) {
  asm volatile("buffer_load_dwordx2 %0, %1, %2, %3 offen sc1" : "=v"(d) : "v"(voff), "s"(rs), "s"(soff) : "memory");
}
template <int OFF> __device__ __forceinline__ void ld4h(unsigned& d, rsrc_t rs, unsigned voff, unsigned soff) {
  asm volatile("buffer_load_dword %0, %1, %2, %3 offen offset:%4 sc1" : "=v"(d) : "v"(voff), "s"(rs), "s"(soff), "n"(OFF) : "memory");
}
__device__ __forceinline__ u32x2 ld8(rsrc_t rs, unsigned voff, unsigned soff) { return __builtin_amdgcn_raw_buffer_load_b64(rs, voff, soff, SC1); }
__device__ __forceinline__ void st16(u32x4 v, rsrc_t rs, unsigned voff, unsigned soff) { __builtin_amdgcn_raw_buffer_store_b128(v, rs, voff + soff, 0, SC1); }
__device__ __forceinline__ void st8(u32x2 v, rsrc_t rs, unsigned voff, unsigned soff) { __builtin_amdgcn_raw_buffer_store_b64(v, rs, voff, soff, SC1); }

__device__ __forceinline__ unsigned ld4(rsrc_t rs, unsigned voff, unsigned soff) { return __builtin_amdgcn_raw_buffer_load_b32(rs, voff, soff, SC1); }
__device__ __forceinline__ void st4(unsigned v, rsrc_t rs, unsigned voff, unsigned soff) { __builtin_amdgcn_raw_buffer_store_b32(v, rs, voff, soff, SC1); }

constexpr unsigned kSpinCap = 1u << 16;
__device__ __forceinline__ bool spin_fail(unsigned& spins, rsrc_t rs, u32x2 T) {
  asm volatile("" ::: "memory");
  ++spins;
  if (spins > kSpinCap) { st8(T, rs, 0, F_TMO); return true; }
  if ((spins & 255u) == 0u) {
    const u32x2 v = ld8(rs, 0, F_TMO);
    if (v[0] == T[0] && v[1] == T[1]) return true;
  }
  __builtin_amdgcn_s_sleep(3);
  return false;
}
__device__ __forceinline__ void poll_flags2(rsrc_t rs, unsigned voff, u32x2 T) {
  for (unsigned spins = 0;;) {
    const u32x4 v = ld16(rs, voff, 0);
    if (__all(v[0] == T[0] && v[1] == T[1] && v[2] == T[0] && v[3] == T[1])) break;
    if (spin_fail(spins, rs, T)) break;
  }
  asm volatile("" ::: "memory");
  __builtin_amdgcn_fence(__ATOMIC_ACQUIRE, "wavefront");
}
__device__ __forceinline__ void poll_flags(rsrc_t rs, unsigned voff, unsigned soff, u32x2 T) {
  for (unsigned spins = 0;;) {
    const u32x2 v = ld8(rs, voff, soff);
    if (__all(v[0] == T[0] && v[1] == T[1])) break;
    if (spin_fail(spins, rs, T)) break;
  }
  asm volatile("" ::: "memory");
  __builtin_amdgcn_fence(__ATOMIC_ACQUIRE, "wavefront");
}

#define STAMP_ON(i) true
#define STAMP_VAL(i) (((i) == 23 || (i) == 22 || (i) == 21) ? __builtin_amdgcn_s_memtime() : __builtin_amdgcn_s_memrealtime())
#define STAMPAT(i)

#define VSTEP(i)

__device__ __forceinline__ unsigned toff(unsigned R, unsigned ks, unsigned g) {
  return (((R >> 5) * 64 + ks * 2 + ((R >> 4) & 1)) << 10) + ((g * 16 + (R & 15)) << 4);
}
__device__ __forceinline__ void unpack8v(const u32x4 u, f32x2* v) {
#pragma unroll
  for (int j = 0; j < 4; ++j) v[j] = f32x2{lo16(u[j]), hi16(u[j])};
}
__device__ __forceinline__ u32x4 pack8v(const f32x2* v) {
  u32x4 o;
#pragma unroll
  for (int j = 0; j < 4; ++j) o[j] = pack2(v[j][0], v[j][1]);
  return o;
}
__device__ __forceinline__ f32x2 rbf1v(f32x2 v) { return f32x2{rbf1(v[0]), rbf1(v[1])}; }
__device__ __forceinline__ float ln_rd(float var) { return __builtin_amdgcn_rcpf(rbf1(__builtin_amdgcn_sqrtf(rbf1(rbf1(var) + 1e-5f)))); }
template <int N> __device__ __forceinline__ void ln_apply(float mb, float rd, const f32x2* v, f32x2* y) {
  const f32x2 mb2 = {mb, mb}, rd2 = {rd, rd};
#pragma unroll
  for (int j = 0; j < N; ++j) y[j] = rbf1v(rbf1v(v[j] - mb2) * rd2);
}
__device__ __forceinline__ float hadd(f32x2 v) {
  float a = v[0];
  asm volatile("" : "+v"(a));
  return a + v[1];
}
__device__ __forceinline__ float add_g(float v) { return add_x32(add_x16(v)); }
__device__ __forceinline__ void add_g3(float& x, float& y, float& z) {
  auto a = __builtin_amdgcn_permlane16_swap(__builtin_bit_cast(unsigned, x), __builtin_bit_cast(unsigned, y), false, false);
  auto b = __builtin_amdgcn_permlane16_swap(__builtin_bit_cast(unsigned, z), __builtin_bit_cast(unsigned, z), false, false);
  const float r = asf(a[0]) + asf(a[1]), t = asf(b[0]) + asf(b[1]);
  auto c = __builtin_amdgcn_permlane32_swap(__builtin_bit_cast(unsigned, r), __builtin_bit_cast(unsigned, r), false, false);
  auto d = __builtin_amdgcn_permlane32_swap(__builtin_bit_cast(unsigned, t), __builtin_bit_cast(unsigned, t), false, false);
  const float u = asf(c[0]) + asf(c[1]);
  z = asf(d[0]) + asf(d[1]);
  auto e = __builtin_amdgcn_permlane16_swap(__builtin_bit_cast(unsigned, u), __builtin_bit_cast(unsigned, u), false, false);
  x = asf(e[0]); y = asf(e[1]);
}
__device__ __forceinline__ u32x4 trade16(u32x4 v) {
  auto a = __builtin_amdgcn_permlane16_swap(v[0], v[2], false, false);
  auto b = __builtin_amdgcn_permlane16_swap(v[1], v[3], false, false);
  return u32x4{(unsigned)a[0], (unsigned)b[0], (unsigned)a[1], (unsigned)b[1]};
}
__device__ __forceinline__ unsigned umax4(u32x4 v) { return max(max(v[0], v[1]), max(v[2], v[3])); }
__device__ __forceinline__ u32x2 unpoison2(u32x2 v) { return u32x2{min(v[0], POISON - 1), min(v[1], POISON - 1)}; }
__device__ __forceinline__ u32x4 unpoison4(u32x4 v) {
  return u32x4{min(v[0], POISON - 1), min(v[1], POISON - 1), min(v[2], POISON - 1), min(v[3], POISON - 1)};
}

typedef union { struct { s16x4 a, b; } hf; u32x4 v; } frag_t;
__device__ __forceinline__ s16x4 trread(const char* smem, unsigned off) {
  return __builtin_amdgcn_ds_read_tr16_b64_v4i16((LDS_AS s16x4*)(smem + off));
}
__device__ __forceinline__ s16x4 trabs(unsigned addr) { return __builtin_amdgcn_ds_read_tr16_b64_v4i16((LDS_AS s16x4*)(size_t)addr); }

constexpr int WA = 6;
constexpr int WB = 8, WR = 4;
#define CH 1, 8, 10, 11, 16, 18, 24, 26, 30, 31
constexpr int CHV[10] = {CH};
constexpr int SPL = CHV[0];
constexpr int FSL = CHV[1], FS0 = CHV[2], FS0D = CHV[3], FS1 = CHV[4], FS1S = CHV[5];
constexpr int FXA = CHV[6], FXB = CHV[7], FXL = CHV[8], FXS = CHV[9];
#define L0S 22, 25, 8, 0, 26, 29, 1, 2
constexpr int L0V[8] = {L0S};
constexpr int CSL = L0V[0], CSS = L0V[1], CSD = L0V[2];
constexpr int abl(int it) { return it ? 25 : L0V[4]; }
constexpr int abs_(int it) { return it ? 27 : L0V[5]; }
constexpr int W2S = L0V[6], WSH = L0V[7];
constexpr int LNL = L0V[3], LNS = LNL + 2;
#define SYNCB 3
#define SPINS 0
#define ROTH 0
#define ROT0 (p + ROTH * h)
constexpr int TCV[7] = {TCH};
constexpr int TS0 = TCV[0], TSN = TCV[1], TM0 = TCV[2], TEX = TCV[3], TM1 = TCV[4], TSD = TCV[5], TSG = TCV[6];
constexpr int QL = 26;
constexpr int SDIR = 24, GSL = 14, RSL = 56, KSL = 3;
constexpr int tp0(int it) { return it ? 3 : 3 + WSH; }
constexpr bool tile_piece(int it, int s) { return s >= tp0(it) && ((s - tp0(it)) >> 2) + 3 < 8 && ((s - tp0(it)) & 3) < 3; }
constexpr int a_req(int s, int n) {
  if (s < 1 || !(n & 1) || (s == 1 && n == 1)) return -1;
  if (s <= 5) return 8 + 3 * s + (n >> 1) - 4;
  return n < 5 && s + 5 < 32 ? 2 * (s + 5) + (n >> 1) : -1;
}
constexpr int vm_n(int it, int s, int n) {
  int c = it ? (a_req(s, n) >= 0) + (s == 0 && n == 0) : (s + 5 < 32 && (n == 2 || n == 3));
  if (n == 0) c += tile_piece(it, s);
  if (n == 4) c += (s == abl(it)) + (it ? (s == FSL ? 5 : s == FXA ? 4 : 0) + (s == FSL - 2) : 0);
  if (!it) c += (s >= W2S && s < W2S + 3 && n == 4) + (s == LNL && n == 5 ? 3 : 0) + (s == LNS + 1 && n == 5) + (s == TEX && n == 5);
  else c += (s == 2 && n == 2) + (s == SPL && n == 4 ? 4 : 0);
  return c;
}
constexpr int vm_at(int it, int s, int n) {
  int c = it ? 8 : 16;
  for (int u = 0; u < 6 * s + n; ++u) c += vm_n(it, u / 6, u % 6);
  return c;
}
constexpr int vm_a(int it, int k) {
  if (k < 4) return 2 * k + 2;
  for (int u = 0; u < 192; ++u)
    if (a_req(u / 6, u % 6) == 2 * k + 1) return vm_at(it, (u + 1) / 6, (u + 1) % 6);
  return 0;
}
constexpr int vm_tile(int it, int t) { return t >= 3 ? vm_at(it, 4 * t - 10 + tp0(it), 1) : t == 1 ? 12 : vm_at(0, W2S + 2, 5); }
constexpr int vm_unit(int u) { return u < 3 ? 5 + 2 * u : u < 5 ? 8 + 2 * u : vm_at(0, u - 5, 4); }
constexpr int unit_slot(int u) { return u % 3 == 0 ? 0 : 16384 + (u % 3) * 16384; }

constexpr int L_XC = LDS_BYTES - 64, L_SKEY = 32704, L_SRED = 4096, L_SFIN = 20480;
__device__ __forceinline__ unsigned key_load(rsrc_t rs, int l, int L, int rep) { return ld4(rs, (l & 15) * 4, OFF_KEY + (L * 16 + rep) * 128); }
__device__ __forceinline__ unsigned umax16(unsigned k) {
  k = max(k, dppu<0xB1>(k));
  k = max(k, dppu<0x4E>(k));
  k = max(k, dppu<0x141>(k));
  return max(k, dppu<0x140>(k));
}
__device__ __forceinline__ unsigned key_row(unsigned key) { return (0xffffu - (key & 0xffffu)) & 2047u; }
__device__ __forceinline__ unsigned key_wait(rsrc_t rs, u32x2 T, int l, int L, int rep, unsigned kv) {
  if (!__all(kv != POISON)) {
    unsigned spins = 0;
    do {
      if (spin_fail(spins, rs, T)) break;
      if (L == 0) __builtin_amdgcn_s_sleep(KSL);
      kv = key_load(rs, l, L, rep);
    } while (!__all(kv != POISON));
  }
  return __builtin_amdgcn_readfirstlane(umax16(kv));
}
__device__ __forceinline__ unsigned ffn_xoff(unsigned key, int L, int w) { return OFF_CA + L * 32768 + (key_row(key) >> 7) * 2048 + w * 256; }
__device__ __forceinline__ void ffn_xwait(rsrc_t rs, u32x2 T, unsigned xo, int l, u32x4 (&xv)[4]) {
#define XV_OK() __all(max(max(umax4(xv[0]), umax4(xv[1])), max(umax4(xv[2]), umax4(xv[3]))) != POISON)
  if (!XV_OK()) {
    unsigned spins = 0;
    do {
      if (spin_fail(spins, rs, T)) break;
#pragma unroll
      for (int i = 0; i < 4; ++i) xv[i] = ld16(rs, (l >> 4) * 16 + i * 64, xo);
    } while (!XV_OK());
  }
#undef XV_OK
}
__device__ __forceinline__ void ffn_gu(const u32x4 (&gv)[4], const u32x4 (&uv)[4], const u32x4 (&xv)[4], f32x4& ga, f32x4& ua) {
  ga = mfma16(gv[0], xv[0], f32x4{0.f, 0.f, 0.f, 0.f}); ua = mfma16(uv[0], xv[0], f32x4{0.f, 0.f, 0.f, 0.f});
#pragma unroll
  for (int i = 1; i < 4; ++i) { ga = mfma16(gv[i], xv[i], ga); ua = mfma16(uv[i], xv[i], ua); }
}
__device__ __forceinline__ void ffn_gu_check(rsrc_t rs, u32x2 T, unsigned xo, int l, const u32x4 (&gv)[4], const u32x4 (&uv)[4], u32x4 (&xv)[4], f32x4& ga, f32x4& ua) {
  if (__any(ga[0] != ga[0])) {
    ffn_xwait(rs, T, xo, l, xv);
    ffn_gu(gv, uv, xv, ga, ua);
  }
}
__device__ __forceinline__ void ffn_gu_store(char* smem, int l, int w, f32x4 ga, f32x4 ua) {
  LDS_AS f32x4* sred = (LDS_AS f32x4*)(smem + L_SRED);
  if ((l & 15) == 0) { sred[w * 8 + (l >> 4)] = ga; sred[w * 8 + 4 + (l >> 4)] = ua; }
}
__device__ __forceinline__ void ffn_d_read(char* smem, int l, int w, f32x4& t, s16x4 (&dfr)[8]) {
  t = ((const LDS_AS f32x4*)(smem + L_SRED))[(l & 7) * 8 + ((l >> 3) & 1) * 4 + (l >> 4)];
#pragma unroll
  for (int i = 0; i < 8; ++i) dfr[i] = trread(smem, L_DN + w * 4096 + (l >> 4) * 1024 + ((l & 15) >> 2) * 256 + i * 32 + (l & 3) * 8);
}
__device__ __forceinline__ float ffn_d_sum(float t) { t += dppf<0xB1>(t); t += dppf<0x4E>(t); return t + dppf<0x141>(t); }
__device__ __forceinline__ float ffn_d_h(float t) {
  const float gg = rbf1(t), uu = rbf1(dppf<0x128>(t));
  return rbf1(gg * __builtin_amdgcn_rcpf(1.0f + __expf(-gg))) * uu;
}
__device__ __forceinline__ f32x4 ffn_d_mma(int l, int t, s16x4 d, u32x2 hh, f32x4 y) {
  const bool me = (l & 15) == t;
  return mfma16k16(d, __builtin_bit_cast(s16x4, u32x2{me ? hh[0] : 0u, me ? hh[1] : 0u}), y);
}
__device__ __forceinline__ void ffn_d_store(rsrc_t rs, int l, int w, int L, int slice, f32x4 y) {
  const unsigned c = l & 15;
  if (c < 8) st16(unpoison4(__builtin_bit_cast(u32x4, y)), rs, (4 * w + (c >> 1)) * 32768 + slice * 128 + (c & 1) * 64 + (l >> 4) * 16, OFF_PART + L * (1u << 20));
}
__device__ __forceinline__ void red_load(rsrc_t rs, int tid, int L, unsigned G, unsigned key, u32x4 (&pv)[4], u32x2& cxn, u32x2& cx1) {
  const unsigned co = L * 32768 + (key_row(key) >> 7) * 2048 + G * 64, jj = (tid & 3) * 16 + (tid & 4) * 2;
#pragma unroll
  for (int k = 0; k < 4; ++k) pv[k] = ld16(rs, min(tid + 512 * k, 1791) * 16, OFF_PART + L * (1u << 20) + G * 32768);
  cxn = ld8(rs, jj, OFF_CA + co);
  cx1 = ld8(rs, jj, OFF_CB + co);
}
__device__ __forceinline__ void red_sum(rsrc_t rs, char* smem, u32x2 T, int tid, int w, int L, unsigned G, unsigned key,
                                        u32x4 (&pv)[4], u32x2& cxn, u32x2& cx1) {
#define PV_OK() __all(max(max(max(umax4(pv[0]), umax4(pv[1])), max(umax4(pv[2]), umax4(pv[3]))), max(max(cxn[0], cxn[1]), max(cx1[0], cx1[1]))) != POISON)
  if (!PV_OK()) {
    unsigned spins = 0;
    do {
      if (spin_fail(spins, rs, T)) break;
      red_load(rs, tid, L, G, key, pv, cxn, cx1);
    } while (!PV_OK());
  }
#undef PV_OK
  f32x4 s = (__builtin_bit_cast(f32x4, pv[0]) + __builtin_bit_cast(f32x4, pv[1])) + __builtin_bit_cast(f32x4, pv[2]);
  if (w < 4) s += __builtin_bit_cast(f32x4, pv[3]);
  ((LDS_AS f32x4*)(smem + L_SFIN))[tid] = s;
}
__device__ __forceinline__ u32x2 red_final(char* smem, int l, unsigned key, u32x2 cxn, u32x2 cx1) {
  const LDS_AS f32x4* sf = (const LDS_AS f32x4*)(smem + L_SFIN) + l;
  f32x4 s = ((sf[0] + sf[64]) + (sf[128] + sf[192])) + ((sf[256] + sf[320]) + (sf[384] + sf[448]));
#pragma unroll
  for (int j = 0; j < 4; ++j) s[j] = sum_hi8(s[j]);
  const unsigned k16 = key >> 16;
  const float rmax = __builtin_bit_cast(float, ((k16 & 0x8000u) ? (k16 & 0x7fffu) : (~k16 & 0xffffu)) << 16);
  const float sig = rbf1(__builtin_amdgcn_rcpf(1.0f + __expf(-rmax)));
  const float xn4[4] = {lo16(cxn[0]), hi16(cxn[0]), lo16(cxn[1]), hi16(cxn[1])};
  const float x14[4] = {lo16(cx1[0]), hi16(cx1[0]), lo16(cx1[1]), hi16(cx1[1])};
  float o[4];
#pragma unroll
  for (int j = 0; j < 4; ++j) o[j] = x14[j] + rbf1(xn4[j] + rbf1(rbf1(s[j]) * sig));
  return u32x2{pack2(o[0], o[1]), pack2(o[2], o[3])};
}

struct RowFin { float mean, var, mb, rd; unsigned key; };
__device__ __forceinline__ void fin_wait(rsrc_t rs, u32x2 T, unsigned voff, unsigned so, u32x4 (&sp)[4]) {
#define ST_OK() __all(max(max(umax4(sp[0]), umax4(sp[1])), max(umax4(sp[2]), umax4(sp[3]))) != POISON)
  if (!ST_OK()) {
    unsigned spins = 0;
    do {
      if (spin_fail(spins, rs, T)) break;
#pragma unroll
      for (int i = 0; i < 4; ++i) sp[i] = ld16(rs, voff + i * 2048, so);
    } while (!ST_OK());
  }
#undef ST_OK
}
__device__ __forceinline__ RowFin fin_key(rsrc_t rs, u32x2 T, unsigned voff, unsigned so, char* skey, int l, int w, unsigned R, float wsum, u32x4 (&sp)[4]) {
  RowFin r;
#define ST_SUM() ((__builtin_bit_cast(f32x4, sp[0]) + __builtin_bit_cast(f32x4, sp[1])) + (__builtin_bit_cast(f32x4, sp[2]) + __builtin_bit_cast(f32x4, sp[3])))
  f32x4 sf = ST_SUM();
  const float z = (sf[0] + sf[1]) + sf[2];
  if (__any(z != z)) {
    fin_wait(rs, T, voff, so, sp);
    sf = ST_SUM();
  }
#undef ST_SUM
  float s0 = sf[0], s1 = sf[1], s2 = sf[2];
  add_g3(s0, s1, s2);
  r.mean = s0 * (1.0f / 1024.0f);
  r.var = fmaxf(__builtin_fmaf(-r.mean, r.mean, s1 * (1.0f / 1024.0f)), 0.f);
  r.mb = rbf1(r.mean); r.rd = ln_rd(r.var);
  const unsigned rb16 = pack2((s2 - r.mb * wsum) * r.rd, 0.f) & 0xffffu;
  r.key = min((((rb16 & 0x8000u) ? (~rb16 & 0xffffu) : (rb16 | 0x8000u)) << 16) | (0xffffu - R), POISON - 1);
  const unsigned wk = umax16(r.key);
  if (l == 0) ((LDS_AS unsigned*)skey)[w] = wk;
  return r;
}
__device__ __forceinline__ void fin_cand(rsrc_t rs, char* skey, int L, int g, int rb, int ct, const RowFin& r, const f32x2 (&v)[8]) {
  const u32x4 k0 = *(const LDS_AS u32x4*)skey, k1 = *(const LDS_AS u32x4*)(skey + 16);
  const unsigned best = max(umax4(k0), umax4(k1));
  if (r.key == best) {
    const unsigned co = L * 32768 + rb * 2048 + ct * 128 + g * 16;
    f32x2 t[8], rd2 = {r.rd, r.rd}, of2 = {-r.mb * r.rd, -r.mb * r.rd};
    asm volatile("" : "+v"(rd2), "+v"(of2));
#pragma unroll
    for (int j = 0; j < 8; ++j) t[j] = __builtin_elementwise_fma(v[j], rd2, of2);
    st16(unpoison4(pack8v(t)), rs, co, OFF_CA); st16(unpoison4(pack8v(t + 4)), rs, co + 64, OFF_CA);
    if (g == 0) st4(best, rs, OFF_KEY + (L * 16 + ct) * 128 + rb * 4, 0);
    st16(unpoison4(pack8v(v)), rs, co, OFF_CB); st16(unpoison4(pack8v(v + 4)), rs, co + 64, OFF_CB);
  }
}
__device__ __forceinline__ void fin_x2(const RowFin& r, f32x2 (&v)[8]) {
  f32x2 sc2 = {1.0f + r.rd, 1.0f + r.rd}, of2 = {-r.mb * r.rd, -r.mb * r.rd};
  asm volatile("" : "+v"(sc2), "+v"(of2));
#pragma unroll
  for (int j = 0; j < 8; ++j) v[j] = __builtin_elementwise_fma(v[j], sc2, of2);
}

extern "C" __device__ unsigned long long dispatch_id() __asm("llvm.amdgcn.dispatch.id");

extern "C" __global__ __launch_bounds__(512, 2) void mega_kernel(const bf16* x, char* ws, const bf16* wqkv, const bf16* wo, const bf16* wr,
                                                                 const bf16* wg, const bf16* wu, const bf16* wd, bf16* out) {
  extern __shared__ __attribute__((aligned(16))) char smem[];
  const int w0_ = __builtin_amdgcn_readfirstlane(threadIdx.x >> 6);
  const unsigned lbase = (unsigned)(size_t)(LDS_AS char*)smem;
  {
    unsigned long long pc;
    unsigned len;
    asm volatile("s_getpc_b64 %0\n\ts_mov_b32 %1, .Ltext_end-mega_kernel\n\t.subsection 1\n.Ltext_end:\n\t.subsection 0" : "=s"(pc), "=s"(len));
    const unsigned o = ((blockIdx.x >> 3) * 64 + (threadIdx.x & 63)) * 128;
    if (w0_ == 7 && o < len)
      asm volatile("s_mov_b32 m0, %0\n\ts_nop 0\n\tglobal_load_lds_dword %1, %2" ::"s"(lbase + L_FXL), "v"(o), "s"(pc & ~127ull) : "memory");
  }
  asm volatile("" : "+s"(x), "+s"(ws), "+s"(wqkv));
#define IDS()                                                                                         \
  int tid = threadIdx.x, w = w0_, bid = blockIdx.x;                                                   \
  asm volatile("" : "+v"(tid));                                                                       \
  asm volatile("" : "+s"(w));                                                                         \
  asm volatile("" : "+s"(bid));                                                                       \
  const int l = tid & 63, xcd = bid & 7, bi = bid >> 3;                                               \
  const int p = (xcd & 1) * 4 + (bi & 3), h = (xcd >> 1) * 8 + (bi >> 2);         \
  const int rb = 2 * xcd + (bi & 1), ct = bi >> 1;                               \
  const int slice = xcd * 28 + bi;                                                   \
  const bool ffn_on = bi < 28;                                                                        \
  const int g = l >> 4;                                                                               \
  const int rt = w < 4 ? w : 11 - w;          \
  KARGS();                                                                                            \
  (void)p; (void)h; (void)rb; (void)ct; (void)slice; (void)ffn_on; (void)g; (void)rt;
  const rsrc_t rs = __builtin_amdgcn_make_buffer_rsrc((void*)ws, 0, WS_BYTES, 0x00020000);
  typedef const void* const __attribute__((address_space(4)))* karg_t;
#define KARGS() karg_t ka = (karg_t)__builtin_amdgcn_kernarg_segment_ptr(); asm volatile("" : "+s"(ka))
#define ARGP(name, i) const GAS bf16* name = (const GAS bf16*)ka[i]; asm volatile("" : "+s"(name))
#define RSRC(ptr, bytes) __builtin_amdgcn_make_buffer_rsrc((void*)(ptr), 0, bytes, 0x00020000)
#define rwq RSRC(pwq, 1024 * 3072 * 2)
#define rwo RSRC(pwo, 1024 * 1024 * 2)
#define rwg RSRC(pwg, 1024 * F_ * 2)
#define rwu RSRC(pwu, 1024 * F_ * 2)
#define rwd RSRC(pwd, 1024 * F_ * 2)
#define rout RSRC(pout, 2048 * 1024 * 2)

#define WQ_VOFF() ((l >> 2) * 6144 + (((l & 3) ^ ((((l >> 4) ^ (l >> 5)) & 1) << 1)) << 4))
#define WQ_PIECE(t, j, dst) dma16(rwq, wqv, ((((t) + rot) & 7) * 128 + ((j) & 7) * 16) * 6144 + ((j) >> 3) * 2048 + h * 64, lbase + (dst) + (j) * 1024)
#define WQ_BOFF(dt) ((8 * g + ((l & 15) >> 2)) * 64 + (l & 3) * 8 + (((dt) ^ (g & 1)) << 5))
#define WQ_POFF() ((4 * g + ((l & 15) >> 2)) * 64 + (l & 3) * 8 + (((g ^ (g >> 1)) & 1) << 5))
#define X_UNIT1(u, j) dma16(rx, xv_, (unsigned)(p * 256 + rt * 32 + ((u) & 1) * 16 + (j) * 8) * 2048 + ((((u) >> 2) + rot) & 7) * 256 + (((u) >> 1) & 1) * 128, lbase + rt * 2048 + unit_slot(u) + (j) * 1024)
#define X_VOFF() ((l >> 3) * 2048 + (((l & 7) ^ ((l >> 3) & 6)) << 4))
#define X_ROFF(hs) (lbase + rt * 2048 + (l & 15) * 128 + (((4 * (hs) + g) ^ (l & 6)) << 4))
#define TOUCH_TO(ptr, dst) asm volatile("s_mov_b32 m0, %0\n\ts_nop 0\n\tglobal_load_lds_dword %1, off" ::"s"(lbase + (dst)), "v"(ptr) : "memory")
#define TOUCH(ptr) TOUCH_TO(ptr, L_FXL)
#define WO_PIECE(j) dma16(rwo, wov, ((w >> 2) * 64 + (w & 3) + 4 * (j)) * 16384 + ct * 128, lbase + 32768 + (j) * 8192 + w * 1024)
#define WD_DMA() _Pragma("unroll") for (int i = 0; i < 4; ++i) WD_DMA1(i)
#define WD_DMA1(i)                                                                                   \
      dma16(rwd, (l >> 4) * 2048 + (l & 15) * 16, (unsigned)(slice * 16 + 4 * (i)) * 2048 + w * 256, lbase + L_DN + w * 4096 + (i) * 1024)
#define FFN_DMA(m) dma16((m) & 1 ? rwu : rwg, fnv, (unsigned)(((4 * w + ((m) >> 1)) * 32) * F_ + slice * 16) * 2, lbase + 65536 + (m) * 8192 + w * 1024)
  u32x4 gv[4], uv[4];
  u32x4 keep[9];
  u32x4 pre[4][2];
#define PRE_LOAD(it_, rot_, s0, s1)                                                                  \
  static_for<(s1) - (s0)>([&](auto c_) __attribute__((always_inline)) {                              \
    constexpr int s = (s0) + decltype(c_)::value;                                                    \
    const unsigned ab_ = OFF_XN + (it_) * (4u << 20) + (((rot_) + (s >> 2)) & 7) * 8192 + ((s >> 1) & 1) * 4096; \
    ld16w<2 * s, (s & 1) * 2048>(pre[s][0], rs, l * 16, ab_ + (p * 8 + rt) * 65536);                         \
    ld16w<2 * s + 1, (s & 1) * 2048 + 1024>(pre[s][1], rs, l * 16, ab_ + (p * 8 + rt) * 65536);                 \
  })
  u32x2 T;
  const GAS bf16* tsrc;
  {
    IDS();
    const int it = 0, rot = ROT0;
    (void)it;
    STAMPAT(0);
    const bf16* pwq = wqkv;
    const rsrc_t rx = RSRC(x, 2048 * 1024 * 2);
    const unsigned wqv = WQ_VOFF(), xv_ = X_VOFF();
    if (tid == 0) *(LDS_AS u32x4*)(smem + L_QC) = u32x4{0u, 0u, 0u, 0u};
#pragma unroll
    for (int i = 0; i < 3; ++i) WQ_PIECE(0, 3 * w + i, L_RING);
    STAMPAT(41);
    X_UNIT1(0, 0); X_UNIT1(0, 1); X_UNIT1(1, 0); X_UNIT1(1, 1);
    STAMPAT(42);
    BAR();
    STAMPAT(43);
    X_UNIT1(2, 0); X_UNIT1(2, 1);
#pragma unroll
    for (int i = 0; i < 3; ++i) WQ_PIECE(1, 3 * w + i, L_RING + STAGE);
    STAMPAT(44);
    if (w == 1 && l < 9) {
      const unsigned prb = 2 * p + (h >> 4), pct = h & 15;
      if (l < 8) st16(u32x4{POISON, POISON, POISON, POISON}, rs, OFF_CA + prb * 2048 + pct * 128 + l * 16, 0);
      else st4(POISON, rs, OFF_KEY + pct * 128 + prb * 4, 0);
    }
    {
      ARGP(pwo, 3); ARGP(pwg, 5); ARGP(pwu, 6);
      const int t = tid < 288 ? tid - 64 : tid - 288, go = (bi * 32 + t / 7) * F_ + xcd * 448 + (t % 7) * 64;
      tsrc = tid < 64 ? pwo + bid * 4096 + tid * 64 : tid < 288 ? pwg + go : pwu + go;
      asm volatile("" : "+v"(tsrc));
    }
    STAMPAT(31);
  }
  unsigned sel0 = 0x7FFFFFFFu;
  f32x2 v[8];
  u32x4 x2p[2];
  float wsum;

  auto layer = [&](auto it_c) __attribute__((always_inline)) {
    constexpr int it = decltype(it_c)::value;
    STAMPAT(1); STAMPAT(22);
    f32x4 acc[2][6];
    f32x2 qs2[2];
    u32x4 k0, k1, v0, v1;
    {
      IDS();
      bool aff = false;
      unsigned aq[2];
      u32x4 av[2];
#define PIN_ACC() asm volatile("" : "+{v[176:179]}"(acc[0][0]), "+{v[180:183]}"(acc[0][1]), "+{v[184:187]}"(acc[0][2]), "+{v[188:191]}"(acc[0][3]), "+{v[192:195]}"(acc[0][4]), "+{v[196:199]}"(acc[0][5]), \
                               "+{v[200:203]}"(acc[1][0]), "+{v[204:207]}"(acc[1][1]), "+{v[208:211]}"(acc[1][2]), "+{v[212:215]}"(acc[1][3]), "+{v[216:219]}"(acc[1][4]), "+{v[220:223]}"(acc[1][5]))
#define PINV4(x) asm volatile("" : "+v"(x[0]), "+v"(x[1]), "+v"(x[2]), "+v"(x[3])::"memory")
      auto qkv_loop = [&](auto it_c) __attribute__((always_inline)) {
        constexpr int IT = decltype(it_c)::value;
        const unsigned abase = OFF_XN + IT * (4u << 20) + (p * 8 + rt) * 65536;
        const int rot = ROT0 + 5 * IT;
        unsigned fkey = 0, kv, bq[2];
        u32x4 fx[4], bv[2], af[WA][2];
        u32x2 cxn, cx1, hh;
        f32x4 sa, sb, cs4[2];
        f32x2 b2[2];
        s16x4 dfr[8];
#define AB_OFF() (OFF_AB + IT * 32768 + (p * 8 + rt) * 512)
#define A_LOAD1(k, a) ld16w<(k) % WA * 2 + (a), ((k) & 1) * 2048 + (a) * 1024>(af[(k) % WA][a], rs, l * 16, abase + ((((k) >> 2) + rot) & 7) * 8192 + (((k) >> 1) & 1) * 4096)
        if constexpr (IT == 1) {
#pragma unroll
          for (int s = 0; s < 4; ++s) { af[s][0] = pre[s][0]; af[s][1] = pre[s][1]; }
        }
        const bf16* pwq = wqkv;
        const rsrc_t rx = RSRC(x, 2048 * 1024 * 2);
        unsigned xv_ = X_VOFF(), xr[2] = {X_ROFF(0), X_ROFF(1)};
        if constexpr (IT == 0) asm volatile("" : "+v"(xv_), "+v"(xr[0]), "+v"(xr[1]));
#define X_READ(u) { af[((u) & ~1) % WA][(u) & 1] = *(const LDS_AS u32x4*)(size_t)(xr[0] + unit_slot(u)); af[((u) | 1) % WA][(u) & 1] = *(const LDS_AS u32x4*)(size_t)(xr[1] + unit_slot(u)); }
        const int tstep = TS0 + bid % TSN + TSD * (w >> TSG);
        const unsigned wqv = WQ_VOFF();
#define QKV_DMA1(t, i)                                                                            \
  {                                                                                               \
    if constexpr (IT == 0) WQ_PIECE(t, 3 * w + (i), L_RING + SLOT(t) * STAGE);                    \
    else dma16s<(i) * 1024>(rs, l * 16, OFF_WI + (h * 8 + (((t) + rot) & 7)) * STAGE + w * 3072, lbase + L_RING + SLOT(t) * STAGE + w * 3072); \
  }
        unsigned bo[2][2];
#pragma unroll
        for (int dt = 0; dt < 2 - IT; ++dt) {
          bo[dt][0] = lbase + L_RING + (IT ? l * 16 : WQ_BOFF(dt)); bo[dt][1] = bo[dt][0] + 2 * STAGE;
          asm volatile("" : "+v"(bo[dt][0]), "+v"(bo[dt][1]));
        }
        auto frag0 = [&](int sl, int f) __attribute__((always_inline)) {
          unsigned pb = lbase + L_RING + WQ_POFF();
          asm volatile("" : "+v"(pb));
          const unsigned o = (pb ^ ((f & 1) << 5)) + sl * STAGE + ((f % 6) >> 1) * 8192 + (f / 6) * 2048;
          frag_t t;
          t.hf.a = trabs(o); t.hf.b = trabs(o + 1024);
          return t.v;
        };
        u32x4 df[4];
        auto dot_mma = [&](const u32x4 (&xf)[4]) __attribute__((always_inline)) {
          sa = mfma16(df[0], xf[0], f32x4{0.f, 0.f, 0.f, 0.f}); sb = mfma16(df[1], xf[1], f32x4{0.f, 0.f, 0.f, 0.f});
          sa = mfma16(df[2], xf[2], sa); sb = mfma16(df[3], xf[3], sb);
        };
        auto dot_store = [&](unsigned dst) __attribute__((always_inline)) {
          if ((l & 15) == 0) st16(unpoison4(__builtin_bit_cast(u32x4, sa + sb)), rs, g * 16, dst + ((h * 6 + w) * 8 + p) * 64);
        };
        auto tile_sum = [&](u32x4 pt, int dst) __attribute__((always_inline)) {
          f32x4 c = __builtin_bit_cast(f32x4, pt);
#pragma unroll
          for (int e = 0; e < 4; ++e) { c[e] += dppf<0x124>(c[e]); c[e] += dppf<0x128>(c[e]); c[e] = add_x16(c[e]); }
          if ((l & 28) == 0) *(LDS_AS f32x4*)(smem + dst + (w * 2 + (l >> 5)) * 64 + (l & 3) * 16) = c;
        };
        frag_t bfr[6];
#define QKV_BO(s, n, x) (bo[((n) ^ (x)) & 1][SLOT((s) >> 2) >> 1] + (SLOT((s) >> 2) & 1) * STAGE + ((n) >> 1) * 8192 + ((s) & 3) * 2048 + (x) * 256)
#define QKV_RDA(s, n)                                                                             \
  {                                                                                               \
    if constexpr (IT == 0) bfr[n].hf.a = trabs(QKV_BO(s, n, 0));                                  \
    else bfr[n].v = *(const LDS_AS u32x4*)(size_t)(bo[0][SLOT((s) >> 2) >> 1] + (SLOT((s) >> 2) & 1) * STAGE + (((s) & 3) * 6 + (n)) * 1024); \
  }
#define QKV_RDB(s, n) { if constexpr (IT == 0) bfr[n].hf.b = trabs(QKV_BO(s, n, 1)); }
#define XP_OFF() (OFF_XNEW + ((rot + 5) & 7) * 256)
#define FP_OFF(i) (w == 3 ? OFF_XNEW + (i) * 1024 : OFF_FX + h * 3072 + w * 1024)
#define FX_OK(n) __all(((n) == 4 ? max(max(umax4(fx[0]), umax4(fx[1])), max(umax4(fx[2]), umax4(fx[3]))) : max(umax4(fx[0]), umax4(fx[1]))) != POISON)
#define SINCE(s0) (vm_at(IT, s, n) - vm_at(IT, s0, 5))
#define DEF4(x) asm volatile("" : "=v"(x[0]), "=v"(x[1]), "=v"(x[2]), "=v"(x[3]))
        LDS_AS unsigned* qcnt = (LDS_AS unsigned*)(smem + L_QC);
        unsigned qc;
        unsigned qadr = lbase + L_QC, qone = 1;
        asm volatile("" : "+v"(qadr), "+v"(qone));
#define count_in(i) asm volatile("s_mov_b64 exec, 1\n\tds_add_u32 %0, %1 offset:%2\n\ts_mov_b64 exec, -1" ::"v"(qadr), "v"(qone), "n"(4 * (i)) : "memory")
        auto count_wait = [&](int i, unsigned want) __attribute__((always_inline)) {
          if (__builtin_expect((unsigned)__builtin_amdgcn_readfirstlane(qc) < want, 0)) {
            while ((unsigned)__builtin_amdgcn_readfirstlane(__hip_atomic_load(qcnt + i, __ATOMIC_RELAXED, __HIP_MEMORY_SCOPE_WORKGROUP)) < want) { if (SPINS) __builtin_amdgcn_s_sleep(SPINS); }
          }
          asm volatile("" ::: "memory");
        };
        if constexpr (IT == 0) {
          wait_vm<12 - vm_unit(1)>();
          STAMPAT(46);
          if (!(SYNCB & 1)) count_in(0);
          X_READ(0); X_READ(1);
          asm volatile("s_waitcnt lgkmcnt(0)" : "+v"(af[0][0]), "+v"(af[0][1]), "+v"(af[1][0]), "+v"(af[1][1])::"memory");
          X_UNIT1(3, 0); X_UNIT1(3, 1); X_UNIT1(4, 0); X_UNIT1(4, 1);
          STAMPAT(38);
          qc = 0;
          if (SYNCB & 1) asm volatile("s_barrier" ::: "memory");
          else count_wait(0, 8);
        }
        STAMPAT(39);
#pragma unroll
        for (int n = 0; n < 6; ++n) { QKV_RDA(0, n); if (n < 5) QKV_RDB(0, n); }
        f32x2 lv[8];
        float lmean;
        u32x2 ep;
        u32x4 xl[2];
        f32x4 sc;
        unsigned long long xok = 0;
        unsigned xg;
        RowFin fin;
        auto x1_need = [&](unsigned long long m) __attribute__((always_inline)) {
          if (__builtin_expect((xok & m) != m, 0)) {
            for (unsigned spins = 0;;) {
              const u32x2 f = ld8(rs, F_X1 + (p * RX + (xcd >> 1) % RX) * 256 + (l & 31) * 8, 0);
              xok = __builtin_amdgcn_ballot_w64(f[0] == T[0] && f[1] == T[1]);
              if ((xok & m) == m) break;
              if (spin_fail(spins, rs, T)) { xok = ~0ull; break; }
              __builtin_amdgcn_s_sleep(KSL);
            }
            asm volatile("" ::: "memory");
          }
        };
        auto gap = [&](auto sc_, auto nc_) __attribute__((always_inline)) {
          constexpr int s = decltype(sc_)::value, n = decltype(nc_)::value;
          if constexpr ((SYNCB >> IT) & 1) {
            constexpr int tc = (s + 2) / 4;
            if constexpr (tc >= 1 && tc < 8 && s == 4 * tc - 2 && n == 5) {
              if constexpr (!IT || tc >= 3) wait_vm<vm_at(IT, s, n) - vm_tile(IT, tc)>();
              if constexpr (tc == 7 && !IT) asm volatile("s_waitcnt lgkmcnt(4)" ::: "memory");
              asm volatile("s_barrier" ::: "memory");
            }
          } else {
            constexpr int ts = (s + 3) / 4, tc = (s + 2) / 4, t0 = 1;
            if constexpr (ts >= 3 && ts < 8 && s == 4 * ts - 3 && n == 2) { wait_vm<vm_at(IT, s, n) - vm_tile(IT, ts)>(); count_in(0); }
            if constexpr (!IT && tc >= t0 && tc < 3 && s == 4 * tc - 2 && n == 1) { wait_vm<vm_at(IT, s, n) - vm_tile(IT, tc)>(); count_in(0); }
            if constexpr (IT && ts >= t0 && ts < 3 && s == 4 * ts - 3 && n == 2) count_in(0);
            if constexpr (tc >= t0 && tc < 8 && s == 4 * tc - 2) {
              if constexpr (n == 3) qc = __hip_atomic_load(qcnt, __ATOMIC_RELAXED, __HIP_MEMORY_SCOPE_WORKGROUP);
              if constexpr (n == 5) count_wait(0, 8 * (tc + 1 - IT));
            }
          }
          if constexpr (IT == 0 && n == 0 && s + 2 < 32) { wait_vm<vm_at(0, s, 0) - vm_unit(s + 2)>(); X_READ(s + 2); SB(); }
          if constexpr (n == 0 && tile_piece(IT, s)) QKV_DMA1(((s - tp0(IT)) >> 2) + 3, (s - tp0(IT)) & 3);
          if constexpr (IT == 0) {
            if constexpr ((n == 2 || n == 3) && s + 5 < 32) {
              if constexpr (n == 2) asm volatile("s_waitcnt lgkmcnt(%0)" ::"n"(s + 1 < QL ? 4 : 1) : "memory");
              X_UNIT1(s + 5, n - 2);
            }
            if constexpr (s >= W2S && s < W2S + 3 && n == 4) QKV_DMA1(2, s - W2S);
            if constexpr (s == LNL && n == 5) {
              asm volatile("buffer_load_dwordx2 %0, off, %1, 0 sc1" : "=v"(ep) : "s"(rs) : "memory");
              asm volatile("buffer_load_dwordx4 %0, %1, %2, %3 offen" : "=v"(xl[0]) : "v"(l * 16), "s"(rx), "s"((p * 256 + h * 8 + w) * 2048) : "memory");
              asm volatile("buffer_load_dwordx4 %0, %1, %2, %3 offen offset:1024" : "=v"(xl[1]) : "v"(l * 16), "s"(rx), "s"((p * 256 + h * 8 + w) * 2048) : "memory");
            }
            if constexpr (s == LNS && n == 1) {
              wait_vm<vm_at(0, s, n) - vm_at(0, LNL + 1, 0)>();
              asm volatile("" : "+v"(ep), "+v"(xl[0]), "+v"(xl[1])::"memory");
              const unsigned long long e = (((unsigned long long)(unsigned)__builtin_amdgcn_readfirstlane(ep[1]) << 32) | (unsigned)__builtin_amdgcn_readfirstlane(ep[0])) + 1ull + dispatch_id() * 0x9E3779B97F4A7C16ull;
              T = u32x2{(unsigned)e, (unsigned)(e >> 32)};
              unpack8v(xl[0], lv); unpack8v(xl[1], lv + 4);
              asm volatile("" : "+v"(lv[0]), "+v"(lv[1]), "+v"(lv[2]), "+v"(lv[3]), "+v"(lv[4]), "+v"(lv[5]), "+v"(lv[6]), "+v"(lv[7]));
            }
            if constexpr (s == LNS && n == 4) {
              const f32x2 sm = ((lv[0] + lv[1]) + (lv[2] + lv[3])) + ((lv[4] + lv[5]) + (lv[6] + lv[7]));
              lmean = sum64(hadd(sm)) * (1.0f / 1024.0f);
              asm volatile("" : "+v"(lmean));
            }
            if constexpr (s == LNS + 1 && n == 1) {
              f32x2 m2 = {lmean, lmean}, q0 = {0.f, 0.f}, q1 = {0.f, 0.f};
              asm volatile("" : "+v"(m2));
#pragma unroll
              for (int j = 0; j < 8; j += 2) {
                const f32x2 d0 = lv[j] - m2, d1 = lv[j + 1] - m2;
                q0 = __builtin_elementwise_fma(d0, d0, q0); q1 = __builtin_elementwise_fma(d1, d1, q1);
              }
              lv[0] = q0 + q1;
              asm volatile("" : "+v"(lv[0]));
            }
            if constexpr (s == LNS + 1 && n == 5) {
              const float rd = ln_rd(sum64(hadd(lv[0])) * (1.0f / 1024.0f));
              if (l == 0) st16(u32x4{__builtin_bit_cast(unsigned, rd), __builtin_bit_cast(unsigned, -rbf1(lmean)), T[0], T[1]}, rs, OFF_AB + (p * 256 + h * 8 + w) * 16, 0);
              if (w == 1 && l < RX) st8(T, rs, F_XN + (p * RX + l) * 256 + h * 8, 0);
            }
          } else {
#define X1_OFF() (F_X1 + (p * RX + (xcd >> 1) % RX) * 256 + (l & 31) * 8)
            if constexpr ((s == 0 && n == 0) || (s == 2 && n == 2)) asm volatile("buffer_load_dwordx2 %0, %1, %2, 0 offen sc1" : "=v"(cxn) : "v"(X1_OFF()), "s"(rs) : "memory");
            if constexpr ((s == 1 && n == 3) || (s == 4 && n == 0)) {
              wait_vm<vm_at(1, s, n) - (s == 1 ? vm_at(1, 0, 1) : vm_at(1, 2, 3))>();
              asm volatile("" : "+v"(cxn)::"memory");
              xok |= __builtin_amdgcn_ballot_w64(cxn[0] == T[0] && cxn[1] == T[1]);
            }
            if constexpr (a_req(s, n) >= 0 && (a_req(s, n) & 7) == 0) x1_need(0x30003ull << (((rot + a_req(s, n) / 8) & 7) * 2));
            const unsigned R = (rb * 4 + (w & 3)) * 32 + (w >> 2) * 16 + (l & 15), so = OFF_STAT + rb * 32768;
            if constexpr (s == SPL && n == 4) {
#pragma unroll
              for (int i = 0; i < 4; ++i) ld16h<0>(fx[i], rs, (4 * g + i) * 2048 + (R & 127) * 16, so);
            }
            if constexpr (s == SPL + 2 && n == 1) {
              wait_vm<SINCE(SPL)>();
              PINV4(fx);
              fin = fin_key(rs, T, g * 8192 + (R & 127) * 16, so, smem + L_SKEY, l, w, R, wsum, fx);
              count_in(2);
            }
            if constexpr (s == SPL + 3 && n == 0) qc = __hip_atomic_load(qcnt + 2, __ATOMIC_RELAXED, __HIP_MEMORY_SCOPE_WORKGROUP);
            if constexpr (s == SPL + 3 && n == 1) { count_wait(2, 8); fin_cand(rs, smem + L_SKEY, 0, g, rb, ct, fin, v); STAMPAT(51); }
            if constexpr (s == SPL + 3 && n == 3) {
              fin_x2(fin, v);
              x2p[0] = pack8v(v); x2p[1] = pack8v(v + 4);
              asm volatile("" : "+v"(x2p[0]), "+v"(x2p[1]));
            }
            if constexpr (s == SPL + 3 && n == 5) {
              if ((w & 3) * 2 + (w >> 2) == ct >> 1) {
                const float f = 1.0f + fin.rd, rd2 = ln_rd(fin.var * f * f), mb2 = rbf1(__builtin_fmaf(fin.mean - fin.mb, fin.rd, fin.mean));
                if (g == 0 && ((l >> 3) & 1) == (ct & 1))
                  st16(u32x4{__builtin_bit_cast(unsigned, f * rd2), __builtin_bit_cast(unsigned, -__builtin_fmaf(fin.mb, fin.rd, mb2) * __builtin_amdgcn_rcpf(f)), T[0], T[1]}, rs, OFF_AB + 32768 + R * 16, 0);
              }
            }
          }
          if constexpr (IT == 1 && a_req(s, n) >= 0) A_LOAD1(a_req(s, n) / 2, a_req(s, n) & 1);
          if constexpr (!IT && s == CSL && n == 4) { asm volatile("" : "=v"(fx[0])); if (w < 6) ld16h<0>(fx[0], rs, l * 16, OFF_CS + (h * 6 + w) * 1024); }
          if constexpr (!IT && s == CSS && n == 2) {
            if (w < 6) {
              wait_vm<SINCE(CSL)>();
              asm volatile("" : "+v"(fx[0])::"memory");
              for (unsigned spins = 0; !__all(fx[0][2] == T[0] && fx[0][3] == T[1]) && !spin_fail(spins, rs, T);) fx[0] = ld16(rs, l * 16, OFF_CS + (h * 6 + w) * 1024);
              const f32x2 c = {sum_hi8(asf(fx[0][0])), sum_hi8(asf(fx[0][1]))};
              if (l < 8) *(LDS_AS f32x2*)(smem + L_CSL + w * 64 + l * 8) = c;
            }
          }
          constexpr int ABL = abl(IT), ABS = abs_(IT);
          if constexpr (s == ABL && n == 4) ld16h<0>(bv[0], rs, (l & 31) * 16, AB_OFF());
          if constexpr (s == ABS && n == 2) {
            wait_vm<SINCE(ABL)>();
            asm volatile("" : "+v"(bv[0])::"memory");
            for (unsigned spins = 0; !__all(bv[0][2] == T[0] && bv[0][3] == T[1]) && !spin_fail(spins, rs, T);) bv[0] = ld16(rs, (l & 31) * 16, AB_OFF());
            const unsigned t0 = bv[0][0], t1 = bv[0][1];
            ((LDS_AS unsigned*)(smem + L_ABW + rt * 2048))[l] = l < 32 ? t0 : t1;
          }
          if constexpr (s == ABS && n == 3) {
#pragma unroll
            for (int a = 0; a < 2; ++a) {
              const LDS_AS unsigned* aw = (const LDS_AS unsigned*)(smem + L_ABW + rt * 2048) + 16 * a;
              aq[a] = aw[l & 15]; bq[a] = aw[32 + (l & 15)];
              av[a] = *(const LDS_AS u32x4*)(aw + 4 * g); bv[a] = *(const LDS_AS u32x4*)(aw + 32 + 4 * g);
            }
          }
          if constexpr (s == ABS && n == 5) {
#pragma unroll
            for (int a = 0; a < 2; ++a) { b2[a] = f32x2{asf(bq[a]), asf(bq[a])}; asm volatile("" : "+v"(b2[a])); }
          }
          if constexpr (s == ABS + 1) {
            constexpr int k = (n + 4) % 6;
            if constexpr (k < 4) cs4[k & 1] = *(const LDS_AS f32x4*)(smem + L_CSL + k * 64 + g * 16);
            else cs4[k & 1][0] = *(const LDS_AS float*)(smem + L_CSL + k * 64 + (l & 15) * 4);
          }
          if constexpr ((s == ABS + 1 && n > 0) || (s == ABS + 2 && n == 0)) {
            constexpr int k = (n + 3) % 6;
            if constexpr (k < 4) {
#pragma unroll
              for (int a = 0; a < 2; ++a) acc[a][k] = __builtin_elementwise_fma(cs4[k & 1], f32x4{b2[a][0], b2[a][1], b2[a][0], b2[a][1]}, acc[a][k]);
            } else {
              f32x2 c2 = {cs4[k & 1][0], cs4[k & 1][0]};
              asm volatile("" : "+v"(c2));
#pragma unroll
              for (int a = 0; a < 2; ++a) acc[a][k] = __builtin_elementwise_fma(f32x4{c2[0], c2[1], c2[0], c2[1]}, __builtin_bit_cast(f32x4, bv[a]), acc[a][k]);
            }
          }
          if constexpr (IT == 0) {
            if constexpr (s == CSD && n < 3) fx[n] = frag0(SLOT(s >> 2), 3 * w + n);
            if constexpr (s == CSD + 1 && n == 1) {
              unsigned one2 = 0x3F803F80u;
              asm volatile("" : "+v"(one2));
              const u32x4 o4 = {one2, one2, one2, one2};
              const f32x4 z4 = {0.f, 0.f, 0.f, 0.f};
              sa = mfma16(fx[0], o4, z4); sb = mfma16(fx[1], o4, z4); sc = mfma16(fx[2], o4, z4);
            }
            if constexpr (s == CSD + 2 && n == 1) {
              const int c = l & 15;
              const f32x4 t0 = sa, t1 = sb, t2 = sc, t = c == 0 ? +t0 : c == 1 ? +t1 : +t2;
              if (c < 3) *(LDS_AS f32x4*)(smem + L_CSP + (w * 3 + c) * 64 + g * 16) = t;
              count_in(2);
            }
            if constexpr (s == CSD + 4 && n == 1) {
              if (w == 2) {
                qc = 0;
                count_wait(2, 8);
                const LDS_AS f32x2* cp = (const LDS_AS f32x2*)(smem + L_CSP) + min(l, 47);
                const f32x2 t = (cp[0] + cp[48]) + (cp[96] + cp[144]);
                if (l < 48) st16(u32x4{__builtin_bit_cast(unsigned, (float)t[0]), __builtin_bit_cast(unsigned, (float)t[1]), T[0], T[1]}, rs, (l >> 3) * 1024 + (l & 7) * 16, OFF_CS + h * 6144 + p * 128);
              }
            }
            if constexpr (s >= 29 && (n & 1)) keep[3 * (s - 29) + (n >> 1)] = frag0(SLOT(s - 24), 3 * w + (n >> 1));
            if constexpr (s >= TS0 && s < TS0 + TSN + TSD * (7 >> TSG) && n == 4) {
              if (tstep == s && ((TM0 >> w) & 1)) TOUCH(tsrc);
            }
            if constexpr (s == TEX && n == 5) {
              const GAS bf16* t2 = ((TM1 >> w) & 1) ? tsrc : (const GAS bf16*)x + p * 262144;
              TOUCH(t2);
            }
          } else {
            if constexpr (s == FSL - 2 && n == 4) ld4h<0>(kv, rs, (l & 15) * 4, OFF_KEY + (bid & 15) * 128);
            if constexpr (s == FSL && n == 4) {
              wait_vm<SINCE(FSL - 2)>();
              asm volatile("" : "+v"(kv)::"memory");
              xg = ffn_xoff(__builtin_amdgcn_readfirstlane(umax16(kv == POISON ? 0u : kv)), 0, w);
              ld4h<0>(kv, rs, (l & 15) * 4, OFF_KEY + (bid & 15) * 128);
              ld16h<0>(fx[0], rs, g * 16, xg); ld16h<64>(fx[1], rs, g * 16, xg); ld16h<128>(fx[2], rs, g * 16, xg); ld16h<192>(fx[3], rs, g * 16, xg);
            }
            if constexpr (s == FS0 && n == 1) {
              wait_vm<SINCE(FSL)>();
              asm volatile("" : "+v"(kv)::"memory");
              PINV4(fx);
              STAMPAT(52);
              fkey = key_wait(rs, T, l, 0, bid & 15, kv);
              STAMPAT(53);
              sel0 = key_row(fkey);
              aff = (int)(sel0 >> 8) == p;
            }
            if constexpr (s == FS0 && n == 2) {
              asm volatile("" : "=v"(sa), "=v"(sb));
              if (ffn_on) {
                if (xg != ffn_xoff(fkey, 0, w)) {
#pragma unroll
                  for (int i = 0; i < 4; ++i) fx[i] = ld16(rs, g * 16 + i * 64, ffn_xoff(fkey, 0, w));
                }
                ffn_gu(gv, uv, fx, sa, sb);
              }
            }
            if constexpr (s == FS0 && n == 3) {
              if (ffn_on) {
                ffn_gu_check(rs, T, ffn_xoff(fkey, 0, w), l, gv, uv, fx, sa, sb);
                ffn_gu_store(smem, l, w, sa, sb);
                count_in(1);
              }
            }
            if constexpr (s == FS0D) {
              if constexpr (n == 0) qc = __hip_atomic_load(qcnt + 1, __ATOMIC_RELAXED, __HIP_MEMORY_SCOPE_WORKGROUP);
              if constexpr (n == 1) {
                asm volatile("" : "=v"(dfr[0]), "=v"(dfr[1]), "=v"(dfr[2]), "=v"(dfr[3]), "=v"(dfr[4]), "=v"(dfr[5]), "=v"(dfr[6]), "=v"(dfr[7]), "=v"(hh), "=v"(sa));
                if (ffn_on) { count_wait(1, 8); ffn_d_read(smem, l, w, sa, dfr); }
              }
              if constexpr (n == 2) {
                if (ffn_on) {
                  for (int e = 0; e < 4; ++e) sa[e] = ffn_d_h(ffn_d_sum(sa[e]));
                  hh = u32x2{pack2(sa[0], sa[1]), pack2(sa[2], sa[3])};
                }
              }
              if constexpr (n == 3) {
                asm volatile("" : "=v"(sa), "=v"(sb));
                if (ffn_on) {
                  sa = f32x4{0.f, 0.f, 0.f, 0.f}; sb = sa;
#pragma unroll
                  for (int i = 0; i < 8; i += 2) { sa = ffn_d_mma(l, i, dfr[i], hh, sa); sb = ffn_d_mma(l, i + 1, dfr[i + 1], hh, sb); }
                }
              }
              if constexpr (n == 4) { if (ffn_on) ffn_d_store(rs, l, w, 0, slice, sa + sb); STAMPAT(54); }
            }
            if constexpr (s == FS1 && n == 4) {
              DEF4(fx);
              asm volatile("" : "=v"(cxn), "=v"(cx1));
              if (!ffn_on) {
                const unsigned G = xcd * 4 + bi - 28, co = (sel0 >> 7) * 2048 + G * 64, jj = (tid & 3) * 16 + (tid & 4) * 2;
#pragma unroll
                for (int k = 0; k < 4; ++k) ld16h<0>(fx[k], rs, min(tid + 512 * k, 1791) * 16, OFF_PART + G * 32768);
                ld8h(cxn, rs, jj, OFF_CA + co); ld8h(cx1, rs, jj, OFF_CB + co);
              }
            }
            if constexpr (s == FS1S && n == 2) {
              if (!ffn_on) {
                wait_vm<SINCE(FS1)>();
                PINV4(fx);
                asm volatile("" : "+v"(cxn), "+v"(cx1)::"memory");
                STAMPAT(55);
                red_sum(rs, smem, T, tid, w, 0, xcd * 4 + bi - 28, fkey, fx, cxn, cx1);
                STAMPAT(56);
                BAR();
                if (w == 0) {
                  const u32x2 ov = red_final(smem, l, fkey, cxn, cx1);
                  if (l < 8) st8(unpoison2(ov), rs, OFF_XNEW + ((xcd * 4 + bi - 28) * 8 + (l & 3) * 2 + (l >> 2)) * 8, 0);
                }
              }
            }
            if constexpr (s == FXA && n == 4) { ld16h<0>(fx[0], rs, g * 16, XP_OFF()); ld16h<64>(fx[1], rs, g * 16, XP_OFF()); ld16h<128>(fx[2], rs, g * 16, XP_OFF()); ld16h<192>(fx[3], rs, g * 16, XP_OFF()); }
            if constexpr (s == FXB) {
#define FX_FRAG(i) (*(const LDS_AS u32x4*)(size_t)(bo[0][0] + SLOT(5) * STAGE + (6 * (i) + min(w, 5)) * 1024))
              if constexpr (n == 0) { STAMPAT(58); df[0] = FX_FRAG(0); df[1] = FX_FRAG(1); }
              if constexpr (n == 1) {
                wait_vm<SINCE(FXA)>();
                PINV4(fx);
                sa = mfma16(df[0], fx[0], f32x4{0.f, 0.f, 0.f, 0.f}); sb = mfma16(df[1], fx[1], f32x4{0.f, 0.f, 0.f, 0.f});
                SB();
                df[0] = FX_FRAG(2); df[1] = FX_FRAG(3);
              }
              if constexpr (n == 2) { sa = mfma16(df[0], fx[2], sa); sb = mfma16(df[1], fx[3], sb); }
              if constexpr (n == 4) {
                const float z = sa[0] + sb[0];
                if (__any(z != z)) {
                  ffn_xwait(rs, T, XP_OFF(), l, fx);
                  f32x4 t0 = mfma16(FX_FRAG(0), fx[0], f32x4{0.f, 0.f, 0.f, 0.f});
                  t0 = mfma16(df[0], fx[2], t0);
                  f32x4 t1 = mfma16(FX_FRAG(1), fx[1], f32x4{0.f, 0.f, 0.f, 0.f});
                  sa = t0; sb = mfma16(df[1], fx[3], t1);
                }
                if (w < 6) dot_store(OFF_FX);
                STAMPAT(57);
              }
            }
            if constexpr (s == FXL && n == 4) {
              asm volatile("" : "=v"(fx[0]), "=v"(fx[1]));
              if (aff && w < 4) { ld16h<0>(fx[0], rs, l * 16, FP_OFF(0)); ld16h<0>(fx[1], rs, l * 16, FP_OFF(1)); }
            }
            if constexpr (s == FXS && n == 4) {
              if (aff && w < 4) {
                STAMPAT(59);
                wait_vm<0>();
                asm volatile("" : "+v"(fx[0]), "+v"(fx[1])::"memory");
                if (!FX_OK(2)) {
                  unsigned spins = 0;
                  do {
                    if (spin_fail(spins, rs, T)) break;
                    fx[0] = ld16(rs, l * 16, FP_OFF(0)); fx[1] = ld16(rs, l * 16, FP_OFF(1));
                  } while (!FX_OK(2));
                }
                STAMPAT(60);
                if (w == 3) {
                  f32x2 nx[8];
                  unpack8v(fx[0], nx); unpack8v(fx[1], nx + 4);
                  const f32x2 sm = ((nx[0] + nx[1]) + (nx[2] + nx[3])) + ((nx[4] + nx[5]) + (nx[6] + nx[7]));
                  const float mean = sum64(hadd(sm)) * (1.0f / 1024.0f);
                  const f32x2 m2 = {mean, mean};
                  f32x2 q = {0.f, 0.f};
#pragma unroll
                  for (int j = 0; j < 8; ++j) { const f32x2 d = nx[j] - m2; q = __builtin_elementwise_fma(d, d, q); }
                  const float rd = ln_rd(sum64(hadd(q)) * (1.0f / 1024.0f));
                  if (l == 0) *(LDS_AS f32x2*)(smem + L_FXL + 384) = f32x2{rd, -rbf1(mean)};
                } else {
                  tile_sum(fx[0], L_FXL);
                }
                count_in(3);
              }
            }
          }
        };
        static_for<32>([&](auto sc_) __attribute__((always_inline)) {
          constexpr int s = decltype(sc_)::value;
          if constexpr (s > 0) PIN_ACC();
          if constexpr (IT == 1) wait_vm<vm_at(IT, s, 0) - vm_a(IT, s)>();
          pin_w<s % WA>(af[s % WA][0], af[s % WA][1]);
          VSTEP(s);
          SB();
          static_for<6>([&](auto nc_) __attribute__((always_inline)) {
            constexpr int n = decltype(nc_)::value;
            if constexpr (s == 0) { acc[0][n] = f32x4{0.f, 0.f, 0.f, 0.f}; acc[1][n] = acc[0][n]; }
            constexpr bool on = s < QL || n >= 2;
            if constexpr (on) acc[0][n] = n < 4 ? mfma16(bfr[n].v, af[s % WA][0], acc[0][n]) : mfma16(af[s % WA][0], bfr[n].v, acc[0][n]);
            SB();
            if constexpr (n == 0) { QKV_RDB(s, 5); }
            else if constexpr (s + 1 < 32 && (s + 1 < QL || n > 2)) QKV_RDB(s + 1, n - 1);
            SB();
            if constexpr (on) acc[1][n] = n < 4 ? mfma16(bfr[n].v, af[s % WA][1], acc[1][n]) : mfma16(af[s % WA][1], bfr[n].v, acc[1][n]);
            SB();
            if constexpr (s + 1 < 32 && (s + 1 < QL || n >= 2)) QKV_RDA(s + 1, n);
            SB();
            gap(sc_, nc_);
            SB();
          });
        });
        PIN_ACC();
        VSTEP(32);
        STAMPAT(18);
        VSTEP(33);
        frag_t qw[32 - QL][2];
        static_for<32 - QL>([&](auto c_) __attribute__((always_inline)) {
          constexpr int s = QL + decltype(c_)::value;
          QKV_RDA(s, 0); QKV_RDB(s, 0); QKV_RDA(s, 1); QKV_RDB(s, 1);
          qw[s - QL][0] = bfr[0]; qw[s - QL][1] = bfr[1];
        });
        SB();
        const bool own = aff && (int)((sel0 >> 5) & 7) == rt;
        const bool m0 = !(sel0 & 16), mq = (l & 15) == (int)(sel0 & 15), mv = g == (int)((sel0 & 15) >> 2);
        auto patch_qk = [&](int n) __attribute__((always_inline)) {
          f32x4 t = *(const LDS_AS f32x4*)(smem + L_FXL + n * 64 + g * 16);
          const f32x4 c = *(const LDS_AS f32x4*)(smem + L_CSL + n * 64 + g * 16);
          const float nb = *(const LDS_AS float*)(smem + L_FXL + 388);
#pragma unroll
          for (int e = 0; e < 4; ++e) { t[e] = __builtin_fmaf(c[e], nb, t[e]); asm volatile("" : "+v"(t[e])); }
          acc[0][n] = (mq && m0) ? t : acc[0][n];
          acc[1][n] = (mq && !m0) ? t : acc[1][n];
        };
        if (own) {
          while (__hip_atomic_load(qcnt + 3, __ATOMIC_RELAXED, __HIP_MEMORY_SCOPE_WORKGROUP) < 4u) {}
          asm volatile("" ::: "memory");
          patch_qk(2); patch_qk(3);
          const f32x2 nab = *(const LDS_AS f32x2*)(smem + L_FXL + 384);
          const LDS_AS float* fl_ = (const LDS_AS float*)(smem + L_FXL + 256) + (l & 15);
          const LDS_AS float* cl_ = (const LDS_AS float*)(smem + L_CSL + 256) + (l & 15);
          const float t4 = __builtin_fmaf(cl_[0], nab[1], fl_[0]), t5 = __builtin_fmaf(cl_[16], nab[1], fl_[16]);
          const unsigned na = __builtin_bit_cast(unsigned, (float)nab[0]);
#pragma unroll
          for (int a = 0; a < 2; ++a) {
            const bool ma = a ? !m0 : m0;
            aq[a] = (mq && ma) ? na : aq[a];
#pragma unroll
            for (int e = 0; e < 4; ++e) {
              const bool me = mv && ma && e == (int)(sel0 & 3);
              acc[a][4][e] = me ? t4 : acc[a][4][e]; acc[a][5][e] = me ? t5 : acc[a][5][e];
              av[a][e] = me ? na : av[a][e];
            }
          }
        }
        constexpr float C2 = 1.4426950408889634f * 0.17677669529663687f;
#pragma unroll
        for (int a = 0; a < 2; ++a) {
          f32x2 a2 = {asf(aq[a]), asf(aq[a])};
          qs2[a] = f32x2{asf(aq[a]) * C2, asf(aq[a]) * C2};
          asm volatile("" : "+v"(a2), "+v"(qs2[a]));
          acc[a][2] *= f32x4{a2[0], a2[1], a2[0], a2[1]}; acc[a][3] *= f32x4{a2[0], a2[1], a2[0], a2[1]};
          acc[a][4] *= __builtin_bit_cast(f32x4, av[a]); acc[a][5] *= __builtin_bit_cast(f32x4, av[a]);
        }
        k0 = pack8(acc[0][2], acc[0][3]); k1 = pack8(acc[1][2], acc[1][3]); v0 = pack8(acc[0][4], acc[1][4]); v1 = pack8(acc[0][5], acc[1][5]);
        const unsigned kvo = OFF_KV + IT * (8u << 20) + (p * 32 + h) * 32768 + rt * 4096;
        if (p < 7) { st16(k0, rs, l * 16, kvo); st16(k1, rs, l * 16 + 1024, kvo); st16(v0, rs, l * 16 + 2048, kvo); st16(v1, rs, l * 16 + 3072, kvo); }
        char* ownp = smem + (8 + rt) * 2048 + l * 16;
        *(u32x4*)(ownp + L_K) = k0;
        if (rt < 7) *(u32x4*)(ownp + L_K + 1024) = k1;
        *(u32x4*)(ownp + L_V) = v0; *(u32x4*)(ownp + L_V + 1024) = v1;
        SB();
        static_for<32 - QL>([&](auto c_) __attribute__((always_inline)) {
          constexpr int s = QL + decltype(c_)::value;
#pragma unroll
          for (int n = 0; n < 2; ++n) { acc[0][n] = mfma16(qw[s - QL][n].v, af[s % WA][0], acc[0][n]); acc[1][n] = mfma16(qw[s - QL][n].v, af[s % WA][1], acc[1][n]); }
        });
        if (own) { patch_qk(0); patch_qk(1); }
      };
      qkv_loop(it_c);
    }
#undef A_LOAD1
#undef QKV_RDA
#undef QKV_RDB
#undef QKV_BO
    STAMPAT(2); STAMPAT(21);
    {
      IDS();
      const GAS bf16 *pwo, *pwg, *pwu;
      asm volatile("s_load_dwordx2 %0, %3, 24\n\ts_load_dwordx2 %1, %3, 40\n\ts_load_dwordx2 %2, %3, 48" : "=&s"(pwo), "=&s"(pwg), "=&s"(pwu) : "s"(ka));
      const int ql = l & 15;
      const f32x4 z4 = {0.f, 0.f, 0.f, 0.f}, ninf = {-INFINITY, -INFINITY, -INFINITY, -INFINITY};
      const unsigned kvo = OFF_KV + it * (8u << 20) + (p * 32 + h) * 32768 + rt * 4096;
      LDS_AS unsigned* rdy = (LDS_AS unsigned*)(smem + L_RDY);
      const unsigned rtag = T[0] + it;
      u32x4 qf[2];
      f32x4 cn[2], oa[2][2], sum[2], S[2][2];
      unsigned one2 = 0x3F803F80u;
      asm volatile("" : "+v"(one2));
      const u32x4 ones = {one2, one2, one2, one2};
      auto ex8 = [&](f32x4 a, f32x4 b) __attribute__((always_inline)) {
        return u32x4{pack2(__builtin_amdgcn_exp2f(a[0]), __builtin_amdgcn_exp2f(a[1])), pack2(__builtin_amdgcn_exp2f(a[2]), __builtin_amdgcn_exp2f(a[3])),
                     pack2(__builtin_amdgcn_exp2f(b[0]), __builtin_amdgcn_exp2f(b[1])), pack2(__builtin_amdgcn_exp2f(b[2]), __builtin_amdgcn_exp2f(b[3]))};
      };
      {
#pragma unroll
        for (int a = 0; a < 2; ++a) {
          const f32x4 c4 = {qs2[a][0], qs2[a][1], qs2[a][0], qs2[a][1]};
          qf[a] = pack8(acc[a][0] * c4, acc[a][1] * c4);
        }
        f32x4 mk;
#pragma unroll
        for (int e = 0; e < 4; ++e) mk[e] = (4 * g + e <= ql) ? 0.f : -INFINITY;
        const f32x4 d0 = mfma16(k0, qf[0], mk), d1 = mfma16(k1, qf[1], mk), d2 = mfma16(k0, qf[1], z4);
        const float m0 = -max_x32(max_x16(fmaxf(fmaxf(d0[0], d0[1]), fmaxf(d0[2], d0[3]))));
        const float m1 = -max_x32(max_x16(fmaxf(fmaxf(d1[0], d1[1]), fmaxf(d1[2], d1[3]))));
        cn[0] = f32x4{m0, m0, m0, m0}; cn[1] = f32x4{m1, m1, m1, m1};
        asm volatile("" : "+v"(cn[0]), "+v"(cn[1]));
        const f32x4 e0 = d0 + cn[0];
        const u32x4 p0 = {pack2(__builtin_amdgcn_exp2f(e0[0]), __builtin_amdgcn_exp2f(e0[1])), pack2(__builtin_amdgcn_exp2f(e0[2]), __builtin_amdgcn_exp2f(e0[3])), 0u, 0u};
        const u32x4 p1 = ex8(d2 + cn[1], d1 + cn[1]);
        oa[0][0] = mfma16(v0, p0, z4); oa[0][1] = mfma16(v1, p0, z4); sum[0] = mfma16(ones, p0, z4);
        oa[1][0] = mfma16(v0, p1, z4); oa[1][1] = mfma16(v1, p1, z4); sum[1] = mfma16(ones, p1, z4);
      }
      const char* kb_ = smem + L_K + l * 16;
      const char* vb_ = smem + L_V + l * 16;
      const unsigned wov = (l >> 3) * 2048 + (((((l & 7) >> 1) ^ ((l >> 4) & 3)) * 2 + (l & 1)) << 4), fnv = (l >> 1) * (F_ * 2) + (l & 1) * 16;
      const unsigned pp = __builtin_amdgcn_readfirstlane(p > 0 ? 32 : 0), pvo = kvo - pp * 32768;
      const unsigned pfo = __builtin_amdgcn_readfirstlane(F_KV + (it * 256 + p * 32 + h - pp) * 128 + rt * 8);
      asm volatile("" : "+v"(oa[0][0]), "+v"(oa[0][1]), "+v"(oa[1][0]), "+v"(oa[1][1]), "+v"(sum[0]), "+v"(sum[1]));
      DRAIN();
      if (l == 0) st8(T, rs, rt * 8, F_KV + (it * 256 + p * 32 + h) * 128);
      STAMPAT(32);
      BAR();
      asm volatile("" : "+s"(pwo), "+s"(pwg), "+s"(pwu));
      STAMPAT(33);
      WO_PIECE(15); WO_PIECE(14);
#define KV_FETCH()                                                                                \
  {                                                                                               \
    dma16s<0>(rs, l * 16, pvo, lbase + L_K + rt * 2048); dma16s<1024>(rs, l * 16, pvo, lbase + L_K + rt * 2048);               \
    dma16s<0>(rs, l * 16, pvo + 2048, lbase + L_V + rt * 2048); dma16s<1024>(rs, l * 16, pvo + 2048, lbase + L_V + rt * 2048); \
  }
#define LDS16(a_) (*(const LDS_AS u32x4*)(size_t)(a_))
      u32x4 kf[2], vf[2];
      int nw = 0;
      auto body = [&](unsigned kad, unsigned vad) __attribute__((always_inline)) {
        u32x4 p0 = ex8(S[0][0], S[0][1]);
        asm volatile("" : "+v"(p0));
        SB();
        S[0][0] = mfma16(kf[0], qf[0], cn[0]); S[0][1] = mfma16(kf[1], qf[0], cn[0]);
        SB();
        oa[0][0] = mfma16(vf[0], p0, oa[0][0]); oa[0][1] = mfma16(vf[1], p0, oa[0][1]); sum[0] = mfma16(ones, p0, sum[0]);
        SB();
        u32x4 p1 = ex8(S[1][0], S[1][1]);
        asm volatile("" : "+v"(p1));
        SB();
        S[1][0] = mfma16(kf[0], qf[1], cn[1]); S[1][1] = mfma16(kf[1], qf[1], cn[1]);
        SB();
        kf[0] = LDS16(kad); kf[1] = LDS16(kad + 1024);
        SB();
        oa[1][0] = mfma16(vf[0], p1, oa[1][0]); oa[1][1] = mfma16(vf[1], p1, oa[1][1]);
        SB();
        vf[0] = LDS16(vad + L_V); vf[1] = LDS16(vad + L_V + 1024);
        SB();
        sum[1] = mfma16(ones, p1, sum[1]);
      };
      auto last = [&]() __attribute__((always_inline)) {
        const u32x4 p0 = ex8(S[0][0], S[0][1]), p1 = ex8(S[1][0], S[1][1]);
        oa[0][0] = mfma16(vf[0], p0, oa[0][0]); oa[0][1] = mfma16(vf[1], p0, oa[0][1]); sum[0] = mfma16(ones, p0, sum[0]);
        oa[1][0] = mfma16(vf[0], p1, oa[1][0]); oa[1][1] = mfma16(vf[1], p1, oa[1][1]); sum[1] = mfma16(ones, p1, sum[1]);
      };
      auto piece = [&]() __attribute__((always_inline)) {
        if constexpr (it == 1) { WO_PIECE(13 - nw); }
        else if (ffn_on) { FFN_DMA(nw); }
        ++nw;
      };
      if (rt > 0) {
        unsigned ad = lbase + l * 16 + (7 + rt) * 2048;
        kf[0] = LDS16(ad); kf[1] = LDS16(ad + 1024); vf[0] = LDS16(ad + L_V); vf[1] = LDS16(ad + L_V + 1024);
        S[0][0] = mfma16(kf[0], qf[0], cn[0]); S[0][1] = mfma16(kf[1], qf[0], cn[0]); S[1][0] = mfma16(kf[0], qf[1], cn[1]); S[1][1] = mfma16(kf[1], qf[1], cn[1]);
        SB();
        ad -= 2048;
        kf[0] = LDS16(ad); kf[1] = LDS16(ad + 1024);
#pragma unroll 1
        for (int i = rt - 1; i > 0; --i) {
          body(ad - 2048, ad);
          piece();
          SB();
          ad -= 2048;
        }
        piece();
        last();
      }
      STAMPAT(26);
      {
        poll_flags(rs, 0, pfo, T);
        KV_FETCH();
        if (p == 0) { cn[0] = ninf; cn[1] = ninf; }
        f32x4 mk;
#pragma unroll
        for (int e = 0; e < 4; ++e) mk[e] = (4 * g + e > ql) ? 0.f : -INFINITY;
        const f32x4 cm0 = cn[0] + mk, cm1 = cn[1] + mk;
        DRAIN();
        if (l == 0) rdy[rt] = rtag;
        STAMPAT(3);
        if constexpr (it == 1) { WO_PIECE(6); WO_PIECE(5); WO_PIECE(4); }
        else {
          WO_PIECE(13); WO_PIECE(12); if (ffn_on) FFN_DMA(7);
#pragma unroll
          for (int i = 0; i < 3; ++i) st16(keep[i], rs, l * 16 + i * 1024, OFF_WI + (h * 8 + ((ROT0 + 5) & 7)) * STAGE + w * 3072);
          unsigned pz1 = POISON;
          asm volatile("" : "+v"(pz1));
          const u32x4 pz = {pz1, pz1, pz1, pz1};
          const unsigned prb = 2 * p + (h >> 4), pct = h & 15;
          st16(pz, rs, OFF_PART + (tid >> 8) * (1u << 20) + bid * 4096 + (tid & 255) * 16, 0);
          if (w < 4) st16(pz, rs, OFF_STAT + (tid >> 7) * 524288 + (prb * 16 + pct) * 2048 + (tid & 127) * 16, 0);
          if (w == 4 && l < 24) st16(pz, rs, (l < 8 ? OFF_CA + 32768 : l < 16 ? OFF_CB : OFF_CB + 32768) + prb * 2048 + pct * 128 + (l & 7) * 16, 0);
          if (w == 7 && l < 24) st16(pz, rs, OFF_FX + ((h * 6 + (l >> 2)) * 8 + p) * 64 + (l & 3) * 16, 0);
          if (w == 6 && l < 3) st4(pz1, rs, l < 2 ? OFF_XNEW + bid * 8 + l * 4 : OFF_KEY + (16 + pct) * 128 + prb * 4, 0);
        }

        int have = rt;
        auto need = [&](int gq) __attribute__((always_inline)) {
          if (__builtin_expect(gq > have && gq < 8, 0)) {
            do {
              const unsigned t = __hip_atomic_load(rdy + (l & 7), __ATOMIC_RELAXED, __HIP_MEMORY_SCOPE_WORKGROUP);
              have = rt - 1 + __builtin_ctz(~((unsigned)__builtin_amdgcn_ballot_w64(t == rtag) & 0xffu) >> rt);
            } while (gq > have);
            asm volatile("" ::: "memory");
          }
        };
        unsigned ad = lbase + l * 16 + rt * 2048;
        kf[0] = LDS16(ad); kf[1] = LDS16(ad + 1024); vf[0] = LDS16(ad + L_V); vf[1] = LDS16(ad + L_V + 1024);
        S[0][0] = mfma16(kf[0], qf[0], cm0); S[0][1] = mfma16(kf[1], qf[0], cn[0]); S[1][0] = ninf; S[1][1] = mfma16(kf[1], qf[1], cm1);
        SB();
        ad += 2048;
        need(rt + 1);
        kf[0] = LDS16(ad); kf[1] = LDS16(ad + 1024);
#pragma unroll 1
        for (int gq = rt + 1; gq < 8; ++gq) {
          need(gq + 1);
          body(ad + 2048, ad);
          piece();
          SB();
          ad += 2048;
        }
        last();
      }
      STAMPAT(25);
      {
        float big = 0.f;
#pragma unroll
        for (int a = 0; a < 2; ++a)
#pragma unroll
          for (int d = 0; d < 2; ++d)
            big = __builtin_elementwise_maximum(__builtin_elementwise_maximum(big, __builtin_elementwise_maximum(fabsf(oa[a][d][0]), fabsf(oa[a][d][1]))),
                                                __builtin_elementwise_maximum(fabsf(oa[a][d][2]), fabsf(oa[a][d][3])));
        bool ok = sum[0][0] >= 0.5f && sum[0][0] < 0x1p64f && sum[1][0] >= 0.5f && sum[1][0] < 0x1p64f && big < INFINITY;
        if (!__all(ok)) {
#pragma unroll
          for (int a = 0; a < 2; ++a) {
            const int qs = 2 * rt + a, qq = 256 + 16 * qs + ql, klo = max(qq - 255, p > 0 ? 0 : 256);
            auto score = [&](int t) __attribute__((always_inline)) {
              f32x4 s = mfma16(t == 31 ? k1 : *(const u32x4*)(kb_ + t * 1024), qf[a], z4);
#pragma unroll
              for (int e = 0; e < 4; ++e) { const int kk = 16 * t + 4 * g + e; s[e] = (kk <= qq && kk >= klo) ? s[e] : -INFINITY; }
              return s;
            };
            float mx = -INFINITY;
#pragma unroll 1
            for (int t = qs; t <= qs + 16; ++t) { const f32x4 s = score(t); mx = fmaxf(fmaxf(mx, fmaxf(s[0], s[1])), fmaxf(s[2], s[3])); }
            mx = max_x32(max_x16(mx));
            oa[a][0] = z4; oa[a][1] = z4; sum[a] = z4;
#pragma unroll 1
            for (int G = qs >> 1; G <= (qs + 16) >> 1; ++G) {
              const f32x4 s0 = score(2 * G), s1 = score(2 * G + 1);
              f32x4 e0, e1;
#pragma unroll
              for (int e = 0; e < 4; ++e) { e0[e] = __builtin_amdgcn_exp2f(s0[e] - mx); e1[e] = __builtin_amdgcn_exp2f(s1[e] - mx); }
              const u32x4 pf = pack8(e0, e1);
              oa[a][0] = mfma16(*(const u32x4*)(vb_ + G * 2048), pf, oa[a][0]);
              oa[a][1] = mfma16(*(const u32x4*)(vb_ + G * 2048 + 1024), pf, oa[a][1]);
              sum[a] = mfma16(ones, pf, sum[a]);
            }
          }
        }
      }
#pragma unroll
      for (int a = 0; a < 2; ++a) {
        const float inv = __builtin_amdgcn_rcpf(sum[a][0]);
        oa[a][0] *= inv; oa[a][1] *= inv;
      }
      const unsigned oo = OFF_O + it * (4u << 20) + ((p * 8 + rt) * 64 + h * 2) * 1024;
      st16(pack8(oa[0][0], oa[0][1]), rs, l * 16, oo);
      st16(pack8(oa[1][0], oa[1][1]), rs, l * 16 + 1024, oo);
      STAMPAT(4);
      DRAIN();
      BAR();
      if (tid == 0) st8(T, rs, h * 8, F_O + (it * 8 + p) * 256);
      if constexpr (it == 0) {
        DEF4(gv); DEF4(uv);
        if (ffn_on) {
          const unsigned ro = 65536 + w * 1024 + (4 * g + ((l & 15) >> 2)) * 32 + (l & 3) * 8;
#pragma unroll
          for (int i = 0; i < 4; ++i) {
            frag_t f;
            f.hf.a = trread(smem, ro + i * 16384); f.hf.b = trread(smem, ro + i * 16384 + 512);
            gv[i] = f.v;
            f.hf.a = trread(smem, ro + i * 16384 + 8192); f.hf.b = trread(smem, ro + i * 16384 + 8704);
            uv[i] = f.v;
          }
        }
        PINV4(gv); PINV4(uv);
        WO_PIECE(11); WO_PIECE(10); WO_PIECE(9); WO_PIECE(8); WO_PIECE(7); WO_PIECE(6); WO_PIECE(5); WO_PIECE(4);
      }
      WO_PIECE(3); WO_PIECE(2); WO_PIECE(1); WO_PIECE(0);
    }
    STAMPAT(5);
    IDS();
    ARGP(pwr, 4); ARGP(pwd, 7); ARGP(pout, 8); ARGP(px, 0);
    const int kh = w >> 2, rt2 = rb * 4 + (w & 3);
    const unsigned R = rt2 * 32 + kh * 16 + (l & 15);
    const unsigned to = (rt2 * 64 + ct * 4 + kh) * 1024 + l * 16;
    const unsigned rmo = ct * 64 + ((g & 1) ? 12 + 4 * g : 4 * g);
    f32x2 wv[8];
    {
      u32x4 wa[2], wrp[2], res[2];
#pragma unroll
      for (int j = 0; j < 2; ++j) {
        wrp[j] = *(const GAS u32x4*)(pwr + rmo + 32 * j);
        wa[j] = *(const GAS u32x4*)(pwr + l * 8 + j * 512);
      }
      {
        f32x2 t[8];
        unpack8v(wa[0], t); unpack8v(wa[1], t + 4);
        const f32x2 s = ((t[0] + t[1]) + (t[2] + t[3])) + ((t[4] + t[5]) + (t[6] + t[7]));
        wsum = sum64(hadd(s));
        unpack8v(trade16(wrp[0]), wv); unpack8v(trade16(wrp[1]), wv + 4);
        asm volatile("" : "+v"(wsum), "+v"(wv[0]), "+v"(wv[1]), "+v"(wv[2]), "+v"(wv[3]), "+v"(wv[4]), "+v"(wv[5]), "+v"(wv[6]), "+v"(wv[7]));
      }
      unsigned bt[4];
#pragma unroll
      for (int n = 0; n < 4; ++n) {
        bt[n] = lbase + 32768 + 65536 + kh * 4096 + (4 * g + ((l & 15) >> 2)) * 128 + (l & 3) * 8 + ((n ^ (2 * (g & 1) + ((l & 15) >> 3))) << 5);
        asm volatile("" : "+v"(bt[n]));
      }
      if (w == 0) poll_flags(rs, (l & 31) * 8, F_O + (it * 8 + (rb >> 1)) * 256, T);
      else if (it == 0 && w < 3) poll_flags2(rs, F_XN + (((w - 1) * 4 + g) * RX + bi % RX) * 256 + (l & 15) * 16, T);
      asm volatile("s_barrier" ::: "memory");
      STAMPAT(6);
      const unsigned abase = OFF_O + it * (4u << 20) + (rt2 * 64 + 32 * kh) * 1024;
      const unsigned av0 = l * 16 + kh * 1024, av1 = l * 16 + (kh ^ 1) * 1024;
      u32x4 af[WB][2];
#define O_LOAD(s_) { af[(s_) % WB][0] = ld16(rs, av0 + (~(s_) & 1) * 2048, abase + ((15 - (s_)) >> 1) * 4096); af[(s_) % WB][1] = ld16(rs, av1 + (~(s_) & 1) * 2048, abase + ((15 - (s_)) >> 1) * 4096); }
#pragma unroll
      for (int s = 0; s < WR; ++s) O_LOAD(s);
#pragma unroll
      for (int j = 0; j < 2; ++j) {
        if constexpr (it == 0) res[j] = __builtin_amdgcn_raw_buffer_load_b128(RSRC(px, 2048 * 1024 * 2), (R * 1024 + rmo + 32 * j) * 2, 0, 0);
        else res[j] = ld16(rs, OFF_XNEW + ((2 * ct + j) * 4 + g) * 16, 0);
      }
      SB();
      f32x4 ac[2][4];
#pragma unroll
      for (int a = 0; a < 2; ++a)
#pragma unroll
        for (int n = 0; n < 4; ++n) ac[a][n] = f32x4{0.f, 0.f, 0.f, 0.f};
      u32x4 bfr[2][4];
#define WO_RD(buf, s, n)                                                                             \
  {                                                                                                  \
    const unsigned o_ = bt[n] + ((15 - (s)) & 7) * 8192;                                             \
    frag_t f_;                                                                                       \
    f_.hf.a = trabs(o_); f_.hf.b = trabs(o_ + 2048);                                                 \
    bfr[buf][n] = f_.v;                                                                              \
  }
#pragma unroll
      for (int n = 0; n < 4; ++n) WO_RD(0, 0, n);
#pragma unroll
      for (int s = 0; s < 16; ++s) {
        const int cur = s & 1;
        asm volatile("" : "+{v[176:179]}"(ac[0][0]), "+{v[180:183]}"(ac[0][1]), "+{v[184:187]}"(ac[0][2]), "+{v[188:191]}"(ac[0][3]),
                          "+{v[192:195]}"(ac[1][0]), "+{v[196:199]}"(ac[1][1]), "+{v[200:203]}"(ac[1][2]), "+{v[204:207]}"(ac[1][3]));
        if (s == (it == 0 ? 3 : 11)) asm volatile("s_barrier" ::: "memory");
        if (s == 7) {
#pragma unroll
          for (int n = 0; n < 4; ++n) { bt[n] -= 65536; asm volatile("" : "+v"(bt[n])); }
        }
        if (s < WB - WR) O_LOAD(WR + 2 * s < 16 ? WR + 2 * s : 0);
        SB();
#pragma unroll
        for (int n = 0; n < 4; ++n) {
          ac[0][n] = mfma16(bfr[cur][n], af[s % WB][0], ac[0][n]);
          ac[1][n] = mfma16(bfr[cur][n], af[s % WB][1], ac[1][n]);
          if (s + 1 < 16) WO_RD(cur ^ 1, s + 1, n);
          if (s == 9) {
            if (it == 1 && n == 0) { res[0] = R == sel0 ? res[0] : x2p[0]; res[1] = R == sel0 ? res[1] : x2p[1]; }
            if (it == 0 && n == 0) { res[0] = trade16(res[0]); res[1] = trade16(res[1]); }
            v[2 * n] = f32x2{lo16(res[n >> 1][2 * (n & 1)]), hi16(res[n >> 1][2 * (n & 1)])};
            v[2 * n + 1] = f32x2{lo16(res[n >> 1][2 * (n & 1) + 1]), hi16(res[n >> 1][2 * (n & 1) + 1])};
            asm volatile("" : "+v"(v[2 * n]), "+v"(v[2 * n + 1]));
          }
          SB();
        }
        if (s < WB - WR) { O_LOAD(WR + 2 * s + 1); }
        else if (s + WB < 16) { O_LOAD(s + WB); }
        SB();
      }
#undef WO_RD
#undef O_LOAD
      STAMPAT(7);
#define XCH_AT(v) ((v) == 7 ? 155648 : (v) * 4096)
#pragma unroll
      for (int n = 0; n < 4; ++n) *(f32x4*)(smem + XCH_AT(w) + n * 1024 + l * 16) = ac[1][n];
#pragma unroll
      for (int n = 0; n < 4; ++n) { v[2 * n] += f32x2{ac[0][n][0], ac[0][n][1]}; v[2 * n + 1] += f32x2{ac[0][n][2], ac[0][n][3]}; }
      BAR();
#pragma unroll
      for (int n = 0; n < 4; ++n) {
        const f32x4 o = *(const f32x4*)(smem + XCH_AT(w ^ 4) + n * 1024 + l * 16);
        v[2 * n] += f32x2{o[0], o[1]}; v[2 * n + 1] += f32x2{o[2], o[3]};
      }
    }
    {
      if (it == 0) { st16(pack8v(v), rs, to, OFF_XN + (4u << 20)); st16(pack8v(v + 4), rs, to + 2048, OFF_XN + (4u << 20)); }
      SB();
      const f32x2 s = ((v[0] + v[1]) + (v[2] + v[3])) + ((v[4] + v[5]) + (v[6] + v[7]));
      f32x2 dp = v[0] * wv[0], q = v[0] * v[0];
#pragma unroll
      for (int j = 1; j < 8; ++j) { dp = __builtin_elementwise_fma(v[j], wv[j], dp); q = __builtin_elementwise_fma(v[j], v[j], q); }
      float mt = hadd(s), qq = hadd(q), dot = hadd(dp);
      add_g3(mt, qq, dot);
      const unsigned so = OFF_STAT + it * 524288 + rb * 32768, lrow = R & 127;
      if (g == 0) st16(unpoison4(__builtin_bit_cast(u32x4, f32x4{mt, qq, dot, 0.f})), rs, ct * 2048 + lrow * 16, so);
      SB();
      if (it == 0) wait_vm<1>();
      if constexpr (it == 0) {
        volatile LDS_AS u32x2* in = (volatile LDS_AS u32x2*)(smem + L_XC);
        if (l == 0) in[w] = T;
        const u32x2 t = in[l & 7];
        if (__all(t[0] == T[0] && t[1] == T[1]) && l < RX) st8(T, rs, l * 256 + ((rb & 1) * 16 + ct) * 8, F_X1 + (it * 8 + (rb >> 1)) * RX * 256);
      }
      STAMPAT(8);
      if (ffn_on) { WD_DMA(); }
      if (it == 0) {
#pragma unroll
        for (int j = 0; j < 9; ++j) *(u32x4*)(smem + L_RING + (j / 3) * STAGE + (w * 3 + j % 3) * 1024 + l * 16) = keep[j];
      }
      STAMPAT(16);
      if constexpr (it == 0) {
        if (w == 0) poll_flags(rs, ((l & 2) * 8 + ((ROT0 + 5) & 7) * 2 + (l & 1)) * 8, F_X1 + (p * RX + (xcd >> 1) % RX) * 256, T);
        if (w == 1) poll_flags(rs, (l & 7) * 256 + h * 8, F_O, T);
        if (tid == 192) *(LDS_AS u32x4*)(smem + L_QC) = u32x4{0u, 0u, 0u, 0u};
        BAR();
        PRE_LOAD(1, ROT0 + 5, 0, 4);
        STAMPAT(9);
      } else {
        __builtin_amdgcn_s_sleep(SDIR);
        u32x4 sp[4];
#pragma unroll
        for (int i = 0; i < 4; ++i) sp[i] = ld16(rs, (4 * g + i) * 2048 + lrow * 16, so);
        const RowFin r = fin_key(rs, T, g * 8192 + lrow * 16, so, smem + L_XC, l, w, R, wsum, sp);
        STAMPAT(9);
        BAR();
        fin_cand(rs, smem + L_XC, 1, g, rb, ct, r, v);
        STAMPAT(10);
        fin_x2(r, v);
      }
    }
    STAMPAT(11);
    if (it == 1) {
      const u32x4 op0 = trade16(pack8v(v)), op1 = trade16(pack8v(v + 4));
#define OUT_STORE()                                                                                 \
  if (R != key_row(key)) {                                                                          \
    __builtin_amdgcn_raw_buffer_store_b128(op0, rout, (R * 1024 + rmo) * 2, 0, SC1);                  \
    __builtin_amdgcn_raw_buffer_store_b128(op1, rout, (R * 1024 + rmo + 32) * 2, 0, SC1);             \
  }
      u32x4 xv[4];
      unsigned guess = 0;
      {
        __builtin_amdgcn_s_sleep(GSL);
        const unsigned k_ = key_load(rs, l, 1, bid & 15);
        guess = ffn_xoff(__builtin_amdgcn_readfirstlane(umax16(k_ == POISON ? 0u : k_)), 1, w);
        ld16h<0>(xv[0], rs, g * 16, guess); ld16h<64>(xv[1], rs, g * 16, guess); ld16h<128>(xv[2], rs, g * 16, guess); ld16h<192>(xv[3], rs, g * 16, guess);
      }
      if (w == 0) {
        const unsigned k_ = key_wait(rs, T, l, 1, bid & 15, key_load(rs, l, 1, bid & 15));
        if (l == 0) *(LDS_AS unsigned*)(smem + L_SKEY + 32) = k_;
      }
      DRAIN();
      PINV4(xv);
      BAR();
      const unsigned key = __builtin_amdgcn_readfirstlane(*(LDS_AS unsigned*)(smem + L_SKEY + 32));
      STAMPAT(12);
      if (ffn_on) {
        if (guess != ffn_xoff(key, 1, w)) {
#pragma unroll
          for (int i = 0; i < 4; ++i) xv[i] = ld16(rs, g * 16 + i * 64, ffn_xoff(key, 1, w));
        }
        f32x4 ga, ua;
        ffn_gu(gv, uv, xv, ga, ua);
        ffn_gu_check(rs, T, ffn_xoff(key, 1, w), l, gv, uv, xv, ga, ua);
        ffn_gu_store(smem, l, w, ga, ua);
        BAR();
        f32x4 t, ya[2] = {{0.f, 0.f, 0.f, 0.f}, {0.f, 0.f, 0.f, 0.f}};
        s16x4 dfr[8];
        ffn_d_read(smem, l, w, t, dfr);
#pragma unroll
        for (int j = 0; j < 4; ++j) t[j] = ffn_d_h(ffn_d_sum(t[j]));
        const u32x2 hh = {pack2(t[0], t[1]), pack2(t[2], t[3])};
#pragma unroll
        for (int i = 0; i < 8; ++i) ya[i & 1] = ffn_d_mma(l, i, dfr[i], hh, ya[i & 1]);
        ffn_d_store(rs, l, w, 1, slice, ya[0] + ya[1]);
        SB();
        OUT_STORE();
        STAMPAT(13);
      } else {
        OUT_STORE();
        const unsigned G = xcd * 4 + bi - 28;
        if (G == 0 && tid == 0) st8(T, rs, F_EP, 0);
        u32x4 pv[4];
        u32x2 cxn, cx1;
        __builtin_amdgcn_s_sleep(RSL);
        red_load(rs, tid, 1, G, key, pv, cxn, cx1);
        red_sum(rs, smem, T, tid, w, 1, G, key, pv, cxn, cx1);
        BAR();
        if (w == 0) {
          const u32x2 ov = red_final(smem, l, key, cxn, cx1);
          if (l < 8) __builtin_amdgcn_raw_buffer_store_b64(ov, rout, (key_row(key) * 1024 + G * 32 + l * 4) * 2, 0, 0);
        }
      }
#undef OUT_STORE
    }
    STAMPAT(15); STAMPAT(23);
  };
  layer(std::integral_constant<int, 0>{});
  layer(std::integral_constant<int, 1>{});
}

extern "C" void kernel_launch(void* const* d_in, const int* in_sizes, int n_in, void* d_out, int out_size,
                              void* d_ws, size_t ws_size, hipStream_t stream) {
  (void)hipFuncSetAttribute((const void*)mega_kernel, hipFuncAttributeMaxDynamicSharedMemorySize, LDS_BYTES);
  mega_kernel<<<256, 512, LDS_BYTES, stream>>>((const bf16*)d_in[0], (char*)d_ws, (const bf16*)d_in[1], (const bf16*)d_in[2], (const bf16*)d_in[4],
                                               (const bf16*)d_in[5], (const bf16*)d_in[6], (const bf16*)d_in[7], (bf16*)d_out);
}
```

```cpp
#include <hip/hip_runtime.h>
#include <stdint.h>
#include <utility>

typedef __bf16 bf16;
typedef __bf16 bf16x8 __attribute__((ext_vector_type(8)));
typedef __bf16 bf16x2 __attribute__((ext_vector_type(2)));
typedef float f32x4 __attribute__((ext_vector_type(4)));
typedef float f32x2 __attribute__((ext_vector_type(2)));
typedef short s16x4 __attribute__((ext_vector_type(4)));
typedef unsigned int u32x4 __attribute__((ext_vector_type(4)));
typedef unsigned int u32x2 __attribute__((ext_vector_type(2)));
#define LDS_AS __attribute__((address_space(3)))
#define GAS __attribute__((address_space(1)))
typedef GAS unsigned gu32;
typedef GAS unsigned long long gu64;
#define RLX_AGENT __ATOMIC_RELAXED, __HIP_MEMORY_SCOPE_AGENT
typedef __amdgpu_buffer_rsrc_t rsrc_t;

constexpr int F_ = 3584;
constexpr int SC1 = 16;

constexpr int RX = 4;
constexpr unsigned F_EP = 0, F_TMO = 128;
constexpr unsigned F_XN = 4096;
#define FLG 256, 1024, 8, 64, 4, 256, 4
constexpr unsigned FLV[7] = {FLG};
constexpr unsigned SO = FLV[0], SKV = FLV[1], SKW = FLV[2], SX1 = FLV[3], RX1 = FLV[4], SKEY = FLV[5], RKEY = FLV[6];
constexpr unsigned F_O = 8u << 20;
constexpr unsigned F_KV = F_O + 262144;
constexpr unsigned F_X1 = 9u << 20;
constexpr unsigned OFF_KEY = 10u << 20;
#define KREP() (RKEY == 16 ? bid & 15 : (bid & 7) * RKEY / 8)
#define KEY_AT(L, rep) (OFF_KEY + ((L) * RKEY + (rep)) * (SKEY < 8 ? 128 : 16 * SKEY))
constexpr unsigned OFF_AB = 2u << 20;
constexpr unsigned OFF_XNEW = 262144;
constexpr unsigned OFF_CS = OFF_AB + 65536;
constexpr unsigned OFF_FX = 393216;
constexpr unsigned OFF_CA = 524288;
constexpr unsigned OFF_CB = 655360;
constexpr unsigned OFF_STAT = 1u << 20;
constexpr unsigned OFF_PART = 3u << 20;
constexpr unsigned OFF_STAMP = 6u << 20;
constexpr unsigned OFF_XN = 8u << 20;
constexpr unsigned OFF_O = 16u << 20;
constexpr unsigned OFF_KV = 32u << 20;
constexpr unsigned OFF_WI = 52u << 20;
constexpr unsigned WS_BYTES = 58u << 20;
constexpr unsigned POISON = 0xFFFFFFFFu;

constexpr int L_ABW = 16640, L_CSP = 17408, L_CSL = 31872, L_FXL = 32256, L_K = 0, L_V = 32768, L_RING = 65536, L_XCH = 0, L_DN = 32768, L_RDY = 31744, L_QC = 31808, LDS_BYTES = 163840;
constexpr int STAGE = 24576;
#define SLOT(t) ((t) & 3)

template <typename F, int... I> __device__ __forceinline__ void static_for_impl(F&& f, std::integer_sequence<int, I...>) {
  (f(std::integral_constant<int, I>{}), ...);
}
template <int N, typename F> __device__ __forceinline__ void static_for(F&& f) { static_for_impl(f, std::make_integer_sequence<int, N>{}); }
__device__ __forceinline__ unsigned pack2(float a, float b) {
  f32x2 v = {a, b};
  return __builtin_bit_cast(unsigned, __builtin_convertvector(v, bf16x2));
}
__device__ __forceinline__ float asf(unsigned u) { return __builtin_bit_cast(float, u); }
__device__ __forceinline__ float lo16(unsigned u) { return __builtin_bit_cast(float, u << 16); }
__device__ __forceinline__ float hi16(unsigned u) { return __builtin_bit_cast(float, u & 0xffff0000u); }
__device__ __forceinline__ u32x4 pack8(const f32x4 a, const f32x4 b) {
  u32x4 o;
  o[0] = pack2(a[0], a[1]); o[1] = pack2(a[2], a[3]); o[2] = pack2(b[0], b[1]); o[3] = pack2(b[2], b[3]);
  return o;
}
__device__ __forceinline__ f32x4 mfma16(u32x4 a, u32x4 b, f32x4 c) {
  return __builtin_amdgcn_mfma_f32_16x16x32_bf16(__builtin_bit_cast(bf16x8, a), __builtin_bit_cast(bf16x8, b), c, 0, 0, 0);
}
__device__ __forceinline__ f32x4 mfma16k16(s16x4 a, s16x4 b, f32x4 c) { return __builtin_amdgcn_mfma_f32_16x16x16bf16_1k(a, b, c, 0, 0, 0); }
template <int CTRL> __device__ __forceinline__ float dppf(float v) {
  return __builtin_bit_cast(float, __builtin_amdgcn_update_dpp(0, __builtin_bit_cast(int, v), CTRL, 0xf, 0xf, false));
}
template <int CTRL> __device__ __forceinline__ unsigned dppu(unsigned v) {
  return (unsigned)__builtin_amdgcn_update_dpp(0, (int)v, CTRL, 0xf, 0xf, false);
}
__device__ __forceinline__ float add_x16(float v) {
  auto r = __builtin_amdgcn_permlane16_swap(__builtin_bit_cast(unsigned, v), __builtin_bit_cast(unsigned, v), false, false);
  return __builtin_bit_cast(float, (unsigned)r[0]) + __builtin_bit_cast(float, (unsigned)r[1]);
}
__device__ __forceinline__ float add_x32(float v) {
  auto r = __builtin_amdgcn_permlane32_swap(__builtin_bit_cast(unsigned, v), __builtin_bit_cast(unsigned, v), false, false);
  return __builtin_bit_cast(float, (unsigned)r[0]) + __builtin_bit_cast(float, (unsigned)r[1]);
}
__device__ __forceinline__ float max_x16(float v) {
  auto r = __builtin_amdgcn_permlane16_swap(__builtin_bit_cast(unsigned, v), __builtin_bit_cast(unsigned, v), false, false);
  return fmaxf(__builtin_bit_cast(float, (unsigned)r[0]), __builtin_bit_cast(float, (unsigned)r[1]));
}
__device__ __forceinline__ float max_x32(float v) {
  auto r = __builtin_amdgcn_permlane32_swap(__builtin_bit_cast(unsigned, v), __builtin_bit_cast(unsigned, v), false, false);
  return fmaxf(__builtin_bit_cast(float, (unsigned)r[0]), __builtin_bit_cast(float, (unsigned)r[1]));
}
__device__ __forceinline__ unsigned umax_x16(unsigned v) {
  auto r = __builtin_amdgcn_permlane16_swap(v, v, false, false);
  return max((unsigned)r[0], (unsigned)r[1]);
}
__device__ __forceinline__ unsigned umax_x32(unsigned v) {
  auto r = __builtin_amdgcn_permlane32_swap(v, v, false, false);
  return max((unsigned)r[0], (unsigned)r[1]);
}
__device__ __forceinline__ float sum_hi8(float v) {
  v += dppf<0x128>(v);
  v = add_x16(v);
  return add_x32(v);
}
__device__ __forceinline__ float sum64(float v) {
  v += dppf<0xB1>(v);
  v += dppf<0x4E>(v);
  v += dppf<0x124>(v);
  v += dppf<0x128>(v);
  v = add_x16(v);
  return add_x32(v);
}

#define BAR() asm volatile("s_waitcnt lgkmcnt(0)\n\ts_barrier" ::: "memory")
#define DRAIN() asm volatile("s_waitcnt vmcnt(0)" ::: "memory")
#define SB() __builtin_amdgcn_sched_barrier(0)
template <int N> __device__ __forceinline__ void wait_vm() { asm volatile("s_waitcnt vmcnt(%0)" ::"n"(N) : "memory"); }

__device__ __forceinline__ void dma16(rsrc_t rs, unsigned voff, unsigned soff, unsigned lds) {
  asm volatile("s_mov_b32 m0, %0\n\ts_nop 0\n\tbuffer_load_dwordx4 %1, %2, %3 offen lds" ::"s"(lds), "v"(voff), "s"(rs), "s"(soff) : "memory");
}
template <int OFF> __device__ __forceinline__ void dma16s(rsrc_t rs, unsigned voff, unsigned soff, unsigned lds) {
  asm volatile("s_mov_b32 m0, %0\n\ts_nop 0\n\tbuffer_load_dwordx4 %1, %2, %3 offen offset:%4 sc1 lds" ::"s"(lds), "v"(voff), "s"(rs), "s"(soff), "n"(OFF) : "memory");
}
__device__ __forceinline__ float rbf1(float x) { return __builtin_bit_cast(float, pack2(0.f, x)); }
__device__ __forceinline__ void vswap(unsigned& a, unsigned& b) { asm volatile("v_swap_b32 %0, %1" : "+v"(a), "+v"(b)); }
__device__ __forceinline__ void vswap(float& a, float& b) { asm volatile("v_swap_b32 %0, %1" : "+v"(a), "+v"(b)); }
template <typename V, int N> __device__ __forceinline__ void vswapn(V& a, V& b) {
#pragma unroll
  for (int i = 0; i < N; ++i) { auto x = a[i], y = b[i]; vswap(x, y); a[i] = x; b[i] = y; }
}
__device__ __forceinline__ u32x4 ld16(rsrc_t rs, unsigned voff, unsigned soff) { return __builtin_amdgcn_raw_buffer_load_b128(rs, voff, soff, SC1); }
template <int OFF> __device__ __forceinline__ void ld16h(u32x4& d, rsrc_t rs, unsigned voff, unsigned soff) {
  asm volatile("buffer_load_dwordx4 %0, %1, %2, %3 offen offset:%4 sc1" : "=v"(d) : "v"(voff), "s"(rs), "s"(soff), "n"(OFF) : "memory");
}
template <int I, int OFF> __device__ __forceinline__ void ld16w(u32x4& d, rsrc_t rs, unsigned voff, unsigned soff) {
  if constexpr (I == 0) asm volatile("buffer_load_dwordx4 %0, %1, %2, %3 offen offset:%4 sc1" : "={v[128:131]}"(d) : "v"(voff), "s"(rs), "s"(soff), "n"(OFF) : "memory");
  else if constexpr (I == 1) asm volatile("buffer_load_dwordx4 %0, %1, %2, %3 offen offset:%4 sc1" : "={v[132:135]}"(d) : "v"(voff), "s"(rs), "s"(soff), "n"(OFF) : "memory");
  else if constexpr (I == 2) asm volatile("buffer_load_dwordx4 %0, %1, %2, %3 offen offset:%4 sc1" : "={v[136:139]}"(d) : "v"(voff), "s"(rs), "s"(soff), "n"(OFF) : "memory");
  else if constexpr (I == 3) asm volatile("buffer_load_dwordx4 %0, %1, %2, %3 offen offset:%4 sc1" : "={v[140:143]}"(d) : "v"(voff), "s"(rs), "s"(soff), "n"(OFF) : "memory");
  else if constexpr (I == 4) asm volatile("buffer_load_dwordx4 %0, %1, %2, %3 offen offset:%4 sc1" : "={v[144:147]}"(d) : "v"(voff), "s"(rs), "s"(soff), "n"(OFF) : "memory");
  else if constexpr (I == 5) asm volatile("buffer_load_dwordx4 %0, %1, %2, %3 offen offset:%4 sc1" : "={v[148:151]}"(d) : "v"(voff), "s"(rs), "s"(soff), "n"(OFF) : "memory");
  else if constexpr (I == 6) asm volatile("buffer_load_dwordx4 %0, %1, %2, %3 offen offset:%4 sc1" : "={v[152:155]}"(d) : "v"(voff), "s"(rs), "s"(soff), "n"(OFF) : "memory");
  else if constexpr (I == 7) asm volatile("buffer_load_dwordx4 %0, %1, %2, %3 offen offset:%4 sc1" : "={v[156:159]}"(d) : "v"(voff), "s"(rs), "s"(soff), "n"(OFF) : "memory");
  else if constexpr (I == 8) asm volatile("buffer_load_dwordx4 %0, %1, %2, %3 offen offset:%4 sc1" : "={v[160:163]}"(d) : "v"(voff), "s"(rs), "s"(soff), "n"(OFF) : "memory");
  else if constexpr (I == 9) asm volatile("buffer_load_dwordx4 %0, %1, %2, %3 offen offset:%4 sc1" : "={v[164:167]}"(d) : "v"(voff), "s"(rs), "s"(soff), "n"(OFF) : "memory");
  else if constexpr (I == 10) asm volatile("buffer_load_dwordx4 %0, %1, %2, %3 offen offset:%4 sc1" : "={v[168:171]}"(d) : "v"(voff), "s"(rs), "s"(soff), "n"(OFF) : "memory");
  else if constexpr (I == 11) asm volatile("buffer_load_dwordx4 %0, %1, %2, %3 offen offset:%4 sc1" : "={v[172:175]}"(d) : "v"(voff), "s"(rs), "s"(soff), "n"(OFF) : "memory");
}
template <int K> __device__ __forceinline__ void pin_w(u32x4& a, u32x4& b) {
  if constexpr (K == 0) asm volatile("" : "+{v[128:131]}"(a), "+{v[132:135]}"(b)::"memory");
  else if constexpr (K == 1) asm volatile("" : "+{v[136:139]}"(a), "+{v[140:143]}"(b)::"memory");
  else if constexpr (K == 2) asm volatile("" : "+{v[144:147]}"(a), "+{v[148:151]}"(b)::"memory");
  else if constexpr (K == 3) asm volatile("" : "+{v[152:155]}"(a), "+{v[156:159]}"(b)::"memory");
  else if constexpr (K == 4) asm volatile("" : "+{v[160:163]}"(a), "+{v[164:167]}"(b)::"memory");
  else if constexpr (K == 5) asm volatile("" : "+{v[168:171]}"(a), "+{v[172:175]}"(b)::"memory");
}
__device__ __forceinline__ void ld8h(u32x2& d, rsrc_t rs, unsigned voff, unsigned soff) {
  asm volatile("buffer_load_dwordx2 %0, %1, %2, %3 offen sc1" : "=v"(d) : "v"(voff), "s"(rs), "s"(soff) : "memory");
}
template <int OFF> __device__ __forceinline__ void ld4h(unsigned& d, rsrc_t rs, unsigned voff, unsigned soff) {
  asm volatile("buffer_load_dword %0, %1, %2, %3 offen offset:%4 sc1" : "=v"(d) : "v"(voff), "s"(rs), "s"(soff), "n"(OFF) : "memory");
}
__device__ __forceinline__ u32x2 ld8(rsrc_t rs, unsigned voff, unsigned soff) { return __builtin_amdgcn_raw_buffer_load_b64(rs, voff, soff, SC1); }
__device__ __forceinline__ void st16(u32x4 v, rsrc_t rs, unsigned voff, unsigned soff) { __builtin_amdgcn_raw_buffer_store_b128(v, rs, voff + soff, 0, SC1); }
__device__ __forceinline__ void st8(u32x2 v, rsrc_t rs, unsigned voff, unsigned soff) { __builtin_amdgcn_raw_buffer_store_b64(v, rs, voff, soff, SC1); }

__device__ __forceinline__ unsigned ld4(rsrc_t rs, unsigned voff, unsigned soff) { return __builtin_amdgcn_raw_buffer_load_b32(rs, voff, soff, SC1); }
__device__ __forceinline__ void st4(unsigned v, rsrc_t rs, unsigned voff, unsigned soff) { __builtin_amdgcn_raw_buffer_store_b32(v, rs, voff, soff, SC1); }

constexpr unsigned kSpinCap = 1u << 16;
__device__ __forceinline__ bool spin_fail(unsigned& spins, rsrc_t rs, u32x2 T) {
  asm volatile("" ::: "memory");
  ++spins;
  if (spins > kSpinCap) { st8(T, rs, 0, F_TMO); return true; }
  if ((spins & 255u) == 0u) {
    const u32x2 v = ld8(rs, 0, F_TMO);
    if (v[0] == T[0] && v[1] == T[1]) return true;
  }
  __builtin_amdgcn_s_sleep(3);
  return false;
}
__device__ __forceinline__ void poll_flags2(rsrc_t rs, unsigned voff, u32x2 T) {
  for (unsigned spins = 0;;) {
    const u32x4 v = ld16(rs, voff, 0);
    if (__all(v[0] == T[0] && v[1] == T[1] && v[2] == T[0] && v[3] == T[1])) break;
    if (spin_fail(spins, rs, T)) break;
  }
  asm volatile("" ::: "memory");
  __builtin_amdgcn_fence(__ATOMIC_ACQUIRE, "wavefront");
}
__device__ __forceinline__ void poll_flags(rsrc_t rs, unsigned voff, unsigned soff, u32x2 T) {
  for (unsigned spins = 0;;) {
    const u32x2 v = ld8(rs, voff, soff);
    if (__all(v[0] == T[0] && v[1] == T[1])) break;
    if (spin_fail(spins, rs, T)) break;
  }
  asm volatile("" ::: "memory");
  __builtin_amdgcn_fence(__ATOMIC_ACQUIRE, "wavefront");
}

#define STAMP_ON(i) true
#define STAMP_VAL(i) (((i) == 23 || (i) == 22 || (i) == 21) ? __builtin_amdgcn_s_memtime() : __builtin_amdgcn_s_memrealtime())
#define STAMPAT(i)

#define VSTEP(i)

__device__ __forceinline__ unsigned toff(unsigned R, unsigned ks, unsigned g) {
  return (((R >> 5) * 64 + ks * 2 + ((R >> 4) & 1)) << 10) + ((g * 16 + (R & 15)) << 4);
}
__device__ __forceinline__ void unpack8v(const u32x4 u, f32x2* v) {
#pragma unroll
  for (int j = 0; j < 4; ++j) v[j] = f32x2{lo16(u[j]), hi16(u[j])};
}
__device__ __forceinline__ u32x4 pack8v(const f32x2* v) {
  u32x4 o;
#pragma unroll
  for (int j = 0; j < 4; ++j) o[j] = pack2(v[j][0], v[j][1]);
  return o;
}
__device__ __forceinline__ f32x2 rbf1v(f32x2 v) { return f32x2{rbf1(v[0]), rbf1(v[1])}; }
__device__ __forceinline__ float ln_rd(float var) { return __builtin_amdgcn_rcpf(rbf1(__builtin_amdgcn_sqrtf(rbf1(rbf1(var) + 1e-5f)))); }
template <int N> __device__ __forceinline__ void ln_apply(float mb, float rd, const f32x2* v, f32x2* y) {
  const f32x2 mb2 = {mb, mb}, rd2 = {rd, rd};
#pragma unroll
  for (int j = 0; j < N; ++j) y[j] = rbf1v(rbf1v(v[j] - mb2) * rd2);
}
__device__ __forceinline__ float hadd(f32x2 v) {
  float a = v[0];
  asm volatile("" : "+v"(a));
  return a + v[1];
}
__device__ __forceinline__ float add_g(float v) { return add_x32(add_x16(v)); }
__device__ __forceinline__ void add_g3(float& x, float& y, float& z) {
  auto a = __builtin_amdgcn_permlane16_swap(__builtin_bit_cast(unsigned, x), __builtin_bit_cast(unsigned, y), false, false);
  auto b = __builtin_amdgcn_permlane16_swap(__builtin_bit_cast(unsigned, z), __builtin_bit_cast(unsigned, z), false, false);
  const float r = asf(a[0]) + asf(a[1]), t = asf(b[0]) + asf(b[1]);
  auto c = __builtin_amdgcn_permlane32_swap(__builtin_bit_cast(unsigned, r), __builtin_bit_cast(unsigned, r), false, false);
  auto d = __builtin_amdgcn_permlane32_swap(__builtin_bit_cast(unsigned, t), __builtin_bit_cast(unsigned, t), false, false);
  const float u = asf(c[0]) + asf(c[1]);
  z = asf(d[0]) + asf(d[1]);
  auto e = __builtin_amdgcn_permlane16_swap(__builtin_bit_cast(unsigned, u), __builtin_bit_cast(unsigned, u), false, false);
  x = asf(e[0]); y = asf(e[1]);
}
__device__ __forceinline__ u32x4 trade16(u32x4 v) {
  auto a = __builtin_amdgcn_permlane16_swap(v[0], v[2], false, false);
  auto b = __builtin_amdgcn_permlane16_swap(v[1], v[3], false, false);
  return u32x4{(unsigned)a[0], (unsigned)b[0], (unsigned)a[1], (unsigned)b[1]};
}
__device__ __forceinline__ unsigned umax4(u32x4 v) { return max(max(v[0], v[1]), max(v[2], v[3])); }
__device__ __forceinline__ u32x2 unpoison2(u32x2 v) { return u32x2{min(v[0], POISON - 1), min(v[1], POISON - 1)}; }
__device__ __forceinline__ u32x4 unpoison4(u32x4 v) {
  return u32x4{min(v[0], POISON - 1), min(v[1], POISON - 1), min(v[2], POISON - 1), min(v[3], POISON - 1)};
}

typedef union { struct { s16x4 a, b; } hf; u32x4 v; } frag_t;
__device__ __forceinline__ s16x4 trread(const char* smem, unsigned off) {
  return __builtin_amdgcn_ds_read_tr16_b64_v4i16((LDS_AS s16x4*)(smem + off));
}
__device__ __forceinline__ s16x4 trabs(unsigned addr) { return __builtin_amdgcn_ds_read_tr16_b64_v4i16((LDS_AS s16x4*)(size_t)addr); }

constexpr int WA = 6;
constexpr int WB = 8, WR = 4;
#define CH 1, 8, 10, 11, 16, 18, 24, 26, 30, 31
constexpr int CHV[10] = {CH};
constexpr int SPL = CHV[0];
constexpr int FSL = CHV[1], FS0 = CHV[2], FS0D = CHV[3], FS1 = CHV[4], FS1S = CHV[5];
constexpr int FXA = CHV[6], FXB = CHV[7], FXL = CHV[8], FXS = CHV[9];
#define L0S 22, 25, 8, 0, 26, 29, 1, 2
constexpr int L0V[8] = {L0S};
constexpr int CSL = L0V[0], CSS = L0V[1], CSD = L0V[2];
constexpr int abl(int it) { return it ? 25 : L0V[4]; }
constexpr int abs_(int it) { return it ? 27 : L0V[5]; }
constexpr int W2S = L0V[6], WSH = L0V[7];
constexpr int LNL = L0V[3], LNS = LNL + 2;
#define SYNCB 3
#define SPINS 0
#define KVS 0
#define ROTH 0
#define ROT0 (p + ROTH * h)
#define TCH 14, 12, 0x1F, 26, 0xE0, 0, 1
constexpr int TCV[7] = {TCH};
constexpr int TS0 = TCV[0], TSN = TCV[1], TM0 = TCV[2], TEX = TCV[3], TM1 = TCV[4], TSD = TCV[5], TSG = TCV[6];
constexpr int QL = 26;
constexpr int SDIR = 24, GSL = 14, RSL = 56, KSL = 3;
constexpr int tp0(int it) { return it ? 3 : 3 + WSH; }
constexpr bool tile_piece(int it, int s) { return s >= tp0(it) && ((s - tp0(it)) >> 2) + 3 < 8 && ((s - tp0(it)) & 3) < 3; }
constexpr int a_req(int s, int n) {
  if (s < 1 || !(n & 1) || (s == 1 && n == 1)) return -1;
  if (s <= 5) return 8 + 3 * s + (n >> 1) - 4;
  return n < 5 && s + 5 < 32 ? 2 * (s + 5) + (n >> 1) : -1;
}
constexpr int vm_n(int it, int s, int n) {
  int c = it ? (a_req(s, n) >= 0) + (s == 0 && n == 0) : (s + 5 < 32 && (n == 2 || n == 3));
  if (n == 0) c += tile_piece(it, s);
  if (n == 4) c += (s == abl(it)) + (it ? (s == FSL ? 5 : s == FXA ? 4 : 0) + (s == FSL - 2) : 0);
  if (!it) c += (s >= W2S && s < W2S + 3 && n == 4) + (s == LNL && n == 5 ? 3 : 0) + (s == LNS + 1 && n == 5) + (s == TEX && n == 5);
  else c += (s == 2 && n == 2) + (s == SPL && n == 4 ? 4 : 0) + (s >= 27 && s < 30 && (n == 0 || n == 3));
  return c;
}
constexpr int vm_at(int it, int s, int n) {
  int c = it ? 8 : 16;
  for (int u = 0; u < 6 * s + n; ++u) c += vm_n(it, u / 6, u % 6);
  return c;
}
constexpr int vm_a(int it, int k) {
  if (k < 4) return 2 * k + 2;
  for (int u = 0; u < 192; ++u)
    if (a_req(u / 6, u % 6) == 2 * k + 1) return vm_at(it, (u + 1) / 6, (u + 1) % 6);
  return 0;
}
constexpr int vm_tile(int it, int t) { return t >= 3 ? vm_at(it, 4 * t - 10 + tp0(it), 1) : t == 1 ? 12 : vm_at(0, W2S + 2, 5); }
constexpr int vm_unit(int u) { return u < 3 ? 5 + 2 * u : u < 5 ? 8 + 2 * u : vm_at(0, u - 5, 4); }
constexpr int unit_slot(int u) { return u % 3 == 0 ? 0 : 16384 + (u % 3) * 16384; }

constexpr int L_XC = LDS_BYTES - 64, L_SKEY = 32704, L_SRED = 4096, L_SFIN = 20480;
__device__ __forceinline__ unsigned key_load(rsrc_t rs, int l, int L, int rep) { return ld4(rs, (l & 15) * SKEY, KEY_AT(L, rep)); }
__device__ __forceinline__ unsigned umax16(unsigned k) {
  k = max(k, dppu<0xB1>(k));
  k = max(k, dppu<0x4E>(k));
  k = max(k, dppu<0x141>(k));
  return max(k, dppu<0x140>(k));
}
__device__ __forceinline__ unsigned key_row(unsigned key) { return (0xffffu - (key & 0xffffu)) & 2047u; }
__device__ __forceinline__ unsigned key_wait(rsrc_t rs, u32x2 T, int l, int L, int rep, unsigned kv) {
  if (!__all(kv != POISON)) {
    unsigned spins = 0;
    do {
      if (spin_fail(spins, rs, T)) break;
      if (L == 0) __builtin_amdgcn_s_sleep(KSL);
      kv = key_load(rs, l, L, rep);
    } while (!__all(kv != POISON));
  }
  return __builtin_amdgcn_readfirstlane(umax16(kv));
}
__device__ __forceinline__ unsigned ffn_xoff(unsigned key, int L, int w) { return OFF_CA + L * 32768 + (key_row(key) >> 7) * 2048 + w * 256; }
__device__ __forceinline__ void ffn_xwait(rsrc_t rs, u32x2 T, unsigned xo, int l, u32x4 (&xv)[4]) {
#define XV_OK() __all(max(max(umax4(xv[0]), umax4(xv[1])), max(umax4(xv[2]), umax4(xv[3]))) != POISON)
  if (!XV_OK()) {
    unsigned spins = 0;
    do {
      if (spin_fail(spins, rs, T)) break;
#pragma unroll
      for (int i = 0; i < 4; ++i) xv[i] = ld16(rs, (l >> 4) * 16 + i * 64, xo);
    } while (!XV_OK());
  }
#undef XV_OK
}
__device__ __forceinline__ void ffn_gu(const u32x4 (&gv)[4], const u32x4 (&uv)[4], const u32x4 (&xv)[4], f32x4& ga, f32x4& ua) {
  ga = mfma16(gv[0], xv[0], f32x4{0.f, 0.f, 0.f, 0.f}); ua = mfma16(uv[0], xv[0], f32x4{0.f, 0.f, 0.f, 0.f});
#pragma unroll
  for (int i = 1; i < 4; ++i) { ga = mfma16(gv[i], xv[i], ga); ua = mfma16(uv[i], xv[i], ua); }
}
__device__ __forceinline__ void ffn_gu_check(rsrc_t rs, u32x2 T, unsigned xo, int l, const u32x4 (&gv)[4], const u32x4 (&uv)[4], u32x4 (&xv)[4], f32x4& ga, f32x4& ua) {
  if (__any(ga[0] != ga[0])) {
    ffn_xwait(rs, T, xo, l, xv);
    ffn_gu(gv, uv, xv, ga, ua);
  }
}
__device__ __forceinline__ void ffn_gu_store(char* smem, int l, int w, f32x4 ga, f32x4 ua) {
  LDS_AS f32x4* sred = (LDS_AS f32x4*)(smem + L_SRED);
  if ((l & 15) == 0) { sred[w * 8 + (l >> 4)] = ga; sred[w * 8 + 4 + (l >> 4)] = ua; }
}
__device__ __forceinline__ void ffn_d_read(char* smem, int l, int w, f32x4& t, s16x4 (&dfr)[8]) {
  t = ((const LDS_AS f32x4*)(smem + L_SRED))[(l & 7) * 8 + ((l >> 3) & 1) * 4 + (l >> 4)];
#pragma unroll
  for (int i = 0; i < 8; ++i) dfr[i] = trread(smem, L_DN + w * 4096 + (l >> 4) * 1024 + ((l & 15) >> 2) * 256 + i * 32 + (l & 3) * 8);
}
__device__ __forceinline__ float ffn_d_sum(float t) { t += dppf<0xB1>(t); t += dppf<0x4E>(t); return t + dppf<0x141>(t); }
__device__ __forceinline__ float ffn_d_h(float t) {
  const float gg = rbf1(t), uu = rbf1(dppf<0x128>(t));
  return rbf1(gg * __builtin_amdgcn_rcpf(1.0f + __expf(-gg))) * uu;
}
__device__ __forceinline__ f32x4 ffn_d_mma(int l, int t, s16x4 d, u32x2 hh, f32x4 y) {
  const bool me = (l & 15) == t;
  return mfma16k16(d, __builtin_bit_cast(s16x4, u32x2{me ? hh[0] : 0u, me ? hh[1] : 0u}), y);
}
__device__ __forceinline__ void ffn_d_store(rsrc_t rs, int l, int w, int L, int slice, f32x4 y) {
  const unsigned c = l & 15;
  if (c < 8) st16(unpoison4(__builtin_bit_cast(u32x4, y)), rs, (4 * w + (c >> 1)) * 32768 + slice * 128 + (c & 1) * 64 + (l >> 4) * 16, OFF_PART + L * (1u << 20));
}
__device__ __forceinline__ void red_load(rsrc_t rs, int tid, int L, unsigned G, unsigned key, u32x4 (&pv)[4], u32x2& cxn, u32x2& cx1) {
  const unsigned co = L * 32768 + (key_row(key) >> 7) * 2048 + G * 64, jj = (tid & 3) * 16 + (tid & 4) * 2;
#pragma unroll
  for (int k = 0; k < 4; ++k) pv[k] = ld16(rs, min(tid + 512 * k, 1791) * 16, OFF_PART + L * (1u << 20) + G * 32768);
  cxn = ld8(rs, jj, OFF_CA + co);
  cx1 = ld8(rs, jj, OFF_CB + co);
}
__device__ __forceinline__ void red_sum(rsrc_t rs, char* smem, u32x2 T, int tid, int w, int L, unsigned G, unsigned key,
                                        u32x4 (&pv)[4], u32x2& cxn, u32x2& cx1) {
#define PV_OK() __all(max(max(max(umax4(pv[0]), umax4(pv[1])), max(umax4(pv[2]), umax4(pv[3]))), max(max(cxn[0], cxn[1]), max(cx1[0], cx1[1]))) != POISON)
  if (!PV_OK()) {
    unsigned spins = 0;
    do {
      if (spin_fail(spins, rs, T)) break;
      red_load(rs, tid, L, G, key, pv, cxn, cx1);
    } while (!PV_OK());
  }
#undef PV_OK
  f32x4 s = (__builtin_bit_cast(f32x4, pv[0]) + __builtin_bit_cast(f32x4, pv[1])) + __builtin_bit_cast(f32x4, pv[2]);
  if (w < 4) s += __builtin_bit_cast(f32x4, pv[3]);
  ((LDS_AS f32x4*)(smem + L_SFIN))[tid] = s;
}
__device__ __forceinline__ u32x2 red_final(char* smem, int l, unsigned key, u32x2 cxn, u32x2 cx1) {
  const LDS_AS f32x4* sf = (const LDS_AS f32x4*)(smem + L_SFIN) + l;
  f32x4 s = ((sf[0] + sf[64]) + (sf[128] + sf[192])) + ((sf[256] + sf[320]) + (sf[384] + sf[448]));
#pragma unroll
  for (int j = 0; j < 4; ++j) s[j] = sum_hi8(s[j]);
  const unsigned k16 = key >> 16;
  const float rmax = __builtin_bit_cast(float, ((k16 & 0x8000u) ? (k16 & 0x7fffu) : (~k16 & 0xffffu)) << 16);
  const float sig = rbf1(__builtin_amdgcn_rcpf(1.0f + __expf(-rmax)));
  const float xn4[4] = {lo16(cxn[0]), hi16(cxn[0]), lo16(cxn[1]), hi16(cxn[1])};
  const float x14[4] = {lo16(cx1[0]), hi16(cx1[0]), lo16(cx1[1]), hi16(cx1[1])};
  float o[4];
#pragma unroll
  for (int j = 0; j < 4; ++j) o[j] = x14[j] + rbf1(xn4[j] + rbf1(rbf1(s[j]) * sig));
  return u32x2{pack2(o[0], o[1]), pack2(o[2], o[3])};
}

struct RowFin { float mean, var, mb, rd; unsigned key; };
__device__ __forceinline__ void fin_wait(rsrc_t rs, u32x2 T, unsigned voff, unsigned so, u32x4 (&sp)[4]) {
#define ST_OK() __all(max(max(umax4(sp[0]), umax4(sp[1])), max(umax4(sp[2]), umax4(sp[3]))) != POISON)
  if (!ST_OK()) {
    unsigned spins = 0;
    do {
      if (spin_fail(spins, rs, T)) break;
#pragma unroll
      for (int i = 0; i < 4; ++i) sp[i] = ld16(rs, voff + i * 2048, so);
    } while (!ST_OK());
  }
#undef ST_OK
}
__device__ __forceinline__ RowFin fin_key(rsrc_t rs, u32x2 T, unsigned voff, unsigned so, char* skey, int l, int w, unsigned R, float wsum, u32x4 (&sp)[4]) {
  RowFin r;
#define ST_SUM() ((__builtin_bit_cast(f32x4, sp[0]) + __builtin_bit_cast(f32x4, sp[1])) + (__builtin_bit_cast(f32x4, sp[2]) + __builtin_bit_cast(f32x4, sp[3])))
  f32x4 sf = ST_SUM();
  const float z = (sf[0] + sf[1]) + sf[2];
  if (__any(z != z)) {
    fin_wait(rs, T, voff, so, sp);
    sf = ST_SUM();
  }
#undef ST_SUM
  float s0 = sf[0], s1 = sf[1], s2 = sf[2];
  add_g3(s0, s1, s2);
  r.mean = s0 * (1.0f / 1024.0f);
  r.var = fmaxf(__builtin_fmaf(-r.mean, r.mean, s1 * (1.0f / 1024.0f)), 0.f);
  r.mb = rbf1(r.mean); r.rd = ln_rd(r.var);
  const unsigned rb16 = pack2((s2 - r.mb * wsum) * r.rd, 0.f) & 0xffffu;
  r.key = min((((rb16 & 0x8000u) ? (~rb16 & 0xffffu) : (rb16 | 0x8000u)) << 16) | (0xffffu - R), POISON - 1);
  const unsigned wk = umax16(r.key);
  if (l == 0) ((LDS_AS unsigned*)skey)[w] = wk;
  return r;
}
__device__ __forceinline__ void fin_cand(rsrc_t rs, char* skey, int L, int g, int rb, int ct, const RowFin& r, const f32x2 (&v)[8]) {
  const u32x4 k0 = *(const LDS_AS u32x4*)skey, k1 = *(const LDS_AS u32x4*)(skey + 16);
  const unsigned best = max(umax4(k0), umax4(k1));
  if (r.key == best) {
    const unsigned co = L * 32768 + rb * 2048 + ct * 128 + g * 16;
    f32x2 t[8], rd2 = {r.rd, r.rd}, of2 = {-r.mb * r.rd, -r.mb * r.rd};
    asm volatile("" : "+v"(rd2), "+v"(of2));
#pragma unroll
    for (int j = 0; j < 8; ++j) t[j] = __builtin_elementwise_fma(v[j], rd2, of2);
    st16(unpoison4(pack8v(t)), rs, co, OFF_CA); st16(unpoison4(pack8v(t + 4)), rs, co + 64, OFF_CA);
    if (g == 0 && ct < (int)RKEY) st4(best, rs, KEY_AT(L, ct) + rb * SKEY, 0);
    st16(unpoison4(pack8v(v)), rs, co, OFF_CB); st16(unpoison4(pack8v(v + 4)), rs, co + 64, OFF_CB);
  }
}
__device__ __forceinline__ void fin_x2(const RowFin& r, f32x2 (&v)[8]) {
  f32x2 sc2 = {1.0f + r.rd, 1.0f + r.rd}, of2 = {-r.mb * r.rd, -r.mb * r.rd};
  asm volatile("" : "+v"(sc2), "+v"(of2));
#pragma unroll
  for (int j = 0; j < 8; ++j) v[j] = __builtin_elementwise_fma(v[j], sc2, of2);
}

extern "C" __device__ unsigned long long dispatch_id() __asm("llvm.amdgcn.dispatch.id");

extern "C" __global__ __launch_bounds__(512, 2) void mega_kernel(const bf16* x, char* ws, const bf16* wqkv, const bf16* wo, const bf16* wr,
                                                                 const bf16* wg, const bf16* wu, const bf16* wd, bf16* out) {
  extern __shared__ __attribute__((aligned(16))) char smem[];
  const int w0_ = __builtin_amdgcn_readfirstlane(threadIdx.x >> 6);
  const unsigned lbase = (unsigned)(size_t)(LDS_AS char*)smem;
  {
    unsigned long long pc;
    unsigned len;
    asm volatile("s_getpc_b64 %0\n\ts_mov_b32 %1, .Ltext_end-mega_kernel\n\t.subsection 1\n.Ltext_end:\n\t.subsection 0" : "=s"(pc), "=s"(len));
    const unsigned o = ((blockIdx.x >> 3) * 64 + (threadIdx.x & 63)) * 128;
    if (w0_ == 7 && o < len)
      asm volatile("s_mov_b32 m0, %0\n\ts_nop 0\n\tglobal_load_lds_dword %1, %2" ::"s"(lbase + L_FXL), "v"(o), "s"(pc & ~127ull) : "memory");
  }
  asm volatile("" : "+s"(x), "+s"(ws), "+s"(wqkv));
#define IDS()                                                                                         \
  int tid = threadIdx.x, w = w0_, bid = blockIdx.x;                                                   \
  asm volatile("" : "+v"(tid));                                                                       \
  asm volatile("" : "+s"(w));                                                                         \
  asm volatile("" : "+s"(bid));                                                                       \
  const int l = tid & 63, xcd = bid & 7, bi = bid >> 3;                                               \
  const int p = (xcd & 1) * 4 + (bi & 3), h = (xcd >> 1) * 8 + (bi >> 2);         \
  const int rb = 2 * xcd + (bi & 1), ct = bi >> 1;                               \
  const int slice = xcd * 28 + bi;                                                   \
  const bool ffn_on = bi < 28;                                                                        \
  const int g = l >> 4;                                                                               \
  const int rt = w < 4 ? w : 11 - w;          \
  KARGS();                                                                                            \
  (void)p; (void)h; (void)rb; (void)ct; (void)slice; (void)ffn_on; (void)g; (void)rt;
  const rsrc_t rs = __builtin_amdgcn_make_buffer_rsrc((void*)ws, 0, WS_BYTES, 0x00020000);
  typedef const void* const __attribute__((address_space(4)))* karg_t;
#define KARGS() karg_t ka = (karg_t)__builtin_amdgcn_kernarg_segment_ptr(); asm volatile("" : "+s"(ka))
#define ARGP(name, i) const GAS bf16* name = (const GAS bf16*)ka[i]; asm volatile("" : "+s"(name))
#define RSRC(ptr, bytes) __builtin_amdgcn_make_buffer_rsrc((void*)(ptr), 0, bytes, 0x00020000)
#define rwq RSRC(pwq, 1024 * 3072 * 2)
#define rwo RSRC(pwo, 1024 * 1024 * 2)
#define rwg RSRC(pwg, 1024 * F_ * 2)
#define rwu RSRC(pwu, 1024 * F_ * 2)
#define rwd RSRC(pwd, 1024 * F_ * 2)
#define rout RSRC(pout, 2048 * 1024 * 2)

#define WQ_VOFF() ((l >> 2) * 6144 + (((l & 3) ^ ((((l >> 4) ^ (l >> 5)) & 1) << 1)) << 4))
#define WQ_PIECE(t, j, dst) dma16(rwq, wqv, ((((t) + rot) & 7) * 128 + ((j) & 7) * 16) * 6144 + ((j) >> 3) * 2048 + h * 64, lbase + (dst) + (j) * 1024)
#define WQ_BOFF(dt) ((8 * g + ((l & 15) >> 2)) * 64 + (l & 3) * 8 + (((dt) ^ (g & 1)) << 5))
#define WQ_POFF() ((4 * g + ((l & 15) >> 2)) * 64 + (l & 3) * 8 + (((g ^ (g >> 1)) & 1) << 5))
#define X_UNIT1(u, j) dma16(rx, xv_, (unsigned)(p * 256 + rt * 32 + ((u) & 1) * 16 + (j) * 8) * 2048 + ((((u) >> 2) + rot) & 7) * 256 + (((u) >> 1) & 1) * 128, lbase + rt * 2048 + unit_slot(u) + (j) * 1024)
#define X_VOFF() ((l >> 3) * 2048 + (((l & 7) ^ ((l >> 3) & 6)) << 4))
#define X_ROFF(hs) (lbase + rt * 2048 + (l & 15) * 128 + (((4 * (hs) + g) ^ (l & 6)) << 4))
#define TOUCH_TO(ptr, dst) asm volatile("s_mov_b32 m0, %0\n\ts_nop 0\n\tglobal_load_lds_dword %1, off" ::"s"(lbase + (dst)), "v"(ptr) : "memory")
#define TOUCH(ptr) TOUCH_TO(ptr, L_FXL)
#define WO_PIECE_W(j, w_) dma16(rwo, wov, (((w_) >> 2) * 64 + ((w_) & 3) + 4 * (j)) * 16384 + ct * 128, lbase + 32768 + (j) * 8192 + (w_) * 1024)
#define WO_PIECE(j) WO_PIECE_W(j, w)
#define WO_VOFF() ((l >> 3) * 2048 + (((((l & 7) >> 1) ^ ((l >> 4) & 3)) * 2 + (l & 1)) << 4))
#define WD_DMA() _Pragma("unroll") for (int i = 0; i < 4; ++i) WD_DMA1(i)
#define WD_DMA1(i)                                                                                   \
      dma16(rwd, (l >> 4) * 2048 + (l & 15) * 16, (unsigned)(slice * 16 + 4 * (i)) * 2048 + w * 256, lbase + L_DN + w * 4096 + (i) * 1024)
#define FFN_DMA(m) dma16((m) & 1 ? rwu : rwg, fnv, (unsigned)(((4 * w + ((m) >> 1)) * 32) * F_ + slice * 16) * 2, lbase + 65536 + (m) * 8192 + w * 1024)
  u32x4 gv[4], uv[4];
  u32x4 keep[9];
  u32x4 pre[4][2];
#define PRE_LOAD(it_, rot_, s0, s1)                                                                  \
  static_for<(s1) - (s0)>([&](auto c_) __attribute__((always_inline)) {                              \
    constexpr int s = (s0) + decltype(c_)::value;                                                    \
    const unsigned ab_ = OFF_XN + (it_) * (4u << 20) + (((rot_) + (s >> 2)) & 7) * 8192 + ((s >> 1) & 1) * 4096; \
    ld16w<2 * s, (s & 1) * 2048>(pre[s][0], rs, l * 16, ab_ + (p * 8 + rt) * 65536);                         \
    ld16w<2 * s + 1, (s & 1) * 2048 + 1024>(pre[s][1], rs, l * 16, ab_ + (p * 8 + rt) * 65536);                 \
  })
  u32x2 T;
  const GAS bf16* tsrc;
  {
    IDS();
    const int it = 0, rot = ROT0;
    (void)it;
    STAMPAT(0);
    const bf16* pwq = wqkv;
    const rsrc_t rx = RSRC(x, 2048 * 1024 * 2);
    const unsigned wqv = WQ_VOFF(), xv_ = X_VOFF();
    if (tid == 0) *(LDS_AS u32x4*)(smem + L_QC) = u32x4{0u, 0u, 0u, 0u};
#pragma unroll
    for (int i = 0; i < 3; ++i) WQ_PIECE(0, 3 * w + i, L_RING);
    STAMPAT(41);
    X_UNIT1(0, 0); X_UNIT1(0, 1); X_UNIT1(1, 0); X_UNIT1(1, 1);
    STAMPAT(42);
    BAR();
    STAMPAT(43);
    X_UNIT1(2, 0); X_UNIT1(2, 1);
#pragma unroll
    for (int i = 0; i < 3; ++i) WQ_PIECE(1, 3 * w + i, L_RING + STAGE);
    STAMPAT(44);
    if (w == 1 && l < 9) {
      const unsigned prb = 2 * p + (h >> 4), pct = h & 15;
      if (l < 8) st16(u32x4{POISON, POISON, POISON, POISON}, rs, OFF_CA + prb * 2048 + pct * 128 + l * 16, 0);
      else if (pct < RKEY) st4(POISON, rs, KEY_AT(0, pct) + prb * SKEY, 0);
    }
    {
      ARGP(pwo, 3); ARGP(pwg, 5); ARGP(pwu, 6);
      const int t = tid < 288 ? tid - 64 : tid - 288, go = (bi * 32 + t / 7) * F_ + xcd * 448 + (t % 7) * 64;
      tsrc = tid < 64 ? pwo + bid * 4096 + tid * 64 : tid < 288 ? pwg + go : pwu + go;
      asm volatile("" : "+v"(tsrc));
    }
    STAMPAT(31);
  }
  unsigned sel0 = 0x7FFFFFFFu;
  f32x2 v[8];
  u32x4 x2p[2];
  float wsum;

  auto layer = [&](auto it_c) __attribute__((always_inline)) {
    constexpr int it = decltype(it_c)::value;
    STAMPAT(1); STAMPAT(22);
    f32x4 acc[2][6];
    f32x2 qs2[2];
    u32x4 k0, k1, v0, v1;
    {
      IDS();
      bool aff = false;
      unsigned aq[2];
      u32x4 av[2];
#define PIN_ACC() asm volatile("" : "+{v[176:179]}"(acc[0][0]), "+{v[180:183]}"(acc[0][1]), "+{v[184:187]}"(acc[0][2]), "+{v[188:191]}"(acc[0][3]), "+{v[192:195]}"(acc[0][4]), "+{v[196:199]}"(acc[0][5]), \
                               "+{v[200:203]}"(acc[1][0]), "+{v[204:207]}"(acc[1][1]), "+{v[208:211]}"(acc[1][2]), "+{v[212:215]}"(acc[1][3]), "+{v[216:219]}"(acc[1][4]), "+{v[220:223]}"(acc[1][5]))
#define PINV4(x) asm volatile("" : "+v"(x[0]), "+v"(x[1]), "+v"(x[2]), "+v"(x[3])::"memory")
      auto qkv_loop = [&](auto it_c) __attribute__((always_inline)) {
        constexpr int IT = decltype(it_c)::value;
        const unsigned abase = OFF_XN + IT * (4u << 20) + (p * 8 + rt) * 65536;
        const int rot = ROT0 + 5 * IT;
        unsigned fkey = 0, kv, bq[2];
        u32x4 fx[4], bv[2], af[WA][2];
        u32x2 cxn, cx1, hh;
        f32x4 sa, sb, cs4[2];
        f32x2 b2[2];
        s16x4 dfr[8];
#define AB_OFF() (OFF_AB + IT * 32768 + (p * 8 + rt) * 512)
#define A_LOAD1(k, a) ld16w<(k) % WA * 2 + (a), ((k) & 1) * 2048 + (a) * 1024>(af[(k) % WA][a], rs, l * 16, abase + ((((k) >> 2) + rot) & 7) * 8192 + (((k) >> 1) & 1) * 4096)
        if constexpr (IT == 1) {
#pragma unroll
          for (int s = 0; s < 4; ++s) { af[s][0] = pre[s][0]; af[s][1] = pre[s][1]; }
        }
        const bf16* pwq = wqkv;
        ARGP(pwo, 3);
        const rsrc_t rx = RSRC(x, 2048 * 1024 * 2);
        unsigned xv_ = X_VOFF(), xr[2] = {X_ROFF(0), X_ROFF(1)};
        if constexpr (IT == 0) asm volatile("" : "+v"(xv_), "+v"(xr[0]), "+v"(xr[1]));
#define X_READ(u) { af[((u) & ~1) % WA][(u) & 1] = *(const LDS_AS u32x4*)(size_t)(xr[0] + unit_slot(u)); af[((u) | 1) % WA][(u) & 1] = *(const LDS_AS u32x4*)(size_t)(xr[1] + unit_slot(u)); }
        const int tstep = TS0 + bid % TSN + TSD * (w >> TSG);
        const unsigned wqv = WQ_VOFF();
#define QKV_DMA1(t, i)                                                                            \
  {                                                                                               \
    if constexpr (IT == 0) WQ_PIECE(t, 3 * w + (i), L_RING + SLOT(t) * STAGE);                    \
    else dma16s<(i) * 1024>(rs, l * 16, OFF_WI + (h * 8 + (((t) + rot) & 7)) * STAGE + w * 3072, lbase + L_RING + SLOT(t) * STAGE + w * 3072); \
  }
        unsigned bo[2][2];
#pragma unroll
        for (int dt = 0; dt < 2 - IT; ++dt) {
          bo[dt][0] = lbase + L_RING + (IT ? l * 16 : WQ_BOFF(dt)); bo[dt][1] = bo[dt][0] + 2 * STAGE;
          asm volatile("" : "+v"(bo[dt][0]), "+v"(bo[dt][1]));
        }
        auto frag0 = [&](int sl, int f) __attribute__((always_inline)) {
          unsigned pb = lbase + L_RING + WQ_POFF();
          asm volatile("" : "+v"(pb));
          const unsigned o = (pb ^ ((f & 1) << 5)) + sl * STAGE + ((f % 6) >> 1) * 8192 + (f / 6) * 2048;
          frag_t t;
          t.hf.a = trabs(o); t.hf.b = trabs(o + 1024);
          return t.v;
        };
        u32x4 df[4];
        auto dot_mma = [&](const u32x4 (&xf)[4]) __attribute__((always_inline)) {
          sa = mfma16(df[0], xf[0], f32x4{0.f, 0.f, 0.f, 0.f}); sb = mfma16(df[1], xf[1], f32x4{0.f, 0.f, 0.f, 0.f});
          sa = mfma16(df[2], xf[2], sa); sb = mfma16(df[3], xf[3], sb);
        };
        auto dot_store = [&](unsigned dst) __attribute__((always_inline)) {
          if ((l & 15) == 0) st16(unpoison4(__builtin_bit_cast(u32x4, sa + sb)), rs, g * 16, dst + ((h * 6 + w) * 8 + p) * 64);
        };
        auto tile_sum = [&](u32x4 pt, int dst) __attribute__((always_inline)) {
          f32x4 c = __builtin_bit_cast(f32x4, pt);
#pragma unroll
          for (int e = 0; e < 4; ++e) { c[e] += dppf<0x124>(c[e]); c[e] += dppf<0x128>(c[e]); c[e] = add_x16(c[e]); }
          if ((l & 28) == 0) *(LDS_AS f32x4*)(smem + dst + (w * 2 + (l >> 5)) * 64 + (l & 3) * 16) = c;
        };
        frag_t bfr[6];
#define QKV_BO(s, n, x) (bo[((n) ^ (x)) & 1][SLOT((s) >> 2) >> 1] + (SLOT((s) >> 2) & 1) * STAGE + ((n) >> 1) * 8192 + ((s) & 3) * 2048 + (x) * 256)
#define QKV_RDA(s, n)                                                                             \
  {                                                                                               \
    if constexpr (IT == 0) bfr[n].hf.a = trabs(QKV_BO(s, n, 0));                                  \
    else bfr[n].v = *(const LDS_AS u32x4*)(size_t)(bo[0][SLOT((s) >> 2) >> 1] + (SLOT((s) >> 2) & 1) * STAGE + (((s) & 3) * 6 + (n)) * 1024); \
  }
#define QKV_RDB(s, n) { if constexpr (IT == 0) bfr[n].hf.b = trabs(QKV_BO(s, n, 1)); }
#define XP_OFF() (OFF_XNEW + ((rot + 6) & 7) * 256)
#define FP_OFF(i) (w == 3 ? OFF_XNEW + (i) * 1024 : OFF_FX + h * 3072 + w * 1024)
#define FX_OK(n) __all(((n) == 4 ? max(max(umax4(fx[0]), umax4(fx[1])), max(umax4(fx[2]), umax4(fx[3]))) : max(umax4(fx[0]), umax4(fx[1]))) != POISON)
#define SINCE(s0) (vm_at(IT, s, n) - vm_at(IT, s0, 5))
#define DEF4(x) asm volatile("" : "=v"(x[0]), "=v"(x[1]), "=v"(x[2]), "=v"(x[3]))
        LDS_AS unsigned* qcnt = (LDS_AS unsigned*)(smem + L_QC);
        unsigned qc;
        unsigned qadr = lbase + L_QC, qone = 1;
        asm volatile("" : "+v"(qadr), "+v"(qone));
#define count_in(i) asm volatile("s_mov_b64 exec, 1\n\tds_add_u32 %0, %1 offset:%2\n\ts_mov_b64 exec, -1" ::"v"(qadr), "v"(qone), "n"(4 * (i)) : "memory")
        auto count_wait = [&](int i, unsigned want) __attribute__((always_inline)) {
          if (__builtin_expect((unsigned)__builtin_amdgcn_readfirstlane(qc) < want, 0)) {
            while ((unsigned)__builtin_amdgcn_readfirstlane(__hip_atomic_load(qcnt + i, __ATOMIC_RELAXED, __HIP_MEMORY_SCOPE_WORKGROUP)) < want) { if (SPINS) __builtin_amdgcn_s_sleep(SPINS); }
          }
          asm volatile("" ::: "memory");
        };
        if constexpr (IT == 0) {
          wait_vm<12 - vm_unit(1)>();
          STAMPAT(46);
          if (!(SYNCB & 1)) count_in(0);
          X_READ(0); X_READ(1);
          asm volatile("s_waitcnt lgkmcnt(0)" : "+v"(af[0][0]), "+v"(af[0][1]), "+v"(af[1][0]), "+v"(af[1][1])::"memory");
          X_UNIT1(3, 0); X_UNIT1(3, 1); X_UNIT1(4, 0); X_UNIT1(4, 1);
          STAMPAT(38);
          qc = 0;
          if (SYNCB & 1) asm volatile("s_barrier" ::: "memory");
          else count_wait(0, 8);
        }
        STAMPAT(39);
#pragma unroll
        for (int n = 0; n < 6; ++n) { QKV_RDA(0, n); if (n < 5) QKV_RDB(0, n); }
        f32x2 lv[8];
        float lmean;
        u32x2 ep;
        u32x4 xl[2];
        f32x4 sc;
#define X1_OFF() (F_X1 + ((p * RX1 + (xcd >> 1) % RX1) * 32 + (rt >> 2) * 16 + (l & 15)) * SX1)
        unsigned long long xok = 0;
        unsigned xg;
        RowFin fin;
        auto x1_need = [&](unsigned long long m) __attribute__((always_inline)) {
          if (__builtin_expect((xok & m) != m, 0)) {
            for (unsigned spins = 0;;) {
              const u32x2 f = ld8(rs, X1_OFF(), 0);
              xok = __builtin_amdgcn_ballot_w64(f[0] == T[0] && f[1] == T[1]);
              if ((xok & m) == m) break;
              if (spin_fail(spins, rs, T)) { xok = ~0ull; break; }
              __builtin_amdgcn_s_sleep(KSL);
            }
            asm volatile("" ::: "memory");
          }
        };
        auto gap = [&](auto sc_, auto nc_) __attribute__((always_inline)) {
          constexpr int s = decltype(sc_)::value, n = decltype(nc_)::value;
          if constexpr ((SYNCB >> IT) & 1) {
            constexpr int tc = (s + 2) / 4;
            if constexpr (tc >= 1 && tc < 8 && s == 4 * tc - 2 && n == 5) {
              if constexpr (!IT || tc >= 3) wait_vm<vm_at(IT, s, n) - vm_tile(IT, tc)>();
              if constexpr (tc == 7 && !IT) asm volatile("s_waitcnt lgkmcnt(4)" ::: "memory");
              asm volatile("s_barrier" ::: "memory");
            }
          } else {
            constexpr int ts = (s + 3) / 4, tc = (s + 2) / 4, t0 = 1;
            if constexpr (ts >= 3 && ts < 8 && s == 4 * ts - 3 && n == 2) { wait_vm<vm_at(IT, s, n) - vm_tile(IT, ts)>(); count_in(0); }
            if constexpr (!IT && tc >= t0 && tc < 3 && s == 4 * tc - 2 && n == 1) { wait_vm<vm_at(IT, s, n) - vm_tile(IT, tc)>(); count_in(0); }
            if constexpr (IT && ts >= t0 && ts < 3 && s == 4 * ts - 3 && n == 2) count_in(0);
            if constexpr (tc >= t0 && tc < 8 && s == 4 * tc - 2) {
              if constexpr (n == 3) qc = __hip_atomic_load(qcnt, __ATOMIC_RELAXED, __HIP_MEMORY_SCOPE_WORKGROUP);
              if constexpr (n == 5) count_wait(0, 8 * (tc + 1 - IT));
            }
          }
          if constexpr (IT == 0 && n == 0 && s + 2 < 32) { wait_vm<vm_at(0, s, 0) - vm_unit(s + 2)>(); X_READ(s + 2); SB(); }
          if constexpr (n == 0 && tile_piece(IT, s)) QKV_DMA1(((s - tp0(IT)) >> 2) + 3, (s - tp0(IT)) & 3);
          if constexpr (IT == 0) {
            if constexpr ((n == 2 || n == 3) && s + 5 < 32) {
              if constexpr (n == 2) asm volatile("s_waitcnt lgkmcnt(%0)" ::"n"(s + 1 < QL ? 4 : 1) : "memory");
              X_UNIT1(s + 5, n - 2);
            }
            if constexpr (s >= W2S && s < W2S + 3 && n == 4) QKV_DMA1(2, s - W2S);
            if constexpr (s == LNL && n == 5) {
              asm volatile("buffer_load_dwordx2 %0, off, %1, 0 sc1" : "=v"(ep) : "s"(rs) : "memory");
              asm volatile("buffer_load_dwordx4 %0, %1, %2, %3 offen" : "=v"(xl[0]) : "v"(l * 16), "s"(rx), "s"((p * 256 + h * 8 + w) * 2048) : "memory");
              asm volatile("buffer_load_dwordx4 %0, %1, %2, %3 offen offset:1024" : "=v"(xl[1]) : "v"(l * 16), "s"(rx), "s"((p * 256 + h * 8 + w) * 2048) : "memory");
            }
            if constexpr (s == LNS && n == 1) {
              wait_vm<vm_at(0, s, n) - vm_at(0, LNL + 1, 0)>();
              asm volatile("" : "+v"(ep), "+v"(xl[0]), "+v"(xl[1])::"memory");
              const unsigned long long e = (((unsigned long long)(unsigned)__builtin_amdgcn_readfirstlane(ep[1]) << 32) | (unsigned)__builtin_amdgcn_readfirstlane(ep[0])) + 1ull + dispatch_id() * 0x9E3779B97F4A7C16ull;
              T = u32x2{(unsigned)e, (unsigned)(e >> 32)};
              unpack8v(xl[0], lv); unpack8v(xl[1], lv + 4);
              asm volatile("" : "+v"(lv[0]), "+v"(lv[1]), "+v"(lv[2]), "+v"(lv[3]), "+v"(lv[4]), "+v"(lv[5]), "+v"(lv[6]), "+v"(lv[7]));
            }
            if constexpr (s == LNS && n == 4) {
              const f32x2 sm = ((lv[0] + lv[1]) + (lv[2] + lv[3])) + ((lv[4] + lv[5]) + (lv[6] + lv[7]));
              lmean = sum64(hadd(sm)) * (1.0f / 1024.0f);
              asm volatile("" : "+v"(lmean));
            }
            if constexpr (s == LNS + 1 && n == 1) {
              f32x2 m2 = {lmean, lmean}, q0 = {0.f, 0.f}, q1 = {0.f, 0.f};
              asm volatile("" : "+v"(m2));
#pragma unroll
              for (int j = 0; j < 8; j += 2) {
                const f32x2 d0 = lv[j] - m2, d1 = lv[j + 1] - m2;
                q0 = __builtin_elementwise_fma(d0, d0, q0); q1 = __builtin_elementwise_fma(d1, d1, q1);
              }
              lv[0] = q0 + q1;
              asm volatile("" : "+v"(lv[0]));
            }
            if constexpr (s == LNS + 1 && n == 5) {
              const float rd = ln_rd(sum64(hadd(lv[0])) * (1.0f / 1024.0f));
              if (l == 0) st16(u32x4{__builtin_bit_cast(unsigned, rd), __builtin_bit_cast(unsigned, -rbf1(lmean)), T[0], T[1]}, rs, OFF_AB + (p * 256 + h * 8 + w) * 16, 0);
              if (w == 1 && l < RX) st8(T, rs, F_XN + (p * RX + l) * 256 + h * 8, 0);
            }
          } else {
            if constexpr ((s == 0 && n == 0) || (s == 2 && n == 2)) asm volatile("buffer_load_dwordx2 %0, %1, %2, 0 offen sc1" : "=v"(cxn) : "v"(X1_OFF()), "s"(rs) : "memory");
            if constexpr ((s == 1 && n == 3) || (s == 4 && n == 0)) {
              wait_vm<vm_at(1, s, n) - (s == 1 ? vm_at(1, 0, 1) : vm_at(1, 2, 3))>();
              asm volatile("" : "+v"(cxn)::"memory");
              xok |= __builtin_amdgcn_ballot_w64(cxn[0] == T[0] && cxn[1] == T[1]);
            }
            if constexpr (a_req(s, n) >= 0 && (a_req(s, n) & 7) == 0) x1_need(3ull << (((rot + a_req(s, n) / 8) & 7) * 2));
            const unsigned R = (rb * 4 + (w & 3)) * 32 + (w >> 2) * 16 + (l & 15), so = OFF_STAT + rb * 32768;
            if constexpr (s == SPL && n == 4) {
#pragma unroll
              for (int i = 0; i < 4; ++i) ld16h<0>(fx[i], rs, (4 * g + i) * 2048 + (R & 127) * 16, so);
            }
            if constexpr (s == SPL + 2 && n == 1) {
              wait_vm<SINCE(SPL)>();
              PINV4(fx);
              fin = fin_key(rs, T, g * 8192 + (R & 127) * 16, so, smem + L_SKEY, l, w, R, wsum, fx);
              count_in(2);
            }
            if constexpr (s == SPL + 3 && n == 0) qc = __hip_atomic_load(qcnt + 2, __ATOMIC_RELAXED, __HIP_MEMORY_SCOPE_WORKGROUP);
            if constexpr (s == SPL + 3 && n == 1) { count_wait(2, 8); fin_cand(rs, smem + L_SKEY, 0, g, rb, ct, fin, v); STAMPAT(51); }
            if constexpr (s == SPL + 3 && n == 3) {
              fin_x2(fin, v);
              x2p[0] = pack8v(v); x2p[1] = pack8v(v + 4);
              asm volatile("" : "+v"(x2p[0]), "+v"(x2p[1]));
            }
            if constexpr (s == SPL + 3 && n == 5) {
              if ((w & 3) * 2 + (w >> 2) == ct >> 1) {
                const float f = 1.0f + fin.rd, rd2 = ln_rd(fin.var * f * f), mb2 = rbf1(__builtin_fmaf(fin.mean - fin.mb, fin.rd, fin.mean));
                if (g == 0 && ((l >> 3) & 1) == (ct & 1))
                  st16(u32x4{__builtin_bit_cast(unsigned, f * rd2), __builtin_bit_cast(unsigned, -__builtin_fmaf(fin.mb, fin.rd, mb2) * __builtin_amdgcn_rcpf(f)), T[0], T[1]}, rs, OFF_AB + 32768 + R * 16, 0);
              }
            }
          }
          if constexpr (IT == 1 && a_req(s, n) >= 0) A_LOAD1(a_req(s, n) / 2, a_req(s, n) & 1);
          if constexpr (!IT && s == CSL && n == 4) { asm volatile("" : "=v"(fx[0])); if (w < 6) ld16h<0>(fx[0], rs, l * 16, OFF_CS + (h * 6 + w) * 1024); }
          if constexpr (!IT && s == CSS && n == 2) {
            if (w < 6) {
              wait_vm<SINCE(CSL)>();
              asm volatile("" : "+v"(fx[0])::"memory");
              for (unsigned spins = 0; !__all(fx[0][2] == T[0] && fx[0][3] == T[1]) && !spin_fail(spins, rs, T);) fx[0] = ld16(rs, l * 16, OFF_CS + (h * 6 + w) * 1024);
              const f32x2 c = {sum_hi8(asf(fx[0][0])), sum_hi8(asf(fx[0][1]))};
              if (l < 8) *(LDS_AS f32x2*)(smem + L_CSL + w * 64 + l * 8) = c;
            }
          }
          constexpr int ABL = abl(IT), ABS = abs_(IT);
          if constexpr (s == ABL && n == 4) ld16h<0>(bv[0], rs, (l & 31) * 16, AB_OFF());
          if constexpr (s == ABS && n == 2) {
            wait_vm<SINCE(ABL)>();
            asm volatile("" : "+v"(bv[0])::"memory");
            for (unsigned spins = 0; !__all(bv[0][2] == T[0] && bv[0][3] == T[1]) && !spin_fail(spins, rs, T);) bv[0] = ld16(rs, (l & 31) * 16, AB_OFF());
            const unsigned t0 = bv[0][0], t1 = bv[0][1];
            ((LDS_AS unsigned*)(smem + L_ABW + rt * 2048))[l] = l < 32 ? t0 : t1;
          }
          if constexpr (s == ABS && n == 3) {
#pragma unroll
            for (int a = 0; a < 2; ++a) {
              const LDS_AS unsigned* aw = (const LDS_AS unsigned*)(smem + L_ABW + rt * 2048) + 16 * a;
              aq[a] = aw[l & 15]; bq[a] = aw[32 + (l & 15)];
              av[a] = *(const LDS_AS u32x4*)(aw + 4 * g); bv[a] = *(const LDS_AS u32x4*)(aw + 32 + 4 * g);
            }
          }
          if constexpr (s == ABS && n == 5) {
#pragma unroll
            for (int a = 0; a < 2; ++a) { b2[a] = f32x2{asf(bq[a]), asf(bq[a])}; asm volatile("" : "+v"(b2[a])); }
          }
          if constexpr (s == ABS + 1) {
            constexpr int k = (n + 4) % 6;
            if constexpr (k < 4) cs4[k & 1] = *(const LDS_AS f32x4*)(smem + L_CSL + k * 64 + g * 16);
            else cs4[k & 1][0] = *(const LDS_AS float*)(smem + L_CSL + k * 64 + (l & 15) * 4);
          }
          if constexpr ((s == ABS + 1 && n > 0) || (s == ABS + 2 && n == 0)) {
            constexpr int k = (n + 3) % 6;
            if constexpr (k < 4) {
#pragma unroll
              for (int a = 0; a < 2; ++a) acc[a][k] = __builtin_elementwise_fma(cs4[k & 1], f32x4{b2[a][0], b2[a][1], b2[a][0], b2[a][1]}, acc[a][k]);
            } else {
              f32x2 c2 = {cs4[k & 1][0], cs4[k & 1][0]};
              asm volatile("" : "+v"(c2));
#pragma unroll
              for (int a = 0; a < 2; ++a) acc[a][k] = __builtin_elementwise_fma(f32x4{c2[0], c2[1], c2[0], c2[1]}, __builtin_bit_cast(f32x4, bv[a]), acc[a][k]);
            }
          }
          if constexpr (IT == 0) {
            if constexpr (s == CSD && n < 3) fx[n] = frag0(SLOT(s >> 2), 3 * w + n);
            if constexpr (s == CSD + 1 && n == 1) {
              unsigned one2 = 0x3F803F80u;
              asm volatile("" : "+v"(one2));
              const u32x4 o4 = {one2, one2, one2, one2};
              const f32x4 z4 = {0.f, 0.f, 0.f, 0.f};
              sa = mfma16(fx[0], o4, z4); sb = mfma16(fx[1], o4, z4); sc = mfma16(fx[2], o4, z4);
            }
            if constexpr (s == CSD + 2 && n == 1) {
              const int c = l & 15;
              const f32x4 t0 = sa, t1 = sb, t2 = sc, t = c == 0 ? +t0 : c == 1 ? +t1 : +t2;
              if (c < 3) *(LDS_AS f32x4*)(smem + L_CSP + (w * 3 + c) * 64 + g * 16) = t;
              count_in(2);
            }
            if constexpr (s == CSD + 4 && n == 1) {
              if (w == 2) {
                qc = 0;
                count_wait(2, 8);
                const LDS_AS f32x2* cp = (const LDS_AS f32x2*)(smem + L_CSP) + min(l, 47);
                const f32x2 t = (cp[0] + cp[48]) + (cp[96] + cp[144]);
                if (l < 48) st16(u32x4{__builtin_bit_cast(unsigned, (float)t[0]), __builtin_bit_cast(unsigned, (float)t[1]), T[0], T[1]}, rs, (l >> 3) * 1024 + (l & 7) * 16, OFF_CS + h * 6144 + p * 128);
              }
            }
            if constexpr (s >= 29 && (n & 1)) keep[3 * (s - 29) + (n >> 1)] = frag0(SLOT(s - 24), 3 * w + (n >> 1));
            if constexpr (s >= TS0 && s < TS0 + TSN + TSD * (7 >> TSG) && n == 4) {
              if (tstep == s && ((TM0 >> w) & 1)) TOUCH(tsrc);
            }
            if constexpr (s == TEX && n == 5) {
              const GAS bf16* t2 = ((TM1 >> w) & 1) ? tsrc : (const GAS bf16*)x + p * 262144;
              TOUCH(t2);
            }
          } else {
            if constexpr (s >= 27 && s < 30 && (n == 0 || n == 3)) { const unsigned wov = WO_VOFF(); WO_PIECE(4 + 2 * (s - 27) + n / 3); }
            if constexpr (s == FSL - 2 && n == 4) ld4h<0>(kv, rs, (l & 15) * SKEY, KEY_AT(0, KREP()));
            if constexpr (s == FSL && n == 4) {
              wait_vm<SINCE(FSL - 2)>();
              asm volatile("" : "+v"(kv)::"memory");
              xg = ffn_xoff(__builtin_amdgcn_readfirstlane(umax16(kv == POISON ? 0u : kv)), 0, w);
              ld4h<0>(kv, rs, (l & 15) * SKEY, KEY_AT(0, KREP()));
              ld16h<0>(fx[0], rs, g * 16, xg); ld16h<64>(fx[1], rs, g * 16, xg); ld16h<128>(fx[2], rs, g * 16, xg); ld16h<192>(fx[3], rs, g * 16, xg);
            }
            if constexpr (s == FS0 && n == 1) {
              wait_vm<SINCE(FSL)>();
              asm volatile("" : "+v"(kv)::"memory");
              PINV4(fx);
              STAMPAT(52);
              fkey = key_wait(rs, T, l, 0, KREP(), kv);
              STAMPAT(53);
              sel0 = key_row(fkey);
              aff = (int)(sel0 >> 8) == p;
            }
            if constexpr (s == FS0 && n == 2) {
              asm volatile("" : "=v"(sa), "=v"(sb));
              if (ffn_on) {
                if (xg != ffn_xoff(fkey, 0, w)) {
#pragma unroll
                  for (int i = 0; i < 4; ++i) fx[i] = ld16(rs, g * 16 + i * 64, ffn_xoff(fkey, 0, w));
                }
                ffn_gu(gv, uv, fx, sa, sb);
              }
            }
            if constexpr (s == FS0 && n == 3) {
              if (ffn_on) {
                ffn_gu_check(rs, T, ffn_xoff(fkey, 0, w), l, gv, uv, fx, sa, sb);
                ffn_gu_store(smem, l, w, sa, sb);
                count_in(1);
              }
            }
            if constexpr (s == FS0D) {
              if constexpr (n == 0) qc = __hip_atomic_load(qcnt + 1, __ATOMIC_RELAXED, __HIP_MEMORY_SCOPE_WORKGROUP);
              if constexpr (n == 1) {
                asm volatile("" : "=v"(dfr[0]), "=v"(dfr[1]), "=v"(dfr[2]), "=v"(dfr[3]), "=v"(dfr[4]), "=v"(dfr[5]), "=v"(dfr[6]), "=v"(dfr[7]), "=v"(hh), "=v"(sa));
                if (ffn_on) { count_wait(1, 8); ffn_d_read(smem, l, w, sa, dfr); }
              }
              if constexpr (n == 2) {
                if (ffn_on) {
                  for (int e = 0; e < 4; ++e) sa[e] = ffn_d_h(ffn_d_sum(sa[e]));
                  hh = u32x2{pack2(sa[0], sa[1]), pack2(sa[2], sa[3])};
                }
              }
              if constexpr (n == 3) {
                asm volatile("" : "=v"(sa), "=v"(sb));
                if (ffn_on) {
                  sa = f32x4{0.f, 0.f, 0.f, 0.f}; sb = sa;
#pragma unroll
                  for (int i = 0; i < 8; i += 2) { sa = ffn_d_mma(l, i, dfr[i], hh, sa); sb = ffn_d_mma(l, i + 1, dfr[i + 1], hh, sb); }
                }
              }
              if constexpr (n == 4) { if (ffn_on) ffn_d_store(rs, l, w, 0, slice, sa + sb); STAMPAT(54); }
            }
            if constexpr (s == FS1 && n == 4) {
              DEF4(fx);
              asm volatile("" : "=v"(cxn), "=v"(cx1));
              if (!ffn_on) {
                const unsigned G = xcd * 4 + bi - 28, co = (sel0 >> 7) * 2048 + G * 64, jj = (tid & 3) * 16 + (tid & 4) * 2;
#pragma unroll
                for (int k = 0; k < 4; ++k) ld16h<0>(fx[k], rs, min(tid + 512 * k, 1791) * 16, OFF_PART + G * 32768);
                ld8h(cxn, rs, jj, OFF_CA + co); ld8h(cx1, rs, jj, OFF_CB + co);
              }
            }
            if constexpr (s == FS1S && n == 2) {
              if (!ffn_on) {
                wait_vm<SINCE(FS1)>();
                PINV4(fx);
                asm volatile("" : "+v"(cxn), "+v"(cx1)::"memory");
                STAMPAT(55);
                red_sum(rs, smem, T, tid, w, 0, xcd * 4 + bi - 28, fkey, fx, cxn, cx1);
                STAMPAT(56);
                BAR();
                if (w == 0) {
                  const u32x2 ov = red_final(smem, l, fkey, cxn, cx1);
                  if (l < 8) st8(unpoison2(ov), rs, OFF_XNEW + ((xcd * 4 + bi - 28) * 8 + (l & 3) * 2 + (l >> 2)) * 8, 0);
                }
              }
            }
            if constexpr (s == FXA && n == 4) { ld16h<0>(fx[0], rs, g * 16, XP_OFF()); ld16h<64>(fx[1], rs, g * 16, XP_OFF()); ld16h<128>(fx[2], rs, g * 16, XP_OFF()); ld16h<192>(fx[3], rs, g * 16, XP_OFF()); }
            if constexpr (s == FXB) {
#define FX_FRAG(i) (*(const LDS_AS u32x4*)(size_t)(bo[0][1] + (6 * (i) + min(w, 5)) * 1024))
              if constexpr (n == 0) { STAMPAT(58); df[0] = FX_FRAG(0); df[1] = FX_FRAG(1); }
              if constexpr (n == 1) {
                wait_vm<SINCE(FXA)>();
                PINV4(fx);
                sa = mfma16(df[0], fx[0], f32x4{0.f, 0.f, 0.f, 0.f}); sb = mfma16(df[1], fx[1], f32x4{0.f, 0.f, 0.f, 0.f});
                SB();
                df[0] = FX_FRAG(2); df[1] = FX_FRAG(3);
              }
              if constexpr (n == 2) { sa = mfma16(df[0], fx[2], sa); sb = mfma16(df[1], fx[3], sb); }
              if constexpr (n == 4) {
                const float z = sa[0] + sb[0];
                if (__any(z != z)) {
                  ffn_xwait(rs, T, XP_OFF(), l, fx);
                  f32x4 t0 = mfma16(FX_FRAG(0), fx[0], f32x4{0.f, 0.f, 0.f, 0.f});
                  t0 = mfma16(df[0], fx[2], t0);
                  f32x4 t1 = mfma16(FX_FRAG(1), fx[1], f32x4{0.f, 0.f, 0.f, 0.f});
                  sa = t0; sb = mfma16(df[1], fx[3], t1);
                }
                if (w < 6) dot_store(OFF_FX);
                STAMPAT(57);
              }
            }
            if constexpr (s == FXL && n == 4) {
              asm volatile("" : "=v"(fx[0]), "=v"(fx[1]));
              if (aff && w < 4) { ld16h<0>(fx[0], rs, l * 16, FP_OFF(0)); ld16h<0>(fx[1], rs, l * 16, FP_OFF(1)); }
            }
            if constexpr (s == FXS && n == 4) {
              if (aff && w < 4) {
                STAMPAT(59);
                wait_vm<0>();
                asm volatile("" : "+v"(fx[0]), "+v"(fx[1])::"memory");
                if (!FX_OK(2)) {
                  unsigned spins = 0;
                  do {
                    if (spin_fail(spins, rs, T)) break;
                    fx[0] = ld16(rs, l * 16, FP_OFF(0)); fx[1] = ld16(rs, l * 16, FP_OFF(1));
                  } while (!FX_OK(2));
                }
                STAMPAT(60);
                if (w == 3) {
                  f32x2 nx[8];
                  unpack8v(fx[0], nx); unpack8v(fx[1], nx + 4);
                  const f32x2 sm = ((nx[0] + nx[1]) + (nx[2] + nx[3])) + ((nx[4] + nx[5]) + (nx[6] + nx[7]));
                  const float mean = sum64(hadd(sm)) * (1.0f / 1024.0f);
                  const f32x2 m2 = {mean, mean};
                  f32x2 q = {0.f, 0.f};
#pragma unroll
                  for (int j = 0; j < 8; ++j) { const f32x2 d = nx[j] - m2; q = __builtin_elementwise_fma(d, d, q); }
                  const float rd = ln_rd(sum64(hadd(q)) * (1.0f / 1024.0f));
                  if (l == 0) *(LDS_AS f32x2*)(smem + L_FXL + 384) = f32x2{rd, -rbf1(mean)};
                } else {
                  tile_sum(fx[0], L_FXL);
                }
                count_in(3);
              }
            }
          }
        };
        static_for<32>([&](auto sc_) __attribute__((always_inline)) {
          constexpr int s = decltype(sc_)::value;
          if constexpr (s > 0) PIN_ACC();
          if constexpr (IT == 1) wait_vm<vm_at(IT, s, 0) - vm_a(IT, s)>();
          pin_w<s % WA>(af[s % WA][0], af[s % WA][1]);
          VSTEP(s);
          SB();
          static_for<6>([&](auto nc_) __attribute__((always_inline)) {
            constexpr int n = decltype(nc_)::value;
            if constexpr (s == 0) { acc[0][n] = f32x4{0.f, 0.f, 0.f, 0.f}; acc[1][n] = acc[0][n]; }
            constexpr bool on = s < QL || n >= 2;
            if constexpr (on) acc[0][n] = n < 4 ? mfma16(bfr[n].v, af[s % WA][0], acc[0][n]) : mfma16(af[s % WA][0], bfr[n].v, acc[0][n]);
            SB();
            if constexpr (n == 0) { QKV_RDB(s, 5); }
            else if constexpr (s + 1 < 32 && (s + 1 < QL || n > 2)) QKV_RDB(s + 1, n - 1);
            SB();
            if constexpr (on) acc[1][n] = n < 4 ? mfma16(bfr[n].v, af[s % WA][1], acc[1][n]) : mfma16(af[s % WA][1], bfr[n].v, acc[1][n]);
            SB();
            if constexpr (s + 1 < 32 && (s + 1 < QL || n >= 2)) QKV_RDA(s + 1, n);
            SB();
            gap(sc_, nc_);
            SB();
          });
        });
        PIN_ACC();
        VSTEP(32);
        STAMPAT(18);
        VSTEP(33);
        frag_t qw[32 - QL][2];
        static_for<32 - QL>([&](auto c_) __attribute__((always_inline)) {
          constexpr int s = QL + decltype(c_)::value;
          QKV_RDA(s, 0); QKV_RDB(s, 0); QKV_RDA(s, 1); QKV_RDB(s, 1);
          qw[s - QL][0] = bfr[0]; qw[s - QL][1] = bfr[1];
        });
        SB();
        const bool own = aff && (int)((sel0 >> 5) & 7) == rt;
        const bool m0 = !(sel0 & 16), mq = (l & 15) == (int)(sel0 & 15), mv = g == (int)((sel0 & 15) >> 2);
        auto patch_qk = [&](int n) __attribute__((always_inline)) {
          f32x4 t = *(const LDS_AS f32x4*)(smem + L_FXL + n * 64 + g * 16);
          const f32x4 c = *(const LDS_AS f32x4*)(smem + L_CSL + n * 64 + g * 16);
          const float nb = *(const LDS_AS float*)(smem + L_FXL + 388);
#pragma unroll
          for (int e = 0; e < 4; ++e) { t[e] = __builtin_fmaf(c[e], nb, t[e]); asm volatile("" : "+v"(t[e])); }
          acc[0][n] = (mq && m0) ? t : acc[0][n];
          acc[1][n] = (mq && !m0) ? t : acc[1][n];
        };
        if (own) {
          while (__hip_atomic_load(qcnt + 3, __ATOMIC_RELAXED, __HIP_MEMORY_SCOPE_WORKGROUP) < 4u) {}
          asm volatile("" ::: "memory");
          patch_qk(2); patch_qk(3);
          const f32x2 nab = *(const LDS_AS f32x2*)(smem + L_FXL + 384);
          const LDS_AS float* fl_ = (const LDS_AS float*)(smem + L_FXL + 256) + (l & 15);
          const LDS_AS float* cl_ = (const LDS_AS float*)(smem + L_CSL + 256) + (l & 15);
          const float t4 = __builtin_fmaf(cl_[0], nab[1], fl_[0]), t5 = __builtin_fmaf(cl_[16], nab[1], fl_[16]);
          const unsigned na = __builtin_bit_cast(unsigned, (float)nab[0]);
#pragma unroll
          for (int a = 0; a < 2; ++a) {
            const bool ma = a ? !m0 : m0;
            aq[a] = (mq && ma) ? na : aq[a];
#pragma unroll
            for (int e = 0; e < 4; ++e) {
              const bool me = mv && ma && e == (int)(sel0 & 3);
              acc[a][4][e] = me ? t4 : acc[a][4][e]; acc[a][5][e] = me ? t5 : acc[a][5][e];
              av[a][e] = me ? na : av[a][e];
            }
          }
        }
        constexpr float C2 = 1.4426950408889634f * 0.17677669529663687f;
#pragma unroll
        for (int a = 0; a < 2; ++a) {
          f32x2 a2 = {asf(aq[a]), asf(aq[a])};
          qs2[a] = f32x2{asf(aq[a]) * C2, asf(aq[a]) * C2};
          asm volatile("" : "+v"(a2), "+v"(qs2[a]));
          acc[a][2] *= f32x4{a2[0], a2[1], a2[0], a2[1]}; acc[a][3] *= f32x4{a2[0], a2[1], a2[0], a2[1]};
          acc[a][4] *= __builtin_bit_cast(f32x4, av[a]); acc[a][5] *= __builtin_bit_cast(f32x4, av[a]);
        }
        k0 = pack8(acc[0][2], acc[0][3]); k1 = pack8(acc[1][2], acc[1][3]); v0 = pack8(acc[0][4], acc[1][4]); v1 = pack8(acc[0][5], acc[1][5]);
        const unsigned kvo = OFF_KV + IT * (8u << 20) + (p * 32 + h) * 32768 + rt * 4096;
        if (p < 7) { st16(k0, rs, l * 16, kvo); st16(k1, rs, l * 16 + 1024, kvo); st16(v0, rs, l * 16 + 2048, kvo); st16(v1, rs, l * 16 + 3072, kvo); }
        char* ownp = smem + (8 + rt) * 2048 + l * 16;
        *(u32x4*)(ownp + L_K) = k0;
        if (rt < 7) *(u32x4*)(ownp + L_K + 1024) = k1;
        *(u32x4*)(ownp + L_V) = v0; *(u32x4*)(ownp + L_V + 1024) = v1;
        SB();
        static_for<32 - QL>([&](auto c_) __attribute__((always_inline)) {
          constexpr int s = QL + decltype(c_)::value;
#pragma unroll
          for (int n = 0; n < 2; ++n) { acc[0][n] = mfma16(qw[s - QL][n].v, af[s % WA][0], acc[0][n]); acc[1][n] = mfma16(qw[s - QL][n].v, af[s % WA][1], acc[1][n]); }
        });
        if (own) { patch_qk(0); patch_qk(1); }
      };
      qkv_loop(it_c);
    }
#undef A_LOAD1
#undef QKV_RDA
#undef QKV_RDB
#undef QKV_BO
    STAMPAT(2); STAMPAT(21);
    {
      IDS();
      const GAS bf16 *pwo, *pwg, *pwu;
      asm volatile("s_load_dwordx2 %0, %3, 24\n\ts_load_dwordx2 %1, %3, 40\n\ts_load_dwordx2 %2, %3, 48" : "=&s"(pwo), "=&s"(pwg), "=&s"(pwu) : "s"(ka));
      const int ql = l & 15;
      const f32x4 z4 = {0.f, 0.f, 0.f, 0.f}, ninf = {-INFINITY, -INFINITY, -INFINITY, -INFINITY};
      LDS_AS unsigned* rdy = (LDS_AS unsigned*)(smem + L_RDY);
      const unsigned rtag = T[0] + it;
      u32x4 qf[2];
      f32x4 cn[2], oa[2][2], sum[2], S[2][2];
      unsigned one2 = 0x3F803F80u;
      asm volatile("" : "+v"(one2));
      const u32x4 ones = {one2, one2, one2, one2};
      auto ex8 = [&](f32x4 a, f32x4 b) __attribute__((always_inline)) {
        return u32x4{pack2(__builtin_amdgcn_exp2f(a[0]), __builtin_amdgcn_exp2f(a[1])), pack2(__builtin_amdgcn_exp2f(a[2]), __builtin_amdgcn_exp2f(a[3])),
                     pack2(__builtin_amdgcn_exp2f(b[0]), __builtin_amdgcn_exp2f(b[1])), pack2(__builtin_amdgcn_exp2f(b[2]), __builtin_amdgcn_exp2f(b[3]))};
      };
      {
#pragma unroll
        for (int a = 0; a < 2; ++a) {
          const f32x4 c4 = {qs2[a][0], qs2[a][1], qs2[a][0], qs2[a][1]};
          qf[a] = pack8(acc[a][0] * c4, acc[a][1] * c4);
        }
        f32x4 mk;
#pragma unroll
        for (int e = 0; e < 4; ++e) mk[e] = (4 * g + e <= ql) ? 0.f : -INFINITY;
        const f32x4 d0 = mfma16(k0, qf[0], mk), d1 = mfma16(k1, qf[1], mk), d2 = mfma16(k0, qf[1], z4);
        const float m0 = -max_x32(max_x16(fmaxf(fmaxf(d0[0], d0[1]), fmaxf(d0[2], d0[3]))));
        const float m1 = -max_x32(max_x16(fmaxf(fmaxf(d1[0], d1[1]), fmaxf(d1[2], d1[3]))));
        cn[0] = f32x4{m0, m0, m0, m0}; cn[1] = f32x4{m1, m1, m1, m1};
        asm volatile("" : "+v"(cn[0]), "+v"(cn[1]));
        DRAIN();
        if (l == 0) st8(T, rs, rt * SKW, F_KV + (it * 256 + p * 32 + h) * SKV);
        STAMPAT(34);
        const f32x4 e0 = d0 + cn[0];
        const u32x4 p0 = {pack2(__builtin_amdgcn_exp2f(e0[0]), __builtin_amdgcn_exp2f(e0[1])), pack2(__builtin_amdgcn_exp2f(e0[2]), __builtin_amdgcn_exp2f(e0[3])), 0u, 0u};
        const u32x4 p1 = ex8(d2 + cn[1], d1 + cn[1]);
        oa[0][0] = mfma16(v0, p0, z4); oa[0][1] = mfma16(v1, p0, z4); sum[0] = mfma16(ones, p0, z4);
        oa[1][0] = mfma16(v0, p1, z4); oa[1][1] = mfma16(v1, p1, z4); sum[1] = mfma16(ones, p1, z4);
      }
      const char* kb_ = smem + L_K + l * 16;
      const char* vb_ = smem + L_V + l * 16;
      const unsigned wov = WO_VOFF(), fnv = (l >> 1) * (F_ * 2) + (l & 1) * 16;
      asm volatile("" : "+v"(oa[0][0]), "+v"(oa[0][1]), "+v"(oa[1][0]), "+v"(oa[1][1]), "+v"(sum[0]), "+v"(sum[1]));
      const unsigned pw = __builtin_amdgcn_readfirstlane(it * 256 + p * 32 + h - 32), pfo = F_KV + pw * SKV + rt * SKW;
      const bool fetcher = rt < 4 && p > 0;
      STAMPAT(32);
      BAR();
      asm volatile("" : "+s"(pwo), "+s"(pwg), "+s"(pwu));
      STAMPAT(33);
#define KV_FETCH(gd)                                                                                                          \
  {                                                                                                                           \
    const unsigned pvo = OFF_KV + pw * 32768 + (gd) * 4096;                                                                   \
    dma16s<0>(rs, l * 16, pvo, lbase + L_K + (gd) * 2048); dma16s<1024>(rs, l * 16, pvo, lbase + L_K + (gd) * 2048);           \
    dma16s<0>(rs, l * 16, pvo + 2048, lbase + L_V + (gd) * 2048); dma16s<1024>(rs, l * 16, pvo + 2048, lbase + L_V + (gd) * 2048); \
  }
      if (fetcher) {
        if (KVS) __builtin_amdgcn_s_sleep(KVS);
        poll_flags(rs, (l & 1) * 4 * SKW, pfo, T);
        KV_FETCH(rt); KV_FETCH(rt + 4);
      }
      STAMPAT(35);
#define LDS16(a_) (*(const LDS_AS u32x4*)(size_t)(a_))
      u32x4 kf[2], vf[2], p0, p1;
#define AE(a, i) { S[a][(i) >> 2][(i) & 3] = __builtin_amdgcn_exp2f(S[a][(i) >> 2][(i) & 3]); asm volatile("" : "+v"(S[a][(i) >> 2][(i) & 3])); SB(); }
#define AC(a, P, j) { P[j] = pack2(S[a][(j) >> 1][2 * ((j) & 1)], S[a][(j) >> 1][2 * ((j) & 1) + 1]); asm volatile("" : "+v"(P[j])); SB(); }
#define PHASE(a, P, M1, M2, L2, M3, M4, L4, M5)                                                                               \
  AE(a, 0) AE(a, 1) M1; SB(); AE(a, 2) AE(a, 3) M2; SB(); L2; SB(); AC(a, P, 0) AC(a, P, 1) AE(a, 4) M3; SB(); AE(a, 5) AE(a, 6) M4; SB(); \
  L4; SB(); AE(a, 7) AC(a, P, 2) M5; SB(); AC(a, P, 3)
#define PV(a, d, P) oa[a][d] = mfma16(vf[d], P, oa[a][d])
#define RS(a, P) sum[a] = mfma16(ones, P, sum[a])
#define SC(a, t, c) S[a][t] = mfma16(kf[t], qf[a], c)
#define K_AT(ad) (kf[0] = LDS16(ad), kf[1] = LDS16((ad) + 1024))
#define V_AT(ad) (vf[0] = LDS16((ad) + L_V), vf[1] = LDS16((ad) + L_V + 1024))
#define A_FIRST(ad, c00, c01, c10, c11, KN) { K_AT(ad); V_AT(ad); SB(); SC(0, 0, c00); SC(0, 1, c01); SB(); PHASE(0, p0, SC(1, 0, c10), SC(1, 1, c11), KN, , , , ) }
#define A_NEXT(ad, c00, c01, c10, c11, KN)                                                                                        \
  { PHASE(1, p1, PV(0, 0, p0), PV(0, 1, p0), , SC(0, 0, c00), SC(0, 1, c01), , RS(0, p0))                                     \
    PHASE(0, p0, PV(1, 0, p1), PV(1, 1, p1), V_AT(ad), SC(1, 0, c10), SC(1, 1, c11), KN, RS(1, p1)) }
#define A_LAST() { PHASE(1, p1, PV(0, 0, p0), PV(0, 1, p0), , RS(0, p0), , , ) SB(); PV(1, 0, p1); PV(1, 1, p1); RS(1, p1); }
      int nw = 0;
      auto piece = [&]() __attribute__((always_inline)) {
        if constexpr (it == 0) { if (ffn_on) { FFN_DMA(nw); } ++nw; }
      };
      unsigned ad = lbase + l * 16 + (6 + rt) * 2048;
      asm volatile("" : "+v"(ad));
      if (rt > 0) {
        A_FIRST(ad + 2048, cn[0], cn[0], cn[1], cn[1], K_AT(ad));
#pragma unroll 1
        for (int i = rt - 1; i > 0; --i) {
          ad -= 2048;
          asm volatile("" : "+v"(ad));
          piece();
          A_NEXT(ad + 2048, cn[0], cn[0], cn[1], cn[1], K_AT(ad));
        }
        piece();
        A_LAST();
      }
      STAMPAT(26);
      {
        f32x4 mk;
#pragma unroll
        for (int e = 0; e < 4; ++e) mk[e] = (4 * g + e > ql) ? 0.f : -INFINITY;
        f32x4 cm0 = cn[0] + mk, cm1 = cn[1] + mk, cmi = ninf;
        asm volatile("" : "+v"(cm0), "+v"(cm1), "+v"(cmi));
        if (rt >= 4) {
          static_for<it ? 12 : 8>([&](auto c_) __attribute__((always_inline)) { constexpr int i = decltype(c_)::value; WO_PIECE_W(15 - i / 2, 2 * (rt & 3) + (i & 1)); });
        } else if (p > 0) {
          DRAIN();
          if (l < 2) rdy[rt + 4 * l] = rtag;
        }
        STAMPAT(3);
        if constexpr (it == 0) {
          if (ffn_on) FFN_DMA(7);
#pragma unroll
          for (int i = 0; i < 3; ++i) st16(keep[i], rs, l * 16 + i * 1024, OFF_WI + (h * 8 + ((ROT0 + 5) & 7)) * STAGE + w * 3072);
          unsigned pz1 = POISON;
          asm volatile("" : "+v"(pz1));
          const u32x4 pz = {pz1, pz1, pz1, pz1};
          const unsigned prb = 2 * p + (h >> 4), pct = h & 15;
          st16(pz, rs, OFF_PART + (tid >> 8) * (1u << 20) + bid * 4096 + (tid & 255) * 16, 0);
          if (w < 4) st16(pz, rs, OFF_STAT + (tid >> 7) * 524288 + (prb * 16 + pct) * 2048 + (tid & 127) * 16, 0);
          if (w == 4 && l < 24) st16(pz, rs, (l < 8 ? OFF_CA + 32768 : l < 16 ? OFF_CB : OFF_CB + 32768) + prb * 2048 + pct * 128 + (l & 7) * 16, 0);
          if (w == 7 && l < 24) st16(pz, rs, OFF_FX + ((h * 6 + (l >> 2)) * 8 + p) * 64 + (l & 3) * 16, 0);
          if (w == 6 && l < (pct < RKEY ? 3 : 2)) st4(pz1, rs, l < 2 ? OFF_XNEW + bid * 8 + l * 4 : KEY_AT(1, pct) + prb * SKEY, 0);
        }
        if (p > 0) {
          unsigned have = 0;
          auto need = [&](int gq) __attribute__((always_inline)) {
            if (__builtin_expect(gq < 8 && !((have >> gq) & 1), 0)) {
              do {
                const unsigned t = __hip_atomic_load(rdy + (l & 7), __ATOMIC_RELAXED, __HIP_MEMORY_SCOPE_WORKGROUP);
                have = (unsigned)__builtin_amdgcn_ballot_w64(t == rtag);
              } while (!((have >> gq) & 1));
              asm volatile("" ::: "memory");
            }
          };
          ad = lbase + l * 16 + rt * 2048;
          asm volatile("" : "+v"(ad));
          need(rt);
          K_AT(ad); V_AT(ad); SB(); SC(0, 0, cm0); SC(0, 1, cn[0]); SB();
          PHASE(0, p0, SC(1, 0, cmi), SC(1, 1, cm1), , , , , )
          need(rt + 1);
          K_AT(ad + 2048);
#pragma unroll 1
          for (int gq = rt + 1; gq < 8; ++gq) {
            ad += 2048;
            asm volatile("" : "+v"(ad));
            piece();
            need(gq + 1);
            A_NEXT(ad, cn[0], cn[0], cn[1], cn[1], K_AT(ad + 2048));
          }
          A_LAST();
        } else {
          if constexpr (it == 0) { for (; nw < 7;) piece(); }
        }
      }
      STAMPAT(25);
      {
        float big = 0.f;
#pragma unroll
        for (int a = 0; a < 2; ++a)
#pragma unroll
          for (int d = 0; d < 2; ++d)
            big = __builtin_elementwise_maximum(__builtin_elementwise_maximum(big, __builtin_elementwise_maximum(fabsf(oa[a][d][0]), fabsf(oa[a][d][1]))),
                                                __builtin_elementwise_maximum(fabsf(oa[a][d][2]), fabsf(oa[a][d][3])));
        bool ok = sum[0][0] >= 0.5f && sum[0][0] < 0x1p64f && sum[1][0] >= 0.5f && sum[1][0] < 0x1p64f && big < INFINITY;
        if (!__all(ok)) {
#pragma unroll
          for (int a = 0; a < 2; ++a) {
            const int qs = 2 * rt + a, qq = 256 + 16 * qs + ql, klo = max(qq - 255, p > 0 ? 0 : 256);
            auto score = [&](int t) __attribute__((always_inline)) {
              f32x4 s = mfma16(t == 31 ? k1 : *(const u32x4*)(kb_ + t * 1024), qf[a], z4);
#pragma unroll
              for (int e = 0; e < 4; ++e) { const int kk = 16 * t + 4 * g + e; s[e] = (kk <= qq && kk >= klo) ? s[e] : -INFINITY; }
              return s;
            };
            float mx = -INFINITY;
#pragma unroll 1
            for (int t = p > 0 ? qs : 16; t <= qs + 16; ++t) { const f32x4 s = score(t); mx = fmaxf(fmaxf(mx, fmaxf(s[0], s[1])), fmaxf(s[2], s[3])); }
            mx = max_x32(max_x16(mx));
            oa[a][0] = z4; oa[a][1] = z4; sum[a] = z4;
#pragma unroll 1
            for (int G = p > 0 ? qs >> 1 : 8; G <= (qs + 16) >> 1; ++G) {
              const f32x4 s0 = score(2 * G), s1 = score(2 * G + 1);
              f32x4 e0, e1;
#pragma unroll
              for (int e = 0; e < 4; ++e) { e0[e] = __builtin_amdgcn_exp2f(s0[e] - mx); e1[e] = __builtin_amdgcn_exp2f(s1[e] - mx); }
              const u32x4 pf = pack8(e0, e1);
              oa[a][0] = mfma16(*(const u32x4*)(vb_ + G * 2048), pf, oa[a][0]);
              oa[a][1] = mfma16(*(const u32x4*)(vb_ + G * 2048 + 1024), pf, oa[a][1]);
              sum[a] = mfma16(ones, pf, sum[a]);
            }
          }
        }
      }
#pragma unroll
      for (int a = 0; a < 2; ++a) {
        const float inv = __builtin_amdgcn_rcpf(sum[a][0]);
        oa[a][0] *= inv; oa[a][1] *= inv;
      }
      const unsigned oo = OFF_O + it * (4u << 20) + ((p * 8 + rt) * 64 + h * 2) * 1024;
      st16(pack8(oa[0][0], oa[0][1]), rs, l * 16, oo);
      st16(pack8(oa[1][0], oa[1][1]), rs, l * 16 + 1024, oo);
      STAMPAT(4);
      DRAIN();
      BAR();
      if (tid == 0) st8(T, rs, h * SO, F_O + (it * 8 + p) * 32 * SO);
      if constexpr (it == 0) {
        DEF4(gv); DEF4(uv);
        if (ffn_on) {
          const unsigned ro = 65536 + w * 1024 + (4 * g + ((l & 15) >> 2)) * 32 + (l & 3) * 8;
#pragma unroll
          for (int i = 0; i < 4; ++i) {
            frag_t f;
            f.hf.a = trread(smem, ro + i * 16384); f.hf.b = trread(smem, ro + i * 16384 + 512);
            gv[i] = f.v;
            f.hf.a = trread(smem, ro + i * 16384 + 8192); f.hf.b = trread(smem, ro + i * 16384 + 8704);
            uv[i] = f.v;
          }
        }
        PINV4(gv); PINV4(uv);
        WO_PIECE(11); WO_PIECE(10); WO_PIECE(9); WO_PIECE(8); WO_PIECE(7); WO_PIECE(6); WO_PIECE(5); WO_PIECE(4);
      }
      WO_PIECE(3); WO_PIECE(2); WO_PIECE(1); WO_PIECE(0);
    }
    STAMPAT(5);
    IDS();
    ARGP(pwr, 4); ARGP(pwd, 7); ARGP(pout, 8); ARGP(px, 0);
    const int kh = w >> 2, rt2 = rb * 4 + (w & 3);
    const unsigned R = rt2 * 32 + kh * 16 + (l & 15);
    const unsigned to = (rt2 * 64 + ct * 4 + kh) * 1024 + l * 16;
    const unsigned rmo = ct * 64 + ((g & 1) ? 12 + 4 * g : 4 * g);
    f32x2 wv[8];
    {
      u32x4 wa[2], wrp[2], res[2];
#pragma unroll
      for (int j = 0; j < 2; ++j) {
        wrp[j] = *(const GAS u32x4*)(pwr + rmo + 32 * j);
        wa[j] = *(const GAS u32x4*)(pwr + l * 8 + j * 512);
        if constexpr (it == 0) res[j] = *(const GAS u32x4*)(px + R * 1024 + rmo + 32 * j);
      }
      {
        f32x2 t[8];
        unpack8v(wa[0], t); unpack8v(wa[1], t + 4);
        const f32x2 s = ((t[0] + t[1]) + (t[2] + t[3])) + ((t[4] + t[5]) + (t[6] + t[7]));
        wsum = sum64(hadd(s));
        unpack8v(trade16(wrp[0]), wv); unpack8v(trade16(wrp[1]), wv + 4);
        if constexpr (it == 0) { res[0] = trade16(res[0]); res[1] = trade16(res[1]); asm volatile("" : "+v"(res[0]), "+v"(res[1])); }
        asm volatile("" : "+v"(wsum), "+v"(wv[0]), "+v"(wv[1]), "+v"(wv[2]), "+v"(wv[3]), "+v"(wv[4]), "+v"(wv[5]), "+v"(wv[6]), "+v"(wv[7]));
      }
      unsigned bt[4];
#pragma unroll
      for (int n = 0; n < 4; ++n) {
        bt[n] = lbase + 32768 + 65536 + kh * 4096 + (4 * g + ((l & 15) >> 2)) * 128 + (l & 3) * 8 + ((n ^ (2 * (g & 1) + ((l & 15) >> 3))) << 5);
        asm volatile("" : "+v"(bt[n]));
      }
      if (w == 0) poll_flags(rs, (l & 31) * SO, F_O + (it * 8 + (rb >> 1)) * 32 * SO, T);
      else if (it == 0 && w < 3) poll_flags2(rs, F_XN + (((w - 1) * 4 + g) * RX + bi % RX) * 256 + (l & 15) * 16, T);
      asm volatile("s_barrier" ::: "memory");
      STAMPAT(6);
      const unsigned abase = OFF_O + it * (4u << 20) + (rt2 * 64 + 32 * kh) * 1024;
      const unsigned av0 = l * 16 + kh * 1024, av1 = l * 16 + (kh ^ 1) * 1024;
      u32x4 af[WB][2];
#define O_LOAD(s_) { af[(s_) % WB][0] = ld16(rs, av0 + (~(s_) & 1) * 2048, abase + ((15 - (s_)) >> 1) * 4096); af[(s_) % WB][1] = ld16(rs, av1 + (~(s_) & 1) * 2048, abase + ((15 - (s_)) >> 1) * 4096); }
#pragma unroll
      for (int s = 0; s < WR; ++s) O_LOAD(s);
#pragma unroll
      for (int j = 0; j < 2; ++j) {
        if constexpr (it == 1) res[j] = ld16(rs, OFF_XNEW + ((2 * ct + j) * 4 + g) * 16, 0);
      }
      SB();
      f32x4 ac[2][4];
#pragma unroll
      for (int a = 0; a < 2; ++a)
#pragma unroll
        for (int n = 0; n < 4; ++n) ac[a][n] = f32x4{0.f, 0.f, 0.f, 0.f};
      u32x4 bfr[2][4];
#define WO_RD(buf, s, n)                                                                             \
  {                                                                                                  \
    const unsigned o_ = bt[n] + ((15 - (s)) & 7) * 8192;                                             \
    frag_t f_;                                                                                       \
    f_.hf.a = trabs(o_); f_.hf.b = trabs(o_ + 2048);                                                 \
    bfr[buf][n] = f_.v;                                                                              \
  }
#pragma unroll
      for (int n = 0; n < 4; ++n) WO_RD(0, 0, n);
#pragma unroll
      for (int s = 0; s < 16; ++s) {
        const int cur = s & 1;
        asm volatile("" : "+{v[176:179]}"(ac[0][0]), "+{v[180:183]}"(ac[0][1]), "+{v[184:187]}"(ac[0][2]), "+{v[188:191]}"(ac[0][3]),
                          "+{v[192:195]}"(ac[1][0]), "+{v[196:199]}"(ac[1][1]), "+{v[200:203]}"(ac[1][2]), "+{v[204:207]}"(ac[1][3]));
        if (s == (it == 0 ? 3 : 11)) asm volatile("s_barrier" ::: "memory");
        if (s == 7) {
#pragma unroll
          for (int n = 0; n < 4; ++n) { bt[n] -= 65536; asm volatile("" : "+v"(bt[n])); }
        }
        if (s < WB - WR) O_LOAD(WR + 2 * s < 16 ? WR + 2 * s : 0);
        SB();
#pragma unroll
        for (int n = 0; n < 4; ++n) {
          ac[0][n] = mfma16(bfr[cur][n], af[s % WB][0], ac[0][n]);
          ac[1][n] = mfma16(bfr[cur][n], af[s % WB][1], ac[1][n]);
          if (s + 1 < 16) WO_RD(cur ^ 1, s + 1, n);
          if (s == 9) {
            if (it == 1 && n == 0) { res[0] = R == sel0 ? res[0] : x2p[0]; res[1] = R == sel0 ? res[1] : x2p[1]; }
            v[2 * n] = f32x2{lo16(res[n >> 1][2 * (n & 1)]), hi16(res[n >> 1][2 * (n & 1)])};
            v[2 * n + 1] = f32x2{lo16(res[n >> 1][2 * (n & 1) + 1]), hi16(res[n >> 1][2 * (n & 1) + 1])};
            asm volatile("" : "+v"(v[2 * n]), "+v"(v[2 * n + 1]));
          }
          SB();
        }
        if (s < WB - WR) { O_LOAD(WR + 2 * s + 1); }
        else if (s + WB < 16) { O_LOAD(s + WB); }
        SB();
      }
#undef WO_RD
#undef O_LOAD
      STAMPAT(7);
#define XCH_AT(v) ((v) == 7 ? 155648 : (v) * 4096)
#pragma unroll
      for (int n = 0; n < 4; ++n) *(f32x4*)(smem + XCH_AT(w) + n * 1024 + l * 16) = ac[1][n];
#pragma unroll
      for (int n = 0; n < 4; ++n) { v[2 * n] += f32x2{ac[0][n][0], ac[0][n][1]}; v[2 * n + 1] += f32x2{ac[0][n][2], ac[0][n][3]}; }
      BAR();
#pragma unroll
      for (int n = 0; n < 4; ++n) {
        const f32x4 o = *(const f32x4*)(smem + XCH_AT(w ^ 4) + n * 1024 + l * 16);
        v[2 * n] += f32x2{o[0], o[1]}; v[2 * n + 1] += f32x2{o[2], o[3]};
      }
    }
    {
      if (it == 0) { st16(pack8v(v), rs, to, OFF_XN + (4u << 20)); st16(pack8v(v + 4), rs, to + 2048, OFF_XN + (4u << 20)); }
      SB();
      const f32x2 s = ((v[0] + v[1]) + (v[2] + v[3])) + ((v[4] + v[5]) + (v[6] + v[7]));
      f32x2 dp = v[0] * wv[0], q = v[0] * v[0];
#pragma unroll
      for (int j = 1; j < 8; ++j) { dp = __builtin_elementwise_fma(v[j], wv[j], dp); q = __builtin_elementwise_fma(v[j], v[j], q); }
      float mt = hadd(s), qq = hadd(q), dot = hadd(dp);
      add_g3(mt, qq, dot);
      const unsigned so = OFF_STAT + it * 524288 + rb * 32768, lrow = R & 127;
      if (g == 0) st16(unpoison4(__builtin_bit_cast(u32x4, f32x4{mt, qq, dot, 0.f})), rs, ct * 2048 + lrow * 16, so);
      SB();
      if (it == 0) wait_vm<1>();
      if constexpr (it == 0) {
        volatile LDS_AS u32x2* in = (volatile LDS_AS u32x2*)(smem + L_XC);
        if (l == 0) in[w] = T;
        const u32x2 t = in[l & 7];
        if (__all(t[0] == T[0] && t[1] == T[1]) && l < (int)RX1) st8(T, rs, (l * 32 + (rb & 1) * 16 + ct) * SX1, F_X1 + (rb >> 1) * RX1 * 32 * SX1);
      }
      STAMPAT(8);
      if (ffn_on) { WD_DMA(); }
      if (it == 0) {
#pragma unroll
        for (int j = 0; j < 9; ++j) *(u32x4*)(smem + L_RING + (j / 3) * STAGE + (w * 3 + j % 3) * 1024 + l * 16) = keep[j];
      }
      STAMPAT(16);
      if constexpr (it == 0) {
        if (w == 0) poll_flags(rs, ((l & 2) * 8 + ((ROT0 + 5) & 7) * 2 + (l & 1)) * SX1, F_X1 + (p * RX1 + (xcd >> 1) % RX1) * 32 * SX1, T);
        if (w == 1) poll_flags(rs, ((l & 7) * 32 + h) * SO, F_O, T);
        if (tid == 192) *(LDS_AS u32x4*)(smem + L_QC) = u32x4{0u, 0u, 0u, 0u};
        BAR();
        PRE_LOAD(1, ROT0 + 5, 0, 4);
        STAMPAT(9);
      } else {
        __builtin_amdgcn_s_sleep(SDIR);
        u32x4 sp[4];
#pragma unroll
        for (int i = 0; i < 4; ++i) sp[i] = ld16(rs, (4 * g + i) * 2048 + lrow * 16, so);
        const RowFin r = fin_key(rs, T, g * 8192 + lrow * 16, so, smem + L_XC, l, w, R, wsum, sp);
        STAMPAT(9);
        BAR();
        fin_cand(rs, smem + L_XC, 1, g, rb, ct, r, v);
        STAMPAT(10);
        fin_x2(r, v);
      }
    }
    STAMPAT(11);
    if (it == 1) {
      const u32x4 op0 = trade16(pack8v(v)), op1 = trade16(pack8v(v + 4));
#define OUT_STORE()                                                                                 \
  if (R != key_row(key)) {                                                                          \
    __builtin_amdgcn_raw_buffer_store_b128(op0, rout, (R * 1024 + rmo) * 2, 0, SC1);                  \
    __builtin_amdgcn_raw_buffer_store_b128(op1, rout, (R * 1024 + rmo + 32) * 2, 0, SC1);             \
  }
      u32x4 xv[4];
      unsigned guess = 0;
      {
        __builtin_amdgcn_s_sleep(GSL);
        const unsigned k_ = key_load(rs, l, 1, KREP());
        guess = ffn_xoff(__builtin_amdgcn_readfirstlane(umax16(k_ == POISON ? 0u : k_)), 1, w);
        ld16h<0>(xv[0], rs, g * 16, guess); ld16h<64>(xv[1], rs, g * 16, guess); ld16h<128>(xv[2], rs, g * 16, guess); ld16h<192>(xv[3], rs, g * 16, guess);
      }
      if (w == 0) {
        const unsigned k_ = key_wait(rs, T, l, 1, KREP(), key_load(rs, l, 1, KREP()));
        if (l == 0) *(LDS_AS unsigned*)(smem + L_SKEY + 32) = k_;
      }
      DRAIN();
      PINV4(xv);
      BAR();
      const unsigned key = __builtin_amdgcn_readfirstlane(*(LDS_AS unsigned*)(smem + L_SKEY + 32));
      STAMPAT(12);
      if (ffn_on) {
        if (guess != ffn_xoff(key, 1, w)) {
#pragma unroll
          for (int i = 0; i < 4; ++i) xv[i] = ld16(rs, g * 16 + i * 64, ffn_xoff(key, 1, w));
        }
        f32x4 ga, ua;
        ffn_gu(gv, uv, xv, ga, ua);
        ffn_gu_check(rs, T, ffn_xoff(key, 1, w), l, gv, uv, xv, ga, ua);
        ffn_gu_store(smem, l, w, ga, ua);
        BAR();
        f32x4 t, ya[2] = {{0.f, 0.f, 0.f, 0.f}, {0.f, 0.f, 0.f, 0.f}};
        s16x4 dfr[8];
        ffn_d_read(smem, l, w, t, dfr);
#pragma unroll
        for (int j = 0; j < 4; ++j) t[j] = ffn_d_h(ffn_d_sum(t[j]));
        const u32x2 hh = {pack2(t[0], t[1]), pack2(t[2], t[3])};
#pragma unroll
        for (int i = 0; i < 8; ++i) ya[i & 1] = ffn_d_mma(l, i, dfr[i], hh, ya[i & 1]);
        ffn_d_store(rs, l, w, 1, slice, ya[0] + ya[1]);
        SB();
        OUT_STORE();
        STAMPAT(13);
      } else {
        OUT_STORE();
        const unsigned G = xcd * 4 + bi - 28;
        if (G == 0 && tid == 0) st8(T, rs, F_EP, 0);
        u32x4 pv[4];
        u32x2 cxn, cx1;
        __builtin_amdgcn_s_sleep(RSL);
        red_load(rs, tid, 1, G, key, pv, cxn, cx1);
        red_sum(rs, smem, T, tid, w, 1, G, key, pv, cxn, cx1);
        BAR();
        if (w == 0) {
          const u32x2 ov = red_final(smem, l, key, cxn, cx1);
          if (l < 8) __builtin_amdgcn_raw_buffer_store_b64(ov, rout, (key_row(key) * 1024 + G * 32 + l * 4) * 2, 0, 0);
        }
      }
#undef OUT_STORE
    }
    STAMPAT(15); STAMPAT(23);
  };
  layer(std::integral_constant<int, 0>{});
  layer(std::integral_constant<int, 1>{});
}

extern "C" void kernel_launch(void* const* d_in, const int* in_sizes, int n_in, void* d_out, int out_size,
                              void* d_ws, size_t ws_size, hipStream_t stream) {
  (void)hipFuncSetAttribute((const void*)mega_kernel, hipFuncAttributeMaxDynamicSharedMemorySize, LDS_BYTES);
  mega_kernel<<<256, 512, LDS_BYTES, stream>>>((const bf16*)d_in[0], (char*)d_ws, (const bf16*)d_in[1], (const bf16*)d_in[2], (const bf16*)d_in[4],
                                               (const bf16*)d_in[5], (const bf16*)d_in[6], (const bf16*)d_in[7], (bf16*)d_out);
}
```

```cpp
#include <hip/hip_runtime.h>
#include <stdint.h>
#include <utility>

typedef __bf16 bf16;
typedef __bf16 bf16x8 __attribute__((ext_vector_type(8)));
typedef __bf16 bf16x2 __attribute__((ext_vector_type(2)));
typedef float f32x4 __attribute__((ext_vector_type(4)));
typedef float f32x2 __attribute__((ext_vector_type(2)));
typedef short s16x4 __attribute__((ext_vector_type(4)));
typedef unsigned int u32x4 __attribute__((ext_vector_type(4)));
typedef unsigned int u32x2 __attribute__((ext_vector_type(2)));
#define LDS_AS __attribute__((address_space(3)))
#define GAS __attribute__((address_space(1)))
typedef GAS unsigned gu32;
typedef GAS unsigned long long gu64;
#define RLX_AGENT __ATOMIC_RELAXED, __HIP_MEMORY_SCOPE_AGENT
typedef __amdgpu_buffer_rsrc_t rsrc_t;

constexpr int F_ = 3584;
constexpr int SC1 = 16;

constexpr int RX = 4;
constexpr unsigned F_EP = 0, F_TMO = 128;
constexpr unsigned F_XN = 4096;
#define FLG 256, 1024, 8, 64, 4, 256, 4
constexpr unsigned FLV[7] = {FLG};
constexpr unsigned SO = FLV[0], SKV = FLV[1], SKW = FLV[2], SX1 = FLV[3], RX1 = FLV[4], SKEY = FLV[5], RKEY = FLV[6];
constexpr unsigned F_O = 8u << 20;
constexpr unsigned F_KV = 11u << 20;
constexpr unsigned F_X1 = 9u << 20;
constexpr unsigned OFF_KEY = 10u << 20;
#define KREP() (RKEY == 16 ? bid & 15 : (bid & 7) * RKEY / 8)
#define KEY_AT(L, rep) (OFF_KEY + ((L) * RKEY + (rep)) * (SKEY < 8 ? 128 : 16 * SKEY))
constexpr unsigned OFF_AB = 2u << 20;
constexpr unsigned OFF_XNEW = 262144;
constexpr unsigned OFF_CS = OFF_AB + 65536;
constexpr unsigned OFF_FX = 393216;
constexpr unsigned OFF_CA = 524288;
constexpr unsigned OFF_CB = 655360;
constexpr unsigned OFF_STAT = 1u << 20;
constexpr unsigned OFF_PART = 3u << 20;
constexpr unsigned OFF_STAMP = 6u << 20;
constexpr unsigned OFF_XN = 8u << 20;
constexpr unsigned OFF_O = 16u << 20;
constexpr unsigned OFF_KV = 32u << 20;
constexpr unsigned OFF_WI = 52u << 20;
constexpr unsigned WS_BYTES = 58u << 20;
constexpr unsigned POISON = 0xFFFFFFFFu;

constexpr int L_ABW = 16640, L_CSP = 17408, L_CSL = 31872, L_FXL = 32256, L_K = 0, L_V = 32768, L_RING = 65536, L_XCH = 0, L_DN = 32768, L_RDY = 31744, L_QC = 31808, LDS_BYTES = 163840;
constexpr int STAGE = 24576;
#define SLOT(t) ((t) & 3)

template <typename F, int... I> __device__ __forceinline__ void static_for_impl(F&& f, std::integer_sequence<int, I...>) {
  (f(std::integral_constant<int, I>{}), ...);
}
template <int N, typename F> __device__ __forceinline__ void static_for(F&& f) { static_for_impl(f, std::make_integer_sequence<int, N>{}); }
__device__ __forceinline__ unsigned pack2(float a, float b) {
  f32x2 v = {a, b};
  return __builtin_bit_cast(unsigned, __builtin_convertvector(v, bf16x2));
}
__device__ __forceinline__ float asf(unsigned u) { return __builtin_bit_cast(float, u); }
__device__ __forceinline__ float lo16(unsigned u) { return __builtin_bit_cast(float, u << 16); }
__device__ __forceinline__ float hi16(unsigned u) { return __builtin_bit_cast(float, u & 0xffff0000u); }
__device__ __forceinline__ u32x4 pack8(const f32x4 a, const f32x4 b) {
  u32x4 o;
  o[0] = pack2(a[0], a[1]); o[1] = pack2(a[2], a[3]); o[2] = pack2(b[0], b[1]); o[3] = pack2(b[2], b[3]);
  return o;
}
__device__ __forceinline__ f32x4 mfma16(u32x4 a, u32x4 b, f32x4 c) {
  return __builtin_amdgcn_mfma_f32_16x16x32_bf16(__builtin_bit_cast(bf16x8, a), __builtin_bit_cast(bf16x8, b), c, 0, 0, 0);
}
__device__ __forceinline__ f32x4 mfma16k16(s16x4 a, s16x4 b, f32x4 c) { return __builtin_amdgcn_mfma_f32_16x16x16bf16_1k(a, b, c, 0, 0, 0); }
template <int CTRL> __device__ __forceinline__ float dppf(float v) {
  return __builtin_bit_cast(float, __builtin_amdgcn_update_dpp(0, __builtin_bit_cast(int, v), CTRL, 0xf, 0xf, false));
}
template <int CTRL> __device__ __forceinline__ unsigned dppu(unsigned v) {
  return (unsigned)__builtin_amdgcn_update_dpp(0, (int)v, CTRL, 0xf, 0xf, false);
}
__device__ __forceinline__ float add_x16(float v) {
  auto r = __builtin_amdgcn_permlane16_swap(__builtin_bit_cast(unsigned, v), __builtin_bit_cast(unsigned, v), false, false);
  return __builtin_bit_cast(float, (unsigned)r[0]) + __builtin_bit_cast(float, (unsigned)r[1]);
}
__device__ __forceinline__ float add_x32(float v) {
  auto r = __builtin_amdgcn_permlane32_swap(__builtin_bit_cast(unsigned, v), __builtin_bit_cast(unsigned, v), false, false);
  return __builtin_bit_cast(float, (unsigned)r[0]) + __builtin_bit_cast(float, (unsigned)r[1]);
}
__device__ __forceinline__ float max_x16(float v) {
  auto r = __builtin_amdgcn_permlane16_swap(__builtin_bit_cast(unsigned, v), __builtin_bit_cast(unsigned, v), false, false);
  return fmaxf(__builtin_bit_cast(float, (unsigned)r[0]), __builtin_bit_cast(float, (unsigned)r[1]));
}
__device__ __forceinline__ float max_x32(float v) {
  auto r = __builtin_amdgcn_permlane32_swap(__builtin_bit_cast(unsigned, v), __builtin_bit_cast(unsigned, v), false, false);
  return fmaxf(__builtin_bit_cast(float, (unsigned)r[0]), __builtin_bit_cast(float, (unsigned)r[1]));
}
__device__ __forceinline__ unsigned umax_x16(unsigned v) {
  auto r = __builtin_amdgcn_permlane16_swap(v, v, false, false);
  return max((unsigned)r[0], (unsigned)r[1]);
}
__device__ __forceinline__ unsigned umax_x32(unsigned v) {
  auto r = __builtin_amdgcn_permlane32_swap(v, v, false, false);
  return max((unsigned)r[0], (unsigned)r[1]);
}
__device__ __forceinline__ float sum_hi8(float v) {
  v += dppf<0x128>(v);
  v = add_x16(v);
  return add_x32(v);
}
__device__ __forceinline__ float sum64(float v) {
  v += dppf<0xB1>(v);
  v += dppf<0x4E>(v);
  v += dppf<0x124>(v);
  v += dppf<0x128>(v);
  v = add_x16(v);
  return add_x32(v);
}

#define BAR() asm volatile("s_waitcnt lgkmcnt(0)\n\ts_barrier" ::: "memory")
#define DRAIN() asm volatile("s_waitcnt vmcnt(0)" ::: "memory")
#define SB() __builtin_amdgcn_sched_barrier(0)
template <int N> __device__ __forceinline__ void wait_vm() { asm volatile("s_waitcnt vmcnt(%0)" ::"n"(N) : "memory"); }

__device__ __forceinline__ void dma16(rsrc_t rs, unsigned voff, unsigned soff, unsigned lds) {
  asm volatile("s_mov_b32 m0, %0\n\ts_nop 0\n\tbuffer_load_dwordx4 %1, %2, %3 offen lds" ::"s"(lds), "v"(voff), "s"(rs), "s"(soff) : "memory");
}
template <int OFF> __device__ __forceinline__ void dma16s(rsrc_t rs, unsigned voff, unsigned soff, unsigned lds) {
  asm volatile("s_mov_b32 m0, %0\n\ts_nop 0\n\tbuffer_load_dwordx4 %1, %2, %3 offen offset:%4 sc1 lds" ::"s"(lds), "v"(voff), "s"(rs), "s"(soff), "n"(OFF) : "memory");
}
__device__ __forceinline__ float rbf1(float x) { return __builtin_bit_cast(float, pack2(0.f, x)); }
__device__ __forceinline__ void vswap(unsigned& a, unsigned& b) { asm volatile("v_swap_b32 %0, %1" : "+v"(a), "+v"(b)); }
__device__ __forceinline__ void vswap(float& a, float& b) { asm volatile("v_swap_b32 %0, %1" : "+v"(a), "+v"(b)); }
template <typename V, int N> __device__ __forceinline__ void vswapn(V& a, V& b) {
#pragma unroll
  for (int i = 0; i < N; ++i) { auto x = a[i], y = b[i]; vswap(x, y); a[i] = x; b[i] = y; }
}
__device__ __forceinline__ u32x4 ld16(rsrc_t rs, unsigned voff, unsigned soff) { return __builtin_amdgcn_raw_buffer_load_b128(rs, voff, soff, SC1); }
template <int OFF> __device__ __forceinline__ void ld16h(u32x4& d, rsrc_t rs, unsigned voff, unsigned soff) {
  asm volatile("buffer_load_dwordx4 %0, %1, %2, %3 offen offset:%4 sc1" : "=v"(d) : "v"(voff), "s"(rs), "s"(soff), "n"(OFF) : "memory");
}
template <int I, int OFF> __device__ __forceinline__ void ld16w(u32x4& d, rsrc_t rs, unsigned voff, unsigned soff) {
  if constexpr (I == 0) asm volatile("buffer_load_dwordx4 %0, %1, %2, %3 offen offset:%4 sc1" : "={v[128:131]}"(d) : "v"(voff), "s"(rs), "s"(soff), "n"(OFF) : "memory");
  else if constexpr (I == 1) asm volatile("buffer_load_dwordx4 %0, %1, %2, %3 offen offset:%4 sc1" : "={v[132:135]}"(d) : "v"(voff), "s"(rs), "s"(soff), "n"(OFF) : "memory");
  else if constexpr (I == 2) asm volatile("buffer_load_dwordx4 %0, %1, %2, %3 offen offset:%4 sc1" : "={v[136:139]}"(d) : "v"(voff), "s"(rs), "s"(soff), "n"(OFF) : "memory");
  else if constexpr (I == 3) asm volatile("buffer_load_dwordx4 %0, %1, %2, %3 offen offset:%4 sc1" : "={v[140:143]}"(d) : "v"(voff), "s"(rs), "s"(soff), "n"(OFF) : "memory");
  else if constexpr (I == 4) asm volatile("buffer_load_dwordx4 %0, %1, %2, %3 offen offset:%4 sc1" : "={v[144:147]}"(d) : "v"(voff), "s"(rs), "s"(soff), "n"(OFF) : "memory");
  else if constexpr (I == 5) asm volatile("buffer_load_dwordx4 %0, %1, %2, %3 offen offset:%4 sc1" : "={v[148:151]}"(d) : "v"(voff), "s"(rs), "s"(soff), "n"(OFF) : "memory");
  else if constexpr (I == 6) asm volatile("buffer_load_dwordx4 %0, %1, %2, %3 offen offset:%4 sc1" : "={v[152:155]}"(d) : "v"(voff), "s"(rs), "s"(soff), "n"(OFF) : "memory");
  else if constexpr (I == 7) asm volatile("buffer_load_dwordx4 %0, %1, %2, %3 offen offset:%4 sc1" : "={v[156:159]}"(d) : "v"(voff), "s"(rs), "s"(soff), "n"(OFF) : "memory");
  else if constexpr (I == 8) asm volatile("buffer_load_dwordx4 %0, %1, %2, %3 offen offset:%4 sc1" : "={v[160:163]}"(d) : "v"(voff), "s"(rs), "s"(soff), "n"(OFF) : "memory");
  else if constexpr (I == 9) asm volatile("buffer_load_dwordx4 %0, %1, %2, %3 offen offset:%4 sc1" : "={v[164:167]}"(d) : "v"(voff), "s"(rs), "s"(soff), "n"(OFF) : "memory");
  else if constexpr (I == 10) asm volatile("buffer_load_dwordx4 %0, %1, %2, %3 offen offset:%4 sc1" : "={v[168:171]}"(d) : "v"(voff), "s"(rs), "s"(soff), "n"(OFF) : "memory");
  else if constexpr (I == 11) asm volatile("buffer_load_dwordx4 %0, %1, %2, %3 offen offset:%4 sc1" : "={v[172:175]}"(d) : "v"(voff), "s"(rs), "s"(soff), "n"(OFF) : "memory");
}
template <int K> __device__ __forceinline__ void pin_w(u32x4& a, u32x4& b) {
  if constexpr (K == 0) asm volatile("" : "+{v[128:131]}"(a), "+{v[132:135]}"(b)::"memory");
  else if constexpr (K == 1) asm volatile("" : "+{v[136:139]}"(a), "+{v[140:143]}"(b)::"memory");
  else if constexpr (K == 2) asm volatile("" : "+{v[144:147]}"(a), "+{v[148:151]}"(b)::"memory");
  else if constexpr (K == 3) asm volatile("" : "+{v[152:155]}"(a), "+{v[156:159]}"(b)::"memory");
  else if constexpr (K == 4) asm volatile("" : "+{v[160:163]}"(a), "+{v[164:167]}"(b)::"memory");
  else if constexpr (K == 5) asm volatile("" : "+{v[168:171]}"(a), "+{v[172:175]}"(b)::"memory");
}
__device__ __forceinline__ void ld8h(u32x2& d, rsrc_t rs, unsigned voff, unsigned soff) {
  asm volatile("buffer_load_dwordx2 %0, %1, %2, %3 offen sc1" : "=v"(d) : "v"(voff), "s"(rs), "s"(soff) : "memory");
}
template <int OFF> __device__ __forceinline__ void ld4h(unsigned& d, rsrc_t rs, unsigned voff, unsigned soff) {
  asm volatile("buffer_load_dword %0, %1, %2, %3 offen offset:%4 sc1" : "=v"(d) : "v"(voff), "s"(rs), "s"(soff), "n"(OFF) : "memory");
}
__device__ __forceinline__ u32x2 ld8(rsrc_t rs, unsigned voff, unsigned soff) { return __builtin_amdgcn_raw_buffer_load_b64(rs, voff, soff, SC1); }
__device__ __forceinline__ void st16(u32x4 v, rsrc_t rs, unsigned voff, unsigned soff) { __builtin_amdgcn_raw_buffer_store_b128(v, rs, voff + soff, 0, SC1); }
__device__ __forceinline__ void st8(u32x2 v, rsrc_t rs, unsigned voff, unsigned soff) { __builtin_amdgcn_raw_buffer_store_b64(v, rs, voff, soff, SC1); }

__device__ __forceinline__ unsigned ld4(rsrc_t rs, unsigned voff, unsigned soff) { return __builtin_amdgcn_raw_buffer_load_b32(rs, voff, soff, SC1); }
__device__ __forceinline__ void st4(unsigned v, rsrc_t rs, unsigned voff, unsigned soff) { __builtin_amdgcn_raw_buffer_store_b32(v, rs, voff, soff, SC1); }

constexpr unsigned kSpinCap = 1u << 16;
__device__ __forceinline__ bool spin_fail(unsigned& spins, rsrc_t rs, u32x2 T) {
  asm volatile("" ::: "memory");
  ++spins;
  if (spins > kSpinCap) { st8(T, rs, 0, F_TMO); return true; }
  if ((spins & 255u) == 0u) {
    const u32x2 v = ld8(rs, 0, F_TMO);
    if (v[0] == T[0] && v[1] == T[1]) return true;
  }
  __builtin_amdgcn_s_sleep(3);
  return false;
}
__device__ __forceinline__ void poll_flags2(rsrc_t rs, unsigned voff, u32x2 T) {
  for (unsigned spins = 0;;) {
    const u32x4 v = ld16(rs, voff, 0);
    if (__all(v[0] == T[0] && v[1] == T[1] && v[2] == T[0] && v[3] == T[1])) break;
    if (spin_fail(spins, rs, T)) break;
  }
  asm volatile("" ::: "memory");
  __builtin_amdgcn_fence(__ATOMIC_ACQUIRE, "wavefront");
}
__device__ __forceinline__ void tag_wait(rsrc_t rs, u32x2 T, unsigned voff, unsigned soff, u32x4& v) {
  if (__builtin_expect(!__all(v[2] == T[0] && v[3] == T[1]), 0)) {
    unsigned spins = 0;
    do {
      if (spin_fail(spins, rs, T)) break;
      v = ld16(rs, voff, soff);
    } while (!__all(v[2] == T[0] && v[3] == T[1]));
    asm volatile("s_waitcnt vmcnt(0)" : "+v"(v)::"memory");
  }
}
__device__ __forceinline__ void poll_flags(rsrc_t rs, unsigned voff, unsigned soff, u32x2 T) {
  for (unsigned spins = 0;;) {
    const u32x2 v = ld8(rs, voff, soff);
    if (__all(v[0] == T[0] && v[1] == T[1])) break;
    if (spin_fail(spins, rs, T)) break;
  }
  asm volatile("" ::: "memory");
  __builtin_amdgcn_fence(__ATOMIC_ACQUIRE, "wavefront");
}

#define STAMP_ON(i) true
#define STAMP_VAL(i) (((i) == 23 || (i) == 22 || (i) == 21) ? __builtin_amdgcn_s_memtime() : __builtin_amdgcn_s_memrealtime())
#define STAMPAT(i)

#define VSTEP(i)

__device__ __forceinline__ unsigned toff(unsigned R, unsigned ks, unsigned g) {
  return (((R >> 5) * 64 + ks * 2 + ((R >> 4) & 1)) << 10) + ((g * 16 + (R & 15)) << 4);
}
__device__ __forceinline__ void unpack8v(const u32x4 u, f32x2* v) {
#pragma unroll
  for (int j = 0; j < 4; ++j) v[j] = f32x2{lo16(u[j]), hi16(u[j])};
}
__device__ __forceinline__ u32x4 pack8v(const f32x2* v) {
  u32x4 o;
#pragma unroll
  for (int j = 0; j < 4; ++j) o[j] = pack2(v[j][0], v[j][1]);
  return o;
}
__device__ __forceinline__ f32x2 rbf1v(f32x2 v) { return f32x2{rbf1(v[0]), rbf1(v[1])}; }
__device__ __forceinline__ float ln_rd(float var) { return __builtin_amdgcn_rcpf(rbf1(__builtin_amdgcn_sqrtf(rbf1(rbf1(var) + 1e-5f)))); }
template <int N> __device__ __forceinline__ void ln_apply(float mb, float rd, const f32x2* v, f32x2* y) {
  const f32x2 mb2 = {mb, mb}, rd2 = {rd, rd};
#pragma unroll
  for (int j = 0; j < N; ++j) y[j] = rbf1v(rbf1v(v[j] - mb2) * rd2);
}
__device__ __forceinline__ float hadd(f32x2 v) {
  float a = v[0];
  asm volatile("" : "+v"(a));
  return a + v[1];
}
__device__ __forceinline__ float add_g(float v) { return add_x32(add_x16(v)); }
__device__ __forceinline__ void add_g3(float& x, float& y, float& z) {
  auto a = __builtin_amdgcn_permlane16_swap(__builtin_bit_cast(unsigned, x), __builtin_bit_cast(unsigned, y), false, false);
  auto b = __builtin_amdgcn_permlane16_swap(__builtin_bit_cast(unsigned, z), __builtin_bit_cast(unsigned, z), false, false);
  const float r = asf(a[0]) + asf(a[1]), t = asf(b[0]) + asf(b[1]);
  auto c = __builtin_amdgcn_permlane32_swap(__builtin_bit_cast(unsigned, r), __builtin_bit_cast(unsigned, r), false, false);
  auto d = __builtin_amdgcn_permlane32_swap(__builtin_bit_cast(unsigned, t), __builtin_bit_cast(unsigned, t), false, false);
  const float u = asf(c[0]) + asf(c[1]);
  z = asf(d[0]) + asf(d[1]);
  auto e = __builtin_amdgcn_permlane16_swap(__builtin_bit_cast(unsigned, u), __builtin_bit_cast(unsigned, u), false, false);
  x = asf(e[0]); y = asf(e[1]);
}
__device__ __forceinline__ u32x4 trade16(u32x4 v) {
  auto a = __builtin_amdgcn_permlane16_swap(v[0], v[2], false, false);
  auto b = __builtin_amdgcn_permlane16_swap(v[1], v[3], false, false);
  return u32x4{(unsigned)a[0], (unsigned)b[0], (unsigned)a[1], (unsigned)b[1]};
}
__device__ __forceinline__ unsigned umax4(u32x4 v) { return max(max(v[0], v[1]), max(v[2], v[3])); }
__device__ __forceinline__ u32x2 unpoison2(u32x2 v) { return u32x2{min(v[0], POISON - 1), min(v[1], POISON - 1)}; }
__device__ __forceinline__ u32x4 unpoison4(u32x4 v) {
  return u32x4{min(v[0], POISON - 1), min(v[1], POISON - 1), min(v[2], POISON - 1), min(v[3], POISON - 1)};
}

typedef union { struct { s16x4 a, b; } hf; u32x4 v; } frag_t;
__device__ __forceinline__ s16x4 trread(const char* smem, unsigned off) {
  return __builtin_amdgcn_ds_read_tr16_b64_v4i16((LDS_AS s16x4*)(smem + off));
}
__device__ __forceinline__ s16x4 trabs(unsigned addr) { return __builtin_amdgcn_ds_read_tr16_b64_v4i16((LDS_AS s16x4*)(size_t)addr); }

constexpr int WA = 6;
constexpr int WB = 8, WR = 4;
#define CH 1, 8, 10, 11, 16, 18, 24, 26, 30, 31
constexpr int CHV[10] = {CH};
constexpr int SPL = CHV[0];
constexpr int FSL = CHV[1], FS0 = CHV[2], FS0D = CHV[3], FS1 = CHV[4], FS1S = CHV[5];
constexpr int FXA = CHV[6], FXB = CHV[7], FXL = CHV[8], FXS = CHV[9];
#define L0S 22, 25, 8, 0, 24, 28, 1, 2
constexpr int L0V[8] = {L0S};
constexpr int CSL = L0V[0], CSS = L0V[1], CSD = L0V[2];
constexpr int abl(int it) { return it ? 25 : L0V[4]; }
constexpr int abs_(int it) { return it ? 27 : L0V[5]; }
constexpr int W2S = L0V[6], WSH = L0V[7];
constexpr int LNL = L0V[3], LNS = LNL + 2;
#define SYNCB 3
#define SPINS 0
#define KVJ 1
#define KVD 2
#define PC2 1
#define PCE 1
#define ROTH 0
#define ROT0 (p + ROTH * h)
#define TCH 40, 6, 0, 26, 0xFF, 0, 1
constexpr int TCV[7] = {TCH};
constexpr int TS0 = TCV[0], TSN = TCV[1], TM0 = TCV[2], TEX = TCV[3], TM1 = TCV[4], TSD = TCV[5], TSG = TCV[6];
constexpr int QL = 26;
constexpr int SDIR = 24, GSL = 14, RSL = 56, KSL = 3;
constexpr int tp0(int it) { return it ? 3 : 3 + WSH; }
constexpr bool tile_piece(int it, int s) { return s >= tp0(it) && ((s - tp0(it)) >> 2) + 3 < 8 && ((s - tp0(it)) & 3) < 3; }
constexpr int a_req(int s, int n) {
  if (s < 1 || !(n & 1) || (s == 1 && n == 1)) return -1;
  if (s <= 5) return 8 + 3 * s + (n >> 1) - 4;
  return n < 5 && s + 5 < 32 ? 2 * (s + 5) + (n >> 1) : -1;
}
constexpr int vm_n(int it, int s, int n) {
  int c = it ? (a_req(s, n) >= 0) + (s == 0 && n == 0) : (s + 5 < 32 && (n == 2 || n == 3));
  if (n == 0) c += tile_piece(it, s);
  if (n == 4) c += (s == abl(it)) + (it ? (s == FSL ? 5 : s == FXA ? 4 : 0) + (s == FSL - 2) : 0);
  if (!it) c += (s >= W2S && s < W2S + 3 && n == 4) + (s == LNL && n == 5 ? 3 : 0) + (s == LNS + 1 && n == 5) + (s == TEX && n == 5);
  else c += (s == 2 && n == 2) + (s == SPL && n == 4 ? 4 : 0) + (s >= 27 && s < 30 && (n == 0 || n == 3));
  return c;
}
constexpr int vm_at(int it, int s, int n) {
  int c = it ? 8 : 16;
  for (int u = 0; u < 6 * s + n; ++u) c += vm_n(it, u / 6, u % 6);
  return c;
}
constexpr int vm_a(int it, int k) {
  if (k < 4) return 2 * k + 2;
  for (int u = 0; u < 192; ++u)
    if (a_req(u / 6, u % 6) == 2 * k + 1) return vm_at(it, (u + 1) / 6, (u + 1) % 6);
  return 0;
}
constexpr int vm_tile(int it, int t) { return t >= 3 ? vm_at(it, 4 * t - 10 + tp0(it), 1) : t == 1 ? 12 : vm_at(0, W2S + 2, 5); }
constexpr int vm_unit(int u) { return u < 3 ? 5 + 2 * u : u < 5 ? 8 + 2 * u : vm_at(0, u - 5, 4); }
constexpr int unit_slot(int u) { return u % 3 == 0 ? 0 : 16384 + (u % 3) * 16384; }

constexpr int L_XC = LDS_BYTES - 64, L_SKEY = 32704, L_SRED = 4096, L_SFIN = 20480;
__device__ __forceinline__ unsigned key_load(rsrc_t rs, int l, int L, int rep) { return ld4(rs, (l & 15) * SKEY, KEY_AT(L, rep)); }
__device__ __forceinline__ unsigned umax16(unsigned k) {
  k = max(k, dppu<0xB1>(k));
  k = max(k, dppu<0x4E>(k));
  k = max(k, dppu<0x141>(k));
  return max(k, dppu<0x140>(k));
}
__device__ __forceinline__ unsigned key_row(unsigned key) { return (0xffffu - (key & 0xffffu)) & 2047u; }
__device__ __forceinline__ unsigned key_wait(rsrc_t rs, u32x2 T, int l, int L, int rep, unsigned kv) {
  if (!__all(kv != POISON)) {
    unsigned spins = 0;
    do {
      if (spin_fail(spins, rs, T)) break;
      if (L == 0) __builtin_amdgcn_s_sleep(KSL);
      kv = key_load(rs, l, L, rep);
    } while (!__all(kv != POISON));
  }
  return __builtin_amdgcn_readfirstlane(umax16(kv));
}
__device__ __forceinline__ unsigned ffn_xoff(unsigned key, int L, int w) { return OFF_CA + L * 32768 + (key_row(key) >> 7) * 2048 + w * 256; }
__device__ __forceinline__ void ffn_xwait(rsrc_t rs, u32x2 T, unsigned xo, int l, u32x4 (&xv)[4]) {
#define XV_OK() __all(max(max(umax4(xv[0]), umax4(xv[1])), max(umax4(xv[2]), umax4(xv[3]))) != POISON)
  if (!XV_OK()) {
    unsigned spins = 0;
    do {
      if (spin_fail(spins, rs, T)) break;
#pragma unroll
      for (int i = 0; i < 4; ++i) xv[i] = ld16(rs, (l >> 4) * 16 + i * 64, xo);
    } while (!XV_OK());
  }
#undef XV_OK
}
__device__ __forceinline__ void ffn_gu(const u32x4 (&gv)[4], const u32x4 (&uv)[4], const u32x4 (&xv)[4], f32x4& ga, f32x4& ua) {
  ga = mfma16(gv[0], xv[0], f32x4{0.f, 0.f, 0.f, 0.f}); ua = mfma16(uv[0], xv[0], f32x4{0.f, 0.f, 0.f, 0.f});
#pragma unroll
  for (int i = 1; i < 4; ++i) { ga = mfma16(gv[i], xv[i], ga); ua = mfma16(uv[i], xv[i], ua); }
}
__device__ __forceinline__ void ffn_gu_check(rsrc_t rs, u32x2 T, unsigned xo, int l, const u32x4 (&gv)[4], const u32x4 (&uv)[4], u32x4 (&xv)[4], f32x4& ga, f32x4& ua) {
  if (__any(ga[0] != ga[0])) {
    ffn_xwait(rs, T, xo, l, xv);
    ffn_gu(gv, uv, xv, ga, ua);
  }
}
__device__ __forceinline__ void ffn_gu_store(char* smem, int l, int w, f32x4 ga, f32x4 ua) {
  LDS_AS f32x4* sred = (LDS_AS f32x4*)(smem + L_SRED);
  if ((l & 15) == 0) { sred[w * 8 + (l >> 4)] = ga; sred[w * 8 + 4 + (l >> 4)] = ua; }
}
__device__ __forceinline__ void ffn_d_read(char* smem, int l, int w, f32x4& t, s16x4 (&dfr)[8]) {
  t = ((const LDS_AS f32x4*)(smem + L_SRED))[(l & 7) * 8 + ((l >> 3) & 1) * 4 + (l >> 4)];
#pragma unroll
  for (int i = 0; i < 8; ++i) dfr[i] = trread(smem, L_DN + w * 4096 + (l >> 4) * 1024 + ((l & 15) >> 2) * 256 + i * 32 + (l & 3) * 8);
}
__device__ __forceinline__ float ffn_d_sum(float t) { t += dppf<0xB1>(t); t += dppf<0x4E>(t); return t + dppf<0x141>(t); }
__device__ __forceinline__ float ffn_d_h(float t) {
  const float gg = rbf1(t), uu = rbf1(dppf<0x128>(t));
  return rbf1(gg * __builtin_amdgcn_rcpf(1.0f + __expf(-gg))) * uu;
}
__device__ __forceinline__ f32x4 ffn_d_mma(int l, int t, s16x4 d, u32x2 hh, f32x4 y) {
  const bool me = (l & 15) == t;
  return mfma16k16(d, __builtin_bit_cast(s16x4, u32x2{me ? hh[0] : 0u, me ? hh[1] : 0u}), y);
}
__device__ __forceinline__ void ffn_d_store(rsrc_t rs, int l, int w, int L, int slice, f32x4 y) {
  const unsigned c = l & 15;
  if (c < 8) st16(unpoison4(__builtin_bit_cast(u32x4, y)), rs, (4 * w + (c >> 1)) * 32768 + slice * 128 + (c & 1) * 64 + (l >> 4) * 16, OFF_PART + L * (1u << 20));
}
__device__ __forceinline__ void red_load(rsrc_t rs, int tid, int L, unsigned G, unsigned key, u32x4 (&pv)[4], u32x2& cxn, u32x2& cx1) {
  const unsigned co = L * 32768 + (key_row(key) >> 7) * 2048 + G * 64, jj = (tid & 3) * 16 + (tid & 4) * 2;
#pragma unroll
  for (int k = 0; k < 4; ++k) pv[k] = ld16(rs, min(tid + 512 * k, 1791) * 16, OFF_PART + L * (1u << 20) + G * 32768);
  cxn = ld8(rs, jj, OFF_CA + co);
  cx1 = ld8(rs, jj, OFF_CB + co);
}
__device__ __forceinline__ void red_sum(rsrc_t rs, char* smem, u32x2 T, int tid, int w, int L, unsigned G, unsigned key,
                                        u32x4 (&pv)[4], u32x2& cxn, u32x2& cx1) {
#define PV_OK() __all(max(max(max(umax4(pv[0]), umax4(pv[1])), max(umax4(pv[2]), umax4(pv[3]))), max(max(cxn[0], cxn[1]), max(cx1[0], cx1[1]))) != POISON)
  if (!PV_OK()) {
    unsigned spins = 0;
    do {
      if (spin_fail(spins, rs, T)) break;
      red_load(rs, tid, L, G, key, pv, cxn, cx1);
    } while (!PV_OK());
  }
#undef PV_OK
  f32x4 s = (__builtin_bit_cast(f32x4, pv[0]) + __builtin_bit_cast(f32x4, pv[1])) + __builtin_bit_cast(f32x4, pv[2]);
  if (w < 4) s += __builtin_bit_cast(f32x4, pv[3]);
  ((LDS_AS f32x4*)(smem + L_SFIN))[tid] = s;
}
__device__ __forceinline__ u32x2 red_final(char* smem, int l, unsigned key, u32x2 cxn, u32x2 cx1) {
  const LDS_AS f32x4* sf = (const LDS_AS f32x4*)(smem + L_SFIN) + l;
  f32x4 s = ((sf[0] + sf[64]) + (sf[128] + sf[192])) + ((sf[256] + sf[320]) + (sf[384] + sf[448]));
#pragma unroll
  for (int j = 0; j < 4; ++j) s[j] = sum_hi8(s[j]);
  const unsigned k16 = key >> 16;
  const float rmax = __builtin_bit_cast(float, ((k16 & 0x8000u) ? (k16 & 0x7fffu) : (~k16 & 0xffffu)) << 16);
  const float sig = rbf1(__builtin_amdgcn_rcpf(1.0f + __expf(-rmax)));
  const float xn4[4] = {lo16(cxn[0]), hi16(cxn[0]), lo16(cxn[1]), hi16(cxn[1])};
  const float x14[4] = {lo16(cx1[0]), hi16(cx1[0]), lo16(cx1[1]), hi16(cx1[1])};
  float o[4];
#pragma unroll
  for (int j = 0; j < 4; ++j) o[j] = x14[j] + rbf1(xn4[j] + rbf1(rbf1(s[j]) * sig));
  return u32x2{pack2(o[0], o[1]), pack2(o[2], o[3])};
}

struct RowFin { float mean, var, mb, rd; unsigned key; };
__device__ __forceinline__ void fin_wait(rsrc_t rs, u32x2 T, unsigned voff, unsigned so, u32x4 (&sp)[4]) {
#define ST_OK() __all(max(max(umax4(sp[0]), umax4(sp[1])), max(umax4(sp[2]), umax4(sp[3]))) != POISON)
  if (!ST_OK()) {
    unsigned spins = 0;
    do {
      if (spin_fail(spins, rs, T)) break;
#pragma unroll
      for (int i = 0; i < 4; ++i) sp[i] = ld16(rs, voff + i * 2048, so);
    } while (!ST_OK());
  }
#undef ST_OK
}
__device__ __forceinline__ RowFin fin_key(rsrc_t rs, u32x2 T, unsigned voff, unsigned so, char* skey, int l, int w, unsigned R, float wsum, u32x4 (&sp)[4]) {
  RowFin r;
#define ST_SUM() ((__builtin_bit_cast(f32x4, sp[0]) + __builtin_bit_cast(f32x4, sp[1])) + (__builtin_bit_cast(f32x4, sp[2]) + __builtin_bit_cast(f32x4, sp[3])))
  f32x4 sf = ST_SUM();
  const float z = (sf[0] + sf[1]) + sf[2];
  if (__any(z != z)) {
    fin_wait(rs, T, voff, so, sp);
    sf = ST_SUM();
  }
#undef ST_SUM
  float s0 = sf[0], s1 = sf[1], s2 = sf[2];
  add_g3(s0, s1, s2);
  r.mean = s0 * (1.0f / 1024.0f);
  r.var = fmaxf(__builtin_fmaf(-r.mean, r.mean, s1 * (1.0f / 1024.0f)), 0.f);
  r.mb = rbf1(r.mean); r.rd = ln_rd(r.var);
  const unsigned rb16 = pack2((s2 - r.mb * wsum) * r.rd, 0.f) & 0xffffu;
  r.key = min((((rb16 & 0x8000u) ? (~rb16 & 0xffffu) : (rb16 | 0x8000u)) << 16) | (0xffffu - R), POISON - 1);
  const unsigned wk = umax16(r.key);
  if (l == 0) ((LDS_AS unsigned*)skey)[w] = wk;
  return r;
}
__device__ __forceinline__ void fin_cand(rsrc_t rs, char* skey, int L, int g, int rb, int ct, const RowFin& r, const f32x2 (&v)[8]) {
  const u32x4 k0 = *(const LDS_AS u32x4*)skey, k1 = *(const LDS_AS u32x4*)(skey + 16);
  const unsigned best = max(umax4(k0), umax4(k1));
  if (r.key == best) {
    const unsigned co = L * 32768 + rb * 2048 + ct * 128 + g * 16;
    f32x2 t[8], rd2 = {r.rd, r.rd}, of2 = {-r.mb * r.rd, -r.mb * r.rd};
    asm volatile("" : "+v"(rd2), "+v"(of2));
#pragma unroll
    for (int j = 0; j < 8; ++j) t[j] = __builtin_elementwise_fma(v[j], rd2, of2);
    st16(unpoison4(pack8v(t)), rs, co, OFF_CA); st16(unpoison4(pack8v(t + 4)), rs, co + 64, OFF_CA);
    if (g == 0 && ct < (int)RKEY) st4(best, rs, KEY_AT(L, ct) + rb * SKEY, 0);
    st16(unpoison4(pack8v(v)), rs, co, OFF_CB); st16(unpoison4(pack8v(v + 4)), rs, co + 64, OFF_CB);
  }
}
__device__ __forceinline__ void fin_x2(const RowFin& r, f32x2 (&v)[8]) {
  f32x2 sc2 = {1.0f + r.rd, 1.0f + r.rd}, of2 = {-r.mb * r.rd, -r.mb * r.rd};
  asm volatile("" : "+v"(sc2), "+v"(of2));
#pragma unroll
  for (int j = 0; j < 8; ++j) v[j] = __builtin_elementwise_fma(v[j], sc2, of2);
}

extern "C" __device__ unsigned long long dispatch_id() __asm("llvm.amdgcn.dispatch.id");

extern "C" __global__ __launch_bounds__(512, 2) void mega_kernel(const bf16* x, char* ws, const bf16* wqkv, const bf16* wo, const bf16* wr,
                                                                 const bf16* wg, const bf16* wu, const bf16* wd, bf16* out) {
  extern __shared__ __attribute__((aligned(16))) char smem[];
  const int w0_ = __builtin_amdgcn_readfirstlane(threadIdx.x >> 6);
  const unsigned lbase = (unsigned)(size_t)(LDS_AS char*)smem;
  {
    unsigned long long pc;
    unsigned len;
    asm volatile("s_getpc_b64 %0\n\ts_mov_b32 %1, .Ltext_end-mega_kernel\n\t.subsection 1\n.Ltext_end:\n\t.subsection 0" : "=s"(pc), "=s"(len));
    const unsigned o = ((blockIdx.x >> 3) * 64 + (threadIdx.x & 63)) * 128;
    if (w0_ == 7 && o < len)
      asm volatile("s_mov_b32 m0, %0\n\ts_nop 0\n\tglobal_load_lds_dword %1, %2" ::"s"(lbase + L_FXL), "v"(o), "s"(pc & ~127ull) : "memory");
  }
  asm volatile("" : "+s"(x), "+s"(ws), "+s"(wqkv));
#define IDS()                                                                                         \
  int tid = threadIdx.x, w = w0_, bid = blockIdx.x;                                                   \
  asm volatile("" : "+v"(tid));                                                                       \
  asm volatile("" : "+s"(w));                                                                         \
  asm volatile("" : "+s"(bid));                                                                       \
  const int l = tid & 63, xcd = bid & 7, bi = bid >> 3;                                               \
  const int p = (xcd & 1) * 4 + (bi & 3), h = (xcd >> 1) * 8 + (bi >> 2);         \
  const int rb = 2 * xcd + (bi & 1), ct = bi >> 1;                               \
  const int slice = xcd * 28 + bi;                                                   \
  const bool ffn_on = bi < 28;                                                                        \
  const int g = l >> 4;                                                                               \
  const int rt = w < 4 ? w : 11 - w;          \
  KARGS();                                                                                            \
  (void)p; (void)h; (void)rb; (void)ct; (void)slice; (void)ffn_on; (void)g; (void)rt;
  const rsrc_t rs = __builtin_amdgcn_make_buffer_rsrc((void*)ws, 0, WS_BYTES, 0x00020000);
  typedef const void* const __attribute__((address_space(4)))* karg_t;
#define KARGS() karg_t ka = (karg_t)__builtin_amdgcn_kernarg_segment_ptr(); asm volatile("" : "+s"(ka))
#define ARGP(name, i) const GAS bf16* name = (const GAS bf16*)ka[i]; asm volatile("" : "+s"(name))
#define RSRC(ptr, bytes) __builtin_amdgcn_make_buffer_rsrc((void*)(ptr), 0, bytes, 0x00020000)
#define rwq RSRC(pwq, 1024 * 3072 * 2)
#define rwo RSRC(pwo, 1024 * 1024 * 2)
#define rwg RSRC(pwg, 1024 * F_ * 2)
#define rwu RSRC(pwu, 1024 * F_ * 2)
#define rwd RSRC(pwd, 1024 * F_ * 2)
#define rout RSRC(pout, 2048 * 1024 * 2)

#define WQ_VOFF() ((l >> 2) * 6144 + (((l & 3) ^ ((((l >> 4) ^ (l >> 5)) & 1) << 1)) << 4))
#define WQ_PIECE(t, j, dst) dma16(rwq, wqv, ((((t) + rot) & 7) * 128 + ((j) & 7) * 16) * 6144 + ((j) >> 3) * 2048 + h * 64, lbase + (dst) + (j) * 1024)
#define WQ_BOFF(dt) ((8 * g + ((l & 15) >> 2)) * 64 + (l & 3) * 8 + (((dt) ^ (g & 1)) << 5))
#define WQ_POFF() ((4 * g + ((l & 15) >> 2)) * 64 + (l & 3) * 8 + (((g ^ (g >> 1)) & 1) << 5))
#define X_UNIT1(u, j) dma16(rx, xv_, (unsigned)(p * 256 + rt * 32 + ((u) & 1) * 16 + (j) * 8) * 2048 + ((((u) >> 2) + rot) & 7) * 256 + (((u) >> 1) & 1) * 128, lbase + rt * 2048 + unit_slot(u) + (j) * 1024)
#define X_VOFF() ((l >> 3) * 2048 + (((l & 7) ^ ((l >> 3) & 6)) << 4))
#define X_ROFF(hs) (lbase + rt * 2048 + (l & 15) * 128 + (((4 * (hs) + g) ^ (l & 6)) << 4))
#define TOUCH_TO(ptr, dst) asm volatile("s_mov_b32 m0, %0\n\ts_nop 0\n\tglobal_load_lds_dword %1, off" ::"s"(lbase + (dst)), "v"(ptr) : "memory")
#define TOUCH(ptr) TOUCH_TO(ptr, L_FXL)
#define WO_PIECE_W(j, w_) dma16(rwo, wov, (((w_) >> 2) * 64 + ((w_) & 3) + 4 * (j)) * 16384 + ct * 128, lbase + 32768 + (j) * 8192 + (w_) * 1024)
#define WO_PIECE(j) WO_PIECE_W(j, w)
#define WO_VOFF() ((l >> 3) * 2048 + (((((l & 7) >> 1) ^ ((l >> 4) & 3)) * 2 + (l & 1)) << 4))
#define WD_DMA() _Pragma("unroll") for (int i = 0; i < 4; ++i) WD_DMA1(i)
#define WD_DMA1(i)                                                                                   \
      dma16(rwd, (l >> 4) * 2048 + (l & 15) * 16, (unsigned)(slice * 16 + 4 * (i)) * 2048 + w * 256, lbase + L_DN + w * 4096 + (i) * 1024)
#define FFN_DMA(m) dma16((m) & 1 ? rwu : rwg, fnv, (unsigned)(((4 * w + ((m) >> 1)) * 32) * F_ + slice * 16) * 2, lbase + 65536 + (m) * 8192 + w * 1024)
  u32x4 gv[4], uv[4];
  u32x4 keep[9];
  u32x4 pre[4][2];
#define PRE_LOAD(it_, rot_, s0, s1)                                                                  \
  static_for<(s1) - (s0)>([&](auto c_) __attribute__((always_inline)) {                              \
    constexpr int s = (s0) + decltype(c_)::value;                                                    \
    const unsigned ab_ = OFF_XN + (it_) * (4u << 20) + (((rot_) + (s >> 2)) & 7) * 8192 + ((s >> 1) & 1) * 4096; \
    ld16w<2 * s, (s & 1) * 2048>(pre[s][0], rs, l * 16, ab_ + (p * 8 + rt) * 65536);                         \
    ld16w<2 * s + 1, (s & 1) * 2048 + 1024>(pre[s][1], rs, l * 16, ab_ + (p * 8 + rt) * 65536);                 \
  })
  u32x2 T;
  const GAS bf16* tsrc;
  {
    IDS();
    const int it = 0, rot = ROT0;
    (void)it;
    STAMPAT(0);
    const bf16* pwq = wqkv;
    const rsrc_t rx = RSRC(x, 2048 * 1024 * 2);
    const unsigned wqv = WQ_VOFF(), xv_ = X_VOFF();
    if (tid == 0) *(LDS_AS u32x4*)(smem + L_QC) = u32x4{0u, 0u, 0u, 0u};
#pragma unroll
    for (int i = 0; i < 3; ++i) WQ_PIECE(0, 3 * w + i, L_RING);
    STAMPAT(41);
    X_UNIT1(0, 0); X_UNIT1(0, 1); X_UNIT1(1, 0); X_UNIT1(1, 1);
    STAMPAT(42);
    BAR();
    STAMPAT(43);
    X_UNIT1(2, 0); X_UNIT1(2, 1);
#pragma unroll
    for (int i = 0; i < 3; ++i) WQ_PIECE(1, 3 * w + i, L_RING + STAGE);
    STAMPAT(44);
    if (w == 1) {
      const unsigned prb = 2 * p + (h >> 4), pct = h & 15;
      st16(u32x4{POISON, POISON, POISON, POISON}, rs, OFF_STAT + (prb * 16 + pct) * 2048 + l * 16, 0);
      st16(u32x4{POISON, POISON, POISON, POISON}, rs, OFF_STAT + (prb * 16 + pct) * 2048 + 1024 + l * 16, 0);
      if (l < 8) st16(u32x4{POISON, POISON, POISON, POISON}, rs, OFF_CA + prb * 2048 + pct * 128 + l * 16, 0);
      else if (l == 8 && pct < RKEY) st4(POISON, rs, KEY_AT(0, pct) + prb * SKEY, 0);
    }
    {
      ARGP(pwo, 3); ARGP(pwg, 5); ARGP(pwu, 6);
      const int t = tid < 288 ? tid - 64 : tid - 288, go = (bi * 32 + t / 7) * F_ + xcd * 448 + (t % 7) * 64;
      tsrc = tid < 64 ? pwo + bid * 4096 + tid * 64 : tid < 288 ? pwg + go : pwu + go;
      asm volatile("" : "+v"(tsrc));
    }
    STAMPAT(31);
  }
  unsigned sel0 = 0x7FFFFFFFu;
  f32x2 v[8];
  u32x4 x2p[2];
  float wsum;

  auto layer = [&](auto it_c) __attribute__((always_inline)) {
    constexpr int it = decltype(it_c)::value;
    STAMPAT(1); STAMPAT(22);
    f32x4 acc[2][6];
    f32x2 qs2[2];
    u32x4 k0, k1, v0, v1;
    {
      IDS();
      bool aff = false;
      unsigned aq[2];
      u32x4 av[2];
#define PIN_ACC() asm volatile("" : "+{v[176:179]}"(acc[0][0]), "+{v[180:183]}"(acc[0][1]), "+{v[184:187]}"(acc[0][2]), "+{v[188:191]}"(acc[0][3]), "+{v[192:195]}"(acc[0][4]), "+{v[196:199]}"(acc[0][5]), \
                               "+{v[200:203]}"(acc[1][0]), "+{v[204:207]}"(acc[1][1]), "+{v[208:211]}"(acc[1][2]), "+{v[212:215]}"(acc[1][3]), "+{v[216:219]}"(acc[1][4]), "+{v[220:223]}"(acc[1][5]))
#define PINV4(x) asm volatile("" : "+v"(x[0]), "+v"(x[1]), "+v"(x[2]), "+v"(x[3])::"memory")
      auto qkv_loop = [&](auto it_c) __attribute__((always_inline)) {
        constexpr int IT = decltype(it_c)::value;
        const unsigned abase = OFF_XN + IT * (4u << 20) + (p * 8 + rt) * 65536;
        const int rot = ROT0 + 5 * IT;
        unsigned fkey = 0, kv, bq[2];
        u32x4 fx[4], bv[2], af[WA][2];
        u32x2 cxn, cx1, hh;
        f32x4 sa, sb, cs4[2];
        f32x2 b2[2];
        s16x4 dfr[8];
#define AB_OFF() (OFF_AB + IT * 32768 + (p * 8 + rt) * 512)
#define A_LOAD1(k, a) ld16w<(k) % WA * 2 + (a), ((k) & 1) * 2048 + (a) * 1024>(af[(k) % WA][a], rs, l * 16, abase + ((((k) >> 2) + rot) & 7) * 8192 + (((k) >> 1) & 1) * 4096)
        if constexpr (IT == 1) {
#pragma unroll
          for (int s = 0; s < 4; ++s) { af[s][0] = pre[s][0]; af[s][1] = pre[s][1]; }
        }
        const bf16* pwq = wqkv;
        ARGP(pwo, 3);
        const rsrc_t rx = RSRC(x, 2048 * 1024 * 2);
        unsigned xv_ = X_VOFF(), xr[2] = {X_ROFF(0), X_ROFF(1)};
        if constexpr (IT == 0) asm volatile("" : "+v"(xv_), "+v"(xr[0]), "+v"(xr[1]));
#define X_READ(u) { af[((u) & ~1) % WA][(u) & 1] = *(const LDS_AS u32x4*)(size_t)(xr[0] + unit_slot(u)); af[((u) | 1) % WA][(u) & 1] = *(const LDS_AS u32x4*)(size_t)(xr[1] + unit_slot(u)); }
        const int tstep = TS0 + bid % TSN + TSD * (w >> TSG);
        const unsigned wqv = WQ_VOFF();
#define QKV_DMA1(t, i)                                                                            \
  {                                                                                               \
    if constexpr (IT == 0) WQ_PIECE(t, 3 * w + (i), L_RING + SLOT(t) * STAGE);                    \
    else dma16s<(i) * 1024>(rs, l * 16, OFF_WI + (h * 8 + (((t) + rot) & 7)) * STAGE + w * 3072, lbase + L_RING + SLOT(t) * STAGE + w * 3072); \
  }
        unsigned bo[2][2];
#pragma unroll
        for (int dt = 0; dt < 2 - IT; ++dt) {
          bo[dt][0] = lbase + L_RING + (IT ? l * 16 : WQ_BOFF(dt)); bo[dt][1] = bo[dt][0] + 2 * STAGE;
          asm volatile("" : "+v"(bo[dt][0]), "+v"(bo[dt][1]));
        }
        auto frag0 = [&](int sl, int f) __attribute__((always_inline)) {
          unsigned pb = lbase + L_RING + WQ_POFF();
          asm volatile("" : "+v"(pb));
          const unsigned o = (pb ^ ((f & 1) << 5)) + sl * STAGE + ((f % 6) >> 1) * 8192 + (f / 6) * 2048;
          frag_t t;
          t.hf.a = trabs(o); t.hf.b = trabs(o + 1024);
          return t.v;
        };
        u32x4 df[4];
        auto dot_mma = [&](const u32x4 (&xf)[4]) __attribute__((always_inline)) {
          sa = mfma16(df[0], xf[0], f32x4{0.f, 0.f, 0.f, 0.f}); sb = mfma16(df[1], xf[1], f32x4{0.f, 0.f, 0.f, 0.f});
          sa = mfma16(df[2], xf[2], sa); sb = mfma16(df[3], xf[3], sb);
        };
        auto dot_store = [&](unsigned dst) __attribute__((always_inline)) {
          if ((l & 15) == 0) st16(unpoison4(__builtin_bit_cast(u32x4, sa + sb)), rs, g * 16, dst + ((h * 6 + w) * 8 + p) * 64);
        };
        auto tile_sum = [&](u32x4 pt, int dst) __attribute__((always_inline)) {
          f32x4 c = __builtin_bit_cast(f32x4, pt);
#pragma unroll
          for (int e = 0; e < 4; ++e) { c[e] += dppf<0x124>(c[e]); c[e] += dppf<0x128>(c[e]); c[e] = add_x16(c[e]); }
          if ((l & 28) == 0) *(LDS_AS f32x4*)(smem + dst + (w * 2 + (l >> 5)) * 64 + (l & 3) * 16) = c;
        };
        frag_t bfr[6];
#define QKV_BO(s, n, x) (bo[((n) ^ (x)) & 1][SLOT((s) >> 2) >> 1] + (SLOT((s) >> 2) & 1) * STAGE + ((n) >> 1) * 8192 + ((s) & 3) * 2048 + (x) * 256)
#define QKV_RDA(s, n)                                                                             \
  {                                                                                               \
    if constexpr (IT == 0) bfr[n].hf.a = trabs(QKV_BO(s, n, 0));                                  \
    else bfr[n].v = *(const LDS_AS u32x4*)(size_t)(bo[0][SLOT((s) >> 2) >> 1] + (SLOT((s) >> 2) & 1) * STAGE + (((s) & 3) * 6 + (n)) * 1024); \
  }
#define QKV_RDB(s, n) { if constexpr (IT == 0) bfr[n].hf.b = trabs(QKV_BO(s, n, 1)); }
#define XP_OFF() (OFF_XNEW + ((rot + 6) & 7) * 256)
#define FP_OFF(i) (w == 3 ? OFF_XNEW + (i) * 1024 : OFF_FX + h * 3072 + w * 1024)
#define FX_OK(n) __all(((n) == 4 ? max(max(umax4(fx[0]), umax4(fx[1])), max(umax4(fx[2]), umax4(fx[3]))) : max(umax4(fx[0]), umax4(fx[1]))) != POISON)
#define SINCE(s0) (vm_at(IT, s, n) - vm_at(IT, s0, 5))
#define DEF4(x) asm volatile("" : "=v"(x[0]), "=v"(x[1]), "=v"(x[2]), "=v"(x[3]))
        LDS_AS unsigned* qcnt = (LDS_AS unsigned*)(smem + L_QC);
        unsigned qc;
        unsigned qadr = lbase + L_QC, qone = 1;
        asm volatile("" : "+v"(qadr), "+v"(qone));
#define count_in(i) asm volatile("s_mov_b64 exec, 1\n\tds_add_u32 %0, %1 offset:%2\n\ts_mov_b64 exec, -1" ::"v"(qadr), "v"(qone), "n"(4 * (i)) : "memory")
        auto count_wait = [&](int i, unsigned want) __attribute__((always_inline)) {
          if (__builtin_expect((unsigned)__builtin_amdgcn_readfirstlane(qc) < want, 0)) {
            while ((unsigned)__builtin_amdgcn_readfirstlane(__hip_atomic_load(qcnt + i, __ATOMIC_RELAXED, __HIP_MEMORY_SCOPE_WORKGROUP)) < want) { if (SPINS) __builtin_amdgcn_s_sleep(SPINS); }
          }
          asm volatile("" ::: "memory");
        };
        if constexpr (IT == 0) {
          wait_vm<12 - vm_unit(1)>();
          STAMPAT(46);
          if (!(SYNCB & 1)) count_in(0);
          X_READ(0); X_READ(1);
          asm volatile("s_waitcnt lgkmcnt(0)" : "+v"(af[0][0]), "+v"(af[0][1]), "+v"(af[1][0]), "+v"(af[1][1])::"memory");
          X_UNIT1(3, 0); X_UNIT1(3, 1); X_UNIT1(4, 0); X_UNIT1(4, 1);
          STAMPAT(38);
          qc = 0;
          if (SYNCB & 1) asm volatile("s_barrier" ::: "memory");
          else count_wait(0, 8);
        }
        STAMPAT(39);
#pragma unroll
        for (int n = 0; n < 6; ++n) { QKV_RDA(0, n); if (n < 5) QKV_RDB(0, n); }
        f32x2 lv[8];
        float lmean;
        u32x2 ep;
        u32x4 xl[2];
        f32x4 sc;
#define X1_OFF() (F_X1 + ((p * RX1 + (xcd >> 1) % RX1) * 32 + (rt >> 2) * 16 + (l & 15)) * SX1)
        unsigned long long xok = 0;
        unsigned xg;
        RowFin fin;
        auto x1_need = [&](unsigned long long m) __attribute__((always_inline)) {
          if (__builtin_expect((xok & m) != m, 0)) {
            for (unsigned spins = 0;;) {
              const u32x2 f = ld8(rs, X1_OFF(), 0);
              xok = __builtin_amdgcn_ballot_w64(f[0] == T[0] && f[1] == T[1]);
              if ((xok & m) == m) break;
              if (spin_fail(spins, rs, T)) { xok = ~0ull; break; }
              __builtin_amdgcn_s_sleep(KSL);
            }
            asm volatile("" ::: "memory");
          }
        };
        auto gap = [&](auto sc_, auto nc_) __attribute__((always_inline)) {
          constexpr int s = decltype(sc_)::value, n = decltype(nc_)::value;
          if constexpr ((SYNCB >> IT) & 1) {
            constexpr int tc = (s + 2) / 4;
            if constexpr (tc >= 1 && tc < 8 && s == 4 * tc - 2 && n == 5) {
              if constexpr (!IT || tc >= 3) wait_vm<vm_at(IT, s, n) - vm_tile(IT, tc)>();
              if constexpr (tc == 7 && !IT) asm volatile("s_waitcnt lgkmcnt(4)" ::: "memory");
              asm volatile("s_barrier" ::: "memory");
            }
          } else {
            constexpr int ts = (s + 3) / 4, tc = (s + 2) / 4, t0 = 1;
            if constexpr (ts >= 3 && ts < 8 && s == 4 * ts - 3 && n == 2) { wait_vm<vm_at(IT, s, n) - vm_tile(IT, ts)>(); count_in(0); }
            if constexpr (!IT && tc >= t0 && tc < 3 && s == 4 * tc - 2 && n == 1) { wait_vm<vm_at(IT, s, n) - vm_tile(IT, tc)>(); count_in(0); }
            if constexpr (IT && ts >= t0 && ts < 3 && s == 4 * ts - 3 && n == 2) count_in(0);
            if constexpr (tc >= t0 && tc < 8 && s == 4 * tc - 2) {
              if constexpr (n == 3) qc = __hip_atomic_load(qcnt, __ATOMIC_RELAXED, __HIP_MEMORY_SCOPE_WORKGROUP);
              if constexpr (n == 5) count_wait(0, 8 * (tc + 1 - IT));
            }
          }
          if constexpr (IT == 0 && n == 0 && s + 2 < 32) { wait_vm<vm_at(0, s, 0) - vm_unit(s + 2)>(); X_READ(s + 2); SB(); }
          if constexpr (n == 0 && tile_piece(IT, s)) QKV_DMA1(((s - tp0(IT)) >> 2) + 3, (s - tp0(IT)) & 3);
          if constexpr (IT == 0) {
            if constexpr ((n == 2 || n == 3) && s + 5 < 32) {
              if constexpr (n == 2) asm volatile("s_waitcnt lgkmcnt(%0)" ::"n"(s + 1 < QL ? 4 : 1) : "memory");
              X_UNIT1(s + 5, n - 2);
            }
            if constexpr (s >= W2S && s < W2S + 3 && n == 4) QKV_DMA1(2, s - W2S);
            if constexpr (s == LNL && n == 5) {
              asm volatile("buffer_load_dwordx2 %0, off, %1, 0 sc1" : "=v"(ep) : "s"(rs) : "memory");
              asm volatile("buffer_load_dwordx4 %0, %1, %2, %3 offen" : "=v"(xl[0]) : "v"(l * 16), "s"(rx), "s"((p * 256 + h * 8 + w) * 2048) : "memory");
              asm volatile("buffer_load_dwordx4 %0, %1, %2, %3 offen offset:1024" : "=v"(xl[1]) : "v"(l * 16), "s"(rx), "s"((p * 256 + h * 8 + w) * 2048) : "memory");
            }
            if constexpr (s == LNS && n == 1) {
              wait_vm<vm_at(0, s, n) - vm_at(0, LNL + 1, 0)>();
              asm volatile("" : "+v"(ep), "+v"(xl[0]), "+v"(xl[1])::"memory");
              const unsigned long long e = (((unsigned long long)(unsigned)__builtin_amdgcn_readfirstlane(ep[1]) << 32) | (unsigned)__builtin_amdgcn_readfirstlane(ep[0])) + 1ull + dispatch_id() * 0x9E3779B97F4A7C16ull;
              T = u32x2{(unsigned)e, (unsigned)(e >> 32)};
              unpack8v(xl[0], lv); unpack8v(xl[1], lv + 4);
              asm volatile("" : "+v"(lv[0]), "+v"(lv[1]), "+v"(lv[2]), "+v"(lv[3]), "+v"(lv[4]), "+v"(lv[5]), "+v"(lv[6]), "+v"(lv[7]));
            }
            if constexpr (s == LNS && n == 4) {
              const f32x2 sm = ((lv[0] + lv[1]) + (lv[2] + lv[3])) + ((lv[4] + lv[5]) + (lv[6] + lv[7]));
              lmean = sum64(hadd(sm)) * (1.0f / 1024.0f);
              asm volatile("" : "+v"(lmean));
            }
            if constexpr (s == LNS + 1 && n == 1) {
              f32x2 m2 = {lmean, lmean}, q0 = {0.f, 0.f}, q1 = {0.f, 0.f};
              asm volatile("" : "+v"(m2));
#pragma unroll
              for (int j = 0; j < 8; j += 2) {
                const f32x2 d0 = lv[j] - m2, d1 = lv[j + 1] - m2;
                q0 = __builtin_elementwise_fma(d0, d0, q0); q1 = __builtin_elementwise_fma(d1, d1, q1);
              }
              lv[0] = q0 + q1;
              asm volatile("" : "+v"(lv[0]));
            }
            if constexpr (s == LNS + 1 && n == 5) {
              const float rd = ln_rd(sum64(hadd(lv[0])) * (1.0f / 1024.0f));
              if (l == 0) st16(u32x4{__builtin_bit_cast(unsigned, rd), __builtin_bit_cast(unsigned, -rbf1(lmean)), T[0], T[1]}, rs, OFF_AB + (p * 256 + h * 8 + w) * 16, 0);
              if (w == 1 && l < RX) st8(T, rs, F_XN + (p * RX + l) * 256 + h * 8, 0);
            }
          } else {
            if constexpr ((s == 0 && n == 0) || (s == 2 && n == 2)) asm volatile("buffer_load_dwordx2 %0, %1, %2, 0 offen sc1" : "=v"(cxn) : "v"(X1_OFF()), "s"(rs) : "memory");
            if constexpr ((s == 1 && n == 3) || (s == 4 && n == 0)) {
              wait_vm<vm_at(1, s, n) - (s == 1 ? vm_at(1, 0, 1) : vm_at(1, 2, 3))>();
              asm volatile("" : "+v"(cxn)::"memory");
              xok |= __builtin_amdgcn_ballot_w64(cxn[0] == T[0] && cxn[1] == T[1]);
            }
#define IMG_OFF() (F_X1 + ((((h >> 3) * 2 + ((l >> 2) & 1)) * RX1 + (xcd >> 1) % RX1) * 32 + (l & 1) * 16 + (h & 7) * 2 + ((l >> 1) & 1)) * SX1)
            if constexpr (s == 0 && n == 2) { asm volatile("" : "=v"(cx1)); if (w == 1) asm volatile("buffer_load_dwordx2 %0, %1, %2, 0 offen sc1" : "=v"(cx1) : "v"(IMG_OFF()), "s"(rs) : "memory"); }
            if constexpr (s == 2 && n == 4) {
              if (w == 1) {
                wait_vm<vm_at(1, 2, 4) - vm_at(1, 0, 3)>();
                asm volatile("" : "+v"(cx1)::"memory");
                if (!__all(cx1[0] == T[0] && cx1[1] == T[1])) poll_flags(rs, IMG_OFF(), 0, T);
              }
            }
            if constexpr (a_req(s, n) >= 0 && (a_req(s, n) & 7) == 0) x1_need(3ull << (((rot + a_req(s, n) / 8) & 7) * 2));
            const unsigned R = (rb * 4 + (w & 3)) * 32 + (w >> 2) * 16 + (l & 15), so = OFF_STAT + rb * 32768;
            if constexpr (s == SPL && n == 4) {
#pragma unroll
              for (int i = 0; i < 4; ++i) ld16h<0>(fx[i], rs, (4 * g + i) * 2048 + (R & 127) * 16, so);
            }
            if constexpr (s == SPL + 2 && n == 1) {
              wait_vm<SINCE(SPL)>();
              PINV4(fx);
              fin = fin_key(rs, T, g * 8192 + (R & 127) * 16, so, smem + L_SKEY, l, w, R, wsum, fx);
              count_in(2);
            }
            if constexpr (s == SPL + 3 && n == 0) qc = __hip_atomic_load(qcnt + 2, __ATOMIC_RELAXED, __HIP_MEMORY_SCOPE_WORKGROUP);
            if constexpr (s == SPL + 3 && n == 1) { count_wait(2, 8); fin_cand(rs, smem + L_SKEY, 0, g, rb, ct, fin, v); STAMPAT(51); }
            if constexpr (s == SPL + 3 && n == 3) {
              fin_x2(fin, v);
              x2p[0] = pack8v(v); x2p[1] = pack8v(v + 4);
              asm volatile("" : "+v"(x2p[0]), "+v"(x2p[1]));
            }
            if constexpr (s == SPL + 3 && n == 5) {
              if ((w & 3) * 2 + (w >> 2) == ct >> 1) {
                const float f = 1.0f + fin.rd, rd2 = ln_rd(fin.var * f * f), mb2 = rbf1(__builtin_fmaf(fin.mean - fin.mb, fin.rd, fin.mean));
                if (g == 0 && ((l >> 3) & 1) == (ct & 1))
                  st16(u32x4{__builtin_bit_cast(unsigned, f * rd2), __builtin_bit_cast(unsigned, -__builtin_fmaf(fin.mb, fin.rd, mb2) * __builtin_amdgcn_rcpf(f)), T[0], T[1]}, rs, OFF_AB + 32768 + R * 16, 0);
              }
            }
          }
          if constexpr (IT == 1 && a_req(s, n) >= 0) A_LOAD1(a_req(s, n) / 2, a_req(s, n) & 1);
          if constexpr (!IT && s == CSL && n == 4) { asm volatile("" : "=v"(fx[0])); if (w < 6) ld16h<0>(fx[0], rs, l * 16, OFF_CS + (h * 6 + w) * 1024); }
          if constexpr (!IT && s == CSS && n == 2) {
            if (w < 6) {
              wait_vm<SINCE(CSL)>();
              asm volatile("" : "+v"(fx[0])::"memory");
              tag_wait(rs, T, l * 16, OFF_CS + (h * 6 + w) * 1024, fx[0]);
              const f32x2 c = {sum_hi8(asf(fx[0][0])), sum_hi8(asf(fx[0][1]))};
              if (l < 8) *(LDS_AS f32x2*)(smem + L_CSL + w * 64 + l * 8) = c;
            }
          }
          constexpr int ABL = abl(IT), ABS = abs_(IT);
          if constexpr (s == ABL && n == 4) ld16h<0>(bv[0], rs, (l & 31) * 16, AB_OFF());
          if constexpr (s == ABS && n == 2) {
            wait_vm<SINCE(ABL)>();
            asm volatile("" : "+v"(bv[0])::"memory");
            tag_wait(rs, T, (l & 31) * 16, AB_OFF(), bv[0]);
            const unsigned t0 = bv[0][0], t1 = bv[0][1];
            ((LDS_AS unsigned*)(smem + L_ABW + rt * 2048))[l] = l < 32 ? t0 : t1;
          }
          if constexpr (s == ABS && n == 3) {
#pragma unroll
            for (int a = 0; a < 2; ++a) {
              const LDS_AS unsigned* aw = (const LDS_AS unsigned*)(smem + L_ABW + rt * 2048) + 16 * a;
              aq[a] = aw[l & 15]; bq[a] = aw[32 + (l & 15)];
              av[a] = *(const LDS_AS u32x4*)(aw + 4 * g); bv[a] = *(const LDS_AS u32x4*)(aw + 32 + 4 * g);
            }
          }
          if constexpr (s == ABS && n == 5) {
#pragma unroll
            for (int a = 0; a < 2; ++a) { b2[a] = f32x2{asf(bq[a]), asf(bq[a])}; asm volatile("" : "+v"(b2[a])); }
          }
          if constexpr (s == ABS + 1) {
            constexpr int k = (n + 4) % 6;
            if constexpr (k < 4) cs4[k & 1] = *(const LDS_AS f32x4*)(smem + L_CSL + k * 64 + g * 16);
            else cs4[k & 1][0] = *(const LDS_AS float*)(smem + L_CSL + k * 64 + (l & 15) * 4);
          }
          if constexpr ((s == ABS + 1 && n > 0) || (s == ABS + 2 && n == 0)) {
            constexpr int k = (n + 3) % 6;
            if constexpr (k < 4) {
#pragma unroll
              for (int a = 0; a < 2; ++a) acc[a][k] = __builtin_elementwise_fma(cs4[k & 1], f32x4{b2[a][0], b2[a][1], b2[a][0], b2[a][1]}, acc[a][k]);
            } else {
              f32x2 c2 = {cs4[k & 1][0], cs4[k & 1][0]};
              asm volatile("" : "+v"(c2));
#pragma unroll
              for (int a = 0; a < 2; ++a) acc[a][k] = __builtin_elementwise_fma(f32x4{c2[0], c2[1], c2[0], c2[1]}, __builtin_bit_cast(f32x4, bv[a]), acc[a][k]);
            }
          }
          if constexpr (IT == 0) {
            if constexpr (s == CSD && n < 3) fx[n] = frag0(SLOT(s >> 2), 3 * w + n);
            if constexpr (s == CSD + 1 && n == 1) {
              unsigned one2 = 0x3F803F80u;
              asm volatile("" : "+v"(one2));
              const u32x4 o4 = {one2, one2, one2, one2};
              const f32x4 z4 = {0.f, 0.f, 0.f, 0.f};
              sa = mfma16(fx[0], o4, z4); sb = mfma16(fx[1], o4, z4); sc = mfma16(fx[2], o4, z4);
            }
            if constexpr (s == CSD + 2 && n == 1) {
              const int c = l & 15;
              const f32x4 t0 = sa, t1 = sb, t2 = sc, t = c == 0 ? +t0 : c == 1 ? +t1 : +t2;
              if (c < 3) *(LDS_AS f32x4*)(smem + L_CSP + (w * 3 + c) * 64 + g * 16) = t;
              count_in(2);
            }
            if constexpr (s == CSD + 4 && n == 1) {
              if (w == 2) {
                qc = 0;
                count_wait(2, 8);
                const LDS_AS f32x2* cp = (const LDS_AS f32x2*)(smem + L_CSP) + min(l, 47);
                const f32x2 t = (cp[0] + cp[48]) + (cp[96] + cp[144]);
                if (l < 48) st16(u32x4{__builtin_bit_cast(unsigned, (float)t[0]), __builtin_bit_cast(unsigned, (float)t[1]), T[0], T[1]}, rs, (l >> 3) * 1024 + (l & 7) * 16, OFF_CS + h * 6144 + p * 128);
              }
            }
            if constexpr (s >= 29 && (n & 1)) keep[3 * (s - 29) + (n >> 1)] = frag0(SLOT(s - 24), 3 * w + (n >> 1));
            if constexpr (s >= TS0 && s < TS0 + TSN + TSD * (7 >> TSG) && n == 4) {
              if (tstep == s && ((TM0 >> w) & 1)) TOUCH(tsrc);
            }
            if constexpr (s == TEX && n == 5) {
              const GAS bf16* t2 = ((TM1 >> w) & 1) ? tsrc : (const GAS bf16*)x + p * 262144;
              TOUCH(t2);
            }
          } else {
            if constexpr (s >= 27 && s < 30 && (n == 0 || n == 3)) { const unsigned wov = WO_VOFF(); WO_PIECE(4 + 2 * (s - 27) + n / 3); }
            if constexpr (s == FSL - 2 && n == 4) ld4h<0>(kv, rs, (l & 15) * SKEY, KEY_AT(0, KREP()));
            if constexpr (s == FSL && n == 4) {
              wait_vm<SINCE(FSL - 2)>();
              asm volatile("" : "+v"(kv)::"memory");
              xg = ffn_xoff(__builtin_amdgcn_readfirstlane(umax16(kv == POISON ? 0u : kv)), 0, w);
              ld4h<0>(kv, rs, (l & 15) * SKEY, KEY_AT(0, KREP()));
              ld16h<0>(fx[0], rs, g * 16, xg); ld16h<64>(fx[1], rs, g * 16, xg); ld16h<128>(fx[2], rs, g * 16, xg); ld16h<192>(fx[3], rs, g * 16, xg);
            }
            if constexpr (s == FS0 && n == 1) {
              wait_vm<SINCE(FSL)>();
              asm volatile("" : "+v"(kv)::"memory");
              PINV4(fx);
              STAMPAT(52);
              fkey = key_wait(rs, T, l, 0, KREP(), kv);
              STAMPAT(53);
              sel0 = key_row(fkey);
              aff = (int)(sel0 >> 8) == p;
            }
            if constexpr (s == FS0 && n == 2) {
              asm volatile("" : "=v"(sa), "=v"(sb));
              if (ffn_on) {
                if (xg != ffn_xoff(fkey, 0, w)) {
#pragma unroll
                  for (int i = 0; i < 4; ++i) fx[i] = ld16(rs, g * 16 + i * 64, ffn_xoff(fkey, 0, w));
                }
                ffn_gu(gv, uv, fx, sa, sb);
              }
            }
            if constexpr (s == FS0 && n == 3) {
              if (ffn_on) {
                ffn_gu_check(rs, T, ffn_xoff(fkey, 0, w), l, gv, uv, fx, sa, sb);
                ffn_gu_store(smem, l, w, sa, sb);
                count_in(1);
              }
            }
            if constexpr (s == FS0D) {
              if constexpr (n == 0) qc = __hip_atomic_load(qcnt + 1, __ATOMIC_RELAXED, __HIP_MEMORY_SCOPE_WORKGROUP);
              if constexpr (n == 1) {
                asm volatile("" : "=v"(dfr[0]), "=v"(dfr[1]), "=v"(dfr[2]), "=v"(dfr[3]), "=v"(dfr[4]), "=v"(dfr[5]), "=v"(dfr[6]), "=v"(dfr[7]), "=v"(hh), "=v"(sa));
                if (ffn_on) { count_wait(1, 8); ffn_d_read(smem, l, w, sa, dfr); }
              }
              if constexpr (n == 2) {
                if (ffn_on) {
                  for (int e = 0; e < 4; ++e) sa[e] = ffn_d_h(ffn_d_sum(sa[e]));
                  hh = u32x2{pack2(sa[0], sa[1]), pack2(sa[2], sa[3])};
                }
              }
              if constexpr (n == 3) {
                asm volatile("" : "=v"(sa), "=v"(sb));
                if (ffn_on) {
                  sa = f32x4{0.f, 0.f, 0.f, 0.f}; sb = sa;
#pragma unroll
                  for (int i = 0; i < 8; i += 2) { sa = ffn_d_mma(l, i, dfr[i], hh, sa); sb = ffn_d_mma(l, i + 1, dfr[i + 1], hh, sb); }
                }
              }
              if constexpr (n == 4) { if (ffn_on) ffn_d_store(rs, l, w, 0, slice, sa + sb); STAMPAT(54); }
            }
            if constexpr (s == FS1 && n == 4) {
              DEF4(fx);
              asm volatile("" : "=v"(cxn), "=v"(cx1));
              if (!ffn_on) {
                const unsigned G = xcd * 4 + bi - 28, co = (sel0 >> 7) * 2048 + G * 64, jj = (tid & 3) * 16 + (tid & 4) * 2;
#pragma unroll
                for (int k = 0; k < 4; ++k) ld16h<0>(fx[k], rs, min(tid + 512 * k, 1791) * 16, OFF_PART + G * 32768);
                ld8h(cxn, rs, jj, OFF_CA + co); ld8h(cx1, rs, jj, OFF_CB + co);
              }
            }
            if constexpr (s == FS1S && n == 2) {
              if (!ffn_on) {
                wait_vm<SINCE(FS1)>();
                PINV4(fx);
                asm volatile("" : "+v"(cxn), "+v"(cx1)::"memory");
                STAMPAT(55);
                red_sum(rs, smem, T, tid, w, 0, xcd * 4 + bi - 28, fkey, fx, cxn, cx1);
                STAMPAT(56);
                BAR();
                if (w == 0) {
                  const u32x2 ov = red_final(smem, l, fkey, cxn, cx1);
                  if (l < 8) st8(unpoison2(ov), rs, OFF_XNEW + ((xcd * 4 + bi - 28) * 8 + (l & 3) * 2 + (l >> 2)) * 8, 0);
                }
              }
            }
            if constexpr (s == FXA && n == 4) { ld16h<0>(fx[0], rs, g * 16, XP_OFF()); ld16h<64>(fx[1], rs, g * 16, XP_OFF()); ld16h<128>(fx[2], rs, g * 16, XP_OFF()); ld16h<192>(fx[3], rs, g * 16, XP_OFF()); }
            if constexpr (s == FXB) {
#define FX_FRAG(i) (*(const LDS_AS u32x4*)(size_t)(bo[0][1] + (6 * (i) + min(w, 5)) * 1024))
              if constexpr (n == 0) { STAMPAT(58); df[0] = FX_FRAG(0); df[1] = FX_FRAG(1); }
              if constexpr (n == 1) {
                wait_vm<SINCE(FXA)>();
                PINV4(fx);
                sa = mfma16(df[0], fx[0], f32x4{0.f, 0.f, 0.f, 0.f}); sb = mfma16(df[1], fx[1], f32x4{0.f, 0.f, 0.f, 0.f});
                SB();
                df[0] = FX_FRAG(2); df[1] = FX_FRAG(3);
              }
              if constexpr (n == 2) { sa = mfma16(df[0], fx[2], sa); sb = mfma16(df[1], fx[3], sb); }
              if constexpr (n == 4) {
                const float z = sa[0] + sb[0];
                if (__any(z != z)) {
                  ffn_xwait(rs, T, XP_OFF(), l, fx);
                  f32x4 t0 = mfma16(FX_FRAG(0), fx[0], f32x4{0.f, 0.f, 0.f, 0.f});
                  t0 = mfma16(df[0], fx[2], t0);
                  f32x4 t1 = mfma16(FX_FRAG(1), fx[1], f32x4{0.f, 0.f, 0.f, 0.f});
                  sa = t0; sb = mfma16(df[1], fx[3], t1);
                }
                if (w < 6) dot_store(OFF_FX);
                STAMPAT(57);
              }
            }
            if constexpr (s == FXL && n == 4) {
              asm volatile("" : "=v"(fx[0]), "=v"(fx[1]));
              if (aff && w < 4) { ld16h<0>(fx[0], rs, l * 16, FP_OFF(0)); ld16h<0>(fx[1], rs, l * 16, FP_OFF(1)); }
            }
            if constexpr (s == FXS && n == 4) {
              if (aff && w < 4) {
                STAMPAT(59);
                wait_vm<0>();
                asm volatile("" : "+v"(fx[0]), "+v"(fx[1])::"memory");
                if (!FX_OK(2)) {
                  unsigned spins = 0;
                  do {
                    if (spin_fail(spins, rs, T)) break;
                    fx[0] = ld16(rs, l * 16, FP_OFF(0)); fx[1] = ld16(rs, l * 16, FP_OFF(1));
                  } while (!FX_OK(2));
                }
                STAMPAT(60);
                if (w == 3) {
                  f32x2 nx[8];
                  unpack8v(fx[0], nx); unpack8v(fx[1], nx + 4);
                  const f32x2 sm = ((nx[0] + nx[1]) + (nx[2] + nx[3])) + ((nx[4] + nx[5]) + (nx[6] + nx[7]));
                  const float mean = sum64(hadd(sm)) * (1.0f / 1024.0f);
                  const f32x2 m2 = {mean, mean};
                  f32x2 q = {0.f, 0.f};
#pragma unroll
                  for (int j = 0; j < 8; ++j) { const f32x2 d = nx[j] - m2; q = __builtin_elementwise_fma(d, d, q); }
                  const float rd = ln_rd(sum64(hadd(q)) * (1.0f / 1024.0f));
                  if (l == 0) *(LDS_AS f32x2*)(smem + L_FXL + 384) = f32x2{rd, -rbf1(mean)};
                } else {
                  tile_sum(fx[0], L_FXL);
                }
                count_in(3);
              }
            }
          }
        };
        static_for<32>([&](auto sc_) __attribute__((always_inline)) {
          constexpr int s = decltype(sc_)::value;
          if constexpr (s > 0) PIN_ACC();
          if constexpr (IT == 1) wait_vm<vm_at(IT, s, 0) - vm_a(IT, s)>();
          pin_w<s % WA>(af[s % WA][0], af[s % WA][1]);
          VSTEP(s);
          SB();
          static_for<6>([&](auto nc_) __attribute__((always_inline)) {
            constexpr int n = decltype(nc_)::value;
            if constexpr (s == 0) { acc[0][n] = f32x4{0.f, 0.f, 0.f, 0.f}; acc[1][n] = acc[0][n]; }
            constexpr bool on = s < QL || n >= 2;
            if constexpr (on) acc[0][n] = n < 4 ? mfma16(bfr[n].v, af[s % WA][0], acc[0][n]) : mfma16(af[s % WA][0], bfr[n].v, acc[0][n]);
            SB();
            if constexpr (n == 0) { QKV_RDB(s, 5); }
            else if constexpr (s + 1 < 32 && (s + 1 < QL || n > 2)) QKV_RDB(s + 1, n - 1);
            SB();
            if constexpr (on) acc[1][n] = n < 4 ? mfma16(bfr[n].v, af[s % WA][1], acc[1][n]) : mfma16(af[s % WA][1], bfr[n].v, acc[1][n]);
            SB();
            if constexpr (s + 1 < 32 && (s + 1 < QL || n >= 2)) QKV_RDA(s + 1, n);
            SB();
            gap(sc_, nc_);
            SB();
          });
        });
        PIN_ACC();
        VSTEP(32);
        STAMPAT(18);
        VSTEP(33);
        frag_t qw[32 - QL][2];
        static_for<32 - QL>([&](auto c_) __attribute__((always_inline)) {
          constexpr int s = QL + decltype(c_)::value;
          QKV_RDA(s, 0); QKV_RDB(s, 0); QKV_RDA(s, 1); QKV_RDB(s, 1);
          qw[s - QL][0] = bfr[0]; qw[s - QL][1] = bfr[1];
        });
        SB();
        const bool own = aff && (int)((sel0 >> 5) & 7) == rt;
        const bool m0 = !(sel0 & 16), mq = (l & 15) == (int)(sel0 & 15), mv = g == (int)((sel0 & 15) >> 2);
        auto patch_qk = [&](int n) __attribute__((always_inline)) {
          f32x4 t = *(const LDS_AS f32x4*)(smem + L_FXL + n * 64 + g * 16);
          const f32x4 c = *(const LDS_AS f32x4*)(smem + L_CSL + n * 64 + g * 16);
          const float nb = *(const LDS_AS float*)(smem + L_FXL + 388);
#pragma unroll
          for (int e = 0; e < 4; ++e) { t[e] = __builtin_fmaf(c[e], nb, t[e]); asm volatile("" : "+v"(t[e])); }
          acc[0][n] = (mq && m0) ? t : acc[0][n];
          acc[1][n] = (mq && !m0) ? t : acc[1][n];
        };
        if (own) {
          while (__hip_atomic_load(qcnt + 3, __ATOMIC_RELAXED, __HIP_MEMORY_SCOPE_WORKGROUP) < 4u) {}
          asm volatile("" ::: "memory");
          patch_qk(2); patch_qk(3);
          const f32x2 nab = *(const LDS_AS f32x2*)(smem + L_FXL + 384);
          const LDS_AS float* fl_ = (const LDS_AS float*)(smem + L_FXL + 256) + (l & 15);
          const LDS_AS float* cl_ = (const LDS_AS float*)(smem + L_CSL + 256) + (l & 15);
          const float t4 = __builtin_fmaf(cl_[0], nab[1], fl_[0]), t5 = __builtin_fmaf(cl_[16], nab[1], fl_[16]);
          const unsigned na = __builtin_bit_cast(unsigned, (float)nab[0]);
#pragma unroll
          for (int a = 0; a < 2; ++a) {
            const bool ma = a ? !m0 : m0;
            aq[a] = (mq && ma) ? na : aq[a];
#pragma unroll
            for (int e = 0; e < 4; ++e) {
              const bool me = mv && ma && e == (int)(sel0 & 3);
              acc[a][4][e] = me ? t4 : acc[a][4][e]; acc[a][5][e] = me ? t5 : acc[a][5][e];
              av[a][e] = me ? na : av[a][e];
            }
          }
        }
        constexpr float C2 = 1.4426950408889634f * 0.17677669529663687f;
#pragma unroll
        for (int a = 0; a < 2; ++a) {
          f32x2 a2 = {asf(aq[a]), asf(aq[a])};
          qs2[a] = f32x2{asf(aq[a]) * C2, asf(aq[a]) * C2};
          asm volatile("" : "+v"(a2), "+v"(qs2[a]));
          acc[a][2] *= f32x4{a2[0], a2[1], a2[0], a2[1]}; acc[a][3] *= f32x4{a2[0], a2[1], a2[0], a2[1]};
          acc[a][4] *= __builtin_bit_cast(f32x4, av[a]); acc[a][5] *= __builtin_bit_cast(f32x4, av[a]);
        }
        k0 = pack8(acc[0][2], acc[0][3]); k1 = pack8(acc[1][2], acc[1][3]); v0 = pack8(acc[0][4], acc[1][4]); v1 = pack8(acc[0][5], acc[1][5]);
        const unsigned kvo = OFF_KV + IT * (8u << 20) + (p * 32 + h) * 32768 + rt * 4096;
        if (p < 7) { st16(k0, rs, l * 16, kvo); st16(k1, rs, l * 16 + 1024, kvo); st16(v0, rs, l * 16 + 2048, kvo); st16(v1, rs, l * 16 + 3072, kvo); }
        char* ownp = smem + (8 + rt) * 2048 + l * 16;
        *(u32x4*)(ownp + L_K) = k0;
        if (rt < 7) *(u32x4*)(ownp + L_K + 1024) = k1;
        *(u32x4*)(ownp + L_V) = v0; *(u32x4*)(ownp + L_V + 1024) = v1;
        SB();
        static_for<32 - QL>([&](auto c_) __attribute__((always_inline)) {
          constexpr int s = QL + decltype(c_)::value;
#pragma unroll
          for (int n = 0; n < 2; ++n) { acc[0][n] = mfma16(qw[s - QL][n].v, af[s % WA][0], acc[0][n]); acc[1][n] = mfma16(qw[s - QL][n].v, af[s % WA][1], acc[1][n]); }
        });
        if (own) { patch_qk(0); patch_qk(1); }
      };
      qkv_loop(it_c);
    }
#undef A_LOAD1
#undef QKV_RDA
#undef QKV_RDB
#undef QKV_BO
    STAMPAT(2); STAMPAT(21);
    {
      IDS();
      const GAS bf16 *pwo, *pwg, *pwu;
      asm volatile("s_load_dwordx2 %0, %3, 24\n\ts_load_dwordx2 %1, %3, 40\n\ts_load_dwordx2 %2, %3, 48" : "=&s"(pwo), "=&s"(pwg), "=&s"(pwu) : "s"(ka));
      const int ql = l & 15;
      const f32x4 z4 = {0.f, 0.f, 0.f, 0.f}, ninf = {-INFINITY, -INFINITY, -INFINITY, -INFINITY};
      LDS_AS unsigned* rdy = (LDS_AS unsigned*)(smem + L_RDY);
      const unsigned rtag = T[0] + it;
      u32x4 qf[2];
      f32x4 cn[2], oa[2][2], sum[2], S[2][2];
      bool told = false;
      auto announce = [&](bool now) __attribute__((always_inline)) {
        if (!told) {
          unsigned st;
          asm volatile("s_getreg_b32 %0, hwreg(HW_REG_IB_STS)" : "=s"(st)::"memory");
          if (now || !(st & 0xC0000Fu)) {
            DRAIN();
            if (l == 0) st8(T, rs, rt * SKW, F_KV + (it * 256 + p * 32 + h) * SKV);
            told = true;
            STAMPAT(34);
          }
        }
      };
      unsigned one2 = 0x3F803F80u;
      asm volatile("" : "+v"(one2));
      const u32x4 ones = {one2, one2, one2, one2};
      auto ex8 = [&](f32x4 a, f32x4 b) __attribute__((always_inline)) {
        return u32x4{pack2(__builtin_amdgcn_exp2f(a[0]), __builtin_amdgcn_exp2f(a[1])), pack2(__builtin_amdgcn_exp2f(a[2]), __builtin_amdgcn_exp2f(a[3])),
                     pack2(__builtin_amdgcn_exp2f(b[0]), __builtin_amdgcn_exp2f(b[1])), pack2(__builtin_amdgcn_exp2f(b[2]), __builtin_amdgcn_exp2f(b[3]))};
      };
      {
#pragma unroll
        for (int a = 0; a < 2; ++a) {
          const f32x4 c4 = {qs2[a][0], qs2[a][1], qs2[a][0], qs2[a][1]};
          qf[a] = pack8(acc[a][0] * c4, acc[a][1] * c4);
        }
        f32x4 mk;
#pragma unroll
        for (int e = 0; e < 4; ++e) mk[e] = (4 * g + e <= ql) ? 0.f : -INFINITY;
        const f32x4 d0 = mfma16(k0, qf[0], mk), d1 = mfma16(k1, qf[1], mk), d2 = mfma16(k0, qf[1], z4);
        const float m0 = -max_x32(max_x16(fmaxf(fmaxf(d0[0], d0[1]), fmaxf(d0[2], d0[3]))));
        const float m1 = -max_x32(max_x16(fmaxf(fmaxf(d1[0], d1[1]), fmaxf(d1[2], d1[3]))));
        cn[0] = f32x4{m0, m0, m0, m0}; cn[1] = f32x4{m1, m1, m1, m1};
        asm volatile("" : "+v"(cn[0]), "+v"(cn[1]));
        announce(false);
        const f32x4 e0 = d0 + cn[0];
        const u32x4 p0 = {pack2(__builtin_amdgcn_exp2f(e0[0]), __builtin_amdgcn_exp2f(e0[1])), pack2(__builtin_amdgcn_exp2f(e0[2]), __builtin_amdgcn_exp2f(e0[3])), 0u, 0u};
        const u32x4 p1 = ex8(d2 + cn[1], d1 + cn[1]);
        oa[0][0] = mfma16(v0, p0, z4); oa[0][1] = mfma16(v1, p0, z4); sum[0] = mfma16(ones, p0, z4);
        oa[1][0] = mfma16(v0, p1, z4); oa[1][1] = mfma16(v1, p1, z4); sum[1] = mfma16(ones, p1, z4);
      }
      const char* kb_ = smem + L_K + l * 16;
      const char* vb_ = smem + L_V + l * 16;
      const unsigned wov = WO_VOFF(), fnv = (l >> 1) * (F_ * 2) + (l & 1) * 16;
      asm volatile("" : "+v"(oa[0][0]), "+v"(oa[0][1]), "+v"(oa[1][0]), "+v"(oa[1][1]), "+v"(sum[0]), "+v"(sum[1]));
      const unsigned pw = __builtin_amdgcn_readfirstlane(it * 256 + p * 32 + h - 32), pfo = F_KV + pw * SKV + rt * SKW;
      const bool fetcher = rt < 4 && p > 0;
      announce(false);
      STAMPAT(32);
      BAR();
      asm volatile("" : "+s"(pwo), "+s"(pwg), "+s"(pwu));
      STAMPAT(33);
#define KV_FETCH(gd)                                                                                                          \
  {                                                                                                                           \
    const unsigned pvo = OFF_KV + pw * 32768 + (gd) * 4096;                                                                   \
    dma16s<0>(rs, l * 16, pvo, lbase + L_K + (gd) * 2048); dma16s<1024>(rs, l * 16, pvo, lbase + L_K + (gd) * 2048);           \
    dma16s<0>(rs, l * 16, pvo + 2048, lbase + L_V + (gd) * 2048); dma16s<1024>(rs, l * 16, pvo + 2048, lbase + L_V + (gd) * 2048); \
  }
      auto fetch = [&]() __attribute__((always_inline)) {
        poll_flags(rs, KVJ ? (l & 1) * 4 * SKW : 0, pfo, T);
        KV_FETCH(rt);
        if (KVJ) { KV_FETCH(rt + 4); if (l == 0) rdy[8 + rt] = rtag; }
        STAMPAT(35);
      };
      announce(false);
      const bool first = fetcher && told;
      if (first) fetch();
#define LDS16(a_) (*(const LDS_AS u32x4*)(size_t)(a_))
      u32x4 kf[2], vf[2], p0, p1;
#define AE(a, i) { S[a][(i) >> 2][(i) & 3] = __builtin_amdgcn_exp2f(S[a][(i) >> 2][(i) & 3]); asm volatile("" : "+v"(S[a][(i) >> 2][(i) & 3])); SB(); }
#define AC(a, P, j) { P[j] = pack2(S[a][(j) >> 1][2 * ((j) & 1)], S[a][(j) >> 1][2 * ((j) & 1) + 1]); asm volatile("" : "+v"(P[j])); SB(); }
#define PHASE(a, P, M1, M2, L2, M3, M4, L4, M5)                                                                               \
  AE(a, 0) AE(a, 1) M1; SB(); AE(a, 2) AE(a, 3) M2; SB(); L2; SB(); AC(a, P, 0) AC(a, P, 1) AE(a, 4) M3; SB(); AE(a, 5) AE(a, 6) M4; SB(); \
  L4; SB(); AE(a, 7) AC(a, P, 2) M5; SB(); AC(a, P, 3)
#define PV(a, d, P) oa[a][d] = mfma16(vf[d], P, oa[a][d])
#define RS(a, P) sum[a] = mfma16(ones, P, sum[a])
#define SC(a, t, c) S[a][t] = mfma16(kf[t], qf[a], c)
#define K_AT(ad) (kf[0] = LDS16(ad), kf[1] = LDS16((ad) + 1024))
#define V_AT(ad) (vf[0] = LDS16((ad) + L_V), vf[1] = LDS16((ad) + L_V + 1024))
#define A_FIRST(ad, c00, c01, c10, c11, KN) { K_AT(ad); V_AT(ad); SB(); SC(0, 0, c00); SC(0, 1, c01); SB(); PHASE(0, p0, SC(1, 0, c10), SC(1, 1, c11), KN, , , , ) }
#define A_NEXT(ad, c00, c01, c10, c11, KN)                                                                                        \
  { PHASE(1, p1, PV(0, 0, p0), PV(0, 1, p0), , SC(0, 0, c00), SC(0, 1, c01), , RS(0, p0))                                     \
    PHASE(0, p0, PV(1, 0, p1), PV(1, 1, p1), V_AT(ad), SC(1, 0, c10), SC(1, 1, c11), KN, RS(1, p1)) }
#define A_LAST() { PHASE(1, p1, PV(0, 0, p0), PV(0, 1, p0), , RS(0, p0), , , ) SB(); PV(1, 0, p1); PV(1, 1, p1); RS(1, p1); }
      int nw = 0;
      auto piece = [&]() __attribute__((always_inline)) {
        if constexpr (it == 0) { if (ffn_on && nw < 8) { FFN_DMA(nw); } ++nw; }
      };
      unsigned ad = lbase + l * 16 + (6 + rt) * 2048;
      asm volatile("" : "+v"(ad));
      if (rt > 0) {
        A_FIRST(ad + 2048, cn[0], cn[0], cn[1], cn[1], K_AT(ad));
#pragma unroll 1
        for (int i = rt - 1; i > 0; --i) {
          ad -= 2048;
          asm volatile("" : "+v"(ad));
          if (PCE && told && rt >= 4) piece();
          announce(false);
          A_NEXT(ad + 2048, cn[0], cn[0], cn[1], cn[1], K_AT(ad));
        }
        announce(false);
        A_LAST();
      }
      announce(true);
      STAMPAT(26);
      {
        f32x4 mk;
#pragma unroll
        for (int e = 0; e < 4; ++e) mk[e] = (4 * g + e > ql) ? 0.f : -INFINITY;
        f32x4 cm0 = cn[0] + mk, cm1 = cn[1] + mk, cmi = ninf;
        asm volatile("" : "+v"(cm0), "+v"(cm1), "+v"(cmi));
        if (rt >= 4) {
          if (p > 0) {
            while (!__all(__hip_atomic_load(rdy + 8 + (l & 3), __ATOMIC_RELAXED, __HIP_MEMORY_SCOPE_WORKGROUP) == rtag)) __builtin_amdgcn_s_sleep(1);
            asm volatile("" ::: "memory");
          }
          if constexpr (it == 0) { for (; nw < 8;) piece(); }
          static_for<it ? 12 : 8>([&](auto c_) __attribute__((always_inline)) { constexpr int i = decltype(c_)::value; WO_PIECE_W(15 - i / 2, 2 * (rt & 3) + (i & 1)); });
        } else if (p > 0) {
          if (!first) fetch();
          if (KVJ) {
            DRAIN();
            if (l < 2) rdy[rt + 4 * l] = rtag;
            if constexpr (it == 0) { for (; nw <= rt;) piece(); }
          } else {
            const u32x2 f2 = ld8(rs, 0, pfo + 4 * SKW);
            const bool up = __all(f2[0] == T[0] && f2[1] == T[1]);
            DRAIN();
            if (l == 0) rdy[rt] = rtag;
            if (!up) poll_flags(rs, 0, pfo + 4 * SKW, T);
            KV_FETCH(rt + 4);
            if (l == 0) rdy[8 + rt] = rtag;
            told = false;
          }
        }
        STAMPAT(3);
        if (p > 0) {
          auto landed = [&](bool now) __attribute__((always_inline)) {
            if (!told) {
              unsigned st;
              asm volatile("s_getreg_b32 %0, hwreg(HW_REG_IB_STS)" : "=s"(st)::"memory");
              if (now || !(st & 0xC0000Fu)) {
                DRAIN();
                if (l == 0) rdy[rt + 4] = rtag;
                told = true;
              }
            }
          };
          unsigned have = 0;
          auto need = [&](int gq) __attribute__((always_inline)) {
            if (__builtin_expect(gq < 8 && !((have >> gq) & 1), 0)) {
              do {
                const unsigned t = __hip_atomic_load(rdy + (l & 7), __ATOMIC_RELAXED, __HIP_MEMORY_SCOPE_WORKGROUP);
                have = (unsigned)__builtin_amdgcn_ballot_w64(t == rtag);
              } while (!((have >> gq) & 1));
              asm volatile("" ::: "memory");
            }
          };
          ad = lbase + l * 16 + rt * 2048;
          asm volatile("" : "+v"(ad));
          need(rt);
          K_AT(ad); V_AT(ad); SB(); SC(0, 0, cm0); SC(0, 1, cn[0]); SB();
          PHASE(0, p0, SC(1, 0, cmi), SC(1, 1, cm1), , , , , )
          if (!KVJ) landed(false);
          need(rt + 1);
          K_AT(ad + 2048);
#pragma unroll 1
          for (int gq = rt + 1; gq < 8; ++gq) {
            ad += 2048;
            asm volatile("" : "+v"(ad));
            if (KVJ) piece();
            else { landed(gq >= rt + KVD); if (told) { piece(); if (PC2) piece(); } }
            need(gq + 1);
            A_NEXT(ad, cn[0], cn[0], cn[1], cn[1], K_AT(ad + 2048));
          }
          A_LAST();
        }
        if constexpr (it == 0) { for (; nw < 8;) piece(); }
      }
      STAMPAT(25);
      {
        float big = 0.f;
#pragma unroll
        for (int a = 0; a < 2; ++a)
#pragma unroll
          for (int d = 0; d < 2; ++d)
            big = __builtin_elementwise_maximum(__builtin_elementwise_maximum(big, __builtin_elementwise_maximum(fabsf(oa[a][d][0]), fabsf(oa[a][d][1]))),
                                                __builtin_elementwise_maximum(fabsf(oa[a][d][2]), fabsf(oa[a][d][3])));
        bool ok = sum[0][0] >= 0.5f && sum[0][0] < 0x1p64f && sum[1][0] >= 0.5f && sum[1][0] < 0x1p64f && big < INFINITY;
        if (!__all(ok)) {
#pragma unroll
          for (int a = 0; a < 2; ++a) {
            const int qs = 2 * rt + a, qq = 256 + 16 * qs + ql, klo = max(qq - 255, p > 0 ? 0 : 256);
            auto score = [&](int t) __attribute__((always_inline)) {
              f32x4 s = mfma16(t == 31 ? k1 : *(const u32x4*)(kb_ + t * 1024), qf[a], z4);
#pragma unroll
              for (int e = 0; e < 4; ++e) { const int kk = 16 * t + 4 * g + e; s[e] = (kk <= qq && kk >= klo) ? s[e] : -INFINITY; }
              return s;
            };
            float mx = -INFINITY;
#pragma unroll 1
            for (int t = p > 0 ? qs : 16; t <= qs + 16; ++t) { const f32x4 s = score(t); mx = fmaxf(fmaxf(mx, fmaxf(s[0], s[1])), fmaxf(s[2], s[3])); }
            mx = max_x32(max_x16(mx));
            oa[a][0] = z4; oa[a][1] = z4; sum[a] = z4;
#pragma unroll 1
            for (int G = p > 0 ? qs >> 1 : 8; G <= (qs + 16) >> 1; ++G) {
              const f32x4 s0 = score(2 * G), s1 = score(2 * G + 1);
              f32x4 e0, e1;
#pragma unroll
              for (int e = 0; e < 4; ++e) { e0[e] = __builtin_amdgcn_exp2f(s0[e] - mx); e1[e] = __builtin_amdgcn_exp2f(s1[e] - mx); }
              const u32x4 pf = pack8(e0, e1);
              oa[a][0] = mfma16(*(const u32x4*)(vb_ + G * 2048), pf, oa[a][0]);
              oa[a][1] = mfma16(*(const u32x4*)(vb_ + G * 2048 + 1024), pf, oa[a][1]);
              sum[a] = mfma16(ones, pf, sum[a]);
            }
          }
        }
      }
#pragma unroll
      for (int a = 0; a < 2; ++a) {
        const float inv = __builtin_amdgcn_rcpf(sum[a][0]);
        oa[a][0] *= inv; oa[a][1] *= inv;
      }
      const unsigned oo = OFF_O + it * (4u << 20) + ((p * 8 + rt) * 64 + h * 2) * 1024;
      st16(pack8(oa[0][0], oa[0][1]), rs, l * 16, oo);
      st16(pack8(oa[1][0], oa[1][1]), rs, l * 16 + 1024, oo);
      STAMPAT(4);
      DRAIN();
      BAR();
      if (tid == 0) st8(T, rs, h * SO, F_O + (it * 8 + p) * 32 * SO);
      if constexpr (it == 0) {
        DEF4(gv); DEF4(uv);
        if (ffn_on) {
          const unsigned ro = 65536 + w * 1024 + (4 * g + ((l & 15) >> 2)) * 32 + (l & 3) * 8;
#pragma unroll
          for (int i = 0; i < 4; ++i) {
            frag_t f;
            f.hf.a = trread(smem, ro + i * 16384); f.hf.b = trread(smem, ro + i * 16384 + 512);
            gv[i] = f.v;
            f.hf.a = trread(smem, ro + i * 16384 + 8192); f.hf.b = trread(smem, ro + i * 16384 + 8704);
            uv[i] = f.v;
          }
        }
        PINV4(gv); PINV4(uv);
        WO_PIECE(11); WO_PIECE(10); WO_PIECE(9); WO_PIECE(8); WO_PIECE(7); WO_PIECE(6); WO_PIECE(5); WO_PIECE(4);
      }
      WO_PIECE(3); WO_PIECE(2); WO_PIECE(1); WO_PIECE(0);
    }
    STAMPAT(5);
    IDS();
    ARGP(pwr, 4); ARGP(pwd, 7); ARGP(pout, 8); ARGP(px, 0);
    const int kh = w >> 2, rt2 = rb * 4 + (w & 3);
    const unsigned R = rt2 * 32 + kh * 16 + (l & 15);
    const unsigned to = (rt2 * 64 + ct * 4 + kh) * 1024 + l * 16;
    const unsigned rmo = ct * 64 + ((g & 1) ? 12 + 4 * g : 4 * g);
    f32x2 wv[8];
    {
      u32x4 wa[2], wrp[2], res[2];
#pragma unroll
      for (int j = 0; j < 2; ++j) {
        wrp[j] = *(const GAS u32x4*)(pwr + rmo + 32 * j);
        wa[j] = *(const GAS u32x4*)(pwr + l * 8 + j * 512);
        if constexpr (it == 0) res[j] = *(const GAS u32x4*)(px + R * 1024 + rmo + 32 * j);
      }
      {
        f32x2 t[8];
        unpack8v(wa[0], t); unpack8v(wa[1], t + 4);
        const f32x2 s = ((t[0] + t[1]) + (t[2] + t[3])) + ((t[4] + t[5]) + (t[6] + t[7]));
        wsum = sum64(hadd(s));
        unpack8v(trade16(wrp[0]), wv); unpack8v(trade16(wrp[1]), wv + 4);
        if constexpr (it == 0) { res[0] = trade16(res[0]); res[1] = trade16(res[1]); asm volatile("" : "+v"(res[0]), "+v"(res[1])); }
        asm volatile("" : "+v"(wsum), "+v"(wv[0]), "+v"(wv[1]), "+v"(wv[2]), "+v"(wv[3]), "+v"(wv[4]), "+v"(wv[5]), "+v"(wv[6]), "+v"(wv[7]));
      }
      unsigned bt[4];
#pragma unroll
      for (int n = 0; n < 4; ++n) {
        bt[n] = lbase + 32768 + 65536 + kh * 4096 + (4 * g + ((l & 15) >> 2)) * 128 + (l & 3) * 8 + ((n ^ (2 * (g & 1) + ((l & 15) >> 3))) << 5);
        asm volatile("" : "+v"(bt[n]));
      }
      if (w == 0) poll_flags(rs, (l & 31) * SO, F_O + (it * 8 + (rb >> 1)) * 32 * SO, T);
      else if (it == 0 && w < 3) poll_flags2(rs, F_XN + (((w - 1) * 4 + g) * RX + bi % RX) * 256 + (l & 15) * 16, T);
      asm volatile("s_barrier" ::: "memory");
      STAMPAT(6);
      const unsigned abase = OFF_O + it * (4u << 20) + (rt2 * 64 + 32 * kh) * 1024;
      const unsigned av0 = l * 16 + kh * 1024, av1 = l * 16 + (kh ^ 1) * 1024;
      u32x4 af[WB][2];
#define O_LOAD(s_) { af[(s_) % WB][0] = ld16(rs, av0 + (~(s_) & 1) * 2048, abase + ((15 - (s_)) >> 1) * 4096); af[(s_) % WB][1] = ld16(rs, av1 + (~(s_) & 1) * 2048, abase + ((15 - (s_)) >> 1) * 4096); }
#pragma unroll
      for (int s = 0; s < WR; ++s) O_LOAD(s);
#pragma unroll
      for (int j = 0; j < 2; ++j) {
        if constexpr (it == 1) res[j] = ld16(rs, OFF_XNEW + ((2 * ct + j) * 4 + g) * 16, 0);
      }
      SB();
      f32x4 ac[2][4];
#pragma unroll
      for (int a = 0; a < 2; ++a)
#pragma unroll
        for (int n = 0; n < 4; ++n) ac[a][n] = f32x4{0.f, 0.f, 0.f, 0.f};
      u32x4 bfr[2][4];
#define WO_RD(buf, s, n)                                                                             \
  {                                                                                                  \
    const unsigned o_ = bt[n] + ((15 - (s)) & 7) * 8192;                                             \
    frag_t f_;                                                                                       \
    f_.hf.a = trabs(o_); f_.hf.b = trabs(o_ + 2048);                                                 \
    bfr[buf][n] = f_.v;                                                                              \
  }
#pragma unroll
      for (int n = 0; n < 4; ++n) WO_RD(0, 0, n);
#pragma unroll
      for (int s = 0; s < 16; ++s) {
        const int cur = s & 1;
        asm volatile("" : "+{v[176:179]}"(ac[0][0]), "+{v[180:183]}"(ac[0][1]), "+{v[184:187]}"(ac[0][2]), "+{v[188:191]}"(ac[0][3]),
                          "+{v[192:195]}"(ac[1][0]), "+{v[196:199]}"(ac[1][1]), "+{v[200:203]}"(ac[1][2]), "+{v[204:207]}"(ac[1][3]));
        if (s == (it == 0 ? 3 : 11)) asm volatile("s_barrier" ::: "memory");
        if (s == 7) {
#pragma unroll
          for (int n = 0; n < 4; ++n) { bt[n] -= 65536; asm volatile("" : "+v"(bt[n])); }
        }
        if (it == 0 && s == 8) {
#pragma unroll
          for (int i = 0; i < 3; ++i) st16(keep[i], rs, l * 16 + i * 1024, OFF_WI + (h * 8 + ((ROT0 + 5) & 7)) * STAGE + w * 3072);
        }
        if (it == 0 && s == 10) {
          unsigned pz1 = POISON;
          asm volatile("" : "+v"(pz1));
          const u32x4 pz = {pz1, pz1, pz1, pz1};
          st16(pz, rs, OFF_PART + (tid >> 8) * (1u << 20) + bid * 4096 + (tid & 255) * 16, 0);
          if (w < 2) st16(pz, rs, OFF_STAT + 524288 + (rb * 16 + ct) * 2048 + tid * 16, 0);
          if (w == 4 && l < 24) st16(pz, rs, (l < 8 ? OFF_CA + 32768 : l < 16 ? OFF_CB : OFF_CB + 32768) + rb * 2048 + ct * 128 + (l & 7) * 16, 0);
          if (w == 7 && l < 24) st16(pz, rs, OFF_FX + ((h * 6 + (l >> 2)) * 8 + p) * 64 + (l & 3) * 16, 0);
          if (w == 6 && l < (ct < (int)RKEY ? 3 : 2)) st4(pz1, rs, l < 2 ? OFF_XNEW + bid * 8 + l * 4 : KEY_AT(1, ct) + rb * SKEY, 0);
        }
        if (s < WB - WR) O_LOAD(WR + 2 * s < 16 ? WR + 2 * s : 0);
        SB();
#pragma unroll
        for (int n = 0; n < 4; ++n) {
          ac[0][n] = mfma16(bfr[cur][n], af[s % WB][0], ac[0][n]);
          ac[1][n] = mfma16(bfr[cur][n], af[s % WB][1], ac[1][n]);
          if (s + 1 < 16) WO_RD(cur ^ 1, s + 1, n);
          if (s == 9) {
            if (it == 1 && n == 0) { res[0] = R == sel0 ? res[0] : x2p[0]; res[1] = R == sel0 ? res[1] : x2p[1]; }
            v[2 * n] = f32x2{lo16(res[n >> 1][2 * (n & 1)]), hi16(res[n >> 1][2 * (n & 1)])};
            v[2 * n + 1] = f32x2{lo16(res[n >> 1][2 * (n & 1) + 1]), hi16(res[n >> 1][2 * (n & 1) + 1])};
            asm volatile("" : "+v"(v[2 * n]), "+v"(v[2 * n + 1]));
          }
          SB();
        }
        if (s < WB - WR) { O_LOAD(WR + 2 * s + 1); }
        else if (s + WB < 16) { O_LOAD(s + WB); }
        SB();
      }
#undef WO_RD
#undef O_LOAD
      STAMPAT(7);
#define XCH_AT(v) ((v) == 7 ? 155648 : (v) * 4096)
#pragma unroll
      for (int n = 0; n < 4; ++n) *(f32x4*)(smem + XCH_AT(w) + n * 1024 + l * 16) = ac[1][n];
#pragma unroll
      for (int n = 0; n < 4; ++n) { v[2 * n] += f32x2{ac[0][n][0], ac[0][n][1]}; v[2 * n + 1] += f32x2{ac[0][n][2], ac[0][n][3]}; }
      BAR();
#pragma unroll
      for (int n = 0; n < 4; ++n) {
        const f32x4 o = *(const f32x4*)(smem + XCH_AT(w ^ 4) + n * 1024 + l * 16);
        v[2 * n] += f32x2{o[0], o[1]}; v[2 * n + 1] += f32x2{o[2], o[3]};
      }
    }
    {
      if (it == 0) { st16(pack8v(v), rs, to, OFF_XN + (4u << 20)); st16(pack8v(v + 4), rs, to + 2048, OFF_XN + (4u << 20)); }
      SB();
      const f32x2 s = ((v[0] + v[1]) + (v[2] + v[3])) + ((v[4] + v[5]) + (v[6] + v[7]));
      f32x2 dp = v[0] * wv[0], q = v[0] * v[0];
#pragma unroll
      for (int j = 1; j < 8; ++j) { dp = __builtin_elementwise_fma(v[j], wv[j], dp); q = __builtin_elementwise_fma(v[j], v[j], q); }
      float mt = hadd(s), qq = hadd(q), dot = hadd(dp);
      add_g3(mt, qq, dot);
      const unsigned so = OFF_STAT + it * 524288 + rb * 32768, lrow = R & 127;
      if (it == 0) wait_vm<2>();
      if (g == 0) st16(unpoison4(__builtin_bit_cast(u32x4, f32x4{mt, qq, dot, 0.f})), rs, ct * 2048 + lrow * 16, so);
      SB();
      if (it == 0) wait_vm<1>();
      if constexpr (it == 0) {
        volatile LDS_AS u32x2* in = (volatile LDS_AS u32x2*)(smem + L_XC);
        if (l == 0) in[w] = T;
        const u32x2 t = in[l & 7];
        if (__all(t[0] == T[0] && t[1] == T[1]) && l < (int)RX1) st8(T, rs, (l * 32 + (rb & 1) * 16 + ct) * SX1, F_X1 + (rb >> 1) * RX1 * 32 * SX1);
      }
      STAMPAT(8);
      if (ffn_on) { WD_DMA(); }
      if (it == 0) {
#pragma unroll
        for (int j = 0; j < 9; ++j) *(u32x4*)(smem + L_RING + (j / 3) * STAGE + (w * 3 + j % 3) * 1024 + l * 16) = keep[j];
      }
      STAMPAT(16);
      if constexpr (it == 0) {
        if (w == 0) poll_flags(rs, ((l & 2) * 8 + ((ROT0 + 5) & 7) * 2 + (l & 1)) * SX1, F_X1 + (p * RX1 + (xcd >> 1) % RX1) * 32 * SX1, T);
        if (tid == 192) *(LDS_AS u32x4*)(smem + L_QC) = u32x4{0u, 0u, 0u, 0u};
        BAR();
        PRE_LOAD(1, ROT0 + 5, 0, 4);
        STAMPAT(9);
      } else {
        __builtin_amdgcn_s_sleep(SDIR);
        u32x4 sp[4];
#pragma unroll
        for (int i = 0; i < 4; ++i) sp[i] = ld16(rs, (4 * g + i) * 2048 + lrow * 16, so);
        const RowFin r = fin_key(rs, T, g * 8192 + lrow * 16, so, smem + L_XC, l, w, R, wsum, sp);
        STAMPAT(9);
        BAR();
        fin_cand(rs, smem + L_XC, 1, g, rb, ct, r, v);
        STAMPAT(10);
        fin_x2(r, v);
      }
    }
    STAMPAT(11);
    if (it == 1) {
      const u32x4 op0 = trade16(pack8v(v)), op1 = trade16(pack8v(v + 4));
#define OUT_STORE()                                                                                 \
  if (R != key_row(key)) {                                                                          \
    __builtin_amdgcn_raw_buffer_store_b128(op0, rout, (R * 1024 + rmo) * 2, 0, SC1);                  \
    __builtin_amdgcn_raw_buffer_store_b128(op1, rout, (R * 1024 + rmo + 32) * 2, 0, SC1);             \
  }
      u32x4 xv[4];
      unsigned guess = 0;
      {
        __builtin_amdgcn_s_sleep(GSL);
        const unsigned k_ = key_load(rs, l, 1, KREP());
        guess = ffn_xoff(__builtin_amdgcn_readfirstlane(umax16(k_ == POISON ? 0u : k_)), 1, w);
        ld16h<0>(xv[0], rs, g * 16, guess); ld16h<64>(xv[1], rs, g * 16, guess); ld16h<128>(xv[2], rs, g * 16, guess); ld16h<192>(xv[3], rs, g * 16, guess);
      }
      if (w == 0) {
        const unsigned k_ = key_wait(rs, T, l, 1, KREP(), key_load(rs, l, 1, KREP()));
        if (l == 0) *(LDS_AS unsigned*)(smem + L_SKEY + 32) = k_;
      }
      DRAIN();
      PINV4(xv);
      BAR();
      const unsigned key = __builtin_amdgcn_readfirstlane(*(LDS_AS unsigned*)(smem + L_SKEY + 32));
      STAMPAT(12);
      if (ffn_on) {
        if (guess != ffn_xoff(key, 1, w)) {
#pragma unroll
          for (int i = 0; i < 4; ++i) xv[i] = ld16(rs, g * 16 + i * 64, ffn_xoff(key, 1, w));
        }
        f32x4 ga, ua;
        ffn_gu(gv, uv, xv, ga, ua);
        ffn_gu_check(rs, T, ffn_xoff(key, 1, w), l, gv, uv, xv, ga, ua);
        ffn_gu_store(smem, l, w, ga, ua);
        BAR();
        f32x4 t, ya[2] = {{0.f, 0.f, 0.f, 0.f}, {0.f, 0.f, 0.f, 0.f}};
        s16x4 dfr[8];
        ffn_d_read(smem, l, w, t, dfr);
#pragma unroll
        for (int j = 0; j < 4; ++j) t[j] = ffn_d_h(ffn_d_sum(t[j]));
        const u32x2 hh = {pack2(t[0], t[1]), pack2(t[2], t[3])};
#pragma unroll
        for (int i = 0; i < 8; ++i) ya[i & 1] = ffn_d_mma(l, i, dfr[i], hh, ya[i & 1]);
        ffn_d_store(rs, l, w, 1, slice, ya[0] + ya[1]);
        SB();
        OUT_STORE();
        STAMPAT(13);
      } else {
        OUT_STORE();
        const unsigned G = xcd * 4 + bi - 28;
        if (G == 0 && tid == 0) st8(T, rs, F_EP, 0);
        u32x4 pv[4];
        u32x2 cxn, cx1;
        __builtin_amdgcn_s_sleep(RSL);
        red_load(rs, tid, 1, G, key, pv, cxn, cx1);
        red_sum(rs, smem, T, tid, w, 1, G, key, pv, cxn, cx1);
        BAR();
        if (w == 0) {
          const u32x2 ov = red_final(smem, l, key, cxn, cx1);
          if (l < 8) __builtin_amdgcn_raw_buffer_store_b64(ov, rout, (key_row(key) * 1024 + G * 32 + l * 4) * 2, 0, 0);
        }
      }
#undef OUT_STORE
    }
    STAMPAT(15); STAMPAT(23);
  };
  layer(std::integral_constant<int, 0>{});
  layer(std::integral_constant<int, 1>{});
}

extern "C" void kernel_launch(void* const* d_in, const int* in_sizes, int n_in, void* d_out, int out_size,
                              void* d_ws, size_t ws_size, hipStream_t stream) {
  (void)hipFuncSetAttribute((const void*)mega_kernel, hipFuncAttributeMaxDynamicSharedMemorySize, LDS_BYTES);
  mega_kernel<<<256, 512, LDS_BYTES, stream>>>((const bf16*)d_in[0], (char*)d_ws, (const bf16*)d_in[1], (const bf16*)d_in[2], (const bf16*)d_in[4],
                                               (const bf16*)d_in[5], (const bf16*)d_in[6], (const bf16*)d_in[7], (bf16*)d_out);
}
```

```cpp
#include <hip/hip_runtime.h>
#include <stdint.h>
#include <utility>

typedef __bf16 bf16;
typedef __bf16 bf16x8 __attribute__((ext_vector_type(8)));
typedef __bf16 bf16x2 __attribute__((ext_vector_type(2)));
typedef float f32x4 __attribute__((ext_vector_type(4)));
typedef float f32x2 __attribute__((ext_vector_type(2)));
typedef short s16x4 __attribute__((ext_vector_type(4)));
typedef unsigned int u32x4 __attribute__((ext_vector_type(4)));
typedef unsigned int u32x2 __attribute__((ext_vector_type(2)));
#define LDS_AS __attribute__((address_space(3)))
#define GAS __attribute__((address_space(1)))
typedef GAS unsigned gu32;
typedef GAS unsigned long long gu64;
#define RLX_AGENT __ATOMIC_RELAXED, __HIP_MEMORY_SCOPE_AGENT
typedef __amdgpu_buffer_rsrc_t rsrc_t;

constexpr int F_ = 3584;
constexpr int SC1 = 16;

constexpr int RX = 4;
constexpr unsigned F_EP = 0, F_TMO = 128;
constexpr unsigned F_XN = 4096;
#define FLG 256, 1024, 8, 64, 4, 256, 4
constexpr unsigned FLV[7] = {FLG};
constexpr unsigned SO = FLV[0], SKV = FLV[1], SKW = FLV[2], SX1 = FLV[3], RX1 = FLV[4], SKEY = FLV[5], RKEY = FLV[6];
constexpr unsigned F_O = 8u << 20;
constexpr unsigned F_KV = 11u << 20;
constexpr unsigned F_X1 = 9u << 20;
constexpr unsigned OFF_KEY = 10u << 20;
#define KREP() (RKEY == 16 ? bid & 15 : (bid & 7) * RKEY / 8)
#define KEY_AT(L, rep) (OFF_KEY + ((L) * RKEY + (rep)) * (SKEY < 8 ? 128 : 16 * SKEY))
constexpr unsigned OFF_AB = 2u << 20;
constexpr unsigned OFF_XNEW = 262144;
constexpr unsigned OFF_CS = OFF_AB + 65536;
constexpr unsigned OFF_FX = 393216;
constexpr unsigned OFF_CA = 524288;
constexpr unsigned OFF_STAT = 1u << 20;
constexpr unsigned OFF_PART = 3u << 20;
constexpr unsigned OFF_STAMP = 6u << 20;
constexpr unsigned OFF_XN = 8u << 20;
constexpr unsigned OFF_O = 16u << 20;
constexpr unsigned OFF_KV = 32u << 20;
constexpr unsigned OFF_WI = 52u << 20;
constexpr unsigned WS_BYTES = 58u << 20;
constexpr unsigned POISON = 0xFFFFFFFFu;

constexpr int L_ABW = 16640, L_CSP = 17408, L_CSL = 31872, L_FXL = 32256, L_K = 0, L_V = 32768, L_RING = 65536, L_XCH = 0, L_DN = 32768, L_RDY = 31744, L_QC = 31808, LDS_BYTES = 163840;
constexpr int STAGE = 24576;
#define SLOT(t) ((t) & 3)

template <typename F, int... I> __device__ __forceinline__ void static_for_impl(F&& f, std::integer_sequence<int, I...>) {
  (f(std::integral_constant<int, I>{}), ...);
}
template <int N, typename F> __device__ __forceinline__ void static_for(F&& f) { static_for_impl(f, std::make_integer_sequence<int, N>{}); }
__device__ __forceinline__ unsigned pack2(float a, float b) {
  f32x2 v = {a, b};
  return __builtin_bit_cast(unsigned, __builtin_convertvector(v, bf16x2));
}
__device__ __forceinline__ float asf(unsigned u) { return __builtin_bit_cast(float, u); }
__device__ __forceinline__ float lo16(unsigned u) { return __builtin_bit_cast(float, u << 16); }
__device__ __forceinline__ float hi16(unsigned u) { return __builtin_bit_cast(float, u & 0xffff0000u); }
__device__ __forceinline__ u32x4 pack8(const f32x4 a, const f32x4 b) {
  u32x4 o;
  o[0] = pack2(a[0], a[1]); o[1] = pack2(a[2], a[3]); o[2] = pack2(b[0], b[1]); o[3] = pack2(b[2], b[3]);
  return o;
}
__device__ __forceinline__ f32x4 mfma16(u32x4 a, u32x4 b, f32x4 c) {
  return __builtin_amdgcn_mfma_f32_16x16x32_bf16(__builtin_bit_cast(bf16x8, a), __builtin_bit_cast(bf16x8, b), c, 0, 0, 0);
}
__device__ __forceinline__ f32x4 mfma16k16(s16x4 a, s16x4 b, f32x4 c) { return __builtin_amdgcn_mfma_f32_16x16x16bf16_1k(a, b, c, 0, 0, 0); }
template <int CTRL> __device__ __forceinline__ float dppf(float v) {
  return __builtin_bit_cast(float, __builtin_amdgcn_update_dpp(0, __builtin_bit_cast(int, v), CTRL, 0xf, 0xf, false));
}
template <int CTRL> __device__ __forceinline__ unsigned dppu(unsigned v) {
  return (unsigned)__builtin_amdgcn_update_dpp(0, (int)v, CTRL, 0xf, 0xf, false);
}
__device__ __forceinline__ float add_x16(float v) {
  auto r = __builtin_amdgcn_permlane16_swap(__builtin_bit_cast(unsigned, v), __builtin_bit_cast(unsigned, v), false, false);
  return __builtin_bit_cast(float, (unsigned)r[0]) + __builtin_bit_cast(float, (unsigned)r[1]);
}
__device__ __forceinline__ float add_x32(float v) {
  auto r = __builtin_amdgcn_permlane32_swap(__builtin_bit_cast(unsigned, v), __builtin_bit_cast(unsigned, v), false, false);
  return __builtin_bit_cast(float, (unsigned)r[0]) + __builtin_bit_cast(float, (unsigned)r[1]);
}
__device__ __forceinline__ float max_x16(float v) {
  auto r = __builtin_amdgcn_permlane16_swap(__builtin_bit_cast(unsigned, v), __builtin_bit_cast(unsigned, v), false, false);
  return fmaxf(__builtin_bit_cast(float, (unsigned)r[0]), __builtin_bit_cast(float, (unsigned)r[1]));
}
__device__ __forceinline__ float max_x32(float v) {
  auto r = __builtin_amdgcn_permlane32_swap(__builtin_bit_cast(unsigned, v), __builtin_bit_cast(unsigned, v), false, false);
  return fmaxf(__builtin_bit_cast(float, (unsigned)r[0]), __builtin_bit_cast(float, (unsigned)r[1]));
}
__device__ __forceinline__ unsigned umax_x16(unsigned v) {
  auto r = __builtin_amdgcn_permlane16_swap(v, v, false, false);
  return max((unsigned)r[0], (unsigned)r[1]);
}
__device__ __forceinline__ unsigned umax_x32(unsigned v) {
  auto r = __builtin_amdgcn_permlane32_swap(v, v, false, false);
  return max((unsigned)r[0], (unsigned)r[1]);
}
__device__ __forceinline__ float sum_hi8(float v) {
  v += dppf<0x128>(v);
  v = add_x16(v);
  return add_x32(v);
}
__device__ __forceinline__ float sum64(float v) {
  v += dppf<0xB1>(v);
  v += dppf<0x4E>(v);
  v += dppf<0x124>(v);
  v += dppf<0x128>(v);
  v = add_x16(v);
  return add_x32(v);
}

#define BAR() asm volatile("s_waitcnt lgkmcnt(0)\n\ts_barrier" ::: "memory")
#define DRAIN() asm volatile("s_waitcnt vmcnt(0)" ::: "memory")
#define SB() __builtin_amdgcn_sched_barrier(0)
template <int N> __device__ __forceinline__ void wait_vm() { asm volatile("s_waitcnt vmcnt(%0)" ::"n"(N) : "memory"); }

__device__ __forceinline__ void dma16(rsrc_t rs, unsigned voff, unsigned soff, unsigned lds) {
  asm volatile("s_mov_b32 m0, %0\n\ts_nop 0\n\tbuffer_load_dwordx4 %1, %2, %3 offen lds" ::"s"(lds), "v"(voff), "s"(rs), "s"(soff) : "memory");
}
template <int OFF> __device__ __forceinline__ void dma16s(rsrc_t rs, unsigned voff, unsigned soff, unsigned lds) {
  asm volatile("s_mov_b32 m0, %0\n\ts_nop 0\n\tbuffer_load_dwordx4 %1, %2, %3 offen offset:%4 sc1 lds" ::"s"(lds), "v"(voff), "s"(rs), "s"(soff), "n"(OFF) : "memory");
}
__device__ __forceinline__ float rbf1(float x) { return __builtin_bit_cast(float, pack2(0.f, x)); }
__device__ __forceinline__ void vswap(unsigned& a, unsigned& b) { asm volatile("v_swap_b32 %0, %1" : "+v"(a), "+v"(b)); }
__device__ __forceinline__ void vswap(float& a, float& b) { asm volatile("v_swap_b32 %0, %1" : "+v"(a), "+v"(b)); }
template <typename V, int N> __device__ __forceinline__ void vswapn(V& a, V& b) {
#pragma unroll
  for (int i = 0; i < N; ++i) { auto x = a[i], y = b[i]; vswap(x, y); a[i] = x; b[i] = y; }
}
__device__ __forceinline__ u32x4 ld16(rsrc_t rs, unsigned voff, unsigned soff) { return __builtin_amdgcn_raw_buffer_load_b128(rs, voff, soff, SC1); }
template <int OFF> __device__ __forceinline__ void ld16h(u32x4& d, rsrc_t rs, unsigned voff, unsigned soff) {
  asm volatile("buffer_load_dwordx4 %0, %1, %2, %3 offen offset:%4 sc1" : "=v"(d) : "v"(voff), "s"(rs), "s"(soff), "n"(OFF) : "memory");
}
template <int I, int OFF> __device__ __forceinline__ void ld16w(u32x4& d, rsrc_t rs, unsigned voff, unsigned soff) {
  if constexpr (I == 0) asm volatile("buffer_load_dwordx4 %0, %1, %2, %3 offen offset:%4 sc1" : "={v[128:131]}"(d) : "v"(voff), "s"(rs), "s"(soff), "n"(OFF) : "memory");
  else if constexpr (I == 1) asm volatile("buffer_load_dwordx4 %0, %1, %2, %3 offen offset:%4 sc1" : "={v[132:135]}"(d) : "v"(voff), "s"(rs), "s"(soff), "n"(OFF) : "memory");
  else if constexpr (I == 2) asm volatile("buffer_load_dwordx4 %0, %1, %2, %3 offen offset:%4 sc1" : "={v[136:139]}"(d) : "v"(voff), "s"(rs), "s"(soff), "n"(OFF) : "memory");
  else if constexpr (I == 3) asm volatile("buffer_load_dwordx4 %0, %1, %2, %3 offen offset:%4 sc1" : "={v[140:143]}"(d) : "v"(voff), "s"(rs), "s"(soff), "n"(OFF) : "memory");
  else if constexpr (I == 4) asm volatile("buffer_load_dwordx4 %0, %1, %2, %3 offen offset:%4 sc1" : "={v[144:147]}"(d) : "v"(voff), "s"(rs), "s"(soff), "n"(OFF) : "memory");
  else if constexpr (I == 5) asm volatile("buffer_load_dwordx4 %0, %1, %2, %3 offen offset:%4 sc1" : "={v[148:151]}"(d) : "v"(voff), "s"(rs), "s"(soff), "n"(OFF) : "memory");
  else if constexpr (I == 6) asm volatile("buffer_load_dwordx4 %0, %1, %2, %3 offen offset:%4 sc1" : "={v[152:155]}"(d) : "v"(voff), "s"(rs), "s"(soff), "n"(OFF) : "memory");
  else if constexpr (I == 7) asm volatile("buffer_load_dwordx4 %0, %1, %2, %3 offen offset:%4 sc1" : "={v[156:159]}"(d) : "v"(voff), "s"(rs), "s"(soff), "n"(OFF) : "memory");
  else if constexpr (I == 8) asm volatile("buffer_load_dwordx4 %0, %1, %2, %3 offen offset:%4 sc1" : "={v[160:163]}"(d) : "v"(voff), "s"(rs), "s"(soff), "n"(OFF) : "memory");
  else if constexpr (I == 9) asm volatile("buffer_load_dwordx4 %0, %1, %2, %3 offen offset:%4 sc1" : "={v[164:167]}"(d) : "v"(voff), "s"(rs), "s"(soff), "n"(OFF) : "memory");
  else if constexpr (I == 10) asm volatile("buffer_load_dwordx4 %0, %1, %2, %3 offen offset:%4 sc1" : "={v[168:171]}"(d) : "v"(voff), "s"(rs), "s"(soff), "n"(OFF) : "memory");
  else if constexpr (I == 11) asm volatile("buffer_load_dwordx4 %0, %1, %2, %3 offen offset:%4 sc1" : "={v[172:175]}"(d) : "v"(voff), "s"(rs), "s"(soff), "n"(OFF) : "memory");
}
template <int K> __device__ __forceinline__ void pin_w(u32x4& a, u32x4& b) {
  if constexpr (K == 0) asm volatile("" : "+{v[128:131]}"(a), "+{v[132:135]}"(b)::"memory");
  else if constexpr (K == 1) asm volatile("" : "+{v[136:139]}"(a), "+{v[140:143]}"(b)::"memory");
  else if constexpr (K == 2) asm volatile("" : "+{v[144:147]}"(a), "+{v[148:151]}"(b)::"memory");
  else if constexpr (K == 3) asm volatile("" : "+{v[152:155]}"(a), "+{v[156:159]}"(b)::"memory");
  else if constexpr (K == 4) asm volatile("" : "+{v[160:163]}"(a), "+{v[164:167]}"(b)::"memory");
  else if constexpr (K == 5) asm volatile("" : "+{v[168:171]}"(a), "+{v[172:175]}"(b)::"memory");
}
__device__ __forceinline__ void ld8h(u32x2& d, rsrc_t rs, unsigned voff, unsigned soff) {
  asm volatile("buffer_load_dwordx2 %0, %1, %2, %3 offen sc1" : "=v"(d) : "v"(voff), "s"(rs), "s"(soff) : "memory");
}
template <int OFF> __device__ __forceinline__ void ld4h(unsigned& d, rsrc_t rs, unsigned voff, unsigned soff) {
  asm volatile("buffer_load_dword %0, %1, %2, %3 offen offset:%4 sc1" : "=v"(d) : "v"(voff), "s"(rs), "s"(soff), "n"(OFF) : "memory");
}
__device__ __forceinline__ u32x2 ld8(rsrc_t rs, unsigned voff, unsigned soff) { return __builtin_amdgcn_raw_buffer_load_b64(rs, voff, soff, SC1); }
__device__ __forceinline__ void st16(u32x4 v, rsrc_t rs, unsigned voff, unsigned soff) { __builtin_amdgcn_raw_buffer_store_b128(v, rs, voff + soff, 0, SC1); }
__device__ __forceinline__ void st8(u32x2 v, rsrc_t rs, unsigned voff, unsigned soff) { __builtin_amdgcn_raw_buffer_store_b64(v, rs, voff, soff, SC1); }

__device__ __forceinline__ unsigned ld4(rsrc_t rs, unsigned voff, unsigned soff) { return __builtin_amdgcn_raw_buffer_load_b32(rs, voff, soff, SC1); }
__device__ __forceinline__ void st4(unsigned v, rsrc_t rs, unsigned voff, unsigned soff) { __builtin_amdgcn_raw_buffer_store_b32(v, rs, voff, soff, SC1); }

constexpr unsigned kSpinCap = 1u << 16;
__device__ __forceinline__ bool spin_fail(unsigned& spins, rsrc_t rs, u32x2 T) {
  asm volatile("" ::: "memory");
  ++spins;
  if (spins > kSpinCap) { st8(T, rs, 0, F_TMO); return true; }
  if ((spins & 255u) == 0u) {
    const u32x2 v = ld8(rs, 0, F_TMO);
    if (v[0] == T[0] && v[1] == T[1]) return true;
  }
  __builtin_amdgcn_s_sleep(3);
  return false;
}
__device__ __forceinline__ void poll_flags2(rsrc_t rs, unsigned voff, u32x2 T) {
  for (unsigned spins = 0;;) {
    const u32x4 v = ld16(rs, voff, 0);
    if (__all(v[0] == T[0] && v[1] == T[1] && v[2] == T[0] && v[3] == T[1])) break;
    if (spin_fail(spins, rs, T)) break;
  }
  asm volatile("" ::: "memory");
  __builtin_amdgcn_fence(__ATOMIC_ACQUIRE, "wavefront");
}
__device__ __forceinline__ void tag_wait(rsrc_t rs, u32x2 T, unsigned voff, unsigned soff, u32x4& v) {
  if (__builtin_expect(!__all(v[2] == T[0] && v[3] == T[1]), 0)) {
    unsigned spins = 0;
    do {
      if (spin_fail(spins, rs, T)) break;
      v = ld16(rs, voff, soff);
    } while (!__all(v[2] == T[0] && v[3] == T[1]));
    asm volatile("s_waitcnt vmcnt(0)" : "+v"(v)::"memory");
  }
}
template <int N> __device__ __forceinline__ void word_wait(rsrc_t rs, u32x2 T, unsigned voff, unsigned soff, u32x4 (&v)[4]) {
#define WW_OK() __all(max(max(max(v[0][0], v[0][1]), max(v[0][2], v[0][3])), max(max(v[N - 1][0], v[N - 1][1]), max(v[N - 1][2], v[N - 1][3]))) != POISON)
  if (__builtin_expect(!WW_OK(), 0)) {
    unsigned spins = 0;
    do {
      if (spin_fail(spins, rs, T)) break;
#pragma unroll
      for (int i = 0; i < N; ++i) v[i] = ld16(rs, voff + i * 2048, soff);
    } while (!WW_OK());
    asm volatile("s_waitcnt vmcnt(0)" : "+v"(v[0]), "+v"(v[N - 1])::"memory");
  }
#undef WW_OK
}
__device__ __forceinline__ void poll_flags(rsrc_t rs, unsigned voff, unsigned soff, u32x2 T) {
  for (unsigned spins = 0;;) {
    const u32x2 v = ld8(rs, voff, soff);
    if (__all(v[0] == T[0] && v[1] == T[1])) break;
    if (spin_fail(spins, rs, T)) break;
  }
  asm volatile("" ::: "memory");
  __builtin_amdgcn_fence(__ATOMIC_ACQUIRE, "wavefront");
}

#define STAMP_ON(i) true
#define STAMP_VAL(i) (((i) == 23 || (i) == 22 || (i) == 21) ? __builtin_amdgcn_s_memtime() : __builtin_amdgcn_s_memrealtime())
#define STAMPAT(i)

#define GSTEP(s, n)
#define VSTEP(i)

__device__ __forceinline__ unsigned toff(unsigned R, unsigned ks, unsigned g) {
  return (((R >> 5) * 64 + ks * 2 + ((R >> 4) & 1)) << 10) + ((g * 16 + (R & 15)) << 4);
}
__device__ __forceinline__ void unpack8v(const u32x4 u, f32x2* v) {
#pragma unroll
  for (int j = 0; j < 4; ++j) v[j] = f32x2{lo16(u[j]), hi16(u[j])};
}
__device__ __forceinline__ u32x4 pack8v(const f32x2* v) {
  u32x4 o;
#pragma unroll
  for (int j = 0; j < 4; ++j) o[j] = pack2(v[j][0], v[j][1]);
  return o;
}
__device__ __forceinline__ f32x2 rbf1v(f32x2 v) { return f32x2{rbf1(v[0]), rbf1(v[1])}; }
__device__ __forceinline__ float ln_rd(float var) { return __builtin_amdgcn_rcpf(rbf1(__builtin_amdgcn_sqrtf(rbf1(rbf1(var) + 1e-5f)))); }
template <int N> __device__ __forceinline__ void ln_apply(float mb, float rd, const f32x2* v, f32x2* y) {
  const f32x2 mb2 = {mb, mb}, rd2 = {rd, rd};
#pragma unroll
  for (int j = 0; j < N; ++j) y[j] = rbf1v(rbf1v(v[j] - mb2) * rd2);
}
__device__ __forceinline__ float hadd(f32x2 v) {
  float a = v[0];
  asm volatile("" : "+v"(a));
  return a + v[1];
}
__device__ __forceinline__ float add_g(float v) { return add_x32(add_x16(v)); }
__device__ __forceinline__ void add_g3(float& x, float& y, float& z) {
  auto a = __builtin_amdgcn_permlane16_swap(__builtin_bit_cast(unsigned, x), __builtin_bit_cast(unsigned, y), false, false);
  auto b = __builtin_amdgcn_permlane16_swap(__builtin_bit_cast(unsigned, z), __builtin_bit_cast(unsigned, z), false, false);
  const float r = asf(a[0]) + asf(a[1]), t = asf(b[0]) + asf(b[1]);
  auto c = __builtin_amdgcn_permlane32_swap(__builtin_bit_cast(unsigned, r), __builtin_bit_cast(unsigned, r), false, false);
  auto d = __builtin_amdgcn_permlane32_swap(__builtin_bit_cast(unsigned, t), __builtin_bit_cast(unsigned, t), false, false);
  const float u = asf(c[0]) + asf(c[1]);
  z = asf(d[0]) + asf(d[1]);
  auto e = __builtin_amdgcn_permlane16_swap(__builtin_bit_cast(unsigned, u), __builtin_bit_cast(unsigned, u), false, false);
  x = asf(e[0]); y = asf(e[1]);
}
__device__ __forceinline__ u32x4 trade16(u32x4 v) {
  auto a = __builtin_amdgcn_permlane16_swap(v[0], v[2], false, false);
  auto b = __builtin_amdgcn_permlane16_swap(v[1], v[3], false, false);
  return u32x4{(unsigned)a[0], (unsigned)b[0], (unsigned)a[1], (unsigned)b[1]};
}
__device__ __forceinline__ unsigned umax4(u32x4 v) { return max(max(v[0], v[1]), max(v[2], v[3])); }
__device__ __forceinline__ u32x2 unpoison2(u32x2 v) { return u32x2{min(v[0], POISON - 1), min(v[1], POISON - 1)}; }
__device__ __forceinline__ u32x4 unpoison4(u32x4 v) {
  return u32x4{min(v[0], POISON - 1), min(v[1], POISON - 1), min(v[2], POISON - 1), min(v[3], POISON - 1)};
}

typedef union { struct { s16x4 a, b; } hf; u32x4 v; } frag_t;
__device__ __forceinline__ s16x4 trread(const char* smem, unsigned off) {
  return __builtin_amdgcn_ds_read_tr16_b64_v4i16((LDS_AS s16x4*)(smem + off));
}
__device__ __forceinline__ s16x4 trabs(unsigned addr) { return __builtin_amdgcn_ds_read_tr16_b64_v4i16((LDS_AS s16x4*)(size_t)addr); }

constexpr int WA = 6;
constexpr int WB = 8, WR = 4;
#define CH 0, 7, 9, 10, 15, 17, 23, 25, 29, 31, 5, 4
constexpr int CHV[12] = {CH};
constexpr int FKL = CHV[10], FSN = CHV[11];
constexpr int SPL = CHV[0];
constexpr int FSL = CHV[1], FS0 = CHV[2], FS0D = CHV[3], FS1 = CHV[4], FS1S = CHV[5];
constexpr int FXA = CHV[6], FXB = CHV[7], FXL = CHV[8], FXS = CHV[9];
#define L0S 22, 25, 8, 0, 24, 28, 1, 2, 4, 14
constexpr int L0V[10] = {L0S};
constexpr int CSL = L0V[0], CSS = L0V[1], CSD = L0V[2];
constexpr int abl(int it) { return it ? 25 : L0V[4]; }
constexpr int abs_(int it) { return it ? 27 : L0V[5]; }
constexpr int W2S = L0V[6], WSH = L0V[7];
constexpr int LNL = L0V[3], LNS = L0V[8], POI = L0V[9];
#define SYNCB 3
#define SPINS 0
#define KVJ 1
#define KVD 2
#define PC2 1
#define PCE 1
#define ROTH 0
#define ROT0 (p + ROTH * h)
#define TCH 40, 6, 0, 26, 0xFF, 0, 1
constexpr int TCV[7] = {TCH};
constexpr int TS0 = TCV[0], TSN = TCV[1], TM0 = TCV[2], TEX = TCV[3], TM1 = TCV[4], TSD = TCV[5], TSG = TCV[6];
constexpr int QL = 26;
#define SLP 18, 4, 28, 3, 0, 0
constexpr int SLV[6] = {SLP};
constexpr int OUTE = SLV[4], OSL = SLV[5];
constexpr int SDIR = SLV[0], GSL = SLV[1], RSL = SLV[2], KSL = SLV[3];
constexpr int tp0(int it) { return it ? 3 : 3 + WSH; }
constexpr bool tile_piece(int it, int s) { return s >= tp0(it) && ((s - tp0(it)) >> 2) + 3 < 8 && ((s - tp0(it)) & 3) < 3; }
constexpr int a_req(int s, int n) {
  if (s < 1 || !(n & 1) || (s == 1 && n == 1)) return -1;
  if (s <= 5) return 8 + 3 * s + (n >> 1) - 4;
  return n < 5 && s + 5 < 32 ? 2 * (s + 5) + (n >> 1) : -1;
}
constexpr int vm_n(int it, int s, int n) {
  int c = it ? (a_req(s, n) >= 0) + (s == 0 && n == 0) : (s + 5 < 32 && (n == 2 || n == 3));
  if (n == 0) c += tile_piece(it, s);
  if (n == 4) c += (s == abl(it));
  if (!it) c += (s >= W2S && s < W2S + 3 && n == 4) + (s == LNL && n == 5 ? 3 : 0) + (s == LNS + 2 && n == 5) + (s == TEX && n == 5);
  else c += (s == 2 && n == 2) + (s == (SPL < 0 ? 0 : SPL) && n == (SPL < 0 ? 1 : 4) ? 4 : 0) + (s >= 27 && s < 30 && (n == 0 || n == 3));
  return c;
}
constexpr int vm_at(int it, int s, int n) {
  int c = it ? 8 : 16;
  for (int u = 0; u < 6 * s + n; ++u) c += vm_n(it, u / 6, u % 6);
  return c;
}
constexpr int vm_a(int it, int k) {
  if (k < 4) return 2 * k + 2;
  for (int u = 0; u < 192; ++u)
    if (a_req(u / 6, u % 6) == 2 * k + 1) return vm_at(it, (u + 1) / 6, (u + 1) % 6);
  return 0;
}
constexpr int vm_tile(int it, int t) { return t >= 3 ? vm_at(it, 4 * t - 10 + tp0(it), 1) : t == 1 ? 12 : vm_at(0, W2S + 2, 5); }
constexpr int vm_unit(int u) { return u < 3 ? 5 + 2 * u : u < 5 ? 8 + 2 * u : vm_at(0, u - 5, 4); }
constexpr int unit_slot(int u) { return u % 3 == 0 ? 0 : 16384 + (u % 3) * 16384; }

constexpr int L_XC = LDS_BYTES - 64, L_SKEY = 32704, L_ROW = 1024, L_OUT = 65536, L_SRED = 4096, L_SFIN = 20480;
__device__ __forceinline__ unsigned key_load(rsrc_t rs, int l, int L, int rep) { return ld4(rs, (l & 15) * SKEY, KEY_AT(L, rep)); }
__device__ __forceinline__ unsigned umax16(unsigned k) {
  k = max(k, dppu<0xB1>(k));
  k = max(k, dppu<0x4E>(k));
  k = max(k, dppu<0x141>(k));
  return max(k, dppu<0x140>(k));
}
__device__ __forceinline__ unsigned key_row(unsigned key) { return (0xffffu - (key & 0xffffu)) & 2047u; }
__device__ __forceinline__ unsigned key_wait(rsrc_t rs, u32x2 T, int l, int L, int rep, unsigned kv) {
  if (!__all(kv != POISON)) {
    unsigned spins = 0;
    do {
      if (spin_fail(spins, rs, T)) break;
      if (L == 0) __builtin_amdgcn_s_sleep(KSL);
      kv = key_load(rs, l, L, rep);
    } while (!__all(kv != POISON));
  }
  return __builtin_amdgcn_readfirstlane(umax16(kv));
}
__device__ __forceinline__ unsigned cand_off(unsigned key, int L) { return OFF_CA + L * 65536 + (key_row(key) >> 7) * 4096; }
#define XO(i) (((i) >> 1) * 256 + ((i) & 1) * 64)
template <int S1, int S2> __device__ __forceinline__ void ffn_xwait(rsrc_t rs, u32x2 T, unsigned xo, int l, u32x4 (&xv)[4]) {
#define XV_OK() __all(max(max(umax4(xv[0]), umax4(xv[1])), max(umax4(xv[2]), umax4(xv[3]))) != POISON)
  if (!XV_OK()) {
    unsigned spins = 0;
    do {
      if (spin_fail(spins, rs, T)) break;
#pragma unroll
      for (int i = 0; i < 4; ++i) xv[i] = ld16(rs, (l >> 4) * 16 + (i & 1) * S1 + (i >> 1) * S2, xo);
    } while (!XV_OK());
  }
#undef XV_OK
}
__device__ __forceinline__ void ffn_gu(const u32x4 (&gv)[4], const u32x4 (&uv)[4], const u32x4 (&xv)[4], f32x4& ga, f32x4& ua) {
  ga = mfma16(gv[0], xv[0], f32x4{0.f, 0.f, 0.f, 0.f}); ua = mfma16(uv[0], xv[0], f32x4{0.f, 0.f, 0.f, 0.f});
#pragma unroll
  for (int i = 1; i < 4; ++i) { ga = mfma16(gv[i], xv[i], ga); ua = mfma16(uv[i], xv[i], ua); }
}
__device__ __forceinline__ void ffn_gu_check(rsrc_t rs, u32x2 T, unsigned xo, int l, const u32x4 (&gv)[4], const u32x4 (&uv)[4], u32x4 (&xv)[4], f32x4& ga, f32x4& ua) {
  if (__builtin_expect(__any(ga[0] != ga[0]), 0)) {
    ffn_xwait<64, 256>(rs, T, xo, l, xv);
    ffn_gu(gv, uv, xv, ga, ua);
  }
}
__device__ __forceinline__ void ffn_gu_store(char* smem, int l, int w, f32x4 ga, f32x4 ua) {
  LDS_AS f32x4* sred = (LDS_AS f32x4*)(smem + L_SRED);
  if ((l & 15) == 0) { sred[w * 8 + (l >> 4)] = ga; sred[w * 8 + 4 + (l >> 4)] = ua; }
}
__device__ __forceinline__ f32x4 ffn_d_read(char* smem, int l) {
  const LDS_AS float* t = (const LDS_AS float*)(smem + L_SRED) + (l & 8) * 16 + (l & 4) * 4 + (l >> 4) * 4 + (l & 3);
  return f32x4{t[0], t[32], t[64], t[96]};
}
__device__ __forceinline__ u32x2 ffn_d_h(f32x4 t) {
  float x = t[0] + t[1];
  asm volatile("" : "+v"(x));
  x += t[2] + t[3];
  x += dppf<0x128>(x);
  const float y = dppf<0x124>(x);
  const bool up = __builtin_amdgcn_inverse_ballot_w64(0xF0F0F0F0F0F0F0F0ull);
  const float gg = rbf1(up ? y : x), uu = rbf1(up ? x : y);
  const float h = rbf1(gg * __builtin_amdgcn_rcpf(1.0f + __expf(-gg))) * uu;
  const unsigned p = pack2(h, dppf<0xB1>(h));
  return u32x2{dppu<0x00>(p), dppu<0xAA>(p)};
}
template <int t> __device__ __forceinline__ f32x4 ffn_d_mma(s16x4 d, u32x2 hh, f32x4 y) {
  const bool me = __builtin_amdgcn_inverse_ballot_w64(0x0001000100010001ull << t);
  return mfma16k16(d, __builtin_bit_cast(s16x4, u32x2{me ? hh[0] : 0u, me ? hh[1] : 0u}), y);
}
__device__ __forceinline__ f32x4 ffn_d_all(const s16x4 (&dfr)[8], u32x2 hh) {
  f32x4 a = {0.f, 0.f, 0.f, 0.f}, b = a;
  a = ffn_d_mma<0>(dfr[0], hh, a); b = ffn_d_mma<1>(dfr[1], hh, b); a = ffn_d_mma<2>(dfr[2], hh, a); b = ffn_d_mma<3>(dfr[3], hh, b);
  a = ffn_d_mma<4>(dfr[4], hh, a); b = ffn_d_mma<5>(dfr[5], hh, b); a = ffn_d_mma<6>(dfr[6], hh, a); b = ffn_d_mma<7>(dfr[7], hh, b);
  return a + b;
}
__device__ __forceinline__ void ffn_d_store(rsrc_t rs, int l, int w, int L, int slice, f32x4 y) {
  const unsigned c = l & 15;
  if (c < 8) st16(unpoison4(__builtin_bit_cast(u32x4, y)), rs, (2 * w + (c >> 2)) * 65536 + slice * 256 + (c & 3) * 64 + (l >> 4) * 16, OFF_PART + L * (1u << 20));
}
#define RED_VO(k) ((min(tid + 512 * (k), 1791) >> 3) * 256 + (tid & 7) * 16)
#define RED_SO(L, G) (OFF_PART + (L) * (1u << 20) + ((G) >> 1) * 65536 + ((G) & 1) * 128)
#define RED_JJ() ((tid & 3) * 16 + (tid & 4) * 2)
__device__ __forceinline__ void red_load(rsrc_t rs, int tid, int L, unsigned G, unsigned key, u32x4 (&pv)[4], u32x2& cxn, u32x2& cx1) {
#pragma unroll
  for (int k = 0; k < 4; ++k) pv[k] = ld16(rs, RED_VO(k), RED_SO(L, G));
  cxn = ld8(rs, RED_JJ(), cand_off(key, L) + XO(G));
  cx1 = ld8(rs, RED_JJ() + 128, cand_off(key, L) + XO(G));
}
__device__ __forceinline__ void red_sum(rsrc_t rs, char* smem, u32x2 T, int tid, int w, int L, unsigned G, unsigned key,
                                        u32x4 (&pv)[4], u32x2& cxn, u32x2& cx1) {
#define PV_OK() __all(max(max(max(umax4(pv[0]), umax4(pv[1])), max(umax4(pv[2]), umax4(pv[3]))), max(max(cxn[0], cxn[1]), max(cx1[0], cx1[1]))) != POISON)
  if (!PV_OK()) {
    unsigned spins = 0;
    do {
      if (spin_fail(spins, rs, T)) break;
#pragma unroll
      for (int k = 0; k < 4; ++k)
        if (umax4(pv[k]) == POISON) pv[k] = ld16(rs, RED_VO(k), RED_SO(L, G));
      if (max(max(cxn[0], cxn[1]), max(cx1[0], cx1[1])) == POISON) { cxn = ld8(rs, RED_JJ(), cand_off(key, L) + XO(G)); cx1 = ld8(rs, RED_JJ() + 128, cand_off(key, L) + XO(G)); }
    } while (!PV_OK());
  }
#undef PV_OK
  f32x4 s = (__builtin_bit_cast(f32x4, pv[0]) + __builtin_bit_cast(f32x4, pv[1])) + __builtin_bit_cast(f32x4, pv[2]);
  if (w < 4) s += __builtin_bit_cast(f32x4, pv[3]);
  ((LDS_AS f32x4*)(smem + L_SFIN))[tid] = s;
}
__device__ __forceinline__ u32x2 red_final(char* smem, int l, unsigned key, u32x2 cxn, u32x2 cx1) {
  const LDS_AS f32x4* sf = (const LDS_AS f32x4*)(smem + L_SFIN) + l;
  f32x4 s = ((sf[0] + sf[64]) + (sf[128] + sf[192])) + ((sf[256] + sf[320]) + (sf[384] + sf[448]));
#pragma unroll
  for (int j = 0; j < 4; ++j) s[j] = sum_hi8(s[j]);
  const unsigned k16 = key >> 16;
  const float rmax = __builtin_bit_cast(float, ((k16 & 0x8000u) ? (k16 & 0x7fffu) : (~k16 & 0xffffu)) << 16);
  const float sig = rbf1(__builtin_amdgcn_rcpf(1.0f + __expf(-rmax)));
  const float xn4[4] = {lo16(cxn[0]), hi16(cxn[0]), lo16(cxn[1]), hi16(cxn[1])};
  const float x14[4] = {lo16(cx1[0]), hi16(cx1[0]), lo16(cx1[1]), hi16(cx1[1])};
  float o[4];
#pragma unroll
  for (int j = 0; j < 4; ++j) o[j] = x14[j] + rbf1(xn4[j] + rbf1(rbf1(s[j]) * sig));
  return u32x2{pack2(o[0], o[1]), pack2(o[2], o[3])};
}

struct RowFin { float mean, var, mb, rd; unsigned key; };
__device__ __forceinline__ void fin_wait(rsrc_t rs, u32x2 T, unsigned voff, unsigned so, u32x4 (&sp)[4]) {
#define ST_OK() __all(max(max(umax4(sp[0]), umax4(sp[1])), max(umax4(sp[2]), umax4(sp[3]))) != POISON)
  if (!ST_OK()) {
    unsigned spins = 0;
    do {
      if (spin_fail(spins, rs, T)) break;
#pragma unroll
      for (int i = 0; i < 4; ++i) sp[i] = ld16(rs, voff + i * 2048, so);
    } while (!ST_OK());
  }
#undef ST_OK
}
__device__ __forceinline__ RowFin fin_key(rsrc_t rs, u32x2 T, unsigned voff, unsigned so, char* skey, int l, int w, unsigned R, float wsum, u32x4 (&sp)[4]) {
  RowFin r;
#define ST_SUM() ((__builtin_bit_cast(f32x4, sp[0]) + __builtin_bit_cast(f32x4, sp[1])) + (__builtin_bit_cast(f32x4, sp[2]) + __builtin_bit_cast(f32x4, sp[3])))
  f32x4 sf = ST_SUM();
  const float z = (sf[0] + sf[1]) + sf[2];
  if (__any(z != z)) {
    fin_wait(rs, T, voff, so, sp);
    sf = ST_SUM();
  }
#undef ST_SUM
  float s0 = sf[0], s1 = sf[1], s2 = sf[2];
  add_g3(s0, s1, s2);
  r.mean = s0 * (1.0f / 1024.0f);
  r.var = fmaxf(__builtin_fmaf(-r.mean, r.mean, s1 * (1.0f / 1024.0f)), 0.f);
  r.mb = rbf1(r.mean); r.rd = ln_rd(r.var);
  const unsigned rb16 = pack2((s2 - r.mb * wsum) * r.rd, 0.f) & 0xffffu;
  r.key = min((((rb16 & 0x8000u) ? (~rb16 & 0xffffu) : (rb16 | 0x8000u)) << 16) | (0xffffu - R), POISON - 1);
  const unsigned wk = umax16(r.key);
  if (l == 0) ((LDS_AS unsigned*)skey)[w] = wk;
  return r;
}
__device__ __forceinline__ unsigned fin_cand(rsrc_t rs, char* skey, int L, int g, int rb, int ct, const RowFin& r, const f32x2 (&v)[8]) {
  const u32x4 k0 = *(const LDS_AS u32x4*)skey, k1 = *(const LDS_AS u32x4*)(skey + 16);
  const unsigned best = max(umax4(k0), umax4(k1));
  if (r.key == best) {
    const unsigned co = L * 65536 + rb * 4096 + ct * 256 + g * 16;
    f32x2 t[8], rd2 = {r.rd, r.rd}, of2 = {-r.mb * r.rd, -r.mb * r.rd};
    asm volatile("" : "+v"(rd2), "+v"(of2));
#pragma unroll
    for (int j = 0; j < 8; ++j) t[j] = __builtin_elementwise_fma(v[j], rd2, of2);
    st16(unpoison4(pack8v(t)), rs, co, OFF_CA); st16(unpoison4(pack8v(t + 4)), rs, co + 64, OFF_CA);
    if (g == 0 && ct < (int)RKEY) st4(best, rs, KEY_AT(L, ct) + rb * SKEY, 0);
    st16(unpoison4(pack8v(v)), rs, co + 128, OFF_CA); st16(unpoison4(pack8v(v + 4)), rs, co + 192, OFF_CA);
  }
  return key_row(best);
}
__device__ __forceinline__ void fin_x2(const RowFin& r, f32x2 (&v)[8]) {
  f32x2 sc2 = {1.0f + r.rd, 1.0f + r.rd}, of2 = {-r.mb * r.rd, -r.mb * r.rd};
  asm volatile("" : "+v"(sc2), "+v"(of2));
#pragma unroll
  for (int j = 0; j < 8; ++j) v[j] = __builtin_elementwise_fma(v[j], sc2, of2);
}

extern "C" __device__ unsigned long long dispatch_id() __asm("llvm.amdgcn.dispatch.id");

extern "C" __global__ __launch_bounds__(512, 2) void mega_kernel(const bf16* x, char* ws, const bf16* wqkv, const bf16* wo, const bf16* wr,
                                                                 const bf16* wg, const bf16* wu, const bf16* wd, bf16* out) {
  extern __shared__ __attribute__((aligned(16))) char smem[];
  const int w0_ = __builtin_amdgcn_readfirstlane(threadIdx.x >> 6);
  const unsigned lbase = (unsigned)(size_t)(LDS_AS char*)smem;
  {
    unsigned long long pc;
    unsigned len;
    asm volatile("s_getpc_b64 %0\n\ts_mov_b32 %1, .Ltext_end-mega_kernel\n\t.subsection 1\n.Ltext_end:\n\t.subsection 0" : "=s"(pc), "=s"(len));
    const unsigned o = ((blockIdx.x >> 3) * 64 + (threadIdx.x & 63)) * 128;
    if (w0_ == 7 && o < len)
      asm volatile("s_mov_b32 m0, %0\n\ts_nop 0\n\tglobal_load_lds_dword %1, %2" ::"s"(lbase + L_FXL), "v"(o), "s"(pc & ~127ull) : "memory");
  }
  asm volatile("" : "+s"(x), "+s"(ws), "+s"(wqkv));
#define IDS()                                                                                         \
  int tid = threadIdx.x, w = w0_, bid = blockIdx.x;                                                   \
  asm volatile("" : "+v"(tid));                                                                       \
  asm volatile("" : "+s"(w));                                                                         \
  asm volatile("" : "+s"(bid));                                                                       \
  const int l = tid & 63, xcd = bid & 7, bi = bid >> 3;                                               \
  const int p = (xcd & 1) * 4 + (bi & 3), h = (xcd >> 1) * 8 + (bi >> 2);         \
  const int rb = 2 * xcd + (bi & 1), ct = bi >> 1;                               \
  const int slice = xcd * 28 + bi;                                                   \
  const bool ffn_on = bi < 28;                                                                        \
  const int g = l >> 4;                                                                               \
  const int rt = w < 4 ? w : 11 - w;          \
  KARGS();                                                                                            \
  (void)p; (void)h; (void)rb; (void)ct; (void)slice; (void)ffn_on; (void)g; (void)rt;
  const rsrc_t rs = __builtin_amdgcn_make_buffer_rsrc((void*)ws, 0, WS_BYTES, 0x00020000);
  typedef const void* const __attribute__((address_space(4)))* karg_t;
#define KARGS() karg_t ka = (karg_t)__builtin_amdgcn_kernarg_segment_ptr(); asm volatile("" : "+s"(ka))
#define ARGP(name, i) const GAS bf16* name = (const GAS bf16*)ka[i]; asm volatile("" : "+s"(name))
#define RSRC(ptr, bytes) __builtin_amdgcn_make_buffer_rsrc((void*)(ptr), 0, bytes, 0x00020000)
#define rwq RSRC(pwq, 1024 * 3072 * 2)
#define rwo RSRC(pwo, 1024 * 1024 * 2)
#define rwg RSRC(pwg, 1024 * F_ * 2)
#define rwu RSRC(pwu, 1024 * F_ * 2)
#define rwd RSRC(pwd, 1024 * F_ * 2)
#define rout RSRC(pout, 2048 * 1024 * 2)

#define WQ_VOFF() ((l >> 2) * 6144 + (((l & 3) ^ ((((l >> 4) ^ (l >> 5)) & 1) << 1)) << 4))
#define WQ_PIECE(t, j, dst) dma16(rwq, wqv, ((((t) + rot) & 7) * 128 + ((j) & 7) * 16) * 6144 + ((j) >> 3) * 2048 + h * 64, lbase + (dst) + (j) * 1024)
#define WQ_BOFF(dt) ((8 * g + ((l & 15) >> 2)) * 64 + (l & 3) * 8 + (((dt) ^ (g & 1)) << 5))
#define WQ_POFF() ((4 * g + ((l & 15) >> 2)) * 64 + (l & 3) * 8 + (((g ^ (g >> 1)) & 1) << 5))
#define X_UNIT1(u, j) dma16(rx, xv_, (unsigned)(p * 256 + rt * 32 + ((u) & 1) * 16 + (j) * 8) * 2048 + ((((u) >> 2) + rot) & 7) * 256 + (((u) >> 1) & 1) * 128, lbase + rt * 2048 + unit_slot(u) + (j) * 1024)
#define X_VOFF() ((l >> 3) * 2048 + (((l & 7) ^ ((l >> 3) & 6)) << 4))
#define X_ROFF(hs) (lbase + rt * 2048 + (l & 15) * 128 + (((4 * (hs) + g) ^ (l & 6)) << 4))
#define TOUCH_TO(ptr, dst) asm volatile("s_mov_b32 m0, %0\n\ts_nop 0\n\tglobal_load_lds_dword %1, off" ::"s"(lbase + (dst)), "v"(ptr) : "memory")
#define TOUCH(ptr) TOUCH_TO(ptr, L_FXL)
#define WO_PIECE_W(j, w_) dma16(rwo, wov, (((w_) >> 2) * 64 + ((w_) & 3) + 4 * (j)) * 16384 + ct * 128, lbase + 32768 + (j) * 8192 + (w_) * 1024)
#define WO_PIECE(j) WO_PIECE_W(j, w)
#define WO_VOFF() ((l >> 3) * 2048 + (((((l & 7) >> 1) ^ ((l >> 4) & 3)) * 2 + (l & 1)) << 4))
#define WD_DMA() _Pragma("unroll") for (int i = 0; i < 4; ++i) WD_DMA1(i)
#define WD_DMA1(i)                                                                                   \
      dma16(rwd, (l >> 4) * 2048 + (l & 15) * 16, (unsigned)(slice * 16 + 4 * (i)) * 2048 + w * 256, lbase + L_DN + w * 4096 + (i) * 1024)
#define FFN_DMA(m) dma16((m) & 1 ? rwu : rwg, fnv, (unsigned)(((4 * w + ((m) >> 1)) * 32) * F_ + slice * 16) * 2, lbase + 65536 + (m) * 8192 + w * 1024)
  u32x4 gv[4], uv[4];
  s16x4 dfr[8];
  u32x4 keep[9];
  u32x4 pre[4][2];
#define PRE_LOAD(it_, rot_, s0, s1)                                                                  \
  static_for<(s1) - (s0)>([&](auto c_) __attribute__((always_inline)) {                              \
    constexpr int s = (s0) + decltype(c_)::value;                                                    \
    const unsigned ab_ = OFF_XN + (it_) * (4u << 20) + (((rot_) + (s >> 2)) & 7) * 8192 + ((s >> 1) & 1) * 4096; \
    ld16w<2 * s, (s & 1) * 2048>(pre[s][0], rs, l * 16, ab_ + (p * 8 + rt) * 65536);                         \
    ld16w<2 * s + 1, (s & 1) * 2048 + 1024>(pre[s][1], rs, l * 16, ab_ + (p * 8 + rt) * 65536);                 \
  })
  u32x2 T;
  const GAS bf16* tsrc;
  {
    IDS();
    const int it = 0, rot = ROT0;
    (void)it;
    STAMPAT(0);
    const bf16* pwq = wqkv;
    const rsrc_t rx = RSRC(x, 2048 * 1024 * 2);
    const unsigned wqv = WQ_VOFF(), xv_ = X_VOFF();
    if (tid == 0) *(LDS_AS u32x4*)(smem + L_QC) = u32x4{0u, 0u, 0u, 0u};
#pragma unroll
    for (int i = 0; i < 3; ++i) WQ_PIECE(0, 3 * w + i, L_RING);
    STAMPAT(41);
    X_UNIT1(0, 0); X_UNIT1(0, 1); X_UNIT1(1, 0); X_UNIT1(1, 1);
    STAMPAT(42);
    BAR();
    STAMPAT(43);
    X_UNIT1(2, 0); X_UNIT1(2, 1);
#pragma unroll
    for (int i = 0; i < 3; ++i) WQ_PIECE(1, 3 * w + i, L_RING + STAGE);
    STAMPAT(44);
    {
      ARGP(pwo, 3); ARGP(pwg, 5); ARGP(pwu, 6);
      const int t = tid < 288 ? tid - 64 : tid - 288, go = (bi * 32 + t / 7) * F_ + xcd * 448 + (t % 7) * 64;
      tsrc = tid < 64 ? pwo + bid * 4096 + tid * 64 : tid < 288 ? pwg + go : pwu + go;
      asm volatile("" : "+v"(tsrc));
    }
    STAMPAT(31);
  }
  unsigned sel0 = 0x7FFFFFFFu;
  f32x2 v[8];
  u32x4 x2p[2];
  float wsum;

  auto layer = [&](auto it_c) __attribute__((always_inline)) {
    constexpr int it = decltype(it_c)::value;
    STAMPAT(1); STAMPAT(22);
    f32x4 acc[2][6];
    f32x2 qs2[2];
    u32x4 k0, k1, v0, v1;
    {
      IDS();
      bool aff = false;
      unsigned aq[2];
      u32x4 av[2];
#define PIN_ACC() asm volatile("" : "+{v[176:179]}"(acc[0][0]), "+{v[180:183]}"(acc[0][1]), "+{v[184:187]}"(acc[0][2]), "+{v[188:191]}"(acc[0][3]), "+{v[192:195]}"(acc[0][4]), "+{v[196:199]}"(acc[0][5]), \
                               "+{v[200:203]}"(acc[1][0]), "+{v[204:207]}"(acc[1][1]), "+{v[208:211]}"(acc[1][2]), "+{v[212:215]}"(acc[1][3]), "+{v[216:219]}"(acc[1][4]), "+{v[220:223]}"(acc[1][5]))
#define PINV4(x) asm volatile("" : "+v"(x[0]), "+v"(x[1]), "+v"(x[2]), "+v"(x[3])::"memory")
      auto qkv_loop = [&](auto it_c) __attribute__((always_inline)) {
        constexpr int IT = decltype(it_c)::value;
        const unsigned abase = OFF_XN + IT * (4u << 20) + (p * 8 + rt) * 65536;
        const int rot = ROT0 + 5 * IT;
        unsigned fkey = 0, kv, bq[2];
        u32x4 fx[4], bv[2], af[WA][2];
        u32x2 cxn, cx1, hh;
        f32x4 sa, sb, cs4[2];
        f32x2 b2[2];
#define AB_OFF() (OFF_AB + IT * 32768 + (p * 8 + rt) * 512)
#define A_LOAD1(k, a) ld16w<(k) % WA * 2 + (a), ((k) & 1) * 2048 + (a) * 1024>(af[(k) % WA][a], rs, l * 16, abase + ((((k) >> 2) + rot) & 7) * 8192 + (((k) >> 1) & 1) * 4096)
        if constexpr (IT == 1) {
#pragma unroll
          for (int s = 0; s < 4; ++s) { af[s][0] = pre[s][0]; af[s][1] = pre[s][1]; }
        }
        const bf16* pwq = wqkv;
        ARGP(pwo, 3);
        const rsrc_t rx = RSRC(x, 2048 * 1024 * 2);
        unsigned xv_ = X_VOFF(), xr[2] = {X_ROFF(0), X_ROFF(1)};
        if constexpr (IT == 0) asm volatile("" : "+v"(xv_), "+v"(xr[0]), "+v"(xr[1]));
#define X_READ(u) { af[((u) & ~1) % WA][(u) & 1] = *(const LDS_AS u32x4*)(size_t)(xr[0] + unit_slot(u)); af[((u) | 1) % WA][(u) & 1] = *(const LDS_AS u32x4*)(size_t)(xr[1] + unit_slot(u)); }
        const int tstep = TS0 + bid % TSN + TSD * (w >> TSG);
        const unsigned wqv = WQ_VOFF();
#define QKV_DMA1(t, i)                                                                            \
  {                                                                                               \
    if constexpr (IT == 0) WQ_PIECE(t, 3 * w + (i), L_RING + SLOT(t) * STAGE);                    \
    else dma16s<(i) * 1024>(rs, l * 16, OFF_WI + (h * 8 + (((t) + rot) & 7)) * STAGE + w * 3072, lbase + L_RING + SLOT(t) * STAGE + w * 3072); \
  }
        unsigned bo[2][2];
#pragma unroll
        for (int dt = 0; dt < 2 - IT; ++dt) {
          bo[dt][0] = lbase + L_RING + (IT ? l * 16 : WQ_BOFF(dt)); bo[dt][1] = bo[dt][0] + 2 * STAGE;
          asm volatile("" : "+v"(bo[dt][0]), "+v"(bo[dt][1]));
        }
        auto frag0 = [&](int sl, int f) __attribute__((always_inline)) {
          unsigned pb = lbase + L_RING + WQ_POFF();
          asm volatile("" : "+v"(pb));
          const unsigned o = (pb ^ ((f & 1) << 5)) + sl * STAGE + ((f % 6) >> 1) * 8192 + (f / 6) * 2048;
          frag_t t;
          t.hf.a = trabs(o); t.hf.b = trabs(o + 1024);
          return t.v;
        };
        u32x4 df[4];
        auto dot_mma = [&](const u32x4 (&xf)[4]) __attribute__((always_inline)) {
          sa = mfma16(df[0], xf[0], f32x4{0.f, 0.f, 0.f, 0.f}); sb = mfma16(df[1], xf[1], f32x4{0.f, 0.f, 0.f, 0.f});
          sa = mfma16(df[2], xf[2], sa); sb = mfma16(df[3], xf[3], sb);
        };
        auto dot_store = [&](unsigned dst) __attribute__((always_inline)) {
          if ((l & 15) == 0) st16(unpoison4(__builtin_bit_cast(u32x4, sa + sb)), rs, g * 16, dst + (h * 8 + p) * 512 + w * 64);
        };
        auto tile_sum = [&](u32x4 pt, int dst) __attribute__((always_inline)) {
          f32x4 c = __builtin_bit_cast(f32x4, pt);
#pragma unroll
          for (int e = 0; e < 4; ++e) { c[e] += dppf<0x124>(c[e]); c[e] += dppf<0x128>(c[e]); c[e] = add_x16(c[e]); }
          if ((l & 28) == 0) *(LDS_AS f32x4*)(smem + dst + (w * 2 + (l >> 5)) * 64 + (l & 3) * 16) = c;
        };
        frag_t bfr[6];
#define QKV_BO(s, n, x) (bo[((n) ^ (x)) & 1][SLOT((s) >> 2) >> 1] + (SLOT((s) >> 2) & 1) * STAGE + ((n) >> 1) * 8192 + ((s) & 3) * 2048 + (x) * 256)
#define QKV_RDA(s, n)                                                                             \
  {                                                                                               \
    if constexpr (IT == 0) bfr[n].hf.a = trabs(QKV_BO(s, n, 0));                                  \
    else bfr[n].v = *(const LDS_AS u32x4*)(size_t)(bo[0][SLOT((s) >> 2) >> 1] + (SLOT((s) >> 2) & 1) * STAGE + (((s) & 3) * 6 + (n)) * 1024); \
  }
#define QKV_RDB(s, n) { if constexpr (IT == 0) bfr[n].hf.b = trabs(QKV_BO(s, n, 1)); }
#define XP_OFF() (OFF_XNEW + ((rot + 6) & 7) * 1024)
#define FP_VO() (w == 3 ? (l >> 2) * 256 + (l & 3) * 16 : ((l >> 2) & 7) * 512 + (l >> 5) * 64 + (l & 3) * 16)
#define FP_OFF(i) (w == 3 ? OFF_XNEW + (i) * 4096 : OFF_FX + h * 4096 + w * 128)
#define FX_OK(n) __all(((n) == 4 ? max(max(umax4(fx[0]), umax4(fx[1])), max(umax4(fx[2]), umax4(fx[3]))) : max(umax4(fx[0]), umax4(fx[1]))) != POISON)
#define SINCE(s0) (vm_at(IT, s, n) - vm_at(IT, s0, 5))
#define DEF4(x) asm volatile("" : "=v"(x[0]), "=v"(x[1]), "=v"(x[2]), "=v"(x[3]))
        LDS_AS unsigned* qcnt = (LDS_AS unsigned*)(smem + L_QC);
        unsigned qc;
        unsigned qadr = lbase + L_QC, qone = 1;
        asm volatile("" : "+v"(qadr), "+v"(qone));
#define count_in(i) asm volatile("s_mov_b64 exec, 1\n\tds_add_u32 %0, %1 offset:%2\n\ts_mov_b64 exec, -1" ::"v"(qadr), "v"(qone), "n"(4 * (i)) : "memory")
        auto count_wait = [&](int i, unsigned want) __attribute__((always_inline)) {
          if (__builtin_expect((unsigned)__builtin_amdgcn_readfirstlane(qc) < want, 0)) {
            while ((unsigned)__builtin_amdgcn_readfirstlane(__hip_atomic_load(qcnt + i, __ATOMIC_RELAXED, __HIP_MEMORY_SCOPE_WORKGROUP)) < want) { if (SPINS) __builtin_amdgcn_s_sleep(SPINS); }
          }
          asm volatile("" ::: "memory");
        };
        if constexpr (IT == 0) {
          wait_vm<12 - vm_unit(1)>();
          STAMPAT(46);
          if (!(SYNCB & 1)) count_in(0);
          X_READ(0); X_READ(1);
          asm volatile("s_waitcnt lgkmcnt(0)" : "+v"(af[0][0]), "+v"(af[0][1]), "+v"(af[1][0]), "+v"(af[1][1])::"memory");
          X_UNIT1(3, 0); X_UNIT1(3, 1); X_UNIT1(4, 0); X_UNIT1(4, 1);
          STAMPAT(38);
          qc = 0;
          if (SYNCB & 1) asm volatile("s_barrier" ::: "memory");
          else count_wait(0, 8);
        }
        STAMPAT(39);
#pragma unroll
        for (int n = 0; n < 6; ++n) { QKV_RDA(0, n); if (n < 5) QKV_RDB(0, n); }
        f32x2 lv[4], xs, xq;
        float lmean;
        u32x2 ep;
        u32x4 xl[2];
        f32x4 sc;
#define X1_OFF() (F_X1 + ((p * RX1 + (xcd >> 1) % RX1) * 32 + (rt >> 2) * 16 + (l & 15)) * SX1)
        unsigned long long xok = 0;
        RowFin fin;
        auto x1_need = [&](unsigned long long m) __attribute__((always_inline)) {
          if (__builtin_expect((xok & m) != m, 0)) {
            for (unsigned spins = 0;;) {
              const u32x2 f = ld8(rs, X1_OFF(), 0);
              xok = __builtin_amdgcn_ballot_w64(f[0] == T[0] && f[1] == T[1]);
              if ((xok & m) == m) break;
              if (spin_fail(spins, rs, T)) { xok = ~0ull; break; }
              __builtin_amdgcn_s_sleep(KSL);
            }
            asm volatile("" ::: "memory");
          }
        };
        auto gap = [&](auto sc_, auto nc_) __attribute__((always_inline)) {
          constexpr int s = decltype(sc_)::value, n = decltype(nc_)::value;
          if constexpr ((SYNCB >> IT) & 1) {
            constexpr int tc = (s + 2) / 4;
            if constexpr (tc >= 1 && tc < 8 && s == 4 * tc - 2 && n == 5) {
              if constexpr (!IT || tc >= 3) wait_vm<vm_at(IT, s, n) - vm_tile(IT, tc)>();
              if constexpr (tc == 7 && !IT) asm volatile("s_waitcnt lgkmcnt(4)" ::: "memory");
              asm volatile("s_barrier" ::: "memory");
            }
          } else {
            constexpr int ts = (s + 3) / 4, tc = (s + 2) / 4, t0 = 1;
            if constexpr (ts >= 3 && ts < 8 && s == 4 * ts - 3 && n == 2) { wait_vm<vm_at(IT, s, n) - vm_tile(IT, ts)>(); count_in(0); }
            if constexpr (!IT && tc >= t0 && tc < 3 && s == 4 * tc - 2 && n == 1) { wait_vm<vm_at(IT, s, n) - vm_tile(IT, tc)>(); count_in(0); }
            if constexpr (IT && ts >= t0 && ts < 3 && s == 4 * ts - 3 && n == 2) count_in(0);
            if constexpr (tc >= t0 && tc < 8 && s == 4 * tc - 2) {
              if constexpr (n == 3) qc = __hip_atomic_load(qcnt, __ATOMIC_RELAXED, __HIP_MEMORY_SCOPE_WORKGROUP);
              if constexpr (n == 5) count_wait(0, 8 * (tc + 1 - IT));
            }
          }
          if constexpr (IT == 0 && n == 0 && s + 2 < 32) { wait_vm<vm_at(0, s, 0) - vm_unit(s + 2)>(); X_READ(s + 2); SB(); }
          if constexpr (n == 0 && tile_piece(IT, s)) QKV_DMA1(((s - tp0(IT)) >> 2) + 3, (s - tp0(IT)) & 3);
          if constexpr (IT == 0) {
            if constexpr ((n == 2 || n == 3) && s + 5 < 32) {
              if constexpr (n == 2) asm volatile("s_waitcnt lgkmcnt(%0)" ::"n"(s + 1 < QL ? 4 : 1) : "memory");
              X_UNIT1(s + 5, n - 2);
            }
            if constexpr (s >= W2S && s < W2S + 3 && n == 4) QKV_DMA1(2, s - W2S);
            if constexpr (s == LNL && n == 5) {
              asm volatile("buffer_load_dwordx2 %0, off, %1, 0 sc1" : "=v"(ep) : "s"(rs) : "memory");
              asm volatile("buffer_load_dwordx4 %0, %1, %2, %3 offen" : "=v"(xl[0]) : "v"(l * 16), "s"(rx), "s"((p * 256 + h * 8 + w) * 2048) : "memory");
              asm volatile("buffer_load_dwordx4 %0, %1, %2, %3 offen offset:1024" : "=v"(xl[1]) : "v"(l * 16), "s"(rx), "s"((p * 256 + h * 8 + w) * 2048) : "memory");
            }
            if constexpr (s == LNS && n == 1) {
              wait_vm<vm_at(0, s, n) - vm_at(0, LNL + 1, 0)>();
              asm volatile("" : "+v"(ep), "+v"(xl[0]), "+v"(xl[1])::"memory");
              const unsigned long long e = (((unsigned long long)(unsigned)__builtin_amdgcn_readfirstlane(ep[1]) << 32) | (unsigned)__builtin_amdgcn_readfirstlane(ep[0])) + 1ull + dispatch_id() * 0x9E3779B97F4A7C16ull;
              T = u32x2{(unsigned)e, (unsigned)(e >> 32)};
              unpack8v(xl[0], lv);
              xs = (lv[0] + lv[1]) + (lv[2] + lv[3]);
              xq = __builtin_elementwise_fma(lv[0], lv[0], lv[1] * lv[1]) + __builtin_elementwise_fma(lv[2], lv[2], lv[3] * lv[3]);
              asm volatile("" : "+v"(xs), "+v"(xq));
            }
            if constexpr (s == LNS && n == 4) {
              unpack8v(xl[1], lv);
              xs += (lv[0] + lv[1]) + (lv[2] + lv[3]);
              xq += __builtin_elementwise_fma(lv[0], lv[0], lv[1] * lv[1]) + __builtin_elementwise_fma(lv[2], lv[2], lv[3] * lv[3]);
              auto t = __builtin_amdgcn_permlane32_swap(__builtin_bit_cast(unsigned, hadd(xs)), __builtin_bit_cast(unsigned, hadd(xq)), false, false);
              lmean = asf(t[0]) + asf(t[1]);
              asm volatile("" : "+v"(lmean));
            }
            if constexpr (s == LNS + 1 && n == 1) { lmean += dppf<0xB1>(lmean); lmean += dppf<0x4E>(lmean); lmean += dppf<0x124>(lmean); asm volatile("" : "+v"(lmean)); }
            if constexpr (s == LNS + 1 && n == 4) { lmean = add_x16(lmean + dppf<0x128>(lmean)) * (1.0f / 1024.0f); asm volatile("" : "+v"(lmean)); }
            if constexpr (s == LNS + 2 && n == 1) {
              const float m2 = asf(__builtin_amdgcn_readlane(__builtin_bit_cast(unsigned, lmean), 32));
              xs[0] = ln_rd(fmaxf(__builtin_fmaf(-lmean, lmean, m2), 0.f));
              asm volatile("" : "+v"(xs));
            }
            if constexpr (s == LNS + 2 && n == 5) {
              if (l == 0) st16(u32x4{__builtin_bit_cast(unsigned, (float)xs[0]), __builtin_bit_cast(unsigned, -rbf1(lmean)), T[0], T[1]}, rs, OFF_AB + (p * 256 + h * 8 + w) * 16, 0);
            }
            if constexpr (s == POI && n == 1) {
              if (w == 1) {
                const unsigned prb = 2 * p + (h >> 4), pct = h & 15;
                unsigned pz1 = POISON;
                asm volatile("" : "+v"(pz1));
                const u32x4 pz = {pz1, pz1, pz1, pz1};
                st16(pz, rs, OFF_STAT + (prb * 16 + pct) * 2048 + l * 16, 0);
                st16(pz, rs, OFF_STAT + (prb * 16 + pct) * 2048 + 1024 + l * 16, 0);
                if (l < 16) st16(pz, rs, OFF_CA + prb * 4096 + pct * 256 + l * 16, 0);
                else if (l == 16 && pct < RKEY) st4(pz1, rs, KEY_AT(0, pct) + prb * SKEY, 0);
              }
            }
            if constexpr (s == POI + 4 && n == 1) { if (w == 1 && l < RX) st8(T, rs, F_XN + (p * RX + l) * 256 + h * 8, 0); }
          } else {
            if constexpr ((s == 0 && n == 0) || (s == 2 && n == 2)) asm volatile("buffer_load_dwordx2 %0, %1, %2, 0 offen sc1" : "=v"(cxn) : "v"(X1_OFF()), "s"(rs) : "memory");
            if constexpr ((s == 1 && n == 3) || (s == 4 && n == 0)) {
              wait_vm<vm_at(1, s, n) - (s == 1 ? vm_at(1, 0, 1) : vm_at(1, 2, 3))>();
              asm volatile("" : "+v"(cxn)::"memory");
              xok |= __builtin_amdgcn_ballot_w64(cxn[0] == T[0] && cxn[1] == T[1]);
            }
#define IMG_OFF() (F_X1 + ((((h >> 3) * 2 + ((l >> 2) & 1)) * RX1 + (xcd >> 1) % RX1) * 32 + (l & 1) * 16 + (h & 7) * 2 + ((l >> 1) & 1)) * SX1)
            if constexpr (s == 0 && n == 2) { asm volatile("" : "=v"(cx1)); if (w == 1) asm volatile("buffer_load_dwordx2 %0, %1, %2, 0 offen sc1" : "=v"(cx1) : "v"(IMG_OFF()), "s"(rs) : "memory"); }
            if constexpr (s == 2 && n == 4) {
              if (w == 1) {
                wait_vm<vm_at(1, 2, 4) - vm_at(1, 0, 3)>();
                asm volatile("" : "+v"(cx1)::"memory");
                if (!__all(cx1[0] == T[0] && cx1[1] == T[1])) poll_flags(rs, IMG_OFF(), 0, T);
              }
            }
            if constexpr (a_req(s, n) >= 0 && (a_req(s, n) & 7) == 0) x1_need(3ull << (((rot + a_req(s, n) / 8) & 7) * 2));
            const unsigned R = (rb * 4 + (w & 3)) * 32 + (w >> 2) * 16 + (l & 15), so = OFF_STAT + rb * 32768;
            if constexpr (s == (SPL < 0 ? 0 : SPL) && n == (SPL < 0 ? 1 : 4)) {
#pragma unroll
              for (int i = 0; i < 4; ++i) ld16h<0>(fx[i], rs, (4 * g + i) * 2048 + (R & 127) * 16, so);
            }
            if constexpr (s == SPL + 2 && n == 1) {
              wait_vm<vm_at(1, s, n) - (SPL < 0 ? vm_at(1, 0, 2) : vm_at(1, SPL, 5))>();
              PINV4(fx);
              fin = fin_key(rs, T, g * 8192 + (R & 127) * 16, so, smem + L_SKEY, l, w, R, wsum, fx);
              count_in(2);
            }
            if constexpr (s == SPL + 3 && n == 0) qc = __hip_atomic_load(qcnt + 2, __ATOMIC_RELAXED, __HIP_MEMORY_SCOPE_WORKGROUP);
            if constexpr (s == SPL + 3 && n == 1) { count_wait(2, 8); fin_cand(rs, smem + L_SKEY, 0, g, rb, ct, fin, v); STAMPAT(51); }
            if constexpr (s == SPL + 3 && n == 3) {
              fin_x2(fin, v);
              x2p[0] = pack8v(v); x2p[1] = pack8v(v + 4);
              asm volatile("" : "+v"(x2p[0]), "+v"(x2p[1]));
            }
            if constexpr (s == SPL + 3 && n == 5) {
              if ((w & 3) * 2 + (w >> 2) == ct >> 1) {
                const float f = 1.0f + fin.rd, rd2 = ln_rd(fin.var * f * f), mb2 = rbf1(__builtin_fmaf(fin.mean - fin.mb, fin.rd, fin.mean));
                if (g == 0 && ((l >> 3) & 1) == (ct & 1))
                  st16(u32x4{__builtin_bit_cast(unsigned, f * rd2), __builtin_bit_cast(unsigned, -__builtin_fmaf(fin.mb, fin.rd, mb2) * __builtin_amdgcn_rcpf(f)), T[0], T[1]}, rs, OFF_AB + 32768 + R * 16, 0);
              }
            }
          }
          if constexpr (IT == 1 && a_req(s, n) >= 0) A_LOAD1(a_req(s, n) / 2, a_req(s, n) & 1);
          if constexpr (!IT && s == CSL && n == 4) { asm volatile("" : "=v"(fx[0])); if (w < 6) ld16h<0>(fx[0], rs, l * 16, OFF_CS + (h * 6 + w) * 1024); }
          if constexpr (!IT && s == CSS && n == 2) {
            if (w < 6) {
              wait_vm<SINCE(CSL)>();
              asm volatile("" : "+v"(fx[0])::"memory");
              tag_wait(rs, T, l * 16, OFF_CS + (h * 6 + w) * 1024, fx[0]);
              const f32x2 c = {sum_hi8(asf(fx[0][0])), sum_hi8(asf(fx[0][1]))};
              if (l < 8) *(LDS_AS f32x2*)(smem + L_CSL + w * 64 + l * 8) = c;
            }
          }
          constexpr int ABL = abl(IT), ABS = abs_(IT);
          if constexpr (s == ABL && n == 4) ld16h<0>(bv[0], rs, (l & 31) * 16, AB_OFF());
          if constexpr (s == ABS && n == 2) {
            wait_vm<SINCE(ABL)>();
            asm volatile("" : "+v"(bv[0])::"memory");
            tag_wait(rs, T, (l & 31) * 16, AB_OFF(), bv[0]);
            const unsigned t0 = bv[0][0], t1 = bv[0][1];
            ((LDS_AS unsigned*)(smem + L_ABW + rt * 2048))[l] = l < 32 ? t0 : t1;
          }
          if constexpr (s == ABS && n == 3) {
#pragma unroll
            for (int a = 0; a < 2; ++a) {
              const LDS_AS unsigned* aw = (const LDS_AS unsigned*)(smem + L_ABW + rt * 2048) + 16 * a;
              aq[a] = aw[l & 15]; bq[a] = aw[32 + (l & 15)];
              av[a] = *(const LDS_AS u32x4*)(aw + 4 * g); bv[a] = *(const LDS_AS u32x4*)(aw + 32 + 4 * g);
            }
          }
          if constexpr (s == ABS && n == 5) {
#pragma unroll
            for (int a = 0; a < 2; ++a) { b2[a] = f32x2{asf(bq[a]), asf(bq[a])}; asm volatile("" : "+v"(b2[a])); }
          }
          if constexpr (s == ABS + 1) {
            constexpr int k = (n + 4) % 6;
            if constexpr (k < 4) cs4[k & 1] = *(const LDS_AS f32x4*)(smem + L_CSL + k * 64 + g * 16);
            else cs4[k & 1][0] = *(const LDS_AS float*)(smem + L_CSL + k * 64 + (l & 15) * 4);
          }
          if constexpr ((s == ABS + 1 && n > 0) || (s == ABS + 2 && n == 0)) {
            constexpr int k = (n + 3) % 6;
            if constexpr (k < 4) {
#pragma unroll
              for (int a = 0; a < 2; ++a) acc[a][k] = __builtin_elementwise_fma(cs4[k & 1], f32x4{b2[a][0], b2[a][1], b2[a][0], b2[a][1]}, acc[a][k]);
            } else {
              f32x2 c2 = {cs4[k & 1][0], cs4[k & 1][0]};
              asm volatile("" : "+v"(c2));
#pragma unroll
              for (int a = 0; a < 2; ++a) acc[a][k] = __builtin_elementwise_fma(f32x4{c2[0], c2[1], c2[0], c2[1]}, __builtin_bit_cast(f32x4, bv[a]), acc[a][k]);
            }
          }
          if constexpr (IT == 0) {
            if constexpr (s == CSD && n < 3) fx[n] = frag0(SLOT(s >> 2), 3 * w + n);
            if constexpr (s == CSD + 1 && n == 1) {
              unsigned one2 = 0x3F803F80u;
              asm volatile("" : "+v"(one2));
              const u32x4 o4 = {one2, one2, one2, one2};
              const f32x4 z4 = {0.f, 0.f, 0.f, 0.f};
              sa = mfma16(fx[0], o4, z4); sb = mfma16(fx[1], o4, z4); sc = mfma16(fx[2], o4, z4);
            }
            if constexpr (s == CSD + 2 && n == 1) {
              const int c = l & 15;
              const f32x4 t0 = sa, t1 = sb, t2 = sc, t = c == 0 ? +t0 : c == 1 ? +t1 : +t2;
              if (c < 3) *(LDS_AS f32x4*)(smem + L_CSP + (w * 3 + c) * 64 + g * 16) = t;
              count_in(2);
            }
            if constexpr (s == CSD + 4 && n == 1) {
              if (w == 2) {
                qc = 0;
                count_wait(2, 8);
                const LDS_AS f32x2* cp = (const LDS_AS f32x2*)(smem + L_CSP) + min(l, 47);
                const f32x2 t = (cp[0] + cp[48]) + (cp[96] + cp[144]);
                if (l < 48) st16(u32x4{__builtin_bit_cast(unsigned, (float)t[0]), __builtin_bit_cast(unsigned, (float)t[1]), T[0], T[1]}, rs, (l >> 3) * 1024 + (l & 7) * 16, OFF_CS + h * 6144 + p * 128);
              }
            }
            if constexpr (s >= 29 && (n & 1)) keep[3 * (s - 29) + (n >> 1)] = frag0(SLOT(s - 24), 3 * w + (n >> 1));
            if constexpr (s >= TS0 && s < TS0 + TSN + TSD * (7 >> TSG) && n == 4) {
              if (tstep == s && ((TM0 >> w) & 1)) TOUCH(tsrc);
            }
            if constexpr (s == TEX && n == 5) {
              const GAS bf16* t2 = ((TM1 >> w) & 1) ? tsrc : (const GAS bf16*)x + p * 262144;
              TOUCH(t2);
            }
          } else {
            if constexpr (s >= 27 && s < 30 && (n == 0 || n == 3)) { const unsigned wov = WO_VOFF(); WO_PIECE(4 + 2 * (s - 27) + n / 3); }
            if constexpr (s == FKL && n == 4) { asm volatile("" : "=v"(kv)); if (w == 0) ld4h<0>(kv, rs, (l & 15) * SKEY, KEY_AT(0, KREP())); }
            if constexpr (s == FSL && n == 1) {
              asm volatile("" : "=v"(fx[0]), "=v"(fx[1]));
              if (w == 0) {
                wait_vm<SINCE(FKL)>();
                asm volatile("" : "+v"(kv)::"memory");
                STAMPAT(52);
                const unsigned k_ = key_wait(rs, T, l, 0, KREP(), kv);
                STAMPAT(53);
                if (l == 0) *(LDS_AS unsigned*)(smem + L_SKEY + 32) = k_;
                fkey = k_;
                ld16h<0>(fx[0], rs, (l >> 3) * 256 + (l & 7) * 16, cand_off(k_, 0)); ld16h<2048>(fx[1], rs, (l >> 3) * 256 + (l & 7) * 16, cand_off(k_, 0));
              }
            }
            if constexpr (s == FS0 - 1 && n == FSN) {
              if (w == 0) {
                wait_vm<vm_at(1, s, n) - vm_at(1, FSL, 2)>();
                asm volatile("" : "+v"(fx[0]), "+v"(fx[1])::"memory");
                word_wait<2>(rs, T, (l >> 3) * 256 + (l & 7) * 16, cand_off(fkey, 0), fx);
                *(LDS_AS u32x4*)(smem + L_ROW + l * 16) = fx[0]; *(LDS_AS u32x4*)(smem + L_ROW + 1024 + l * 16) = fx[1];
                count_in(0);
              }
            }
            if constexpr (s == FS0 - 1 && n == 1) {
              asm volatile("" : "=v"(dfr[0]), "=v"(dfr[1]), "=v"(dfr[2]), "=v"(dfr[3]), "=v"(dfr[4]), "=v"(dfr[5]), "=v"(dfr[6]), "=v"(dfr[7]));
              if (ffn_on) {
#pragma unroll
                for (int i = 0; i < 8; ++i) dfr[i] = trread(smem, L_DN + w * 4096 + g * 1024 + ((l & 15) >> 2) * 256 + i * 32 + (l & 3) * 8);
              }
            }
            if constexpr (s == FS0) {
              if constexpr (n == 0) qc = __hip_atomic_load(qcnt, __ATOMIC_RELAXED, __HIP_MEMORY_SCOPE_WORKGROUP);
              if constexpr (n == 1) {
                count_wait(0, 1);
                kv = *(const LDS_AS unsigned*)(smem + L_SKEY + 32);
#pragma unroll
                for (int i = 0; i < 4; ++i) fx[i] = *(const LDS_AS u32x4*)(smem + L_ROW + w * 256 + i * 64 + g * 16);
              }
              if constexpr (n == 2) {
                fkey = __builtin_amdgcn_readfirstlane(kv);
                sel0 = key_row(fkey);
                aff = (int)(sel0 >> 8) == p;
                asm volatile("" : "=v"(sa), "=v"(sb));
                if (ffn_on) ffn_gu(gv, uv, fx, sa, sb);
              }
              if constexpr (n == 4) {
                if (ffn_on) {
                  ffn_gu_store(smem, l, w, sa, sb);
                  count_in(1);
                }
              }
            }
            if constexpr (s == FS0D) {
              if constexpr (n == 0) qc = __hip_atomic_load(qcnt + 1, __ATOMIC_RELAXED, __HIP_MEMORY_SCOPE_WORKGROUP);
              if constexpr (n == 1) { asm volatile("" : "=v"(sa)); if (ffn_on) { count_wait(1, 8); sa = ffn_d_read(smem, l); } }
              if constexpr (n == 2) { asm volatile("" : "=v"(hh)); if (ffn_on) hh = ffn_d_h(sa); }
              if constexpr (n == 4) { asm volatile("" : "=v"(sa)); if (ffn_on) sa = ffn_d_all(dfr, hh); }
              if constexpr (n == 5) { if (ffn_on) ffn_d_store(rs, l, w, 0, slice, sa); STAMPAT(54); }
            }
            if constexpr (s == FS1 && n == 4) {
              DEF4(fx);
              asm volatile("" : "=v"(cxn), "=v"(cx1));
              if (!ffn_on) {
                const unsigned G = xcd * 4 + bi - 28;
#pragma unroll
                for (int k = 0; k < 4; ++k) ld16h<0>(fx[k], rs, RED_VO(k), RED_SO(0, G));
                ld8h(cxn, rs, RED_JJ(), cand_off(fkey, 0) + XO(G)); ld8h(cx1, rs, RED_JJ() + 128, cand_off(fkey, 0) + XO(G));
              }
            }
            if constexpr (s == FS1S && n == 2) {
              if (!ffn_on) {
                wait_vm<SINCE(FS1)>();
                PINV4(fx);
                asm volatile("" : "+v"(cxn), "+v"(cx1)::"memory");
                STAMPAT(55);
                red_sum(rs, smem, T, tid, w, 0, xcd * 4 + bi - 28, fkey, fx, cxn, cx1);
                STAMPAT(56);
                BAR();
                if (w == 0) {
                  const u32x2 ov = red_final(smem, l, fkey, cxn, cx1);
                  if (l < 8) st8(unpoison2(ov), rs, OFF_XNEW + (xcd * 4 + bi - 28) * 256 + ((l & 3) * 2 + (l >> 2)) * 8, 0);
                }
              }
            }
            if constexpr (s == FXA && n == 4) { asm volatile("" : "=v"(fx[0])); if (w == 1) ld16h<0>(fx[0], rs, ((l >> 2) & 3) * 256 + (l & 3) * 16, XP_OFF()); }
            if constexpr (s == FXB - 1 && n == FSN) {
              if (w == 1) {
                wait_vm<SINCE(FXA)>();
                asm volatile("" : "+v"(fx[0])::"memory");
                word_wait<1>(rs, T, ((l >> 2) & 3) * 256 + (l & 3) * 16, XP_OFF(), fx);
                if (l < 16) *(LDS_AS u32x4*)(smem + L_ROW + l * 16) = fx[0];
                count_in(0);
              }
            }
            if constexpr (s == FXB) {
#define FX_FRAG(i) (*(const LDS_AS u32x4*)(size_t)(bo[0][1] + (6 * (i) + w) * 1024))
              if constexpr (n == 0) { STAMPAT(58); qc = __hip_atomic_load(qcnt, __ATOMIC_RELAXED, __HIP_MEMORY_SCOPE_WORKGROUP); }
              if constexpr (n == 1) {
                DEF4(fx); DEF4(df);
                if (w < 6) {
                  count_wait(0, 2);
#pragma unroll
                  for (int i = 0; i < 4; ++i) { fx[i] = *(const LDS_AS u32x4*)(smem + L_ROW + i * 64 + g * 16); df[i] = FX_FRAG(i); }
                }
              }
              if constexpr (n == 2) { asm volatile("" : "=v"(sa), "=v"(sb)); if (w < 6) dot_mma(fx); }
              if constexpr (n == 4) { if (w < 6) dot_store(OFF_FX); STAMPAT(57); }
            }
            if constexpr (s == FXL && n == 4) {
              asm volatile("" : "=v"(fx[0]), "=v"(fx[1]));
              if (aff && w < 4) { ld16h<0>(fx[0], rs, FP_VO(), FP_OFF(0)); ld16h<0>(fx[1], rs, FP_VO(), FP_OFF(1)); }
            }
            if constexpr (s == FXS && n == 4) {
              if (aff && w < 4) {
                STAMPAT(59);
                wait_vm<0>();
                asm volatile("" : "+v"(fx[0]), "+v"(fx[1])::"memory");
                if (!FX_OK(2)) {
                  unsigned spins = 0;
                  do {
                    if (spin_fail(spins, rs, T)) break;
                    fx[0] = ld16(rs, FP_VO(), FP_OFF(0)); fx[1] = ld16(rs, FP_VO(), FP_OFF(1));
                  } while (!FX_OK(2));
                }
                STAMPAT(60);
                if (w == 3) {
                  f32x2 nx[8];
                  unpack8v(fx[0], nx); unpack8v(fx[1], nx + 4);
                  const f32x2 sm = ((nx[0] + nx[1]) + (nx[2] + nx[3])) + ((nx[4] + nx[5]) + (nx[6] + nx[7]));
                  const float mean = sum64(hadd(sm)) * (1.0f / 1024.0f);
                  const f32x2 m2 = {mean, mean};
                  f32x2 q = {0.f, 0.f};
#pragma unroll
                  for (int j = 0; j < 8; ++j) { const f32x2 d = nx[j] - m2; q = __builtin_elementwise_fma(d, d, q); }
                  const float rd = ln_rd(sum64(hadd(q)) * (1.0f / 1024.0f));
                  if (l == 0) *(LDS_AS f32x2*)(smem + L_FXL + 384) = f32x2{rd, -rbf1(mean)};
                } else {
                  tile_sum(fx[0], L_FXL);
                }
                count_in(3);
              }
            }
          }
        };
        static_for<32>([&](auto sc_) __attribute__((always_inline)) {
          constexpr int s = decltype(sc_)::value;
          if constexpr (s > 0) PIN_ACC();
          if constexpr (IT == 1) wait_vm<vm_at(IT, s, 0) - vm_a(IT, s)>();
          pin_w<s % WA>(af[s % WA][0], af[s % WA][1]);
          VSTEP(s);
          SB();
          static_for<6>([&](auto nc_) __attribute__((always_inline)) {
            constexpr int n = decltype(nc_)::value;
            if constexpr (s == 0) { acc[0][n] = f32x4{0.f, 0.f, 0.f, 0.f}; acc[1][n] = acc[0][n]; }
            constexpr bool on = s < QL || n >= 2;
            if constexpr (on) acc[0][n] = n < 4 ? mfma16(bfr[n].v, af[s % WA][0], acc[0][n]) : mfma16(af[s % WA][0], bfr[n].v, acc[0][n]);
            SB();
            if constexpr (n == 0) { QKV_RDB(s, 5); }
            else if constexpr (s + 1 < 32 && (s + 1 < QL || n > 2)) QKV_RDB(s + 1, n - 1);
            SB();
            if constexpr (on) acc[1][n] = n < 4 ? mfma16(bfr[n].v, af[s % WA][1], acc[1][n]) : mfma16(af[s % WA][1], bfr[n].v, acc[1][n]);
            SB();
            if constexpr (s + 1 < 32 && (s + 1 < QL || n >= 2)) QKV_RDA(s + 1, n);
            SB();
            gap(sc_, nc_);
            GSTEP(s, n);
            SB();
          });
        });
        PIN_ACC();
        VSTEP(32);
        STAMPAT(18);
        VSTEP(33);
        frag_t qw[32 - QL][2];
        static_for<32 - QL>([&](auto c_) __attribute__((always_inline)) {
          constexpr int s = QL + decltype(c_)::value;
          QKV_RDA(s, 0); QKV_RDB(s, 0); QKV_RDA(s, 1); QKV_RDB(s, 1);
          qw[s - QL][0] = bfr[0]; qw[s - QL][1] = bfr[1];
        });
        SB();
        const bool own = aff && (int)((sel0 >> 5) & 7) == rt;
        const bool m0 = !(sel0 & 16), mq = (l & 15) == (int)(sel0 & 15), mv = g == (int)((sel0 & 15) >> 2);
        auto patch_qk = [&](int n) __attribute__((always_inline)) {
          f32x4 t = *(const LDS_AS f32x4*)(smem + L_FXL + n * 64 + g * 16);
          const f32x4 c = *(const LDS_AS f32x4*)(smem + L_CSL + n * 64 + g * 16);
          const float nb = *(const LDS_AS float*)(smem + L_FXL + 388);
#pragma unroll
          for (int e = 0; e < 4; ++e) { t[e] = __builtin_fmaf(c[e], nb, t[e]); asm volatile("" : "+v"(t[e])); }
          acc[0][n] = (mq && m0) ? t : acc[0][n];
          acc[1][n] = (mq && !m0) ? t : acc[1][n];
        };
        if (own) {
          while (__hip_atomic_load(qcnt + 3, __ATOMIC_RELAXED, __HIP_MEMORY_SCOPE_WORKGROUP) < 4u) {}
          asm volatile("" ::: "memory");
          patch_qk(2); patch_qk(3);
          const f32x2 nab = *(const LDS_AS f32x2*)(smem + L_FXL + 384);
          const LDS_AS float* fl_ = (const LDS_AS float*)(smem + L_FXL + 256) + (l & 15);
          const LDS_AS float* cl_ = (const LDS_AS float*)(smem + L_CSL + 256) + (l & 15);
          const float t4 = __builtin_fmaf(cl_[0], nab[1], fl_[0]), t5 = __builtin_fmaf(cl_[16], nab[1], fl_[16]);
          const unsigned na = __builtin_bit_cast(unsigned, (float)nab[0]);
#pragma unroll
          for (int a = 0; a < 2; ++a) {
            const bool ma = a ? !m0 : m0;
            aq[a] = (mq && ma) ? na : aq[a];
#pragma unroll
            for (int e = 0; e < 4; ++e) {
              const bool me = mv && ma && e == (int)(sel0 & 3);
              acc[a][4][e] = me ? t4 : acc[a][4][e]; acc[a][5][e] = me ? t5 : acc[a][5][e];
              av[a][e] = me ? na : av[a][e];
            }
          }
        }
        constexpr float C2 = 1.4426950408889634f * 0.17677669529663687f;
#pragma unroll
        for (int a = 0; a < 2; ++a) {
          f32x2 a2 = {asf(aq[a]), asf(aq[a])};
          qs2[a] = f32x2{asf(aq[a]) * C2, asf(aq[a]) * C2};
          asm volatile("" : "+v"(a2), "+v"(qs2[a]));
          acc[a][2] *= f32x4{a2[0], a2[1], a2[0], a2[1]}; acc[a][3] *= f32x4{a2[0], a2[1], a2[0], a2[1]};
          acc[a][4] *= __builtin_bit_cast(f32x4, av[a]); acc[a][5] *= __builtin_bit_cast(f32x4, av[a]);
        }
        k0 = pack8(acc[0][2], acc[0][3]); k1 = pack8(acc[1][2], acc[1][3]); v0 = pack8(acc[0][4], acc[1][4]); v1 = pack8(acc[0][5], acc[1][5]);
        const unsigned kvo = OFF_KV + IT * (8u << 20) + (p * 32 + h) * 32768 + rt * 4096;
        if (p < 7) { st16(k0, rs, l * 16, kvo); st16(k1, rs, l * 16 + 1024, kvo); st16(v0, rs, l * 16 + 2048, kvo); st16(v1, rs, l * 16 + 3072, kvo); }
        char* ownp = smem + (8 + rt) * 2048 + l * 16;
        *(u32x4*)(ownp + L_K) = k0;
        if (rt < 7) *(u32x4*)(ownp + L_K + 1024) = k1;
        *(u32x4*)(ownp + L_V) = v0; *(u32x4*)(ownp + L_V + 1024) = v1;
        SB();
        static_for<32 - QL>([&](auto c_) __attribute__((always_inline)) {
          constexpr int s = QL + decltype(c_)::value;
#pragma unroll
          for (int n = 0; n < 2; ++n) { acc[0][n] = mfma16(qw[s - QL][n].v, af[s % WA][0], acc[0][n]); acc[1][n] = mfma16(qw[s - QL][n].v, af[s % WA][1], acc[1][n]); }
        });
        if (own) { patch_qk(0); patch_qk(1); }
      };
      qkv_loop(it_c);
    }
#undef A_LOAD1
#undef QKV_RDA
#undef QKV_RDB
#undef QKV_BO
    STAMPAT(2); STAMPAT(21);
    {
      IDS();
      const GAS bf16 *pwo, *pwg, *pwu;
      asm volatile("s_load_dwordx2 %0, %3, 24\n\ts_load_dwordx2 %1, %3, 40\n\ts_load_dwordx2 %2, %3, 48" : "=&s"(pwo), "=&s"(pwg), "=&s"(pwu) : "s"(ka));
      const int ql = l & 15;
      const f32x4 z4 = {0.f, 0.f, 0.f, 0.f}, ninf = {-INFINITY, -INFINITY, -INFINITY, -INFINITY};
      LDS_AS unsigned* rdy = (LDS_AS unsigned*)(smem + L_RDY);
      const unsigned rtag = T[0] + it;
      u32x4 qf[2];
      f32x4 cn[2], oa[2][2], sum[2], S[2][2];
      bool told = false;
      auto announce = [&](bool now) __attribute__((always_inline)) {
        if (!told) {
          unsigned st;
          asm volatile("s_getreg_b32 %0, hwreg(HW_REG_IB_STS)" : "=s"(st)::"memory");
          if (now || !(st & 0xC0000Fu)) {
            DRAIN();
            if (l == 0) st8(T, rs, rt * SKW, F_KV + (it * 256 + p * 32 + h) * SKV);
            told = true;
            STAMPAT(34);
          }
        }
      };
      unsigned one2 = 0x3F803F80u;
      asm volatile("" : "+v"(one2));
      const u32x4 ones = {one2, one2, one2, one2};
      auto ex8 = [&](f32x4 a, f32x4 b) __attribute__((always_inline)) {
        return u32x4{pack2(__builtin_amdgcn_exp2f(a[0]), __builtin_amdgcn_exp2f(a[1])), pack2(__builtin_amdgcn_exp2f(a[2]), __builtin_amdgcn_exp2f(a[3])),
                     pack2(__builtin_amdgcn_exp2f(b[0]), __builtin_amdgcn_exp2f(b[1])), pack2(__builtin_amdgcn_exp2f(b[2]), __builtin_amdgcn_exp2f(b[3]))};
      };
      {
#pragma unroll
        for (int a = 0; a < 2; ++a) {
          const f32x4 c4 = {qs2[a][0], qs2[a][1], qs2[a][0], qs2[a][1]};
          qf[a] = pack8(acc[a][0] * c4, acc[a][1] * c4);
        }
        f32x4 mk;
#pragma unroll
        for (int e = 0; e < 4; ++e) mk[e] = (4 * g + e <= ql) ? 0.f : -INFINITY;
        const f32x4 d0 = mfma16(k0, qf[0], mk), d1 = mfma16(k1, qf[1], mk), d2 = mfma16(k0, qf[1], z4);
        const float m0 = -max_x32(max_x16(fmaxf(fmaxf(d0[0], d0[1]), fmaxf(d0[2], d0[3]))));
        const float m1 = -max_x32(max_x16(fmaxf(fmaxf(d1[0], d1[1]), fmaxf(d1[2], d1[3]))));
        cn[0] = f32x4{m0, m0, m0, m0}; cn[1] = f32x4{m1, m1, m1, m1};
        asm volatile("" : "+v"(cn[0]), "+v"(cn[1]));
        announce(false);
        const f32x4 e0 = d0 + cn[0];
        const u32x4 p0 = {pack2(__builtin_amdgcn_exp2f(e0[0]), __builtin_amdgcn_exp2f(e0[1])), pack2(__builtin_amdgcn_exp2f(e0[2]), __builtin_amdgcn_exp2f(e0[3])), 0u, 0u};
        const u32x4 p1 = ex8(d2 + cn[1], d1 + cn[1]);
        oa[0][0] = mfma16(v0, p0, z4); oa[0][1] = mfma16(v1, p0, z4); sum[0] = mfma16(ones, p0, z4);
        oa[1][0] = mfma16(v0, p1, z4); oa[1][1] = mfma16(v1, p1, z4); sum[1] = mfma16(ones, p1, z4);
      }
      const char* kb_ = smem + L_K + l * 16;
      const char* vb_ = smem + L_V + l * 16;
      const unsigned wov = WO_VOFF(), fnv = (l >> 1) * (F_ * 2) + (l & 1) * 16;
      asm volatile("" : "+v"(oa[0][0]), "+v"(oa[0][1]), "+v"(oa[1][0]), "+v"(oa[1][1]), "+v"(sum[0]), "+v"(sum[1]));
      const unsigned pw = __builtin_amdgcn_readfirstlane(it * 256 + p * 32 + h - 32), pfo = F_KV + pw * SKV + rt * SKW;
      const bool fetcher = rt < 4 && p > 0;
      announce(false);
      STAMPAT(32);
      BAR();
      asm volatile("" : "+s"(pwo), "+s"(pwg), "+s"(pwu));
      STAMPAT(33);
#define KV_FETCH(gd)                                                                                                          \
  {                                                                                                                           \
    const unsigned pvo = OFF_KV + pw * 32768 + (gd) * 4096;                                                                   \
    dma16s<0>(rs, l * 16, pvo, lbase + L_K + (gd) * 2048); dma16s<1024>(rs, l * 16, pvo, lbase + L_K + (gd) * 2048);           \
    dma16s<0>(rs, l * 16, pvo + 2048, lbase + L_V + (gd) * 2048); dma16s<1024>(rs, l * 16, pvo + 2048, lbase + L_V + (gd) * 2048); \
  }
      auto fetch = [&]() __attribute__((always_inline)) {
        poll_flags(rs, KVJ ? (l & 1) * 4 * SKW : 0, pfo, T);
        KV_FETCH(rt);
        if (KVJ) { KV_FETCH(rt + 4); if (l == 0) rdy[8 + rt] = rtag; }
        STAMPAT(35);
      };
      announce(false);
      const bool first = fetcher && told;
      if (first) fetch();
#define LDS16(a_) (*(const LDS_AS u32x4*)(size_t)(a_))
      u32x4 kf[2], vf[2], p0, p1;
#define AE(a, i) { S[a][(i) >> 2][(i) & 3] = __builtin_amdgcn_exp2f(S[a][(i) >> 2][(i) & 3]); asm volatile("" : "+v"(S[a][(i) >> 2][(i) & 3])); SB(); }
#define AC(a, P, j) { P[j] = pack2(S[a][(j) >> 1][2 * ((j) & 1)], S[a][(j) >> 1][2 * ((j) & 1) + 1]); asm volatile("" : "+v"(P[j])); SB(); }
#define PHASE(a, P, M1, M2, L2, M3, M4, L4, M5)                                                                               \
  AE(a, 0) AE(a, 1) M1; SB(); AE(a, 2) AE(a, 3) M2; SB(); L2; SB(); AC(a, P, 0) AC(a, P, 1) AE(a, 4) M3; SB(); AE(a, 5) AE(a, 6) M4; SB(); \
  L4; SB(); AE(a, 7) AC(a, P, 2) M5; SB(); AC(a, P, 3)
#define PV(a, d, P) oa[a][d] = mfma16(vf[d], P, oa[a][d])
#define RS(a, P) sum[a] = mfma16(ones, P, sum[a])
#define SC(a, t, c) S[a][t] = mfma16(kf[t], qf[a], c)
#define K_AT(ad) (kf[0] = LDS16(ad), kf[1] = LDS16((ad) + 1024))
#define V_AT(ad) (vf[0] = LDS16((ad) + L_V), vf[1] = LDS16((ad) + L_V + 1024))
#define A_FIRST(ad, c00, c01, c10, c11, KN) { K_AT(ad); V_AT(ad); SB(); SC(0, 0, c00); SC(0, 1, c01); SB(); PHASE(0, p0, SC(1, 0, c10), SC(1, 1, c11), KN, , , , ) }
#define A_NEXT(ad, c00, c01, c10, c11, KN)                                                                                        \
  { PHASE(1, p1, PV(0, 0, p0), PV(0, 1, p0), , SC(0, 0, c00), SC(0, 1, c01), , RS(0, p0))                                     \
    PHASE(0, p0, PV(1, 0, p1), PV(1, 1, p1), V_AT(ad), SC(1, 0, c10), SC(1, 1, c11), KN, RS(1, p1)) }
#define A_LAST() { PHASE(1, p1, PV(0, 0, p0), PV(0, 1, p0), , RS(0, p0), , , ) SB(); PV(1, 0, p1); PV(1, 1, p1); RS(1, p1); }
      int nw = 0;
      auto piece = [&]() __attribute__((always_inline)) {
        if constexpr (it == 0) { if (ffn_on && nw < 8) { FFN_DMA(nw); } ++nw; }
      };
      unsigned ad = lbase + l * 16 + (6 + rt) * 2048;
      asm volatile("" : "+v"(ad));
      if (rt > 0) {
        A_FIRST(ad + 2048, cn[0], cn[0], cn[1], cn[1], K_AT(ad));
#pragma unroll 1
        for (int i = rt - 1; i > 0; --i) {
          ad -= 2048;
          asm volatile("" : "+v"(ad));
          if (PCE && told && rt >= 4) piece();
          announce(false);
          A_NEXT(ad + 2048, cn[0], cn[0], cn[1], cn[1], K_AT(ad));
        }
        announce(false);
        A_LAST();
      }
      announce(true);
      STAMPAT(26);
      {
        f32x4 mk;
#pragma unroll
        for (int e = 0; e < 4; ++e) mk[e] = (4 * g + e > ql) ? 0.f : -INFINITY;
        f32x4 cm0 = cn[0] + mk, cm1 = cn[1] + mk, cmi = ninf;
        asm volatile("" : "+v"(cm0), "+v"(cm1), "+v"(cmi));
        if (rt >= 4) {
          if (p > 0) {
            while (!__all(__hip_atomic_load(rdy + 8 + (l & 3), __ATOMIC_RELAXED, __HIP_MEMORY_SCOPE_WORKGROUP) == rtag)) __builtin_amdgcn_s_sleep(1);
            asm volatile("" ::: "memory");
          }
          if constexpr (it == 0) { for (; nw < 8;) piece(); }
          static_for<it ? 12 : 8>([&](auto c_) __attribute__((always_inline)) { constexpr int i = decltype(c_)::value; WO_PIECE_W(15 - i / 2, 2 * (rt & 3) + (i & 1)); });
        } else if (p > 0) {
          if (!first) fetch();
          if (KVJ) {
            DRAIN();
            if (l < 2) rdy[rt + 4 * l] = rtag;
            if constexpr (it == 0) { for (; nw <= rt;) piece(); }
          } else {
            const u32x2 f2 = ld8(rs, 0, pfo + 4 * SKW);
            const bool up = __all(f2[0] == T[0] && f2[1] == T[1]);
            DRAIN();
            if (l == 0) rdy[rt] = rtag;
            if (!up) poll_flags(rs, 0, pfo + 4 * SKW, T);
            KV_FETCH(rt + 4);
            if (l == 0) rdy[8 + rt] = rtag;
            told = false;
          }
        }
        STAMPAT(3);
        if (p > 0) {
          auto landed = [&](bool now) __attribute__((always_inline)) {
            if (!told) {
              unsigned st;
              asm volatile("s_getreg_b32 %0, hwreg(HW_REG_IB_STS)" : "=s"(st)::"memory");
              if (now || !(st & 0xC0000Fu)) {
                DRAIN();
                if (l == 0) rdy[rt + 4] = rtag;
                told = true;
              }
            }
          };
          unsigned have = 0;
          auto need = [&](int gq) __attribute__((always_inline)) {
            if (__builtin_expect(gq < 8 && !((have >> gq) & 1), 0)) {
              do {
                const unsigned t = __hip_atomic_load(rdy + (l & 7), __ATOMIC_RELAXED, __HIP_MEMORY_SCOPE_WORKGROUP);
                have = (unsigned)__builtin_amdgcn_ballot_w64(t == rtag);
              } while (!((have >> gq) & 1));
              asm volatile("" ::: "memory");
            }
          };
          ad = lbase + l * 16 + rt * 2048;
          asm volatile("" : "+v"(ad));
          need(rt);
          K_AT(ad); V_AT(ad); SB(); SC(0, 0, cm0); SC(0, 1, cn[0]); SB();
          PHASE(0, p0, SC(1, 0, cmi), SC(1, 1, cm1), , , , , )
          if (!KVJ) landed(false);
          need(rt + 1);
          K_AT(ad + 2048);
#pragma unroll 1
          for (int gq = rt + 1; gq < 8; ++gq) {
            ad += 2048;
            asm volatile("" : "+v"(ad));
            if (KVJ) piece();
            else { landed(gq >= rt + KVD); if (told) { piece(); if (PC2) piece(); } }
            need(gq + 1);
            A_NEXT(ad, cn[0], cn[0], cn[1], cn[1], K_AT(ad + 2048));
          }
          A_LAST();
        }
        if constexpr (it == 0) { for (; nw < 8;) piece(); }
      }
      STAMPAT(25);
      {
        float big = 0.f;
#pragma unroll
        for (int a = 0; a < 2; ++a)
#pragma unroll
          for (int d = 0; d < 2; ++d)
            big = __builtin_elementwise_maximum(__builtin_elementwise_maximum(big, __builtin_elementwise_maximum(fabsf(oa[a][d][0]), fabsf(oa[a][d][1]))),
                                                __builtin_elementwise_maximum(fabsf(oa[a][d][2]), fabsf(oa[a][d][3])));
        bool ok = sum[0][0] >= 0.5f && sum[0][0] < 0x1p64f && sum[1][0] >= 0.5f && sum[1][0] < 0x1p64f && big < INFINITY;
        if (!__all(ok)) {
#pragma unroll
          for (int a = 0; a < 2; ++a) {
            const int qs = 2 * rt + a, qq = 256 + 16 * qs + ql, klo = max(qq - 255, p > 0 ? 0 : 256);
            auto score = [&](int t) __attribute__((always_inline)) {
              f32x4 s = mfma16(t == 31 ? k1 : *(const u32x4*)(kb_ + t * 1024), qf[a], z4);
#pragma unroll
              for (int e = 0; e < 4; ++e) { const int kk = 16 * t + 4 * g + e; s[e] = (kk <= qq && kk >= klo) ? s[e] : -INFINITY; }
              return s;
            };
            float mx = -INFINITY;
#pragma unroll 1
            for (int t = p > 0 ? qs : 16; t <= qs + 16; ++t) { const f32x4 s = score(t); mx = fmaxf(fmaxf(mx, fmaxf(s[0], s[1])), fmaxf(s[2], s[3])); }
            mx = max_x32(max_x16(mx));
            oa[a][0] = z4; oa[a][1] = z4; sum[a] = z4;
#pragma unroll 1
            for (int G = p > 0 ? qs >> 1 : 8; G <= (qs + 16) >> 1; ++G) {
              const f32x4 s0 = score(2 * G), s1 = score(2 * G + 1);
              f32x4 e0, e1;
#pragma unroll
              for (int e = 0; e < 4; ++e) { e0[e] = __builtin_amdgcn_exp2f(s0[e] - mx); e1[e] = __builtin_amdgcn_exp2f(s1[e] - mx); }
              const u32x4 pf = pack8(e0, e1);
              oa[a][0] = mfma16(*(const u32x4*)(vb_ + G * 2048), pf, oa[a][0]);
              oa[a][1] = mfma16(*(const u32x4*)(vb_ + G * 2048 + 1024), pf, oa[a][1]);
              sum[a] = mfma16(ones, pf, sum[a]);
            }
          }
        }
      }
#pragma unroll
      for (int a = 0; a < 2; ++a) {
        const float inv = __builtin_amdgcn_rcpf(sum[a][0]);
        oa[a][0] *= inv; oa[a][1] *= inv;
      }
      const unsigned oo = OFF_O + it * (4u << 20) + ((p * 8 + rt) * 64 + h * 2) * 1024;
      st16(pack8(oa[0][0], oa[0][1]), rs, l * 16, oo);
      st16(pack8(oa[1][0], oa[1][1]), rs, l * 16 + 1024, oo);
      STAMPAT(4);
      DRAIN();
      BAR();
      if (tid == 0) st8(T, rs, h * SO, F_O + (it * 8 + p) * 32 * SO);
      if constexpr (it == 0) {
        DEF4(gv); DEF4(uv);
        if (ffn_on) {
          const unsigned ro = 65536 + w * 1024 + (4 * g + ((l & 15) >> 2)) * 32 + (l & 3) * 8;
#pragma unroll
          for (int i = 0; i < 4; ++i) {
            frag_t f;
            f.hf.a = trread(smem, ro + i * 16384); f.hf.b = trread(smem, ro + i * 16384 + 512);
            gv[i] = f.v;
            f.hf.a = trread(smem, ro + i * 16384 + 8192); f.hf.b = trread(smem, ro + i * 16384 + 8704);
            uv[i] = f.v;
          }
        }
        PINV4(gv); PINV4(uv);
        WO_PIECE(11); WO_PIECE(10); WO_PIECE(9); WO_PIECE(8); WO_PIECE(7); WO_PIECE(6); WO_PIECE(5); WO_PIECE(4);
      }
      WO_PIECE(3); WO_PIECE(2); WO_PIECE(1); WO_PIECE(0);
    }
    STAMPAT(5);
    IDS();
    ARGP(pwr, 4); ARGP(pwd, 7); ARGP(pout, 8); ARGP(px, 0);
    const int kh = w >> 2, rt2 = rb * 4 + (w & 3);
    const unsigned R = rt2 * 32 + kh * 16 + (l & 15);
    const unsigned to = (rt2 * 64 + ct * 4 + kh) * 1024 + l * 16;
    const unsigned rmo = ct * 64 + ((g & 1) ? 12 + 4 * g : 4 * g);
    f32x2 wv[8];
    {
      u32x4 wa[2], wrp[2], res[2];
#pragma unroll
      for (int j = 0; j < 2; ++j) {
        wrp[j] = *(const GAS u32x4*)(pwr + rmo + 32 * j);
        wa[j] = *(const GAS u32x4*)(pwr + l * 8 + j * 512);
        if constexpr (it == 0) res[j] = *(const GAS u32x4*)(px + R * 1024 + rmo + 32 * j);
      }
      {
        f32x2 t[8];
        unpack8v(wa[0], t); unpack8v(wa[1], t + 4);
        const f32x2 s = ((t[0] + t[1]) + (t[2] + t[3])) + ((t[4] + t[5]) + (t[6] + t[7]));
        wsum = sum64(hadd(s));
        unpack8v(trade16(wrp[0]), wv); unpack8v(trade16(wrp[1]), wv + 4);
        if constexpr (it == 0) { res[0] = trade16(res[0]); res[1] = trade16(res[1]); asm volatile("" : "+v"(res[0]), "+v"(res[1])); }
        asm volatile("" : "+v"(wsum), "+v"(wv[0]), "+v"(wv[1]), "+v"(wv[2]), "+v"(wv[3]), "+v"(wv[4]), "+v"(wv[5]), "+v"(wv[6]), "+v"(wv[7]));
      }
      unsigned bt[4];
#pragma unroll
      for (int n = 0; n < 4; ++n) {
        bt[n] = lbase + 32768 + 65536 + kh * 4096 + (4 * g + ((l & 15) >> 2)) * 128 + (l & 3) * 8 + ((n ^ (2 * (g & 1) + ((l & 15) >> 3))) << 5);
        asm volatile("" : "+v"(bt[n]));
      }
      if (w == 0) poll_flags(rs, (l & 31) * SO, F_O + (it * 8 + (rb >> 1)) * 32 * SO, T);
      else if (it == 0 && w < 3) poll_flags2(rs, F_XN + (((w - 1) * 4 + g) * RX + bi % RX) * 256 + (l & 15) * 16, T);
      asm volatile("s_barrier" ::: "memory");
      STAMPAT(6);
      const unsigned abase = OFF_O + it * (4u << 20) + (rt2 * 64 + 32 * kh) * 1024;
      const unsigned av0 = l * 16 + kh * 1024, av1 = l * 16 + (kh ^ 1) * 1024;
      u32x4 af[WB][2];
#define O_LOAD(s_) { af[(s_) % WB][0] = ld16(rs, av0 + (~(s_) & 1) * 2048, abase + ((15 - (s_)) >> 1) * 4096); af[(s_) % WB][1] = ld16(rs, av1 + (~(s_) & 1) * 2048, abase + ((15 - (s_)) >> 1) * 4096); }
#pragma unroll
      for (int s = 0; s < WR; ++s) O_LOAD(s);
#pragma unroll
      for (int j = 0; j < 2; ++j) {
        if constexpr (it == 1) res[j] = ld16(rs, OFF_XNEW + (2 * ct + j) * 256 + g * 16, 0);
      }
      SB();
      f32x4 ac[2][4];
#pragma unroll
      for (int a = 0; a < 2; ++a)
#pragma unroll
        for (int n = 0; n < 4; ++n) ac[a][n] = f32x4{0.f, 0.f, 0.f, 0.f};
      u32x4 bfr[2][4];
#define WO_RD(buf, s, n)                                                                             \
  {                                                                                                  \
    const unsigned o_ = bt[n] + ((15 - (s)) & 7) * 8192;                                             \
    frag_t f_;                                                                                       \
    f_.hf.a = trabs(o_); f_.hf.b = trabs(o_ + 2048);                                                 \
    bfr[buf][n] = f_.v;                                                                              \
  }
#pragma unroll
      for (int n = 0; n < 4; ++n) WO_RD(0, 0, n);
#pragma unroll
      for (int s = 0; s < 16; ++s) {
        const int cur = s & 1;
        asm volatile("" : "+{v[176:179]}"(ac[0][0]), "+{v[180:183]}"(ac[0][1]), "+{v[184:187]}"(ac[0][2]), "+{v[188:191]}"(ac[0][3]),
                          "+{v[192:195]}"(ac[1][0]), "+{v[196:199]}"(ac[1][1]), "+{v[200:203]}"(ac[1][2]), "+{v[204:207]}"(ac[1][3]));
        if (s == (it == 0 ? 3 : 11)) asm volatile("s_barrier" ::: "memory");
        if (s == 7) {
#pragma unroll
          for (int n = 0; n < 4; ++n) { bt[n] -= 65536; asm volatile("" : "+v"(bt[n])); }
        }
        if (it == 0 && s == 8) {
#pragma unroll
          for (int i = 0; i < 3; ++i) st16(keep[i], rs, l * 16 + i * 1024, OFF_WI + (h * 8 + ((ROT0 + 5) & 7)) * STAGE + w * 3072);
        }
        if (it == 0 && s == 10) {
          unsigned pz1 = POISON;
          asm volatile("" : "+v"(pz1));
          const u32x4 pz = {pz1, pz1, pz1, pz1};
          st16(pz, rs, OFF_PART + (tid >> 8) * (1u << 20) + bid * 4096 + (tid & 255) * 16, 0);
          if (w < 2) st16(pz, rs, OFF_STAT + 524288 + (rb * 16 + ct) * 2048 + tid * 16, 0);
          if (w == 4 && l < 40) st16(pz, rs, l < 16 ? OFF_CA + 65536 + rb * 4096 + ct * 256 + l * 16 : OFF_FX + (h * 8 + p) * 512 + (l - 16) * 16, 0);
          if (w == 6 && l < (ct < (int)RKEY ? 3 : 2)) st4(pz1, rs, l < 2 ? OFF_XNEW + (bid >> 3) * 256 + (bid & 7) * 8 + l * 4 : KEY_AT(1, ct) + rb * SKEY, 0);
        }
        if (s < WB - WR) O_LOAD(WR + 2 * s < 16 ? WR + 2 * s : 0);
        SB();
#pragma unroll
        for (int n = 0; n < 4; ++n) {
          ac[0][n] = mfma16(bfr[cur][n], af[s % WB][0], ac[0][n]);
          ac[1][n] = mfma16(bfr[cur][n], af[s % WB][1], ac[1][n]);
          if (s + 1 < 16) WO_RD(cur ^ 1, s + 1, n);
          if (s == 9) {
            if (it == 1 && n == 0) { res[0] = R == sel0 ? res[0] : x2p[0]; res[1] = R == sel0 ? res[1] : x2p[1]; }
            v[2 * n] = f32x2{lo16(res[n >> 1][2 * (n & 1)]), hi16(res[n >> 1][2 * (n & 1)])};
            v[2 * n + 1] = f32x2{lo16(res[n >> 1][2 * (n & 1) + 1]), hi16(res[n >> 1][2 * (n & 1) + 1])};
            asm volatile("" : "+v"(v[2 * n]), "+v"(v[2 * n + 1]));
          }
          SB();
        }
        if (s < WB - WR) { O_LOAD(WR + 2 * s + 1); }
        else if (s + WB < 16) { O_LOAD(s + WB); }
        SB();
      }
#undef WO_RD
#undef O_LOAD
      STAMPAT(7);
#define XCH_AT(v) ((v) == 7 ? 155648 : (v) * 4096)
#pragma unroll
      for (int n = 0; n < 4; ++n) *(f32x4*)(smem + XCH_AT(w) + n * 1024 + l * 16) = ac[1][n];
#pragma unroll
      for (int n = 0; n < 4; ++n) { v[2 * n] += f32x2{ac[0][n][0], ac[0][n][1]}; v[2 * n + 1] += f32x2{ac[0][n][2], ac[0][n][3]}; }
      BAR();
#pragma unroll
      for (int n = 0; n < 4; ++n) {
        const f32x4 o = *(const f32x4*)(smem + XCH_AT(w ^ 4) + n * 1024 + l * 16);
        v[2 * n] += f32x2{o[0], o[1]}; v[2 * n + 1] += f32x2{o[2], o[3]};
      }
    }
    unsigned crow = 0;
    {
      if (it == 0) { st16(pack8v(v), rs, to, OFF_XN + (4u << 20)); st16(pack8v(v + 4), rs, to + 2048, OFF_XN + (4u << 20)); }
      SB();
      const f32x2 s = ((v[0] + v[1]) + (v[2] + v[3])) + ((v[4] + v[5]) + (v[6] + v[7]));
      f32x2 dp = v[0] * wv[0], q = v[0] * v[0];
#pragma unroll
      for (int j = 1; j < 8; ++j) { dp = __builtin_elementwise_fma(v[j], wv[j], dp); q = __builtin_elementwise_fma(v[j], v[j], q); }
      float mt = hadd(s), qq = hadd(q), dot = hadd(dp);
      add_g3(mt, qq, dot);
      const unsigned so = OFF_STAT + it * 524288 + rb * 32768, lrow = R & 127;
      if (it == 0) wait_vm<2>();
      if (g == 0) st16(unpoison4(__builtin_bit_cast(u32x4, f32x4{mt, qq, dot, 0.f})), rs, ct * 2048 + lrow * 16, so);
      SB();
      if (it == 0) wait_vm<1>();
      if constexpr (it == 0) {
        volatile LDS_AS u32x2* in = (volatile LDS_AS u32x2*)(smem + L_XC);
        if (l == 0) in[w] = T;
        const u32x2 t = in[l & 7];
        if (__all(t[0] == T[0] && t[1] == T[1]) && l < (int)RX1) st8(T, rs, (l * 32 + (rb & 1) * 16 + ct) * SX1, F_X1 + (rb >> 1) * RX1 * 32 * SX1);
      }
      STAMPAT(8);
      if (it == 0) {
        if (ffn_on) { WD_DMA(); }
#pragma unroll
        for (int j = 0; j < 9; ++j) *(u32x4*)(smem + L_RING + (j / 3) * STAGE + (w * 3 + j % 3) * 1024 + l * 16) = keep[j];
      }
      STAMPAT(16);
      if constexpr (it == 0) {
        if (w == 0) poll_flags(rs, ((l & 2) * 8 + ((ROT0 + 5) & 7) * 2 + (l & 1)) * SX1, F_X1 + (p * RX1 + (xcd >> 1) % RX1) * 32 * SX1, T);
        if (tid == 192) *(LDS_AS u32x4*)(smem + L_QC) = u32x4{0u, 0u, 0u, 0u};
        BAR();
        PRE_LOAD(1, ROT0 + 5, 0, 4);
        STAMPAT(9);
      } else {
        __builtin_amdgcn_s_sleep(SDIR);
        u32x4 sp[4];
#pragma unroll
        for (int i = 0; i < 4; ++i) sp[i] = ld16(rs, (4 * g + i) * 2048 + lrow * 16, so);
        const RowFin r = fin_key(rs, T, g * 8192 + lrow * 16, so, smem + L_XC, l, w, R, wsum, sp);
        STAMPAT(9);
        BAR();
        crow = fin_cand(rs, smem + L_XC, 1, g, rb, ct, r, v);
        STAMPAT(10);
        fin_x2(r, v);
      }
    }
    STAMPAT(11);
    if (it == 1) {
      u32x4 op[2];
      {
        char* tp = smem + L_OUT + w * 2048;
        const unsigned c0 = (g & 1) * 2 + (g >> 1), r = l & 15;
        *(u32x4*)(tp + r * 128 + ((c0 ^ (r & 7)) << 4)) = trade16(pack8v(v));
        *(u32x4*)(tp + r * 128 + (((c0 + 4) ^ (r & 7)) << 4)) = trade16(pack8v(v + 4));
#pragma unroll
        for (int j = 0; j < 2; ++j) op[j] = *(const u32x4*)(tp + j * 1024 + (l >> 3) * 128 + (((l ^ (l >> 3)) & 7) << 4));
      }
      const unsigned Ro = R - (l & 15) + (l >> 3), oo = (Ro * 1024 + ct * 64) * 2 + (l & 7) * 16;
#define OUT_STORE(c0, c1)                                                                           \
  {                                                                                                 \
    if (c0) __builtin_amdgcn_raw_buffer_store_b128(op[0], rout, oo, 0, SC1);                        \
    if (c1) __builtin_amdgcn_raw_buffer_store_b128(op[1], rout, oo + 16384, 0, SC1);                \
  }
      if (OUTE) OUT_STORE(Ro != crow, Ro + 8 != crow);
      if (w == 0) {
        __builtin_amdgcn_s_sleep(GSL);
        u32x4 xr[4];
        unsigned cur = 0, best;
        asm volatile("" : "=v"(xr[0]), "=v"(xr[1]));
        for (unsigned spins = 0;;) {
          const unsigned k_ = key_load(rs, l, 1, KREP());
          const bool all = __all(k_ != POISON);
          best = __builtin_amdgcn_readfirstlane(umax16(k_ == POISON ? 0u : k_));
          if (best != cur && ffn_on) {
            cur = best;
            ld16h<0>(xr[0], rs, (l >> 3) * 256 + (l & 7) * 16, cand_off(cur, 1)); ld16h<2048>(xr[1], rs, (l >> 3) * 256 + (l & 7) * 16, cand_off(cur, 1));
          }
          if (all || spin_fail(spins, rs, T)) break;
        }
        STAMPAT(17);
        if (l == 0) *(LDS_AS unsigned*)(smem + L_SKEY + 32) = best;
        asm volatile("s_waitcnt vmcnt(0)" : "+v"(xr[0]), "+v"(xr[1])::"memory");
        if (ffn_on) {
          word_wait<2>(rs, T, (l >> 3) * 256 + (l & 7) * 16, cand_off(best, 1), xr);
          *(LDS_AS u32x4*)(smem + L_ROW + l * 16) = xr[0]; *(LDS_AS u32x4*)(smem + L_ROW + 1024 + l * 16) = xr[1];
        }
      }
      BAR();
      const unsigned key = __builtin_amdgcn_readfirstlane(*(LDS_AS unsigned*)(smem + L_SKEY + 32)), krow = key_row(key);
      STAMPAT(12);
      if (ffn_on) {
        u32x4 xv[4];
#pragma unroll
        for (int i = 0; i < 4; ++i) xv[i] = *(const LDS_AS u32x4*)(smem + L_ROW + w * 256 + i * 64 + g * 16);
        f32x4 ga, ua;
        ffn_gu(gv, uv, xv, ga, ua);
        ffn_gu_store(smem, l, w, ga, ua);
        BAR();
        ffn_d_store(rs, l, w, 1, slice, ffn_d_all(dfr, ffn_d_h(ffn_d_read(smem, l))));
        STAMPAT(13);
        SB();
        if (OSL) __builtin_amdgcn_s_sleep(OSL);
      }
      if (OUTE) OUT_STORE(Ro == crow && Ro != krow, Ro + 8 == crow && Ro + 8 != krow)
      else OUT_STORE(Ro != krow, Ro + 8 != krow);
      if (!ffn_on) {
        const unsigned G = xcd * 4 + bi - 28;
        if (G == 0 && tid == 0) st8(T, rs, F_EP, 0);
        u32x4 pv[4];
        u32x2 cxn, cx1;
        __builtin_amdgcn_s_sleep(RSL);
        red_load(rs, tid, 1, G, key, pv, cxn, cx1);
        red_sum(rs, smem, T, tid, w, 1, G, key, pv, cxn, cx1);
        STAMPAT(14);
        BAR();
        if (w == 0) {
          const u32x2 ov = red_final(smem, l, key, cxn, cx1);
          if (l < 8) __builtin_amdgcn_raw_buffer_store_b64(ov, rout, (krow * 1024 + G * 32 + l * 4) * 2, 0, 0);
        }
      }
#undef OUT_STORE
    }
    STAMPAT(15); STAMPAT(23);
  };
  layer(std::integral_constant<int, 0>{});
  layer(std::integral_constant<int, 1>{});
}

extern "C" void kernel_launch(void* const* d_in, const int* in_sizes, int n_in, void* d_out, int out_size,
                              void* d_ws, size_t ws_size, hipStream_t stream) {
  (void)hipFuncSetAttribute((const void*)mega_kernel, hipFuncAttributeMaxDynamicSharedMemorySize, LDS_BYTES);
  mega_kernel<<<256, 512, LDS_BYTES, stream>>>((const bf16*)d_in[0], (char*)d_ws, (const bf16*)d_in[1], (const bf16*)d_in[2], (const bf16*)d_in[4],
                                               (const bf16*)d_in[5], (const bf16*)d_in[6], (const bf16*)d_in[7], (bf16*)d_out);
}
```

```cpp
#include <hip/hip_runtime.h>
#include <stdint.h>
#include <utility>

typedef __bf16 bf16;
typedef __bf16 bf16x8 __attribute__((ext_vector_type(8)));
typedef __bf16 bf16x2 __attribute__((ext_vector_type(2)));
typedef float f32x4 __attribute__((ext_vector_type(4)));
typedef float f32x2 __attribute__((ext_vector_type(2)));
typedef short s16x4 __attribute__((ext_vector_type(4)));
typedef unsigned int u32x4 __attribute__((ext_vector_type(4)));
typedef unsigned int u32x2 __attribute__((ext_vector_type(2)));
#define LDS_AS __attribute__((address_space(3)))
#define GAS __attribute__((address_space(1)))
typedef GAS unsigned gu32;
typedef GAS unsigned long long gu64;
#define RLX_AGENT __ATOMIC_RELAXED, __HIP_MEMORY_SCOPE_AGENT
typedef __amdgpu_buffer_rsrc_t rsrc_t;

constexpr int F_ = 3584;
constexpr int SC1 = 16;

constexpr int RX = 4;
constexpr unsigned F_EP = 0, F_TMO = 128;
constexpr unsigned F_XN = 4096;
#define FLG 256, 1024, 8, 64, 4, 256, 4
constexpr unsigned FLV[7] = {FLG};
constexpr unsigned SO = FLV[0], SKV = FLV[1], SKW = FLV[2], SX1 = FLV[3], RX1 = FLV[4], SKEY = FLV[5], RKEY = FLV[6];
constexpr unsigned F_O = 8u << 20;
constexpr unsigned F_KV = 11u << 20;
constexpr unsigned F_X1 = 9u << 20;
constexpr unsigned OFF_KEY = 10u << 20;
#define KREP() (RKEY == 16 ? bid & 15 : (bid & 7) * RKEY / 8)
#define KEY_AT(L, rep) (OFF_KEY + ((L) * RKEY + (rep)) * (SKEY < 8 ? 128 : 16 * SKEY))
constexpr unsigned OFF_AB = 2u << 20;
constexpr unsigned OFF_XNEW = 262144;
constexpr unsigned OFF_CS = OFF_AB + 65536;
constexpr unsigned OFF_FX = 393216;
constexpr unsigned OFF_CA = 524288;
constexpr unsigned OFF_STAT = 1u << 20;
constexpr unsigned OFF_PART = 3u << 20;
constexpr unsigned OFF_XN = 8u << 20;
constexpr unsigned OFF_O = 16u << 20;
constexpr unsigned OFF_KV = 32u << 20;
constexpr unsigned OFF_WI = 52u << 20;
constexpr unsigned WS_BYTES = 58u << 20;
constexpr unsigned POISON = 0xFFFFFFFFu;

constexpr int L_ABW = 16640, L_CSP = 17408, L_CSL = 31872, L_FXL = 32256, L_K = 0, L_V = 32768, L_RING = 65536, L_XCH = 0, L_DN = 32768, L_RDY = 31744, L_QC = 31808, LDS_BYTES = 163840;
constexpr int STAGE = 24576;
#define SLOT(t) ((t) & 3)

template <typename F, int... I> __device__ __forceinline__ void static_for_impl(F&& f, std::integer_sequence<int, I...>) {
  (f(std::integral_constant<int, I>{}), ...);
}
template <int N, typename F> __device__ __forceinline__ void static_for(F&& f) { static_for_impl(f, std::make_integer_sequence<int, N>{}); }
__device__ __forceinline__ unsigned pack2(float a, float b) {
  f32x2 v = {a, b};
  return __builtin_bit_cast(unsigned, __builtin_convertvector(v, bf16x2));
}
__device__ __forceinline__ float asf(unsigned u) { return __builtin_bit_cast(float, u); }
__device__ __forceinline__ float lo16(unsigned u) { return __builtin_bit_cast(float, u << 16); }
__device__ __forceinline__ float hi16(unsigned u) { return __builtin_bit_cast(float, u & 0xffff0000u); }
__device__ __forceinline__ u32x4 pack8(const f32x4 a, const f32x4 b) {
  u32x4 o;
  o[0] = pack2(a[0], a[1]); o[1] = pack2(a[2], a[3]); o[2] = pack2(b[0], b[1]); o[3] = pack2(b[2], b[3]);
  return o;
}
__device__ __forceinline__ f32x4 mfma16(u32x4 a, u32x4 b, f32x4 c) {
  return __builtin_amdgcn_mfma_f32_16x16x32_bf16(__builtin_bit_cast(bf16x8, a), __builtin_bit_cast(bf16x8, b), c, 0, 0, 0);
}
__device__ __forceinline__ f32x4 mfma16k16(s16x4 a, s16x4 b, f32x4 c) { return __builtin_amdgcn_mfma_f32_16x16x16bf16_1k(a, b, c, 0, 0, 0); }
template <int CTRL> __device__ __forceinline__ float dppf(float v) {
  return __builtin_bit_cast(float, __builtin_amdgcn_update_dpp(0, __builtin_bit_cast(int, v), CTRL, 0xf, 0xf, false));
}
template <int CTRL> __device__ __forceinline__ unsigned dppu(unsigned v) {
  return (unsigned)__builtin_amdgcn_update_dpp(0, (int)v, CTRL, 0xf, 0xf, false);
}
__device__ __forceinline__ float add_x16(float v) {
  auto r = __builtin_amdgcn_permlane16_swap(__builtin_bit_cast(unsigned, v), __builtin_bit_cast(unsigned, v), false, false);
  return __builtin_bit_cast(float, (unsigned)r[0]) + __builtin_bit_cast(float, (unsigned)r[1]);
}
__device__ __forceinline__ float add_x32(float v) {
  auto r = __builtin_amdgcn_permlane32_swap(__builtin_bit_cast(unsigned, v), __builtin_bit_cast(unsigned, v), false, false);
  return __builtin_bit_cast(float, (unsigned)r[0]) + __builtin_bit_cast(float, (unsigned)r[1]);
}
__device__ __forceinline__ float max_x16(float v) {
  auto r = __builtin_amdgcn_permlane16_swap(__builtin_bit_cast(unsigned, v), __builtin_bit_cast(unsigned, v), false, false);
  return fmaxf(__builtin_bit_cast(float, (unsigned)r[0]), __builtin_bit_cast(float, (unsigned)r[1]));
}
__device__ __forceinline__ float max_x32(float v) {
  auto r = __builtin_amdgcn_permlane32_swap(__builtin_bit_cast(unsigned, v), __builtin_bit_cast(unsigned, v), false, false);
  return fmaxf(__builtin_bit_cast(float, (unsigned)r[0]), __builtin_bit_cast(float, (unsigned)r[1]));
}
__device__ __forceinline__ unsigned umax_x16(unsigned v) {
  auto r = __builtin_amdgcn_permlane16_swap(v, v, false, false);
  return max((unsigned)r[0], (unsigned)r[1]);
}
__device__ __forceinline__ unsigned umax_x32(unsigned v) {
  auto r = __builtin_amdgcn_permlane32_swap(v, v, false, false);
  return max((unsigned)r[0], (unsigned)r[1]);
}
__device__ __forceinline__ float sum_hi8(float v) {
  v += dppf<0x128>(v);
  v = add_x16(v);
  return add_x32(v);
}
__device__ __forceinline__ float sum64(float v) {
  v += dppf<0xB1>(v);
  v += dppf<0x4E>(v);
  v += dppf<0x124>(v);
  v += dppf<0x128>(v);
  v = add_x16(v);
  return add_x32(v);
}

#define BAR() asm volatile("s_waitcnt lgkmcnt(0)\n\ts_barrier" ::: "memory")
#define DRAIN() asm volatile("s_waitcnt vmcnt(0)" ::: "memory")
#define SB() __builtin_amdgcn_sched_barrier(0)
template <int N> __device__ __forceinline__ void wait_vm() { asm volatile("s_waitcnt vmcnt(%0)" ::"n"(N) : "memory"); }

__device__ __forceinline__ void dma16(rsrc_t rs, unsigned voff, unsigned soff, unsigned lds) {
  asm volatile("s_mov_b32 m0, %0\n\ts_nop 0\n\tbuffer_load_dwordx4 %1, %2, %3 offen lds" ::"s"(lds), "v"(voff), "s"(rs), "s"(soff) : "memory");
}
template <int OFF> __device__ __forceinline__ void dma16s(rsrc_t rs, unsigned voff, unsigned soff, unsigned lds) {
  asm volatile("s_mov_b32 m0, %0\n\ts_nop 0\n\tbuffer_load_dwordx4 %1, %2, %3 offen offset:%4 sc1 lds" ::"s"(lds), "v"(voff), "s"(rs), "s"(soff), "n"(OFF) : "memory");
}
__device__ __forceinline__ float rbf1(float x) { return __builtin_bit_cast(float, pack2(0.f, x)); }
__device__ __forceinline__ void vswap(unsigned& a, unsigned& b) { asm volatile("v_swap_b32 %0, %1" : "+v"(a), "+v"(b)); }
__device__ __forceinline__ void vswap(float& a, float& b) { asm volatile("v_swap_b32 %0, %1" : "+v"(a), "+v"(b)); }
template <typename V, int N> __device__ __forceinline__ void vswapn(V& a, V& b) {
#pragma unroll
  for (int i = 0; i < N; ++i) { auto x = a[i], y = b[i]; vswap(x, y); a[i] = x; b[i] = y; }
}
__device__ __forceinline__ u32x4 ld16(rsrc_t rs, unsigned voff, unsigned soff) { return __builtin_amdgcn_raw_buffer_load_b128(rs, voff, soff, SC1); }
template <int OFF> __device__ __forceinline__ void ld16h(u32x4& d, rsrc_t rs, unsigned voff, unsigned soff) {
  asm volatile("buffer_load_dwordx4 %0, %1, %2, %3 offen offset:%4 sc1" : "=v"(d) : "v"(voff), "s"(rs), "s"(soff), "n"(OFF) : "memory");
}
template <int I, int OFF> __device__ __forceinline__ void ld16w(u32x4& d, rsrc_t rs, unsigned voff, unsigned soff) {
  if constexpr (I == 0) asm volatile("buffer_load_dwordx4 %0, %1, %2, %3 offen offset:%4 sc1" : "={v[128:131]}"(d) : "v"(voff), "s"(rs), "s"(soff), "n"(OFF) : "memory");
  else if constexpr (I == 1) asm volatile("buffer_load_dwordx4 %0, %1, %2, %3 offen offset:%4 sc1" : "={v[132:135]}"(d) : "v"(voff), "s"(rs), "s"(soff), "n"(OFF) : "memory");
  else if constexpr (I == 2) asm volatile("buffer_load_dwordx4 %0, %1, %2, %3 offen offset:%4 sc1" : "={v[136:139]}"(d) : "v"(voff), "s"(rs), "s"(soff), "n"(OFF) : "memory");
  else if constexpr (I == 3) asm volatile("buffer_load_dwordx4 %0, %1, %2, %3 offen offset:%4 sc1" : "={v[140:143]}"(d) : "v"(voff), "s"(rs), "s"(soff), "n"(OFF) : "memory");
  else if constexpr (I == 4) asm volatile("buffer_load_dwordx4 %0, %1, %2, %3 offen offset:%4 sc1" : "={v[144:147]}"(d) : "v"(voff), "s"(rs), "s"(soff), "n"(OFF) : "memory");
  else if constexpr (I == 5) asm volatile("buffer_load_dwordx4 %0, %1, %2, %3 offen offset:%4 sc1" : "={v[148:151]}"(d) : "v"(voff), "s"(rs), "s"(soff), "n"(OFF) : "memory");
  else if constexpr (I == 6) asm volatile("buffer_load_dwordx4 %0, %1, %2, %3 offen offset:%4 sc1" : "={v[152:155]}"(d) : "v"(voff), "s"(rs), "s"(soff), "n"(OFF) : "memory");
  else if constexpr (I == 7) asm volatile("buffer_load_dwordx4 %0, %1, %2, %3 offen offset:%4 sc1" : "={v[156:159]}"(d) : "v"(voff), "s"(rs), "s"(soff), "n"(OFF) : "memory");
  else if constexpr (I == 8) asm volatile("buffer_load_dwordx4 %0, %1, %2, %3 offen offset:%4 sc1" : "={v[160:163]}"(d) : "v"(voff), "s"(rs), "s"(soff), "n"(OFF) : "memory");
  else if constexpr (I == 9) asm volatile("buffer_load_dwordx4 %0, %1, %2, %3 offen offset:%4 sc1" : "={v[164:167]}"(d) : "v"(voff), "s"(rs), "s"(soff), "n"(OFF) : "memory");
  else if constexpr (I == 10) asm volatile("buffer_load_dwordx4 %0, %1, %2, %3 offen offset:%4 sc1" : "={v[168:171]}"(d) : "v"(voff), "s"(rs), "s"(soff), "n"(OFF) : "memory");
  else if constexpr (I == 11) asm volatile("buffer_load_dwordx4 %0, %1, %2, %3 offen offset:%4 sc1" : "={v[172:175]}"(d) : "v"(voff), "s"(rs), "s"(soff), "n"(OFF) : "memory");
}
template <int K> __device__ __forceinline__ void pin_w(u32x4& a, u32x4& b) {
  if constexpr (K == 0) asm volatile("" : "+{v[128:131]}"(a), "+{v[132:135]}"(b)::"memory");
  else if constexpr (K == 1) asm volatile("" : "+{v[136:139]}"(a), "+{v[140:143]}"(b)::"memory");
  else if constexpr (K == 2) asm volatile("" : "+{v[144:147]}"(a), "+{v[148:151]}"(b)::"memory");
  else if constexpr (K == 3) asm volatile("" : "+{v[152:155]}"(a), "+{v[156:159]}"(b)::"memory");
  else if constexpr (K == 4) asm volatile("" : "+{v[160:163]}"(a), "+{v[164:167]}"(b)::"memory");
  else if constexpr (K == 5) asm volatile("" : "+{v[168:171]}"(a), "+{v[172:175]}"(b)::"memory");
}
__device__ __forceinline__ void ld8h(u32x2& d, rsrc_t rs, unsigned voff, unsigned soff) {
  asm volatile("buffer_load_dwordx2 %0, %1, %2, %3 offen sc1" : "=v"(d) : "v"(voff), "s"(rs), "s"(soff) : "memory");
}
template <int OFF> __device__ __forceinline__ void ld4h(unsigned& d, rsrc_t rs, unsigned voff, unsigned soff) {
  asm volatile("buffer_load_dword %0, %1, %2, %3 offen offset:%4 sc1" : "=v"(d) : "v"(voff), "s"(rs), "s"(soff), "n"(OFF) : "memory");
}
__device__ __forceinline__ u32x2 ld8(rsrc_t rs, unsigned voff, unsigned soff) { return __builtin_amdgcn_raw_buffer_load_b64(rs, voff, soff, SC1); }
__device__ __forceinline__ void st16(u32x4 v, rsrc_t rs, unsigned voff, unsigned soff) { __builtin_amdgcn_raw_buffer_store_b128(v, rs, voff + soff, 0, SC1); }
__device__ __forceinline__ void st8(u32x2 v, rsrc_t rs, unsigned voff, unsigned soff) { __builtin_amdgcn_raw_buffer_store_b64(v, rs, voff, soff, SC1); }

__device__ __forceinline__ unsigned ld4(rsrc_t rs, unsigned voff, unsigned soff) { return __builtin_amdgcn_raw_buffer_load_b32(rs, voff, soff, SC1); }
__device__ __forceinline__ void st4(unsigned v, rsrc_t rs, unsigned voff, unsigned soff) { __builtin_amdgcn_raw_buffer_store_b32(v, rs, voff, soff, SC1); }

constexpr unsigned kSpinCap = 1u << 16;
__device__ __forceinline__ bool spin_fail(unsigned& spins, rsrc_t rs, u32x2 T) {
  asm volatile("" ::: "memory");
  ++spins;
  if (spins > kSpinCap) { st8(T, rs, 0, F_TMO); return true; }
  if ((spins & 255u) == 0u) {
    const u32x2 v = ld8(rs, 0, F_TMO);
    if (v[0] == T[0] && v[1] == T[1]) return true;
  }
  __builtin_amdgcn_s_sleep(3);
  return false;
}
__device__ __forceinline__ void poll_flags2(rsrc_t rs, unsigned voff, u32x2 T) {
  for (unsigned spins = 0;;) {
    const u32x4 v = ld16(rs, voff, 0);
    if (__all(v[0] == T[0] && v[1] == T[1] && v[2] == T[0] && v[3] == T[1])) break;
    if (spin_fail(spins, rs, T)) break;
  }
  asm volatile("" ::: "memory");
  __builtin_amdgcn_fence(__ATOMIC_ACQUIRE, "wavefront");
}
__device__ __forceinline__ void tag_wait(rsrc_t rs, u32x2 T, unsigned voff, unsigned soff, u32x4& v) {
  if (__builtin_expect(!__all(v[2] == T[0] && v[3] == T[1]), 0)) {
    unsigned spins = 0;
    do {
      if (spin_fail(spins, rs, T)) break;
      v = ld16(rs, voff, soff);
    } while (!__all(v[2] == T[0] && v[3] == T[1]));
    asm volatile("s_waitcnt vmcnt(0)" : "+v"(v)::"memory");
  }
}
template <int N> __device__ __forceinline__ void word_wait(rsrc_t rs, u32x2 T, unsigned voff, unsigned soff, u32x4 (&v)[4]) {
#define WW_OK() __all(max(max(max(v[0][0], v[0][1]), max(v[0][2], v[0][3])), max(max(v[N - 1][0], v[N - 1][1]), max(v[N - 1][2], v[N - 1][3]))) != POISON)
  if (__builtin_expect(!WW_OK(), 0)) {
    unsigned spins = 0;
    do {
      if (spin_fail(spins, rs, T)) break;
#pragma unroll
      for (int i = 0; i < N; ++i) v[i] = ld16(rs, voff + i * 2048, soff);
    } while (!WW_OK());
    asm volatile("s_waitcnt vmcnt(0)" : "+v"(v[0]), "+v"(v[N - 1])::"memory");
  }
#undef WW_OK
}
__device__ __forceinline__ void poll_flags(rsrc_t rs, unsigned voff, unsigned soff, u32x2 T) {
  for (unsigned spins = 0;;) {
    const u32x2 v = ld8(rs, voff, soff);
    if (__all(v[0] == T[0] && v[1] == T[1])) break;
    if (spin_fail(spins, rs, T)) break;
  }
  asm volatile("" ::: "memory");
  __builtin_amdgcn_fence(__ATOMIC_ACQUIRE, "wavefront");
}

__device__ __forceinline__ unsigned toff(unsigned R, unsigned ks, unsigned g) {
  return (((R >> 5) * 64 + ks * 2 + ((R >> 4) & 1)) << 10) + ((g * 16 + (R & 15)) << 4);
}
__device__ __forceinline__ void unpack8v(const u32x4 u, f32x2* v) {
#pragma unroll
  for (int j = 0; j < 4; ++j) v[j] = f32x2{lo16(u[j]), hi16(u[j])};
}
__device__ __forceinline__ u32x4 pack8v(const f32x2* v) {
  u32x4 o;
#pragma unroll
  for (int j = 0; j < 4; ++j) o[j] = pack2(v[j][0], v[j][1]);
  return o;
}
__device__ __forceinline__ f32x2 rbf1v(f32x2 v) { return f32x2{rbf1(v[0]), rbf1(v[1])}; }
__device__ __forceinline__ float ln_rd(float var) { return __builtin_amdgcn_rcpf(rbf1(__builtin_amdgcn_sqrtf(rbf1(rbf1(var) + 1e-5f)))); }
template <int N> __device__ __forceinline__ void ln_apply(float mb, float rd, const f32x2* v, f32x2* y) {
  const f32x2 mb2 = {mb, mb}, rd2 = {rd, rd};
#pragma unroll
  for (int j = 0; j < N; ++j) y[j] = rbf1v(rbf1v(v[j] - mb2) * rd2);
}
__device__ __forceinline__ float hadd(f32x2 v) {
  float a = v[0];
  asm volatile("" : "+v"(a));
  return a + v[1];
}
__device__ __forceinline__ float add_g(float v) { return add_x32(add_x16(v)); }
__device__ __forceinline__ void add_g3(float& x, float& y, float& z) {
  auto a = __builtin_amdgcn_permlane16_swap(__builtin_bit_cast(unsigned, x), __builtin_bit_cast(unsigned, y), false, false);
  auto b = __builtin_amdgcn_permlane16_swap(__builtin_bit_cast(unsigned, z), __builtin_bit_cast(unsigned, z), false, false);
  const float r = asf(a[0]) + asf(a[1]), t = asf(b[0]) + asf(b[1]);
  auto c = __builtin_amdgcn_permlane32_swap(__builtin_bit_cast(unsigned, r), __builtin_bit_cast(unsigned, r), false, false);
  auto d = __builtin_amdgcn_permlane32_swap(__builtin_bit_cast(unsigned, t), __builtin_bit_cast(unsigned, t), false, false);
  const float u = asf(c[0]) + asf(c[1]);
  z = asf(d[0]) + asf(d[1]);
  auto e = __builtin_amdgcn_permlane16_swap(__builtin_bit_cast(unsigned, u), __builtin_bit_cast(unsigned, u), false, false);
  x = asf(e[0]); y = asf(e[1]);
}
__device__ __forceinline__ u32x4 trade16(u32x4 v) {
  auto a = __builtin_amdgcn_permlane16_swap(v[0], v[2], false, false);
  auto b = __builtin_amdgcn_permlane16_swap(v[1], v[3], false, false);
  return u32x4{(unsigned)a[0], (unsigned)b[0], (unsigned)a[1], (unsigned)b[1]};
}
__device__ __forceinline__ unsigned umax4(u32x4 v) { return max(max(v[0], v[1]), max(v[2], v[3])); }
__device__ __forceinline__ u32x2 unpoison2(u32x2 v) { return u32x2{min(v[0], POISON - 1), min(v[1], POISON - 1)}; }
__device__ __forceinline__ u32x4 unpoison4(u32x4 v) {
  return u32x4{min(v[0], POISON - 1), min(v[1], POISON - 1), min(v[2], POISON - 1), min(v[3], POISON - 1)};
}

typedef union { struct { s16x4 a, b; } hf; u32x4 v; } frag_t;
__device__ __forceinline__ s16x4 trread(const char* smem, unsigned off) {
  return __builtin_amdgcn_ds_read_tr16_b64_v4i16((LDS_AS s16x4*)(smem + off));
}
__device__ __forceinline__ s16x4 trabs(unsigned addr) { return __builtin_amdgcn_ds_read_tr16_b64_v4i16((LDS_AS s16x4*)(size_t)addr); }

constexpr int WA = 6;
constexpr int WB = 8, WR = 4;
#define CH 0, 7, 9, 10, 15, 17, 23, 25, 29, 31, 5, 4
constexpr int CHV[12] = {CH};
constexpr int FKL = CHV[10], FSN = CHV[11];
constexpr int SPL = CHV[0];
constexpr int FSL = CHV[1], FS0 = CHV[2], FS0D = CHV[3], FS1 = CHV[4], FS1S = CHV[5];
constexpr int FXA = CHV[6], FXB = CHV[7], FXL = CHV[8], FXS = CHV[9];
#define L0S 22, 25, 8, 0, 24, 28, 1, 2, 4, 14
constexpr int L0V[10] = {L0S};
constexpr int CSL = L0V[0], CSS = L0V[1], CSD = L0V[2];
constexpr int abl(int it) { return it ? 25 : L0V[4]; }
constexpr int abs_(int it) { return it ? 27 : L0V[5]; }
constexpr int W2S = L0V[6], WSH = L0V[7];
constexpr int LNL = L0V[3], LNS = L0V[8], POI = L0V[9];
#define SYNCB 3
#define SPINS 0
#define KVJ 1
#define KVD 2
#define PC2 1
#define PCE 1
#define ROTH 0
#define ROT0 (p + ROTH * h)
#define TCH 40, 6, 0, 26, 0xFF, 0, 1
constexpr int TCV[7] = {TCH};
constexpr int TS0 = TCV[0], TSN = TCV[1], TM0 = TCV[2], TEX = TCV[3], TM1 = TCV[4], TSD = TCV[5], TSG = TCV[6];
constexpr int QL = 26;
#define SLP 18, 4, 28, 3, 0, 0
constexpr int SLV[6] = {SLP};
constexpr int OUTE = SLV[4], OSL = SLV[5];
constexpr int SDIR = SLV[0], GSL = SLV[1], RSL = SLV[2], KSL = SLV[3];
constexpr int tp0(int it) { return it ? 3 : 3 + WSH; }
constexpr bool tile_piece(int it, int s) { return s >= tp0(it) && ((s - tp0(it)) >> 2) + 3 < 8 && ((s - tp0(it)) & 3) < 3; }
constexpr int a_req(int s, int n) {
  if (s < 1 || !(n & 1) || (s == 1 && n == 1)) return -1;
  if (s <= 5) return 8 + 3 * s + (n >> 1) - 4;
  return n < 5 && s + 5 < 32 ? 2 * (s + 5) + (n >> 1) : -1;
}
constexpr int vm_n(int it, int s, int n) {
  int c = it ? (a_req(s, n) >= 0) + (s == 0 && n == 0) : (s + 5 < 32 && (n == 2 || n == 3));
  if (n == 0) c += tile_piece(it, s);
  if (n == 4) c += (s == abl(it));
  if (!it) c += (s >= W2S && s < W2S + 3 && n == 4) + (s == LNL && n == 5 ? 3 : 0) + (s == LNS + 2 && n == 5) + (s == TEX && n == 5);
  else c += (s == 2 && n == 2) + (s == (SPL < 0 ? 0 : SPL) && n == (SPL < 0 ? 1 : 4) ? 4 : 0) + (s >= 27 && s < 30 && (n == 0 || n == 3));
  return c;
}
constexpr int vm_at(int it, int s, int n) {
  int c = it ? 8 : 16;
  for (int u = 0; u < 6 * s + n; ++u) c += vm_n(it, u / 6, u % 6);
  return c;
}
constexpr int vm_a(int it, int k) {
  if (k < 4) return 2 * k + 2;
  for (int u = 0; u < 192; ++u)
    if (a_req(u / 6, u % 6) == 2 * k + 1) return vm_at(it, (u + 1) / 6, (u + 1) % 6);
  return 0;
}
constexpr int vm_tile(int it, int t) { return t >= 3 ? vm_at(it, 4 * t - 10 + tp0(it), 1) : t == 1 ? 12 : vm_at(0, W2S + 2, 5); }
constexpr int vm_unit(int u) { return u < 3 ? 5 + 2 * u : u < 5 ? 8 + 2 * u : vm_at(0, u - 5, 4); }
constexpr int unit_slot(int u) { return u % 3 == 0 ? 0 : 16384 + (u % 3) * 16384; }

constexpr int L_XC = LDS_BYTES - 64, L_SKEY = 32704, L_ROW = 1024, L_OUT = 65536, L_SRED = 4096, L_SFIN = 20480;
__device__ __forceinline__ unsigned key_load(rsrc_t rs, int l, int L, int rep) { return ld4(rs, (l & 15) * SKEY, KEY_AT(L, rep)); }
__device__ __forceinline__ unsigned umax16(unsigned k) {
  k = max(k, dppu<0xB1>(k));
  k = max(k, dppu<0x4E>(k));
  k = max(k, dppu<0x141>(k));
  return max(k, dppu<0x140>(k));
}
__device__ __forceinline__ unsigned key_row(unsigned key) { return (0xffffu - (key & 0xffffu)) & 2047u; }
__device__ __forceinline__ unsigned key_wait(rsrc_t rs, u32x2 T, int l, int L, int rep, unsigned kv) {
  if (!__all(kv != POISON)) {
    unsigned spins = 0;
    do {
      if (spin_fail(spins, rs, T)) break;
      if (L == 0) __builtin_amdgcn_s_sleep(KSL);
      kv = key_load(rs, l, L, rep);
    } while (!__all(kv != POISON));
  }
  return __builtin_amdgcn_readfirstlane(umax16(kv));
}
__device__ __forceinline__ unsigned cand_off(unsigned key, int L) { return OFF_CA + L * 65536 + (key_row(key) >> 7) * 4096; }
#define XO(i) (((i) >> 1) * 256 + ((i) & 1) * 64)
template <int S1, int S2> __device__ __forceinline__ void ffn_xwait(rsrc_t rs, u32x2 T, unsigned xo, int l, u32x4 (&xv)[4]) {
#define XV_OK() __all(max(max(umax4(xv[0]), umax4(xv[1])), max(umax4(xv[2]), umax4(xv[3]))) != POISON)
  if (!XV_OK()) {
    unsigned spins = 0;
    do {
      if (spin_fail(spins, rs, T)) break;
#pragma unroll
      for (int i = 0; i < 4; ++i) xv[i] = ld16(rs, (l >> 4) * 16 + (i & 1) * S1 + (i >> 1) * S2, xo);
    } while (!XV_OK());
  }
#undef XV_OK
}
__device__ __forceinline__ void ffn_gu(const u32x4 (&gv)[4], const u32x4 (&uv)[4], const u32x4 (&xv)[4], f32x4& ga, f32x4& ua) {
  ga = mfma16(gv[0], xv[0], f32x4{0.f, 0.f, 0.f, 0.f}); ua = mfma16(uv[0], xv[0], f32x4{0.f, 0.f, 0.f, 0.f});
#pragma unroll
  for (int i = 1; i < 4; ++i) { ga = mfma16(gv[i], xv[i], ga); ua = mfma16(uv[i], xv[i], ua); }
}
__device__ __forceinline__ void ffn_gu_check(rsrc_t rs, u32x2 T, unsigned xo, int l, const u32x4 (&gv)[4], const u32x4 (&uv)[4], u32x4 (&xv)[4], f32x4& ga, f32x4& ua) {
  if (__builtin_expect(__any(ga[0] != ga[0]), 0)) {
    ffn_xwait<64, 256>(rs, T, xo, l, xv);
    ffn_gu(gv, uv, xv, ga, ua);
  }
}
__device__ __forceinline__ void ffn_gu_store(char* smem, int l, int w, f32x4 ga, f32x4 ua) {
  LDS_AS f32x4* sred = (LDS_AS f32x4*)(smem + L_SRED);
  if ((l & 15) == 0) { sred[w * 8 + (l >> 4)] = ga; sred[w * 8 + 4 + (l >> 4)] = ua; }
}
__device__ __forceinline__ f32x4 ffn_d_read(char* smem, int l) {
  const LDS_AS float* t = (const LDS_AS float*)(smem + L_SRED) + (l & 8) * 16 + (l & 4) * 4 + (l >> 4) * 4 + (l & 3);
  return f32x4{t[0], t[32], t[64], t[96]};
}
__device__ __forceinline__ u32x2 ffn_d_h(f32x4 t) {
  float x = t[0] + t[1];
  asm volatile("" : "+v"(x));
  x += t[2] + t[3];
  x += dppf<0x128>(x);
  const float y = dppf<0x124>(x);
  const bool up = __builtin_amdgcn_inverse_ballot_w64(0xF0F0F0F0F0F0F0F0ull);
  const float gg = rbf1(up ? y : x), uu = rbf1(up ? x : y);
  const float h = rbf1(gg * __builtin_amdgcn_rcpf(1.0f + __expf(-gg))) * uu;
  const unsigned p = pack2(h, dppf<0xB1>(h));
  return u32x2{dppu<0x00>(p), dppu<0xAA>(p)};
}
template <int t> __device__ __forceinline__ f32x4 ffn_d_mma(s16x4 d, u32x2 hh, f32x4 y) {
  const bool me = __builtin_amdgcn_inverse_ballot_w64(0x0001000100010001ull << t);
  return mfma16k16(d, __builtin_bit_cast(s16x4, u32x2{me ? hh[0] : 0u, me ? hh[1] : 0u}), y);
}
__device__ __forceinline__ f32x4 ffn_d_all(const s16x4 (&dfr)[8], u32x2 hh) {
  f32x4 a = {0.f, 0.f, 0.f, 0.f}, b = a;
  a = ffn_d_mma<0>(dfr[0], hh, a); b = ffn_d_mma<1>(dfr[1], hh, b); a = ffn_d_mma<2>(dfr[2], hh, a); b = ffn_d_mma<3>(dfr[3], hh, b);
  a = ffn_d_mma<4>(dfr[4], hh, a); b = ffn_d_mma<5>(dfr[5], hh, b); a = ffn_d_mma<6>(dfr[6], hh, a); b = ffn_d_mma<7>(dfr[7], hh, b);
  return a + b;
}
__device__ __forceinline__ void ffn_d_store(rsrc_t rs, int l, int w, int L, int slice, f32x4 y) {
  const unsigned c = l & 15;
  if (c < 8) st16(unpoison4(__builtin_bit_cast(u32x4, y)), rs, (2 * w + (c >> 2)) * 65536 + slice * 256 + (c & 3) * 64 + (l >> 4) * 16, OFF_PART + L * (1u << 20));
}
#define RED_VO(k) ((min(tid + 512 * (k), 1791) >> 3) * 256 + (tid & 7) * 16)
#define RED_SO(L, G) (OFF_PART + (L) * (1u << 20) + ((G) >> 1) * 65536 + ((G) & 1) * 128)
#define RED_JJ() ((tid & 3) * 16 + (tid & 4) * 2)
__device__ __forceinline__ void red_load(rsrc_t rs, int tid, int L, unsigned G, unsigned key, u32x4 (&pv)[4], u32x2& cxn, u32x2& cx1) {
#pragma unroll
  for (int k = 0; k < 4; ++k) pv[k] = ld16(rs, RED_VO(k), RED_SO(L, G));
  cxn = ld8(rs, RED_JJ(), cand_off(key, L) + XO(G));
  cx1 = ld8(rs, RED_JJ() + 128, cand_off(key, L) + XO(G));
}
__device__ __forceinline__ void red_sum(rsrc_t rs, char* smem, u32x2 T, int tid, int w, int L, unsigned G, unsigned key,
                                        u32x4 (&pv)[4], u32x2& cxn, u32x2& cx1) {
#define PV_OK() __all(max(max(max(umax4(pv[0]), umax4(pv[1])), max(umax4(pv[2]), umax4(pv[3]))), max(max(cxn[0], cxn[1]), max(cx1[0], cx1[1]))) != POISON)
  if (!PV_OK()) {
    unsigned spins = 0;
    do {
      if (spin_fail(spins, rs, T)) break;
#pragma unroll
      for (int k = 0; k < 4; ++k)
        if (umax4(pv[k]) == POISON) pv[k] = ld16(rs, RED_VO(k), RED_SO(L, G));
      if (max(max(cxn[0], cxn[1]), max(cx1[0], cx1[1])) == POISON) { cxn = ld8(rs, RED_JJ(), cand_off(key, L) + XO(G)); cx1 = ld8(rs, RED_JJ() + 128, cand_off(key, L) + XO(G)); }
    } while (!PV_OK());
  }
#undef PV_OK
  f32x4 s = (__builtin_bit_cast(f32x4, pv[0]) + __builtin_bit_cast(f32x4, pv[1])) + __builtin_bit_cast(f32x4, pv[2]);
  if (w < 4) s += __builtin_bit_cast(f32x4, pv[3]);
  ((LDS_AS f32x4*)(smem + L_SFIN))[tid] = s;
}
__device__ __forceinline__ u32x2 red_final(char* smem, int l, unsigned key, u32x2 cxn, u32x2 cx1) {
  const LDS_AS f32x4* sf = (const LDS_AS f32x4*)(smem + L_SFIN) + l;
  f32x4 s = ((sf[0] + sf[64]) + (sf[128] + sf[192])) + ((sf[256] + sf[320]) + (sf[384] + sf[448]));
#pragma unroll
  for (int j = 0; j < 4; ++j) s[j] = sum_hi8(s[j]);
  const unsigned k16 = key >> 16;
  const float rmax = __builtin_bit_cast(float, ((k16 & 0x8000u) ? (k16 & 0x7fffu) : (~k16 & 0xffffu)) << 16);
  const float sig = rbf1(__builtin_amdgcn_rcpf(1.0f + __expf(-rmax)));
  const float xn4[4] = {lo16(cxn[0]), hi16(cxn[0]), lo16(cxn[1]), hi16(cxn[1])};
  const float x14[4] = {lo16(cx1[0]), hi16(cx1[0]), lo16(cx1[1]), hi16(cx1[1])};
  float o[4];
#pragma unroll
  for (int j = 0; j < 4; ++j) o[j] = x14[j] + rbf1(xn4[j] + rbf1(rbf1(s[j]) * sig));
  return u32x2{pack2(o[0], o[1]), pack2(o[2], o[3])};
}

struct RowFin { float mean, var, mb, rd; unsigned key; };
__device__ __forceinline__ void fin_wait(rsrc_t rs, u32x2 T, unsigned voff, unsigned so, u32x4 (&sp)[4]) {
#define ST_OK() __all(max(max(umax4(sp[0]), umax4(sp[1])), max(umax4(sp[2]), umax4(sp[3]))) != POISON)
  if (!ST_OK()) {
    unsigned spins = 0;
    do {
      if (spin_fail(spins, rs, T)) break;
#pragma unroll
      for (int i = 0; i < 4; ++i) sp[i] = ld16(rs, voff + i * 2048, so);
    } while (!ST_OK());
  }
#undef ST_OK
}
__device__ __forceinline__ RowFin fin_key(rsrc_t rs, u32x2 T, unsigned voff, unsigned so, char* skey, int l, int w, unsigned R, float wsum, u32x4 (&sp)[4]) {
  RowFin r;
#define ST_SUM() ((__builtin_bit_cast(f32x4, sp[0]) + __builtin_bit_cast(f32x4, sp[1])) + (__builtin_bit_cast(f32x4, sp[2]) + __builtin_bit_cast(f32x4, sp[3])))
  f32x4 sf = ST_SUM();
  const float z = (sf[0] + sf[1]) + sf[2];
  if (__any(z != z)) {
    fin_wait(rs, T, voff, so, sp);
    sf = ST_SUM();
  }
#undef ST_SUM
  float s0 = sf[0], s1 = sf[1], s2 = sf[2];
  add_g3(s0, s1, s2);
  r.mean = s0 * (1.0f / 1024.0f);
  r.var = fmaxf(__builtin_fmaf(-r.mean, r.mean, s1 * (1.0f / 1024.0f)), 0.f);
  r.mb = rbf1(r.mean); r.rd = ln_rd(r.var);
  const unsigned rb16 = pack2((s2 - r.mb * wsum) * r.rd, 0.f) & 0xffffu;
  r.key = min((((rb16 & 0x8000u) ? (~rb16 & 0xffffu) : (rb16 | 0x8000u)) << 16) | (0xffffu - R), POISON - 1);
  const unsigned wk = umax16(r.key);
  if (l == 0) ((LDS_AS unsigned*)skey)[w] = wk;
  return r;
}
__device__ __forceinline__ unsigned fin_cand(rsrc_t rs, char* skey, int L, int g, int rb, int ct, const RowFin& r, const f32x2 (&v)[8]) {
  const u32x4 k0 = *(const LDS_AS u32x4*)skey, k1 = *(const LDS_AS u32x4*)(skey + 16);
  const unsigned best = max(umax4(k0), umax4(k1));
  if (r.key == best) {
    const unsigned co = L * 65536 + rb * 4096 + ct * 256 + g * 16;
    f32x2 t[8], rd2 = {r.rd, r.rd}, of2 = {-r.mb * r.rd, -r.mb * r.rd};
    asm volatile("" : "+v"(rd2), "+v"(of2));
#pragma unroll
    for (int j = 0; j < 8; ++j) t[j] = __builtin_elementwise_fma(v[j], rd2, of2);
    st16(unpoison4(pack8v(t)), rs, co, OFF_CA); st16(unpoison4(pack8v(t + 4)), rs, co + 64, OFF_CA);
    if (g == 0 && ct < (int)RKEY) st4(best, rs, KEY_AT(L, ct) + rb * SKEY, 0);
    st16(unpoison4(pack8v(v)), rs, co + 128, OFF_CA); st16(unpoison4(pack8v(v + 4)), rs, co + 192, OFF_CA);
  }
  return key_row(best);
}
__device__ __forceinline__ void fin_x2(const RowFin& r, f32x2 (&v)[8]) {
  f32x2 sc2 = {1.0f + r.rd, 1.0f + r.rd}, of2 = {-r.mb * r.rd, -r.mb * r.rd};
  asm volatile("" : "+v"(sc2), "+v"(of2));
#pragma unroll
  for (int j = 0; j < 8; ++j) v[j] = __builtin_elementwise_fma(v[j], sc2, of2);
}

extern "C" __device__ unsigned long long dispatch_id() __asm("llvm.amdgcn.dispatch.id");

extern "C" __global__ __launch_bounds__(512, 2) void mega_kernel(const bf16* x, char* ws, const bf16* wqkv, const bf16* wo, const bf16* wr,
                                                                 const bf16* wg, const bf16* wu, const bf16* wd, bf16* out) {
  extern __shared__ __attribute__((aligned(16))) char smem[];
  const int w0_ = __builtin_amdgcn_readfirstlane(threadIdx.x >> 6);
  const unsigned lbase = (unsigned)(size_t)(LDS_AS char*)smem;
  {
    unsigned long long pc;
    unsigned len;
    asm volatile("s_getpc_b64 %0\n\ts_mov_b32 %1, .Ltext_end-mega_kernel\n\t.subsection 1\n.Ltext_end:\n\t.subsection 0" : "=s"(pc), "=s"(len));
    const unsigned o = ((blockIdx.x >> 3) * 64 + (threadIdx.x & 63)) * 128;
    if (w0_ == 7 && o < len)
      asm volatile("s_mov_b32 m0, %0\n\ts_nop 0\n\tglobal_load_lds_dword %1, %2" ::"s"(lbase + L_FXL), "v"(o), "s"(pc & ~127ull) : "memory");
  }
  asm volatile("" : "+s"(x), "+s"(ws), "+s"(wqkv));
#define IDS()                                                                                         \
  int tid = threadIdx.x, w = w0_, bid = blockIdx.x;                                                   \
  asm volatile("" : "+v"(tid));                                                                       \
  asm volatile("" : "+s"(w));                                                                         \
  asm volatile("" : "+s"(bid));                                                                       \
  const int l = tid & 63, xcd = bid & 7, bi = bid >> 3;                                               \
  const int p = (xcd & 1) * 4 + (bi & 3), h = (xcd >> 1) * 8 + (bi >> 2);         \
  const int rb = 2 * xcd + (bi & 1), ct = bi >> 1;                               \
  const int slice = xcd * 28 + bi;                                                   \
  const bool ffn_on = bi < 28;                                                                        \
  const int g = l >> 4;                                                                               \
  const int rt = w < 4 ? w : 11 - w;          \
  KARGS();                                                                                            \
  (void)p; (void)h; (void)rb; (void)ct; (void)slice; (void)ffn_on; (void)g; (void)rt;
  const rsrc_t rs = __builtin_amdgcn_make_buffer_rsrc((void*)ws, 0, WS_BYTES, 0x00020000);
  typedef const void* const __attribute__((address_space(4)))* karg_t;
#define KARGS() karg_t ka = (karg_t)__builtin_amdgcn_kernarg_segment_ptr(); asm volatile("" : "+s"(ka))
#define ARGP(name, i) const GAS bf16* name = (const GAS bf16*)ka[i]; asm volatile("" : "+s"(name))
#define RSRC(ptr, bytes) __builtin_amdgcn_make_buffer_rsrc((void*)(ptr), 0, bytes, 0x00020000)
#define rwq RSRC(pwq, 1024 * 3072 * 2)
#define rwo RSRC(pwo, 1024 * 1024 * 2)
#define rwg RSRC(pwg, 1024 * F_ * 2)
#define rwu RSRC(pwu, 1024 * F_ * 2)
#define rwd RSRC(pwd, 1024 * F_ * 2)
#define rout RSRC(pout, 2048 * 1024 * 2)

#define WQ_VOFF() ((l >> 2) * 6144 + (((l & 3) ^ ((((l >> 4) ^ (l >> 5)) & 1) << 1)) << 4))
#define WQ_PIECE(t, j, dst) dma16(rwq, wqv, ((((t) + rot) & 7) * 128 + ((j) & 7) * 16) * 6144 + ((j) >> 3) * 2048 + h * 64, lbase + (dst) + (j) * 1024)
#define WQ_BOFF(dt) ((8 * g + ((l & 15) >> 2)) * 64 + (l & 3) * 8 + (((dt) ^ (g & 1)) << 5))
#define WQ_POFF() ((4 * g + ((l & 15) >> 2)) * 64 + (l & 3) * 8 + (((g ^ (g >> 1)) & 1) << 5))
#define X_UNIT1(u, j) dma16(rx, xv_, (unsigned)(p * 256 + rt * 32 + ((u) & 1) * 16 + (j) * 8) * 2048 + ((((u) >> 2) + rot) & 7) * 256 + (((u) >> 1) & 1) * 128, lbase + rt * 2048 + unit_slot(u) + (j) * 1024)
#define X_VOFF() ((l >> 3) * 2048 + (((l & 7) ^ ((l >> 3) & 6)) << 4))
#define X_ROFF(hs) (lbase + rt * 2048 + (l & 15) * 128 + (((4 * (hs) + g) ^ (l & 6)) << 4))
#define TOUCH_TO(ptr, dst) asm volatile("s_mov_b32 m0, %0\n\ts_nop 0\n\tglobal_load_lds_dword %1, off" ::"s"(lbase + (dst)), "v"(ptr) : "memory")
#define TOUCH(ptr) TOUCH_TO(ptr, L_FXL)
#define WO_PIECE_W(j, w_) dma16(rwo, wov, (((w_) >> 2) * 64 + ((w_) & 3) + 4 * (j)) * 16384 + ct * 128, lbase + 32768 + (j) * 8192 + (w_) * 1024)
#define WO_PIECE(j) WO_PIECE_W(j, w)
#define WO_VOFF() ((l >> 3) * 2048 + (((((l & 7) >> 1) ^ ((l >> 4) & 3)) * 2 + (l & 1)) << 4))
#define WD_DMA() _Pragma("unroll") for (int i = 0; i < 4; ++i) WD_DMA1(i)
#define WD_DMA1(i)                                                                                   \
      dma16(rwd, (l >> 4) * 2048 + (l & 15) * 16, (unsigned)(slice * 16 + 4 * (i)) * 2048 + w * 256, lbase + L_DN + w * 4096 + (i) * 1024)
#define FFN_DMA(m) dma16((m) & 1 ? rwu : rwg, fnv, (unsigned)(((4 * w + ((m) >> 1)) * 32) * F_ + slice * 16) * 2, lbase + 65536 + (m) * 8192 + w * 1024)
  u32x4 gv[4], uv[4];
  s16x4 dfr[8];
  u32x4 keep[9];
  u32x4 pre[4][2];
#define PRE_LOAD(it_, rot_, s0, s1)                                                                  \
  static_for<(s1) - (s0)>([&](auto c_) __attribute__((always_inline)) {                              \
    constexpr int s = (s0) + decltype(c_)::value;                                                    \
    const unsigned ab_ = OFF_XN + (it_) * (4u << 20) + (((rot_) + (s >> 2)) & 7) * 8192 + ((s >> 1) & 1) * 4096; \
    ld16w<2 * s, (s & 1) * 2048>(pre[s][0], rs, l * 16, ab_ + (p * 8 + rt) * 65536);                         \
    ld16w<2 * s + 1, (s & 1) * 2048 + 1024>(pre[s][1], rs, l * 16, ab_ + (p * 8 + rt) * 65536);                 \
  })
  u32x2 T;
  const GAS bf16* tsrc;
  {
    IDS();
    const int it = 0, rot = ROT0;
    (void)it;
    const bf16* pwq = wqkv;
    const rsrc_t rx = RSRC(x, 2048 * 1024 * 2);
    const unsigned wqv = WQ_VOFF(), xv_ = X_VOFF();
    if (tid == 0) *(LDS_AS u32x4*)(smem + L_QC) = u32x4{0u, 0u, 0u, 0u};
#pragma unroll
    for (int i = 0; i < 3; ++i) WQ_PIECE(0, 3 * w + i, L_RING);
    X_UNIT1(0, 0); X_UNIT1(0, 1); X_UNIT1(1, 0); X_UNIT1(1, 1);
    BAR();
    X_UNIT1(2, 0); X_UNIT1(2, 1);
#pragma unroll
    for (int i = 0; i < 3; ++i) WQ_PIECE(1, 3 * w + i, L_RING + STAGE);
    {
      ARGP(pwo, 3); ARGP(pwg, 5); ARGP(pwu, 6);
      const int t = tid < 288 ? tid - 64 : tid - 288, go = (bi * 32 + t / 7) * F_ + xcd * 448 + (t % 7) * 64;
      tsrc = tid < 64 ? pwo + bid * 4096 + tid * 64 : tid < 288 ? pwg + go : pwu + go;
      asm volatile("" : "+v"(tsrc));
    }
  }
  unsigned sel0 = 0x7FFFFFFFu;
  f32x2 v[8];
  u32x4 x2p[2];
  float wsum;

  auto layer = [&](auto it_c) __attribute__((always_inline)) {
    constexpr int it = decltype(it_c)::value;
    f32x4 acc[2][6];
    f32x2 qs2[2];
    u32x4 k0, k1, v0, v1;
    {
      IDS();
      bool aff = false;
      unsigned aq[2];
      u32x4 av[2];
#define PIN_ACC() asm volatile("" : "+{v[176:179]}"(acc[0][0]), "+{v[180:183]}"(acc[0][1]), "+{v[184:187]}"(acc[0][2]), "+{v[188:191]}"(acc[0][3]), "+{v[192:195]}"(acc[0][4]), "+{v[196:199]}"(acc[0][5]), \
                               "+{v[200:203]}"(acc[1][0]), "+{v[204:207]}"(acc[1][1]), "+{v[208:211]}"(acc[1][2]), "+{v[212:215]}"(acc[1][3]), "+{v[216:219]}"(acc[1][4]), "+{v[220:223]}"(acc[1][5]))
#define PINV4(x) asm volatile("" : "+v"(x[0]), "+v"(x[1]), "+v"(x[2]), "+v"(x[3])::"memory")
      auto qkv_loop = [&](auto it_c) __attribute__((always_inline)) {
        constexpr int IT = decltype(it_c)::value;
        const unsigned abase = OFF_XN + IT * (4u << 20) + (p * 8 + rt) * 65536;
        const int rot = ROT0 + 5 * IT;
        unsigned fkey = 0, kv, bq[2];
        u32x4 fx[4], bv[2], af[WA][2];
        u32x2 cxn, cx1, hh;
        f32x4 sa, sb, cs4[2];
        f32x2 b2[2];
#define AB_OFF() (OFF_AB + IT * 32768 + (p * 8 + rt) * 512)
#define A_LOAD1(k, a) ld16w<(k) % WA * 2 + (a), ((k) & 1) * 2048 + (a) * 1024>(af[(k) % WA][a], rs, l * 16, abase + ((((k) >> 2) + rot) & 7) * 8192 + (((k) >> 1) & 1) * 4096)
        if constexpr (IT == 1) {
#pragma unroll
          for (int s = 0; s < 4; ++s) { af[s][0] = pre[s][0]; af[s][1] = pre[s][1]; }
        }
        const bf16* pwq = wqkv;
        ARGP(pwo, 3);
        const rsrc_t rx = RSRC(x, 2048 * 1024 * 2);
        unsigned xv_ = X_VOFF(), xr[2] = {X_ROFF(0), X_ROFF(1)};
        if constexpr (IT == 0) asm volatile("" : "+v"(xv_), "+v"(xr[0]), "+v"(xr[1]));
#define X_READ(u) { af[((u) & ~1) % WA][(u) & 1] = *(const LDS_AS u32x4*)(size_t)(xr[0] + unit_slot(u)); af[((u) | 1) % WA][(u) & 1] = *(const LDS_AS u32x4*)(size_t)(xr[1] + unit_slot(u)); }
        const int tstep = TS0 + bid % TSN + TSD * (w >> TSG);
        const unsigned wqv = WQ_VOFF();
#define QKV_DMA1(t, i)                                                                            \
  {                                                                                               \
    if constexpr (IT == 0) WQ_PIECE(t, 3 * w + (i), L_RING + SLOT(t) * STAGE);                    \
    else dma16s<(i) * 1024>(rs, l * 16, OFF_WI + (h * 8 + (((t) + rot) & 7)) * STAGE + w * 3072, lbase + L_RING + SLOT(t) * STAGE + w * 3072); \
  }
        unsigned bo[2][2];
#pragma unroll
        for (int dt = 0; dt < 2 - IT; ++dt) {
          bo[dt][0] = lbase + L_RING + (IT ? l * 16 : WQ_BOFF(dt)); bo[dt][1] = bo[dt][0] + 2 * STAGE;
          asm volatile("" : "+v"(bo[dt][0]), "+v"(bo[dt][1]));
        }
        auto frag0 = [&](int sl, int f) __attribute__((always_inline)) {
          unsigned pb = lbase + L_RING + WQ_POFF();
          asm volatile("" : "+v"(pb));
          const unsigned o = (pb ^ ((f & 1) << 5)) + sl * STAGE + ((f % 6) >> 1) * 8192 + (f / 6) * 2048;
          frag_t t;
          t.hf.a = trabs(o); t.hf.b = trabs(o + 1024);
          return t.v;
        };
        u32x4 df[4];
        auto dot_mma = [&](const u32x4 (&xf)[4]) __attribute__((always_inline)) {
          sa = mfma16(df[0], xf[0], f32x4{0.f, 0.f, 0.f, 0.f}); sb = mfma16(df[1], xf[1], f32x4{0.f, 0.f, 0.f, 0.f});
          sa = mfma16(df[2], xf[2], sa); sb = mfma16(df[3], xf[3], sb);
        };
        auto dot_store = [&](unsigned dst) __attribute__((always_inline)) {
          if ((l & 15) == 0) st16(unpoison4(__builtin_bit_cast(u32x4, sa + sb)), rs, g * 16, dst + (h * 8 + p) * 512 + w * 64);
        };
        auto tile_sum = [&](u32x4 pt, int dst) __attribute__((always_inline)) {
          f32x4 c = __builtin_bit_cast(f32x4, pt);
#pragma unroll
          for (int e = 0; e < 4; ++e) { c[e] += dppf<0x124>(c[e]); c[e] += dppf<0x128>(c[e]); c[e] = add_x16(c[e]); }
          if ((l & 28) == 0) *(LDS_AS f32x4*)(smem + dst + (w * 2 + (l >> 5)) * 64 + (l & 3) * 16) = c;
        };
        frag_t bfr[6];
#define QKV_BO(s, n, x) (bo[((n) ^ (x)) & 1][SLOT((s) >> 2) >> 1] + (SLOT((s) >> 2) & 1) * STAGE + ((n) >> 1) * 8192 + ((s) & 3) * 2048 + (x) * 256)
#define QKV_RDA(s, n)                                                                             \
  {                                                                                               \
    if constexpr (IT == 0) bfr[n].hf.a = trabs(QKV_BO(s, n, 0));                                  \
    else bfr[n].v = *(const LDS_AS u32x4*)(size_t)(bo[0][SLOT((s) >> 2) >> 1] + (SLOT((s) >> 2) & 1) * STAGE + (((s) & 3) * 6 + (n)) * 1024); \
  }
#define QKV_RDB(s, n) { if constexpr (IT == 0) bfr[n].hf.b = trabs(QKV_BO(s, n, 1)); }
#define XP_OFF() (OFF_XNEW + ((rot + 6) & 7) * 1024)
#define FP_VO() (w == 3 ? (l >> 2) * 256 + (l & 3) * 16 : ((l >> 2) & 7) * 512 + (l >> 5) * 64 + (l & 3) * 16)
#define FP_OFF(i) (w == 3 ? OFF_XNEW + (i) * 4096 : OFF_FX + h * 4096 + w * 128)
#define FX_OK(n) __all(((n) == 4 ? max(max(umax4(fx[0]), umax4(fx[1])), max(umax4(fx[2]), umax4(fx[3]))) : max(umax4(fx[0]), umax4(fx[1]))) != POISON)
#define SINCE(s0) (vm_at(IT, s, n) - vm_at(IT, s0, 5))
#define DEF4(x) asm volatile("" : "=v"(x[0]), "=v"(x[1]), "=v"(x[2]), "=v"(x[3]))
        LDS_AS unsigned* qcnt = (LDS_AS unsigned*)(smem + L_QC);
        unsigned qc;
        unsigned qadr = lbase + L_QC, qone = 1;
        asm volatile("" : "+v"(qadr), "+v"(qone));
#define count_in(i) asm volatile("s_mov_b64 exec, 1\n\tds_add_u32 %0, %1 offset:%2\n\ts_mov_b64 exec, -1" ::"v"(qadr), "v"(qone), "n"(4 * (i)) : "memory")
        auto count_wait = [&](int i, unsigned want) __attribute__((always_inline)) {
          if (__builtin_expect((unsigned)__builtin_amdgcn_readfirstlane(qc) < want, 0)) {
            while ((unsigned)__builtin_amdgcn_readfirstlane(__hip_atomic_load(qcnt + i, __ATOMIC_RELAXED, __HIP_MEMORY_SCOPE_WORKGROUP)) < want) { if (SPINS) __builtin_amdgcn_s_sleep(SPINS); }
          }
          asm volatile("" ::: "memory");
        };
        if constexpr (IT == 0) {
          wait_vm<12 - vm_unit(1)>();
          if (!(SYNCB & 1)) count_in(0);
          X_READ(0); X_READ(1);
          asm volatile("s_waitcnt lgkmcnt(0)" : "+v"(af[0][0]), "+v"(af[0][1]), "+v"(af[1][0]), "+v"(af[1][1])::"memory");
          X_UNIT1(3, 0); X_UNIT1(3, 1); X_UNIT1(4, 0); X_UNIT1(4, 1);
          qc = 0;
          if (SYNCB & 1) asm volatile("s_barrier" ::: "memory");
          else count_wait(0, 8);
        }
#pragma unroll
        for (int n = 0; n < 6; ++n) { QKV_RDA(0, n); if (n < 5) QKV_RDB(0, n); }
        f32x2 lv[4], xs, xq;
        float lmean;
        u32x2 ep;
        u32x4 xl[2];
        f32x4 sc;
#define X1_OFF() (F_X1 + ((p * RX1 + (xcd >> 1) % RX1) * 32 + (rt >> 2) * 16 + (l & 15)) * SX1)
        unsigned long long xok = 0;
        RowFin fin;
        auto x1_need = [&](unsigned long long m) __attribute__((always_inline)) {
          if (__builtin_expect((xok & m) != m, 0)) {
            for (unsigned spins = 0;;) {
              const u32x2 f = ld8(rs, X1_OFF(), 0);
              xok = __builtin_amdgcn_ballot_w64(f[0] == T[0] && f[1] == T[1]);
              if ((xok & m) == m) break;
              if (spin_fail(spins, rs, T)) { xok = ~0ull; break; }
              __builtin_amdgcn_s_sleep(KSL);
            }
            asm volatile("" ::: "memory");
          }
        };
        auto gap = [&](auto sc_, auto nc_) __attribute__((always_inline)) {
          constexpr int s = decltype(sc_)::value, n = decltype(nc_)::value;
          if constexpr ((SYNCB >> IT) & 1) {
            constexpr int tc = (s + 2) / 4;
            if constexpr (tc >= 1 && tc < 8 && s == 4 * tc - 2 && n == 5) {
              if constexpr (!IT || tc >= 3) wait_vm<vm_at(IT, s, n) - vm_tile(IT, tc)>();
              if constexpr (tc == 7 && !IT) asm volatile("s_waitcnt lgkmcnt(4)" ::: "memory");
              asm volatile("s_barrier" ::: "memory");
            }
          } else {
            constexpr int ts = (s + 3) / 4, tc = (s + 2) / 4, t0 = 1;
            if constexpr (ts >= 3 && ts < 8 && s == 4 * ts - 3 && n == 2) { wait_vm<vm_at(IT, s, n) - vm_tile(IT, ts)>(); count_in(0); }
            if constexpr (!IT && tc >= t0 && tc < 3 && s == 4 * tc - 2 && n == 1) { wait_vm<vm_at(IT, s, n) - vm_tile(IT, tc)>(); count_in(0); }
            if constexpr (IT && ts >= t0 && ts < 3 && s == 4 * ts - 3 && n == 2) count_in(0);
            if constexpr (tc >= t0 && tc < 8 && s == 4 * tc - 2) {
              if constexpr (n == 3) qc = __hip_atomic_load(qcnt, __ATOMIC_RELAXED, __HIP_MEMORY_SCOPE_WORKGROUP);
              if constexpr (n == 5) count_wait(0, 8 * (tc + 1 - IT));
            }
          }
          if constexpr (IT == 0 && n == 0 && s + 2 < 32) { wait_vm<vm_at(0, s, 0) - vm_unit(s + 2)>(); X_READ(s + 2); SB(); }
          if constexpr (n == 0 && tile_piece(IT, s)) QKV_DMA1(((s - tp0(IT)) >> 2) + 3, (s - tp0(IT)) & 3);
          if constexpr (IT == 0) {
            if constexpr ((n == 2 || n == 3) && s + 5 < 32) {
              if constexpr (n == 2) asm volatile("s_waitcnt lgkmcnt(%0)" ::"n"(s + 1 < QL ? 4 : 1) : "memory");
              X_UNIT1(s + 5, n - 2);
            }
            if constexpr (s >= W2S && s < W2S + 3 && n == 4) QKV_DMA1(2, s - W2S);
            if constexpr (s == LNL && n == 5) {
              asm volatile("buffer_load_dwordx2 %0, off, %1, 0 sc1" : "=v"(ep) : "s"(rs) : "memory");
              asm volatile("buffer_load_dwordx4 %0, %1, %2, %3 offen" : "=v"(xl[0]) : "v"(l * 16), "s"(rx), "s"((p * 256 + h * 8 + w) * 2048) : "memory");
              asm volatile("buffer_load_dwordx4 %0, %1, %2, %3 offen offset:1024" : "=v"(xl[1]) : "v"(l * 16), "s"(rx), "s"((p * 256 + h * 8 + w) * 2048) : "memory");
            }
            if constexpr (s == LNS && n == 1) {
              wait_vm<vm_at(0, s, n) - vm_at(0, LNL + 1, 0)>();
              asm volatile("" : "+v"(ep), "+v"(xl[0]), "+v"(xl[1])::"memory");
              const unsigned long long e = (((unsigned long long)(unsigned)__builtin_amdgcn_readfirstlane(ep[1]) << 32) | (unsigned)__builtin_amdgcn_readfirstlane(ep[0])) + 1ull + dispatch_id() * 0x9E3779B97F4A7C16ull;
              T = u32x2{(unsigned)e, (unsigned)(e >> 32)};
              unpack8v(xl[0], lv);
              xs = (lv[0] + lv[1]) + (lv[2] + lv[3]);
              xq = __builtin_elementwise_fma(lv[0], lv[0], lv[1] * lv[1]) + __builtin_elementwise_fma(lv[2], lv[2], lv[3] * lv[3]);
              asm volatile("" : "+v"(xs), "+v"(xq));
            }
            if constexpr (s == LNS && n == 4) {
              unpack8v(xl[1], lv);
              xs += (lv[0] + lv[1]) + (lv[2] + lv[3]);
              xq += __builtin_elementwise_fma(lv[0], lv[0], lv[1] * lv[1]) + __builtin_elementwise_fma(lv[2], lv[2], lv[3] * lv[3]);
              auto t = __builtin_amdgcn_permlane32_swap(__builtin_bit_cast(unsigned, hadd(xs)), __builtin_bit_cast(unsigned, hadd(xq)), false, false);
              lmean = asf(t[0]) + asf(t[1]);
              asm volatile("" : "+v"(lmean));
            }
            if constexpr (s == LNS + 1 && n == 1) { lmean += dppf<0xB1>(lmean); lmean += dppf<0x4E>(lmean); lmean += dppf<0x124>(lmean); asm volatile("" : "+v"(lmean)); }
            if constexpr (s == LNS + 1 && n == 4) { lmean = add_x16(lmean + dppf<0x128>(lmean)) * (1.0f / 1024.0f); asm volatile("" : "+v"(lmean)); }
            if constexpr (s == LNS + 2 && n == 1) {
              const float m2 = asf(__builtin_amdgcn_readlane(__builtin_bit_cast(unsigned, lmean), 32));
              xs[0] = ln_rd(fmaxf(__builtin_fmaf(-lmean, lmean, m2), 0.f));
              asm volatile("" : "+v"(xs));
            }
            if constexpr (s == LNS + 2 && n == 5) {
              if (l == 0) st16(u32x4{__builtin_bit_cast(unsigned, (float)xs[0]), __builtin_bit_cast(unsigned, -rbf1(lmean)), T[0], T[1]}, rs, OFF_AB + (p * 256 + h * 8 + w) * 16, 0);
            }
            if constexpr (s == POI && n == 1) {
              if (w == 1) {
                const unsigned prb = 2 * p + (h >> 4), pct = h & 15;
                unsigned pz1 = POISON;
                asm volatile("" : "+v"(pz1));
                const u32x4 pz = {pz1, pz1, pz1, pz1};
                st16(pz, rs, OFF_STAT + (prb * 16 + pct) * 2048 + l * 16, 0);
                st16(pz, rs, OFF_STAT + (prb * 16 + pct) * 2048 + 1024 + l * 16, 0);
                if (l < 16) st16(pz, rs, OFF_CA + prb * 4096 + pct * 256 + l * 16, 0);
                else if (l == 16 && pct < RKEY) st4(pz1, rs, KEY_AT(0, pct) + prb * SKEY, 0);
              }
            }
            if constexpr (s == POI + 4 && n == 1) { if (w == 1 && l < RX) st8(T, rs, F_XN + (p * RX + l) * 256 + h * 8, 0); }
          } else {
            if constexpr ((s == 0 && n == 0) || (s == 2 && n == 2)) asm volatile("buffer_load_dwordx2 %0, %1, %2, 0 offen sc1" : "=v"(cxn) : "v"(X1_OFF()), "s"(rs) : "memory");
            if constexpr ((s == 1 && n == 3) || (s == 4 && n == 0)) {
              wait_vm<vm_at(1, s, n) - (s == 1 ? vm_at(1, 0, 1) : vm_at(1, 2, 3))>();
              asm volatile("" : "+v"(cxn)::"memory");
              xok |= __builtin_amdgcn_ballot_w64(cxn[0] == T[0] && cxn[1] == T[1]);
            }
#define IMG_OFF() (F_X1 + ((((h >> 3) * 2 + ((l >> 2) & 1)) * RX1 + (xcd >> 1) % RX1) * 32 + (l & 1) * 16 + (h & 7) * 2 + ((l >> 1) & 1)) * SX1)
            if constexpr (s == 0 && n == 2) { asm volatile("" : "=v"(cx1)); if (w == 1) asm volatile("buffer_load_dwordx2 %0, %1, %2, 0 offen sc1" : "=v"(cx1) : "v"(IMG_OFF()), "s"(rs) : "memory"); }
            if constexpr (s == 2 && n == 4) {
              if (w == 1) {
                wait_vm<vm_at(1, 2, 4) - vm_at(1, 0, 3)>();
                asm volatile("" : "+v"(cx1)::"memory");
                if (!__all(cx1[0] == T[0] && cx1[1] == T[1])) poll_flags(rs, IMG_OFF(), 0, T);
              }
            }
            if constexpr (a_req(s, n) >= 0 && (a_req(s, n) & 7) == 0) x1_need(3ull << (((rot + a_req(s, n) / 8) & 7) * 2));
            const unsigned R = (rb * 4 + (w & 3)) * 32 + (w >> 2) * 16 + (l & 15), so = OFF_STAT + rb * 32768;
            if constexpr (s == (SPL < 0 ? 0 : SPL) && n == (SPL < 0 ? 1 : 4)) {
#pragma unroll
              for (int i = 0; i < 4; ++i) ld16h<0>(fx[i], rs, (4 * g + i) * 2048 + (R & 127) * 16, so);
            }
            if constexpr (s == SPL + 2 && n == 1) {
              wait_vm<vm_at(1, s, n) - (SPL < 0 ? vm_at(1, 0, 2) : vm_at(1, SPL, 5))>();
              PINV4(fx);
              fin = fin_key(rs, T, g * 8192 + (R & 127) * 16, so, smem + L_SKEY, l, w, R, wsum, fx);
              count_in(2);
            }
            if constexpr (s == SPL + 3 && n == 0) qc = __hip_atomic_load(qcnt + 2, __ATOMIC_RELAXED, __HIP_MEMORY_SCOPE_WORKGROUP);
            if constexpr (s == SPL + 3 && n == 1) { count_wait(2, 8); fin_cand(rs, smem + L_SKEY, 0, g, rb, ct, fin, v); }
            if constexpr (s == SPL + 3 && n == 3) {
              fin_x2(fin, v);
              x2p[0] = pack8v(v); x2p[1] = pack8v(v + 4);
              asm volatile("" : "+v"(x2p[0]), "+v"(x2p[1]));
            }
            if constexpr (s == SPL + 3 && n == 5) {
              if ((w & 3) * 2 + (w >> 2) == ct >> 1) {
                const float f = 1.0f + fin.rd, rd2 = ln_rd(fin.var * f * f), mb2 = rbf1(__builtin_fmaf(fin.mean - fin.mb, fin.rd, fin.mean));
                if (g == 0 && ((l >> 3) & 1) == (ct & 1))
                  st16(u32x4{__builtin_bit_cast(unsigned, f * rd2), __builtin_bit_cast(unsigned, -__builtin_fmaf(fin.mb, fin.rd, mb2) * __builtin_amdgcn_rcpf(f)), T[0], T[1]}, rs, OFF_AB + 32768 + R * 16, 0);
              }
            }
          }
          if constexpr (IT == 1 && a_req(s, n) >= 0) A_LOAD1(a_req(s, n) / 2, a_req(s, n) & 1);
          if constexpr (!IT && s == CSL && n == 4) { asm volatile("" : "=v"(fx[0])); if (w < 6) ld16h<0>(fx[0], rs, l * 16, OFF_CS + (h * 6 + w) * 1024); }
          if constexpr (!IT && s == CSS && n == 2) {
            if (w < 6) {
              wait_vm<SINCE(CSL)>();
              asm volatile("" : "+v"(fx[0])::"memory");
              tag_wait(rs, T, l * 16, OFF_CS + (h * 6 + w) * 1024, fx[0]);
              const f32x2 c = {sum_hi8(asf(fx[0][0])), sum_hi8(asf(fx[0][1]))};
              if (l < 8) *(LDS_AS f32x2*)(smem + L_CSL + w * 64 + l * 8) = c;
            }
          }
          constexpr int ABL = abl(IT), ABS = abs_(IT);
          if constexpr (s == ABL && n == 4) ld16h<0>(bv[0], rs, (l & 31) * 16, AB_OFF());
          if constexpr (s == ABS && n == 2) {
            wait_vm<SINCE(ABL)>();
            asm volatile("" : "+v"(bv[0])::"memory");
            tag_wait(rs, T, (l & 31) * 16, AB_OFF(), bv[0]);
            const unsigned t0 = bv[0][0], t1 = bv[0][1];
            ((LDS_AS unsigned*)(smem + L_ABW + rt * 2048))[l] = l < 32 ? t0 : t1;
          }
          if constexpr (s == ABS && n == 3) {
#pragma unroll
            for (int a = 0; a < 2; ++a) {
              const LDS_AS unsigned* aw = (const LDS_AS unsigned*)(smem + L_ABW + rt * 2048) + 16 * a;
              aq[a] = aw[l & 15]; bq[a] = aw[32 + (l & 15)];
              av[a] = *(const LDS_AS u32x4*)(aw + 4 * g); bv[a] = *(const LDS_AS u32x4*)(aw + 32 + 4 * g);
            }
          }
          if constexpr (s == ABS && n == 5) {
#pragma unroll
            for (int a = 0; a < 2; ++a) { b2[a] = f32x2{asf(bq[a]), asf(bq[a])}; asm volatile("" : "+v"(b2[a])); }
          }
          if constexpr (s == ABS + 1) {
            constexpr int k = (n + 4) % 6;
            if constexpr (k < 4) cs4[k & 1] = *(const LDS_AS f32x4*)(smem + L_CSL + k * 64 + g * 16);
            else cs4[k & 1][0] = *(const LDS_AS float*)(smem + L_CSL + k * 64 + (l & 15) * 4);
          }
          if constexpr ((s == ABS + 1 && n > 0) || (s == ABS + 2 && n == 0)) {
            constexpr int k = (n + 3) % 6;
            if constexpr (k < 4) {
#pragma unroll
              for (int a = 0; a < 2; ++a) acc[a][k] = __builtin_elementwise_fma(cs4[k & 1], f32x4{b2[a][0], b2[a][1], b2[a][0], b2[a][1]}, acc[a][k]);
            } else {
              f32x2 c2 = {cs4[k & 1][0], cs4[k & 1][0]};
              asm volatile("" : "+v"(c2));
#pragma unroll
              for (int a = 0; a < 2; ++a) acc[a][k] = __builtin_elementwise_fma(f32x4{c2[0], c2[1], c2[0], c2[1]}, __builtin_bit_cast(f32x4, bv[a]), acc[a][k]);
            }
          }
          if constexpr (IT == 0) {
            if constexpr (s == CSD && n < 3) fx[n] = frag0(SLOT(s >> 2), 3 * w + n);
            if constexpr (s == CSD + 1 && n == 1) {
              unsigned one2 = 0x3F803F80u;
              asm volatile("" : "+v"(one2));
              const u32x4 o4 = {one2, one2, one2, one2};
              const f32x4 z4 = {0.f, 0.f, 0.f, 0.f};
              sa = mfma16(fx[0], o4, z4); sb = mfma16(fx[1], o4, z4); sc = mfma16(fx[2], o4, z4);
            }
            if constexpr (s == CSD + 2 && n == 1) {
              const int c = l & 15;
              const f32x4 t0 = sa, t1 = sb, t2 = sc, t = c == 0 ? +t0 : c == 1 ? +t1 : +t2;
              if (c < 3) *(LDS_AS f32x4*)(smem + L_CSP + (w * 3 + c) * 64 + g * 16) = t;
              count_in(2);
            }
            if constexpr (s == CSD + 4 && n == 1) {
              if (w == 2) {
                qc = 0;
                count_wait(2, 8);
                const LDS_AS f32x2* cp = (const LDS_AS f32x2*)(smem + L_CSP) + min(l, 47);
                const f32x2 t = (cp[0] + cp[48]) + (cp[96] + cp[144]);
                if (l < 48) st16(u32x4{__builtin_bit_cast(unsigned, (float)t[0]), __builtin_bit_cast(unsigned, (float)t[1]), T[0], T[1]}, rs, (l >> 3) * 1024 + (l & 7) * 16, OFF_CS + h * 6144 + p * 128);
              }
            }
            if constexpr (s >= 29 && (n & 1)) keep[3 * (s - 29) + (n >> 1)] = frag0(SLOT(s - 24), 3 * w + (n >> 1));
            if constexpr (s >= TS0 && s < TS0 + TSN + TSD * (7 >> TSG) && n == 4) {
              if (tstep == s && ((TM0 >> w) & 1)) TOUCH(tsrc);
            }
            if constexpr (s == TEX && n == 5) {
              const GAS bf16* t2 = ((TM1 >> w) & 1) ? tsrc : (const GAS bf16*)x + p * 262144;
              TOUCH(t2);
            }
          } else {
            if constexpr (s >= 27 && s < 30 && (n == 0 || n == 3)) { const unsigned wov = WO_VOFF(); WO_PIECE(4 + 2 * (s - 27) + n / 3); }
            if constexpr (s == FKL && n == 4) { asm volatile("" : "=v"(kv)); if (w == 0) ld4h<0>(kv, rs, (l & 15) * SKEY, KEY_AT(0, KREP())); }
            if constexpr (s == FSL && n == 1) {
              asm volatile("" : "=v"(fx[0]), "=v"(fx[1]));
              if (w == 0) {
                wait_vm<SINCE(FKL)>();
                asm volatile("" : "+v"(kv)::"memory");
                const unsigned k_ = key_wait(rs, T, l, 0, KREP(), kv);
                if (l == 0) *(LDS_AS unsigned*)(smem + L_SKEY + 32) = k_;
                fkey = k_;
                ld16h<0>(fx[0], rs, (l >> 3) * 256 + (l & 7) * 16, cand_off(k_, 0)); ld16h<2048>(fx[1], rs, (l >> 3) * 256 + (l & 7) * 16, cand_off(k_, 0));
              }
            }
            if constexpr (s == FS0 - 1 && n == FSN) {
              if (w == 0) {
                wait_vm<vm_at(1, s, n) - vm_at(1, FSL, 2)>();
                asm volatile("" : "+v"(fx[0]), "+v"(fx[1])::"memory");
                word_wait<2>(rs, T, (l >> 3) * 256 + (l & 7) * 16, cand_off(fkey, 0), fx);
                *(LDS_AS u32x4*)(smem + L_ROW + l * 16) = fx[0]; *(LDS_AS u32x4*)(smem + L_ROW + 1024 + l * 16) = fx[1];
                count_in(0);
              }
            }
            if constexpr (s == FS0 - 1 && n == 1) {
              asm volatile("" : "=v"(dfr[0]), "=v"(dfr[1]), "=v"(dfr[2]), "=v"(dfr[3]), "=v"(dfr[4]), "=v"(dfr[5]), "=v"(dfr[6]), "=v"(dfr[7]));
              if (ffn_on) {
#pragma unroll
                for (int i = 0; i < 8; ++i) dfr[i] = trread(smem, L_DN + w * 4096 + g * 1024 + ((l & 15) >> 2) * 256 + i * 32 + (l & 3) * 8);
              }
            }
            if constexpr (s == FS0) {
              if constexpr (n == 0) qc = __hip_atomic_load(qcnt, __ATOMIC_RELAXED, __HIP_MEMORY_SCOPE_WORKGROUP);
              if constexpr (n == 1) {
                count_wait(0, 1);
                kv = *(const LDS_AS unsigned*)(smem + L_SKEY + 32);
#pragma unroll
                for (int i = 0; i < 4; ++i) fx[i] = *(const LDS_AS u32x4*)(smem + L_ROW + w * 256 + i * 64 + g * 16);
              }
              if constexpr (n == 2) {
                fkey = __builtin_amdgcn_readfirstlane(kv);
                sel0 = key_row(fkey);
                aff = (int)(sel0 >> 8) == p;
                asm volatile("" : "=v"(sa), "=v"(sb));
                if (ffn_on) ffn_gu(gv, uv, fx, sa, sb);
              }
              if constexpr (n == 4) {
                if (ffn_on) {
                  ffn_gu_store(smem, l, w, sa, sb);
                  count_in(1);
                }
              }
            }
            if constexpr (s == FS0D) {
              if constexpr (n == 0) qc = __hip_atomic_load(qcnt + 1, __ATOMIC_RELAXED, __HIP_MEMORY_SCOPE_WORKGROUP);
              if constexpr (n == 1) { asm volatile("" : "=v"(sa)); if (ffn_on) { count_wait(1, 8); sa = ffn_d_read(smem, l); } }
              if constexpr (n == 2) { asm volatile("" : "=v"(hh)); if (ffn_on) hh = ffn_d_h(sa); }
              if constexpr (n == 4) { asm volatile("" : "=v"(sa)); if (ffn_on) sa = ffn_d_all(dfr, hh); }
              if constexpr (n == 5) { if (ffn_on) ffn_d_store(rs, l, w, 0, slice, sa); }
            }
            if constexpr (s == FS1 && n == 4) {
              DEF4(fx);
              asm volatile("" : "=v"(cxn), "=v"(cx1));
              if (!ffn_on) {
                const unsigned G = xcd * 4 + bi - 28;
#pragma unroll
                for (int k = 0; k < 4; ++k) ld16h<0>(fx[k], rs, RED_VO(k), RED_SO(0, G));
                ld8h(cxn, rs, RED_JJ(), cand_off(fkey, 0) + XO(G)); ld8h(cx1, rs, RED_JJ() + 128, cand_off(fkey, 0) + XO(G));
              }
            }
            if constexpr (s == FS1S && n == 2) {
              if (!ffn_on) {
                wait_vm<SINCE(FS1)>();
                PINV4(fx);
                asm volatile("" : "+v"(cxn), "+v"(cx1)::"memory");
                red_sum(rs, smem, T, tid, w, 0, xcd * 4 + bi - 28, fkey, fx, cxn, cx1);
                BAR();
                if (w == 0) {
                  const u32x2 ov = red_final(smem, l, fkey, cxn, cx1);
                  if (l < 8) st8(unpoison2(ov), rs, OFF_XNEW + (xcd * 4 + bi - 28) * 256 + ((l & 3) * 2 + (l >> 2)) * 8, 0);
                }
              }
            }
            if constexpr (s == FXA && n == 4) { asm volatile("" : "=v"(fx[0])); if (w == 1) ld16h<0>(fx[0], rs, ((l >> 2) & 3) * 256 + (l & 3) * 16, XP_OFF()); }
            if constexpr (s == FXB - 1 && n == FSN) {
              if (w == 1) {
                wait_vm<SINCE(FXA)>();
                asm volatile("" : "+v"(fx[0])::"memory");
                word_wait<1>(rs, T, ((l >> 2) & 3) * 256 + (l & 3) * 16, XP_OFF(), fx);
                if (l < 16) *(LDS_AS u32x4*)(smem + L_ROW + l * 16) = fx[0];
                count_in(0);
              }
            }
            if constexpr (s == FXB) {
#define FX_FRAG(i) (*(const LDS_AS u32x4*)(size_t)(bo[0][1] + (6 * (i) + w) * 1024))
              if constexpr (n == 0) { qc = __hip_atomic_load(qcnt, __ATOMIC_RELAXED, __HIP_MEMORY_SCOPE_WORKGROUP); }
              if constexpr (n == 1) {
                DEF4(fx); DEF4(df);
                if (w < 6) {
                  count_wait(0, 2);
#pragma unroll
                  for (int i = 0; i < 4; ++i) { fx[i] = *(const LDS_AS u32x4*)(smem + L_ROW + i * 64 + g * 16); df[i] = FX_FRAG(i); }
                }
              }
              if constexpr (n == 2) { asm volatile("" : "=v"(sa), "=v"(sb)); if (w < 6) dot_mma(fx); }
              if constexpr (n == 4) { if (w < 6) dot_store(OFF_FX); }
            }
            if constexpr (s == FXL && n == 4) {
              asm volatile("" : "=v"(fx[0]), "=v"(fx[1]));
              if (aff && w < 4) { ld16h<0>(fx[0], rs, FP_VO(), FP_OFF(0)); ld16h<0>(fx[1], rs, FP_VO(), FP_OFF(1)); }
            }
            if constexpr (s == FXS && n == 4) {
              if (aff && w < 4) {
                wait_vm<0>();
                asm volatile("" : "+v"(fx[0]), "+v"(fx[1])::"memory");
                if (!FX_OK(2)) {
                  unsigned spins = 0;
                  do {
                    if (spin_fail(spins, rs, T)) break;
                    fx[0] = ld16(rs, FP_VO(), FP_OFF(0)); fx[1] = ld16(rs, FP_VO(), FP_OFF(1));
                  } while (!FX_OK(2));
                }
                if (w == 3) {
                  f32x2 nx[8];
                  unpack8v(fx[0], nx); unpack8v(fx[1], nx + 4);
                  const f32x2 sm = ((nx[0] + nx[1]) + (nx[2] + nx[3])) + ((nx[4] + nx[5]) + (nx[6] + nx[7]));
                  const float mean = sum64(hadd(sm)) * (1.0f / 1024.0f);
                  const f32x2 m2 = {mean, mean};
                  f32x2 q = {0.f, 0.f};
#pragma unroll
                  for (int j = 0; j < 8; ++j) { const f32x2 d = nx[j] - m2; q = __builtin_elementwise_fma(d, d, q); }
                  const float rd = ln_rd(sum64(hadd(q)) * (1.0f / 1024.0f));
                  if (l == 0) *(LDS_AS f32x2*)(smem + L_FXL + 384) = f32x2{rd, -rbf1(mean)};
                } else {
                  tile_sum(fx[0], L_FXL);
                }
                count_in(3);
              }
            }
          }
        };
        static_for<32>([&](auto sc_) __attribute__((always_inline)) {
          constexpr int s = decltype(sc_)::value;
          if constexpr (s > 0) PIN_ACC();
          if constexpr (IT == 1) wait_vm<vm_at(IT, s, 0) - vm_a(IT, s)>();
          pin_w<s % WA>(af[s % WA][0], af[s % WA][1]);
          SB();
          static_for<6>([&](auto nc_) __attribute__((always_inline)) {
            constexpr int n = decltype(nc_)::value;
            if constexpr (s == 0) { acc[0][n] = f32x4{0.f, 0.f, 0.f, 0.f}; acc[1][n] = acc[0][n]; }
            constexpr bool on = s < QL || n >= 2;
            if constexpr (on) acc[0][n] = n < 4 ? mfma16(bfr[n].v, af[s % WA][0], acc[0][n]) : mfma16(af[s % WA][0], bfr[n].v, acc[0][n]);
            SB();
            if constexpr (n == 0) { QKV_RDB(s, 5); }
            else if constexpr (s + 1 < 32 && (s + 1 < QL || n > 2)) QKV_RDB(s + 1, n - 1);
            SB();
            if constexpr (on) acc[1][n] = n < 4 ? mfma16(bfr[n].v, af[s % WA][1], acc[1][n]) : mfma16(af[s % WA][1], bfr[n].v, acc[1][n]);
            SB();
            if constexpr (s + 1 < 32 && (s + 1 < QL || n >= 2)) QKV_RDA(s + 1, n);
            SB();
            gap(sc_, nc_);
            SB();
          });
        });
        PIN_ACC();
        frag_t qw[32 - QL][2];
        static_for<32 - QL>([&](auto c_) __attribute__((always_inline)) {
          constexpr int s = QL + decltype(c_)::value;
          QKV_RDA(s, 0); QKV_RDB(s, 0); QKV_RDA(s, 1); QKV_RDB(s, 1);
          qw[s - QL][0] = bfr[0]; qw[s - QL][1] = bfr[1];
        });
        SB();
        const bool own = aff && (int)((sel0 >> 5) & 7) == rt;
        const bool m0 = !(sel0 & 16), mq = (l & 15) == (int)(sel0 & 15), mv = g == (int)((sel0 & 15) >> 2);
        auto patch_qk = [&](int n) __attribute__((always_inline)) {
          f32x4 t = *(const LDS_AS f32x4*)(smem + L_FXL + n * 64 + g * 16);
          const f32x4 c = *(const LDS_AS f32x4*)(smem + L_CSL + n * 64 + g * 16);
          const float nb = *(const LDS_AS float*)(smem + L_FXL + 388);
#pragma unroll
          for (int e = 0; e < 4; ++e) { t[e] = __builtin_fmaf(c[e], nb, t[e]); asm volatile("" : "+v"(t[e])); }
          acc[0][n] = (mq && m0) ? t : acc[0][n];
          acc[1][n] = (mq && !m0) ? t : acc[1][n];
        };
        if (own) {
          while (__hip_atomic_load(qcnt + 3, __ATOMIC_RELAXED, __HIP_MEMORY_SCOPE_WORKGROUP) < 4u) {}
          asm volatile("" ::: "memory");
          patch_qk(2); patch_qk(3);
          const f32x2 nab = *(const LDS_AS f32x2*)(smem + L_FXL + 384);
          const LDS_AS float* fl_ = (const LDS_AS float*)(smem + L_FXL + 256) + (l & 15);
          const LDS_AS float* cl_ = (const LDS_AS float*)(smem + L_CSL + 256) + (l & 15);
          const float t4 = __builtin_fmaf(cl_[0], nab[1], fl_[0]), t5 = __builtin_fmaf(cl_[16], nab[1], fl_[16]);
          const unsigned na = __builtin_bit_cast(unsigned, (float)nab[0]);
#pragma unroll
          for (int a = 0; a < 2; ++a) {
            const bool ma = a ? !m0 : m0;
            aq[a] = (mq && ma) ? na : aq[a];
#pragma unroll
            for (int e = 0; e < 4; ++e) {
              const bool me = mv && ma && e == (int)(sel0 & 3);
              acc[a][4][e] = me ? t4 : acc[a][4][e]; acc[a][5][e] = me ? t5 : acc[a][5][e];
              av[a][e] = me ? na : av[a][e];
            }
          }
        }
        constexpr float C2 = 1.4426950408889634f * 0.17677669529663687f;
#pragma unroll
        for (int a = 0; a < 2; ++a) {
          f32x2 a2 = {asf(aq[a]), asf(aq[a])};
          qs2[a] = f32x2{asf(aq[a]) * C2, asf(aq[a]) * C2};
          asm volatile("" : "+v"(a2), "+v"(qs2[a]));
          acc[a][2] *= f32x4{a2[0], a2[1], a2[0], a2[1]}; acc[a][3] *= f32x4{a2[0], a2[1], a2[0], a2[1]};
          acc[a][4] *= __builtin_bit_cast(f32x4, av[a]); acc[a][5] *= __builtin_bit_cast(f32x4, av[a]);
        }
        k0 = pack8(acc[0][2], acc[0][3]); k1 = pack8(acc[1][2], acc[1][3]); v0 = pack8(acc[0][4], acc[1][4]); v1 = pack8(acc[0][5], acc[1][5]);
        const unsigned kvo = OFF_KV + IT * (8u << 20) + (p * 32 + h) * 32768 + rt * 4096;
        if (p < 7) { st16(k0, rs, l * 16, kvo); st16(k1, rs, l * 16 + 1024, kvo); st16(v0, rs, l * 16 + 2048, kvo); st16(v1, rs, l * 16 + 3072, kvo); }
        char* ownp = smem + (8 + rt) * 2048 + l * 16;
        *(u32x4*)(ownp + L_K) = k0;
        if (rt < 7) *(u32x4*)(ownp + L_K + 1024) = k1;
        *(u32x4*)(ownp + L_V) = v0; *(u32x4*)(ownp + L_V + 1024) = v1;
        SB();
        static_for<32 - QL>([&](auto c_) __attribute__((always_inline)) {
          constexpr int s = QL + decltype(c_)::value;
#pragma unroll
          for (int n = 0; n < 2; ++n) { acc[0][n] = mfma16(qw[s - QL][n].v, af[s % WA][0], acc[0][n]); acc[1][n] = mfma16(qw[s - QL][n].v, af[s % WA][1], acc[1][n]); }
        });
        if (own) { patch_qk(0); patch_qk(1); }
      };
      qkv_loop(it_c);
    }
#undef A_LOAD1
#undef QKV_RDA
#undef QKV_RDB
#undef QKV_BO
    {
      IDS();
      const GAS bf16 *pwo, *pwg, *pwu;
      asm volatile("s_load_dwordx2 %0, %3, 24\n\ts_load_dwordx2 %1, %3, 40\n\ts_load_dwordx2 %2, %3, 48" : "=&s"(pwo), "=&s"(pwg), "=&s"(pwu) : "s"(ka));
      const int ql = l & 15;
      const f32x4 z4 = {0.f, 0.f, 0.f, 0.f}, ninf = {-INFINITY, -INFINITY, -INFINITY, -INFINITY};
      LDS_AS unsigned* rdy = (LDS_AS unsigned*)(smem + L_RDY);
      const unsigned rtag = T[0] + it;
      u32x4 qf[2];
      f32x4 cn[2], oa[2][2], sum[2], S[2][2];
      bool told = false;
      auto announce = [&](bool now) __attribute__((always_inline)) {
        if (!told) {
          unsigned st;
          asm volatile("s_getreg_b32 %0, hwreg(HW_REG_IB_STS)" : "=s"(st)::"memory");
          if (now || !(st & 0xC0000Fu)) {
            DRAIN();
            if (l == 0) st8(T, rs, rt * SKW, F_KV + (it * 256 + p * 32 + h) * SKV);
            told = true;
          }
        }
      };
      unsigned one2 = 0x3F803F80u;
      asm volatile("" : "+v"(one2));
      const u32x4 ones = {one2, one2, one2, one2};
      auto ex8 = [&](f32x4 a, f32x4 b) __attribute__((always_inline)) {
        return u32x4{pack2(__builtin_amdgcn_exp2f(a[0]), __builtin_amdgcn_exp2f(a[1])), pack2(__builtin_amdgcn_exp2f(a[2]), __builtin_amdgcn_exp2f(a[3])),
                     pack2(__builtin_amdgcn_exp2f(b[0]), __builtin_amdgcn_exp2f(b[1])), pack2(__builtin_amdgcn_exp2f(b[2]), __builtin_amdgcn_exp2f(b[3]))};
      };
      {
#pragma unroll
        for (int a = 0; a < 2; ++a) {
          const f32x4 c4 = {qs2[a][0], qs2[a][1], qs2[a][0], qs2[a][1]};
          qf[a] = pack8(acc[a][0] * c4, acc[a][1] * c4);
        }
        f32x4 mk;
#pragma unroll
        for (int e = 0; e < 4; ++e) mk[e] = (4 * g + e <= ql) ? 0.f : -INFINITY;
        const f32x4 d0 = mfma16(k0, qf[0], mk), d1 = mfma16(k1, qf[1], mk), d2 = mfma16(k0, qf[1], z4);
        const float m0 = -max_x32(max_x16(fmaxf(fmaxf(d0[0], d0[1]), fmaxf(d0[2], d0[3]))));
        const float m1 = -max_x32(max_x16(fmaxf(fmaxf(d1[0], d1[1]), fmaxf(d1[2], d1[3]))));
        cn[0] = f32x4{m0, m0, m0, m0}; cn[1] = f32x4{m1, m1, m1, m1};
        asm volatile("" : "+v"(cn[0]), "+v"(cn[1]));
        announce(false);
        const f32x4 e0 = d0 + cn[0];
        const u32x4 p0 = {pack2(__builtin_amdgcn_exp2f(e0[0]), __builtin_amdgcn_exp2f(e0[1])), pack2(__builtin_amdgcn_exp2f(e0[2]), __builtin_amdgcn_exp2f(e0[3])), 0u, 0u};
        const u32x4 p1 = ex8(d2 + cn[1], d1 + cn[1]);
        oa[0][0] = mfma16(v0, p0, z4); oa[0][1] = mfma16(v1, p0, z4); sum[0] = mfma16(ones, p0, z4);
        oa[1][0] = mfma16(v0, p1, z4); oa[1][1] = mfma16(v1, p1, z4); sum[1] = mfma16(ones, p1, z4);
      }
      const char* kb_ = smem + L_K + l * 16;
      const char* vb_ = smem + L_V + l * 16;
      const unsigned wov = WO_VOFF(), fnv = (l >> 1) * (F_ * 2) + (l & 1) * 16;
      asm volatile("" : "+v"(oa[0][0]), "+v"(oa[0][1]), "+v"(oa[1][0]), "+v"(oa[1][1]), "+v"(sum[0]), "+v"(sum[1]));
      const unsigned pw = __builtin_amdgcn_readfirstlane(it * 256 + p * 32 + h - 32), pfo = F_KV + pw * SKV + rt * SKW;
      const bool fetcher = rt < 4 && p > 0;
      announce(false);
      BAR();
      asm volatile("" : "+s"(pwo), "+s"(pwg), "+s"(pwu));
#define KV_FETCH(gd)                                                                                                          \
  {                                                                                                                           \
    const unsigned pvo = OFF_KV + pw * 32768 + (gd) * 4096;                                                                   \
    dma16s<0>(rs, l * 16, pvo, lbase + L_K + (gd) * 2048); dma16s<1024>(rs, l * 16, pvo, lbase + L_K + (gd) * 2048);           \
    dma16s<0>(rs, l * 16, pvo + 2048, lbase + L_V + (gd) * 2048); dma16s<1024>(rs, l * 16, pvo + 2048, lbase + L_V + (gd) * 2048); \
  }
      auto fetch = [&]() __attribute__((always_inline)) {
        poll_flags(rs, KVJ ? (l & 1) * 4 * SKW : 0, pfo, T);
        KV_FETCH(rt);
        if (KVJ) { KV_FETCH(rt + 4); if (l == 0) rdy[8 + rt] = rtag; }
      };
      announce(false);
      const bool first = fetcher && told;
      if (first) fetch();
#define LDS16(a_) (*(const LDS_AS u32x4*)(size_t)(a_))
      u32x4 kf[2], vf[2], p0, p1;
#define AE(a, i) { S[a][(i) >> 2][(i) & 3] = __builtin_amdgcn_exp2f(S[a][(i) >> 2][(i) & 3]); asm volatile("" : "+v"(S[a][(i) >> 2][(i) & 3])); SB(); }
#define AC(a, P, j) { P[j] = pack2(S[a][(j) >> 1][2 * ((j) & 1)], S[a][(j) >> 1][2 * ((j) & 1) + 1]); asm volatile("" : "+v"(P[j])); SB(); }
#define PHASE(a, P, M1, M2, L2, M3, M4, L4, M5)                                                                               \
  AE(a, 0) AE(a, 1) M1; SB(); AE(a, 2) AE(a, 3) M2; SB(); L2; SB(); AC(a, P, 0) AC(a, P, 1) AE(a, 4) M3; SB(); AE(a, 5) AE(a, 6) M4; SB(); \
  L4; SB(); AE(a, 7) AC(a, P, 2) M5; SB(); AC(a, P, 3)
#define PV(a, d, P) oa[a][d] = mfma16(vf[d], P, oa[a][d])
#define RS(a, P) sum[a] = mfma16(ones, P, sum[a])
#define SC(a, t, c) S[a][t] = mfma16(kf[t], qf[a], c)
#define K_AT(ad) (kf[0] = LDS16(ad), kf[1] = LDS16((ad) + 1024))
#define V_AT(ad) (vf[0] = LDS16((ad) + L_V), vf[1] = LDS16((ad) + L_V + 1024))
#define A_FIRST(ad, c00, c01, c10, c11, KN) { K_AT(ad); V_AT(ad); SB(); SC(0, 0, c00); SC(0, 1, c01); SB(); PHASE(0, p0, SC(1, 0, c10), SC(1, 1, c11), KN, , , , ) }
#define A_NEXT(ad, c00, c01, c10, c11, KN)                                                                                        \
  { PHASE(1, p1, PV(0, 0, p0), PV(0, 1, p0), , SC(0, 0, c00), SC(0, 1, c01), , RS(0, p0))                                     \
    PHASE(0, p0, PV(1, 0, p1), PV(1, 1, p1), V_AT(ad), SC(1, 0, c10), SC(1, 1, c11), KN, RS(1, p1)) }
#define A_LAST() { PHASE(1, p1, PV(0, 0, p0), PV(0, 1, p0), , RS(0, p0), , , ) SB(); PV(1, 0, p1); PV(1, 1, p1); RS(1, p1); }
      int nw = 0;
      auto piece = [&]() __attribute__((always_inline)) {
        if constexpr (it == 0) { if (ffn_on && nw < 8) { FFN_DMA(nw); } ++nw; }
      };
      unsigned ad = lbase + l * 16 + (6 + rt) * 2048;
      asm volatile("" : "+v"(ad));
      if (rt > 0) {
        A_FIRST(ad + 2048, cn[0], cn[0], cn[1], cn[1], K_AT(ad));
#pragma unroll 1
        for (int i = rt - 1; i > 0; --i) {
          ad -= 2048;
          asm volatile("" : "+v"(ad));
          if (PCE && told && rt >= 4) piece();
          announce(false);
          A_NEXT(ad + 2048, cn[0], cn[0], cn[1], cn[1], K_AT(ad));
        }
        announce(false);
        A_LAST();
      }
      announce(true);
      {
        f32x4 mk;
#pragma unroll
        for (int e = 0; e < 4; ++e) mk[e] = (4 * g + e > ql) ? 0.f : -INFINITY;
        f32x4 cm0 = cn[0] + mk, cm1 = cn[1] + mk, cmi = ninf;
        asm volatile("" : "+v"(cm0), "+v"(cm1), "+v"(cmi));
        if (rt >= 4) {
          if (p > 0) {
            while (!__all(__hip_atomic_load(rdy + 8 + (l & 3), __ATOMIC_RELAXED, __HIP_MEMORY_SCOPE_WORKGROUP) == rtag)) __builtin_amdgcn_s_sleep(1);
            asm volatile("" ::: "memory");
          }
          if constexpr (it == 0) { for (; nw < 8;) piece(); }
          static_for<it ? 12 : 8>([&](auto c_) __attribute__((always_inline)) { constexpr int i = decltype(c_)::value; WO_PIECE_W(15 - i / 2, 2 * (rt & 3) + (i & 1)); });
        } else if (p > 0) {
          if (!first) fetch();
          if (KVJ) {
            DRAIN();
            if (l < 2) rdy[rt + 4 * l] = rtag;
            if constexpr (it == 0) { for (; nw <= rt;) piece(); }
          } else {
            const u32x2 f2 = ld8(rs, 0, pfo + 4 * SKW);
            const bool up = __all(f2[0] == T[0] && f2[1] == T[1]);
            DRAIN();
            if (l == 0) rdy[rt] = rtag;
            if (!up) poll_flags(rs, 0, pfo + 4 * SKW, T);
            KV_FETCH(rt + 4);
            if (l == 0) rdy[8 + rt] = rtag;
            told = false;
          }
        }
        if (p > 0) {
          auto landed = [&](bool now) __attribute__((always_inline)) {
            if (!told) {
              unsigned st;
              asm volatile("s_getreg_b32 %0, hwreg(HW_REG_IB_STS)" : "=s"(st)::"memory");
              if (now || !(st & 0xC0000Fu)) {
                DRAIN();
                if (l == 0) rdy[rt + 4] = rtag;
                told = true;
              }
            }
          };
          unsigned have = 0;
          auto need = [&](int gq) __attribute__((always_inline)) {
            if (__builtin_expect(gq < 8 && !((have >> gq) & 1), 0)) {
              do {
                const unsigned t = __hip_atomic_load(rdy + (l & 7), __ATOMIC_RELAXED, __HIP_MEMORY_SCOPE_WORKGROUP);
                have = (unsigned)__builtin_amdgcn_ballot_w64(t == rtag);
              } while (!((have >> gq) & 1));
              asm volatile("" ::: "memory");
            }
          };
          ad = lbase + l * 16 + rt * 2048;
          asm volatile("" : "+v"(ad));
          need(rt);
          K_AT(ad); V_AT(ad); SB(); SC(0, 0, cm0); SC(0, 1, cn[0]); SB();
          PHASE(0, p0, SC(1, 0, cmi), SC(1, 1, cm1), , , , , )
          if (!KVJ) landed(false);
          need(rt + 1);
          K_AT(ad + 2048);
#pragma unroll 1
          for (int gq = rt + 1; gq < 8; ++gq) {
            ad += 2048;
            asm volatile("" : "+v"(ad));
            if (KVJ) piece();
            else { landed(gq >= rt + KVD); if (told) { piece(); if (PC2) piece(); } }
            need(gq + 1);
            A_NEXT(ad, cn[0], cn[0], cn[1], cn[1], K_AT(ad + 2048));
          }
          A_LAST();
        }
        if constexpr (it == 0) { for (; nw < 8;) piece(); }
      }
      {
        float big = 0.f;
#pragma unroll
        for (int a = 0; a < 2; ++a)
#pragma unroll
          for (int d = 0; d < 2; ++d)
            big = __builtin_elementwise_maximum(__builtin_elementwise_maximum(big, __builtin_elementwise_maximum(fabsf(oa[a][d][0]), fabsf(oa[a][d][1]))),
                                                __builtin_elementwise_maximum(fabsf(oa[a][d][2]), fabsf(oa[a][d][3])));
        bool ok = sum[0][0] >= 0.5f && sum[0][0] < 0x1p64f && sum[1][0] >= 0.5f && sum[1][0] < 0x1p64f && big < INFINITY;
        if (!__all(ok)) {
#pragma unroll
          for (int a = 0; a < 2; ++a) {
            const int qs = 2 * rt + a, qq = 256 + 16 * qs + ql, klo = max(qq - 255, p > 0 ? 0 : 256);
            auto score = [&](int t) __attribute__((always_inline)) {
              f32x4 s = mfma16(t == 31 ? k1 : *(const u32x4*)(kb_ + t * 1024), qf[a], z4);
#pragma unroll
              for (int e = 0; e < 4; ++e) { const int kk = 16 * t + 4 * g + e; s[e] = (kk <= qq && kk >= klo) ? s[e] : -INFINITY; }
              return s;
            };
            float mx = -INFINITY;
#pragma unroll 1
            for (int t = p > 0 ? qs : 16; t <= qs + 16; ++t) { const f32x4 s = score(t); mx = fmaxf(fmaxf(mx, fmaxf(s[0], s[1])), fmaxf(s[2], s[3])); }
            mx = max_x32(max_x16(mx));
            oa[a][0] = z4; oa[a][1] = z4; sum[a] = z4;
#pragma unroll 1
            for (int G = p > 0 ? qs >> 1 : 8; G <= (qs + 16) >> 1; ++G) {
              const f32x4 s0 = score(2 * G), s1 = score(2 * G + 1);
              f32x4 e0, e1;
#pragma unroll
              for (int e = 0; e < 4; ++e) { e0[e] = __builtin_amdgcn_exp2f(s0[e] - mx); e1[e] = __builtin_amdgcn_exp2f(s1[e] - mx); }
              const u32x4 pf = pack8(e0, e1);
              oa[a][0] = mfma16(*(const u32x4*)(vb_ + G * 2048), pf, oa[a][0]);
              oa[a][1] = mfma16(*(const u32x4*)(vb_ + G * 2048 + 1024), pf, oa[a][1]);
              sum[a] = mfma16(ones, pf, sum[a]);
            }
          }
        }
      }
#pragma unroll
      for (int a = 0; a < 2; ++a) {
        const float inv = __builtin_amdgcn_rcpf(sum[a][0]);
        oa[a][0] *= inv; oa[a][1] *= inv;
      }
      const unsigned oo = OFF_O + it * (4u << 20) + ((p * 8 + rt) * 64 + h * 2) * 1024;
      st16(pack8(oa[0][0], oa[0][1]), rs, l * 16, oo);
      st16(pack8(oa[1][0], oa[1][1]), rs, l * 16 + 1024, oo);
      DRAIN();
      BAR();
      if (tid == 0) st8(T, rs, h * SO, F_O + (it * 8 + p) * 32 * SO);
      if constexpr (it == 0) {
        DEF4(gv); DEF4(uv);
        if (ffn_on) {
          const unsigned ro = 65536 + w * 1024 + (4 * g + ((l & 15) >> 2)) * 32 + (l & 3) * 8;
#pragma unroll
          for (int i = 0; i < 4; ++i) {
            frag_t f;
            f.hf.a = trread(smem, ro + i * 16384); f.hf.b = trread(smem, ro + i * 16384 + 512);
            gv[i] = f.v;
            f.hf.a = trread(smem, ro + i * 16384 + 8192); f.hf.b = trread(smem, ro + i * 16384 + 8704);
            uv[i] = f.v;
          }
        }
        PINV4(gv); PINV4(uv);
        WO_PIECE(11); WO_PIECE(10); WO_PIECE(9); WO_PIECE(8); WO_PIECE(7); WO_PIECE(6); WO_PIECE(5); WO_PIECE(4);
      }
      WO_PIECE(3); WO_PIECE(2); WO_PIECE(1); WO_PIECE(0);
    }
    IDS();
    ARGP(pwr, 4); ARGP(pwd, 7); ARGP(pout, 8); ARGP(px, 0);
    const int kh = w >> 2, rt2 = rb * 4 + (w & 3);
    const unsigned R = rt2 * 32 + kh * 16 + (l & 15);
    const unsigned to = (rt2 * 64 + ct * 4 + kh) * 1024 + l * 16;
    const unsigned rmo = ct * 64 + ((g & 1) ? 12 + 4 * g : 4 * g);
    f32x2 wv[8];
    {
      u32x4 wa[2], wrp[2], res[2];
#pragma unroll
      for (int j = 0; j < 2; ++j) {
        wrp[j] = *(const GAS u32x4*)(pwr + rmo + 32 * j);
        wa[j] = *(const GAS u32x4*)(pwr + l * 8 + j * 512);
        if constexpr (it == 0) res[j] = *(const GAS u32x4*)(px + R * 1024 + rmo + 32 * j);
      }
      {
        f32x2 t[8];
        unpack8v(wa[0], t); unpack8v(wa[1], t + 4);
        const f32x2 s = ((t[0] + t[1]) + (t[2] + t[3])) + ((t[4] + t[5]) + (t[6] + t[7]));
        wsum = sum64(hadd(s));
        unpack8v(trade16(wrp[0]), wv); unpack8v(trade16(wrp[1]), wv + 4);
        if constexpr (it == 0) { res[0] = trade16(res[0]); res[1] = trade16(res[1]); asm volatile("" : "+v"(res[0]), "+v"(res[1])); }
        asm volatile("" : "+v"(wsum), "+v"(wv[0]), "+v"(wv[1]), "+v"(wv[2]), "+v"(wv[3]), "+v"(wv[4]), "+v"(wv[5]), "+v"(wv[6]), "+v"(wv[7]));
      }
      unsigned bt[4];
#pragma unroll
      for (int n = 0; n < 4; ++n) {
        bt[n] = lbase + 32768 + 65536 + kh * 4096 + (4 * g + ((l & 15) >> 2)) * 128 + (l & 3) * 8 + ((n ^ (2 * (g & 1) + ((l & 15) >> 3))) << 5);
        asm volatile("" : "+v"(bt[n]));
      }
      if (w == 0) poll_flags(rs, (l & 31) * SO, F_O + (it * 8 + (rb >> 1)) * 32 * SO, T);
      else if (it == 0 && w < 3) poll_flags2(rs, F_XN + (((w - 1) * 4 + g) * RX + bi % RX) * 256 + (l & 15) * 16, T);
      asm volatile("s_barrier" ::: "memory");
      const unsigned abase = OFF_O + it * (4u << 20) + (rt2 * 64 + 32 * kh) * 1024;
      const unsigned av0 = l * 16 + kh * 1024, av1 = l * 16 + (kh ^ 1) * 1024;
      u32x4 af[WB][2];
#define O_LOAD(s_) { af[(s_) % WB][0] = ld16(rs, av0 + (~(s_) & 1) * 2048, abase + ((15 - (s_)) >> 1) * 4096); af[(s_) % WB][1] = ld16(rs, av1 + (~(s_) & 1) * 2048, abase + ((15 - (s_)) >> 1) * 4096); }
#pragma unroll
      for (int s = 0; s < WR; ++s) O_LOAD(s);
#pragma unroll
      for (int j = 0; j < 2; ++j) {
        if constexpr (it == 1) res[j] = ld16(rs, OFF_XNEW + (2 * ct + j) * 256 + g * 16, 0);
      }
      SB();
      f32x4 ac[2][4];
#pragma unroll
      for (int a = 0; a < 2; ++a)
#pragma unroll
        for (int n = 0; n < 4; ++n) ac[a][n] = f32x4{0.f, 0.f, 0.f, 0.f};
      u32x4 bfr[2][4];
#define WO_RD(buf, s, n)                                                                             \
  {                                                                                                  \
    const unsigned o_ = bt[n] + ((15 - (s)) & 7) * 8192;                                             \
    frag_t f_;                                                                                       \
    f_.hf.a = trabs(o_); f_.hf.b = trabs(o_ + 2048);                                                 \
    bfr[buf][n] = f_.v;                                                                              \
  }
#pragma unroll
      for (int n = 0; n < 4; ++n) WO_RD(0, 0, n);
#pragma unroll
      for (int s = 0; s < 16; ++s) {
        const int cur = s & 1;
        asm volatile("" : "+{v[176:179]}"(ac[0][0]), "+{v[180:183]}"(ac[0][1]), "+{v[184:187]}"(ac[0][2]), "+{v[188:191]}"(ac[0][3]),
                          "+{v[192:195]}"(ac[1][0]), "+{v[196:199]}"(ac[1][1]), "+{v[200:203]}"(ac[1][2]), "+{v[204:207]}"(ac[1][3]));
        if (s == (it == 0 ? 3 : 11)) asm volatile("s_barrier" ::: "memory");
        if (s == 7) {
#pragma unroll
          for (int n = 0; n < 4; ++n) { bt[n] -= 65536; asm volatile("" : "+v"(bt[n])); }
        }
        if (it == 0 && s == 8) {
#pragma unroll
          for (int i = 0; i < 3; ++i) st16(keep[i], rs, l * 16 + i * 1024, OFF_WI + (h * 8 + ((ROT0 + 5) & 7)) * STAGE + w * 3072);
        }
        if (it == 0 && s == 10) {
          unsigned pz1 = POISON;
          asm volatile("" : "+v"(pz1));
          const u32x4 pz = {pz1, pz1, pz1, pz1};
          st16(pz, rs, OFF_PART + (tid >> 8) * (1u << 20) + bid * 4096 + (tid & 255) * 16, 0);
          if (w < 2) st16(pz, rs, OFF_STAT + 524288 + (rb * 16 + ct) * 2048 + tid * 16, 0);
          if (w == 4 && l < 40) st16(pz, rs, l < 16 ? OFF_CA + 65536 + rb * 4096 + ct * 256 + l * 16 : OFF_FX + (h * 8 + p) * 512 + (l - 16) * 16, 0);
          if (w == 6 && l < (ct < (int)RKEY ? 3 : 2)) st4(pz1, rs, l < 2 ? OFF_XNEW + (bid >> 3) * 256 + (bid & 7) * 8 + l * 4 : KEY_AT(1, ct) + rb * SKEY, 0);
        }
        if (s < WB - WR) O_LOAD(WR + 2 * s < 16 ? WR + 2 * s : 0);
        SB();
#pragma unroll
        for (int n = 0; n < 4; ++n) {
          ac[0][n] = mfma16(bfr[cur][n], af[s % WB][0], ac[0][n]);
          ac[1][n] = mfma16(bfr[cur][n], af[s % WB][1], ac[1][n]);
          if (s + 1 < 16) WO_RD(cur ^ 1, s + 1, n);
          if (s == 9) {
            if (it == 1 && n == 0) { res[0] = R == sel0 ? res[0] : x2p[0]; res[1] = R == sel0 ? res[1] : x2p[1]; }
            v[2 * n] = f32x2{lo16(res[n >> 1][2 * (n & 1)]), hi16(res[n >> 1][2 * (n & 1)])};
            v[2 * n + 1] = f32x2{lo16(res[n >> 1][2 * (n & 1) + 1]), hi16(res[n >> 1][2 * (n & 1) + 1])};
            asm volatile("" : "+v"(v[2 * n]), "+v"(v[2 * n + 1]));
          }
          SB();
        }
        if (s < WB - WR) { O_LOAD(WR + 2 * s + 1); }
        else if (s + WB < 16) { O_LOAD(s + WB); }
        SB();
      }
#undef WO_RD
#undef O_LOAD
#define XCH_AT(v) ((v) == 7 ? 155648 : (v) * 4096)
#pragma unroll
      for (int n = 0; n < 4; ++n) *(f32x4*)(smem + XCH_AT(w) + n * 1024 + l * 16) = ac[1][n];
#pragma unroll
      for (int n = 0; n < 4; ++n) { v[2 * n] += f32x2{ac[0][n][0], ac[0][n][1]}; v[2 * n + 1] += f32x2{ac[0][n][2], ac[0][n][3]}; }
      BAR();
#pragma unroll
      for (int n = 0; n < 4; ++n) {
        const f32x4 o = *(const f32x4*)(smem + XCH_AT(w ^ 4) + n * 1024 + l * 16);
        v[2 * n] += f32x2{o[0], o[1]}; v[2 * n + 1] += f32x2{o[2], o[3]};
      }
    }
    unsigned crow = 0;
    {
      if (it == 0) { st16(pack8v(v), rs, to, OFF_XN + (4u << 20)); st16(pack8v(v + 4), rs, to + 2048, OFF_XN + (4u << 20)); }
      SB();
      const f32x2 s = ((v[0] + v[1]) + (v[2] + v[3])) + ((v[4] + v[5]) + (v[6] + v[7]));
      f32x2 dp = v[0] * wv[0], q = v[0] * v[0];
#pragma unroll
      for (int j = 1; j < 8; ++j) { dp = __builtin_elementwise_fma(v[j], wv[j], dp); q = __builtin_elementwise_fma(v[j], v[j], q); }
      float mt = hadd(s), qq = hadd(q), dot = hadd(dp);
      add_g3(mt, qq, dot);
      const unsigned so = OFF_STAT + it * 524288 + rb * 32768, lrow = R & 127;
      if (it == 0) wait_vm<2>();
      if (g == 0) st16(unpoison4(__builtin_bit_cast(u32x4, f32x4{mt, qq, dot, 0.f})), rs, ct * 2048 + lrow * 16, so);
      SB();
      if (it == 0) wait_vm<1>();
      if constexpr (it == 0) {
        volatile LDS_AS u32x2* in = (volatile LDS_AS u32x2*)(smem + L_XC);
        if (l == 0) in[w] = T;
        const u32x2 t = in[l & 7];
        if (__all(t[0] == T[0] && t[1] == T[1]) && l < (int)RX1) st8(T, rs, (l * 32 + (rb & 1) * 16 + ct) * SX1, F_X1 + (rb >> 1) * RX1 * 32 * SX1);
      }
      if (it == 0) {
        if (ffn_on) { WD_DMA(); }
#pragma unroll
        for (int j = 0; j < 9; ++j) *(u32x4*)(smem + L_RING + (j / 3) * STAGE + (w * 3 + j % 3) * 1024 + l * 16) = keep[j];
      }
      if constexpr (it == 0) {
        if (w == 0) poll_flags(rs, ((l & 2) * 8 + ((ROT0 + 5) & 7) * 2 + (l & 1)) * SX1, F_X1 + (p * RX1 + (xcd >> 1) % RX1) * 32 * SX1, T);
        if (tid == 192) *(LDS_AS u32x4*)(smem + L_QC) = u32x4{0u, 0u, 0u, 0u};
        BAR();
        PRE_LOAD(1, ROT0 + 5, 0, 4);
      } else {
        __builtin_amdgcn_s_sleep(SDIR);
        u32x4 sp[4];
#pragma unroll
        for (int i = 0; i < 4; ++i) sp[i] = ld16(rs, (4 * g + i) * 2048 + lrow * 16, so);
        const RowFin r = fin_key(rs, T, g * 8192 + lrow * 16, so, smem + L_XC, l, w, R, wsum, sp);
        BAR();
        crow = fin_cand(rs, smem + L_XC, 1, g, rb, ct, r, v);
        fin_x2(r, v);
      }
    }
    if (it == 1) {
      u32x4 op[2];
      {
        char* tp = smem + L_OUT + w * 2048;
        const unsigned c0 = (g & 1) * 2 + (g >> 1), r = l & 15;
        *(u32x4*)(tp + r * 128 + ((c0 ^ (r & 7)) << 4)) = trade16(pack8v(v));
        *(u32x4*)(tp + r * 128 + (((c0 + 4) ^ (r & 7)) << 4)) = trade16(pack8v(v + 4));
#pragma unroll
        for (int j = 0; j < 2; ++j) op[j] = *(const u32x4*)(tp + j * 1024 + (l >> 3) * 128 + (((l ^ (l >> 3)) & 7) << 4));
      }
      const unsigned Ro = R - (l & 15) + (l >> 3), oo = (Ro * 1024 + ct * 64) * 2 + (l & 7) * 16;
#define OUT_STORE(c0, c1)                                                                           \
  {                                                                                                 \
    if (c0) __builtin_amdgcn_raw_buffer_store_b128(op[0], rout, oo, 0, SC1);                        \
    if (c1) __builtin_amdgcn_raw_buffer_store_b128(op[1], rout, oo + 16384, 0, SC1);                \
  }
      if (OUTE) OUT_STORE(Ro != crow, Ro + 8 != crow);
      if (w == 0) {
        __builtin_amdgcn_s_sleep(GSL);
        u32x4 xr[4];
        unsigned cur = 0, best;
        asm volatile("" : "=v"(xr[0]), "=v"(xr[1]));
        for (unsigned spins = 0;;) {
          const unsigned k_ = key_load(rs, l, 1, KREP());
          const bool all = __all(k_ != POISON);
          best = __builtin_amdgcn_readfirstlane(umax16(k_ == POISON ? 0u : k_));
          if (best != cur && ffn_on) {
            cur = best;
            ld16h<0>(xr[0], rs, (l >> 3) * 256 + (l & 7) * 16, cand_off(cur, 1)); ld16h<2048>(xr[1], rs, (l >> 3) * 256 + (l & 7) * 16, cand_off(cur, 1));
          }
          if (all || spin_fail(spins, rs, T)) break;
        }
        if (l == 0) *(LDS_AS unsigned*)(smem + L_SKEY + 32) = best;
        asm volatile("s_waitcnt vmcnt(0)" : "+v"(xr[0]), "+v"(xr[1])::"memory");
        if (ffn_on) {
          word_wait<2>(rs, T, (l >> 3) * 256 + (l & 7) * 16, cand_off(best, 1), xr);
          *(LDS_AS u32x4*)(smem + L_ROW + l * 16) = xr[0]; *(LDS_AS u32x4*)(smem + L_ROW + 1024 + l * 16) = xr[1];
        }
      }
      BAR();
      const unsigned key = __builtin_amdgcn_readfirstlane(*(LDS_AS unsigned*)(smem + L_SKEY + 32)), krow = key_row(key);
      if (ffn_on) {
        u32x4 xv[4];
#pragma unroll
        for (int i = 0; i < 4; ++i) xv[i] = *(const LDS_AS u32x4*)(smem + L_ROW + w * 256 + i * 64 + g * 16);
        f32x4 ga, ua;
        ffn_gu(gv, uv, xv, ga, ua);
        ffn_gu_store(smem, l, w, ga, ua);
        BAR();
        ffn_d_store(rs, l, w, 1, slice, ffn_d_all(dfr, ffn_d_h(ffn_d_read(smem, l))));
        SB();
        if (OSL) __builtin_amdgcn_s_sleep(OSL);
      }
      if (OUTE) OUT_STORE(Ro == crow && Ro != krow, Ro + 8 == crow && Ro + 8 != krow)
      else OUT_STORE(Ro != krow, Ro + 8 != krow);
      if (!ffn_on) {
        const unsigned G = xcd * 4 + bi - 28;
        if (G == 0 && tid == 0) st8(T, rs, F_EP, 0);
        u32x4 pv[4];
        u32x2 cxn, cx1;
        __builtin_amdgcn_s_sleep(RSL);
        red_load(rs, tid, 1, G, key, pv, cxn, cx1);
        red_sum(rs, smem, T, tid, w, 1, G, key, pv, cxn, cx1);
        BAR();
        if (w == 0) {
          const u32x2 ov = red_final(smem, l, key, cxn, cx1);
          if (l < 8) __builtin_amdgcn_raw_buffer_store_b64(ov, rout, (krow * 1024 + G * 32 + l * 4) * 2, 0, 0);
        }
      }
#undef OUT_STORE
    }
  };
  layer(std::integral_constant<int, 0>{});
  layer(std::integral_constant<int, 1>{});
}

extern "C" void kernel_launch(void* const* d_in, const int* in_sizes, int n_in, void* d_out, int out_size,
                              void* d_ws, size_t ws_size, hipStream_t stream) {
  (void)hipFuncSetAttribute((const void*)mega_kernel, hipFuncAttributeMaxDynamicSharedMemorySize, LDS_BYTES);
  mega_kernel<<<256, 512, LDS_BYTES, stream>>>((const bf16*)d_in[0], (char*)d_ws, (const bf16*)d_in[1], (const bf16*)d_in[2], (const bf16*)d_in[4],
                                               (const bf16*)d_in[5], (const bf16*)d_in[6], (const bf16*)d_in[7], (bf16*)d_out);
}
```

```cpp
#include <hip/hip_runtime.h>
#include <stdint.h>
#include <utility>

typedef __bf16 bf16;
typedef __bf16 bf16x8 __attribute__((ext_vector_type(8)));
typedef __bf16 bf16x2 __attribute__((ext_vector_type(2)));
typedef float f32x4 __attribute__((ext_vector_type(4)));
typedef float f32x2 __attribute__((ext_vector_type(2)));
typedef short s16x4 __attribute__((ext_vector_type(4)));
typedef unsigned int u32x4 __attribute__((ext_vector_type(4)));
typedef unsigned int u32x2 __attribute__((ext_vector_type(2)));
#define LDS_AS __attribute__((address_space(3)))
#define GAS __attribute__((address_space(1)))
typedef GAS unsigned gu32;
typedef GAS unsigned long long gu64;
#define RLX_AGENT __ATOMIC_RELAXED, __HIP_MEMORY_SCOPE_AGENT
typedef __amdgpu_buffer_rsrc_t rsrc_t;

constexpr int F_ = 3584;
constexpr int SC1 = 16;

constexpr int RX = 4;
constexpr unsigned F_EP = 0, F_TMO = 128;
constexpr unsigned F_XN = 4096;
#define FLG 256, 1024, 8, 64, 4, 256, 4
constexpr unsigned FLV[7] = {FLG};
constexpr unsigned SO = FLV[0], SKV = FLV[1], SKW = FLV[2], SX1 = FLV[3], RX1 = FLV[4], SKEY = FLV[5], RKEY = FLV[6];
constexpr unsigned F_O = 8u << 20;
constexpr unsigned F_KV = 11u << 20;
constexpr unsigned F_X1 = 9u << 20;
constexpr unsigned OFF_KEY = 10u << 20;
#define KREP() (RKEY == 16 ? bid & 15 : (bid & 7) * RKEY / 8)
#define KEY_AT(L, rep) (OFF_KEY + ((L) * RKEY + (rep)) * (SKEY < 8 ? 128 : 16 * SKEY))
constexpr unsigned OFF_AB = 2u << 20;
constexpr unsigned OFF_XNEW = 262144;
constexpr unsigned OFF_CS = OFF_AB + 65536;
constexpr unsigned OFF_FX = 393216;
constexpr unsigned OFF_CA = 524288;
constexpr unsigned OFF_STAT = 1u << 20;
constexpr unsigned OFF_PART = 3u << 20;
constexpr unsigned OFF_STAMP = 6u << 20;
constexpr unsigned OFF_XN = 8u << 20;
constexpr unsigned OFF_O = 16u << 20;
constexpr unsigned OFF_KV = 32u << 20;
constexpr unsigned OFF_WI = 52u << 20;
constexpr unsigned WS_BYTES = 58u << 20;
constexpr unsigned POISON = 0xFFFFFFFFu;

constexpr int L_ABW = 16640, L_CSP = 17408, L_CSL = 31872, L_FXL = 32256, L_K = 0, L_V = 32768, L_RING = 65536, L_XCH = 0, L_DN = 32768, L_RDY = 31744, L_QC = 31808, LDS_BYTES = 163840;
constexpr int STAGE = 24576;
#define SLOT(t) ((t) & 3)

template <typename F, int... I> __device__ __forceinline__ void static_for_impl(F&& f, std::integer_sequence<int, I...>) {
  (f(std::integral_constant<int, I>{}), ...);
}
template <int N, typename F> __device__ __forceinline__ void static_for(F&& f) { static_for_impl(f, std::make_integer_sequence<int, N>{}); }
__device__ __forceinline__ unsigned pack2(float a, float b) {
  f32x2 v = {a, b};
  return __builtin_bit_cast(unsigned, __builtin_convertvector(v, bf16x2));
}
__device__ __forceinline__ float asf(unsigned u) { return __builtin_bit_cast(float, u); }
__device__ __forceinline__ float lo16(unsigned u) { return __builtin_bit_cast(float, u << 16); }
__device__ __forceinline__ float hi16(unsigned u) { return __builtin_bit_cast(float, u & 0xffff0000u); }
__device__ __forceinline__ u32x4 pack8(const f32x4 a, const f32x4 b) {
  u32x4 o;
  o[0] = pack2(a[0], a[1]); o[1] = pack2(a[2], a[3]); o[2] = pack2(b[0], b[1]); o[3] = pack2(b[2], b[3]);
  return o;
}
__device__ __forceinline__ f32x4 mfma16(u32x4 a, u32x4 b, f32x4 c) {
  return __builtin_amdgcn_mfma_f32_16x16x32_bf16(__builtin_bit_cast(bf16x8, a), __builtin_bit_cast(bf16x8, b), c, 0, 0, 0);
}
__device__ __forceinline__ f32x4 mfma16k16(s16x4 a, s16x4 b, f32x4 c) { return __builtin_amdgcn_mfma_f32_16x16x16bf16_1k(a, b, c, 0, 0, 0); }
template <int CTRL> __device__ __forceinline__ float dppf(float v) {
  return __builtin_bit_cast(float, __builtin_amdgcn_update_dpp(0, __builtin_bit_cast(int, v), CTRL, 0xf, 0xf, false));
}
template <int CTRL> __device__ __forceinline__ unsigned dppu(unsigned v) {
  return (unsigned)__builtin_amdgcn_update_dpp(0, (int)v, CTRL, 0xf, 0xf, false);
}
__device__ __forceinline__ float add_x16(float v) {
  auto r = __builtin_amdgcn_permlane16_swap(__builtin_bit_cast(unsigned, v), __builtin_bit_cast(unsigned, v), false, false);
  return __builtin_bit_cast(float, (unsigned)r[0]) + __builtin_bit_cast(float, (unsigned)r[1]);
}
__device__ __forceinline__ float add_x32(float v) {
  auto r = __builtin_amdgcn_permlane32_swap(__builtin_bit_cast(unsigned, v), __builtin_bit_cast(unsigned, v), false, false);
  return __builtin_bit_cast(float, (unsigned)r[0]) + __builtin_bit_cast(float, (unsigned)r[1]);
}
__device__ __forceinline__ float max_x16(float v) {
  auto r = __builtin_amdgcn_permlane16_swap(__builtin_bit_cast(unsigned, v), __builtin_bit_cast(unsigned, v), false, false);
  return fmaxf(__builtin_bit_cast(float, (unsigned)r[0]), __builtin_bit_cast(float, (unsigned)r[1]));
}
__device__ __forceinline__ float max_x32(float v) {
  auto r = __builtin_amdgcn_permlane32_swap(__builtin_bit_cast(unsigned, v), __builtin_bit_cast(unsigned, v), false, false);
  return fmaxf(__builtin_bit_cast(float, (unsigned)r[0]), __builtin_bit_cast(float, (unsigned)r[1]));
}
__device__ __forceinline__ unsigned umax_x16(unsigned v) {
  auto r = __builtin_amdgcn_permlane16_swap(v, v, false, false);
  return max((unsigned)r[0], (unsigned)r[1]);
}
__device__ __forceinline__ unsigned umax_x32(unsigned v) {
  auto r = __builtin_amdgcn_permlane32_swap(v, v, false, false);
  return max((unsigned)r[0], (unsigned)r[1]);
}
__device__ __forceinline__ float sum_hi8(float v) {
  v += dppf<0x128>(v);
  v = add_x16(v);
  return add_x32(v);
}
__device__ __forceinline__ float sum64(float v) {
  v += dppf<0xB1>(v);
  v += dppf<0x4E>(v);
  v += dppf<0x124>(v);
  v += dppf<0x128>(v);
  v = add_x16(v);
  return add_x32(v);
}

#define BAR() asm volatile("s_waitcnt lgkmcnt(0)\n\ts_barrier" ::: "memory")
#define DRAIN() asm volatile("s_waitcnt vmcnt(0)" ::: "memory")
#define SB() __builtin_amdgcn_sched_barrier(0)
template <int N> __device__ __forceinline__ void wait_vm() { asm volatile("s_waitcnt vmcnt(%0)" ::"n"(N) : "memory"); }

__device__ __forceinline__ void dma16(rsrc_t rs, unsigned voff, unsigned soff, unsigned lds) {
  asm volatile("s_mov_b32 m0, %0\n\ts_nop 0\n\tbuffer_load_dwordx4 %1, %2, %3 offen lds" ::"s"(lds), "v"(voff), "s"(rs), "s"(soff) : "memory");
}
template <int OFF> __device__ __forceinline__ void dma16s(rsrc_t rs, unsigned voff, unsigned soff, unsigned lds) {
  asm volatile("s_mov_b32 m0, %0\n\ts_nop 0\n\tbuffer_load_dwordx4 %1, %2, %3 offen offset:%4 sc1 lds" ::"s"(lds), "v"(voff), "s"(rs), "s"(soff), "n"(OFF) : "memory");
}
__device__ __forceinline__ float rbf1(float x) { return __builtin_bit_cast(float, pack2(0.f, x)); }
__device__ __forceinline__ void vswap(unsigned& a, unsigned& b) { asm volatile("v_swap_b32 %0, %1" : "+v"(a), "+v"(b)); }
__device__ __forceinline__ void vswap(float& a, float& b) { asm volatile("v_swap_b32 %0, %1" : "+v"(a), "+v"(b)); }
template <typename V, int N> __device__ __forceinline__ void vswapn(V& a, V& b) {
#pragma unroll
  for (int i = 0; i < N; ++i) { auto x = a[i], y = b[i]; vswap(x, y); a[i] = x; b[i] = y; }
}
__device__ __forceinline__ u32x4 ld16(rsrc_t rs, unsigned voff, unsigned soff) { return __builtin_amdgcn_raw_buffer_load_b128(rs, voff, soff, SC1); }
template <int OFF> __device__ __forceinline__ void ld16h(u32x4& d, rsrc_t rs, unsigned voff, unsigned soff) {
  asm volatile("buffer_load_dwordx4 %0, %1, %2, %3 offen offset:%4 sc1" : "=v"(d) : "v"(voff), "s"(rs), "s"(soff), "n"(OFF) : "memory");
}
template <int I, int OFF> __device__ __forceinline__ void ld16w(u32x4& d, rsrc_t rs, unsigned voff, unsigned soff) {
  if constexpr (I == 0) asm volatile("buffer_load_dwordx4 %0, %1, %2, %3 offen offset:%4 sc1" : "={v[128:131]}"(d) : "v"(voff), "s"(rs), "s"(soff), "n"(OFF) : "memory");
  else if constexpr (I == 1) asm volatile("buffer_load_dwordx4 %0, %1, %2, %3 offen offset:%4 sc1" : "={v[132:135]}"(d) : "v"(voff), "s"(rs), "s"(soff), "n"(OFF) : "memory");
  else if constexpr (I == 2) asm volatile("buffer_load_dwordx4 %0, %1, %2, %3 offen offset:%4 sc1" : "={v[136:139]}"(d) : "v"(voff), "s"(rs), "s"(soff), "n"(OFF) : "memory");
  else if constexpr (I == 3) asm volatile("buffer_load_dwordx4 %0, %1, %2, %3 offen offset:%4 sc1" : "={v[140:143]}"(d) : "v"(voff), "s"(rs), "s"(soff), "n"(OFF) : "memory");
  else if constexpr (I == 4) asm volatile("buffer_load_dwordx4 %0, %1, %2, %3 offen offset:%4 sc1" : "={v[144:147]}"(d) : "v"(voff), "s"(rs), "s"(soff), "n"(OFF) : "memory");
  else if constexpr (I == 5) asm volatile("buffer_load_dwordx4 %0, %1, %2, %3 offen offset:%4 sc1" : "={v[148:151]}"(d) : "v"(voff), "s"(rs), "s"(soff), "n"(OFF) : "memory");
  else if constexpr (I == 6) asm volatile("buffer_load_dwordx4 %0, %1, %2, %3 offen offset:%4 sc1" : "={v[152:155]}"(d) : "v"(voff), "s"(rs), "s"(soff), "n"(OFF) : "memory");
  else if constexpr (I == 7) asm volatile("buffer_load_dwordx4 %0, %1, %2, %3 offen offset:%4 sc1" : "={v[156:159]}"(d) : "v"(voff), "s"(rs), "s"(soff), "n"(OFF) : "memory");
  else if constexpr (I == 8) asm volatile("buffer_load_dwordx4 %0, %1, %2, %3 offen offset:%4 sc1" : "={v[160:163]}"(d) : "v"(voff), "s"(rs), "s"(soff), "n"(OFF) : "memory");
  else if constexpr (I == 9) asm volatile("buffer_load_dwordx4 %0, %1, %2, %3 offen offset:%4 sc1" : "={v[164:167]}"(d) : "v"(voff), "s"(rs), "s"(soff), "n"(OFF) : "memory");
  else if constexpr (I == 10) asm volatile("buffer_load_dwordx4 %0, %1, %2, %3 offen offset:%4 sc1" : "={v[168:171]}"(d) : "v"(voff), "s"(rs), "s"(soff), "n"(OFF) : "memory");
  else if constexpr (I == 11) asm volatile("buffer_load_dwordx4 %0, %1, %2, %3 offen offset:%4 sc1" : "={v[172:175]}"(d) : "v"(voff), "s"(rs), "s"(soff), "n"(OFF) : "memory");
}
template <int K> __device__ __forceinline__ void pin_w(u32x4& a, u32x4& b) {
  if constexpr (K == 0) asm volatile("" : "+{v[128:131]}"(a), "+{v[132:135]}"(b)::"memory");
  else if constexpr (K == 1) asm volatile("" : "+{v[136:139]}"(a), "+{v[140:143]}"(b)::"memory");
  else if constexpr (K == 2) asm volatile("" : "+{v[144:147]}"(a), "+{v[148:151]}"(b)::"memory");
  else if constexpr (K == 3) asm volatile("" : "+{v[152:155]}"(a), "+{v[156:159]}"(b)::"memory");
  else if constexpr (K == 4) asm volatile("" : "+{v[160:163]}"(a), "+{v[164:167]}"(b)::"memory");
  else if constexpr (K == 5) asm volatile("" : "+{v[168:171]}"(a), "+{v[172:175]}"(b)::"memory");
}
__device__ __forceinline__ void ld8h(u32x2& d, rsrc_t rs, unsigned voff, unsigned soff) {
  asm volatile("buffer_load_dwordx2 %0, %1, %2, %3 offen sc1" : "=v"(d) : "v"(voff), "s"(rs), "s"(soff) : "memory");
}
template <int OFF> __device__ __forceinline__ void ld4h(unsigned& d, rsrc_t rs, unsigned voff, unsigned soff) {
  asm volatile("buffer_load_dword %0, %1, %2, %3 offen offset:%4 sc1" : "=v"(d) : "v"(voff), "s"(rs), "s"(soff), "n"(OFF) : "memory");
}
__device__ __forceinline__ u32x2 ld8(rsrc_t rs, unsigned voff, unsigned soff) { return __builtin_amdgcn_raw_buffer_load_b64(rs, voff, soff, SC1); }
__device__ __forceinline__ void st16(u32x4 v, rsrc_t rs, unsigned voff, unsigned soff) { __builtin_amdgcn_raw_buffer_store_b128(v, rs, voff + soff, 0, SC1); }
__device__ __forceinline__ void st8(u32x2 v, rsrc_t rs, unsigned voff, unsigned soff) { __builtin_amdgcn_raw_buffer_store_b64(v, rs, voff, soff, SC1); }

__device__ __forceinline__ unsigned ld4(rsrc_t rs, unsigned voff, unsigned soff) { return __builtin_amdgcn_raw_buffer_load_b32(rs, voff, soff, SC1); }
__device__ __forceinline__ void st4(unsigned v, rsrc_t rs, unsigned voff, unsigned soff) { __builtin_amdgcn_raw_buffer_store_b32(v, rs, voff, soff, SC1); }

constexpr unsigned kSpinCap = 1u << 16;
__device__ __forceinline__ bool spin_fail(unsigned& spins, rsrc_t rs, u32x2 T) {
  asm volatile("" ::: "memory");
  ++spins;
  if (spins > kSpinCap) { st8(T, rs, 0, F_TMO); return true; }
  if ((spins & 255u) == 0u) {
    const u32x2 v = ld8(rs, 0, F_TMO);
    if (v[0] == T[0] && v[1] == T[1]) return true;
  }
  __builtin_amdgcn_s_sleep(3);
  return false;
}
__device__ __forceinline__ void poll_flags2(rsrc_t rs, unsigned voff, u32x2 T) {
  for (unsigned spins = 0;;) {
    const u32x4 v = ld16(rs, voff, 0);
    if (__all(v[0] == T[0] && v[1] == T[1] && v[2] == T[0] && v[3] == T[1])) break;
    if (spin_fail(spins, rs, T)) break;
  }
  asm volatile("" ::: "memory");
  __builtin_amdgcn_fence(__ATOMIC_ACQUIRE, "wavefront");
}
__device__ __forceinline__ void tag_wait(rsrc_t rs, u32x2 T, unsigned voff, unsigned soff, u32x4& v) {
  if (__builtin_expect(!__all(v[2] == T[0] && v[3] == T[1]), 0)) {
    unsigned spins = 0;
    do {
      if (spin_fail(spins, rs, T)) break;
      v = ld16(rs, voff, soff);
    } while (!__all(v[2] == T[0] && v[3] == T[1]));
    asm volatile("s_waitcnt vmcnt(0)" : "+v"(v)::"memory");
  }
}
template <int N> __device__ __forceinline__ void word_wait(rsrc_t rs, u32x2 T, unsigned voff, unsigned soff, u32x4 (&v)[4]) {
#define WW_OK() __all(max(max(max(v[0][0], v[0][1]), max(v[0][2], v[0][3])), max(max(v[N - 1][0], v[N - 1][1]), max(v[N - 1][2], v[N - 1][3]))) != POISON)
  if (__builtin_expect(!WW_OK(), 0)) {
    unsigned spins = 0;
    do {
      if (spin_fail(spins, rs, T)) break;
#pragma unroll
      for (int i = 0; i < N; ++i) v[i] = ld16(rs, voff + i * 2048, soff);
    } while (!WW_OK());
    asm volatile("s_waitcnt vmcnt(0)" : "+v"(v[0]), "+v"(v[N - 1])::"memory");
  }
#undef WW_OK
}
__device__ __forceinline__ void poll_flags(rsrc_t rs, unsigned voff, unsigned soff, u32x2 T) {
  for (unsigned spins = 0;;) {
    const u32x2 v = ld8(rs, voff, soff);
    if (__all(v[0] == T[0] && v[1] == T[1])) break;
    if (spin_fail(spins, rs, T)) break;
  }
  asm volatile("" ::: "memory");
  __builtin_amdgcn_fence(__ATOMIC_ACQUIRE, "wavefront");
}

#define STAMP_ON(i) true
#define STAMP_VAL(i) (((i) == 23 || (i) == 22 || (i) == 21) ? __builtin_amdgcn_s_memtime() : __builtin_amdgcn_s_memrealtime())
#define STAMPAT(i)

#define GSTEP(s, n)
#define VSTEP(i)

__device__ __forceinline__ unsigned toff(unsigned R, unsigned ks, unsigned g) {
  return (((R >> 5) * 64 + ks * 2 + ((R >> 4) & 1)) << 10) + ((g * 16 + (R & 15)) << 4);
}
__device__ __forceinline__ void unpack8v(const u32x4 u, f32x2* v) {
#pragma unroll
  for (int j = 0; j < 4; ++j) v[j] = f32x2{lo16(u[j]), hi16(u[j])};
}
__device__ __forceinline__ u32x4 pack8v(const f32x2* v) {
  u32x4 o;
#pragma unroll
  for (int j = 0; j < 4; ++j) o[j] = pack2(v[j][0], v[j][1]);
  return o;
}
__device__ __forceinline__ f32x2 rbf1v(f32x2 v) { return f32x2{rbf1(v[0]), rbf1(v[1])}; }
__device__ __forceinline__ float ln_rd(float var) { return __builtin_amdgcn_rcpf(rbf1(__builtin_amdgcn_sqrtf(rbf1(rbf1(var) + 1e-5f)))); }
template <int N> __device__ __forceinline__ void ln_apply(float mb, float rd, const f32x2* v, f32x2* y) {
  const f32x2 mb2 = {mb, mb}, rd2 = {rd, rd};
#pragma unroll
  for (int j = 0; j < N; ++j) y[j] = rbf1v(rbf1v(v[j] - mb2) * rd2);
}
__device__ __forceinline__ float hadd(f32x2 v) {
  float a = v[0];
  asm volatile("" : "+v"(a));
  return a + v[1];
}
__device__ __forceinline__ float add_g(float v) { return add_x32(add_x16(v)); }
__device__ __forceinline__ void add_g3(float& x, float& y, float& z) {
  auto a = __builtin_amdgcn_permlane16_swap(__builtin_bit_cast(unsigned, x), __builtin_bit_cast(unsigned, y), false, false);
  auto b = __builtin_amdgcn_permlane16_swap(__builtin_bit_cast(unsigned, z), __builtin_bit_cast(unsigned, z), false, false);
  const float r = asf(a[0]) + asf(a[1]), t = asf(b[0]) + asf(b[1]);
  auto c = __builtin_amdgcn_permlane32_swap(__builtin_bit_cast(unsigned, r), __builtin_bit_cast(unsigned, r), false, false);
  auto d = __builtin_amdgcn_permlane32_swap(__builtin_bit_cast(unsigned, t), __builtin_bit_cast(unsigned, t), false, false);
  const float u = asf(c[0]) + asf(c[1]);
  z = asf(d[0]) + asf(d[1]);
  auto e = __builtin_amdgcn_permlane16_swap(__builtin_bit_cast(unsigned, u), __builtin_bit_cast(unsigned, u), false, false);
  x = asf(e[0]); y = asf(e[1]);
}
__device__ __forceinline__ u32x4 trade16(u32x4 v) {
  auto a = __builtin_amdgcn_permlane16_swap(v[0], v[2], false, false);
  auto b = __builtin_amdgcn_permlane16_swap(v[1], v[3], false, false);
  return u32x4{(unsigned)a[0], (unsigned)b[0], (unsigned)a[1], (unsigned)b[1]};
}
__device__ __forceinline__ unsigned umax4(u32x4 v) { return max(max(v[0], v[1]), max(v[2], v[3])); }
__device__ __forceinline__ u32x2 unpoison2(u32x2 v) { return u32x2{min(v[0], POISON - 1), min(v[1], POISON - 1)}; }
__device__ __forceinline__ u32x4 unpoison4(u32x4 v) {
  return u32x4{min(v[0], POISON - 1), min(v[1], POISON - 1), min(v[2], POISON - 1), min(v[3], POISON - 1)};
}

typedef union { struct { s16x4 a, b; } hf; u32x4 v; } frag_t;
__device__ __forceinline__ s16x4 trread(const char* smem, unsigned off) {
  return __builtin_amdgcn_ds_read_tr16_b64_v4i16((LDS_AS s16x4*)(smem + off));
}
__device__ __forceinline__ s16x4 trabs(unsigned addr) { return __builtin_amdgcn_ds_read_tr16_b64_v4i16((LDS_AS s16x4*)(size_t)addr); }

constexpr int WA = 6;
constexpr int WB = 8, WR = 4;
#define CH 0, 7, 9, 10, 15, 17, 23, 25, 29, 31, 5, 4
constexpr int CHV[12] = {CH};
constexpr int FKL = CHV[10], FSN = CHV[11];
constexpr int SPL = CHV[0];
constexpr int FSL = CHV[1], FS0 = CHV[2], FS0D = CHV[3], FS1 = CHV[4], FS1S = CHV[5];
constexpr int FXA = CHV[6], FXB = CHV[7], FXL = CHV[8], FXS = CHV[9];
#define ROTM 7
#define ROT0 (p & ROTM)
#define PO() (p & 7 & ~ROTM)
#define L0S 22, 25, 8, 0, 24, 28, 1, 2, 4, 14
constexpr int L0V[10] = {L0S};
constexpr int CSL = L0V[0], CSS = L0V[1], CSD = L0V[2];
constexpr int abl(int it) { return it ? 25 : L0V[4]; }
constexpr int abs_(int it) { return it ? 27 : L0V[5]; }
constexpr int W2S = L0V[6], WSH = L0V[7];
static_assert(ROTM == 7 || (CSD & 3) == 3, "two tiles must be there");
constexpr int LNL = L0V[3], LNS = L0V[8], POI = L0V[9];
#define SYNCB 3
#define SPINS 0
#define KVJ 1
#define KVD 2
#define PC2 1
#define PCE 1
#define TCH 40, 6, 0, 26, 0xFF, 0, 1
constexpr int TCV[7] = {TCH};
constexpr int TS0 = TCV[0], TSN = TCV[1], TM0 = TCV[2], TEX = TCV[3], TM1 = TCV[4], TSD = TCV[5], TSG = TCV[6];
constexpr int QL = 26;
#define SLP 18, 4, 28, 3, 0, 0
constexpr int SLV[6] = {SLP};
constexpr int OUTE = SLV[4], OSL = SLV[5];
constexpr int SDIR = SLV[0], GSL = SLV[1], RSL = SLV[2], KSL = SLV[3];
constexpr int tp0(int it) { return it ? 3 : 3 + WSH; }
constexpr bool tile_piece(int it, int s) { return s >= tp0(it) && ((s - tp0(it)) >> 2) + 3 < 8 && ((s - tp0(it)) & 3) < 3; }
constexpr int a_req(int s, int n) {
  if (s < 1 || !(n & 1) || (s == 1 && n == 1)) return -1;
  if (s <= 5) return 8 + 3 * s + (n >> 1) - 4;
  return n < 5 && s + 5 < 32 ? 2 * (s + 5) + (n >> 1) : -1;
}
constexpr int vm_n(int it, int s, int n) {
  int c = it ? (a_req(s, n) >= 0) + (s == 0 && n == 0) : (s + 5 < 32 && (n == 2 || n == 3));
  if (n == 0) c += tile_piece(it, s);
  if (n == 4) c += (s == abl(it));
  if (!it) c += (s >= W2S && s < W2S + 3 && n == 4) + (s == LNL && n == 5 ? 3 : 0) + (s == LNS + 2 && n == 5) + (s == TEX && n == 5);
  else c += (s == 2 && n == 2) + (s == (SPL < 0 ? 0 : SPL) && n == (SPL < 0 ? 1 : 4) ? 4 : 0) + (s >= 27 && s < 30 && (n == 0 || n == 3));
  return c;
}
constexpr int vm_at(int it, int s, int n) {
  int c = it ? 8 : 16;
  for (int u = 0; u < 6 * s + n; ++u) c += vm_n(it, u / 6, u % 6);
  return c;
}
constexpr int vm_a(int it, int k) {
  if (k < 4) return 2 * k + 2;
  for (int u = 0; u < 192; ++u)
    if (a_req(u / 6, u % 6) == 2 * k + 1) return vm_at(it, (u + 1) / 6, (u + 1) % 6);
  return 0;
}
constexpr int vm_tile(int it, int t) { return t >= 3 ? vm_at(it, 4 * t - 10 + tp0(it), 1) : t == 1 ? 12 : vm_at(0, W2S + 2, 5); }
constexpr int vm_unit(int u) { return u < 3 ? 5 + 2 * u : u < 5 ? 8 + 2 * u : vm_at(0, u - 5, 4); }
constexpr int unit_slot(int u) { return u % 3 == 0 ? 0 : 16384 + (u % 3) * 16384; }

constexpr int L_XC = LDS_BYTES - 64, L_SKEY = 32704, L_ROW = 1024, L_OUT = 65536, L_SRED = 4096, L_SFIN = 20480;
__device__ __forceinline__ unsigned key_load(rsrc_t rs, int l, int L, int rep) { return ld4(rs, (l & 15) * SKEY, KEY_AT(L, rep)); }
__device__ __forceinline__ unsigned umax16(unsigned k) {
  k = max(k, dppu<0xB1>(k));
  k = max(k, dppu<0x4E>(k));
  k = max(k, dppu<0x141>(k));
  return max(k, dppu<0x140>(k));
}
__device__ __forceinline__ unsigned key_row(unsigned key) { return (0xffffu - (key & 0xffffu)) & 2047u; }
__device__ __forceinline__ unsigned key_wait(rsrc_t rs, u32x2 T, int l, int L, int rep, unsigned kv) {
  if (!__all(kv != POISON)) {
    unsigned spins = 0;
    do {
      if (spin_fail(spins, rs, T)) break;
      if (L == 0) __builtin_amdgcn_s_sleep(KSL);
      kv = key_load(rs, l, L, rep);
    } while (!__all(kv != POISON));
  }
  return __builtin_amdgcn_readfirstlane(umax16(kv));
}
__device__ __forceinline__ unsigned cand_off(unsigned key, int L) { return OFF_CA + L * 65536 + (key_row(key) >> 7) * 4096; }
#define XO(i) (((i) >> 1) * 256 + ((i) & 1) * 64)
template <int S1, int S2> __device__ __forceinline__ void ffn_xwait(rsrc_t rs, u32x2 T, unsigned xo, int l, u32x4 (&xv)[4]) {
#define XV_OK() __all(max(max(umax4(xv[0]), umax4(xv[1])), max(umax4(xv[2]), umax4(xv[3]))) != POISON)
  if (!XV_OK()) {
    unsigned spins = 0;
    do {
      if (spin_fail(spins, rs, T)) break;
#pragma unroll
      for (int i = 0; i < 4; ++i) xv[i] = ld16(rs, (l >> 4) * 16 + (i & 1) * S1 + (i >> 1) * S2, xo);
    } while (!XV_OK());
  }
#undef XV_OK
}
__device__ __forceinline__ void ffn_gu(const u32x4 (&gv)[4], const u32x4 (&uv)[4], const u32x4 (&xv)[4], f32x4& ga, f32x4& ua) {
  ga = mfma16(gv[0], xv[0], f32x4{0.f, 0.f, 0.f, 0.f}); ua = mfma16(uv[0], xv[0], f32x4{0.f, 0.f, 0.f, 0.f});
#pragma unroll
  for (int i = 1; i < 4; ++i) { ga = mfma16(gv[i], xv[i], ga); ua = mfma16(uv[i], xv[i], ua); }
}
__device__ __forceinline__ void ffn_gu_check(rsrc_t rs, u32x2 T, unsigned xo, int l, const u32x4 (&gv)[4], const u32x4 (&uv)[4], u32x4 (&xv)[4], f32x4& ga, f32x4& ua) {
  if (__builtin_expect(__any(ga[0] != ga[0]), 0)) {
    ffn_xwait<64, 256>(rs, T, xo, l, xv);
    ffn_gu(gv, uv, xv, ga, ua);
  }
}
__device__ __forceinline__ void ffn_gu_store(char* smem, int l, int w, f32x4 ga, f32x4 ua) {
  LDS_AS f32x4* sred = (LDS_AS f32x4*)(smem + L_SRED);
  if ((l & 15) == 0) { sred[w * 8 + (l >> 4)] = ga; sred[w * 8 + 4 + (l >> 4)] = ua; }
}
__device__ __forceinline__ f32x4 ffn_d_read(char* smem, int l) {
  const LDS_AS float* t = (const LDS_AS float*)(smem + L_SRED) + (l & 8) * 16 + (l & 4) * 4 + (l >> 4) * 4 + (l & 3);
  return f32x4{t[0], t[32], t[64], t[96]};
}
__device__ __forceinline__ u32x2 ffn_d_h(f32x4 t) {
  float x = t[0] + t[1];
  asm volatile("" : "+v"(x));
  x += t[2] + t[3];
  x += dppf<0x128>(x);
  const float y = dppf<0x124>(x);
  const bool up = __builtin_amdgcn_inverse_ballot_w64(0xF0F0F0F0F0F0F0F0ull);
  const float gg = rbf1(up ? y : x), uu = rbf1(up ? x : y);
  const float h = rbf1(gg * __builtin_amdgcn_rcpf(1.0f + __expf(-gg))) * uu;
  const unsigned p = pack2(h, dppf<0xB1>(h));
  return u32x2{dppu<0x00>(p), dppu<0xAA>(p)};
}
template <int t> __device__ __forceinline__ f32x4 ffn_d_mma(s16x4 d, u32x2 hh, f32x4 y) {
  const bool me = __builtin_amdgcn_inverse_ballot_w64(0x0001000100010001ull << t);
  return mfma16k16(d, __builtin_bit_cast(s16x4, u32x2{me ? hh[0] : 0u, me ? hh[1] : 0u}), y);
}
__device__ __forceinline__ f32x4 ffn_d_all(const s16x4 (&dfr)[8], u32x2 hh) {
  f32x4 a = {0.f, 0.f, 0.f, 0.f}, b = a;
  a = ffn_d_mma<0>(dfr[0], hh, a); b = ffn_d_mma<1>(dfr[1], hh, b); a = ffn_d_mma<2>(dfr[2], hh, a); b = ffn_d_mma<3>(dfr[3], hh, b);
  a = ffn_d_mma<4>(dfr[4], hh, a); b = ffn_d_mma<5>(dfr[5], hh, b); a = ffn_d_mma<6>(dfr[6], hh, a); b = ffn_d_mma<7>(dfr[7], hh, b);
  return a + b;
}
__device__ __forceinline__ void ffn_d_store(rsrc_t rs, int l, int w, int L, int slice, f32x4 y) {
  const unsigned c = l & 15;
  if (c < 8) st16(unpoison4(__builtin_bit_cast(u32x4, y)), rs, (2 * w + (c >> 2)) * 65536 + slice * 256 + (c & 3) * 64 + (l >> 4) * 16, OFF_PART + L * (1u << 20));
}
#define RED_VO(k) ((min(tid + 512 * (k), 1791) >> 3) * 256 + (tid & 7) * 16)
#define RED_SO(L, G) (OFF_PART + (L) * (1u << 20) + ((G) >> 1) * 65536 + ((G) & 1) * 128)
#define RED_JJ() ((tid & 3) * 16 + (tid & 4) * 2)
__device__ __forceinline__ void red_load(rsrc_t rs, int tid, int L, unsigned G, unsigned key, u32x4 (&pv)[4], u32x2& cxn, u32x2& cx1) {
#pragma unroll
  for (int k = 0; k < 4; ++k) pv[k] = ld16(rs, RED_VO(k), RED_SO(L, G));
  cxn = ld8(rs, RED_JJ(), cand_off(key, L) + XO(G));
  cx1 = ld8(rs, RED_JJ() + 128, cand_off(key, L) + XO(G));
}
__device__ __forceinline__ void red_sum(rsrc_t rs, char* smem, u32x2 T, int tid, int w, int L, unsigned G, unsigned key,
                                        u32x4 (&pv)[4], u32x2& cxn, u32x2& cx1) {
#define PV_OK() __all(max(max(max(umax4(pv[0]), umax4(pv[1])), max(umax4(pv[2]), umax4(pv[3]))), max(max(cxn[0], cxn[1]), max(cx1[0], cx1[1]))) != POISON)
  if (!PV_OK()) {
    unsigned spins = 0;
    do {
      if (spin_fail(spins, rs, T)) break;
#pragma unroll
      for (int k = 0; k < 4; ++k)
        if (umax4(pv[k]) == POISON) pv[k] = ld16(rs, RED_VO(k), RED_SO(L, G));
      if (max(max(cxn[0], cxn[1]), max(cx1[0], cx1[1])) == POISON) { cxn = ld8(rs, RED_JJ(), cand_off(key, L) + XO(G)); cx1 = ld8(rs, RED_JJ() + 128, cand_off(key, L) + XO(G)); }
    } while (!PV_OK());
  }
#undef PV_OK
  f32x4 s = (__builtin_bit_cast(f32x4, pv[0]) + __builtin_bit_cast(f32x4, pv[1])) + __builtin_bit_cast(f32x4, pv[2]);
  if (w < 4) s += __builtin_bit_cast(f32x4, pv[3]);
  ((LDS_AS f32x4*)(smem + L_SFIN))[tid] = s;
}
__device__ __forceinline__ u32x2 red_final(char* smem, int l, unsigned key, u32x2 cxn, u32x2 cx1) {
  const LDS_AS f32x4* sf = (const LDS_AS f32x4*)(smem + L_SFIN) + l;
  f32x4 s = ((sf[0] + sf[64]) + (sf[128] + sf[192])) + ((sf[256] + sf[320]) + (sf[384] + sf[448]));
#pragma unroll
  for (int j = 0; j < 4; ++j) s[j] = sum_hi8(s[j]);
  const unsigned k16 = key >> 16;
  const float rmax = __builtin_bit_cast(float, ((k16 & 0x8000u) ? (k16 & 0x7fffu) : (~k16 & 0xffffu)) << 16);
  const float sig = rbf1(__builtin_amdgcn_rcpf(1.0f + __expf(-rmax)));
  const float xn4[4] = {lo16(cxn[0]), hi16(cxn[0]), lo16(cxn[1]), hi16(cxn[1])};
  const float x14[4] = {lo16(cx1[0]), hi16(cx1[0]), lo16(cx1[1]), hi16(cx1[1])};
  float o[4];
#pragma unroll
  for (int j = 0; j < 4; ++j) o[j] = x14[j] + rbf1(xn4[j] + rbf1(rbf1(s[j]) * sig));
  return u32x2{pack2(o[0], o[1]), pack2(o[2], o[3])};
}

struct RowFin { float mean, var, mb, rd; unsigned key; };
__device__ __forceinline__ void fin_wait(rsrc_t rs, u32x2 T, unsigned voff, unsigned so, u32x4 (&sp)[4]) {
#define ST_OK() __all(max(max(umax4(sp[0]), umax4(sp[1])), max(umax4(sp[2]), umax4(sp[3]))) != POISON)
  if (!ST_OK()) {
    unsigned spins = 0;
    do {
      if (spin_fail(spins, rs, T)) break;
#pragma unroll
      for (int i = 0; i < 4; ++i) sp[i] = ld16(rs, voff + i * 2048, so);
    } while (!ST_OK());
  }
#undef ST_OK
}
__device__ __forceinline__ RowFin fin_key(rsrc_t rs, u32x2 T, unsigned voff, unsigned so, char* skey, int l, int w, unsigned R, float wsum, u32x4 (&sp)[4]) {
  RowFin r;
#define ST_SUM() ((__builtin_bit_cast(f32x4, sp[0]) + __builtin_bit_cast(f32x4, sp[1])) + (__builtin_bit_cast(f32x4, sp[2]) + __builtin_bit_cast(f32x4, sp[3])))
  f32x4 sf = ST_SUM();
  const float z = (sf[0] + sf[1]) + sf[2];
  if (__any(z != z)) {
    fin_wait(rs, T, voff, so, sp);
    sf = ST_SUM();
  }
#undef ST_SUM
  float s0 = sf[0], s1 = sf[1], s2 = sf[2];
  add_g3(s0, s1, s2);
  r.mean = s0 * (1.0f / 1024.0f);
  r.var = fmaxf(__builtin_fmaf(-r.mean, r.mean, s1 * (1.0f / 1024.0f)), 0.f);
  r.mb = rbf1(r.mean); r.rd = ln_rd(r.var);
  const unsigned rb16 = pack2((s2 - r.mb * wsum) * r.rd, 0.f) & 0xffffu;
  r.key = min((((rb16 & 0x8000u) ? (~rb16 & 0xffffu) : (rb16 | 0x8000u)) << 16) | (0xffffu - R), POISON - 1);
  const unsigned wk = umax16(r.key);
  if (l == 0) ((LDS_AS unsigned*)skey)[w] = wk;
  return r;
}
__device__ __forceinline__ unsigned fin_cand(rsrc_t rs, char* skey, int L, int g, int rb, int ct, const RowFin& r, const f32x2 (&v)[8]) {
  const u32x4 k0 = *(const LDS_AS u32x4*)skey, k1 = *(const LDS_AS u32x4*)(skey + 16);
  const unsigned best = max(umax4(k0), umax4(k1));
  if (r.key == best) {
    const unsigned co = L * 65536 + rb * 4096 + ct * 256 + g * 16;
    f32x2 t[8], rd2 = {r.rd, r.rd}, of2 = {-r.mb * r.rd, -r.mb * r.rd};
    asm volatile("" : "+v"(rd2), "+v"(of2));
#pragma unroll
    for (int j = 0; j < 8; ++j) t[j] = __builtin_elementwise_fma(v[j], rd2, of2);
    st16(unpoison4(pack8v(t)), rs, co, OFF_CA); st16(unpoison4(pack8v(t + 4)), rs, co + 64, OFF_CA);
    if (g == 0 && ct < (int)RKEY) st4(best, rs, KEY_AT(L, ct) + rb * SKEY, 0);
    st16(unpoison4(pack8v(v)), rs, co + 128, OFF_CA); st16(unpoison4(pack8v(v + 4)), rs, co + 192, OFF_CA);
  }
  return key_row(best);
}
__device__ __forceinline__ void fin_x2(const RowFin& r, f32x2 (&v)[8]) {
  f32x2 sc2 = {1.0f + r.rd, 1.0f + r.rd}, of2 = {-r.mb * r.rd, -r.mb * r.rd};
  asm volatile("" : "+v"(sc2), "+v"(of2));
#pragma unroll
  for (int j = 0; j < 8; ++j) v[j] = __builtin_elementwise_fma(v[j], sc2, of2);
}

extern "C" __device__ unsigned long long dispatch_id() __asm("llvm.amdgcn.dispatch.id");

extern "C" __global__ __launch_bounds__(512, 2) void mega_kernel(const bf16* x, char* ws, const bf16* wqkv, const bf16* wo, const bf16* wr,
                                                                 const bf16* wg, const bf16* wu, const bf16* wd, bf16* out) {
  extern __shared__ __attribute__((aligned(16))) char smem[];
  const int w0_ = __builtin_amdgcn_readfirstlane(threadIdx.x >> 6);
  const unsigned lbase = (unsigned)(size_t)(LDS_AS char*)smem;
  {
    unsigned long long pc;
    unsigned len;
    asm volatile("s_getpc_b64 %0\n\ts_mov_b32 %1, .Ltext_end-mega_kernel\n\t.subsection 1\n.Ltext_end:\n\t.subsection 0" : "=s"(pc), "=s"(len));
    const unsigned o = ((blockIdx.x >> 3) * 64 + (threadIdx.x & 63)) * 128;
    if (w0_ == 7 && o < len)
      asm volatile("s_mov_b32 m0, %0\n\ts_nop 0\n\tglobal_load_lds_dword %1, %2" ::"s"(lbase + L_FXL), "v"(o), "s"(pc & ~127ull) : "memory");
  }
  asm volatile("" : "+s"(x), "+s"(ws), "+s"(wqkv));
#define IDS()                                                                                         \
  int tid = threadIdx.x, w = w0_, bid = blockIdx.x;                                                   \
  asm volatile("" : "+v"(tid));                                                                       \
  asm volatile("" : "+s"(w));                                                                         \
  asm volatile("" : "+s"(bid));                                                                       \
  const int l = tid & 63, xcd = bid & 7, bi = bid >> 3;                                               \
  const int p = (xcd & 1) * 4 + (bi & 3), h = (xcd >> 1) * 8 + (bi >> 2);         \
  const int rb = 2 * xcd + (bi & 1), ct = bi >> 1;                               \
  const int slice = xcd * 28 + bi;                                                   \
  const bool ffn_on = bi < 28;                                                                        \
  const int g = l >> 4;                                                                               \
  const int rt = w < 4 ? w : 11 - w;          \
  KARGS();                                                                                            \
  (void)p; (void)h; (void)rb; (void)ct; (void)slice; (void)ffn_on; (void)g; (void)rt;
  const rsrc_t rs = __builtin_amdgcn_make_buffer_rsrc((void*)ws, 0, WS_BYTES, 0x00020000);
  typedef const void* const __attribute__((address_space(4)))* karg_t;
#define KARGS() karg_t ka = (karg_t)__builtin_amdgcn_kernarg_segment_ptr(); asm volatile("" : "+s"(ka))
#define ARGP(name, i) const GAS bf16* name = (const GAS bf16*)ka[i]; asm volatile("" : "+s"(name))
#define RSRC(ptr, bytes) __builtin_amdgcn_make_buffer_rsrc((void*)(ptr), 0, bytes, 0x00020000)
#define rwq RSRC(pwq, 1024 * 3072 * 2)
#define rwo RSRC(pwo, 1024 * 1024 * 2)
#define rwg RSRC(pwg, 1024 * F_ * 2)
#define rwu RSRC(pwu, 1024 * F_ * 2)
#define rwd RSRC(pwd, 1024 * F_ * 2)
#define rout RSRC(pout, 2048 * 1024 * 2)

#define WQ_VOFF() ((l >> 2) * 6144 + (((l & 3) ^ ((((l >> 4) ^ (l >> 5)) & 1) << 1)) << 4))
#define WQ_PIECE(t, j, dst) dma16(rwq, wqv, ((((t) + rot) & 7) * 128 + ((j) & 7) * 16) * 6144 + ((j) >> 3) * 2048 + h * 64, lbase + (dst) + (j) * 1024)
#define WQ_BOFF(dt) ((8 * g + ((l & 15) >> 2)) * 64 + (l & 3) * 8 + (((dt) ^ (g & 1)) << 5))
#define WQ_POFF() ((4 * g + ((l & 15) >> 2)) * 64 + (l & 3) * 8 + (((g ^ (g >> 1)) & 1) << 5))
#define X_UNIT1(u, j) dma16(rx, xv_, (unsigned)(p * 256 + rt * 32 + ((u) & 1) * 16 + (j) * 8) * 2048 + ((((u) >> 2) + rot) & 7) * 256 + (((u) >> 1) & 1) * 128, lbase + rt * 2048 + unit_slot(u) + (j) * 1024)
#define X_VOFF() ((l >> 3) * 2048 + (((l & 7) ^ ((l >> 3) & 6)) << 4))
#define X_ROFF(hs) (lbase + rt * 2048 + (l & 15) * 128 + (((4 * (hs) + g) ^ (l & 6)) << 4))
#define TOUCH_TO(ptr, dst) asm volatile("s_mov_b32 m0, %0\n\ts_nop 0\n\tglobal_load_lds_dword %1, off" ::"s"(lbase + (dst)), "v"(ptr) : "memory")
#define TOUCH(ptr) TOUCH_TO(ptr, L_FXL)
#define WO_PIECE_W(j, w_) dma16(rwo, wov, (((w_) >> 2) * 64 + ((w_) & 3) + 4 * (j)) * 16384 + ct * 128, lbase + 32768 + (j) * 8192 + (w_) * 1024)
#define WO_PIECE(j) WO_PIECE_W(j, w)
#define WO_VOFF() ((l >> 3) * 2048 + (((((l & 7) >> 1) ^ ((l >> 4) & 3)) * 2 + (l & 1)) << 4))
#define WD_DMA() _Pragma("unroll") for (int i = 0; i < 4; ++i) WD_DMA1(i)
#define WD_DMA1(i)                                                                                   \
      dma16(rwd, (l >> 4) * 2048 + (l & 15) * 16, (unsigned)(slice * 16 + 4 * (i)) * 2048 + w * 256, lbase + L_DN + w * 4096 + (i) * 1024)
#define FFN_DMA(m) dma16((m) & 1 ? rwu : rwg, fnv, (unsigned)(((4 * w + ((m) >> 1)) * 32) * F_ + slice * 16) * 2, lbase + 65536 + (m) * 8192 + w * 1024)
  u32x4 gv[4], uv[4];
  s16x4 dfr[8];
  u32x4 keep[9];
  u32x4 pre[4][2];
#define PRE_LOAD(it_, rot_, s0, s1)                                                                  \
  static_for<(s1) - (s0)>([&](auto c_) __attribute__((always_inline)) {                              \
    constexpr int s = (s0) + decltype(c_)::value;                                                    \
    const unsigned ab_ = OFF_XN + (it_) * (4u << 20) + (((rot_) + (s >> 2)) & 7) * 8192 + ((s >> 1) & 1) * 4096; \
    ld16w<2 * s, (s & 1) * 2048>(pre[s][0], rs, l * 16, ab_ + (p * 8 + rt) * 65536);                         \
    ld16w<2 * s + 1, (s & 1) * 2048 + 1024>(pre[s][1], rs, l * 16, ab_ + (p * 8 + rt) * 65536);                 \
  })
  u32x2 T;
  const GAS bf16* tsrc;
  {
    IDS();
    const int it = 0, rot = ROT0;
    (void)it;
    STAMPAT(0);
    const bf16* pwq = wqkv;
    const rsrc_t rx = RSRC(x, 2048 * 1024 * 2);
    const unsigned wqv = WQ_VOFF(), xv_ = X_VOFF();
    if (tid == 0) *(LDS_AS u32x4*)(smem + L_QC) = u32x4{0u, 0u, 0u, 0u};
#pragma unroll
    for (int i = 0; i < 3; ++i) WQ_PIECE(0, 3 * w + i, L_RING);
    STAMPAT(41);
    X_UNIT1(0, 0); X_UNIT1(0, 1); X_UNIT1(1, 0); X_UNIT1(1, 1);
    STAMPAT(42);
    BAR();
    STAMPAT(43);
    X_UNIT1(2, 0); X_UNIT1(2, 1);
#pragma unroll
    for (int i = 0; i < 3; ++i) WQ_PIECE(1, 3 * w + i, L_RING + STAGE);
    STAMPAT(44);
    {
      ARGP(pwo, 3); ARGP(pwg, 5); ARGP(pwu, 6);
      const int t = tid < 288 ? tid - 64 : tid - 288, go = (bi * 32 + t / 7) * F_ + xcd * 448 + (t % 7) * 64;
      tsrc = tid < 64 ? pwo + bid * 4096 + tid * 64 : tid < 288 ? pwg + go : pwu + go;
      asm volatile("" : "+v"(tsrc));
    }
    STAMPAT(31);
  }
  unsigned sel0 = 0x7FFFFFFFu;
  f32x2 v[8];
  u32x4 x2p[2];
  float wsum;

  auto layer = [&](auto it_c) __attribute__((always_inline)) {
    constexpr int it = decltype(it_c)::value;
    STAMPAT(1); STAMPAT(22);
    f32x4 acc[2][6];
    f32x2 qs2[2];
    u32x4 k0, k1, v0, v1;
    {
      IDS();
      bool aff = false;
      unsigned aq[2];
      u32x4 av[2];
#define PIN_ACC() asm volatile("" : "+{v[176:179]}"(acc[0][0]), "+{v[180:183]}"(acc[0][1]), "+{v[184:187]}"(acc[0][2]), "+{v[188:191]}"(acc[0][3]), "+{v[192:195]}"(acc[0][4]), "+{v[196:199]}"(acc[0][5]), \
                               "+{v[200:203]}"(acc[1][0]), "+{v[204:207]}"(acc[1][1]), "+{v[208:211]}"(acc[1][2]), "+{v[212:215]}"(acc[1][3]), "+{v[216:219]}"(acc[1][4]), "+{v[220:223]}"(acc[1][5]))
#define PINV4(x) asm volatile("" : "+v"(x[0]), "+v"(x[1]), "+v"(x[2]), "+v"(x[3])::"memory")
      auto qkv_loop = [&](auto it_c) __attribute__((always_inline)) {
        constexpr int IT = decltype(it_c)::value;
        const unsigned abase = OFF_XN + IT * (4u << 20) + (p * 8 + rt) * 65536;
        const int rot = ROT0 + 5 * IT;
        unsigned fkey = 0, kv, bq[2];
        u32x4 fx[4], bv[2], af[WA][2];
        u32x2 cxn, cx1, hh;
        f32x4 sa, sb, cs4[2];
        f32x2 b2[2];
#define AB_OFF() (OFF_AB + IT * 32768 + (p * 8 + rt) * 512)
#define A_LOAD1(k, a) ld16w<(k) % WA * 2 + (a), ((k) & 1) * 2048 + (a) * 1024>(af[(k) % WA][a], rs, l * 16, abase + ((((k) >> 2) + rot) & 7) * 8192 + (((k) >> 1) & 1) * 4096)
        if constexpr (IT == 1) {
#pragma unroll
          for (int s = 0; s < 4; ++s) { af[s][0] = pre[s][0]; af[s][1] = pre[s][1]; }
        }
        const bf16* pwq = wqkv;
        ARGP(pwo, 3);
        const rsrc_t rx = RSRC(x, 2048 * 1024 * 2);
        unsigned xv_ = X_VOFF(), xr[2] = {X_ROFF(0), X_ROFF(1)};
        if constexpr (IT == 0) asm volatile("" : "+v"(xv_), "+v"(xr[0]), "+v"(xr[1]));
#define X_READ(u) { af[((u) & ~1) % WA][(u) & 1] = *(const LDS_AS u32x4*)(size_t)(xr[0] + unit_slot(u)); af[((u) | 1) % WA][(u) & 1] = *(const LDS_AS u32x4*)(size_t)(xr[1] + unit_slot(u)); }
        const int tstep = TS0 + bid % TSN + TSD * (w >> TSG);
        const unsigned wqv = WQ_VOFF();
#define QKV_DMA1(t, i)                                                                            \
  {                                                                                               \
    if constexpr (IT == 0) WQ_PIECE(t, 3 * w + (i), L_RING + SLOT(t) * STAGE);                    \
    else dma16s<(i) * 1024>(rs, l * 16, OFF_WI + (h * 8 + (((t) + rot) & 7)) * STAGE + w * 3072, lbase + L_RING + SLOT(t) * STAGE + w * 3072); \
  }
        unsigned bo[2][2];
#pragma unroll
        for (int dt = 0; dt < 2 - IT; ++dt) {
          bo[dt][0] = lbase + L_RING + (IT ? l * 16 : WQ_BOFF(dt)); bo[dt][1] = bo[dt][0] + 2 * STAGE;
          asm volatile("" : "+v"(bo[dt][0]), "+v"(bo[dt][1]));
        }
        auto frag0 = [&](int sl, int f) __attribute__((always_inline)) {
          unsigned pb = lbase + L_RING + WQ_POFF();
          asm volatile("" : "+v"(pb));
          const unsigned o = (pb ^ ((f & 1) << 5)) + sl * STAGE + ((f % 6) >> 1) * 8192 + (f / 6) * 2048;
          frag_t t;
          t.hf.a = trabs(o); t.hf.b = trabs(o + 1024);
          return t.v;
        };
        u32x4 df[4];
        auto dot_mma = [&](const u32x4 (&xf)[4]) __attribute__((always_inline)) {
          sa = mfma16(df[0], xf[0], f32x4{0.f, 0.f, 0.f, 0.f}); sb = mfma16(df[1], xf[1], f32x4{0.f, 0.f, 0.f, 0.f});
          sa = mfma16(df[2], xf[2], sa); sb = mfma16(df[3], xf[3], sb);
        };
        auto dot_store = [&](unsigned dst) __attribute__((always_inline)) {
          if ((l & 15) == 0) st16(unpoison4(__builtin_bit_cast(u32x4, sa + sb)), rs, g * 16, dst + (h * 8 + p) * 512 + w * 64);
        };
        auto tile_sum = [&](u32x4 pt, int dst) __attribute__((always_inline)) {
          f32x4 c = __builtin_bit_cast(f32x4, pt);
#pragma unroll
          for (int e = 0; e < 4; ++e) { c[e] += dppf<0x124>(c[e]); c[e] += dppf<0x128>(c[e]); c[e] = add_x16(c[e]); }
          if ((l & 28) == 0) *(LDS_AS f32x4*)(smem + dst + (w * 2 + (l >> 5)) * 64 + (l & 3) * 16) = c;
        };
        frag_t bfr[6];
#define QKV_BO(s, n, x) (bo[((n) ^ (x)) & 1][SLOT((s) >> 2) >> 1] + (SLOT((s) >> 2) & 1) * STAGE + ((n) >> 1) * 8192 + ((s) & 3) * 2048 + (x) * 256)
#define QKV_RDA(s, n)                                                                             \
  {                                                                                               \
    if constexpr (IT == 0) bfr[n].hf.a = trabs(QKV_BO(s, n, 0));                                  \
    else bfr[n].v = *(const LDS_AS u32x4*)(size_t)(bo[0][SLOT((s) >> 2) >> 1] + (SLOT((s) >> 2) & 1) * STAGE + (((s) & 3) * 6 + (n)) * 1024); \
  }
#define QKV_RDB(s, n) { if constexpr (IT == 0) bfr[n].hf.b = trabs(QKV_BO(s, n, 1)); }
#define XP_OFF() (OFF_XNEW + ((rot + 5 + PO()) & 7) * 1024)
#define FP_VO() (w == 3 ? (l >> 2) * 256 + (l & 3) * 16 : ((l >> 2) & 7) * 512 + (l >> 5) * 64 + (l & 3) * 16)
#define FP_OFF(i) (w == 3 ? OFF_XNEW + (i) * 4096 : OFF_FX + h * 4096 + w * 128)
#define FX_OK(n) __all(((n) == 4 ? max(max(umax4(fx[0]), umax4(fx[1])), max(umax4(fx[2]), umax4(fx[3]))) : max(umax4(fx[0]), umax4(fx[1]))) != POISON)
#define SINCE(s0) (vm_at(IT, s, n) - vm_at(IT, s0, 5))
#define DEF4(x) asm volatile("" : "=v"(x[0]), "=v"(x[1]), "=v"(x[2]), "=v"(x[3]))
        LDS_AS unsigned* qcnt = (LDS_AS unsigned*)(smem + L_QC);
        unsigned qc;
        unsigned qadr = lbase + L_QC, qone = 1;
        asm volatile("" : "+v"(qadr), "+v"(qone));
#define count_in(i) asm volatile("s_mov_b64 exec, 1\n\tds_add_u32 %0, %1 offset:%2\n\ts_mov_b64 exec, -1" ::"v"(qadr), "v"(qone), "n"(4 * (i)) : "memory")
        auto count_wait = [&](int i, unsigned want) __attribute__((always_inline)) {
          if (__builtin_expect((unsigned)__builtin_amdgcn_readfirstlane(qc) < want, 0)) {
            while ((unsigned)__builtin_amdgcn_readfirstlane(__hip_atomic_load(qcnt + i, __ATOMIC_RELAXED, __HIP_MEMORY_SCOPE_WORKGROUP)) < want) { if (SPINS) __builtin_amdgcn_s_sleep(SPINS); }
          }
          asm volatile("" ::: "memory");
        };
        if constexpr (IT == 0) {
          wait_vm<12 - vm_unit(1)>();
          STAMPAT(46);
          if (!(SYNCB & 1)) count_in(0);
          X_READ(0); X_READ(1);
          asm volatile("s_waitcnt lgkmcnt(0)" : "+v"(af[0][0]), "+v"(af[0][1]), "+v"(af[1][0]), "+v"(af[1][1])::"memory");
          X_UNIT1(3, 0); X_UNIT1(3, 1); X_UNIT1(4, 0); X_UNIT1(4, 1);
          STAMPAT(38);
          qc = 0;
          if (SYNCB & 1) asm volatile("s_barrier" ::: "memory");
          else count_wait(0, 8);
        }
        STAMPAT(39);
#pragma unroll
        for (int n = 0; n < 6; ++n) { QKV_RDA(0, n); if (n < 5) QKV_RDB(0, n); }
        f32x2 lv[4], xs, xq;
        float lmean;
        u32x2 ep;
        u32x4 xl[2];
        f32x4 sc;
#define X1_OFF() (F_X1 + ((p * RX1 + (xcd >> 1) % RX1) * 32 + (rt >> 2) * 16 + (l & 15)) * SX1)
        unsigned long long xok = 0;
        RowFin fin;
        auto x1_need = [&](unsigned long long m) __attribute__((always_inline)) {
          if (__builtin_expect((xok & m) != m, 0)) {
            for (unsigned spins = 0;;) {
              const u32x2 f = ld8(rs, X1_OFF(), 0);
              xok = __builtin_amdgcn_ballot_w64(f[0] == T[0] && f[1] == T[1]);
              if ((xok & m) == m) break;
              if (spin_fail(spins, rs, T)) { xok = ~0ull; break; }
              __builtin_amdgcn_s_sleep(KSL);
            }
            asm volatile("" ::: "memory");
          }
        };
        auto gap = [&](auto sc_, auto nc_) __attribute__((always_inline)) {
          constexpr int s = decltype(sc_)::value, n = decltype(nc_)::value;
          if constexpr ((SYNCB >> IT) & 1) {
            constexpr int tc = (s + 2) / 4;
            if constexpr (tc >= 1 && tc < 8 && s == 4 * tc - 2 && n == 5) {
              if constexpr (!IT || tc >= 3) wait_vm<vm_at(IT, s, n) - vm_tile(IT, tc)>();
              if constexpr (tc == 7 && !IT) asm volatile("s_waitcnt lgkmcnt(4)" ::: "memory");
              asm volatile("s_barrier" ::: "memory");
            }
          } else {
            constexpr int ts = (s + 3) / 4, tc = (s + 2) / 4, t0 = 1;
            if constexpr (ts >= 3 && ts < 8 && s == 4 * ts - 3 && n == 2) { wait_vm<vm_at(IT, s, n) - vm_tile(IT, ts)>(); count_in(0); }
            if constexpr (!IT && tc >= t0 && tc < 3 && s == 4 * tc - 2 && n == 1) { wait_vm<vm_at(IT, s, n) - vm_tile(IT, tc)>(); count_in(0); }
            if constexpr (IT && ts >= t0 && ts < 3 && s == 4 * ts - 3 && n == 2) count_in(0);
            if constexpr (tc >= t0 && tc < 8 && s == 4 * tc - 2) {
              if constexpr (n == 3) qc = __hip_atomic_load(qcnt, __ATOMIC_RELAXED, __HIP_MEMORY_SCOPE_WORKGROUP);
              if constexpr (n == 5) count_wait(0, 8 * (tc + 1 - IT));
            }
          }
          if constexpr (IT == 0 && n == 0 && s + 2 < 32) { wait_vm<vm_at(0, s, 0) - vm_unit(s + 2)>(); X_READ(s + 2); SB(); }
          if constexpr (n == 0 && tile_piece(IT, s)) QKV_DMA1(((s - tp0(IT)) >> 2) + 3, (s - tp0(IT)) & 3);
          if constexpr (IT == 0) {
            if constexpr ((n == 2 || n == 3) && s + 5 < 32) {
              if constexpr (n == 2) asm volatile("s_waitcnt lgkmcnt(%0)" ::"n"(s + 1 < QL ? 4 : 1) : "memory");
              X_UNIT1(s + 5, n - 2);
            }
            if constexpr (s >= W2S && s < W2S + 3 && n == 4) QKV_DMA1(2, s - W2S);
            if constexpr (s == LNL && n == 5) {
              asm volatile("buffer_load_dwordx2 %0, off, %1, 0 sc1" : "=v"(ep) : "s"(rs) : "memory");
              asm volatile("buffer_load_dwordx4 %0, %1, %2, %3 offen" : "=v"(xl[0]) : "v"(l * 16), "s"(rx), "s"((p * 256 + h * 8 + w) * 2048) : "memory");
              asm volatile("buffer_load_dwordx4 %0, %1, %2, %3 offen offset:1024" : "=v"(xl[1]) : "v"(l * 16), "s"(rx), "s"((p * 256 + h * 8 + w) * 2048) : "memory");
            }
            if constexpr (s == LNS && n == 1) {
              wait_vm<vm_at(0, s, n) - vm_at(0, LNL + 1, 0)>();
              asm volatile("" : "+v"(ep), "+v"(xl[0]), "+v"(xl[1])::"memory");
              const unsigned long long e = (((unsigned long long)(unsigned)__builtin_amdgcn_readfirstlane(ep[1]) << 32) | (unsigned)__builtin_amdgcn_readfirstlane(ep[0])) + 1ull + dispatch_id() * 0x9E3779B97F4A7C16ull;
              T = u32x2{(unsigned)e, (unsigned)(e >> 32)};
              unpack8v(xl[0], lv);
              xs = (lv[0] + lv[1]) + (lv[2] + lv[3]);
              xq = __builtin_elementwise_fma(lv[0], lv[0], lv[1] * lv[1]) + __builtin_elementwise_fma(lv[2], lv[2], lv[3] * lv[3]);
              asm volatile("" : "+v"(xs), "+v"(xq));
            }
            if constexpr (s == LNS && n == 4) {
              unpack8v(xl[1], lv);
              xs += (lv[0] + lv[1]) + (lv[2] + lv[3]);
              xq += __builtin_elementwise_fma(lv[0], lv[0], lv[1] * lv[1]) + __builtin_elementwise_fma(lv[2], lv[2], lv[3] * lv[3]);
              auto t = __builtin_amdgcn_permlane32_swap(__builtin_bit_cast(unsigned, hadd(xs)), __builtin_bit_cast(unsigned, hadd(xq)), false, false);
              lmean = asf(t[0]) + asf(t[1]);
              asm volatile("" : "+v"(lmean));
            }
            if constexpr (s == LNS + 1 && n == 1) { lmean += dppf<0xB1>(lmean); lmean += dppf<0x4E>(lmean); lmean += dppf<0x124>(lmean); asm volatile("" : "+v"(lmean)); }
            if constexpr (s == LNS + 1 && n == 4) { lmean = add_x16(lmean + dppf<0x128>(lmean)) * (1.0f / 1024.0f); asm volatile("" : "+v"(lmean)); }
            if constexpr (s == LNS + 2 && n == 1) {
              const float m2 = asf(__builtin_amdgcn_readlane(__builtin_bit_cast(unsigned, lmean), 32));
              xs[0] = ln_rd(fmaxf(__builtin_fmaf(-lmean, lmean, m2), 0.f));
              asm volatile("" : "+v"(xs));
            }
            if constexpr (s == LNS + 2 && n == 5) {
              if (l == 0) st16(u32x4{__builtin_bit_cast(unsigned, (float)xs[0]), __builtin_bit_cast(unsigned, -rbf1(lmean)), T[0], T[1]}, rs, OFF_AB + (p * 256 + h * 8 + w) * 16, 0);
            }
            if constexpr (s == POI && n == 1) {
              if (w == 1) {
                const unsigned prb = 2 * p + (h >> 4), pct = h & 15;
                unsigned pz1 = POISON;
                asm volatile("" : "+v"(pz1));
                const u32x4 pz = {pz1, pz1, pz1, pz1};
                st16(pz, rs, OFF_STAT + (prb * 16 + pct) * 2048 + l * 16, 0);
                st16(pz, rs, OFF_STAT + (prb * 16 + pct) * 2048 + 1024 + l * 16, 0);
                if (l < 16) st16(pz, rs, OFF_CA + prb * 4096 + pct * 256 + l * 16, 0);
                else if (l == 16 && pct < RKEY) st4(pz1, rs, KEY_AT(0, pct) + prb * SKEY, 0);
              }
            }
            if constexpr (s == POI + 4 && n == 1) { if (w == 1 && l < RX) st8(T, rs, F_XN + (p * RX + l) * 256 + h * 8, 0); }
          } else {
            if constexpr ((s == 0 && n == 0) || (s == 2 && n == 2)) asm volatile("buffer_load_dwordx2 %0, %1, %2, 0 offen sc1" : "=v"(cxn) : "v"(X1_OFF()), "s"(rs) : "memory");
            if constexpr ((s == 1 && n == 3) || (s == 4 && n == 0)) {
              wait_vm<vm_at(1, s, n) - (s == 1 ? vm_at(1, 0, 1) : vm_at(1, 2, 3))>();
              asm volatile("" : "+v"(cxn)::"memory");
              xok |= __builtin_amdgcn_ballot_w64(cxn[0] == T[0] && cxn[1] == T[1]);
            }
#define IMG_OFF() (F_X1 + ((((h >> 3) * 2 + ((l >> 2) & 1)) * RX1 + (xcd >> 1) % RX1) * 32 + (l & 1) * 16 + (h & 7) * 2 + ((l >> 1) & 1)) * SX1)
            if constexpr (s == 0 && n == 2) { asm volatile("" : "=v"(cx1)); if (w == 1) asm volatile("buffer_load_dwordx2 %0, %1, %2, 0 offen sc1" : "=v"(cx1) : "v"(IMG_OFF()), "s"(rs) : "memory"); }
            if constexpr (s == 2 && n == 4) {
              if (w == 1) {
                wait_vm<vm_at(1, 2, 4) - vm_at(1, 0, 3)>();
                asm volatile("" : "+v"(cx1)::"memory");
                if (!__all(cx1[0] == T[0] && cx1[1] == T[1])) poll_flags(rs, IMG_OFF(), 0, T);
              }
            }
            if constexpr (a_req(s, n) >= 0 && (a_req(s, n) & 7) == 0) x1_need(3ull << (((rot + a_req(s, n) / 8) & 7) * 2));
            const unsigned R = (rb * 4 + (w & 3)) * 32 + (w >> 2) * 16 + (l & 15), so = OFF_STAT + rb * 32768;
            if constexpr (s == (SPL < 0 ? 0 : SPL) && n == (SPL < 0 ? 1 : 4)) {
#pragma unroll
              for (int i = 0; i < 4; ++i) ld16h<0>(fx[i], rs, (4 * g + i) * 2048 + (R & 127) * 16, so);
            }
            if constexpr (s == SPL + 2 && n == 1) {
              wait_vm<vm_at(1, s, n) - (SPL < 0 ? vm_at(1, 0, 2) : vm_at(1, SPL, 5))>();
              PINV4(fx);
              fin = fin_key(rs, T, g * 8192 + (R & 127) * 16, so, smem + L_SKEY, l, w, R, wsum, fx);
              count_in(2);
            }
            if constexpr (s == SPL + 3 && n == 0) qc = __hip_atomic_load(qcnt + 2, __ATOMIC_RELAXED, __HIP_MEMORY_SCOPE_WORKGROUP);
            if constexpr (s == SPL + 3 && n == 1) { count_wait(2, 8); fin_cand(rs, smem + L_SKEY, 0, g, rb, ct, fin, v); STAMPAT(51); }
            if constexpr (s == SPL + 3 && n == 3) {
              fin_x2(fin, v);
              x2p[0] = pack8v(v); x2p[1] = pack8v(v + 4);
              asm volatile("" : "+v"(x2p[0]), "+v"(x2p[1]));
            }
            if constexpr (s == SPL + 3 && n == 5) {
              if ((w & 3) * 2 + (w >> 2) == ct >> 1) {
                const float f = 1.0f + fin.rd, rd2 = ln_rd(fin.var * f * f), mb2 = rbf1(__builtin_fmaf(fin.mean - fin.mb, fin.rd, fin.mean));
                if (g == 0 && ((l >> 3) & 1) == (ct & 1))
                  st16(u32x4{__builtin_bit_cast(unsigned, f * rd2), __builtin_bit_cast(unsigned, -__builtin_fmaf(fin.mb, fin.rd, mb2) * __builtin_amdgcn_rcpf(f)), T[0], T[1]}, rs, OFF_AB + 32768 + R * 16, 0);
              }
            }
          }
          if constexpr (IT == 1 && a_req(s, n) >= 0) A_LOAD1(a_req(s, n) / 2, a_req(s, n) & 1);
          if constexpr (!IT && s == CSL && n == 4) { asm volatile("" : "=v"(fx[0])); if (w < 6) ld16h<0>(fx[0], rs, l * 16, OFF_CS + (h * 6 + w) * 1024); }
          if constexpr (!IT && s == CSS && n == 2) {
            if (w < 6) {
              wait_vm<SINCE(CSL)>();
              asm volatile("" : "+v"(fx[0])::"memory");
              tag_wait(rs, T, l * 16, OFF_CS + (h * 6 + w) * 1024, fx[0]);
              const f32x2 c = {sum_hi8(asf(fx[0][0])), sum_hi8(asf(fx[0][1]))};
              if (l < 8) *(LDS_AS f32x2*)(smem + L_CSL + w * 64 + l * 8) = c;
            }
          }
          constexpr int ABL = abl(IT), ABS = abs_(IT);
          if constexpr (s == ABL && n == 4) ld16h<0>(bv[0], rs, (l & 31) * 16, AB_OFF());
          if constexpr (s == ABS && n == 2) {
            wait_vm<SINCE(ABL)>();
            asm volatile("" : "+v"(bv[0])::"memory");
            tag_wait(rs, T, (l & 31) * 16, AB_OFF(), bv[0]);
            const unsigned t0 = bv[0][0], t1 = bv[0][1];
            ((LDS_AS unsigned*)(smem + L_ABW + rt * 2048))[l] = l < 32 ? t0 : t1;
          }
          if constexpr (s == ABS && n == 3) {
#pragma unroll
            for (int a = 0; a < 2; ++a) {
              const LDS_AS unsigned* aw = (const LDS_AS unsigned*)(smem + L_ABW + rt * 2048) + 16 * a;
              aq[a] = aw[l & 15]; bq[a] = aw[32 + (l & 15)];
              av[a] = *(const LDS_AS u32x4*)(aw + 4 * g); bv[a] = *(const LDS_AS u32x4*)(aw + 32 + 4 * g);
            }
          }
          if constexpr (s == ABS && n == 5) {
#pragma unroll
            for (int a = 0; a < 2; ++a) { b2[a] = f32x2{asf(bq[a]), asf(bq[a])}; asm volatile("" : "+v"(b2[a])); }
          }
          if constexpr (s == ABS + 1) {
            constexpr int k = (n + 4) % 6;
            if constexpr (k < 4) cs4[k & 1] = *(const LDS_AS f32x4*)(smem + L_CSL + k * 64 + g * 16);
            else cs4[k & 1][0] = *(const LDS_AS float*)(smem + L_CSL + k * 64 + (l & 15) * 4);
          }
          if constexpr ((s == ABS + 1 && n > 0) || (s == ABS + 2 && n == 0)) {
            constexpr int k = (n + 3) % 6;
            if constexpr (k < 4) {
#pragma unroll
              for (int a = 0; a < 2; ++a) acc[a][k] = __builtin_elementwise_fma(cs4[k & 1], f32x4{b2[a][0], b2[a][1], b2[a][0], b2[a][1]}, acc[a][k]);
            } else {
              f32x2 c2 = {cs4[k & 1][0], cs4[k & 1][0]};
              asm volatile("" : "+v"(c2));
#pragma unroll
              for (int a = 0; a < 2; ++a) acc[a][k] = __builtin_elementwise_fma(f32x4{c2[0], c2[1], c2[0], c2[1]}, __builtin_bit_cast(f32x4, bv[a]), acc[a][k]);
            }
          }
          if constexpr (IT == 0) {
            if constexpr (s == CSD && n < 3) fx[n] = frag0(SLOT((s >> 2) + PO()), 3 * w + n);
            if constexpr (s == CSD + 1 && n == 1) {
              unsigned one2 = 0x3F803F80u;
              asm volatile("" : "+v"(one2));
              const u32x4 o4 = {one2, one2, one2, one2};
              const f32x4 z4 = {0.f, 0.f, 0.f, 0.f};
              sa = mfma16(fx[0], o4, z4); sb = mfma16(fx[1], o4, z4); sc = mfma16(fx[2], o4, z4);
            }
            if constexpr (s == CSD + 2 && n == 1) {
              const int c = l & 15;
              const f32x4 t0 = sa, t1 = sb, t2 = sc, t = c == 0 ? +t0 : c == 1 ? +t1 : +t2;
              if (c < 3) *(LDS_AS f32x4*)(smem + L_CSP + (w * 3 + c) * 64 + g * 16) = t;
              count_in(2);
            }
            if constexpr (s == CSD + 4 && n == 1) {
              if (w == 2) {
                qc = 0;
                count_wait(2, 8);
                const LDS_AS f32x2* cp = (const LDS_AS f32x2*)(smem + L_CSP) + min(l, 47);
                const f32x2 t = (cp[0] + cp[48]) + (cp[96] + cp[144]);
                if (l < 48) st16(u32x4{__builtin_bit_cast(unsigned, (float)t[0]), __builtin_bit_cast(unsigned, (float)t[1]), T[0], T[1]}, rs, (l >> 3) * 1024 + (l & 7) * 16, OFF_CS + h * 6144 + p * 128);
              }
            }
            if constexpr (s >= 29 && (n & 1)) keep[3 * (s - 29) + (n >> 1)] = frag0(SLOT(s - 24), 3 * w + (n >> 1));
            if constexpr (s >= TS0 && s < TS0 + TSN + TSD * (7 >> TSG) && n == 4) {
              if (tstep == s && ((TM0 >> w) & 1)) TOUCH(tsrc);
            }
            if constexpr (s == TEX && n == 5) {
              const GAS bf16* t2 = ((TM1 >> w) & 1) ? tsrc : (const GAS bf16*)x + p * 262144;
              TOUCH(t2);
            }
          } else {
            if constexpr (s >= 27 && s < 30 && (n == 0 || n == 3)) { const unsigned wov = WO_VOFF(); WO_PIECE(4 + 2 * (s - 27) + n / 3); }
            if constexpr (s == FKL && n == 4) { asm volatile("" : "=v"(kv)); if (w == 0) ld4h<0>(kv, rs, (l & 15) * SKEY, KEY_AT(0, KREP())); }
            if constexpr (s == FSL && n == 1) {
              asm volatile("" : "=v"(fx[0]), "=v"(fx[1]));
              if (w == 0) {
                wait_vm<SINCE(FKL)>();
                asm volatile("" : "+v"(kv)::"memory");
                STAMPAT(52);
                const unsigned k_ = key_wait(rs, T, l, 0, KREP(), kv);
                STAMPAT(53);
                if (l == 0) *(LDS_AS unsigned*)(smem + L_SKEY + 32) = k_;
                fkey = k_;
                ld16h<0>(fx[0], rs, (l >> 3) * 256 + (l & 7) * 16, cand_off(k_, 0)); ld16h<2048>(fx[1], rs, (l >> 3) * 256 + (l & 7) * 16, cand_off(k_, 0));
              }
            }
            if constexpr (s == FS0 - 1 && n == FSN) {
              if (w == 0) {
                wait_vm<vm_at(1, s, n) - vm_at(1, FSL, 2)>();
                asm volatile("" : "+v"(fx[0]), "+v"(fx[1])::"memory");
                word_wait<2>(rs, T, (l >> 3) * 256 + (l & 7) * 16, cand_off(fkey, 0), fx);
                *(LDS_AS u32x4*)(smem + L_ROW + l * 16) = fx[0]; *(LDS_AS u32x4*)(smem + L_ROW + 1024 + l * 16) = fx[1];
                count_in(0);
              }
            }
            if constexpr (s == FS0 - 1 && n == 1) {
              asm volatile("" : "=v"(dfr[0]), "=v"(dfr[1]), "=v"(dfr[2]), "=v"(dfr[3]), "=v"(dfr[4]), "=v"(dfr[5]), "=v"(dfr[6]), "=v"(dfr[7]));
              if (ffn_on) {
#pragma unroll
                for (int i = 0; i < 8; ++i) dfr[i] = trread(smem, L_DN + w * 4096 + g * 1024 + ((l & 15) >> 2) * 256 + i * 32 + (l & 3) * 8);
              }
            }
            if constexpr (s == FS0) {
              if constexpr (n == 0) qc = __hip_atomic_load(qcnt, __ATOMIC_RELAXED, __HIP_MEMORY_SCOPE_WORKGROUP);
              if constexpr (n == 1) {
                count_wait(0, 1);
                kv = *(const LDS_AS unsigned*)(smem + L_SKEY + 32);
#pragma unroll
                for (int i = 0; i < 4; ++i) fx[i] = *(const LDS_AS u32x4*)(smem + L_ROW + w * 256 + i * 64 + g * 16);
              }
              if constexpr (n == 2) {
                fkey = __builtin_amdgcn_readfirstlane(kv);
                sel0 = key_row(fkey);
                aff = (int)(sel0 >> 8) == p;
                asm volatile("" : "=v"(sa), "=v"(sb));
                if (ffn_on) ffn_gu(gv, uv, fx, sa, sb);
              }
              if constexpr (n == 4) {
                if (ffn_on) {
                  ffn_gu_store(smem, l, w, sa, sb);
                  count_in(1);
                }
              }
            }
            if constexpr (s == FS0D) {
              if constexpr (n == 0) qc = __hip_atomic_load(qcnt + 1, __ATOMIC_RELAXED, __HIP_MEMORY_SCOPE_WORKGROUP);
              if constexpr (n == 1) { asm volatile("" : "=v"(sa)); if (ffn_on) { count_wait(1, 8); sa = ffn_d_read(smem, l); } }
              if constexpr (n == 2) { asm volatile("" : "=v"(hh)); if (ffn_on) hh = ffn_d_h(sa); }
              if constexpr (n == 4) { asm volatile("" : "=v"(sa)); if (ffn_on) sa = ffn_d_all(dfr, hh); }
              if constexpr (n == 5) { if (ffn_on) ffn_d_store(rs, l, w, 0, slice, sa); STAMPAT(54); }
            }
            if constexpr (s == FS1 && n == 4) {
              DEF4(fx);
              asm volatile("" : "=v"(cxn), "=v"(cx1));
              if (!ffn_on) {
                const unsigned G = xcd * 4 + bi - 28;
#pragma unroll
                for (int k = 0; k < 4; ++k) ld16h<0>(fx[k], rs, RED_VO(k), RED_SO(0, G));
                ld8h(cxn, rs, RED_JJ(), cand_off(fkey, 0) + XO(G)); ld8h(cx1, rs, RED_JJ() + 128, cand_off(fkey, 0) + XO(G));
              }
            }
            if constexpr (s == FS1S && n == 2) {
              if (!ffn_on) {
                wait_vm<SINCE(FS1)>();
                PINV4(fx);
                asm volatile("" : "+v"(cxn), "+v"(cx1)::"memory");
                STAMPAT(55);
                red_sum(rs, smem, T, tid, w, 0, xcd * 4 + bi - 28, fkey, fx, cxn, cx1);
                STAMPAT(56);
                BAR();
                if (w == 0) {
                  const u32x2 ov = red_final(smem, l, fkey, cxn, cx1);
                  if (l < 8) st8(unpoison2(ov), rs, OFF_XNEW + (xcd * 4 + bi - 28) * 256 + ((l & 3) * 2 + (l >> 2)) * 8, 0);
                }
              }
            }
            if constexpr (s == FXA && n == 4) { asm volatile("" : "=v"(fx[0])); if (w == 1) ld16h<0>(fx[0], rs, ((l >> 2) & 3) * 256 + (l & 3) * 16, XP_OFF()); }
            if constexpr (s == FXB - 1 && n == FSN) {
              if (w == 1) {
                wait_vm<SINCE(FXA)>();
                asm volatile("" : "+v"(fx[0])::"memory");
                word_wait<1>(rs, T, ((l >> 2) & 3) * 256 + (l & 3) * 16, XP_OFF(), fx);
                if (l < 16) *(LDS_AS u32x4*)(smem + L_ROW + l * 16) = fx[0];
                count_in(0);
              }
            }
            if constexpr (s == FXB) {
#define FX_FRAG(i) (*(const LDS_AS u32x4*)(size_t)(bo[0][0] + (1 + PO()) * STAGE + (6 * (i) + w) * 1024))
              if constexpr (n == 0) { STAMPAT(58); qc = __hip_atomic_load(qcnt, __ATOMIC_RELAXED, __HIP_MEMORY_SCOPE_WORKGROUP); }
              if constexpr (n == 1) {
                DEF4(fx); DEF4(df);
                if (w < 6) {
                  count_wait(0, 2);
#pragma unroll
                  for (int i = 0; i < 4; ++i) { fx[i] = *(const LDS_AS u32x4*)(smem + L_ROW + i * 64 + g * 16); df[i] = FX_FRAG(i); }
                }
              }
              if constexpr (n == 2) { asm volatile("" : "=v"(sa), "=v"(sb)); if (w < 6) dot_mma(fx); }
              if constexpr (n == 4) { if (w < 6) dot_store(OFF_FX); STAMPAT(57); }
            }
            if constexpr (s == FXL && n == 4) {
              asm volatile("" : "=v"(fx[0]), "=v"(fx[1]));
              if (aff && w < 4) { ld16h<0>(fx[0], rs, FP_VO(), FP_OFF(0)); ld16h<0>(fx[1], rs, FP_VO(), FP_OFF(1)); }
            }
            if constexpr (s == FXS && n == 4) {
              if (aff && w < 4) {
                STAMPAT(59);
                wait_vm<0>();
                asm volatile("" : "+v"(fx[0]), "+v"(fx[1])::"memory");
                if (!FX_OK(2)) {
                  unsigned spins = 0;
                  do {
                    if (spin_fail(spins, rs, T)) break;
                    fx[0] = ld16(rs, FP_VO(), FP_OFF(0)); fx[1] = ld16(rs, FP_VO(), FP_OFF(1));
                  } while (!FX_OK(2));
                }
                STAMPAT(60);
                if (w == 3) {
                  f32x2 nx[8];
                  unpack8v(fx[0], nx); unpack8v(fx[1], nx + 4);
                  const f32x2 sm = ((nx[0] + nx[1]) + (nx[2] + nx[3])) + ((nx[4] + nx[5]) + (nx[6] + nx[7]));
                  const float mean = sum64(hadd(sm)) * (1.0f / 1024.0f);
                  const f32x2 m2 = {mean, mean};
                  f32x2 q = {0.f, 0.f};
#pragma unroll
                  for (int j = 0; j < 8; ++j) { const f32x2 d = nx[j] - m2; q = __builtin_elementwise_fma(d, d, q); }
                  const float rd = ln_rd(sum64(hadd(q)) * (1.0f / 1024.0f));
                  if (l == 0) *(LDS_AS f32x2*)(smem + L_FXL + 384) = f32x2{rd, -rbf1(mean)};
                } else {
                  tile_sum(fx[0], L_FXL);
                }
                count_in(3);
              }
            }
          }
        };
        static_for<32>([&](auto sc_) __attribute__((always_inline)) {
          constexpr int s = decltype(sc_)::value;
          if constexpr (s > 0) PIN_ACC();
          if constexpr (IT == 1) wait_vm<vm_at(IT, s, 0) - vm_a(IT, s)>();
          pin_w<s % WA>(af[s % WA][0], af[s % WA][1]);
          VSTEP(s);
          SB();
          static_for<6>([&](auto nc_) __attribute__((always_inline)) {
            constexpr int n = decltype(nc_)::value;
            if constexpr (s == 0) { acc[0][n] = f32x4{0.f, 0.f, 0.f, 0.f}; acc[1][n] = acc[0][n]; }
            constexpr bool on = s < QL || n >= 2;
            if constexpr (on) acc[0][n] = n < 4 ? mfma16(bfr[n].v, af[s % WA][0], acc[0][n]) : mfma16(af[s % WA][0], bfr[n].v, acc[0][n]);
            SB();
            if constexpr (n == 0) { QKV_RDB(s, 5); }
            else if constexpr (s + 1 < 32 && (s + 1 < QL || n > 2)) QKV_RDB(s + 1, n - 1);
            SB();
            if constexpr (on) acc[1][n] = n < 4 ? mfma16(bfr[n].v, af[s % WA][1], acc[1][n]) : mfma16(af[s % WA][1], bfr[n].v, acc[1][n]);
            SB();
            if constexpr (s + 1 < 32 && (s + 1 < QL || n >= 2)) QKV_RDA(s + 1, n);
            SB();
            gap(sc_, nc_);
            GSTEP(s, n);
            SB();
          });
        });
        PIN_ACC();
        VSTEP(32);
        STAMPAT(18);
        VSTEP(33);
        frag_t qw[32 - QL][2];
        static_for<32 - QL>([&](auto c_) __attribute__((always_inline)) {
          constexpr int s = QL + decltype(c_)::value;
          QKV_RDA(s, 0); QKV_RDB(s, 0); QKV_RDA(s, 1); QKV_RDB(s, 1);
          qw[s - QL][0] = bfr[0]; qw[s - QL][1] = bfr[1];
        });
        SB();
        const bool own = aff && (int)((sel0 >> 5) & 7) == rt;
        const bool m0 = !(sel0 & 16), mq = (l & 15) == (int)(sel0 & 15), mv = g == (int)((sel0 & 15) >> 2);
        auto patch_qk = [&](int n) __attribute__((always_inline)) {
          f32x4 t = *(const LDS_AS f32x4*)(smem + L_FXL + n * 64 + g * 16);
          const f32x4 c = *(const LDS_AS f32x4*)(smem + L_CSL + n * 64 + g * 16);
          const float nb = *(const LDS_AS float*)(smem + L_FXL + 388);
#pragma unroll
          for (int e = 0; e < 4; ++e) { t[e] = __builtin_fmaf(c[e], nb, t[e]); asm volatile("" : "+v"(t[e])); }
          acc[0][n] = (mq && m0) ? t : acc[0][n];
          acc[1][n] = (mq && !m0) ? t : acc[1][n];
        };
        if (own) {
          while (__hip_atomic_load(qcnt + 3, __ATOMIC_RELAXED, __HIP_MEMORY_SCOPE_WORKGROUP) < 4u) {}
          asm volatile("" ::: "memory");
          patch_qk(2); patch_qk(3);
          const f32x2 nab = *(const LDS_AS f32x2*)(smem + L_FXL + 384);
          const LDS_AS float* fl_ = (const LDS_AS float*)(smem + L_FXL + 256) + (l & 15);
          const LDS_AS float* cl_ = (const LDS_AS float*)(smem + L_CSL + 256) + (l & 15);
          const float t4 = __builtin_fmaf(cl_[0], nab[1], fl_[0]), t5 = __builtin_fmaf(cl_[16], nab[1], fl_[16]);
          const unsigned na = __builtin_bit_cast(unsigned, (float)nab[0]);
#pragma unroll
          for (int a = 0; a < 2; ++a) {
            const bool ma = a ? !m0 : m0;
            aq[a] = (mq && ma) ? na : aq[a];
#pragma unroll
            for (int e = 0; e < 4; ++e) {
              const bool me = mv && ma && e == (int)(sel0 & 3);
              acc[a][4][e] = me ? t4 : acc[a][4][e]; acc[a][5][e] = me ? t5 : acc[a][5][e];
              av[a][e] = me ? na : av[a][e];
            }
          }
        }
        constexpr float C2 = 1.4426950408889634f * 0.17677669529663687f;
#pragma unroll
        for (int a = 0; a < 2; ++a) {
          f32x2 a2 = {asf(aq[a]), asf(aq[a])};
          qs2[a] = f32x2{asf(aq[a]) * C2, asf(aq[a]) * C2};
          asm volatile("" : "+v"(a2), "+v"(qs2[a]));
          acc[a][2] *= f32x4{a2[0], a2[1], a2[0], a2[1]}; acc[a][3] *= f32x4{a2[0], a2[1], a2[0], a2[1]};
          acc[a][4] *= __builtin_bit_cast(f32x4, av[a]); acc[a][5] *= __builtin_bit_cast(f32x4, av[a]);
        }
        k0 = pack8(acc[0][2], acc[0][3]); k1 = pack8(acc[1][2], acc[1][3]); v0 = pack8(acc[0][4], acc[1][4]); v1 = pack8(acc[0][5], acc[1][5]);
        const unsigned kvo = OFF_KV + IT * (8u << 20) + (p * 32 + h) * 32768 + rt * 4096;
        if (p < 7) { st16(k0, rs, l * 16, kvo); st16(k1, rs, l * 16 + 1024, kvo); st16(v0, rs, l * 16 + 2048, kvo); st16(v1, rs, l * 16 + 3072, kvo); }
        char* ownp = smem + (8 + rt) * 2048 + l * 16;
        *(u32x4*)(ownp + L_K) = k0;
        if (rt < 7) *(u32x4*)(ownp + L_K + 1024) = k1;
        *(u32x4*)(ownp + L_V) = v0; *(u32x4*)(ownp + L_V + 1024) = v1;
        SB();
        static_for<32 - QL>([&](auto c_) __attribute__((always_inline)) {
          constexpr int s = QL + decltype(c_)::value;
#pragma unroll
          for (int n = 0; n < 2; ++n) { acc[0][n] = mfma16(qw[s - QL][n].v, af[s % WA][0], acc[0][n]); acc[1][n] = mfma16(qw[s - QL][n].v, af[s % WA][1], acc[1][n]); }
        });
        if (own) { patch_qk(0); patch_qk(1); }
      };
      qkv_loop(it_c);
    }
#undef A_LOAD1
#undef QKV_RDA
#undef QKV_RDB
#undef QKV_BO
    STAMPAT(2); STAMPAT(21);
    {
      IDS();
      const GAS bf16 *pwo, *pwg, *pwu;
      asm volatile("s_load_dwordx2 %0, %3, 24\n\ts_load_dwordx2 %1, %3, 40\n\ts_load_dwordx2 %2, %3, 48" : "=&s"(pwo), "=&s"(pwg), "=&s"(pwu) : "s"(ka));
      const int ql = l & 15;
      const f32x4 z4 = {0.f, 0.f, 0.f, 0.f}, ninf = {-INFINITY, -INFINITY, -INFINITY, -INFINITY};
      LDS_AS unsigned* rdy = (LDS_AS unsigned*)(smem + L_RDY);
      const unsigned rtag = T[0] + it;
      u32x4 qf[2];
      f32x4 cn[2], oa[2][2], sum[2], S[2][2];
      bool told = false;
      auto announce = [&](bool now) __attribute__((always_inline)) {
        if (!told) {
          unsigned st;
          asm volatile("s_getreg_b32 %0, hwreg(HW_REG_IB_STS)" : "=s"(st)::"memory");
          if (now || !(st & 0xC0000Fu)) {
            DRAIN();
            if (l == 0) st8(T, rs, rt * SKW, F_KV + (it * 256 + p * 32 + h) * SKV);
            told = true;
            STAMPAT(34);
          }
        }
      };
      unsigned one2 = 0x3F803F80u;
      asm volatile("" : "+v"(one2));
      const u32x4 ones = {one2, one2, one2, one2};
      auto ex8 = [&](f32x4 a, f32x4 b) __attribute__((always_inline)) {
        return u32x4{pack2(__builtin_amdgcn_exp2f(a[0]), __builtin_amdgcn_exp2f(a[1])), pack2(__builtin_amdgcn_exp2f(a[2]), __builtin_amdgcn_exp2f(a[3])),
                     pack2(__builtin_amdgcn_exp2f(b[0]), __builtin_amdgcn_exp2f(b[1])), pack2(__builtin_amdgcn_exp2f(b[2]), __builtin_amdgcn_exp2f(b[3]))};
      };
      {
#pragma unroll
        for (int a = 0; a < 2; ++a) {
          const f32x4 c4 = {qs2[a][0], qs2[a][1], qs2[a][0], qs2[a][1]};
          qf[a] = pack8(acc[a][0] * c4, acc[a][1] * c4);
        }
        f32x4 mk;
#pragma unroll
        for (int e = 0; e < 4; ++e) mk[e] = (4 * g + e <= ql) ? 0.f : -INFINITY;
        const f32x4 d0 = mfma16(k0, qf[0], mk), d1 = mfma16(k1, qf[1], mk), d2 = mfma16(k0, qf[1], z4);
        const float m0 = -max_x32(max_x16(fmaxf(fmaxf(d0[0], d0[1]), fmaxf(d0[2], d0[3]))));
        const float m1 = -max_x32(max_x16(fmaxf(fmaxf(d1[0], d1[1]), fmaxf(d1[2], d1[3]))));
        cn[0] = f32x4{m0, m0, m0, m0}; cn[1] = f32x4{m1, m1, m1, m1};
        asm volatile("" : "+v"(cn[0]), "+v"(cn[1]));
        announce(false);
        const f32x4 e0 = d0 + cn[0];
        const u32x4 p0 = {pack2(__builtin_amdgcn_exp2f(e0[0]), __builtin_amdgcn_exp2f(e0[1])), pack2(__builtin_amdgcn_exp2f(e0[2]), __builtin_amdgcn_exp2f(e0[3])), 0u, 0u};
        const u32x4 p1 = ex8(d2 + cn[1], d1 + cn[1]);
        oa[0][0] = mfma16(v0, p0, z4); oa[0][1] = mfma16(v1, p0, z4); sum[0] = mfma16(ones, p0, z4);
        oa[1][0] = mfma16(v0, p1, z4); oa[1][1] = mfma16(v1, p1, z4); sum[1] = mfma16(ones, p1, z4);
      }
      const char* kb_ = smem + L_K + l * 16;
      const char* vb_ = smem + L_V + l * 16;
      const unsigned wov = WO_VOFF(), fnv = (l >> 1) * (F_ * 2) + (l & 1) * 16;
      asm volatile("" : "+v"(oa[0][0]), "+v"(oa[0][1]), "+v"(oa[1][0]), "+v"(oa[1][1]), "+v"(sum[0]), "+v"(sum[1]));
      const unsigned pw = __builtin_amdgcn_readfirstlane(it * 256 + p * 32 + h - 32), pfo = F_KV + pw * SKV + rt * SKW;
      const bool fetcher = rt < 4 && p > 0;
      announce(false);
      STAMPAT(32);
      BAR();
      asm volatile("" : "+s"(pwo), "+s"(pwg), "+s"(pwu));
      STAMPAT(33);
#define KV_FETCH(gd)                                                                                                          \
  {                                                                                                                           \
    const unsigned pvo = OFF_KV + pw * 32768 + (gd) * 4096;                                                                   \
    dma16s<0>(rs, l * 16, pvo, lbase + L_K + (gd) * 2048); dma16s<1024>(rs, l * 16, pvo, lbase + L_K + (gd) * 2048);           \
    dma16s<0>(rs, l * 16, pvo + 2048, lbase + L_V + (gd) * 2048); dma16s<1024>(rs, l * 16, pvo + 2048, lbase + L_V + (gd) * 2048); \
  }
      auto fetch = [&]() __attribute__((always_inline)) {
        poll_flags(rs, KVJ ? (l & 1) * 4 * SKW : 0, pfo, T);
        KV_FETCH(rt);
        if (KVJ) { KV_FETCH(rt + 4); if (l == 0) rdy[8 + rt] = rtag; }
        STAMPAT(35);
      };
      announce(false);
      const bool first = fetcher && told;
      if (first) fetch();
#define LDS16(a_) (*(const LDS_AS u32x4*)(size_t)(a_))
      u32x4 kf[2], vf[2], p0, p1;
#define AE(a, i) { S[a][(i) >> 2][(i) & 3] = __builtin_amdgcn_exp2f(S[a][(i) >> 2][(i) & 3]); asm volatile("" : "+v"(S[a][(i) >> 2][(i) & 3])); SB(); }
#define AC(a, P, j) { P[j] = pack2(S[a][(j) >> 1][2 * ((j) & 1)], S[a][(j) >> 1][2 * ((j) & 1) + 1]); asm volatile("" : "+v"(P[j])); SB(); }
#define PHASE(a, P, M1, M2, L2, M3, M4, L4, M5)                                                                               \
  AE(a, 0) AE(a, 1) M1; SB(); AE(a, 2) AE(a, 3) M2; SB(); L2; SB(); AC(a, P, 0) AC(a, P, 1) AE(a, 4) M3; SB(); AE(a, 5) AE(a, 6) M4; SB(); \
  L4; SB(); AE(a, 7) AC(a, P, 2) M5; SB(); AC(a, P, 3)
#define PV(a, d, P) oa[a][d] = mfma16(vf[d], P, oa[a][d])
#define RS(a, P) sum[a] = mfma16(ones, P, sum[a])
#define SC(a, t, c) S[a][t] = mfma16(kf[t], qf[a], c)
#define K_AT(ad) (kf[0] = LDS16(ad), kf[1] = LDS16((ad) + 1024))
#define V_AT(ad) (vf[0] = LDS16((ad) + L_V), vf[1] = LDS16((ad) + L_V + 1024))
#define A_FIRST(ad, c00, c01, c10, c11, KN) { K_AT(ad); V_AT(ad); SB(); SC(0, 0, c00); SC(0, 1, c01); SB(); PHASE(0, p0, SC(1, 0, c10), SC(1, 1, c11), KN, , , , ) }
#define A_NEXT(ad, c00, c01, c10, c11, KN)                                                                                        \
  { PHASE(1, p1, PV(0, 0, p0), PV(0, 1, p0), , SC(0, 0, c00), SC(0, 1, c01), , RS(0, p0))                                     \
    PHASE(0, p0, PV(1, 0, p1), PV(1, 1, p1), V_AT(ad), SC(1, 0, c10), SC(1, 1, c11), KN, RS(1, p1)) }
#define A_LAST() { PHASE(1, p1, PV(0, 0, p0), PV(0, 1, p0), , RS(0, p0), , , ) SB(); PV(1, 0, p1); PV(1, 1, p1); RS(1, p1); }
      int nw = 0;
      auto piece = [&]() __attribute__((always_inline)) {
        if constexpr (it == 0) { if (ffn_on && nw < 8) { FFN_DMA(nw); } ++nw; }
      };
      unsigned ad = lbase + l * 16 + (6 + rt) * 2048;
      asm volatile("" : "+v"(ad));
      if (rt > 0) {
        A_FIRST(ad + 2048, cn[0], cn[0], cn[1], cn[1], K_AT(ad));
#pragma unroll 1
        for (int i = rt - 1; i > 0; --i) {
          ad -= 2048;
          asm volatile("" : "+v"(ad));
          if (PCE && told && rt >= 4) piece();
          announce(false);
          A_NEXT(ad + 2048, cn[0], cn[0], cn[1], cn[1], K_AT(ad));
        }
        announce(false);
        A_LAST();
      }
      announce(true);
      STAMPAT(26);
      {
        f32x4 mk;
#pragma unroll
        for (int e = 0; e < 4; ++e) mk[e] = (4 * g + e > ql) ? 0.f : -INFINITY;
        f32x4 cm0 = cn[0] + mk, cm1 = cn[1] + mk, cmi = ninf;
        asm volatile("" : "+v"(cm0), "+v"(cm1), "+v"(cmi));
        if (rt >= 4) {
          if (p > 0) {
            while (!__all(__hip_atomic_load(rdy + 8 + (l & 3), __ATOMIC_RELAXED, __HIP_MEMORY_SCOPE_WORKGROUP) == rtag)) __builtin_amdgcn_s_sleep(1);
            asm volatile("" ::: "memory");
          }
          if constexpr (it == 0) { for (; nw < 8;) piece(); }
          static_for<it ? 12 : 8>([&](auto c_) __attribute__((always_inline)) { constexpr int i = decltype(c_)::value; WO_PIECE_W(15 - i / 2, 2 * (rt & 3) + (i & 1)); });
        } else if (p > 0) {
          if (!first) fetch();
          if (KVJ) {
            DRAIN();
            if (l < 2) rdy[rt + 4 * l] = rtag;
            if constexpr (it == 0) { for (; nw <= rt;) piece(); }
          } else {
            const u32x2 f2 = ld8(rs, 0, pfo + 4 * SKW);
            const bool up = __all(f2[0] == T[0] && f2[1] == T[1]);
            DRAIN();
            if (l == 0) rdy[rt] = rtag;
            if (!up) poll_flags(rs, 0, pfo + 4 * SKW, T);
            KV_FETCH(rt + 4);
            if (l == 0) rdy[8 + rt] = rtag;
            told = false;
          }
        }
        STAMPAT(3);
        if (p > 0) {
          auto landed = [&](bool now) __attribute__((always_inline)) {
            if (!told) {
              unsigned st;
              asm volatile("s_getreg_b32 %0, hwreg(HW_REG_IB_STS)" : "=s"(st)::"memory");
              if (now || !(st & 0xC0000Fu)) {
                DRAIN();
                if (l == 0) rdy[rt + 4] = rtag;
                told = true;
              }
            }
          };
          unsigned have = 0;
          auto need = [&](int gq) __attribute__((always_inline)) {
            if (__builtin_expect(gq < 8 && !((have >> gq) & 1), 0)) {
              do {
                const unsigned t = __hip_atomic_load(rdy + (l & 7), __ATOMIC_RELAXED, __HIP_MEMORY_SCOPE_WORKGROUP);
                have = (unsigned)__builtin_amdgcn_ballot_w64(t == rtag);
              } while (!((have >> gq) & 1));
              asm volatile("" ::: "memory");
            }
          };
          ad = lbase + l * 16 + rt * 2048;
          asm volatile("" : "+v"(ad));
          need(rt);
          K_AT(ad); V_AT(ad); SB(); SC(0, 0, cm0); SC(0, 1, cn[0]); SB();
          PHASE(0, p0, SC(1, 0, cmi), SC(1, 1, cm1), , , , , )
          if (!KVJ) landed(false);
          need(rt + 1);
          K_AT(ad + 2048);
#pragma unroll 1
          for (int gq = rt + 1; gq < 8; ++gq) {
            ad += 2048;
            asm volatile("" : "+v"(ad));
            if (KVJ) piece();
            else { landed(gq >= rt + KVD); if (told) { piece(); if (PC2) piece(); } }
            need(gq + 1);
            A_NEXT(ad, cn[0], cn[0], cn[1], cn[1], K_AT(ad + 2048));
          }
          A_LAST();
        }
        if constexpr (it == 0) { for (; nw < 8;) piece(); }
      }
      STAMPAT(25);
      {
        float big = 0.f;
#pragma unroll
        for (int a = 0; a < 2; ++a)
#pragma unroll
          for (int d = 0; d < 2; ++d)
            big = __builtin_elementwise_maximum(__builtin_elementwise_maximum(big, __builtin_elementwise_maximum(fabsf(oa[a][d][0]), fabsf(oa[a][d][1]))),
                                                __builtin_elementwise_maximum(fabsf(oa[a][d][2]), fabsf(oa[a][d][3])));
        bool ok = sum[0][0] >= 0.5f && sum[0][0] < 0x1p64f && sum[1][0] >= 0.5f && sum[1][0] < 0x1p64f && big < INFINITY;
        if (!__all(ok)) {
#pragma unroll
          for (int a = 0; a < 2; ++a) {
            const int qs = 2 * rt + a, qq = 256 + 16 * qs + ql, klo = max(qq - 255, p > 0 ? 0 : 256);
            auto score = [&](int t) __attribute__((always_inline)) {
              f32x4 s = mfma16(t == 31 ? k1 : *(const u32x4*)(kb_ + t * 1024), qf[a], z4);
#pragma unroll
              for (int e = 0; e < 4; ++e) { const int kk = 16 * t + 4 * g + e; s[e] = (kk <= qq && kk >= klo) ? s[e] : -INFINITY; }
              return s;
            };
            float mx = -INFINITY;
#pragma unroll 1
            for (int t = p > 0 ? qs : 16; t <= qs + 16; ++t) { const f32x4 s = score(t); mx = fmaxf(fmaxf(mx, fmaxf(s[0], s[1])), fmaxf(s[2], s[3])); }
            mx = max_x32(max_x16(mx));
            oa[a][0] = z4; oa[a][1] = z4; sum[a] = z4;
#pragma unroll 1
            for (int G = p > 0 ? qs >> 1 : 8; G <= (qs + 16) >> 1; ++G) {
              const f32x4 s0 = score(2 * G), s1 = score(2 * G + 1);
              f32x4 e0, e1;
#pragma unroll
              for (int e = 0; e < 4; ++e) { e0[e] = __builtin_amdgcn_exp2f(s0[e] - mx); e1[e] = __builtin_amdgcn_exp2f(s1[e] - mx); }
              const u32x4 pf = pack8(e0, e1);
              oa[a][0] = mfma16(*(const u32x4*)(vb_ + G * 2048), pf, oa[a][0]);
              oa[a][1] = mfma16(*(const u32x4*)(vb_ + G * 2048 + 1024), pf, oa[a][1]);
              sum[a] = mfma16(ones, pf, sum[a]);
            }
          }
        }
      }
#pragma unroll
      for (int a = 0; a < 2; ++a) {
        const float inv = __builtin_amdgcn_rcpf(sum[a][0]);
        oa[a][0] *= inv; oa[a][1] *= inv;
      }
      const unsigned oo = OFF_O + it * (4u << 20) + ((p * 8 + rt) * 64 + h * 2) * 1024;
      st16(pack8(oa[0][0], oa[0][1]), rs, l * 16, oo);
      st16(pack8(oa[1][0], oa[1][1]), rs, l * 16 + 1024, oo);
      STAMPAT(4);
      DRAIN();
      BAR();
      if (tid == 0) st8(T, rs, h * SO, F_O + (it * 8 + p) * 32 * SO);
      if constexpr (it == 0) {
        DEF4(gv); DEF4(uv);
        if (ffn_on) {
          const unsigned ro = 65536 + w * 1024 + (4 * g + ((l & 15) >> 2)) * 32 + (l & 3) * 8;
#pragma unroll
          for (int i = 0; i < 4; ++i) {
            frag_t f;
            f.hf.a = trread(smem, ro + i * 16384); f.hf.b = trread(smem, ro + i * 16384 + 512);
            gv[i] = f.v;
            f.hf.a = trread(smem, ro + i * 16384 + 8192); f.hf.b = trread(smem, ro + i * 16384 + 8704);
            uv[i] = f.v;
          }
        }
        PINV4(gv); PINV4(uv);
        WO_PIECE(11); WO_PIECE(10); WO_PIECE(9); WO_PIECE(8); WO_PIECE(7); WO_PIECE(6); WO_PIECE(5); WO_PIECE(4);
      }
      WO_PIECE(3); WO_PIECE(2); WO_PIECE(1); WO_PIECE(0);
    }
    STAMPAT(5);
    IDS();
    ARGP(pwr, 4); ARGP(pwd, 7); ARGP(pout, 8); ARGP(px, 0);
    const int kh = w >> 2, rt2 = rb * 4 + (w & 3);
    const unsigned R = rt2 * 32 + kh * 16 + (l & 15);
    const unsigned to = (rt2 * 64 + ct * 4 + kh) * 1024 + l * 16;
    const unsigned rmo = ct * 64 + ((g & 1) ? 12 + 4 * g : 4 * g);
    f32x2 wv[8];
    {
      u32x4 wa[2], wrp[2], res[2];
#pragma unroll
      for (int j = 0; j < 2; ++j) {
        wrp[j] = *(const GAS u32x4*)(pwr + rmo + 32 * j);
        wa[j] = *(const GAS u32x4*)(pwr + l * 8 + j * 512);
        if constexpr (it == 0) res[j] = *(const GAS u32x4*)(px + R * 1024 + rmo + 32 * j);
      }
      {
        f32x2 t[8];
        unpack8v(wa[0], t); unpack8v(wa[1], t + 4);
        const f32x2 s = ((t[0] + t[1]) + (t[2] + t[3])) + ((t[4] + t[5]) + (t[6] + t[7]));
        wsum = sum64(hadd(s));
        unpack8v(trade16(wrp[0]), wv); unpack8v(trade16(wrp[1]), wv + 4);
        if constexpr (it == 0) { res[0] = trade16(res[0]); res[1] = trade16(res[1]); asm volatile("" : "+v"(res[0]), "+v"(res[1])); }
        asm volatile("" : "+v"(wsum), "+v"(wv[0]), "+v"(wv[1]), "+v"(wv[2]), "+v"(wv[3]), "+v"(wv[4]), "+v"(wv[5]), "+v"(wv[6]), "+v"(wv[7]));
      }
      unsigned bt[4];
#pragma unroll
      for (int n = 0; n < 4; ++n) {
        bt[n] = lbase + 32768 + 65536 + kh * 4096 + (4 * g + ((l & 15) >> 2)) * 128 + (l & 3) * 8 + ((n ^ (2 * (g & 1) + ((l & 15) >> 3))) << 5);
        asm volatile("" : "+v"(bt[n]));
      }
      if (w == 0) poll_flags(rs, (l & 31) * SO, F_O + (it * 8 + (rb >> 1)) * 32 * SO, T);
      else if (it == 0 && w < 3) poll_flags2(rs, F_XN + (((w - 1) * 4 + g) * RX + bi % RX) * 256 + (l & 15) * 16, T);
      asm volatile("s_barrier" ::: "memory");
      STAMPAT(6);
      const unsigned abase = OFF_O + it * (4u << 20) + (rt2 * 64 + 32 * kh) * 1024;
      const unsigned av0 = l * 16 + kh * 1024, av1 = l * 16 + (kh ^ 1) * 1024;
      u32x4 af[WB][2];
#define O_LOAD(s_) { af[(s_) % WB][0] = ld16(rs, av0 + (~(s_) & 1) * 2048, abase + ((15 - (s_)) >> 1) * 4096); af[(s_) % WB][1] = ld16(rs, av1 + (~(s_) & 1) * 2048, abase + ((15 - (s_)) >> 1) * 4096); }
#pragma unroll
      for (int s = 0; s < WR; ++s) O_LOAD(s);
#pragma unroll
      for (int j = 0; j < 2; ++j) {
        if constexpr (it == 1) res[j] = ld16(rs, OFF_XNEW + (2 * ct + j) * 256 + g * 16, 0);
      }
      SB();
      f32x4 ac[2][4];
#pragma unroll
      for (int a = 0; a < 2; ++a)
#pragma unroll
        for (int n = 0; n < 4; ++n) ac[a][n] = f32x4{0.f, 0.f, 0.f, 0.f};
      u32x4 bfr[2][4];
#define WO_RD(buf, s, n)                                                                             \
  {                                                                                                  \
    const unsigned o_ = bt[n] + ((15 - (s)) & 7) * 8192;                                             \
    frag_t f_;                                                                                       \
    f_.hf.a = trabs(o_); f_.hf.b = trabs(o_ + 2048);                                                 \
    bfr[buf][n] = f_.v;                                                                              \
  }
#pragma unroll
      for (int n = 0; n < 4; ++n) WO_RD(0, 0, n);
#pragma unroll
      for (int s = 0; s < 16; ++s) {
        const int cur = s & 1;
        asm volatile("" : "+{v[176:179]}"(ac[0][0]), "+{v[180:183]}"(ac[0][1]), "+{v[184:187]}"(ac[0][2]), "+{v[188:191]}"(ac[0][3]),
                          "+{v[192:195]}"(ac[1][0]), "+{v[196:199]}"(ac[1][1]), "+{v[200:203]}"(ac[1][2]), "+{v[204:207]}"(ac[1][3]));
        if (s == (it == 0 ? 3 : 11)) asm volatile("s_barrier" ::: "memory");
        if (s == 7) {
#pragma unroll
          for (int n = 0; n < 4; ++n) { bt[n] -= 65536; asm volatile("" : "+v"(bt[n])); }
        }
        if (it == 0 && s == 8) {
#pragma unroll
          for (int i = 0; i < 3; ++i) {
            if (PO()) st16(keep[3 + i], rs, l * 16 + i * 1024, OFF_WI + (h * 8 + ((ROT0 + 6) & 7)) * STAGE + w * 3072);
            else st16(keep[i], rs, l * 16 + i * 1024, OFF_WI + (h * 8 + ((ROT0 + 5) & 7)) * STAGE + w * 3072);
          }
        }
        if (it == 0 && s == 10) {
          unsigned pz1 = POISON;
          asm volatile("" : "+v"(pz1));
          const u32x4 pz = {pz1, pz1, pz1, pz1};
          st16(pz, rs, OFF_PART + (tid >> 8) * (1u << 20) + bid * 4096 + (tid & 255) * 16, 0);
          if (w < 2) st16(pz, rs, OFF_STAT + 524288 + (rb * 16 + ct) * 2048 + tid * 16, 0);
          if (w == 4 && l < 40) st16(pz, rs, l < 16 ? OFF_CA + 65536 + rb * 4096 + ct * 256 + l * 16 : OFF_FX + (h * 8 + p) * 512 + (l - 16) * 16, 0);
          if (w == 6 && l < (ct < (int)RKEY ? 3 : 2)) st4(pz1, rs, l < 2 ? OFF_XNEW + (bid >> 3) * 256 + (bid & 7) * 8 + l * 4 : KEY_AT(1, ct) + rb * SKEY, 0);
        }
        if (s < WB - WR) O_LOAD(WR + 2 * s < 16 ? WR + 2 * s : 0);
        SB();
#pragma unroll
        for (int n = 0; n < 4; ++n) {
          ac[0][n] = mfma16(bfr[cur][n], af[s % WB][0], ac[0][n]);
          ac[1][n] = mfma16(bfr[cur][n], af[s % WB][1], ac[1][n]);
          if (s + 1 < 16) WO_RD(cur ^ 1, s + 1, n);
          if (s == 9) {
            if (it == 1 && n == 0) { res[0] = R == sel0 ? res[0] : x2p[0]; res[1] = R == sel0 ? res[1] : x2p[1]; }
            v[2 * n] = f32x2{lo16(res[n >> 1][2 * (n & 1)]), hi16(res[n >> 1][2 * (n & 1)])};
            v[2 * n + 1] = f32x2{lo16(res[n >> 1][2 * (n & 1) + 1]), hi16(res[n >> 1][2 * (n & 1) + 1])};
            asm volatile("" : "+v"(v[2 * n]), "+v"(v[2 * n + 1]));
          }
          SB();
        }
        if (s < WB - WR) { O_LOAD(WR + 2 * s + 1); }
        else if (s + WB < 16) { O_LOAD(s + WB); }
        SB();
      }
#undef WO_RD
#undef O_LOAD
      STAMPAT(7);
#define XCH_AT(v) ((v) == 7 ? 155648 : (v) * 4096)
#pragma unroll
      for (int n = 0; n < 4; ++n) *(f32x4*)(smem + XCH_AT(w) + n * 1024 + l * 16) = ac[1][n];
#pragma unroll
      for (int n = 0; n < 4; ++n) { v[2 * n] += f32x2{ac[0][n][0], ac[0][n][1]}; v[2 * n + 1] += f32x2{ac[0][n][2], ac[0][n][3]}; }
      BAR();
#pragma unroll
      for (int n = 0; n < 4; ++n) {
        const f32x4 o = *(const f32x4*)(smem + XCH_AT(w ^ 4) + n * 1024 + l * 16);
        v[2 * n] += f32x2{o[0], o[1]}; v[2 * n + 1] += f32x2{o[2], o[3]};
      }
    }
    unsigned crow = 0;
    {
      if (it == 0) { st16(pack8v(v), rs, to, OFF_XN + (4u << 20)); st16(pack8v(v + 4), rs, to + 2048, OFF_XN + (4u << 20)); }
      SB();
      const f32x2 s = ((v[0] + v[1]) + (v[2] + v[3])) + ((v[4] + v[5]) + (v[6] + v[7]));
      f32x2 dp = v[0] * wv[0], q = v[0] * v[0];
#pragma unroll
      for (int j = 1; j < 8; ++j) { dp = __builtin_elementwise_fma(v[j], wv[j], dp); q = __builtin_elementwise_fma(v[j], v[j], q); }
      float mt = hadd(s), qq = hadd(q), dot = hadd(dp);
      add_g3(mt, qq, dot);
      const unsigned so = OFF_STAT + it * 524288 + rb * 32768, lrow = R & 127;
      if (it == 0) wait_vm<2>();
      if (g == 0) st16(unpoison4(__builtin_bit_cast(u32x4, f32x4{mt, qq, dot, 0.f})), rs, ct * 2048 + lrow * 16, so);
      SB();
      if (it == 0) wait_vm<1>();
      if constexpr (it == 0) {
        volatile LDS_AS u32x2* in = (volatile LDS_AS u32x2*)(smem + L_XC);
        if (l == 0) in[w] = T;
        const u32x2 t = in[l & 7];
        if (__all(t[0] == T[0] && t[1] == T[1]) && l < (int)RX1) st8(T, rs, (l * 32 + (rb & 1) * 16 + ct) * SX1, F_X1 + (rb >> 1) * RX1 * 32 * SX1);
      }
      STAMPAT(8);
      if (it == 0) {
        if (ffn_on) { WD_DMA(); }
#pragma unroll
        for (int j = 0; j < 9; ++j) *(u32x4*)(smem + L_RING + (j / 3) * STAGE + (w * 3 + j % 3) * 1024 + l * 16) = keep[j];
      }
      STAMPAT(16);
      if constexpr (it == 0) {
        if (w == 0) poll_flags(rs, ((l & 2) * 8 + ((ROT0 + 5) & 7) * 2 + (l & 1)) * SX1, F_X1 + (p * RX1 + (xcd >> 1) % RX1) * 32 * SX1, T);
        if (tid == 192) *(LDS_AS u32x4*)(smem + L_QC) = u32x4{0u, 0u, 0u, 0u};
        BAR();
        PRE_LOAD(1, ROT0 + 5, 0, 4);
        STAMPAT(9);
      } else {
        __builtin_amdgcn_s_sleep(SDIR);
        u32x4 sp[4];
#pragma unroll
        for (int i = 0; i < 4; ++i) sp[i] = ld16(rs, (4 * g + i) * 2048 + lrow * 16, so);
        const RowFin r = fin_key(rs, T, g * 8192 + lrow * 16, so, smem + L_XC, l, w, R, wsum, sp);
        STAMPAT(9);
        BAR();
        crow = fin_cand(rs, smem + L_XC, 1, g, rb, ct, r, v);
        STAMPAT(10);
        fin_x2(r, v);
      }
    }
    STAMPAT(11);
    if (it == 1) {
      u32x4 op[2];
      {
        char* tp = smem + L_OUT + w * 2048;
        const unsigned c0 = (g & 1) * 2 + (g >> 1), r = l & 15;
        *(u32x4*)(tp + r * 128 + ((c0 ^ (r & 7)) << 4)) = trade16(pack8v(v));
        *(u32x4*)(tp + r * 128 + (((c0 + 4) ^ (r & 7)) << 4)) = trade16(pack8v(v + 4));
#pragma unroll
        for (int j = 0; j < 2; ++j) op[j] = *(const u32x4*)(tp + j * 1024 + (l >> 3) * 128 + (((l ^ (l >> 3)) & 7) << 4));
      }
      const unsigned Ro = R - (l & 15) + (l >> 3), oo = (Ro * 1024 + ct * 64) * 2 + (l & 7) * 16;
#define OUT_STORE(c0, c1)                                                                           \
  {                                                                                                 \
    if (c0) __builtin_amdgcn_raw_buffer_store_b128(op[0], rout, oo, 0, SC1);                        \
    if (c1) __builtin_amdgcn_raw_buffer_store_b128(op[1], rout, oo + 16384, 0, SC1);                \
  }
      if (OUTE) OUT_STORE(Ro != crow, Ro + 8 != crow);
      if (w == 0) {
        __builtin_amdgcn_s_sleep(GSL);
        u32x4 xr[4];
        unsigned cur = 0, best;
        asm volatile("" : "=v"(xr[0]), "=v"(xr[1]));
        for (unsigned spins = 0;;) {
          const unsigned k_ = key_load(rs, l, 1, KREP());
          const bool all = __all(k_ != POISON);
          best = __builtin_amdgcn_readfirstlane(umax16(k_ == POISON ? 0u : k_));
          if (best != cur && ffn_on) {
            cur = best;
            ld16h<0>(xr[0], rs, (l >> 3) * 256 + (l & 7) * 16, cand_off(cur, 1)); ld16h<2048>(xr[1], rs, (l >> 3) * 256 + (l & 7) * 16, cand_off(cur, 1));
          }
          if (all || spin_fail(spins, rs, T)) break;
        }
        STAMPAT(17);
        if (l == 0) *(LDS_AS unsigned*)(smem + L_SKEY + 32) = best;
        asm volatile("s_waitcnt vmcnt(0)" : "+v"(xr[0]), "+v"(xr[1])::"memory");
        if (ffn_on) {
          word_wait<2>(rs, T, (l >> 3) * 256 + (l & 7) * 16, cand_off(best, 1), xr);
          *(LDS_AS u32x4*)(smem + L_ROW + l * 16) = xr[0]; *(LDS_AS u32x4*)(smem + L_ROW + 1024 + l * 16) = xr[1];
        }
      }
      BAR();
      const unsigned key = __builtin_amdgcn_readfirstlane(*(LDS_AS unsigned*)(smem + L_SKEY + 32)), krow = key_row(key);
      STAMPAT(12);
      if (ffn_on) {
        u32x4 xv[4];
#pragma unroll
        for (int i = 0; i < 4; ++i) xv[i] = *(const LDS_AS u32x4*)(smem + L_ROW + w * 256 + i * 64 + g * 16);
        f32x4 ga, ua;
        ffn_gu(gv, uv, xv, ga, ua);
        ffn_gu_store(smem, l, w, ga, ua);
        BAR();
        ffn_d_store(rs, l, w, 1, slice, ffn_d_all(dfr, ffn_d_h(ffn_d_read(smem, l))));
        STAMPAT(13);
        SB();
        if (OSL) __builtin_amdgcn_s_sleep(OSL);
      }
      if (OUTE) OUT_STORE(Ro == crow && Ro != krow, Ro + 8 == crow && Ro + 8 != krow)
      else OUT_STORE(Ro != krow, Ro + 8 != krow);
      if (!ffn_on) {
        const unsigned G = xcd * 4 + bi - 28;
        if (G == 0 && tid == 0) st8(T, rs, F_EP, 0);
        u32x4 pv[4];
        u32x2 cxn, cx1;
        __builtin_amdgcn_s_sleep(RSL);
        red_load(rs, tid, 1, G, key, pv, cxn, cx1);
        red_sum(rs, smem, T, tid, w, 1, G, key, pv, cxn, cx1);
        STAMPAT(14);
        BAR();
        if (w == 0) {
          const u32x2 ov = red_final(smem, l, key, cxn, cx1);
          if (l < 8) __builtin_amdgcn_raw_buffer_store_b64(ov, rout, (krow * 1024 + G * 32 + l * 4) * 2, 0, 0);
        }
      }
#undef OUT_STORE
    }
    STAMPAT(15); STAMPAT(23);
  };
  layer(std::integral_constant<int, 0>{});
  layer(std::integral_constant<int, 1>{});
}

extern "C" void kernel_launch(void* const* d_in, const int* in_sizes, int n_in, void* d_out, int out_size,
                              void* d_ws, size_t ws_size, hipStream_t stream) {
  (void)hipFuncSetAttribute((const void*)mega_kernel, hipFuncAttributeMaxDynamicSharedMemorySize, LDS_BYTES);
  mega_kernel<<<256, 512, LDS_BYTES, stream>>>((const bf16*)d_in[0], (char*)d_ws, (const bf16*)d_in[1], (const bf16*)d_in[2], (const bf16*)d_in[4],
                                               (const bf16*)d_in[5], (const bf16*)d_in[6], (const bf16*)d_in[7], (bf16*)d_out);
}
```

```cpp
#include <hip/hip_runtime.h>
#include <stdint.h>
#include <utility>

typedef __bf16 bf16;
typedef __bf16 bf16x8 __attribute__((ext_vector_type(8)));
typedef __bf16 bf16x2 __attribute__((ext_vector_type(2)));
typedef float f32x4 __attribute__((ext_vector_type(4)));
typedef float f32x2 __attribute__((ext_vector_type(2)));
typedef short s16x4 __attribute__((ext_vector_type(4)));
typedef unsigned int u32x4 __attribute__((ext_vector_type(4)));
typedef unsigned int u32x2 __attribute__((ext_vector_type(2)));
#define LDS_AS __attribute__((address_space(3)))
#define GAS __attribute__((address_space(1)))
typedef GAS unsigned gu32;
typedef GAS unsigned long long gu64;
#define RLX_AGENT __ATOMIC_RELAXED, __HIP_MEMORY_SCOPE_AGENT
typedef __amdgpu_buffer_rsrc_t rsrc_t;

constexpr int F_ = 3584;
constexpr int SC1 = 16;

constexpr int RX = 4;
constexpr unsigned F_EP = 0, F_TMO = 128;
constexpr unsigned F_XN = 4096;
#define FLG 256, 1024, 8, 64, 4, 256, 4
constexpr unsigned FLV[7] = {FLG};
constexpr unsigned SO = FLV[0], SKV = FLV[1], SKW = FLV[2], SX1 = FLV[3], RX1 = FLV[4], SKEY = FLV[5], RKEY = FLV[6];
constexpr unsigned F_O = 8u << 20;
constexpr unsigned F_KV = 11u << 20;
constexpr unsigned F_X1 = 9u << 20;
constexpr unsigned OFF_KEY = 10u << 20;
#define KREP() (RKEY == 16 ? bid & 15 : (bid & 7) * RKEY / 8)
#define KEY_AT(L, rep) (OFF_KEY + ((L) * RKEY + (rep)) * (SKEY < 8 ? 128 : 16 * SKEY))
constexpr unsigned OFF_AB = 2u << 20;
constexpr unsigned OFF_XNEW = 262144;
constexpr unsigned OFF_CS = OFF_AB + 65536;
constexpr unsigned OFF_FX = 393216;
constexpr unsigned OFF_CA = 524288;
constexpr unsigned OFF_STAT = 1u << 20;
constexpr unsigned OFF_PART = 3u << 20;
constexpr unsigned OFF_STAMP = 6u << 20;
constexpr unsigned OFF_XN = 8u << 20;
constexpr unsigned OFF_O = 16u << 20;
constexpr unsigned OFF_KV = 32u << 20;
constexpr unsigned OFF_WI = 52u << 20;
constexpr unsigned WS_BYTES = 58u << 20;
constexpr unsigned POISON = 0xFFFFFFFFu;

constexpr int L_ABW = 16640, L_CSP = 17408, L_CSL = 31872, L_FXL = 32256, L_K = 0, L_V = 32768, L_RING = 65536, L_XCH = 0, L_DN = 32768, L_RDY = 31744, L_QC = 31808, LDS_BYTES = 163840;
constexpr int STAGE = 24576;
#define SLOT(t) ((t) & 3)

template <typename F, int... I> __device__ __forceinline__ void static_for_impl(F&& f, std::integer_sequence<int, I...>) {
  (f(std::integral_constant<int, I>{}), ...);
}
template <int N, typename F> __device__ __forceinline__ void static_for(F&& f) { static_for_impl(f, std::make_integer_sequence<int, N>{}); }
__device__ __forceinline__ unsigned pack2(float a, float b) {
  f32x2 v = {a, b};
  return __builtin_bit_cast(unsigned, __builtin_convertvector(v, bf16x2));
}
__device__ __forceinline__ float asf(unsigned u) { return __builtin_bit_cast(float, u); }
__device__ __forceinline__ float lo16(unsigned u) { return __builtin_bit_cast(float, u << 16); }
__device__ __forceinline__ float hi16(unsigned u) { return __builtin_bit_cast(float, u & 0xffff0000u); }
__device__ __forceinline__ u32x4 pack8(const f32x4 a, const f32x4 b) {
  u32x4 o;
  o[0] = pack2(a[0], a[1]); o[1] = pack2(a[2], a[3]); o[2] = pack2(b[0], b[1]); o[3] = pack2(b[2], b[3]);
  return o;
}
__device__ __forceinline__ f32x4 mfma16(u32x4 a, u32x4 b, f32x4 c) {
  return __builtin_amdgcn_mfma_f32_16x16x32_bf16(__builtin_bit_cast(bf16x8, a), __builtin_bit_cast(bf16x8, b), c, 0, 0, 0);
}
__device__ __forceinline__ f32x4 mfma16k16(s16x4 a, s16x4 b, f32x4 c) { return __builtin_amdgcn_mfma_f32_16x16x16bf16_1k(a, b, c, 0, 0, 0); }
template <int CTRL> __device__ __forceinline__ float dppf(float v) {
  return __builtin_bit_cast(float, __builtin_amdgcn_update_dpp(0, __builtin_bit_cast(int, v), CTRL, 0xf, 0xf, false));
}
template <int CTRL> __device__ __forceinline__ unsigned dppu(unsigned v) {
  return (unsigned)__builtin_amdgcn_update_dpp(0, (int)v, CTRL, 0xf, 0xf, false);
}
__device__ __forceinline__ float add_x16(float v) {
  auto r = __builtin_amdgcn_permlane16_swap(__builtin_bit_cast(unsigned, v), __builtin_bit_cast(unsigned, v), false, false);
  return __builtin_bit_cast(float, (unsigned)r[0]) + __builtin_bit_cast(float, (unsigned)r[1]);
}
__device__ __forceinline__ float add_x32(float v) {
  auto r = __builtin_amdgcn_permlane32_swap(__builtin_bit_cast(unsigned, v), __builtin_bit_cast(unsigned, v), false, false);
  return __builtin_bit_cast(float, (unsigned)r[0]) + __builtin_bit_cast(float, (unsigned)r[1]);
}
__device__ __forceinline__ float max_x16(float v) {
  auto r = __builtin_amdgcn_permlane16_swap(__builtin_bit_cast(unsigned, v), __builtin_bit_cast(unsigned, v), false, false);
  return fmaxf(__builtin_bit_cast(float, (unsigned)r[0]), __builtin_bit_cast(float, (unsigned)r[1]));
}
__device__ __forceinline__ float max_x32(float v) {
  auto r = __builtin_amdgcn_permlane32_swap(__builtin_bit_cast(unsigned, v), __builtin_bit_cast(unsigned, v), false, false);
  return fmaxf(__builtin_bit_cast(float, (unsigned)r[0]), __builtin_bit_cast(float, (unsigned)r[1]));
}
__device__ __forceinline__ unsigned umax_x16(unsigned v) {
  auto r = __builtin_amdgcn_permlane16_swap(v, v, false, false);
  return max((unsigned)r[0], (unsigned)r[1]);
}
__device__ __forceinline__ unsigned umax_x32(unsigned v) {
  auto r = __builtin_amdgcn_permlane32_swap(v, v, false, false);
  return max((unsigned)r[0], (unsigned)r[1]);
}
__device__ __forceinline__ float sum_hi8(float v) {
  v += dppf<0x128>(v);
  v = add_x16(v);
  return add_x32(v);
}
__device__ __forceinline__ float sum64(float v) {
  v += dppf<0xB1>(v);
  v += dppf<0x4E>(v);
  v += dppf<0x124>(v);
  v += dppf<0x128>(v);
  v = add_x16(v);
  return add_x32(v);
}

#define BAR() asm volatile("s_waitcnt lgkmcnt(0)\n\ts_barrier" ::: "memory")
#define DRAIN() asm volatile("s_waitcnt vmcnt(0)" ::: "memory")
#define SB() __builtin_amdgcn_sched_barrier(0)
template <int N> __device__ __forceinline__ void wait_vm() { asm volatile("s_waitcnt vmcnt(%0)" ::"n"(N) : "memory"); }

__device__ __forceinline__ void dma16(rsrc_t rs, unsigned voff, unsigned soff, unsigned lds) {
  asm volatile("s_mov_b32 m0, %0\n\ts_nop 0\n\tbuffer_load_dwordx4 %1, %2, %3 offen lds" ::"s"(lds), "v"(voff), "s"(rs), "s"(soff) : "memory");
}
#define NTM 2
#define OAUX 2
template <int NT> __device__ __forceinline__ void dma16n(rsrc_t rs, unsigned voff, unsigned soff, unsigned lds) {
  if constexpr (NT) asm volatile("s_mov_b32 m0, %0\n\ts_nop 0\n\tbuffer_load_dwordx4 %1, %2, %3 offen nt lds" ::"s"(lds), "v"(voff), "s"(rs), "s"(soff) : "memory");
  else dma16(rs, voff, soff, lds);
}
template <int OFF> __device__ __forceinline__ void dma16s(rsrc_t rs, unsigned voff, unsigned soff, unsigned lds) {
  asm volatile("s_mov_b32 m0, %0\n\ts_nop 0\n\tbuffer_load_dwordx4 %1, %2, %3 offen offset:%4 sc1 lds" ::"s"(lds), "v"(voff), "s"(rs), "s"(soff), "n"(OFF) : "memory");
}
__device__ __forceinline__ float rbf1(float x) { return __builtin_bit_cast(float, pack2(0.f, x)); }
__device__ __forceinline__ void vswap(unsigned& a, unsigned& b) { asm volatile("v_swap_b32 %0, %1" : "+v"(a), "+v"(b)); }
__device__ __forceinline__ void vswap(float& a, float& b) { asm volatile("v_swap_b32 %0, %1" : "+v"(a), "+v"(b)); }
template <typename V, int N> __device__ __forceinline__ void vswapn(V& a, V& b) {
#pragma unroll
  for (int i = 0; i < N; ++i) { auto x = a[i], y = b[i]; vswap(x, y); a[i] = x; b[i] = y; }
}
__device__ __forceinline__ u32x4 ld16(rsrc_t rs, unsigned voff, unsigned soff) { return __builtin_amdgcn_raw_buffer_load_b128(rs, voff, soff, SC1); }
template <int OFF> __device__ __forceinline__ void ld16h(u32x4& d, rsrc_t rs, unsigned voff, unsigned soff) {
  asm volatile("buffer_load_dwordx4 %0, %1, %2, %3 offen offset:%4 sc1" : "=v"(d) : "v"(voff), "s"(rs), "s"(soff), "n"(OFF) : "memory");
}
template <int I, int OFF> __device__ __forceinline__ void ld16w(u32x4& d, rsrc_t rs, unsigned voff, unsigned soff) {
  if constexpr (I == 0) asm volatile("buffer_load_dwordx4 %0, %1, %2, %3 offen offset:%4 sc1" : "={v[128:131]}"(d) : "v"(voff), "s"(rs), "s"(soff), "n"(OFF) : "memory");
  else if constexpr (I == 1) asm volatile("buffer_load_dwordx4 %0, %1, %2, %3 offen offset:%4 sc1" : "={v[132:135]}"(d) : "v"(voff), "s"(rs), "s"(soff), "n"(OFF) : "memory");
  else if constexpr (I == 2) asm volatile("buffer_load_dwordx4 %0, %1, %2, %3 offen offset:%4 sc1" : "={v[136:139]}"(d) : "v"(voff), "s"(rs), "s"(soff), "n"(OFF) : "memory");
  else if constexpr (I == 3) asm volatile("buffer_load_dwordx4 %0, %1, %2, %3 offen offset:%4 sc1" : "={v[140:143]}"(d) : "v"(voff), "s"(rs), "s"(soff), "n"(OFF) : "memory");
  else if constexpr (I == 4) asm volatile("buffer_load_dwordx4 %0, %1, %2, %3 offen offset:%4 sc1" : "={v[144:147]}"(d) : "v"(voff), "s"(rs), "s"(soff), "n"(OFF) : "memory");
  else if constexpr (I == 5) asm volatile("buffer_load_dwordx4 %0, %1, %2, %3 offen offset:%4 sc1" : "={v[148:151]}"(d) : "v"(voff), "s"(rs), "s"(soff), "n"(OFF) : "memory");
  else if constexpr (I == 6) asm volatile("buffer_load_dwordx4 %0, %1, %2, %3 offen offset:%4 sc1" : "={v[152:155]}"(d) : "v"(voff), "s"(rs), "s"(soff), "n"(OFF) : "memory");
  else if constexpr (I == 7) asm volatile("buffer_load_dwordx4 %0, %1, %2, %3 offen offset:%4 sc1" : "={v[156:159]}"(d) : "v"(voff), "s"(rs), "s"(soff), "n"(OFF) : "memory");
  else if constexpr (I == 8) asm volatile("buffer_load_dwordx4 %0, %1, %2, %3 offen offset:%4 sc1" : "={v[160:163]}"(d) : "v"(voff), "s"(rs), "s"(soff), "n"(OFF) : "memory");
  else if constexpr (I == 9) asm volatile("buffer_load_dwordx4 %0, %1, %2, %3 offen offset:%4 sc1" : "={v[164:167]}"(d) : "v"(voff), "s"(rs), "s"(soff), "n"(OFF) : "memory");
  else if constexpr (I == 10) asm volatile("buffer_load_dwordx4 %0, %1, %2, %3 offen offset:%4 sc1" : "={v[168:171]}"(d) : "v"(voff), "s"(rs), "s"(soff), "n"(OFF) : "memory");
  else if constexpr (I == 11) asm volatile("buffer_load_dwordx4 %0, %1, %2, %3 offen offset:%4 sc1" : "={v[172:175]}"(d) : "v"(voff), "s"(rs), "s"(soff), "n"(OFF) : "memory");
}
template <int K> __device__ __forceinline__ void pin_w(u32x4& a, u32x4& b) {
  if constexpr (K == 0) asm volatile("" : "+{v[128:131]}"(a), "+{v[132:135]}"(b)::"memory");
  else if constexpr (K == 1) asm volatile("" : "+{v[136:139]}"(a), "+{v[140:143]}"(b)::"memory");
  else if constexpr (K == 2) asm volatile("" : "+{v[144:147]}"(a), "+{v[148:151]}"(b)::"memory");
  else if constexpr (K == 3) asm volatile("" : "+{v[152:155]}"(a), "+{v[156:159]}"(b)::"memory");
  else if constexpr (K == 4) asm volatile("" : "+{v[160:163]}"(a), "+{v[164:167]}"(b)::"memory");
  else if constexpr (K == 5) asm volatile("" : "+{v[168:171]}"(a), "+{v[172:175]}"(b)::"memory");
}
__device__ __forceinline__ void ld8h(u32x2& d, rsrc_t rs, unsigned voff, unsigned soff) {
  asm volatile("buffer_load_dwordx2 %0, %1, %2, %3 offen sc1" : "=v"(d) : "v"(voff), "s"(rs), "s"(soff) : "memory");
}
template <int OFF> __device__ __forceinline__ void ld4h(unsigned& d, rsrc_t rs, unsigned voff, unsigned soff) {
  asm volatile("buffer_load_dword %0, %1, %2, %3 offen offset:%4 sc1" : "=v"(d) : "v"(voff), "s"(rs), "s"(soff), "n"(OFF) : "memory");
}
__device__ __forceinline__ u32x2 ld8(rsrc_t rs, unsigned voff, unsigned soff) { return __builtin_amdgcn_raw_buffer_load_b64(rs, voff, soff, SC1); }
__device__ __forceinline__ void st16(u32x4 v, rsrc_t rs, unsigned voff, unsigned soff) { __builtin_amdgcn_raw_buffer_store_b128(v, rs, voff + soff, 0, SC1); }
__device__ __forceinline__ void st8(u32x2 v, rsrc_t rs, unsigned voff, unsigned soff) { __builtin_amdgcn_raw_buffer_store_b64(v, rs, voff, soff, SC1); }

__device__ __forceinline__ unsigned ld4(rsrc_t rs, unsigned voff, unsigned soff) { return __builtin_amdgcn_raw_buffer_load_b32(rs, voff, soff, SC1); }
__device__ __forceinline__ void st4(unsigned v, rsrc_t rs, unsigned voff, unsigned soff) { __builtin_amdgcn_raw_buffer_store_b32(v, rs, voff, soff, SC1); }

constexpr unsigned kSpinCap = 1u << 16;
__device__ __forceinline__ bool spin_fail(unsigned& spins, rsrc_t rs, u32x2 T) {
  asm volatile("" ::: "memory");
  ++spins;
  if (spins > kSpinCap) { st8(T, rs, 0, F_TMO); return true; }
  if ((spins & 255u) == 0u) {
    const u32x2 v = ld8(rs, 0, F_TMO);
    if (v[0] == T[0] && v[1] == T[1]) return true;
  }
  __builtin_amdgcn_s_sleep(3);
  return false;
}
__device__ __forceinline__ void poll_flags2(rsrc_t rs, unsigned voff, u32x2 T) {
  for (unsigned spins = 0;;) {
    const u32x4 v = ld16(rs, voff, 0);
    if (__all(v[0] == T[0] && v[1] == T[1] && v[2] == T[0] && v[3] == T[1])) break;
    if (spin_fail(spins, rs, T)) break;
  }
  asm volatile("" ::: "memory");
  __builtin_amdgcn_fence(__ATOMIC_ACQUIRE, "wavefront");
}
__device__ __forceinline__ void tag_wait(rsrc_t rs, u32x2 T, unsigned voff, unsigned soff, u32x4& v) {
  if (__builtin_expect(!__all(v[2] == T[0] && v[3] == T[1]), 0)) {
    unsigned spins = 0;
    do {
      if (spin_fail(spins, rs, T)) break;
      v = ld16(rs, voff, soff);
    } while (!__all(v[2] == T[0] && v[3] == T[1]));
    asm volatile("s_waitcnt vmcnt(0)" : "+v"(v)::"memory");
  }
}
template <int N> __device__ __forceinline__ void word_wait(rsrc_t rs, u32x2 T, unsigned voff, unsigned soff, u32x4 (&v)[4]) {
#define WW_OK() __all(max(max(max(v[0][0], v[0][1]), max(v[0][2], v[0][3])), max(max(v[N - 1][0], v[N - 1][1]), max(v[N - 1][2], v[N - 1][3]))) != POISON)
  if (__builtin_expect(!WW_OK(), 0)) {
    unsigned spins = 0;
    do {
      if (spin_fail(spins, rs, T)) break;
#pragma unroll
      for (int i = 0; i < N; ++i) v[i] = ld16(rs, voff + i * 2048, soff);
    } while (!WW_OK());
    asm volatile("s_waitcnt vmcnt(0)" : "+v"(v[0]), "+v"(v[N - 1])::"memory");
  }
#undef WW_OK
}
__device__ __forceinline__ void poll_flags(rsrc_t rs, unsigned voff, unsigned soff, u32x2 T) {
  for (unsigned spins = 0;;) {
    const u32x2 v = ld8(rs, voff, soff);
    if (__all(v[0] == T[0] && v[1] == T[1])) break;
    if (spin_fail(spins, rs, T)) break;
  }
  asm volatile("" ::: "memory");
  __builtin_amdgcn_fence(__ATOMIC_ACQUIRE, "wavefront");
}

#define STAMP_ON(i) true
#define STAMP_VAL(i) (((i) == 23 || (i) == 22 || (i) == 21) ? __builtin_amdgcn_s_memtime() : __builtin_amdgcn_s_memrealtime())
#define STAMPAT(i)

#define GSTEP(s, n)
#define VSTEP(i)

__device__ __forceinline__ unsigned toff(unsigned R, unsigned ks, unsigned g) {
  return (((R >> 5) * 64 + ks * 2 + ((R >> 4) & 1)) << 10) + ((g * 16 + (R & 15)) << 4);
}
__device__ __forceinline__ void unpack8v(const u32x4 u, f32x2* v) {
#pragma unroll
  for (int j = 0; j < 4; ++j) v[j] = f32x2{lo16(u[j]), hi16(u[j])};
}
__device__ __forceinline__ u32x4 pack8v(const f32x2* v) {
  u32x4 o;
#pragma unroll
  for (int j = 0; j < 4; ++j) o[j] = pack2(v[j][0], v[j][1]);
  return o;
}
__device__ __forceinline__ f32x2 rbf1v(f32x2 v) { return f32x2{rbf1(v[0]), rbf1(v[1])}; }
__device__ __forceinline__ float ln_rd(float var) { return __builtin_amdgcn_rcpf(rbf1(__builtin_amdgcn_sqrtf(rbf1(rbf1(var) + 1e-5f)))); }
template <int N> __device__ __forceinline__ void ln_apply(float mb, float rd, const f32x2* v, f32x2* y) {
  const f32x2 mb2 = {mb, mb}, rd2 = {rd, rd};
#pragma unroll
  for (int j = 0; j < N; ++j) y[j] = rbf1v(rbf1v(v[j] - mb2) * rd2);
}
__device__ __forceinline__ float hadd(f32x2 v) {
  float a = v[0];
  asm volatile("" : "+v"(a));
  return a + v[1];
}
__device__ __forceinline__ float add_g(float v) { return add_x32(add_x16(v)); }
__device__ __forceinline__ void add_g3(float& x, float& y, float& z) {
  auto a = __builtin_amdgcn_permlane16_swap(__builtin_bit_cast(unsigned, x), __builtin_bit_cast(unsigned, y), false, false);
  auto b = __builtin_amdgcn_permlane16_swap(__builtin_bit_cast(unsigned, z), __builtin_bit_cast(unsigned, z), false, false);
  const float r = asf(a[0]) + asf(a[1]), t = asf(b[0]) + asf(b[1]);
  auto c = __builtin_amdgcn_permlane32_swap(__builtin_bit_cast(unsigned, r), __builtin_bit_cast(unsigned, r), false, false);
  auto d = __builtin_amdgcn_permlane32_swap(__builtin_bit_cast(unsigned, t), __builtin_bit_cast(unsigned, t), false, false);
  const float u = asf(c[0]) + asf(c[1]);
  z = asf(d[0]) + asf(d[1]);
  auto e = __builtin_amdgcn_permlane16_swap(__builtin_bit_cast(unsigned, u), __builtin_bit_cast(unsigned, u), false, false);
  x = asf(e[0]); y = asf(e[1]);
}
__device__ __forceinline__ u32x4 trade16(u32x4 v) {
  auto a = __builtin_amdgcn_permlane16_swap(v[0], v[2], false, false);
  auto b = __builtin_amdgcn_permlane16_swap(v[1], v[3], false, false);
  return u32x4{(unsigned)a[0], (unsigned)b[0], (unsigned)a[1], (unsigned)b[1]};
}
__device__ __forceinline__ unsigned umax4(u32x4 v) { return max(max(v[0], v[1]), max(v[2], v[3])); }
__device__ __forceinline__ u32x2 unpoison2(u32x2 v) { return u32x2{min(v[0], POISON - 1), min(v[1], POISON - 1)}; }
__device__ __forceinline__ u32x4 unpoison4(u32x4 v) {
  return u32x4{min(v[0], POISON - 1), min(v[1], POISON - 1), min(v[2], POISON - 1), min(v[3], POISON - 1)};
}

typedef union { struct { s16x4 a, b; } hf; u32x4 v; } frag_t;
__device__ __forceinline__ s16x4 trread(const char* smem, unsigned off) {
  return __builtin_amdgcn_ds_read_tr16_b64_v4i16((LDS_AS s16x4*)(smem + off));
}
__device__ __forceinline__ s16x4 trabs(unsigned addr) { return __builtin_amdgcn_ds_read_tr16_b64_v4i16((LDS_AS s16x4*)(size_t)addr); }

constexpr int WA = 6;
#define WOP 4, 3, 6, 8, 1, 4, 0
constexpr int WOV[7] = {WOP};
constexpr int WLA = WOV[4], WPR = WOV[5], QPR = WOV[6];
constexpr int WB = WOV[0], WR = WOV[1], WIS = WOV[2], WPS = WOV[3];
#define CH 0, 7, 9, 10, 16, 19, 24, 26, 30, 31, 5, 2
constexpr int CHV[12] = {CH};
constexpr int FKL = CHV[10], FSN = CHV[11];
constexpr int SPL = CHV[0];
constexpr int FSL = CHV[1], FS0 = CHV[2], FS0D = CHV[3], FS1 = CHV[4], FS1S = CHV[5];
constexpr int FXA = CHV[6], FXB = CHV[7], FXL = CHV[8], FXS = CHV[9];
#define ROTM 7
#define ROT0 (p & ROTM)
#define PO() (p & 7 & ~ROTM)
#define L0S 22, 25, 8, 0, 24, 28, 1, 2, 4, 14
constexpr int L0V[10] = {L0S};
constexpr int CSL = L0V[0], CSS = L0V[1], CSD = L0V[2];
constexpr int abl(int it) { return it ? 25 : L0V[4]; }
constexpr int abs_(int it) { return it ? 27 : L0V[5]; }
constexpr int W2S = L0V[6], WSH = L0V[7];
static_assert(ROTM == 7 || (CSD & 3) == 3, "two tiles must be there");
#define HLPW 0
#define LNP 0
constexpr int PRE0 = 12 + (LNP ? 3 : 0);
constexpr int LNL = L0V[3], LNS = L0V[8], POI = L0V[9];
#define SYNCB 3
#define SPINS 0
#define KVJ 1
#define KVD 2
#define PC2 1
#define PCE 1
#define TCH 40, 6, 0, 26, 0xFF, 0, 1
constexpr int TCV[7] = {TCH};
constexpr int TS0 = TCV[0], TSN = TCV[1], TM0 = TCV[2], TEX = TCV[3], TM1 = TCV[4], TSD = TCV[5], TSG = TCV[6];
constexpr int QL = 26;
#define SLP 18, 4, 28, 3, 0, 0
constexpr int SLV[6] = {SLP};
constexpr int OUTE = SLV[4], OSL = SLV[5];
constexpr int SDIR = SLV[0], GSL = SLV[1], RSL = SLV[2], KSL = SLV[3];
constexpr int tp0(int it) { return it ? 3 : 3 + WSH; }
constexpr bool tile_piece(int it, int s) { return s >= tp0(it) && ((s - tp0(it)) >> 2) + 3 < 8 && ((s - tp0(it)) & 3) < 3; }
#define NPRE 3
#define QAD 4
constexpr int a_req(int s, int n) {
  int r = 2 * NPRE;
  for (int u = 1; u < 192; u += 2) {
    const bool go = r < 64 && (r >> 1) <= u / 6 + QAD && (r < 8 || u >= 9);
    if (u == 6 * s + n) return go ? r : -1;
    r += go;
  }
  return -1;
}
constexpr int vm_n(int it, int s, int n) {
  int c = it ? (a_req(s, n) >= 0) + (s == 0 && n == 0) : (s + 5 < 32 && (n == 2 || n == 3));
  if (n == 0) c += tile_piece(it, s);
  if (n == 4) c += (s == abl(it));
  if (!it) c += (s >= W2S && s < W2S + 3 && n == 4) + (LNP == 0 && s == LNL && n == 5 ? 3 : 0) + (s == 0 && n < 2 ? 2 : 0) + (s == LNS + 2 && n == 5) + (s == TEX && n == 5);
  else c += (s == 2 && n == 2) + (s == (SPL < 0 ? 0 : SPL) && n == (SPL < 0 ? 1 : 4) ? 4 : 0) + (s >= 27 && s < 30 && (n == 0 || n == 3));
  return c;
}
constexpr int vm_at(int it, int s, int n) {
  int c = it ? 2 * NPRE : PRE0;
  for (int u = 0; u < 6 * s + n; ++u) c += vm_n(it, u / 6, u % 6);
  return c;
}
constexpr int vm_a(int it, int k) {
  if (k < NPRE) return 2 * k + 2;
  for (int u = 0; u < 192; ++u)
    if (a_req(u / 6, u % 6) == 2 * k + 1) return vm_at(it, (u + 1) / 6, (u + 1) % 6);
  return 0;
}
constexpr int vm_tile(int it, int t) { return t >= 3 ? vm_at(it, 4 * t - 10 + tp0(it), 1) : t == 1 ? 12 + (LNP >= 2 ? 3 : 0) : vm_at(0, W2S + 2, 5); }
constexpr int vm_unit(int u) { return u < 3 ? 5 + 2 * u + (LNP == 3 || (LNP == 2 && u == 2) ? 3 : 0) : u < 5 ? vm_at(0, 0, u - 2) : vm_at(0, u - 5, 4); }
constexpr int unit_slot(int u) { return u % 3 == 0 ? 0 : 16384 + (u % 3) * 16384; }

constexpr int L_XC = LDS_BYTES - 64, L_SKEY = 32704, L_ROW = 1024, L_OUT = 65536, L_SRED = 4096, L_SFIN = 20480;
__device__ __forceinline__ unsigned key_load(rsrc_t rs, int l, int L, int rep) { return ld4(rs, (l & 15) * SKEY, KEY_AT(L, rep)); }
__device__ __forceinline__ unsigned umax16(unsigned k) {
  k = max(k, dppu<0xB1>(k));
  k = max(k, dppu<0x4E>(k));
  k = max(k, dppu<0x141>(k));
  return max(k, dppu<0x140>(k));
}
__device__ __forceinline__ unsigned key_row(unsigned key) { return (0xffffu - (key & 0xffffu)) & 2047u; }
__device__ __forceinline__ unsigned key_wait(rsrc_t rs, u32x2 T, int l, int L, int rep, unsigned kv) {
  if (!__all(kv != POISON)) {
    unsigned spins = 0;
    do {
      if (spin_fail(spins, rs, T)) break;
      if (L == 0) __builtin_amdgcn_s_sleep(KSL);
      kv = key_load(rs, l, L, rep);
    } while (!__all(kv != POISON));
  }
  return __builtin_amdgcn_readfirstlane(umax16(kv));
}
__device__ __forceinline__ unsigned cand_off(unsigned key, int L) { return OFF_CA + L * 65536 + (key_row(key) >> 7) * 4096; }
#define XO(i) (((i) >> 1) * 256 + ((i) & 1) * 64)
template <int S1, int S2> __device__ __forceinline__ void ffn_xwait(rsrc_t rs, u32x2 T, unsigned xo, int l, u32x4 (&xv)[4]) {
#define XV_OK() __all(max(max(umax4(xv[0]), umax4(xv[1])), max(umax4(xv[2]), umax4(xv[3]))) != POISON)
  if (!XV_OK()) {
    unsigned spins = 0;
    do {
      if (spin_fail(spins, rs, T)) break;
#pragma unroll
      for (int i = 0; i < 4; ++i) xv[i] = ld16(rs, (l >> 4) * 16 + (i & 1) * S1 + (i >> 1) * S2, xo);
    } while (!XV_OK());
  }
#undef XV_OK
}
__device__ __forceinline__ void ffn_gu(const u32x4 (&gv)[4], const u32x4 (&uv)[4], const u32x4 (&xv)[4], f32x4& ga, f32x4& ua) {
  ga = mfma16(gv[0], xv[0], f32x4{0.f, 0.f, 0.f, 0.f}); ua = mfma16(uv[0], xv[0], f32x4{0.f, 0.f, 0.f, 0.f});
#pragma unroll
  for (int i = 1; i < 4; ++i) { ga = mfma16(gv[i], xv[i], ga); ua = mfma16(uv[i], xv[i], ua); }
}
__device__ __forceinline__ void ffn_gu_check(rsrc_t rs, u32x2 T, unsigned xo, int l, const u32x4 (&gv)[4], const u32x4 (&uv)[4], u32x4 (&xv)[4], f32x4& ga, f32x4& ua) {
  if (__builtin_expect(__any(ga[0] != ga[0]), 0)) {
    ffn_xwait<64, 256>(rs, T, xo, l, xv);
    ffn_gu(gv, uv, xv, ga, ua);
  }
}
__device__ __forceinline__ void ffn_gu_store(char* smem, int l, int w, f32x4 ga, f32x4 ua) {
  LDS_AS f32x4* sred = (LDS_AS f32x4*)(smem + L_SRED);
  if ((l & 15) == 0) { sred[w * 8 + (l >> 4)] = ga; sred[w * 8 + 4 + (l >> 4)] = ua; }
}
__device__ __forceinline__ f32x4 ffn_d_read(char* smem, int l) {
  const LDS_AS float* t = (const LDS_AS float*)(smem + L_SRED) + (l & 8) * 16 + (l & 4) * 4 + (l >> 4) * 4 + (l & 3);
  return f32x4{t[0], t[32], t[64], t[96]};
}
__device__ __forceinline__ u32x2 ffn_d_h(f32x4 t) {
  float x = t[0] + t[1];
  asm volatile("" : "+v"(x));
  x += t[2] + t[3];
  x += dppf<0x128>(x);
  const float y = dppf<0x124>(x);
  const bool up = __builtin_amdgcn_inverse_ballot_w64(0xF0F0F0F0F0F0F0F0ull);
  const float gg = rbf1(up ? y : x), uu = rbf1(up ? x : y);
  const float h = rbf1(gg * __builtin_amdgcn_rcpf(1.0f + __expf(-gg))) * uu;
  const unsigned p = pack2(h, dppf<0xB1>(h));
  return u32x2{dppu<0x00>(p), dppu<0xAA>(p)};
}
template <int t> __device__ __forceinline__ f32x4 ffn_d_mma(s16x4 d, u32x2 hh, f32x4 y) {
  const bool me = __builtin_amdgcn_inverse_ballot_w64(0x0001000100010001ull << t);
  return mfma16k16(d, __builtin_bit_cast(s16x4, u32x2{me ? hh[0] : 0u, me ? hh[1] : 0u}), y);
}
__device__ __forceinline__ f32x4 ffn_d_all(const s16x4 (&dfr)[8], u32x2 hh) {
  f32x4 a = {0.f, 0.f, 0.f, 0.f}, b = a;
  a = ffn_d_mma<0>(dfr[0], hh, a); b = ffn_d_mma<1>(dfr[1], hh, b); a = ffn_d_mma<2>(dfr[2], hh, a); b = ffn_d_mma<3>(dfr[3], hh, b);
  a = ffn_d_mma<4>(dfr[4], hh, a); b = ffn_d_mma<5>(dfr[5], hh, b); a = ffn_d_mma<6>(dfr[6], hh, a); b = ffn_d_mma<7>(dfr[7], hh, b);
  return a + b;
}
__device__ __forceinline__ void ffn_d_store(rsrc_t rs, int l, int w, int L, int slice, f32x4 y) {
  const unsigned c = l & 15;
  if (c < 8) st16(unpoison4(__builtin_bit_cast(u32x4, y)), rs, (2 * w + (c >> 2)) * 65536 + slice * 256 + (c & 3) * 64 + (l >> 4) * 16, OFF_PART + L * (1u << 20));
}
#define RED_VO(k) ((min(tid + 512 * (k), 1791) >> 3) * 256 + (tid & 7) * 16)
#define RED_SO(L, G) (OFF_PART + (L) * (1u << 20) + ((G) >> 1) * 65536 + ((G) & 1) * 128)
#define RED_JJ() ((tid & 3) * 16 + (tid & 4) * 2)
__device__ __forceinline__ void red_load(rsrc_t rs, int tid, int L, unsigned G, unsigned key, u32x4 (&pv)[4], u32x2& cxn, u32x2& cx1) {
#pragma unroll
  for (int k = 0; k < 4; ++k) pv[k] = ld16(rs, RED_VO(k), RED_SO(L, G));
  cxn = ld8(rs, RED_JJ(), cand_off(key, L) + XO(G));
  cx1 = ld8(rs, RED_JJ() + 128, cand_off(key, L) + XO(G));
}
__device__ __forceinline__ void red_sum(rsrc_t rs, char* smem, u32x2 T, int tid, int w, int L, unsigned G, unsigned key,
                                        u32x4 (&pv)[4], u32x2& cxn, u32x2& cx1) {
#define PV_OK() __all(max(max(max(umax4(pv[0]), umax4(pv[1])), max(umax4(pv[2]), umax4(pv[3]))), max(max(cxn[0], cxn[1]), max(cx1[0], cx1[1]))) != POISON)
  if (!PV_OK()) {
    unsigned spins = 0;
    do {
      if (spin_fail(spins, rs, T)) break;
#pragma unroll
      for (int k = 0; k < 4; ++k)
        if (umax4(pv[k]) == POISON) pv[k] = ld16(rs, RED_VO(k), RED_SO(L, G));
      if (max(max(cxn[0], cxn[1]), max(cx1[0], cx1[1])) == POISON) { cxn = ld8(rs, RED_JJ(), cand_off(key, L) + XO(G)); cx1 = ld8(rs, RED_JJ() + 128, cand_off(key, L) + XO(G)); }
    } while (!PV_OK());
  }
#undef PV_OK
  f32x4 s = (__builtin_bit_cast(f32x4, pv[0]) + __builtin_bit_cast(f32x4, pv[1])) + __builtin_bit_cast(f32x4, pv[2]);
  if (w < 4) s += __builtin_bit_cast(f32x4, pv[3]);
  ((LDS_AS f32x4*)(smem + L_SFIN))[tid] = s;
}
__device__ __forceinline__ u32x2 red_final(char* smem, int l, unsigned key, u32x2 cxn, u32x2 cx1) {
  const LDS_AS f32x4* sf = (const LDS_AS f32x4*)(smem + L_SFIN) + l;
  f32x4 s = ((sf[0] + sf[64]) + (sf[128] + sf[192])) + ((sf[256] + sf[320]) + (sf[384] + sf[448]));
#pragma unroll
  for (int j = 0; j < 4; ++j) s[j] = sum_hi8(s[j]);
  const unsigned k16 = key >> 16;
  const float rmax = __builtin_bit_cast(float, ((k16 & 0x8000u) ? (k16 & 0x7fffu) : (~k16 & 0xffffu)) << 16);
  const float sig = rbf1(__builtin_amdgcn_rcpf(1.0f + __expf(-rmax)));
  const float xn4[4] = {lo16(cxn[0]), hi16(cxn[0]), lo16(cxn[1]), hi16(cxn[1])};
  const float x14[4] = {lo16(cx1[0]), hi16(cx1[0]), lo16(cx1[1]), hi16(cx1[1])};
  float o[4];
#pragma unroll
  for (int j = 0; j < 4; ++j) o[j] = x14[j] + rbf1(xn4[j] + rbf1(rbf1(s[j]) * sig));
  return u32x2{pack2(o[0], o[1]), pack2(o[2], o[3])};
}

struct RowFin { float mean, var, mb, rd; unsigned key; };
__device__ __forceinline__ void fin_wait(rsrc_t rs, u32x2 T, unsigned voff, unsigned so, u32x4 (&sp)[4]) {
#define ST_OK() __all(max(max(umax4(sp[0]), umax4(sp[1])), max(umax4(sp[2]), umax4(sp[3]))) != POISON)
  if (!ST_OK()) {
    unsigned spins = 0;
    do {
      if (spin_fail(spins, rs, T)) break;
#pragma unroll
      for (int i = 0; i < 4; ++i) sp[i] = ld16(rs, voff + i * 2048, so);
    } while (!ST_OK());
  }
#undef ST_OK
}
__device__ __forceinline__ RowFin fin_key(rsrc_t rs, u32x2 T, unsigned voff, unsigned so, char* skey, int l, int w, unsigned R, float wsum, u32x4 (&sp)[4]) {
  RowFin r;
#define ST_SUM() ((__builtin_bit_cast(f32x4, sp[0]) + __builtin_bit_cast(f32x4, sp[1])) + (__builtin_bit_cast(f32x4, sp[2]) + __builtin_bit_cast(f32x4, sp[3])))
  f32x4 sf = ST_SUM();
  const float z = (sf[0] + sf[1]) + sf[2];
  if (__any(z != z)) {
    fin_wait(rs, T, voff, so, sp);
    sf = ST_SUM();
  }
#undef ST_SUM
  float s0 = sf[0], s1 = sf[1], s2 = sf[2];
  add_g3(s0, s1, s2);
  r.mean = s0 * (1.0f / 1024.0f);
  r.var = fmaxf(__builtin_fmaf(-r.mean, r.mean, s1 * (1.0f / 1024.0f)), 0.f);
  r.mb = rbf1(r.mean); r.rd = ln_rd(r.var);
  const unsigned rb16 = pack2((s2 - r.mb * wsum) * r.rd, 0.f) & 0xffffu;
  r.key = min((((rb16 & 0x8000u) ? (~rb16 & 0xffffu) : (rb16 | 0x8000u)) << 16) | (0xffffu - R), POISON - 1);
  const unsigned wk = umax16(r.key);
  if (l == 0) ((LDS_AS unsigned*)skey)[w] = wk;
  return r;
}
__device__ __forceinline__ unsigned fin_cand(rsrc_t rs, char* skey, int L, int g, int rb, int ct, const RowFin& r, const f32x2 (&v)[8]) {
  const u32x4 k0 = *(const LDS_AS u32x4*)skey, k1 = *(const LDS_AS u32x4*)(skey + 16);
  const unsigned best = max(umax4(k0), umax4(k1));
  if (r.key == best) {
    const unsigned co = L * 65536 + rb * 4096 + ct * 256 + g * 16;
    f32x2 t[8], rd2 = {r.rd, r.rd}, of2 = {-r.mb * r.rd, -r.mb * r.rd};
    asm volatile("" : "+v"(rd2), "+v"(of2));
#pragma unroll
    for (int j = 0; j < 8; ++j) t[j] = __builtin_elementwise_fma(v[j], rd2, of2);
    st16(unpoison4(pack8v(t)), rs, co, OFF_CA); st16(unpoison4(pack8v(t + 4)), rs, co + 64, OFF_CA);
    if (g == 0 && ct < (int)RKEY) st4(best, rs, KEY_AT(L, ct) + rb * SKEY, 0);
    st16(unpoison4(pack8v(v)), rs, co + 128, OFF_CA); st16(unpoison4(pack8v(v + 4)), rs, co + 192, OFF_CA);
  }
  return key_row(best);
}
__device__ __forceinline__ void fin_x2(const RowFin& r, f32x2 (&v)[8]) {
  f32x2 sc2 = {1.0f + r.rd, 1.0f + r.rd}, of2 = {-r.mb * r.rd, -r.mb * r.rd};
  asm volatile("" : "+v"(sc2), "+v"(of2));
#pragma unroll
  for (int j = 0; j < 8; ++j) v[j] = __builtin_elementwise_fma(v[j], sc2, of2);
}

extern "C" __device__ unsigned long long dispatch_id() __asm("llvm.amdgcn.dispatch.id");

extern "C" __global__ __launch_bounds__(512, 2) void mega_kernel(const bf16* x, char* ws, const bf16* wqkv, const bf16* wo, const bf16* wr,
                                                                 const bf16* wg, const bf16* wu, const bf16* wd, bf16* out) {
  extern __shared__ __attribute__((aligned(16))) char smem[];
  const int w0_ = __builtin_amdgcn_readfirstlane(threadIdx.x >> 6);
  const unsigned lbase = (unsigned)(size_t)(LDS_AS char*)smem;
  {
    unsigned long long pc;
    unsigned len;
    asm volatile("s_getpc_b64 %0\n\ts_mov_b32 %1, .Ltext_end-mega_kernel\n\t.subsection 1\n.Ltext_end:\n\t.subsection 0" : "=s"(pc), "=s"(len));
    const unsigned o = ((blockIdx.x >> 3) * 64 + (threadIdx.x & 63)) * 128;
    if (w0_ == 7 && o < len)
      asm volatile("s_mov_b32 m0, %0\n\ts_nop 0\n\tglobal_load_lds_dword %1, %2" ::"s"(lbase + L_FXL), "v"(o), "s"(pc & ~127ull) : "memory");
  }
  asm volatile("" : "+s"(x), "+s"(ws), "+s"(wqkv));
#define IDS()                                                                                         \
  int tid = threadIdx.x, w = w0_, bid = blockIdx.x;                                                   \
  asm volatile("" : "+v"(tid));                                                                       \
  asm volatile("" : "+s"(w));                                                                         \
  asm volatile("" : "+s"(bid));                                                                       \
  const int l = tid & 63, xcd = bid & 7, bi = bid >> 3;                                               \
  const int p = (xcd & 1) * 4 + (bi & 3), h = (xcd >> 1) * 8 + (bi >> 2);         \
  const int rb = 2 * xcd + (bi & 1), ct = bi >> 1;                               \
  const int slice = xcd * 28 + bi;                                                   \
  const bool ffn_on = bi < 28;                                                                        \
  const int g = l >> 4;                                                                               \
  const int rt = w < 4 ? w : 11 - w;          \
  KARGS();                                                                                            \
  (void)p; (void)h; (void)rb; (void)ct; (void)slice; (void)ffn_on; (void)g; (void)rt;
  const rsrc_t rs = __builtin_amdgcn_make_buffer_rsrc((void*)ws, 0, WS_BYTES, 0x00020000);
  typedef const void* const __attribute__((address_space(4)))* karg_t;
#define KARGS() karg_t ka = (karg_t)__builtin_amdgcn_kernarg_segment_ptr(); asm volatile("" : "+s"(ka))
#define ARGP(name, i) const GAS bf16* name = (const GAS bf16*)ka[i]; asm volatile("" : "+s"(name))
#define RSRC(ptr, bytes) __builtin_amdgcn_make_buffer_rsrc((void*)(ptr), 0, bytes, 0x00020000)
#define rwq RSRC(pwq, 1024 * 3072 * 2)
#define rwo RSRC(pwo, 1024 * 1024 * 2)
#define rwg RSRC(pwg, 1024 * F_ * 2)
#define rwu RSRC(pwu, 1024 * F_ * 2)
#define rwd RSRC(pwd, 1024 * F_ * 2)
#define rout RSRC(pout, 2048 * 1024 * 2)

#define WQ_VOFF() ((l >> 2) * 6144 + (((l & 3) ^ ((((l >> 4) ^ (l >> 5)) & 1) << 1)) << 4))
#define WQ_PIECE(t, j, dst) dma16(rwq, wqv, ((((t) + rot) & 7) * 128 + ((j) & 7) * 16) * 6144 + ((j) >> 3) * 2048 + h * 64, lbase + (dst) + (j) * 1024)
#define WQ_BOFF(dt) ((8 * g + ((l & 15) >> 2)) * 64 + (l & 3) * 8 + (((dt) ^ (g & 1)) << 5))
#define WQ_POFF() ((4 * g + ((l & 15) >> 2)) * 64 + (l & 3) * 8 + (((g ^ (g >> 1)) & 1) << 5))
#define X_UNIT1(u, j) dma16(rx, xv_, (unsigned)(p * 256 + rt * 32 + ((u) & 1) * 16 + (j) * 8) * 2048 + ((((u) >> 2) + rot) & 7) * 256 + (((u) >> 1) & 1) * 128, lbase + rt * 2048 + unit_slot(u) + (j) * 1024)
#define X_VOFF() ((l >> 3) * 2048 + (((l & 7) ^ ((l >> 3) & 6)) << 4))
#define X_ROFF(hs) (lbase + rt * 2048 + (l & 15) * 128 + (((4 * (hs) + g) ^ (l & 6)) << 4))
#define TOUCH_TO(ptr, dst) asm volatile("s_mov_b32 m0, %0\n\ts_nop 0\n\tglobal_load_lds_dword %1, off" ::"s"(lbase + (dst)), "v"(ptr) : "memory")
#define TOUCH(ptr) TOUCH_TO(ptr, L_FXL)
#define WO_PIECE_W(j, w_) dma16(rwo, wov, (((w_) >> 2) * 64 + ((w_) & 3) + 4 * (j)) * 16384 + ct * 128, lbase + 32768 + (j) * 8192 + (w_) * 1024)
#define WO_PIECE(j) WO_PIECE_W(j, w)
#define WO_VOFF() ((l >> 3) * 2048 + (((((l & 7) >> 1) ^ ((l >> 4) & 3)) * 2 + (l & 1)) << 4))
#define WD_DMA() _Pragma("unroll") for (int i = 0; i < 4; ++i) WD_DMA1(i)
#define WD_DMA1(i)                                                                                   \
      dma16n<(NTM >> 1) & 1>(rwd, (l >> 4) * 2048 + (l & 15) * 16, (unsigned)(slice * 16 + 4 * (i)) * 2048 + w * 256, lbase + L_DN + w * 4096 + (i) * 1024)
#define FFN_DMA(m) dma16n<(NTM >> 2) & 1>((m) & 1 ? rwu : rwg, fnv, (unsigned)(((4 * w + ((m) >> 1)) * 32) * F_ + slice * 16) * 2, lbase + 65536 + (m) * 8192 + w * 1024)
  u32x4 gv[4], uv[4];
  s16x4 dfr[8];
  u32x4 keep[9];
  u32x4 pre[4][2];
#define PRE_LOAD(it_, rot_, s0, s1)                                                                  \
  static_for<(s1) - (s0)>([&](auto c_) __attribute__((always_inline)) {                              \
    constexpr int s = (s0) + decltype(c_)::value;                                                    \
    const unsigned ab_ = OFF_XN + (it_) * (4u << 20) + (((rot_) + (s >> 2)) & 7) * 8192 + ((s >> 1) & 1) * 4096; \
    ld16w<2 * s, (s & 1) * 2048>(pre[s][0], rs, l * 16, ab_ + (p * 8 + rt) * 65536);                         \
    ld16w<2 * s + 1, (s & 1) * 2048 + 1024>(pre[s][1], rs, l * 16, ab_ + (p * 8 + rt) * 65536);                 \
  })
  u32x2 T;
  u32x2 ep;
  u32x4 xl[2];
#define LN_LOAD()                                                                                                                                   \
  {                                                                                                                                                 \
    asm volatile("buffer_load_dwordx4 %0, %1, %2, %3 offen" : "=v"(xl[0]) : "v"(l * 16), "s"(rx), "s"((p * 256 + h * 8 + w) * 2048) : "memory");      \
    asm volatile("buffer_load_dwordx4 %0, %1, %2, %3 offen offset:1024" : "=v"(xl[1]) : "v"(l * 16), "s"(rx), "s"((p * 256 + h * 8 + w) * 2048) : "memory"); \
    asm volatile("buffer_load_dwordx2 %0, off, %1, 0 sc1" : "=v"(ep) : "s"(rs) : "memory");                                                         \
  }
  const GAS bf16* tsrc;
  {
    IDS();
    const int it = 0, rot = ROT0;
    (void)it;
    STAMPAT(0);
    const bf16* pwq = wqkv;
    const rsrc_t rx = RSRC(x, 2048 * 1024 * 2);
    const unsigned wqv = WQ_VOFF(), xv_ = X_VOFF();
    if (tid == 0) *(LDS_AS u32x4*)(smem + L_QC) = u32x4{0u, 0u, 0u, 0u};
    if (w == HLPW && l < 24) TOUCH((const GAS bf16*)wqkv + (bid * 24 + l) * 512 + ((3 - (unsigned)((size_t)wqkv >> 7)) & 7) * 64);
    if (LNP == 3) LN_LOAD();
#pragma unroll
    for (int i = 0; i < 3; ++i) WQ_PIECE(0, 3 * w + i, L_RING);
    STAMPAT(41);
    X_UNIT1(0, 0); X_UNIT1(0, 1); X_UNIT1(1, 0); X_UNIT1(1, 1);
    if (LNP == 2) LN_LOAD();
    STAMPAT(42);
    BAR();
    STAMPAT(43);
    X_UNIT1(2, 0); X_UNIT1(2, 1);
#pragma unroll
    for (int i = 0; i < 3; ++i) WQ_PIECE(1, 3 * w + i, L_RING + STAGE);
    if (LNP == 1) LN_LOAD();
    STAMPAT(44);
    STAMPAT(31);
  }
  unsigned sel0 = 0x7FFFFFFFu;
  f32x2 v[8];
  u32x4 x2p[2];
  float wsum;

  auto layer = [&](auto it_c) __attribute__((always_inline)) {
    constexpr int it = decltype(it_c)::value;
    STAMPAT(1); STAMPAT(22);
    f32x4 acc[2][6];
    f32x2 qs2[2];
    u32x4 k0, k1, v0, v1;
    {
      IDS();
      bool aff = false;
      unsigned aq[2];
      u32x4 av[2];
#define PIN_ACC() asm volatile("" : "+{v[176:179]}"(acc[0][0]), "+{v[180:183]}"(acc[0][1]), "+{v[184:187]}"(acc[0][2]), "+{v[188:191]}"(acc[0][3]), "+{v[192:195]}"(acc[0][4]), "+{v[196:199]}"(acc[0][5]), \
                               "+{v[200:203]}"(acc[1][0]), "+{v[204:207]}"(acc[1][1]), "+{v[208:211]}"(acc[1][2]), "+{v[212:215]}"(acc[1][3]), "+{v[216:219]}"(acc[1][4]), "+{v[220:223]}"(acc[1][5]))
#define PINV4(x) asm volatile("" : "+v"(x[0]), "+v"(x[1]), "+v"(x[2]), "+v"(x[3])::"memory")
      auto qkv_loop = [&](auto it_c) __attribute__((always_inline)) {
        constexpr int IT = decltype(it_c)::value;
        const unsigned abase = OFF_XN + IT * (4u << 20) + (p * 8 + rt) * 65536;
        const int rot = ROT0 + 5 * IT;
        unsigned fkey = 0, kv, bq[2];
        u32x4 fx[4], bv[2], af[WA][2];
        u32x2 cxn, cx1, hh;
        f32x4 sa, sb, cs4[2];
        f32x2 b2[2];
#define AB_OFF() (OFF_AB + IT * 32768 + (p * 8 + rt) * 512)
#define A_LOAD1(k, a) ld16w<(k) % WA * 2 + (a), ((k) & 1) * 2048 + (a) * 1024>(af[(k) % WA][a], rs, l * 16, abase + ((((k) >> 2) + rot) & 7) * 8192 + (((k) >> 1) & 1) * 4096)
        if constexpr (IT == 1) {
#pragma unroll
          for (int s = 0; s < NPRE; ++s) { af[s][0] = pre[s][0]; af[s][1] = pre[s][1]; }
        }
        const bf16* pwq = wqkv;
        ARGP(pwo, 3);
        const rsrc_t rx = RSRC(x, 2048 * 1024 * 2);
        unsigned xv_ = X_VOFF(), xr[2] = {X_ROFF(0), X_ROFF(1)};
        if constexpr (IT == 0) asm volatile("" : "+v"(xv_), "+v"(xr[0]), "+v"(xr[1]));
#define X_READ(u) { af[((u) & ~1) % WA][(u) & 1] = *(const LDS_AS u32x4*)(size_t)(xr[0] + unit_slot(u)); af[((u) | 1) % WA][(u) & 1] = *(const LDS_AS u32x4*)(size_t)(xr[1] + unit_slot(u)); }
        const int tstep = TS0 + bid % TSN + TSD * (w >> TSG);
        const unsigned wqv = WQ_VOFF();
#define QKV_DMA1(t, i)                                                                            \
  {                                                                                               \
    if constexpr (IT == 0) WQ_PIECE(t, 3 * w + (i), L_RING + SLOT(t) * STAGE);                    \
    else dma16s<(i) * 1024>(rs, l * 16, OFF_WI + (h * 8 + (((t) + rot) & 7)) * STAGE + w * 3072, lbase + L_RING + SLOT(t) * STAGE + w * 3072); \
  }
        unsigned bo[2][2];
#pragma unroll
        for (int dt = 0; dt < 2 - IT; ++dt) {
          bo[dt][0] = lbase + L_RING + (IT ? l * 16 : WQ_BOFF(dt)); bo[dt][1] = bo[dt][0] + 2 * STAGE;
          asm volatile("" : "+v"(bo[dt][0]), "+v"(bo[dt][1]));
        }
        auto frag0 = [&](int sl, int f) __attribute__((always_inline)) {
          unsigned pb = lbase + L_RING + WQ_POFF();
          asm volatile("" : "+v"(pb));
          const unsigned o = (pb ^ ((f & 1) << 5)) + sl * STAGE + ((f % 6) >> 1) * 8192 + (f / 6) * 2048;
          frag_t t;
          t.hf.a = trabs(o); t.hf.b = trabs(o + 1024);
          return t.v;
        };
        u32x4 df[4];
        auto dot_mma = [&](const u32x4 (&xf)[4]) __attribute__((always_inline)) {
          sa = mfma16(df[0], xf[0], f32x4{0.f, 0.f, 0.f, 0.f}); sb = mfma16(df[1], xf[1], f32x4{0.f, 0.f, 0.f, 0.f});
          sa = mfma16(df[2], xf[2], sa); sb = mfma16(df[3], xf[3], sb);
        };
        auto dot_store = [&](unsigned dst) __attribute__((always_inline)) {
          if ((l & 15) == 0) st16(unpoison4(__builtin_bit_cast(u32x4, sa + sb)), rs, g * 16, dst + (h * 8 + p) * 512 + w * 64);
        };
        auto tile_sum = [&](u32x4 pt, int dst) __attribute__((always_inline)) {
          f32x4 c = __builtin_bit_cast(f32x4, pt);
#pragma unroll
          for (int e = 0; e < 4; ++e) { c[e] += dppf<0x124>(c[e]); c[e] += dppf<0x128>(c[e]); c[e] = add_x16(c[e]); }
          if ((l & 28) == 0) *(LDS_AS f32x4*)(smem + dst + (w * 2 + (l >> 5)) * 64 + (l & 3) * 16) = c;
        };
        frag_t bfr[6];
#define QKV_BO(s, n, x) (bo[((n) ^ (x)) & 1][SLOT((s) >> 2) >> 1] + (SLOT((s) >> 2) & 1) * STAGE + ((n) >> 1) * 8192 + ((s) & 3) * 2048 + (x) * 256)
#define QKV_RDA(s, n)                                                                             \
  {                                                                                               \
    if constexpr (IT == 0) bfr[n].hf.a = trabs(QKV_BO(s, n, 0));                                  \
    else bfr[n].v = *(const LDS_AS u32x4*)(size_t)(bo[0][SLOT((s) >> 2) >> 1] + (SLOT((s) >> 2) & 1) * STAGE + (((s) & 3) * 6 + (n)) * 1024); \
  }
#define QKV_RDB(s, n) { if constexpr (IT == 0) bfr[n].hf.b = trabs(QKV_BO(s, n, 1)); }
#define XP_OFF() (OFF_XNEW + ((rot + 5 + PO()) & 7) * 1024)
#define FP_VO() (w == 3 ? (l >> 2) * 256 + (l & 3) * 16 : ((l >> 2) & 7) * 512 + (l >> 5) * 64 + (l & 3) * 16)
#define FP_OFF(i) (w == 3 ? OFF_XNEW + (i) * 4096 : OFF_FX + h * 4096 + w * 128)
#define FX_OK(n) __all(((n) == 4 ? max(max(umax4(fx[0]), umax4(fx[1])), max(umax4(fx[2]), umax4(fx[3]))) : max(umax4(fx[0]), umax4(fx[1]))) != POISON)
#define SINCE(s0) (vm_at(IT, s, n) - vm_at(IT, s0, 5))
#define DEF4(x) asm volatile("" : "=v"(x[0]), "=v"(x[1]), "=v"(x[2]), "=v"(x[3]))
        LDS_AS unsigned* qcnt = (LDS_AS unsigned*)(smem + L_QC);
        unsigned qc;
        unsigned qadr = lbase + L_QC, qone = 1;
        asm volatile("" : "+v"(qadr), "+v"(qone));
#define count_in(i) asm volatile("s_mov_b64 exec, 1\n\tds_add_u32 %0, %1 offset:%2\n\ts_mov_b64 exec, -1" ::"v"(qadr), "v"(qone), "n"(4 * (i)) : "memory")
        auto count_wait = [&](int i, unsigned want) __attribute__((always_inline)) {
          if (__builtin_expect((unsigned)__builtin_amdgcn_readfirstlane(qc) < want, 0)) {
            while ((unsigned)__builtin_amdgcn_readfirstlane(__hip_atomic_load(qcnt + i, __ATOMIC_RELAXED, __HIP_MEMORY_SCOPE_WORKGROUP)) < want) { if (SPINS) __builtin_amdgcn_s_sleep(SPINS); }
          }
          asm volatile("" ::: "memory");
        };
        if constexpr (IT == 0) {
          wait_vm<PRE0 - vm_unit(1)>();
          STAMPAT(46);
          X_READ(0); X_READ(1);
          asm volatile("s_barrier" ::: "memory");
        }
        STAMPAT(39);
#pragma unroll
        for (int n = 0; n < 6; ++n) { QKV_RDA(0, n); if (n < 5) QKV_RDB(0, n); }
        f32x2 lv[4], xs, xq;
        float lmean;
        f32x4 sc;
#define X1_OFF() (F_X1 + ((p * RX1 + (xcd >> 1) % RX1) * 32 + (rt >> 2) * 16 + (l & 15)) * SX1)
        unsigned long long xok = 0;
        RowFin fin;
        auto x1_need = [&](unsigned long long m) __attribute__((always_inline)) {
          if (__builtin_expect((xok & m) != m, 0)) {
            for (unsigned spins = 0;;) {
              const u32x2 f = ld8(rs, X1_OFF(), 0);
              xok = __builtin_amdgcn_ballot_w64(f[0] == T[0] && f[1] == T[1]);
              if ((xok & m) == m) break;
              if (spin_fail(spins, rs, T)) { xok = ~0ull; break; }
              __builtin_amdgcn_s_sleep(KSL);
            }
            asm volatile("" ::: "memory");
          }
        };
        auto gap = [&](auto sc_, auto nc_) __attribute__((always_inline)) {
          constexpr int s = decltype(sc_)::value, n = decltype(nc_)::value, ar = a_req(s, n);
          if constexpr ((SYNCB >> IT) & 1) {
            constexpr int tc = (s + 2) / 4;
            if constexpr (tc >= 1 && tc < 8 && s == 4 * tc - 2 && n == 5) {
              if constexpr (!IT || tc >= 3) wait_vm<vm_at(IT, s, n) - vm_tile(IT, tc)>();
              if constexpr (tc == 7 && !IT) asm volatile("s_waitcnt lgkmcnt(4)" ::: "memory");
              asm volatile("s_barrier" ::: "memory");
            }
          } else {
            constexpr int ts = (s + 3) / 4, tc = (s + 2) / 4, t0 = 1;
            if constexpr (ts >= 3 && ts < 8 && s == 4 * ts - 3 && n == 2) { wait_vm<vm_at(IT, s, n) - vm_tile(IT, ts)>(); count_in(0); }
            if constexpr (!IT && tc >= t0 && tc < 3 && s == 4 * tc - 2 && n == 1) { wait_vm<vm_at(IT, s, n) - vm_tile(IT, tc)>(); count_in(0); }
            if constexpr (IT && ts >= t0 && ts < 3 && s == 4 * ts - 3 && n == 2) count_in(0);
            if constexpr (tc >= t0 && tc < 8 && s == 4 * tc - 2) {
              if constexpr (n == 3) qc = __hip_atomic_load(qcnt, __ATOMIC_RELAXED, __HIP_MEMORY_SCOPE_WORKGROUP);
              if constexpr (n == 5) count_wait(0, 8 * (tc + 1 - IT));
            }
          }
          if constexpr (IT == 0 && n == 0 && s + 2 < 32) { wait_vm<vm_at(0, s, 0) - vm_unit(s + 2)>(); X_READ(s + 2); SB(); }
          if constexpr (n == 0 && tile_piece(IT, s)) QKV_DMA1(((s - tp0(IT)) >> 2) + 3, (s - tp0(IT)) & 3);
          if constexpr (IT == 0) {
            if constexpr ((n == 2 || n == 3) && s + 5 < 32) {
              if constexpr (n == 2) asm volatile("s_waitcnt lgkmcnt(%0)" ::"n"(s + 1 < QL ? 4 : 1) : "memory");
              X_UNIT1(s + 5, n - 2);
            }
            if constexpr (s >= W2S && s < W2S + 3 && n == 4) QKV_DMA1(2, s - W2S);
            if constexpr (LNP == 0 && s == LNL && n == 5) LN_LOAD();
            if constexpr (s == 0 && n < 2) { X_UNIT1(3 + n, 0); X_UNIT1(3 + n, 1); }
            if constexpr (s == LNS && n == 1) {
              if constexpr (LNP == 0) wait_vm<vm_at(0, s, n) - vm_at(0, LNL + 1, 0)>();
              asm volatile("" : "+v"(ep), "+v"(xl[0]), "+v"(xl[1])::"memory");
              const unsigned long long e = (((unsigned long long)(unsigned)__builtin_amdgcn_readfirstlane(ep[1]) << 32) | (unsigned)__builtin_amdgcn_readfirstlane(ep[0])) + 1ull + dispatch_id() * 0x9E3779B97F4A7C16ull;
              T = u32x2{(unsigned)e, (unsigned)(e >> 32)};
              unpack8v(xl[0], lv);
              xs = (lv[0] + lv[1]) + (lv[2] + lv[3]);
              xq = __builtin_elementwise_fma(lv[0], lv[0], lv[1] * lv[1]) + __builtin_elementwise_fma(lv[2], lv[2], lv[3] * lv[3]);
              asm volatile("" : "+v"(xs), "+v"(xq));
            }
            if constexpr (s == LNS && n == 4) {
              unpack8v(xl[1], lv);
              xs += (lv[0] + lv[1]) + (lv[2] + lv[3]);
              xq += __builtin_elementwise_fma(lv[0], lv[0], lv[1] * lv[1]) + __builtin_elementwise_fma(lv[2], lv[2], lv[3] * lv[3]);
              auto t = __builtin_amdgcn_permlane32_swap(__builtin_bit_cast(unsigned, hadd(xs)), __builtin_bit_cast(unsigned, hadd(xq)), false, false);
              lmean = asf(t[0]) + asf(t[1]);
              asm volatile("" : "+v"(lmean));
            }
            if constexpr (s == LNS + 1 && n == 1) { lmean += dppf<0xB1>(lmean); lmean += dppf<0x4E>(lmean); lmean += dppf<0x124>(lmean); asm volatile("" : "+v"(lmean)); }
            if constexpr (s == LNS + 1 && n == 4) { lmean = add_x16(lmean + dppf<0x128>(lmean)) * (1.0f / 1024.0f); asm volatile("" : "+v"(lmean)); }
            if constexpr (s == LNS + 2 && n == 1) {
              const float m2 = asf(__builtin_amdgcn_readlane(__builtin_bit_cast(unsigned, lmean), 32));
              xs[0] = ln_rd(fmaxf(__builtin_fmaf(-lmean, lmean, m2), 0.f));
              asm volatile("" : "+v"(xs));
            }
            if constexpr (s == LNS + 2 && n == 5) {
              if (l == 0) st16(u32x4{__builtin_bit_cast(unsigned, (float)xs[0]), __builtin_bit_cast(unsigned, -rbf1(lmean)), T[0], T[1]}, rs, OFF_AB + (p * 256 + h * 8 + w) * 16, 0);
            }
            if constexpr (s == POI && n == 1) {
              if (w == 1) {
                const unsigned prb = 2 * p + (h >> 4), pct = h & 15;
                unsigned pz1 = POISON;
                asm volatile("" : "+v"(pz1));
                const u32x4 pz = {pz1, pz1, pz1, pz1};
                st16(pz, rs, OFF_STAT + (prb * 16 + pct) * 2048 + l * 16, 0);
                st16(pz, rs, OFF_STAT + (prb * 16 + pct) * 2048 + 1024 + l * 16, 0);
                if (l < 16) st16(pz, rs, OFF_CA + prb * 4096 + pct * 256 + l * 16, 0);
                else if (l == 16 && pct < RKEY) st4(pz1, rs, KEY_AT(0, pct) + prb * SKEY, 0);
              }
            }
            if constexpr (s == POI + 4 && n == 1) { if (w == 1 && l < RX) st8(T, rs, F_XN + (p * RX + l) * 256 + h * 8, 0); }
          } else {
            if constexpr ((s == 0 && n == 0) || (s == 2 && n == 2)) asm volatile("buffer_load_dwordx2 %0, %1, %2, 0 offen sc1" : "=v"(cxn) : "v"(X1_OFF()), "s"(rs) : "memory");
            if constexpr ((s == 1 && n == 3) || (s == 4 && n == 0)) {
              wait_vm<vm_at(1, s, n) - (s == 1 ? vm_at(1, 0, 1) : vm_at(1, 2, 3))>();
              asm volatile("" : "+v"(cxn)::"memory");
              xok |= __builtin_amdgcn_ballot_w64(cxn[0] == T[0] && cxn[1] == T[1]);
            }
#define IMG_OFF() (F_X1 + ((((h >> 3) * 2 + ((l >> 2) & 1)) * RX1 + (xcd >> 1) % RX1) * 32 + (l & 1) * 16 + (h & 7) * 2 + ((l >> 1) & 1)) * SX1)
            if constexpr (s == 0 && n == 2) { asm volatile("" : "=v"(cx1)); if (w == 1) asm volatile("buffer_load_dwordx2 %0, %1, %2, 0 offen sc1" : "=v"(cx1) : "v"(IMG_OFF()), "s"(rs) : "memory"); }
            if constexpr (s == 2 && n == 4) {
              if (w == 1) {
                wait_vm<vm_at(1, 2, 4) - vm_at(1, 0, 3)>();
                asm volatile("" : "+v"(cx1)::"memory");
                if (!__all(cx1[0] == T[0] && cx1[1] == T[1])) poll_flags(rs, IMG_OFF(), 0, T);
              }
            }
            if constexpr (ar >= 0 && (ar & 7) == 0) x1_need(3ull << (((rot + ar / 8) & 7) * 2));
            const unsigned R = (rb * 4 + (w & 3)) * 32 + (w >> 2) * 16 + (l & 15), so = OFF_STAT + rb * 32768;
            if constexpr (s == (SPL < 0 ? 0 : SPL) && n == (SPL < 0 ? 1 : 4)) {
#pragma unroll
              for (int i = 0; i < 4; ++i) ld16h<0>(fx[i], rs, (4 * g + i) * 2048 + (R & 127) * 16, so);
            }
            if constexpr (s == SPL + 2 && n == 1) {
              wait_vm<vm_at(1, s, n) - (SPL < 0 ? vm_at(1, 0, 2) : vm_at(1, SPL, 5))>();
              PINV4(fx);
              fin = fin_key(rs, T, g * 8192 + (R & 127) * 16, so, smem + L_SKEY, l, w, R, wsum, fx);
              count_in(2);
            }
            if constexpr (s == SPL + 3 && n == 0) qc = __hip_atomic_load(qcnt + 2, __ATOMIC_RELAXED, __HIP_MEMORY_SCOPE_WORKGROUP);
            if constexpr (s == SPL + 3 && n == 1) { count_wait(2, 8); fin_cand(rs, smem + L_SKEY, 0, g, rb, ct, fin, v); STAMPAT(51); }
            if constexpr (s == SPL + 3 && n == 3) {
              fin_x2(fin, v);
              x2p[0] = pack8v(v); x2p[1] = pack8v(v + 4);
              asm volatile("" : "+v"(x2p[0]), "+v"(x2p[1]));
            }
            if constexpr (s == SPL + 3 && n == 5) {
              if ((w & 3) * 2 + (w >> 2) == ct >> 1) {
                const float f = 1.0f + fin.rd, rd2 = ln_rd(fin.var * f * f), mb2 = rbf1(__builtin_fmaf(fin.mean - fin.mb, fin.rd, fin.mean));
                if (g == 0 && ((l >> 3) & 1) == (ct & 1))
                  st16(u32x4{__builtin_bit_cast(unsigned, f * rd2), __builtin_bit_cast(unsigned, -__builtin_fmaf(fin.mb, fin.rd, mb2) * __builtin_amdgcn_rcpf(f)), T[0], T[1]}, rs, OFF_AB + 32768 + R * 16, 0);
              }
            }
          }
          if constexpr (IT == 1 && ar >= 0) A_LOAD1(ar / 2, ar & 1);
          if constexpr (!IT && s == CSL && n == 4) { asm volatile("" : "=v"(fx[0])); if (w < 6) ld16h<0>(fx[0], rs, l * 16, OFF_CS + (h * 6 + w) * 1024); }
          if constexpr (!IT && s == CSS && n == 2) {
            if (w < 6) {
              wait_vm<SINCE(CSL)>();
              asm volatile("" : "+v"(fx[0])::"memory");
              tag_wait(rs, T, l * 16, OFF_CS + (h * 6 + w) * 1024, fx[0]);
              const f32x2 c = {sum_hi8(asf(fx[0][0])), sum_hi8(asf(fx[0][1]))};
              if (l < 8) *(LDS_AS f32x2*)(smem + L_CSL + w * 64 + l * 8) = c;
            }
          }
          constexpr int ABL = abl(IT), ABS = abs_(IT);
          if constexpr (s == ABL && n == 4) ld16h<0>(bv[0], rs, (l & 31) * 16, AB_OFF());
          if constexpr (s == ABS && n == 2) {
            wait_vm<SINCE(ABL)>();
            asm volatile("" : "+v"(bv[0])::"memory");
            tag_wait(rs, T, (l & 31) * 16, AB_OFF(), bv[0]);
            const unsigned t0 = bv[0][0], t1 = bv[0][1];
            ((LDS_AS unsigned*)(smem + L_ABW + rt * 2048))[l] = l < 32 ? t0 : t1;
          }
          if constexpr (s == ABS && n == 3) {
#pragma unroll
            for (int a = 0; a < 2; ++a) {
              const LDS_AS unsigned* aw = (const LDS_AS unsigned*)(smem + L_ABW + rt * 2048) + 16 * a;
              aq[a] = aw[l & 15]; bq[a] = aw[32 + (l & 15)];
              av[a] = *(const LDS_AS u32x4*)(aw + 4 * g); bv[a] = *(const LDS_AS u32x4*)(aw + 32 + 4 * g);
            }
          }
          if constexpr (s == ABS && n == 5) {
#pragma unroll
            for (int a = 0; a < 2; ++a) { b2[a] = f32x2{asf(bq[a]), asf(bq[a])}; asm volatile("" : "+v"(b2[a])); }
          }
          if constexpr (s == ABS + 1) {
            constexpr int k = (n + 4) % 6;
            if constexpr (k < 4) cs4[k & 1] = *(const LDS_AS f32x4*)(smem + L_CSL + k * 64 + g * 16);
            else cs4[k & 1][0] = *(const LDS_AS float*)(smem + L_CSL + k * 64 + (l & 15) * 4);
          }
          if constexpr ((s == ABS + 1 && n > 0) || (s == ABS + 2 && n == 0)) {
            constexpr int k = (n + 3) % 6;
            if constexpr (k < 4) {
#pragma unroll
              for (int a = 0; a < 2; ++a) acc[a][k] = __builtin_elementwise_fma(cs4[k & 1], f32x4{b2[a][0], b2[a][1], b2[a][0], b2[a][1]}, acc[a][k]);
            } else {
              f32x2 c2 = {cs4[k & 1][0], cs4[k & 1][0]};
              asm volatile("" : "+v"(c2));
#pragma unroll
              for (int a = 0; a < 2; ++a) acc[a][k] = __builtin_elementwise_fma(f32x4{c2[0], c2[1], c2[0], c2[1]}, __builtin_bit_cast(f32x4, bv[a]), acc[a][k]);
            }
          }
          if constexpr (IT == 0) {
            if constexpr (s == CSD && n < 3) fx[n] = frag0(SLOT((s >> 2) + PO()), 3 * w + n);
            if constexpr (s == CSD + 1 && n == 1) {
              unsigned one2 = 0x3F803F80u;
              asm volatile("" : "+v"(one2));
              const u32x4 o4 = {one2, one2, one2, one2};
              const f32x4 z4 = {0.f, 0.f, 0.f, 0.f};
              sa = mfma16(fx[0], o4, z4); sb = mfma16(fx[1], o4, z4); sc = mfma16(fx[2], o4, z4);
            }
            if constexpr (s == CSD + 2 && n == 1) {
              const int c = l & 15;
              const f32x4 t0 = sa, t1 = sb, t2 = sc, t = c == 0 ? +t0 : c == 1 ? +t1 : +t2;
              if (c < 3) *(LDS_AS f32x4*)(smem + L_CSP + (w * 3 + c) * 64 + g * 16) = t;
              count_in(2);
            }
            if constexpr (s == CSD + 4 && n == 1) {
              if (w == 2) {
                qc = 0;
                count_wait(2, 8);
                const LDS_AS f32x2* cp = (const LDS_AS f32x2*)(smem + L_CSP) + min(l, 47);
                const f32x2 t = (cp[0] + cp[48]) + (cp[96] + cp[144]);
                if (l < 48) st16(u32x4{__builtin_bit_cast(unsigned, (float)t[0]), __builtin_bit_cast(unsigned, (float)t[1]), T[0], T[1]}, rs, (l >> 3) * 1024 + (l & 7) * 16, OFF_CS + h * 6144 + p * 128);
              }
            }
            if constexpr (s >= 29 && (n & 1)) keep[3 * (s - 29) + (n >> 1)] = frag0(SLOT(s - 24), 3 * w + (n >> 1));
            if constexpr (s == (TS0 < TEX ? TS0 : TEX) - 2 && n == 1) {
              ARGP(pwg, 5); ARGP(pwu, 6);
              const int t = tid < 288 ? tid - 64 : tid - 288, go = (bi * 32 + t / 7) * F_ + xcd * 448 + (t % 7) * 64;
              tsrc = tid < 64 ? pwo + bid * 4096 + tid * 64 : tid < 288 ? pwg + go : pwu + go;
              asm volatile("" : "+v"(tsrc));
            }
            if constexpr (s >= TS0 && s < TS0 + TSN + TSD * (7 >> TSG) && n == 4) {
              if (tstep == s && ((TM0 >> w) & 1)) TOUCH(tsrc);
            }
            if constexpr (s == TEX && n == 5) {
              const GAS bf16* t2 = ((TM1 >> w) & 1) ? tsrc : (const GAS bf16*)x + p * 262144;
              if ((NTM & 1) && w > 0) asm volatile("s_mov_b32 m0, %0\n\ts_nop 0\n\tglobal_load_lds_dword %1, off nt" ::"s"(lbase + L_FXL), "v"(t2) : "memory");
              else TOUCH(t2);
            }
          } else {
            if constexpr (s >= 27 && s < 30 && (n == 0 || n == 3)) { const unsigned wov = WO_VOFF(); WO_PIECE(4 + 2 * (s - 27) + n / 3); }
            if constexpr (s == FKL && n == 4) { asm volatile("" : "=v"(kv)); if (w == 0) ld4h<0>(kv, rs, (l & 15) * SKEY, KEY_AT(0, KREP())); }
            if constexpr (s == FSL && n == 1) {
              asm volatile("" : "=v"(fx[0]), "=v"(fx[1]));
              if (w == 0) {
                wait_vm<SINCE(FKL)>();
                asm volatile("" : "+v"(kv)::"memory");
                STAMPAT(52);
                const unsigned k_ = key_wait(rs, T, l, 0, KREP(), kv);
                STAMPAT(53);
                if (l == 0) *(LDS_AS unsigned*)(smem + L_SKEY + 32) = k_;
                fkey = k_;
                ld16h<0>(fx[0], rs, (l >> 3) * 256 + (l & 7) * 16, cand_off(k_, 0)); ld16h<2048>(fx[1], rs, (l >> 3) * 256 + (l & 7) * 16, cand_off(k_, 0));
              }
            }
            if constexpr (s == FS0 - 1 && n == FSN) {
              if (w == 0) {
                wait_vm<vm_at(1, s, n) - vm_at(1, FSL, 2)>();
                asm volatile("" : "+v"(fx[0]), "+v"(fx[1])::"memory");
                word_wait<2>(rs, T, (l >> 3) * 256 + (l & 7) * 16, cand_off(fkey, 0), fx);
                *(LDS_AS u32x4*)(smem + L_ROW + l * 16) = fx[0]; *(LDS_AS u32x4*)(smem + L_ROW + 1024 + l * 16) = fx[1];
                count_in(0);
              }
            }
            if constexpr (s == FS0 - 1 && n == 1) {
              asm volatile("" : "=v"(dfr[0]), "=v"(dfr[1]), "=v"(dfr[2]), "=v"(dfr[3]), "=v"(dfr[4]), "=v"(dfr[5]), "=v"(dfr[6]), "=v"(dfr[7]));
              if (ffn_on) {
#pragma unroll
                for (int i = 0; i < 8; ++i) dfr[i] = trread(smem, L_DN + w * 4096 + g * 1024 + ((l & 15) >> 2) * 256 + i * 32 + (l & 3) * 8);
              }
            }
            if constexpr (s == FS0) {
              if constexpr (n == 0) qc = __hip_atomic_load(qcnt, __ATOMIC_RELAXED, __HIP_MEMORY_SCOPE_WORKGROUP);
              if constexpr (n == 1) {
                count_wait(0, 1);
                kv = *(const LDS_AS unsigned*)(smem + L_SKEY + 32);
#pragma unroll
                for (int i = 0; i < 4; ++i) fx[i] = *(const LDS_AS u32x4*)(smem + L_ROW + w * 256 + i * 64 + g * 16);
              }
              if constexpr (n == 2) {
                fkey = __builtin_amdgcn_readfirstlane(kv);
                sel0 = key_row(fkey);
                aff = (int)(sel0 >> 8) == p;
                asm volatile("" : "=v"(sa), "=v"(sb));
                if (ffn_on) ffn_gu(gv, uv, fx, sa, sb);
              }
              if constexpr (n == 4) {
                if (ffn_on) {
                  ffn_gu_store(smem, l, w, sa, sb);
                  count_in(1);
                }
              }
            }
            if constexpr (s == FS0D) {
              if constexpr (n == 0) qc = __hip_atomic_load(qcnt + 1, __ATOMIC_RELAXED, __HIP_MEMORY_SCOPE_WORKGROUP);
              if constexpr (n == 1) { asm volatile("" : "=v"(sa)); if (ffn_on) { count_wait(1, 8); sa = ffn_d_read(smem, l); } }
              if constexpr (n == 2) { asm volatile("" : "=v"(hh)); if (ffn_on) hh = ffn_d_h(sa); }
              if constexpr (n == 4) { asm volatile("" : "=v"(sa)); if (ffn_on) sa = ffn_d_all(dfr, hh); }
              if constexpr (n == 5) { if (ffn_on) ffn_d_store(rs, l, w, 0, slice, sa); STAMPAT(54); }
            }
            if constexpr (s == FS1 && n == 4) {
              DEF4(fx);
              asm volatile("" : "=v"(cxn), "=v"(cx1));
              if (!ffn_on) {
                const unsigned G = xcd * 4 + bi - 28;
#pragma unroll
                for (int k = 0; k < 4; ++k) ld16h<0>(fx[k], rs, RED_VO(k), RED_SO(0, G));
                ld8h(cxn, rs, RED_JJ(), cand_off(fkey, 0) + XO(G)); ld8h(cx1, rs, RED_JJ() + 128, cand_off(fkey, 0) + XO(G));
              }
            }
            if constexpr (s == FS1S && n == 2) {
              if (!ffn_on) {
                wait_vm<SINCE(FS1)>();
                PINV4(fx);
                asm volatile("" : "+v"(cxn), "+v"(cx1)::"memory");
                STAMPAT(55);
                red_sum(rs, smem, T, tid, w, 0, xcd * 4 + bi - 28, fkey, fx, cxn, cx1);
                STAMPAT(56);
                BAR();
                if (w == 0) {
                  const u32x2 ov = red_final(smem, l, fkey, cxn, cx1);
                  if (l < 8) st8(unpoison2(ov), rs, OFF_XNEW + (xcd * 4 + bi - 28) * 256 + ((l & 3) * 2 + (l >> 2)) * 8, 0);
                }
              }
            }
            if constexpr (s == FXA && n == 4) { asm volatile("" : "=v"(fx[0])); if (w == 1) ld16h<0>(fx[0], rs, ((l >> 2) & 3) * 256 + (l & 3) * 16, XP_OFF()); }
            if constexpr (s == FXB - 1 && n == FSN) {
              if (w == 1) {
                wait_vm<SINCE(FXA)>();
                asm volatile("" : "+v"(fx[0])::"memory");
                word_wait<1>(rs, T, ((l >> 2) & 3) * 256 + (l & 3) * 16, XP_OFF(), fx);
                if (l < 16) *(LDS_AS u32x4*)(smem + L_ROW + l * 16) = fx[0];
                count_in(0);
              }
            }
            if constexpr (s == FXB) {
#define FX_FRAG(i) (*(const LDS_AS u32x4*)(size_t)(bo[0][0] + (1 + PO()) * STAGE + (6 * (i) + w) * 1024))
              if constexpr (n == 0) { STAMPAT(58); qc = __hip_atomic_load(qcnt, __ATOMIC_RELAXED, __HIP_MEMORY_SCOPE_WORKGROUP); }
              if constexpr (n == 1) {
                DEF4(fx); DEF4(df);
                if (w < 6) {
                  count_wait(0, 2);
#pragma unroll
                  for (int i = 0; i < 4; ++i) { fx[i] = *(const LDS_AS u32x4*)(smem + L_ROW + i * 64 + g * 16); df[i] = FX_FRAG(i); }
                }
              }
              if constexpr (n == 2) { asm volatile("" : "=v"(sa), "=v"(sb)); if (w < 6) dot_mma(fx); }
              if constexpr (n == 4) { if (w < 6) dot_store(OFF_FX); STAMPAT(57); }
            }
            if constexpr (s == FXL && n == 4) {
              asm volatile("" : "=v"(fx[0]), "=v"(fx[1]));
              if (aff && w < 4) { ld16h<0>(fx[0], rs, FP_VO(), FP_OFF(0)); ld16h<0>(fx[1], rs, FP_VO(), FP_OFF(1)); }
            }
            if constexpr (s == FXS && n == 4) {
              if (aff && w < 4) {
                STAMPAT(59);
                wait_vm<0>();
                asm volatile("" : "+v"(fx[0]), "+v"(fx[1])::"memory");
                if (!FX_OK(2)) {
                  unsigned spins = 0;
                  do {
                    if (spin_fail(spins, rs, T)) break;
                    fx[0] = ld16(rs, FP_VO(), FP_OFF(0)); fx[1] = ld16(rs, FP_VO(), FP_OFF(1));
                  } while (!FX_OK(2));
                }
                STAMPAT(60);
                if (w == 3) {
                  f32x2 nx[8];
                  unpack8v(fx[0], nx); unpack8v(fx[1], nx + 4);
                  const f32x2 sm = ((nx[0] + nx[1]) + (nx[2] + nx[3])) + ((nx[4] + nx[5]) + (nx[6] + nx[7]));
                  const float mean = sum64(hadd(sm)) * (1.0f / 1024.0f);
                  const f32x2 m2 = {mean, mean};
                  f32x2 q = {0.f, 0.f};
#pragma unroll
                  for (int j = 0; j < 8; ++j) { const f32x2 d = nx[j] - m2; q = __builtin_elementwise_fma(d, d, q); }
                  const float rd = ln_rd(sum64(hadd(q)) * (1.0f / 1024.0f));
                  if (l == 0) *(LDS_AS f32x2*)(smem + L_FXL + 384) = f32x2{rd, -rbf1(mean)};
                } else {
                  tile_sum(fx[0], L_FXL);
                }
                count_in(3);
              }
            }
          }
        };
        static_for<32>([&](auto sc_) __attribute__((always_inline)) {
          constexpr int s = decltype(sc_)::value;
          if constexpr (s > 0) PIN_ACC();
          if constexpr (QPR && s == QPR) { if (w >= 4) __builtin_amdgcn_s_setprio(1); }
          if constexpr (IT == 1) wait_vm<vm_at(IT, s, 0) - vm_a(IT, s)>();
          pin_w<s % WA>(af[s % WA][0], af[s % WA][1]);
          VSTEP(s);
          SB();
          static_for<6>([&](auto nc_) __attribute__((always_inline)) {
            constexpr int n = decltype(nc_)::value;
            if constexpr (s == 0) { acc[0][n] = f32x4{0.f, 0.f, 0.f, 0.f}; acc[1][n] = acc[0][n]; }
            constexpr bool on = s < QL || n >= 2;
            if constexpr (on) acc[0][n] = n < 4 ? mfma16(bfr[n].v, af[s % WA][0], acc[0][n]) : mfma16(af[s % WA][0], bfr[n].v, acc[0][n]);
            SB();
            if constexpr (n == 0) { QKV_RDB(s, 5); }
            else if constexpr (s + 1 < 32 && (s + 1 < QL || n > 2)) QKV_RDB(s + 1, n - 1);
            SB();
            if constexpr (on) acc[1][n] = n < 4 ? mfma16(bfr[n].v, af[s % WA][1], acc[1][n]) : mfma16(af[s % WA][1], bfr[n].v, acc[1][n]);
            SB();
            if constexpr (s + 1 < 32 && (s + 1 < QL || n >= 2)) QKV_RDA(s + 1, n);
            SB();
            gap(sc_, nc_);
            GSTEP(s, n);
            SB();
          });
        });
        PIN_ACC();
        VSTEP(32);
        STAMPAT(18);
        VSTEP(33);
        frag_t qw[32 - QL][2];
        static_for<32 - QL>([&](auto c_) __attribute__((always_inline)) {
          constexpr int s = QL + decltype(c_)::value;
          QKV_RDA(s, 0); QKV_RDB(s, 0); QKV_RDA(s, 1); QKV_RDB(s, 1);
          qw[s - QL][0] = bfr[0]; qw[s - QL][1] = bfr[1];
        });
        SB();
        const bool own = aff && (int)((sel0 >> 5) & 7) == rt;
        const bool m0 = !(sel0 & 16), mq = (l & 15) == (int)(sel0 & 15), mv = g == (int)((sel0 & 15) >> 2);
        auto patch_qk = [&](int n) __attribute__((always_inline)) {
          f32x4 t = *(const LDS_AS f32x4*)(smem + L_FXL + n * 64 + g * 16);
          const f32x4 c = *(const LDS_AS f32x4*)(smem + L_CSL + n * 64 + g * 16);
          const float nb = *(const LDS_AS float*)(smem + L_FXL + 388);
#pragma unroll
          for (int e = 0; e < 4; ++e) { t[e] = __builtin_fmaf(c[e], nb, t[e]); asm volatile("" : "+v"(t[e])); }
          acc[0][n] = (mq && m0) ? t : acc[0][n];
          acc[1][n] = (mq && !m0) ? t : acc[1][n];
        };
        if (own) {
          while (__hip_atomic_load(qcnt + 3, __ATOMIC_RELAXED, __HIP_MEMORY_SCOPE_WORKGROUP) < 4u) {}
          asm volatile("" ::: "memory");
          patch_qk(2); patch_qk(3);
          const f32x2 nab = *(const LDS_AS f32x2*)(smem + L_FXL + 384);
          const LDS_AS float* fl_ = (const LDS_AS float*)(smem + L_FXL + 256) + (l & 15);
          const LDS_AS float* cl_ = (const LDS_AS float*)(smem + L_CSL + 256) + (l & 15);
          const float t4 = __builtin_fmaf(cl_[0], nab[1], fl_[0]), t5 = __builtin_fmaf(cl_[16], nab[1], fl_[16]);
          const unsigned na = __builtin_bit_cast(unsigned, (float)nab[0]);
#pragma unroll
          for (int a = 0; a < 2; ++a) {
            const bool ma = a ? !m0 : m0;
            aq[a] = (mq && ma) ? na : aq[a];
#pragma unroll
            for (int e = 0; e < 4; ++e) {
              const bool me = mv && ma && e == (int)(sel0 & 3);
              acc[a][4][e] = me ? t4 : acc[a][4][e]; acc[a][5][e] = me ? t5 : acc[a][5][e];
              av[a][e] = me ? na : av[a][e];
            }
          }
        }
        constexpr float C2 = 1.4426950408889634f * 0.17677669529663687f;
#pragma unroll
        for (int a = 0; a < 2; ++a) {
          f32x2 a2 = {asf(aq[a]), asf(aq[a])};
          qs2[a] = f32x2{asf(aq[a]) * C2, asf(aq[a]) * C2};
          asm volatile("" : "+v"(a2), "+v"(qs2[a]));
          acc[a][2] *= f32x4{a2[0], a2[1], a2[0], a2[1]}; acc[a][3] *= f32x4{a2[0], a2[1], a2[0], a2[1]};
          acc[a][4] *= __builtin_bit_cast(f32x4, av[a]); acc[a][5] *= __builtin_bit_cast(f32x4, av[a]);
        }
        k0 = pack8(acc[0][2], acc[0][3]); k1 = pack8(acc[1][2], acc[1][3]); v0 = pack8(acc[0][4], acc[1][4]); v1 = pack8(acc[0][5], acc[1][5]);
        const unsigned kvo = OFF_KV + IT * (8u << 20) + (p * 32 + h) * 32768 + rt * 4096;
        if (p < 7) { st16(k0, rs, l * 16, kvo); st16(k1, rs, l * 16 + 1024, kvo); st16(v0, rs, l * 16 + 2048, kvo); st16(v1, rs, l * 16 + 3072, kvo); }
        char* ownp = smem + (8 + rt) * 2048 + l * 16;
        *(u32x4*)(ownp + L_K) = k0;
        if (rt < 7) *(u32x4*)(ownp + L_K + 1024) = k1;
        *(u32x4*)(ownp + L_V) = v0; *(u32x4*)(ownp + L_V + 1024) = v1;
        SB();
        static_for<32 - QL>([&](auto c_) __attribute__((always_inline)) {
          constexpr int s = QL + decltype(c_)::value;
#pragma unroll
          for (int n = 0; n < 2; ++n) { acc[0][n] = mfma16(qw[s - QL][n].v, af[s % WA][0], acc[0][n]); acc[1][n] = mfma16(qw[s - QL][n].v, af[s % WA][1], acc[1][n]); }
        });
        if (own) { patch_qk(0); patch_qk(1); }
      };
      qkv_loop(it_c);
    }
#undef A_LOAD1
#undef QKV_RDA
#undef QKV_RDB
#undef QKV_BO
    STAMPAT(2); STAMPAT(21);
    {
      IDS();
      const GAS bf16 *pwo, *pwg, *pwu;
      asm volatile("s_load_dwordx2 %0, %3, 24\n\ts_load_dwordx2 %1, %3, 40\n\ts_load_dwordx2 %2, %3, 48" : "=&s"(pwo), "=&s"(pwg), "=&s"(pwu) : "s"(ka));
      const int ql = l & 15;
      const f32x4 z4 = {0.f, 0.f, 0.f, 0.f}, ninf = {-INFINITY, -INFINITY, -INFINITY, -INFINITY};
      LDS_AS unsigned* rdy = (LDS_AS unsigned*)(smem + L_RDY);
      const unsigned rtag = T[0] + it;
      u32x4 qf[2];
      f32x4 cn[2], oa[2][2], sum[2], S[2][2];
      bool told = false;
      auto announce = [&](bool now) __attribute__((always_inline)) {
        if (!told) {
          unsigned st;
          asm volatile("s_getreg_b32 %0, hwreg(HW_REG_IB_STS)" : "=s"(st)::"memory");
          if (now || !(st & 0xC0000Fu)) {
            DRAIN();
            if (l == 0) st8(T, rs, rt * SKW, F_KV + (it * 256 + p * 32 + h) * SKV);
            told = true;
            STAMPAT(34);
          }
        }
      };
      unsigned one2 = 0x3F803F80u;
      asm volatile("" : "+v"(one2));
      const u32x4 ones = {one2, one2, one2, one2};
      auto ex8 = [&](f32x4 a, f32x4 b) __attribute__((always_inline)) {
        return u32x4{pack2(__builtin_amdgcn_exp2f(a[0]), __builtin_amdgcn_exp2f(a[1])), pack2(__builtin_amdgcn_exp2f(a[2]), __builtin_amdgcn_exp2f(a[3])),
                     pack2(__builtin_amdgcn_exp2f(b[0]), __builtin_amdgcn_exp2f(b[1])), pack2(__builtin_amdgcn_exp2f(b[2]), __builtin_amdgcn_exp2f(b[3]))};
      };
      {
#pragma unroll
        for (int a = 0; a < 2; ++a) {
          const f32x4 c4 = {qs2[a][0], qs2[a][1], qs2[a][0], qs2[a][1]};
          qf[a] = pack8(acc[a][0] * c4, acc[a][1] * c4);
        }
        f32x4 mk;
#pragma unroll
        for (int e = 0; e < 4; ++e) mk[e] = (4 * g + e <= ql) ? 0.f : -INFINITY;
        const f32x4 d0 = mfma16(k0, qf[0], mk), d1 = mfma16(k1, qf[1], mk), d2 = mfma16(k0, qf[1], z4);
        const float m0 = -max_x32(max_x16(fmaxf(fmaxf(d0[0], d0[1]), fmaxf(d0[2], d0[3]))));
        const float m1 = -max_x32(max_x16(fmaxf(fmaxf(d1[0], d1[1]), fmaxf(d1[2], d1[3]))));
        cn[0] = f32x4{m0, m0, m0, m0}; cn[1] = f32x4{m1, m1, m1, m1};
        asm volatile("" : "+v"(cn[0]), "+v"(cn[1]));
        announce(false);
        const f32x4 e0 = d0 + cn[0];
        const u32x4 p0 = {pack2(__builtin_amdgcn_exp2f(e0[0]), __builtin_amdgcn_exp2f(e0[1])), pack2(__builtin_amdgcn_exp2f(e0[2]), __builtin_amdgcn_exp2f(e0[3])), 0u, 0u};
        const u32x4 p1 = ex8(d2 + cn[1], d1 + cn[1]);
        oa[0][0] = mfma16(v0, p0, z4); oa[0][1] = mfma16(v1, p0, z4); sum[0] = mfma16(ones, p0, z4);
        oa[1][0] = mfma16(v0, p1, z4); oa[1][1] = mfma16(v1, p1, z4); sum[1] = mfma16(ones, p1, z4);
      }
      const char* kb_ = smem + L_K + l * 16;
      const char* vb_ = smem + L_V + l * 16;
      const unsigned wov = WO_VOFF(), fnv = (l >> 1) * (F_ * 2) + (l & 1) * 16;
      asm volatile("" : "+v"(oa[0][0]), "+v"(oa[0][1]), "+v"(oa[1][0]), "+v"(oa[1][1]), "+v"(sum[0]), "+v"(sum[1]));
      const unsigned pw = __builtin_amdgcn_readfirstlane(it * 256 + p * 32 + h - 32), pfo = F_KV + pw * SKV + rt * SKW;
      const bool fetcher = rt < 4 && p > 0;
      announce(false);
      if (QPR) __builtin_amdgcn_s_setprio(0);
      STAMPAT(32);
      BAR();
      asm volatile("" : "+s"(pwo), "+s"(pwg), "+s"(pwu));
      STAMPAT(33);
#define KV_FETCH(gd)                                                                                                          \
  {                                                                                                                           \
    const unsigned pvo = OFF_KV + pw * 32768 + (gd) * 4096;                                                                   \
    dma16s<0>(rs, l * 16, pvo, lbase + L_K + (gd) * 2048); dma16s<1024>(rs, l * 16, pvo, lbase + L_K + (gd) * 2048);           \
    dma16s<0>(rs, l * 16, pvo + 2048, lbase + L_V + (gd) * 2048); dma16s<1024>(rs, l * 16, pvo + 2048, lbase + L_V + (gd) * 2048); \
  }
      auto fetch = [&]() __attribute__((always_inline)) {
        poll_flags(rs, KVJ ? (l & 1) * 4 * SKW : 0, pfo, T);
        KV_FETCH(rt);
        if (KVJ) { KV_FETCH(rt + 4); if (l == 0) rdy[8 + rt] = rtag; }
        STAMPAT(35);
      };
      announce(false);
      const bool first = fetcher && told;
      if (first) fetch();
#define LDS16(a_) (*(const LDS_AS u32x4*)(size_t)(a_))
      u32x4 kf[2], vf[2], p0, p1;
#define AE(a, i) { S[a][(i) >> 2][(i) & 3] = __builtin_amdgcn_exp2f(S[a][(i) >> 2][(i) & 3]); asm volatile("" : "+v"(S[a][(i) >> 2][(i) & 3])); SB(); }
#define AC(a, P, j) { P[j] = pack2(S[a][(j) >> 1][2 * ((j) & 1)], S[a][(j) >> 1][2 * ((j) & 1) + 1]); asm volatile("" : "+v"(P[j])); SB(); }
#define PHASE(a, P, M1, M2, L2, M3, M4, L4, M5)                                                                               \
  AE(a, 0) AE(a, 1) M1; SB(); AE(a, 2) AE(a, 3) M2; SB(); L2; SB(); AC(a, P, 0) AC(a, P, 1) AE(a, 4) M3; SB(); AE(a, 5) AE(a, 6) M4; SB(); \
  L4; SB(); AE(a, 7) AC(a, P, 2) M5; SB(); AC(a, P, 3)
#define PV(a, d, P) oa[a][d] = mfma16(vf[d], P, oa[a][d])
#define RS(a, P) sum[a] = mfma16(ones, P, sum[a])
#define SC(a, t, c) S[a][t] = mfma16(kf[t], qf[a], c)
#define K_AT(ad) (kf[0] = LDS16(ad), kf[1] = LDS16((ad) + 1024))
#define V_AT(ad) (vf[0] = LDS16((ad) + L_V), vf[1] = LDS16((ad) + L_V + 1024))
#define A_FIRST(ad, c00, c01, c10, c11, KN) { K_AT(ad); V_AT(ad); SB(); SC(0, 0, c00); SC(0, 1, c01); SB(); PHASE(0, p0, SC(1, 0, c10), SC(1, 1, c11), KN, , , , ) }
#define A_NEXT(ad, c00, c01, c10, c11, KN)                                                                                        \
  { PHASE(1, p1, PV(0, 0, p0), PV(0, 1, p0), , SC(0, 0, c00), SC(0, 1, c01), , RS(0, p0))                                     \
    PHASE(0, p0, PV(1, 0, p1), PV(1, 1, p1), V_AT(ad), SC(1, 0, c10), SC(1, 1, c11), KN, RS(1, p1)) }
#define A_LAST() { PHASE(1, p1, PV(0, 0, p0), PV(0, 1, p0), , RS(0, p0), , , ) SB(); PV(1, 0, p1); PV(1, 1, p1); RS(1, p1); }
      int nw = 0;
      auto piece = [&]() __attribute__((always_inline)) {
        if constexpr (it == 0) { if (ffn_on && nw < 8) { FFN_DMA(nw); } ++nw; }
      };
      unsigned ad = lbase + l * 16 + (6 + rt) * 2048;
      asm volatile("" : "+v"(ad));
      if (rt > 0) {
        A_FIRST(ad + 2048, cn[0], cn[0], cn[1], cn[1], K_AT(ad));
#pragma unroll 1
        for (int i = rt - 1; i > 0; --i) {
          ad -= 2048;
          asm volatile("" : "+v"(ad));
          if (PCE && told && rt >= 4) piece();
          announce(false);
          A_NEXT(ad + 2048, cn[0], cn[0], cn[1], cn[1], K_AT(ad));
        }
        announce(false);
        A_LAST();
      }
      announce(true);
      STAMPAT(26);
      {
        f32x4 mk;
#pragma unroll
        for (int e = 0; e < 4; ++e) mk[e] = (4 * g + e > ql) ? 0.f : -INFINITY;
        f32x4 cm0 = cn[0] + mk, cm1 = cn[1] + mk, cmi = ninf;
        asm volatile("" : "+v"(cm0), "+v"(cm1), "+v"(cmi));
        if (rt >= 4) {
          if (p > 0) {
            while (!__all(__hip_atomic_load(rdy + 8 + (l & 3), __ATOMIC_RELAXED, __HIP_MEMORY_SCOPE_WORKGROUP) == rtag)) __builtin_amdgcn_s_sleep(1);
            asm volatile("" ::: "memory");
          }
          if constexpr (it == 0) { for (; nw < 8;) piece(); }
          static_for<it ? 12 : 8>([&](auto c_) __attribute__((always_inline)) { constexpr int i = decltype(c_)::value; WO_PIECE_W(15 - i / 2, 2 * (rt & 3) + (i & 1)); });
        } else if (p > 0) {
          if (!first) fetch();
          if (KVJ) {
            DRAIN();
            if (l < 2) rdy[rt + 4 * l] = rtag;
            if constexpr (it == 0) { for (; nw <= rt;) piece(); }
          } else {
            const u32x2 f2 = ld8(rs, 0, pfo + 4 * SKW);
            const bool up = __all(f2[0] == T[0] && f2[1] == T[1]);
            DRAIN();
            if (l == 0) rdy[rt] = rtag;
            if (!up) poll_flags(rs, 0, pfo + 4 * SKW, T);
            KV_FETCH(rt + 4);
            if (l == 0) rdy[8 + rt] = rtag;
            told = false;
          }
        }
        STAMPAT(3);
        if (p > 0) {
          auto landed = [&](bool now) __attribute__((always_inline)) {
            if (!told) {
              unsigned st;
              asm volatile("s_getreg_b32 %0, hwreg(HW_REG_IB_STS)" : "=s"(st)::"memory");
              if (now || !(st & 0xC0000Fu)) {
                DRAIN();
                if (l == 0) rdy[rt + 4] = rtag;
                told = true;
              }
            }
          };
          unsigned have = 0;
          auto need = [&](int gq) __attribute__((always_inline)) {
            if (__builtin_expect(gq < 8 && !((have >> gq) & 1), 0)) {
              do {
                const unsigned t = __hip_atomic_load(rdy + (l & 7), __ATOMIC_RELAXED, __HIP_MEMORY_SCOPE_WORKGROUP);
                have = (unsigned)__builtin_amdgcn_ballot_w64(t == rtag);
              } while (!((have >> gq) & 1));
              asm volatile("" ::: "memory");
            }
          };
          ad = lbase + l * 16 + rt * 2048;
          asm volatile("" : "+v"(ad));
          need(rt);
          K_AT(ad); V_AT(ad); SB(); SC(0, 0, cm0); SC(0, 1, cn[0]); SB();
          PHASE(0, p0, SC(1, 0, cmi), SC(1, 1, cm1), , , , , )
          if (!KVJ) landed(false);
          need(rt + 1);
          K_AT(ad + 2048);
#pragma unroll 1
          for (int gq = rt + 1; gq < 8; ++gq) {
            ad += 2048;
            asm volatile("" : "+v"(ad));
            if (KVJ) piece();
            else { landed(gq >= rt + KVD); if (told) { piece(); if (PC2) piece(); } }
            need(gq + 1);
            A_NEXT(ad, cn[0], cn[0], cn[1], cn[1], K_AT(ad + 2048));
          }
          A_LAST();
        }
        if constexpr (it == 0) { for (; nw < 8;) piece(); }
      }
      STAMPAT(25);
      {
        float big = 0.f;
#pragma unroll
        for (int a = 0; a < 2; ++a)
#pragma unroll
          for (int d = 0; d < 2; ++d)
            big = __builtin_elementwise_maximum(__builtin_elementwise_maximum(big, __builtin_elementwise_maximum(fabsf(oa[a][d][0]), fabsf(oa[a][d][1]))),
                                                __builtin_elementwise_maximum(fabsf(oa[a][d][2]), fabsf(oa[a][d][3])));
        bool ok = sum[0][0] >= 0.5f && sum[0][0] < 0x1p64f && sum[1][0] >= 0.5f && sum[1][0] < 0x1p64f && big < INFINITY;
        if (!__all(ok)) {
#pragma unroll
          for (int a = 0; a < 2; ++a) {
            const int qs = 2 * rt + a, qq = 256 + 16 * qs + ql, klo = max(qq - 255, p > 0 ? 0 : 256);
            auto score = [&](int t) __attribute__((always_inline)) {
              f32x4 s = mfma16(t == 31 ? k1 : *(const u32x4*)(kb_ + t * 1024), qf[a], z4);
#pragma unroll
              for (int e = 0; e < 4; ++e) { const int kk = 16 * t + 4 * g + e; s[e] = (kk <= qq && kk >= klo) ? s[e] : -INFINITY; }
              return s;
            };
            float mx = -INFINITY;
#pragma unroll 1
            for (int t = p > 0 ? qs : 16; t <= qs + 16; ++t) { const f32x4 s = score(t); mx = fmaxf(fmaxf(mx, fmaxf(s[0], s[1])), fmaxf(s[2], s[3])); }
            mx = max_x32(max_x16(mx));
            oa[a][0] = z4; oa[a][1] = z4; sum[a] = z4;
#pragma unroll 1
            for (int G = p > 0 ? qs >> 1 : 8; G <= (qs + 16) >> 1; ++G) {
              const f32x4 s0 = score(2 * G), s1 = score(2 * G + 1);
              f32x4 e0, e1;
#pragma unroll
              for (int e = 0; e < 4; ++e) { e0[e] = __builtin_amdgcn_exp2f(s0[e] - mx); e1[e] = __builtin_amdgcn_exp2f(s1[e] - mx); }
              const u32x4 pf = pack8(e0, e1);
              oa[a][0] = mfma16(*(const u32x4*)(vb_ + G * 2048), pf, oa[a][0]);
              oa[a][1] = mfma16(*(const u32x4*)(vb_ + G * 2048 + 1024), pf, oa[a][1]);
              sum[a] = mfma16(ones, pf, sum[a]);
            }
          }
        }
      }
#pragma unroll
      for (int a = 0; a < 2; ++a) {
        const float inv = __builtin_amdgcn_rcpf(sum[a][0]);
        oa[a][0] *= inv; oa[a][1] *= inv;
      }
      const unsigned oo = OFF_O + it * (4u << 20) + ((p * 8 + rt) * 64 + h * 2) * 1024;
      st16(pack8(oa[0][0], oa[0][1]), rs, l * 16, oo);
      st16(pack8(oa[1][0], oa[1][1]), rs, l * 16 + 1024, oo);
      STAMPAT(4);
      DRAIN();
      BAR();
      if (tid == 0) st8(T, rs, h * SO, F_O + (it * 8 + p) * 32 * SO);
      if constexpr (it == 0) {
        DEF4(gv); DEF4(uv);
        if (ffn_on) {
          const unsigned ro = 65536 + w * 1024 + (4 * g + ((l & 15) >> 2)) * 32 + (l & 3) * 8;
#pragma unroll
          for (int i = 0; i < 4; ++i) {
            frag_t f;
            f.hf.a = trread(smem, ro + i * 16384); f.hf.b = trread(smem, ro + i * 16384 + 512);
            gv[i] = f.v;
            f.hf.a = trread(smem, ro + i * 16384 + 8192); f.hf.b = trread(smem, ro + i * 16384 + 8704);
            uv[i] = f.v;
          }
        }
        PINV4(gv); PINV4(uv);
        WO_PIECE(11); WO_PIECE(10); WO_PIECE(9); WO_PIECE(8); WO_PIECE(7); WO_PIECE(6); WO_PIECE(5); WO_PIECE(4);
      }
      WO_PIECE(3); WO_PIECE(2); WO_PIECE(1); WO_PIECE(0);
    }
    STAMPAT(5);
    IDS();
    ARGP(pwr, 4); ARGP(pwd, 7); ARGP(pout, 8); ARGP(px, 0);
    const int kh = w >> 2, rt2 = rb * 4 + (w & 3);
    const unsigned R = rt2 * 32 + kh * 16 + (l & 15);
    const unsigned to = (rt2 * 64 + ct * 4 + kh) * 1024 + l * 16;
    const unsigned rmo = ct * 64 + ((g & 1) ? 12 + 4 * g : 4 * g);
    f32x2 wv[8];
    {
      u32x4 wa[2], wrp[2], res[2];
#pragma unroll
      for (int j = 0; j < 2; ++j) {
        wrp[j] = *(const GAS u32x4*)(pwr + rmo + 32 * j);
        wa[j] = *(const GAS u32x4*)(pwr + l * 8 + j * 512);
        if constexpr (it == 0) res[j] = *(const GAS u32x4*)(px + R * 1024 + rmo + 32 * j);
        else res[j] = ld16(rs, OFF_XNEW + (2 * ct + j) * 256 + g * 16, 0);
      }
      {
        f32x2 t[8];
        unpack8v(wa[0], t); unpack8v(wa[1], t + 4);
        const f32x2 s = ((t[0] + t[1]) + (t[2] + t[3])) + ((t[4] + t[5]) + (t[6] + t[7]));
        wsum = sum64(hadd(s));
        unpack8v(trade16(wrp[0]), wv); unpack8v(trade16(wrp[1]), wv + 4);
        if constexpr (it == 0) { res[0] = trade16(res[0]); res[1] = trade16(res[1]); }
        else { res[0] = R == sel0 ? res[0] : x2p[0]; res[1] = R == sel0 ? res[1] : x2p[1]; }
        unpack8v(res[0], v + 0); unpack8v(res[1], v + 4);
        asm volatile("" : "+v"(v[0]), "+v"(v[1]), "+v"(v[2]), "+v"(v[3]), "+v"(v[4]), "+v"(v[5]), "+v"(v[6]), "+v"(v[7]));
        asm volatile("" : "+v"(wsum), "+v"(wv[0]), "+v"(wv[1]), "+v"(wv[2]), "+v"(wv[3]), "+v"(wv[4]), "+v"(wv[5]), "+v"(wv[6]), "+v"(wv[7]));
      }
      unsigned bt[4];
#pragma unroll
      for (int n = 0; n < 4; ++n) {
        bt[n] = lbase + 32768 + 65536 + kh * 4096 + (4 * g + ((l & 15) >> 2)) * 128 + (l & 3) * 8 + ((n ^ (2 * (g & 1) + ((l & 15) >> 3))) << 5);
        asm volatile("" : "+v"(bt[n]));
      }
      if (w == 0) poll_flags(rs, (l & 31) * SO, F_O + (it * 8 + (rb >> 1)) * 32 * SO, T);
      else if (it == 0 && w < 3) poll_flags2(rs, F_XN + (((w - 1) * 4 + g) * RX + bi % RX) * 256 + (l & 15) * 16, T);
      asm volatile("s_barrier" ::: "memory");
      STAMPAT(6);
      const unsigned abase = OFF_O + it * (4u << 20) + (rt2 * 64 + 32 * kh) * 1024;
      const unsigned av0 = l * 16 + kh * 1024, av1 = l * 16 + (kh ^ 1) * 1024;
      u32x4 af[WB][2];
#define O_LOAD(s_) { af[(s_) % WB][0] = ld16(rs, av0 + (~(s_) & 1) * 2048, abase + ((15 - (s_)) >> 1) * 4096); af[(s_) % WB][1] = ld16(rs, av1 + (~(s_) & 1) * 2048, abase + ((15 - (s_)) >> 1) * 4096); }
#pragma unroll
      for (int s = 0; s < WR; ++s) O_LOAD(s);
      SB();
      f32x4 ac[2][4];
#pragma unroll
      for (int a = 0; a < 2; ++a)
#pragma unroll
        for (int n = 0; n < 4; ++n) ac[a][n] = f32x4{0.f, 0.f, 0.f, 0.f};
      u32x4 bfr[WLA + 1][4];
#define WO_RD(buf, s, n)                                                                             \
  {                                                                                                  \
    const unsigned o_ = bt[n] + ((15 - (s)) & 7) * 8192;     \
    frag_t f_;                                                                                       \
    f_.hf.a = trabs(o_); f_.hf.b = trabs(o_ + 2048);                                                 \
    bfr[buf][n] = f_.v;                                                                              \
  }
#pragma unroll
      for (int n = 0; n < 4 * WLA; ++n) WO_RD(n >> 2, n >> 2, n & 3);
#pragma unroll
      for (int s = 0; s < 16; ++s) {
        const int cur = s % (WLA + 1);
        if (WPR && s == WPR && w >= 4) __builtin_amdgcn_s_setprio(1);
        asm volatile("" : "+{v[176:179]}"(ac[0][0]), "+{v[180:183]}"(ac[0][1]), "+{v[184:187]}"(ac[0][2]), "+{v[188:191]}"(ac[0][3]),
                          "+{v[192:195]}"(ac[1][0]), "+{v[196:199]}"(ac[1][1]), "+{v[200:203]}"(ac[1][2]), "+{v[204:207]}"(ac[1][3]));
        if (s == (it == 0 ? 4 : 12) - WLA) asm volatile("s_barrier" ::: "memory");
        if (s == 8 - WLA) {
#pragma unroll
          for (int n = 0; n < 4; ++n) { bt[n] -= 65536; asm volatile("" : "+v"(bt[n])); }
        }
        if (it == 0 && s == WIS) {
#pragma unroll
          for (int i = 0; i < 3; ++i) {
            if (PO()) st16(keep[3 + i], rs, l * 16 + i * 1024, OFF_WI + (h * 8 + ((ROT0 + 6) & 7)) * STAGE + w * 3072);
            else st16(keep[i], rs, l * 16 + i * 1024, OFF_WI + (h * 8 + ((ROT0 + 5) & 7)) * STAGE + w * 3072);
          }
        }
        if (it == 0 && s == WPS) {
          unsigned pz1 = POISON;
          asm volatile("" : "+v"(pz1));
          const u32x4 pz = {pz1, pz1, pz1, pz1};
          st16(pz, rs, OFF_PART + (tid >> 8) * (1u << 20) + bid * 4096 + (tid & 255) * 16, 0);
          if (w < 2) st16(pz, rs, OFF_STAT + 524288 + (rb * 16 + ct) * 2048 + tid * 16, 0);
          if (w == 4 && l < 40) st16(pz, rs, l < 16 ? OFF_CA + 65536 + rb * 4096 + ct * 256 + l * 16 : OFF_FX + (h * 8 + p) * 512 + (l - 16) * 16, 0);
          if (w == 6 && l < (ct < (int)RKEY ? 3 : 2)) st4(pz1, rs, l < 2 ? OFF_XNEW + (bid >> 3) * 256 + (bid & 7) * 8 + l * 4 : KEY_AT(1, ct) + rb * SKEY, 0);
        }
        if (s < WB - WR) O_LOAD(WR + 2 * s < 16 ? WR + 2 * s : 0);
        SB();
#pragma unroll
        for (int n = 0; n < 4; ++n) {
          ac[0][n] = mfma16(bfr[cur][n], af[s % WB][0], ac[0][n]);
          ac[1][n] = mfma16(bfr[cur][n], af[s % WB][1], ac[1][n]);
          if (s + WLA < 16) WO_RD((s + WLA) % (WLA + 1), s + WLA, n);
          SB();
        }
        if (s < WB - WR) { O_LOAD(WR + 2 * s + 1); }
        else if (s + WB < 16) { O_LOAD(s + WB); }
        SB();
      }
      if (WPR) __builtin_amdgcn_s_setprio(0);
#undef WO_RD
#undef O_LOAD
      STAMPAT(7);
#define XCH_AT(v) ((v) == 7 ? 155648 : (v) * 4096)
#pragma unroll
      for (int n = 0; n < 4; ++n) *(f32x4*)(smem + XCH_AT(w) + n * 1024 + l * 16) = ac[1][n];
#pragma unroll
      for (int n = 0; n < 4; ++n) { v[2 * n] += f32x2{ac[0][n][0], ac[0][n][1]}; v[2 * n + 1] += f32x2{ac[0][n][2], ac[0][n][3]}; }
      BAR();
#pragma unroll
      for (int n = 0; n < 4; ++n) {
        const f32x4 o = *(const f32x4*)(smem + XCH_AT(w ^ 4) + n * 1024 + l * 16);
        v[2 * n] += f32x2{o[0], o[1]}; v[2 * n + 1] += f32x2{o[2], o[3]};
      }
    }
    unsigned crow = 0;
    {
      if (it == 0) { st16(pack8v(v), rs, to, OFF_XN + (4u << 20)); st16(pack8v(v + 4), rs, to + 2048, OFF_XN + (4u << 20)); }
      SB();
      const f32x2 s = ((v[0] + v[1]) + (v[2] + v[3])) + ((v[4] + v[5]) + (v[6] + v[7]));
      f32x2 dp = v[0] * wv[0], q = v[0] * v[0];
#pragma unroll
      for (int j = 1; j < 8; ++j) { dp = __builtin_elementwise_fma(v[j], wv[j], dp); q = __builtin_elementwise_fma(v[j], v[j], q); }
      float mt = hadd(s), qq = hadd(q), dot = hadd(dp);
      add_g3(mt, qq, dot);
      const unsigned so = OFF_STAT + it * 524288 + rb * 32768, lrow = R & 127;
      if (it == 0) wait_vm<2>();
      if (g == 0) st16(unpoison4(__builtin_bit_cast(u32x4, f32x4{mt, qq, dot, 0.f})), rs, ct * 2048 + lrow * 16, so);
      SB();
      if (it == 0) wait_vm<1>();
      if constexpr (it == 0) {
        volatile LDS_AS u32x2* in = (volatile LDS_AS u32x2*)(smem + L_XC);
        if (l == 0) in[w] = T;
        const u32x2 t = in[l & 7];
        if (__all(t[0] == T[0] && t[1] == T[1]) && l < (int)RX1) st8(T, rs, (l * 32 + (rb & 1) * 16 + ct) * SX1, F_X1 + (rb >> 1) * RX1 * 32 * SX1);
      }
      STAMPAT(8);
      if (it == 0) {
        if (ffn_on) { WD_DMA(); }
#pragma unroll
        for (int j = 0; j < 9; ++j) *(u32x4*)(smem + L_RING + (j / 3) * STAGE + (w * 3 + j % 3) * 1024 + l * 16) = keep[j];
      }
      STAMPAT(16);
      if constexpr (it == 0) {
        if (w == 0) poll_flags(rs, ((l & 2) * 8 + ((ROT0 + 5) & 7) * 2 + (l & 1)) * SX1, F_X1 + (p * RX1 + (xcd >> 1) % RX1) * 32 * SX1, T);
        if (tid == 192) *(LDS_AS u32x4*)(smem + L_QC) = u32x4{0u, 0u, 0u, 0u};
        BAR();
        PRE_LOAD(1, ROT0 + 5, 0, NPRE);
        STAMPAT(9);
      } else {
        __builtin_amdgcn_s_sleep(SDIR);
        u32x4 sp[4];
#pragma unroll
        for (int i = 0; i < 4; ++i) sp[i] = ld16(rs, (4 * g + i) * 2048 + lrow * 16, so);
        const RowFin r = fin_key(rs, T, g * 8192 + lrow * 16, so, smem + L_XC, l, w, R, wsum, sp);
        STAMPAT(9);
        BAR();
        crow = fin_cand(rs, smem + L_XC, 1, g, rb, ct, r, v);
        STAMPAT(10);
        fin_x2(r, v);
      }
    }
    STAMPAT(11);
    if (it == 1) {
      u32x4 op[2];
      {
        char* tp = smem + L_OUT + w * 2048;
        const unsigned c0 = (g & 1) * 2 + (g >> 1), r = l & 15;
        *(u32x4*)(tp + r * 128 + ((c0 ^ (r & 7)) << 4)) = trade16(pack8v(v));
        *(u32x4*)(tp + r * 128 + (((c0 + 4) ^ (r & 7)) << 4)) = trade16(pack8v(v + 4));
#pragma unroll
        for (int j = 0; j < 2; ++j) op[j] = *(const u32x4*)(tp + j * 1024 + (l >> 3) * 128 + (((l ^ (l >> 3)) & 7) << 4));
      }
      const unsigned Ro = R - (l & 15) + (l >> 3), oo = (Ro * 1024 + ct * 64) * 2 + (l & 7) * 16;
#define OUT_STORE(c0, c1)                                                                           \
  {                                                                                                 \
    if (c0) __builtin_amdgcn_raw_buffer_store_b128(op[0], rout, oo, 0, OAUX);                        \
    if (c1) __builtin_amdgcn_raw_buffer_store_b128(op[1], rout, oo + 16384, 0, OAUX);                \
  }
      if (OUTE) OUT_STORE(Ro != crow, Ro + 8 != crow);
      if (w == 0) {
        __builtin_amdgcn_s_sleep(GSL);
        u32x4 xr[4];
        unsigned cur = 0, best;
        asm volatile("" : "=v"(xr[0]), "=v"(xr[1]));
        for (unsigned spins = 0;;) {
          const unsigned k_ = key_load(rs, l, 1, KREP());
          const bool all = __all(k_ != POISON);
          best = __builtin_amdgcn_readfirstlane(umax16(k_ == POISON ? 0u : k_));
          if (best != cur && ffn_on) {
            cur = best;
            ld16h<0>(xr[0], rs, (l >> 3) * 256 + (l & 7) * 16, cand_off(cur, 1)); ld16h<2048>(xr[1], rs, (l >> 3) * 256 + (l & 7) * 16, cand_off(cur, 1));
          }
          if (all || spin_fail(spins, rs, T)) break;
        }
        STAMPAT(17);
        if (l == 0) *(LDS_AS unsigned*)(smem + L_SKEY + 32) = best;
        asm volatile("s_waitcnt vmcnt(0)" : "+v"(xr[0]), "+v"(xr[1])::"memory");
        if (ffn_on) {
          word_wait<2>(rs, T, (l >> 3) * 256 + (l & 7) * 16, cand_off(best, 1), xr);
          *(LDS_AS u32x4*)(smem + L_ROW + l * 16) = xr[0]; *(LDS_AS u32x4*)(smem + L_ROW + 1024 + l * 16) = xr[1];
        }
      }
      BAR();
      const unsigned key = __builtin_amdgcn_readfirstlane(*(LDS_AS unsigned*)(smem + L_SKEY + 32)), krow = key_row(key);
      STAMPAT(12);
      if (ffn_on) {
        u32x4 xv[4];
#pragma unroll
        for (int i = 0; i < 4; ++i) xv[i] = *(const LDS_AS u32x4*)(smem + L_ROW + w * 256 + i * 64 + g * 16);
        f32x4 ga, ua;
        ffn_gu(gv, uv, xv, ga, ua);
        ffn_gu_store(smem, l, w, ga, ua);
        BAR();
        ffn_d_store(rs, l, w, 1, slice, ffn_d_all(dfr, ffn_d_h(ffn_d_read(smem, l))));
        STAMPAT(13);
        SB();
        if (OSL) __builtin_amdgcn_s_sleep(OSL);
      }
      if (OUTE) OUT_STORE(Ro == crow && Ro != krow, Ro + 8 == crow && Ro + 8 != krow)
      else OUT_STORE(Ro != krow, Ro + 8 != krow);
      if (!ffn_on) {
        const unsigned G = xcd * 4 + bi - 28;
        if (G == 0 && tid == 0) st8(T, rs, F_EP, 0);
        u32x4 pv[4];
        u32x2 cxn, cx1;
        __builtin_amdgcn_s_sleep(RSL);
        red_load(rs, tid, 1, G, key, pv, cxn, cx1);
        red_sum(rs, smem, T, tid, w, 1, G, key, pv, cxn, cx1);
        STAMPAT(14);
        BAR();
        if (w == 0) {
          const u32x2 ov = red_final(smem, l, key, cxn, cx1);
          if (l < 8) __builtin_amdgcn_raw_buffer_store_b64(ov, rout, (krow * 1024 + G * 32 + l * 4) * 2, 0, 0);
        }
      }
#undef OUT_STORE
    }
    STAMPAT(15); STAMPAT(23);
  };
  layer(std::integral_constant<int, 0>{});
  layer(std::integral_constant<int, 1>{});
}

extern "C" void kernel_launch(void* const* d_in, const int* in_sizes, int n_in, void* d_out, int out_size,
                              void* d_ws, size_t ws_size, hipStream_t stream) {
  (void)hipFuncSetAttribute((const void*)mega_kernel, hipFuncAttributeMaxDynamicSharedMemorySize, LDS_BYTES);
  mega_kernel<<<256, 512, LDS_BYTES, stream>>>((const bf16*)d_in[0], (char*)d_ws, (const bf16*)d_in[1], (const bf16*)d_in[2], (const bf16*)d_in[4],
                                               (const bf16*)d_in[5], (const bf16*)d_in[6], (const bf16*)d_in[7], (bf16*)d_out);
}
```

```cpp
#define WO2 0,6,4,1,8
#include <hip/hip_runtime.h>
#include <stdint.h>
#include <utility>

typedef __bf16 bf16;
typedef __bf16 bf16x8 __attribute__((ext_vector_type(8)));
typedef __bf16 bf16x2 __attribute__((ext_vector_type(2)));
typedef float f32x4 __attribute__((ext_vector_type(4)));
typedef float f32x2 __attribute__((ext_vector_type(2)));
typedef short s16x4 __attribute__((ext_vector_type(4)));
typedef unsigned int u32x4 __attribute__((ext_vector_type(4)));
typedef unsigned int u32x2 __attribute__((ext_vector_type(2)));
#define LDS_AS __attribute__((address_space(3)))
#define GAS __attribute__((address_space(1)))
typedef GAS unsigned gu32;
typedef GAS unsigned long long gu64;
#define RLX_AGENT __ATOMIC_RELAXED, __HIP_MEMORY_SCOPE_AGENT
typedef __amdgpu_buffer_rsrc_t rsrc_t;

constexpr int F_ = 3584;
constexpr int SC1 = 16;

constexpr int RX = 4;
constexpr unsigned F_EP = 0, F_TMO = 128;
constexpr unsigned F_XN = 4096;
#define FLG 256, 1024, 8, 64, 4, 256, 4
constexpr unsigned FLV[7] = {FLG};
constexpr unsigned SO = FLV[0], SKV = FLV[1], SKW = FLV[2], SX1 = FLV[3], RX1 = FLV[4], SKEY = FLV[5], RKEY = FLV[6];
constexpr unsigned F_O = 8u << 20;
constexpr unsigned F_KV = 11u << 20;
constexpr unsigned F_WOI = F_O + 262144;
constexpr unsigned F_X1 = 9u << 20;
constexpr unsigned OFF_KEY = 10u << 20;
#define KREP() (RKEY == 16 ? bid & 15 : (bid & 7) * RKEY / 8)
#define KEY_AT(L, rep) (OFF_KEY + ((L) * RKEY + (rep)) * (SKEY < 8 ? 128 : 16 * SKEY))
constexpr unsigned OFF_AB = 2u << 20;
constexpr unsigned OFF_XNEW = 262144;
constexpr unsigned OFF_CS = OFF_AB + 65536;
constexpr unsigned OFF_FX = 393216;
constexpr unsigned OFF_CA = 524288;
constexpr unsigned OFF_STAT = 1u << 20;
constexpr unsigned OFF_PART = 3u << 20;
constexpr unsigned OFF_STAMP = 6u << 20;
constexpr unsigned OFF_XN = 8u << 20;
constexpr unsigned OFF_O = 16u << 20;
constexpr unsigned OFF_KV = 32u << 20;
constexpr unsigned OFF_WI = 52u << 20;
constexpr unsigned OFF_WOI = 58u << 20;
constexpr unsigned WS_BYTES = 60u << 20;
constexpr unsigned POISON = 0xFFFFFFFFu;

constexpr int L_ABW = 16640, L_CSP = 17408, L_CSL = 31872, L_FXL = 32256, L_K = 0, L_V = 32768, L_RING = 65536, L_XCH = 0, L_DN = 32768, L_RDY = 31744, L_QC = 31808, LDS_BYTES = 163840;
constexpr int STAGE = 24576;
#define SLOT(t) ((t) & 3)

template <typename F, int... I> __device__ __forceinline__ void static_for_impl(F&& f, std::integer_sequence<int, I...>) {
  (f(std::integral_constant<int, I>{}), ...);
}
template <int N, typename F> __device__ __forceinline__ void static_for(F&& f) { static_for_impl(f, std::make_integer_sequence<int, N>{}); }
__device__ __forceinline__ unsigned pack2(float a, float b) {
  f32x2 v = {a, b};
  return __builtin_bit_cast(unsigned, __builtin_convertvector(v, bf16x2));
}
__device__ __forceinline__ float asf(unsigned u) { return __builtin_bit_cast(float, u); }
__device__ __forceinline__ float lo16(unsigned u) { return __builtin_bit_cast(float, u << 16); }
__device__ __forceinline__ float hi16(unsigned u) { return __builtin_bit_cast(float, u & 0xffff0000u); }
__device__ __forceinline__ u32x4 pack8(const f32x4 a, const f32x4 b) {
  u32x4 o;
  o[0] = pack2(a[0], a[1]); o[1] = pack2(a[2], a[3]); o[2] = pack2(b[0], b[1]); o[3] = pack2(b[2], b[3]);
  return o;
}
__device__ __forceinline__ f32x4 mfma16(u32x4 a, u32x4 b, f32x4 c) {
  return __builtin_amdgcn_mfma_f32_16x16x32_bf16(__builtin_bit_cast(bf16x8, a), __builtin_bit_cast(bf16x8, b), c, 0, 0, 0);
}
__device__ __forceinline__ f32x4 mfma16k16(s16x4 a, s16x4 b, f32x4 c) { return __builtin_amdgcn_mfma_f32_16x16x16bf16_1k(a, b, c, 0, 0, 0); }
template <int CTRL> __device__ __forceinline__ float dppf(float v) {
  return __builtin_bit_cast(float, __builtin_amdgcn_update_dpp(0, __builtin_bit_cast(int, v), CTRL, 0xf, 0xf, false));
}
template <int CTRL> __device__ __forceinline__ unsigned dppu(unsigned v) {
  return (unsigned)__builtin_amdgcn_update_dpp(0, (int)v, CTRL, 0xf, 0xf, false);
}
__device__ __forceinline__ float add_x16(float v) {
  auto r = __builtin_amdgcn_permlane16_swap(__builtin_bit_cast(unsigned, v), __builtin_bit_cast(unsigned, v), false, false);
  return __builtin_bit_cast(float, (unsigned)r[0]) + __builtin_bit_cast(float, (unsigned)r[1]);
}
__device__ __forceinline__ float add_x32(float v) {
  auto r = __builtin_amdgcn_permlane32_swap(__builtin_bit_cast(unsigned, v), __builtin_bit_cast(unsigned, v), false, false);
  return __builtin_bit_cast(float, (unsigned)r[0]) + __builtin_bit_cast(float, (unsigned)r[1]);
}
__device__ __forceinline__ float max_x16(float v) {
  auto r = __builtin_amdgcn_permlane16_swap(__builtin_bit_cast(unsigned, v), __builtin_bit_cast(unsigned, v), false, false);
  return fmaxf(__builtin_bit_cast(float, (unsigned)r[0]), __builtin_bit_cast(float, (unsigned)r[1]));
}
__device__ __forceinline__ float max_x32(float v) {
  auto r = __builtin_amdgcn_permlane32_swap(__builtin_bit_cast(unsigned, v), __builtin_bit_cast(unsigned, v), false, false);
  return fmaxf(__builtin_bit_cast(float, (unsigned)r[0]), __builtin_bit_cast(float, (unsigned)r[1]));
}
__device__ __forceinline__ unsigned umax_x16(unsigned v) {
  auto r = __builtin_amdgcn_permlane16_swap(v, v, false, false);
  return max((unsigned)r[0], (unsigned)r[1]);
}
__device__ __forceinline__ unsigned umax_x32(unsigned v) {
  auto r = __builtin_amdgcn_permlane32_swap(v, v, false, false);
  return max((unsigned)r[0], (unsigned)r[1]);
}
__device__ __forceinline__ float sum_hi8(float v) {
  v += dppf<0x128>(v);
  v = add_x16(v);
  return add_x32(v);
}
__device__ __forceinline__ float sum64(float v) {
  v += dppf<0xB1>(v);
  v += dppf<0x4E>(v);
  v += dppf<0x124>(v);
  v += dppf<0x128>(v);
  v = add_x16(v);
  return add_x32(v);
}

#define BAR() asm volatile("s_waitcnt lgkmcnt(0)\n\ts_barrier" ::: "memory")
#define DRAIN() asm volatile("s_waitcnt vmcnt(0)" ::: "memory")
#define SB() __builtin_amdgcn_sched_barrier(0)
template <int N> __device__ __forceinline__ void wait_vm() { asm volatile("s_waitcnt vmcnt(%0)" ::"n"(N) : "memory"); }

__device__ __forceinline__ void dma16(rsrc_t rs, unsigned voff, unsigned soff, unsigned lds) {
  asm volatile("s_mov_b32 m0, %0\n\ts_nop 0\n\tbuffer_load_dwordx4 %1, %2, %3 offen lds" ::"s"(lds), "v"(voff), "s"(rs), "s"(soff) : "memory");
}
#define NTM 2
#define OAUX 16
template <int NT> __device__ __forceinline__ void dma16n(rsrc_t rs, unsigned voff, unsigned soff, unsigned lds) {
  if constexpr (NT) asm volatile("s_mov_b32 m0, %0\n\ts_nop 0\n\tbuffer_load_dwordx4 %1, %2, %3 offen nt lds" ::"s"(lds), "v"(voff), "s"(rs), "s"(soff) : "memory");
  else dma16(rs, voff, soff, lds);
}
template <int OFF> __device__ __forceinline__ void dma16s(rsrc_t rs, unsigned voff, unsigned soff, unsigned lds) {
  asm volatile("s_mov_b32 m0, %0\n\ts_nop 0\n\tbuffer_load_dwordx4 %1, %2, %3 offen offset:%4 sc1 lds" ::"s"(lds), "v"(voff), "s"(rs), "s"(soff), "n"(OFF) : "memory");
}
__device__ __forceinline__ float rbf1(float x) { return __builtin_bit_cast(float, pack2(0.f, x)); }
__device__ __forceinline__ void vswap(unsigned& a, unsigned& b) { asm volatile("v_swap_b32 %0, %1" : "+v"(a), "+v"(b)); }
__device__ __forceinline__ void vswap(float& a, float& b) { asm volatile("v_swap_b32 %0, %1" : "+v"(a), "+v"(b)); }
template <typename V, int N> __device__ __forceinline__ void vswapn(V& a, V& b) {
#pragma unroll
  for (int i = 0; i < N; ++i) { auto x = a[i], y = b[i]; vswap(x, y); a[i] = x; b[i] = y; }
}
__device__ __forceinline__ u32x4 ld16(rsrc_t rs, unsigned voff, unsigned soff) { return __builtin_amdgcn_raw_buffer_load_b128(rs, voff, soff, SC1); }
template <int OFF> __device__ __forceinline__ void ld16h(u32x4& d, rsrc_t rs, unsigned voff, unsigned soff) {
  asm volatile("buffer_load_dwordx4 %0, %1, %2, %3 offen offset:%4 sc1" : "=v"(d) : "v"(voff), "s"(rs), "s"(soff), "n"(OFF) : "memory");
}
template <int I, int OFF> __device__ __forceinline__ void ld16w(u32x4& d, rsrc_t rs, unsigned voff, unsigned soff) {
  if constexpr (I == 0) asm volatile("buffer_load_dwordx4 %0, %1, %2, %3 offen offset:%4 sc1" : "={v[128:131]}"(d) : "v"(voff), "s"(rs), "s"(soff), "n"(OFF) : "memory");
  else if constexpr (I == 1) asm volatile("buffer_load_dwordx4 %0, %1, %2, %3 offen offset:%4 sc1" : "={v[132:135]}"(d) : "v"(voff), "s"(rs), "s"(soff), "n"(OFF) : "memory");
  else if constexpr (I == 2) asm volatile("buffer_load_dwordx4 %0, %1, %2, %3 offen offset:%4 sc1" : "={v[136:139]}"(d) : "v"(voff), "s"(rs), "s"(soff), "n"(OFF) : "memory");
  else if constexpr (I == 3) asm volatile("buffer_load_dwordx4 %0, %1, %2, %3 offen offset:%4 sc1" : "={v[140:143]}"(d) : "v"(voff), "s"(rs), "s"(soff), "n"(OFF) : "memory");
  else if constexpr (I == 4) asm volatile("buffer_load_dwordx4 %0, %1, %2, %3 offen offset:%4 sc1" : "={v[144:147]}"(d) : "v"(voff), "s"(rs), "s"(soff), "n"(OFF) : "memory");
  else if constexpr (I == 5) asm volatile("buffer_load_dwordx4 %0, %1, %2, %3 offen offset:%4 sc1" : "={v[148:151]}"(d) : "v"(voff), "s"(rs), "s"(soff), "n"(OFF) : "memory");
  else if constexpr (I == 6) asm volatile("buffer_load_dwordx4 %0, %1, %2, %3 offen offset:%4 sc1" : "={v[152:155]}"(d) : "v"(voff), "s"(rs), "s"(soff), "n"(OFF) : "memory");
  else if constexpr (I == 7) asm volatile("buffer_load_dwordx4 %0, %1, %2, %3 offen offset:%4 sc1" : "={v[156:159]}"(d) : "v"(voff), "s"(rs), "s"(soff), "n"(OFF) : "memory");
  else if constexpr (I == 8) asm volatile("buffer_load_dwordx4 %0, %1, %2, %3 offen offset:%4 sc1" : "={v[160:163]}"(d) : "v"(voff), "s"(rs), "s"(soff), "n"(OFF) : "memory");
  else if constexpr (I == 9) asm volatile("buffer_load_dwordx4 %0, %1, %2, %3 offen offset:%4 sc1" : "={v[164:167]}"(d) : "v"(voff), "s"(rs), "s"(soff), "n"(OFF) : "memory");
  else if constexpr (I == 10) asm volatile("buffer_load_dwordx4 %0, %1, %2, %3 offen offset:%4 sc1" : "={v[168:171]}"(d) : "v"(voff), "s"(rs), "s"(soff), "n"(OFF) : "memory");
  else if constexpr (I == 11) asm volatile("buffer_load_dwordx4 %0, %1, %2, %3 offen offset:%4 sc1" : "={v[172:175]}"(d) : "v"(voff), "s"(rs), "s"(soff), "n"(OFF) : "memory");
}
template <int K> __device__ __forceinline__ void pin_w(u32x4& a, u32x4& b) {
  if constexpr (K == 0) asm volatile("" : "+{v[128:131]}"(a), "+{v[132:135]}"(b)::"memory");
  else if constexpr (K == 1) asm volatile("" : "+{v[136:139]}"(a), "+{v[140:143]}"(b)::"memory");
  else if constexpr (K == 2) asm volatile("" : "+{v[144:147]}"(a), "+{v[148:151]}"(b)::"memory");
  else if constexpr (K == 3) asm volatile("" : "+{v[152:155]}"(a), "+{v[156:159]}"(b)::"memory");
  else if constexpr (K == 4) asm volatile("" : "+{v[160:163]}"(a), "+{v[164:167]}"(b)::"memory");
  else if constexpr (K == 5) asm volatile("" : "+{v[168:171]}"(a), "+{v[172:175]}"(b)::"memory");
}
__device__ __forceinline__ void ld8h(u32x2& d, rsrc_t rs, unsigned voff, unsigned soff) {
  asm volatile("buffer_load_dwordx2 %0, %1, %2, %3 offen sc1" : "=v"(d) : "v"(voff), "s"(rs), "s"(soff) : "memory");
}
template <int OFF> __device__ __forceinline__ void ld4h(unsigned& d, rsrc_t rs, unsigned voff, unsigned soff) {
  asm volatile("buffer_load_dword %0, %1, %2, %3 offen offset:%4 sc1" : "=v"(d) : "v"(voff), "s"(rs), "s"(soff), "n"(OFF) : "memory");
}
__device__ __forceinline__ u32x2 ld8(rsrc_t rs, unsigned voff, unsigned soff) { return __builtin_amdgcn_raw_buffer_load_b64(rs, voff, soff, SC1); }
__device__ __forceinline__ void st16(u32x4 v, rsrc_t rs, unsigned voff, unsigned soff) { __builtin_amdgcn_raw_buffer_store_b128(v, rs, voff + soff, 0, SC1); }
__device__ __forceinline__ void st8(u32x2 v, rsrc_t rs, unsigned voff, unsigned soff) { __builtin_amdgcn_raw_buffer_store_b64(v, rs, voff, soff, SC1); }

__device__ __forceinline__ unsigned ld4(rsrc_t rs, unsigned voff, unsigned soff) { return __builtin_amdgcn_raw_buffer_load_b32(rs, voff, soff, SC1); }
__device__ __forceinline__ void st4(unsigned v, rsrc_t rs, unsigned voff, unsigned soff) { __builtin_amdgcn_raw_buffer_store_b32(v, rs, voff, soff, SC1); }

constexpr unsigned kSpinCap = 1u << 16;
__device__ __forceinline__ bool spin_fail(unsigned& spins, rsrc_t rs, u32x2 T) {
  asm volatile("" ::: "memory");
  ++spins;
  if (spins > kSpinCap) { st8(T, rs, 0, F_TMO); return true; }
  if ((spins & 255u) == 0u) {
    const u32x2 v = ld8(rs, 0, F_TMO);
    if (v[0] == T[0] && v[1] == T[1]) return true;
  }
  __builtin_amdgcn_s_sleep(3);
  return false;
}
__device__ __forceinline__ void poll_flags2(rsrc_t rs, unsigned voff, u32x2 T) {
  for (unsigned spins = 0;;) {
    const u32x4 v = ld16(rs, voff, 0);
    if (__all(v[0] == T[0] && v[1] == T[1] && v[2] == T[0] && v[3] == T[1])) break;
    if (spin_fail(spins, rs, T)) break;
  }
  asm volatile("" ::: "memory");
  __builtin_amdgcn_fence(__ATOMIC_ACQUIRE, "wavefront");
}
__device__ __forceinline__ void tag_wait(rsrc_t rs, u32x2 T, unsigned voff, unsigned soff, u32x4& v) {
  if (__builtin_expect(!__all(v[2] == T[0] && v[3] == T[1]), 0)) {
    unsigned spins = 0;
    do {
      if (spin_fail(spins, rs, T)) break;
      v = ld16(rs, voff, soff);
    } while (!__all(v[2] == T[0] && v[3] == T[1]));
    asm volatile("s_waitcnt vmcnt(0)" : "+v"(v)::"memory");
  }
}
template <int N> __device__ __forceinline__ void word_wait(rsrc_t rs, u32x2 T, unsigned voff, unsigned soff, u32x4 (&v)[4]) {
#define WW_OK() __all(max(max(max(v[0][0], v[0][1]), max(v[0][2], v[0][3])), max(max(v[N - 1][0], v[N - 1][1]), max(v[N - 1][2], v[N - 1][3]))) != POISON)
  if (__builtin_expect(!WW_OK(), 0)) {
    unsigned spins = 0;
    do {
      if (spin_fail(spins, rs, T)) break;
#pragma unroll
      for (int i = 0; i < N; ++i) v[i] = ld16(rs, voff + i * 2048, soff);
    } while (!WW_OK());
    asm volatile("s_waitcnt vmcnt(0)" : "+v"(v[0]), "+v"(v[N - 1])::"memory");
  }
#undef WW_OK
}
__device__ __forceinline__ void poll_flags(rsrc_t rs, unsigned voff, unsigned soff, u32x2 T) {
  for (unsigned spins = 0;;) {
    const u32x2 v = ld8(rs, voff, soff);
    if (__all(v[0] == T[0] && v[1] == T[1])) break;
    if (spin_fail(spins, rs, T)) break;
  }
  asm volatile("" ::: "memory");
  __builtin_amdgcn_fence(__ATOMIC_ACQUIRE, "wavefront");
}

#define STAMP_ON(i) true
#define STAMP_VAL(i) (((i) == 23 || (i) == 22 || (i) == 21) ? __builtin_amdgcn_s_memtime() : __builtin_amdgcn_s_memrealtime())
#define STAMPAT(i)

#define GSTEP(s, n)
#define VSTEP(i)

__device__ __forceinline__ unsigned toff(unsigned R, unsigned ks, unsigned g) {
  return (((R >> 5) * 64 + ks * 2 + ((R >> 4) & 1)) << 10) + ((g * 16 + (R & 15)) << 4);
}
__device__ __forceinline__ void unpack8v(const u32x4 u, f32x2* v) {
#pragma unroll
  for (int j = 0; j < 4; ++j) v[j] = f32x2{lo16(u[j]), hi16(u[j])};
}
__device__ __forceinline__ u32x4 pack8v(const f32x2* v) {
  u32x4 o;
#pragma unroll
  for (int j = 0; j < 4; ++j) o[j] = pack2(v[j][0], v[j][1]);
  return o;
}
__device__ __forceinline__ f32x2 rbf1v(f32x2 v) { return f32x2{rbf1(v[0]), rbf1(v[1])}; }
__device__ __forceinline__ float ln_rd(float var) { return __builtin_amdgcn_rcpf(rbf1(__builtin_amdgcn_sqrtf(rbf1(rbf1(var) + 1e-5f)))); }
template <int N> __device__ __forceinline__ void ln_apply(float mb, float rd, const f32x2* v, f32x2* y) {
  const f32x2 mb2 = {mb, mb}, rd2 = {rd, rd};
#pragma unroll
  for (int j = 0; j < N; ++j) y[j] = rbf1v(rbf1v(v[j] - mb2) * rd2);
}
__device__ __forceinline__ float hadd(f32x2 v) {
  float a = v[0];
  asm volatile("" : "+v"(a));
  return a + v[1];
}
__device__ __forceinline__ float add_g(float v) { return add_x32(add_x16(v)); }
__device__ __forceinline__ void add_g3(float& x, float& y, float& z) {
  auto a = __builtin_amdgcn_permlane16_swap(__builtin_bit_cast(unsigned, x), __builtin_bit_cast(unsigned, y), false, false);
  auto b = __builtin_amdgcn_permlane16_swap(__builtin_bit_cast(unsigned, z), __builtin_bit_cast(unsigned, z), false, false);
  const float r = asf(a[0]) + asf(a[1]), t = asf(b[0]) + asf(b[1]);
  auto c = __builtin_amdgcn_permlane32_swap(__builtin_bit_cast(unsigned, r), __builtin_bit_cast(unsigned, r), false, false);
  auto d = __builtin_amdgcn_permlane32_swap(__builtin_bit_cast(unsigned, t), __builtin_bit_cast(unsigned, t), false, false);
  const float u = asf(c[0]) + asf(c[1]);
  z = asf(d[0]) + asf(d[1]);
  auto e = __builtin_amdgcn_permlane16_swap(__builtin_bit_cast(unsigned, u), __builtin_bit_cast(unsigned, u), false, false);
  x = asf(e[0]); y = asf(e[1]);
}
__device__ __forceinline__ u32x4 trade16(u32x4 v) {
  auto a = __builtin_amdgcn_permlane16_swap(v[0], v[2], false, false);
  auto b = __builtin_amdgcn_permlane16_swap(v[1], v[3], false, false);
  return u32x4{(unsigned)a[0], (unsigned)b[0], (unsigned)a[1], (unsigned)b[1]};
}
__device__ __forceinline__ unsigned umax4(u32x4 v) { return max(max(v[0], v[1]), max(v[2], v[3])); }
__device__ __forceinline__ u32x2 unpoison2(u32x2 v) { return u32x2{min(v[0], POISON - 1), min(v[1], POISON - 1)}; }
__device__ __forceinline__ u32x4 unpoison4(u32x4 v) {
  return u32x4{min(v[0], POISON - 1), min(v[1], POISON - 1), min(v[2], POISON - 1), min(v[3], POISON - 1)};
}

typedef union { struct { s16x4 a, b; } hf; u32x4 v; } frag_t;
__device__ __forceinline__ s16x4 trread(const char* smem, unsigned off) {
  return __builtin_amdgcn_ds_read_tr16_b64_v4i16((LDS_AS s16x4*)(smem + off));
}
__device__ __forceinline__ s16x4 trabs(unsigned addr) { return __builtin_amdgcn_ds_read_tr16_b64_v4i16((LDS_AS s16x4*)(size_t)addr); }

constexpr int WA = 6;
#define WOP 4, 3, 6, 8, 1, 4, 0
constexpr int WOV[7] = {WOP};
constexpr int WLA = WOV[4], WPR = WOV[5], QPR = WOV[6];
constexpr int WB = WOV[0], WR = WOV[1], WIS = WOV[2], WPS = WOV[3];
#define CH 0, 7, 9, 10, 16, 19, 24, 26, 30, 31, 5, 2
constexpr int CHV[12] = {CH};
constexpr int FKL = CHV[10], FSN = CHV[11];
constexpr int SPL = CHV[0];
constexpr int FSL = CHV[1], FS0 = CHV[2], FS0D = CHV[3], FS1 = CHV[4], FS1S = CHV[5];
constexpr int FXA = CHV[6], FXB = CHV[7], FXL = CHV[8], FXS = CHV[9];
#define ROTM 7
#define ROT0 (p & ROTM)
#define PO() (p & 7 & ~ROTM)
#define L0S 22, 25, 8, 0, 24, 28, 1, 2, 4, 14
constexpr int L0V[10] = {L0S};
constexpr int CSL = L0V[0], CSS = L0V[1], CSD = L0V[2];
constexpr int abl(int it) { return it ? 25 : L0V[4]; }
constexpr int abs_(int it) { return it ? 27 : L0V[5]; }
constexpr int W2S = L0V[6], WSH = L0V[7];
static_assert(ROTM == 7 || (CSD & 3) == 3, "two tiles must be there");
#define HLPW 0
#define OFE 1
#define LNP 0
constexpr int PRE0 = 12 + (LNP ? 3 : 0);
constexpr int LNL = L0V[3], LNS = L0V[8], POI = L0V[9];
#define SYNCB 3
#define SPINS 0
#define KVJ 1
#define KVD 2
#define PC2 1
#define PCE 1
#define TCH 40, 6, 0, 26, 0xFF, 0, 1
constexpr int TCV[7] = {TCH};
constexpr int TS0 = TCV[0], TSN = TCV[1], TM0 = TCV[2], TEX = TCV[3], TM1 = TCV[4], TSD = TCV[5], TSG = TCV[6];
constexpr int QL = 26;
#define SLP 18, 4, 28, 3, 0, 0
constexpr int SLV[6] = {SLP};
constexpr int OUTE = SLV[4], OSL = SLV[5];
constexpr int SDIR = SLV[0], GSL = SLV[1], RSL = SLV[2], KSL = SLV[3];
constexpr int tp0(int it) { return it ? 3 : 3 + WSH; }
constexpr bool tile_piece(int it, int s) { return s >= tp0(it) && ((s - tp0(it)) >> 2) + 3 < 8 && ((s - tp0(it)) & 3) < 3; }
#define NPRE 3
#define QAD 4
constexpr int W2V[5] = {WO2};
constexpr int WOS = 12;
static_assert(WOS + QAD < 18, "covered by the operand waits in front of the barrier of step 18");
constexpr int WON = W2V[0], W2B = W2V[1], W2R = W2V[2], W2L = W2V[3], W2P = W2V[4];
constexpr int a_req(int s, int n) {
  int r = 2 * NPRE;
  for (int u = 1; u < 192; u += 2) {
    const bool go = r < 64 && (r >> 1) <= u / 6 + QAD && (r < 8 || u >= 9);
    if (u == 6 * s + n) return go ? r : -1;
    r += go;
  }
  return -1;
}
constexpr int vm_n(int it, int s, int n) {
  int c = it ? (a_req(s, n) >= 0) + (s == 0 && n == 0) : (s + 5 < 32 && (n == 2 || n == 3));
  if (n == 0) c += tile_piece(it, s);
  if (n == 4) c += (s == abl(it));
  if (!it) c += (s >= W2S && s < W2S + 3 && n == 4) + (LNP == 0 && s == LNL && n == 5 ? 3 : 0) + (s == 0 && n < 2 ? 2 : 0) + (s == LNS + 2 && n == 5) + (s == TEX && n == 5);
  else c += (s == 2 && n == 2) + (s == (SPL < 0 ? 0 : SPL) && n == (SPL < 0 ? 1 : 4) ? 4 : 0) + (s >= 27 && s < 30 && (n == 0 || n == 3)) + (WON && s == WOS && n == 4);
  return c;
}
constexpr int vm_at(int it, int s, int n) {
  int c = it ? 2 * NPRE : PRE0;
  for (int u = 0; u < 6 * s + n; ++u) c += vm_n(it, u / 6, u % 6);
  return c;
}
constexpr int vm_a(int it, int k) {
  if (k < NPRE) return 2 * k + 2;
  for (int u = 0; u < 192; ++u)
    if (a_req(u / 6, u % 6) == 2 * k + 1) return vm_at(it, (u + 1) / 6, (u + 1) % 6);
  return 0;
}
constexpr int vm_tile(int it, int t) { return t >= 3 ? vm_at(it, 4 * t - 10 + tp0(it), 1) : t == 1 ? 12 + (LNP >= 2 ? 3 : 0) : vm_at(0, W2S + 2, 5); }
constexpr int vm_unit(int u) { return u < 3 ? 5 + 2 * u + (LNP == 3 || (LNP == 2 && u == 2) ? 3 : 0) : u < 5 ? vm_at(0, 0, u - 2) : vm_at(0, u - 5, 4); }
constexpr int unit_slot(int u) { return u % 3 == 0 ? 0 : 16384 + (u % 3) * 16384; }

constexpr int L_XC = LDS_BYTES - 64, L_SKEY = 32704, L_ROW = 1024, L_OUT = 65536, L_SRED = 4096, L_SFIN = 20480;
__device__ __forceinline__ unsigned key_load(rsrc_t rs, int l, int L, int rep) { return ld4(rs, (l & 15) * SKEY, KEY_AT(L, rep)); }
__device__ __forceinline__ unsigned umax16(unsigned k) {
  k = max(k, dppu<0xB1>(k));
  k = max(k, dppu<0x4E>(k));
  k = max(k, dppu<0x141>(k));
  return max(k, dppu<0x140>(k));
}
__device__ __forceinline__ unsigned key_row(unsigned key) { return (0xffffu - (key & 0xffffu)) & 2047u; }
__device__ __forceinline__ unsigned key_wait(rsrc_t rs, u32x2 T, int l, int L, int rep, unsigned kv) {
  if (!__all(kv != POISON)) {
    unsigned spins = 0;
    do {
      if (spin_fail(spins, rs, T)) break;
      if (L == 0) __builtin_amdgcn_s_sleep(KSL);
      kv = key_load(rs, l, L, rep);
    } while (!__all(kv != POISON));
  }
  return __builtin_amdgcn_readfirstlane(umax16(kv));
}
__device__ __forceinline__ unsigned cand_off(unsigned key, int L) { return OFF_CA + L * 65536 + (key_row(key) >> 7) * 4096; }
#define XO(i) (((i) >> 1) * 256 + ((i) & 1) * 64)
template <int S1, int S2> __device__ __forceinline__ void ffn_xwait(rsrc_t rs, u32x2 T, unsigned xo, int l, u32x4 (&xv)[4]) {
#define XV_OK() __all(max(max(umax4(xv[0]), umax4(xv[1])), max(umax4(xv[2]), umax4(xv[3]))) != POISON)
  if (!XV_OK()) {
    unsigned spins = 0;
    do {
      if (spin_fail(spins, rs, T)) break;
#pragma unroll
      for (int i = 0; i < 4; ++i) xv[i] = ld16(rs, (l >> 4) * 16 + (i & 1) * S1 + (i >> 1) * S2, xo);
    } while (!XV_OK());
  }
#undef XV_OK
}
__device__ __forceinline__ void ffn_gu(const u32x4 (&gv)[4], const u32x4 (&uv)[4], const u32x4 (&xv)[4], f32x4& ga, f32x4& ua) {
  ga = mfma16(gv[0], xv[0], f32x4{0.f, 0.f, 0.f, 0.f}); ua = mfma16(uv[0], xv[0], f32x4{0.f, 0.f, 0.f, 0.f});
#pragma unroll
  for (int i = 1; i < 4; ++i) { ga = mfma16(gv[i], xv[i], ga); ua = mfma16(uv[i], xv[i], ua); }
}
__device__ __forceinline__ void ffn_gu_check(rsrc_t rs, u32x2 T, unsigned xo, int l, const u32x4 (&gv)[4], const u32x4 (&uv)[4], u32x4 (&xv)[4], f32x4& ga, f32x4& ua) {
  if (__builtin_expect(__any(ga[0] != ga[0]), 0)) {
    ffn_xwait<64, 256>(rs, T, xo, l, xv);
    ffn_gu(gv, uv, xv, ga, ua);
  }
}
__device__ __forceinline__ void ffn_gu_store(char* smem, int l, int w, f32x4 ga, f32x4 ua) {
  LDS_AS f32x4* sred = (LDS_AS f32x4*)(smem + L_SRED);
  if ((l & 15) == 0) { sred[w * 8 + (l >> 4)] = ga; sred[w * 8 + 4 + (l >> 4)] = ua; }
}
__device__ __forceinline__ f32x4 ffn_d_read(char* smem, int l) {
  const LDS_AS float* t = (const LDS_AS float*)(smem + L_SRED) + (l & 8) * 16 + (l & 4) * 4 + (l >> 4) * 4 + (l & 3);
  return f32x4{t[0], t[32], t[64], t[96]};
}
__device__ __forceinline__ u32x2 ffn_d_h(f32x4 t) {
  float x = t[0] + t[1];
  asm volatile("" : "+v"(x));
  x += t[2] + t[3];
  x += dppf<0x128>(x);
  const float y = dppf<0x124>(x);
  const bool up = __builtin_amdgcn_inverse_ballot_w64(0xF0F0F0F0F0F0F0F0ull);
  const float gg = rbf1(up ? y : x), uu = rbf1(up ? x : y);
  const float h = rbf1(gg * __builtin_amdgcn_rcpf(1.0f + __expf(-gg))) * uu;
  const unsigned p = pack2(h, dppf<0xB1>(h));
  return u32x2{dppu<0x00>(p), dppu<0xAA>(p)};
}
template <int t> __device__ __forceinline__ f32x4 ffn_d_mma(s16x4 d, u32x2 hh, f32x4 y) {
  const bool me = __builtin_amdgcn_inverse_ballot_w64(0x0001000100010001ull << t);
  return mfma16k16(d, __builtin_bit_cast(s16x4, u32x2{me ? hh[0] : 0u, me ? hh[1] : 0u}), y);
}
__device__ __forceinline__ f32x4 ffn_d_all(const s16x4 (&dfr)[8], u32x2 hh) {
  f32x4 a = {0.f, 0.f, 0.f, 0.f}, b = a;
  a = ffn_d_mma<0>(dfr[0], hh, a); b = ffn_d_mma<1>(dfr[1], hh, b); a = ffn_d_mma<2>(dfr[2], hh, a); b = ffn_d_mma<3>(dfr[3], hh, b);
  a = ffn_d_mma<4>(dfr[4], hh, a); b = ffn_d_mma<5>(dfr[5], hh, b); a = ffn_d_mma<6>(dfr[6], hh, a); b = ffn_d_mma<7>(dfr[7], hh, b);
  return a + b;
}
__device__ __forceinline__ void ffn_d_store(rsrc_t rs, int l, int w, int L, int slice, f32x4 y) {
  const unsigned c = l & 15;
  if (c < 8) st16(unpoison4(__builtin_bit_cast(u32x4, y)), rs, (2 * w + (c >> 2)) * 65536 + slice * 256 + (c & 3) * 64 + (l >> 4) * 16, OFF_PART + L * (1u << 20));
}
#define RED_VO(k) ((min(tid + 512 * (k), 1791) >> 3) * 256 + (tid & 7) * 16)
#define RED_SO(L, G) (OFF_PART + (L) * (1u << 20) + ((G) >> 1) * 65536 + ((G) & 1) * 128)
#define RED_JJ() ((tid & 3) * 16 + (tid & 4) * 2)
__device__ __forceinline__ void red_load(rsrc_t rs, int tid, int L, unsigned G, unsigned key, u32x4 (&pv)[4], u32x2& cxn, u32x2& cx1) {
#pragma unroll
  for (int k = 0; k < 4; ++k) pv[k] = ld16(rs, RED_VO(k), RED_SO(L, G));
  cxn = ld8(rs, RED_JJ(), cand_off(key, L) + XO(G));
  cx1 = ld8(rs, RED_JJ() + 128, cand_off(key, L) + XO(G));
}
__device__ __forceinline__ void red_sum(rsrc_t rs, char* smem, u32x2 T, int tid, int w, int L, unsigned G, unsigned key,
                                        u32x4 (&pv)[4], u32x2& cxn, u32x2& cx1) {
#define PV_OK() __all(max(max(max(umax4(pv[0]), umax4(pv[1])), max(umax4(pv[2]), umax4(pv[3]))), max(max(cxn[0], cxn[1]), max(cx1[0], cx1[1]))) != POISON)
  if (!PV_OK()) {
    unsigned spins = 0;
    do {
      if (spin_fail(spins, rs, T)) break;
#pragma unroll
      for (int k = 0; k < 4; ++k)
        if (umax4(pv[k]) == POISON) pv[k] = ld16(rs, RED_VO(k), RED_SO(L, G));
      if (max(max(cxn[0], cxn[1]), max(cx1[0], cx1[1])) == POISON) { cxn = ld8(rs, RED_JJ(), cand_off(key, L) + XO(G)); cx1 = ld8(rs, RED_JJ() + 128, cand_off(key, L) + XO(G)); }
    } while (!PV_OK());
  }
#undef PV_OK
  f32x4 s = (__builtin_bit_cast(f32x4, pv[0]) + __builtin_bit_cast(f32x4, pv[1])) + __builtin_bit_cast(f32x4, pv[2]);
  if (w < 4) s += __builtin_bit_cast(f32x4, pv[3]);
  ((LDS_AS f32x4*)(smem + L_SFIN))[tid] = s;
}
__device__ __forceinline__ u32x2 red_final(char* smem, int l, unsigned key, u32x2 cxn, u32x2 cx1) {
  const LDS_AS f32x4* sf = (const LDS_AS f32x4*)(smem + L_SFIN) + l;
  f32x4 s = ((sf[0] + sf[64]) + (sf[128] + sf[192])) + ((sf[256] + sf[320]) + (sf[384] + sf[448]));
#pragma unroll
  for (int j = 0; j < 4; ++j) s[j] = sum_hi8(s[j]);
  const unsigned k16 = key >> 16;
  const float rmax = __builtin_bit_cast(float, ((k16 & 0x8000u) ? (k16 & 0x7fffu) : (~k16 & 0xffffu)) << 16);
  const float sig = rbf1(__builtin_amdgcn_rcpf(1.0f + __expf(-rmax)));
  const float xn4[4] = {lo16(cxn[0]), hi16(cxn[0]), lo16(cxn[1]), hi16(cxn[1])};
  const float x14[4] = {lo16(cx1[0]), hi16(cx1[0]), lo16(cx1[1]), hi16(cx1[1])};
  float o[4];
#pragma unroll
  for (int j = 0; j < 4; ++j) o[j] = x14[j] + rbf1(xn4[j] + rbf1(rbf1(s[j]) * sig));
  return u32x2{pack2(o[0], o[1]), pack2(o[2], o[3])};
}

struct RowFin { float mean, var, mb, rd; unsigned key; };
__device__ __forceinline__ void fin_wait(rsrc_t rs, u32x2 T, unsigned voff, unsigned so, u32x4 (&sp)[4]) {
#define ST_OK() __all(max(max(umax4(sp[0]), umax4(sp[1])), max(umax4(sp[2]), umax4(sp[3]))) != POISON)
  if (!ST_OK()) {
    unsigned spins = 0;
    do {
      if (spin_fail(spins, rs, T)) break;
#pragma unroll
      for (int i = 0; i < 4; ++i) sp[i] = ld16(rs, voff + i * 2048, so);
    } while (!ST_OK());
  }
#undef ST_OK
}
__device__ __forceinline__ RowFin fin_key(rsrc_t rs, u32x2 T, unsigned voff, unsigned so, char* skey, int l, int w, unsigned R, float wsum, u32x4 (&sp)[4]) {
  RowFin r;
#define ST_SUM() ((__builtin_bit_cast(f32x4, sp[0]) + __builtin_bit_cast(f32x4, sp[1])) + (__builtin_bit_cast(f32x4, sp[2]) + __builtin_bit_cast(f32x4, sp[3])))
  f32x4 sf = ST_SUM();
  const float z = (sf[0] + sf[1]) + sf[2];
  if (__any(z != z)) {
    fin_wait(rs, T, voff, so, sp);
    sf = ST_SUM();
  }
#undef ST_SUM
  float s0 = sf[0], s1 = sf[1], s2 = sf[2];
  add_g3(s0, s1, s2);
  r.mean = s0 * (1.0f / 1024.0f);
  r.var = fmaxf(__builtin_fmaf(-r.mean, r.mean, s1 * (1.0f / 1024.0f)), 0.f);
  r.mb = rbf1(r.mean); r.rd = ln_rd(r.var);
  const unsigned rb16 = pack2((s2 - r.mb * wsum) * r.rd, 0.f) & 0xffffu;
  r.key = min((((rb16 & 0x8000u) ? (~rb16 & 0xffffu) : (rb16 | 0x8000u)) << 16) | (0xffffu - R), POISON - 1);
  const unsigned wk = umax16(r.key);
  if (l == 0) ((LDS_AS unsigned*)skey)[w] = wk;
  return r;
}
__device__ __forceinline__ unsigned fin_cand(rsrc_t rs, char* skey, int L, int g, int rb, int ct, const RowFin& r, const f32x2 (&v)[8]) {
  const u32x4 k0 = *(const LDS_AS u32x4*)skey, k1 = *(const LDS_AS u32x4*)(skey + 16);
  const unsigned best = max(umax4(k0), umax4(k1));
  if (r.key == best) {
    const unsigned co = L * 65536 + rb * 4096 + ct * 256 + g * 16;
    f32x2 t[8], rd2 = {r.rd, r.rd}, of2 = {-r.mb * r.rd, -r.mb * r.rd};
    asm volatile("" : "+v"(rd2), "+v"(of2));
#pragma unroll
    for (int j = 0; j < 8; ++j) t[j] = __builtin_elementwise_fma(v[j], rd2, of2);
    st16(unpoison4(pack8v(t)), rs, co, OFF_CA); st16(unpoison4(pack8v(t + 4)), rs, co + 64, OFF_CA);
    if (g == 0 && ct < (int)RKEY) st4(best, rs, KEY_AT(L, ct) + rb * SKEY, 0);
    st16(unpoison4(pack8v(v)), rs, co + 128, OFF_CA); st16(unpoison4(pack8v(v + 4)), rs, co + 192, OFF_CA);
  }
  return key_row(best);
}
__device__ __forceinline__ void fin_x2(const RowFin& r, f32x2 (&v)[8]) {
  f32x2 sc2 = {1.0f + r.rd, 1.0f + r.rd}, of2 = {-r.mb * r.rd, -r.mb * r.rd};
  asm volatile("" : "+v"(sc2), "+v"(of2));
#pragma unroll
  for (int j = 0; j < 8; ++j) v[j] = __builtin_elementwise_fma(v[j], sc2, of2);
}

extern "C" __device__ unsigned long long dispatch_id() __asm("llvm.amdgcn.dispatch.id");

extern "C" __global__ __launch_bounds__(512, 2) void mega_kernel(const bf16* x, char* ws, const bf16* wqkv, const bf16* wo, const bf16* wr,
                                                                 const bf16* wg, const bf16* wu, const bf16* wd, bf16* out) {
  extern __shared__ __attribute__((aligned(16))) char smem[];
  const int w0_ = __builtin_amdgcn_readfirstlane(threadIdx.x >> 6);
  const unsigned lbase = (unsigned)(size_t)(LDS_AS char*)smem;
  {
    unsigned long long pc;
    unsigned len;
    asm volatile("s_getpc_b64 %0\n\ts_mov_b32 %1, .Ltext_end-mega_kernel\n\t.subsection 1\n.Ltext_end:\n\t.subsection 0" : "=s"(pc), "=s"(len));
    const unsigned o = ((blockIdx.x >> 3) * 64 + (threadIdx.x & 63)) * 128;
    if (w0_ == 7 && o < len)
      asm volatile("s_mov_b32 m0, %0\n\ts_nop 0\n\tglobal_load_lds_dword %1, %2" ::"s"(lbase + L_FXL), "v"(o), "s"(pc & ~127ull) : "memory");
  }
  asm volatile("" : "+s"(x), "+s"(ws), "+s"(wqkv));
#define IDS()                                                                                         \
  int tid = threadIdx.x, w = w0_, bid = blockIdx.x;                                                   \
  asm volatile("" : "+v"(tid));                                                                       \
  asm volatile("" : "+s"(w));                                                                         \
  asm volatile("" : "+s"(bid));                                                                       \
  const int l = tid & 63, xcd = bid & 7, bi = bid >> 3;                                               \
  const int p = (xcd & 1) * 4 + (bi & 3), h = (xcd >> 1) * 8 + (bi >> 2);         \
  const int rb = 2 * xcd + (bi & 1), ct = bi >> 1;                               \
  const int slice = xcd * 28 + bi;                                                   \
  const bool ffn_on = bi < 28;                                                                        \
  const int g = l >> 4;                                                                               \
  const int rt = w < 4 ? w : 11 - w;          \
  KARGS();                                                                                            \
  (void)p; (void)h; (void)rb; (void)ct; (void)slice; (void)ffn_on; (void)g; (void)rt;
  const rsrc_t rs = __builtin_amdgcn_make_buffer_rsrc((void*)ws, 0, WS_BYTES, 0x00020000);
  typedef const void* const __attribute__((address_space(4)))* karg_t;
#define KARGS() karg_t ka = (karg_t)__builtin_amdgcn_kernarg_segment_ptr(); asm volatile("" : "+s"(ka))
#define ARGP(name, i) const GAS bf16* name = (const GAS bf16*)ka[i]; asm volatile("" : "+s"(name))
#define RSRC(ptr, bytes) __builtin_amdgcn_make_buffer_rsrc((void*)(ptr), 0, bytes, 0x00020000)
#define rwq RSRC(pwq, 1024 * 3072 * 2)
#define rwo RSRC(pwo, 1024 * 1024 * 2)
#define rwg RSRC(pwg, 1024 * F_ * 2)
#define rwu RSRC(pwu, 1024 * F_ * 2)
#define rwd RSRC(pwd, 1024 * F_ * 2)
#define rout RSRC(pout, 2048 * 1024 * 2)

#define WQ_VOFF() ((l >> 2) * 6144 + (((l & 3) ^ ((((l >> 4) ^ (l >> 5)) & 1) << 1)) << 4))
#define WQ_PIECE(t, j, dst) dma16(rwq, wqv, ((((t) + rot) & 7) * 128 + ((j) & 7) * 16) * 6144 + ((j) >> 3) * 2048 + h * 64, lbase + (dst) + (j) * 1024)
#define WQ_BOFF(dt) ((8 * g + ((l & 15) >> 2)) * 64 + (l & 3) * 8 + (((dt) ^ (g & 1)) << 5))
#define WQ_POFF() ((4 * g + ((l & 15) >> 2)) * 64 + (l & 3) * 8 + (((g ^ (g >> 1)) & 1) << 5))
#define X_UNIT1(u, j) dma16(rx, xv_, (unsigned)(p * 256 + rt * 32 + ((u) & 1) * 16 + (j) * 8) * 2048 + ((((u) >> 2) + rot) & 7) * 256 + (((u) >> 1) & 1) * 128, lbase + rt * 2048 + unit_slot(u) + (j) * 1024)
#define X_VOFF() ((l >> 3) * 2048 + (((l & 7) ^ ((l >> 3) & 6)) << 4))
#define X_ROFF(hs) (lbase + rt * 2048 + (l & 15) * 128 + (((4 * (hs) + g) ^ (l & 6)) << 4))
#define TOUCH_TO(ptr, dst) asm volatile("s_mov_b32 m0, %0\n\ts_nop 0\n\tglobal_load_lds_dword %1, off" ::"s"(lbase + (dst)), "v"(ptr) : "memory")
#define TOUCH(ptr) TOUCH_TO(ptr, L_FXL)
#define WO_PIECE_W(j, w_)                                                                                                                 \
  {                                                                                                                                       \
    if constexpr (it == 1 && WON) dma16s<0>(rs, l * 16, OFF_WOI + ct * 131072 + (j) * 8192 + (w_) * 1024, lbase + 32768 + (j) * 8192 + (w_) * 1024); \
    else dma16(rwo, wov, (((w_) >> 2) * 64 + ((w_) & 3) + 4 * (j)) * 16384 + ct * 128, lbase + 32768 + (j) * 8192 + (w_) * 1024);           \
  }
#define WO_PIECE(j) WO_PIECE_W(j, w)
#define WO_VOFF() ((l >> 3) * 2048 + (((((l & 7) >> 1) ^ ((l >> 4) & 3)) * 2 + (l & 1)) << 4))
#define WD_DMA() _Pragma("unroll") for (int i = 0; i < 4; ++i) WD_DMA1(i)
#define WD_DMA1(i)                                                                                   \
      dma16n<(NTM >> 1) & 1>(rwd, (l >> 4) * 2048 + (l & 15) * 16, (unsigned)(slice * 16 + 4 * (i)) * 2048 + w * 256, lbase + L_DN + w * 4096 + (i) * 1024)
#define FFN_DMA(m) dma16n<(NTM >> 2) & 1>((m) & 1 ? rwu : rwg, fnv, (unsigned)(((4 * w + ((m) >> 1)) * 32) * F_ + slice * 16) * 2, lbase + 65536 + (m) * 8192 + w * 1024)
  u32x4 gv[4], uv[4];
  s16x4 dfr[8];
  u32x4 keep[9];
  u32x4 pre[4][2];
#define PRE_LOAD(it_, rot_, s0, s1)                                                                  \
  static_for<(s1) - (s0)>([&](auto c_) __attribute__((always_inline)) {                              \
    constexpr int s = (s0) + decltype(c_)::value;                                                    \
    const unsigned ab_ = OFF_XN + (it_) * (4u << 20) + (((rot_) + (s >> 2)) & 7) * 8192 + ((s >> 1) & 1) * 4096; \
    ld16w<2 * s, (s & 1) * 2048>(pre[s][0], rs, l * 16, ab_ + (p * 8 + rt) * 65536);                         \
    ld16w<2 * s + 1, (s & 1) * 2048 + 1024>(pre[s][1], rs, l * 16, ab_ + (p * 8 + rt) * 65536);                 \
  })
  u32x2 T;
  u32x2 ep;
  u32x4 xl[2];
#define LN_LOAD()                                                                                                                                   \
  {                                                                                                                                                 \
    asm volatile("buffer_load_dwordx4 %0, %1, %2, %3 offen" : "=v"(xl[0]) : "v"(l * 16), "s"(rx), "s"((p * 256 + h * 8 + w) * 2048) : "memory");      \
    asm volatile("buffer_load_dwordx4 %0, %1, %2, %3 offen offset:1024" : "=v"(xl[1]) : "v"(l * 16), "s"(rx), "s"((p * 256 + h * 8 + w) * 2048) : "memory"); \
    asm volatile("buffer_load_dwordx2 %0, off, %1, 0 sc1" : "=v"(ep) : "s"(rs) : "memory");                                                         \
  }
  const GAS bf16* tsrc;
  {
    IDS();
    const int it = 0, rot = ROT0;
    (void)it;
    STAMPAT(0);
    const bf16* pwq = wqkv;
    const rsrc_t rx = RSRC(x, 2048 * 1024 * 2);
    const unsigned wqv = WQ_VOFF(), xv_ = X_VOFF();
    if (tid == 0) *(LDS_AS u32x4*)(smem + L_QC) = u32x4{0u, 0u, 0u, 0u};
    if (w == HLPW && l < 24) TOUCH((const GAS bf16*)wqkv + (bid * 24 + l) * 512 + ((3 - (unsigned)((size_t)wqkv >> 7)) & 7) * 64);
    if (LNP == 3) LN_LOAD();
#pragma unroll
    for (int i = 0; i < 3; ++i) WQ_PIECE(0, 3 * w + i, L_RING);
    STAMPAT(41);
    X_UNIT1(0, 0); X_UNIT1(0, 1); X_UNIT1(1, 0); X_UNIT1(1, 1);
    if (LNP == 2) LN_LOAD();
    STAMPAT(42);
    BAR();
    STAMPAT(43);
    X_UNIT1(2, 0); X_UNIT1(2, 1);
#pragma unroll
    for (int i = 0; i < 3; ++i) WQ_PIECE(1, 3 * w + i, L_RING + STAGE);
    if (LNP == 1) LN_LOAD();
    STAMPAT(44);
    STAMPAT(31);
  }
  unsigned sel0 = 0x7FFFFFFFu;
  f32x2 v[8];
  u32x4 x2p[2], wof;
  float wsum;

  auto layer = [&](auto it_c) __attribute__((always_inline)) {
    constexpr int it = decltype(it_c)::value;
    STAMPAT(1); STAMPAT(22);
    f32x4 acc[2][6];
    f32x2 qs2[2];
    u32x4 k0, k1, v0, v1;
    {
      IDS();
      bool aff = false;
      unsigned aq[2];
      u32x4 av[2];
#define PIN_ACC() asm volatile("" : "+{v[176:179]}"(acc[0][0]), "+{v[180:183]}"(acc[0][1]), "+{v[184:187]}"(acc[0][2]), "+{v[188:191]}"(acc[0][3]), "+{v[192:195]}"(acc[0][4]), "+{v[196:199]}"(acc[0][5]), \
                               "+{v[200:203]}"(acc[1][0]), "+{v[204:207]}"(acc[1][1]), "+{v[208:211]}"(acc[1][2]), "+{v[212:215]}"(acc[1][3]), "+{v[216:219]}"(acc[1][4]), "+{v[220:223]}"(acc[1][5]))
#define PINV4(x) asm volatile("" : "+v"(x[0]), "+v"(x[1]), "+v"(x[2]), "+v"(x[3])::"memory")
      auto qkv_loop = [&](auto it_c) __attribute__((always_inline)) {
        constexpr int IT = decltype(it_c)::value;
        const unsigned abase = OFF_XN + IT * (4u << 20) + (p * 8 + rt) * 65536;
        const int rot = ROT0 + 5 * IT;
        unsigned fkey = 0, kv, bq[2];
        u32x4 fx[4], bv[2], af[WA][2];
        u32x2 cxn, cx1, hh;
        f32x4 sa, sb, cs4[2];
        f32x2 b2[2];
#define AB_OFF() (OFF_AB + IT * 32768 + (p * 8 + rt) * 512)
#define A_LOAD1(k, a) ld16w<(k) % WA * 2 + (a), ((k) & 1) * 2048 + (a) * 1024>(af[(k) % WA][a], rs, l * 16, abase + ((((k) >> 2) + rot) & 7) * 8192 + (((k) >> 1) & 1) * 4096)
        if constexpr (IT == 1) {
#pragma unroll
          for (int s = 0; s < NPRE; ++s) { af[s][0] = pre[s][0]; af[s][1] = pre[s][1]; }
        }
        const bf16* pwq = wqkv;
        ARGP(pwo, 3);
        const rsrc_t rx = RSRC(x, 2048 * 1024 * 2);
        unsigned xv_ = X_VOFF(), xr[2] = {X_ROFF(0), X_ROFF(1)};
        if constexpr (IT == 0) asm volatile("" : "+v"(xv_), "+v"(xr[0]), "+v"(xr[1]));
#define X_READ(u) { af[((u) & ~1) % WA][(u) & 1] = *(const LDS_AS u32x4*)(size_t)(xr[0] + unit_slot(u)); af[((u) | 1) % WA][(u) & 1] = *(const LDS_AS u32x4*)(size_t)(xr[1] + unit_slot(u)); }
        const int tstep = TS0 + bid % TSN + TSD * (w >> TSG);
        const unsigned wqv = WQ_VOFF();
#define QKV_DMA1(t, i)                                                                            \
  {                                                                                               \
    if constexpr (IT == 0) WQ_PIECE(t, 3 * w + (i), L_RING + SLOT(t) * STAGE);                    \
    else dma16s<(i) * 1024>(rs, l * 16, OFF_WI + (h * 8 + (((t) + rot) & 7)) * STAGE + w * 3072, lbase + L_RING + SLOT(t) * STAGE + w * 3072); \
  }
        unsigned bo[2][2];
#pragma unroll
        for (int dt = 0; dt < 2 - IT; ++dt) {
          bo[dt][0] = lbase + L_RING + (IT ? l * 16 : WQ_BOFF(dt)); bo[dt][1] = bo[dt][0] + 2 * STAGE;
          asm volatile("" : "+v"(bo[dt][0]), "+v"(bo[dt][1]));
        }
        auto frag0 = [&](int sl, int f) __attribute__((always_inline)) {
          unsigned pb = lbase + L_RING + WQ_POFF();
          asm volatile("" : "+v"(pb));
          const unsigned o = (pb ^ ((f & 1) << 5)) + sl * STAGE + ((f % 6) >> 1) * 8192 + (f / 6) * 2048;
          frag_t t;
          t.hf.a = trabs(o); t.hf.b = trabs(o + 1024);
          return t.v;
        };
        u32x4 df[4];
        auto dot_mma = [&](const u32x4 (&xf)[4]) __attribute__((always_inline)) {
          sa = mfma16(df[0], xf[0], f32x4{0.f, 0.f, 0.f, 0.f}); sb = mfma16(df[1], xf[1], f32x4{0.f, 0.f, 0.f, 0.f});
          sa = mfma16(df[2], xf[2], sa); sb = mfma16(df[3], xf[3], sb);
        };
        auto dot_store = [&](unsigned dst) __attribute__((always_inline)) {
          if ((l & 15) == 0) st16(unpoison4(__builtin_bit_cast(u32x4, sa + sb)), rs, g * 16, dst + (h * 8 + p) * 512 + w * 64);
        };
        auto tile_sum = [&](u32x4 pt, int dst) __attribute__((always_inline)) {
          f32x4 c = __builtin_bit_cast(f32x4, pt);
#pragma unroll
          for (int e = 0; e < 4; ++e) { c[e] += dppf<0x124>(c[e]); c[e] += dppf<0x128>(c[e]); c[e] = add_x16(c[e]); }
          if ((l & 28) == 0) *(LDS_AS f32x4*)(smem + dst + (w * 2 + (l >> 5)) * 64 + (l & 3) * 16) = c;
        };
        frag_t bfr[6];
#define QKV_BO(s, n, x) (bo[((n) ^ (x)) & 1][SLOT((s) >> 2) >> 1] + (SLOT((s) >> 2) & 1) * STAGE + ((n) >> 1) * 8192 + ((s) & 3) * 2048 + (x) * 256)
#define QKV_RDA(s, n)                                                                             \
  {                                                                                               \
    if constexpr (IT == 0) bfr[n].hf.a = trabs(QKV_BO(s, n, 0));                                  \
    else bfr[n].v = *(const LDS_AS u32x4*)(size_t)(bo[0][SLOT((s) >> 2) >> 1] + (SLOT((s) >> 2) & 1) * STAGE + (((s) & 3) * 6 + (n)) * 1024); \
  }
#define QKV_RDB(s, n) { if constexpr (IT == 0) bfr[n].hf.b = trabs(QKV_BO(s, n, 1)); }
#define XP_OFF() (OFF_XNEW + ((rot + 5 + PO()) & 7) * 1024)
#define FP_VO() (w == 3 ? (l >> 2) * 256 + (l & 3) * 16 : ((l >> 2) & 7) * 512 + (l >> 5) * 64 + (l & 3) * 16)
#define FP_OFF(i) (w == 3 ? OFF_XNEW + (i) * 4096 : OFF_FX + h * 4096 + w * 128)
#define FX_OK(n) __all(((n) == 4 ? max(max(umax4(fx[0]), umax4(fx[1])), max(umax4(fx[2]), umax4(fx[3]))) : max(umax4(fx[0]), umax4(fx[1]))) != POISON)
#define SINCE(s0) (vm_at(IT, s, n) - vm_at(IT, s0, 5))
#define DEF4(x) asm volatile("" : "=v"(x[0]), "=v"(x[1]), "=v"(x[2]), "=v"(x[3]))
        LDS_AS unsigned* qcnt = (LDS_AS unsigned*)(smem + L_QC);
        unsigned qc;
        unsigned qadr = lbase + L_QC, qone = 1;
        asm volatile("" : "+v"(qadr), "+v"(qone));
#define count_in(i) asm volatile("s_mov_b64 exec, 1\n\tds_add_u32 %0, %1 offset:%2\n\ts_mov_b64 exec, -1" ::"v"(qadr), "v"(qone), "n"(4 * (i)) : "memory")
        auto count_wait = [&](int i, unsigned want) __attribute__((always_inline)) {
          if (__builtin_expect((unsigned)__builtin_amdgcn_readfirstlane(qc) < want, 0)) {
            while ((unsigned)__builtin_amdgcn_readfirstlane(__hip_atomic_load(qcnt + i, __ATOMIC_RELAXED, __HIP_MEMORY_SCOPE_WORKGROUP)) < want) { if (SPINS) __builtin_amdgcn_s_sleep(SPINS); }
          }
          asm volatile("" ::: "memory");
        };
        if constexpr (IT == 0) {
          wait_vm<PRE0 - vm_unit(1)>();
          STAMPAT(46);
          X_READ(0); X_READ(1);
          asm volatile("s_barrier" ::: "memory");
        }
        STAMPAT(39);
#pragma unroll
        for (int n = 0; n < 6; ++n) { QKV_RDA(0, n); if (n < 5) QKV_RDB(0, n); }
        f32x2 lv[4], xs, xq;
        float lmean;
        f32x4 sc;
#define X1_OFF() (F_X1 + ((p * RX1 + (xcd >> 1) % RX1) * 32 + (rt >> 2) * 16 + (l & 15)) * SX1)
        unsigned long long xok = 0;
        RowFin fin;
        auto x1_need = [&](unsigned long long m) __attribute__((always_inline)) {
          if (__builtin_expect((xok & m) != m, 0)) {
            for (unsigned spins = 0;;) {
              const u32x2 f = ld8(rs, X1_OFF(), 0);
              xok = __builtin_amdgcn_ballot_w64(f[0] == T[0] && f[1] == T[1]);
              if ((xok & m) == m) break;
              if (spin_fail(spins, rs, T)) { xok = ~0ull; break; }
              __builtin_amdgcn_s_sleep(KSL);
            }
            asm volatile("" ::: "memory");
          }
        };
        auto gap = [&](auto sc_, auto nc_) __attribute__((always_inline)) {
          constexpr int s = decltype(sc_)::value, n = decltype(nc_)::value, ar = a_req(s, n);
          if constexpr ((SYNCB >> IT) & 1) {
            constexpr int tc = (s + 2) / 4;
            if constexpr (tc >= 1 && tc < 8 && s == 4 * tc - 2 && n == 5) {
              if constexpr (!IT || tc >= 3) wait_vm<vm_at(IT, s, n) - vm_tile(IT, tc)>();
              if constexpr (tc == 7 && !IT) asm volatile("s_waitcnt lgkmcnt(4)" ::: "memory");
              asm volatile("s_barrier" ::: "memory");
            }
          } else {
            constexpr int ts = (s + 3) / 4, tc = (s + 2) / 4, t0 = 1;
            if constexpr (ts >= 3 && ts < 8 && s == 4 * ts - 3 && n == 2) { wait_vm<vm_at(IT, s, n) - vm_tile(IT, ts)>(); count_in(0); }
            if constexpr (!IT && tc >= t0 && tc < 3 && s == 4 * tc - 2 && n == 1) { wait_vm<vm_at(IT, s, n) - vm_tile(IT, tc)>(); count_in(0); }
            if constexpr (IT && ts >= t0 && ts < 3 && s == 4 * ts - 3 && n == 2) count_in(0);
            if constexpr (tc >= t0 && tc < 8 && s == 4 * tc - 2) {
              if constexpr (n == 3) qc = __hip_atomic_load(qcnt, __ATOMIC_RELAXED, __HIP_MEMORY_SCOPE_WORKGROUP);
              if constexpr (n == 5) count_wait(0, 8 * (tc + 1 - IT));
            }
          }
          if constexpr (IT == 0 && n == 0 && s + 2 < 32) { wait_vm<vm_at(0, s, 0) - vm_unit(s + 2)>(); X_READ(s + 2); SB(); }
          if constexpr (n == 0 && tile_piece(IT, s)) QKV_DMA1(((s - tp0(IT)) >> 2) + 3, (s - tp0(IT)) & 3);
          if constexpr (IT == 0) {
            if constexpr ((n == 2 || n == 3) && s + 5 < 32) {
              if constexpr (n == 2) asm volatile("s_waitcnt lgkmcnt(%0)" ::"n"(s + 1 < QL ? 4 : 1) : "memory");
              X_UNIT1(s + 5, n - 2);
            }
            if constexpr (s >= W2S && s < W2S + 3 && n == 4) QKV_DMA1(2, s - W2S);
            if constexpr (LNP == 0 && s == LNL && n == 5) LN_LOAD();
            if constexpr (s == 0 && n < 2) { X_UNIT1(3 + n, 0); X_UNIT1(3 + n, 1); }
            if constexpr (s == LNS && n == 1) {
              if constexpr (LNP == 0) wait_vm<vm_at(0, s, n) - vm_at(0, LNL + 1, 0)>();
              asm volatile("" : "+v"(ep), "+v"(xl[0]), "+v"(xl[1])::"memory");
              const unsigned long long e = (((unsigned long long)(unsigned)__builtin_amdgcn_readfirstlane(ep[1]) << 32) | (unsigned)__builtin_amdgcn_readfirstlane(ep[0])) + 1ull + dispatch_id() * 0x9E3779B97F4A7C16ull;
              T = u32x2{(unsigned)e, (unsigned)(e >> 32)};
              unpack8v(xl[0], lv);
              xs = (lv[0] + lv[1]) + (lv[2] + lv[3]);
              xq = __builtin_elementwise_fma(lv[0], lv[0], lv[1] * lv[1]) + __builtin_elementwise_fma(lv[2], lv[2], lv[3] * lv[3]);
              asm volatile("" : "+v"(xs), "+v"(xq));
            }
            if constexpr (s == LNS && n == 4) {
              unpack8v(xl[1], lv);
              xs += (lv[0] + lv[1]) + (lv[2] + lv[3]);
              xq += __builtin_elementwise_fma(lv[0], lv[0], lv[1] * lv[1]) + __builtin_elementwise_fma(lv[2], lv[2], lv[3] * lv[3]);
              auto t = __builtin_amdgcn_permlane32_swap(__builtin_bit_cast(unsigned, hadd(xs)), __builtin_bit_cast(unsigned, hadd(xq)), false, false);
              lmean = asf(t[0]) + asf(t[1]);
              asm volatile("" : "+v"(lmean));
            }
            if constexpr (s == LNS + 1 && n == 1) { lmean += dppf<0xB1>(lmean); lmean += dppf<0x4E>(lmean); lmean += dppf<0x124>(lmean); asm volatile("" : "+v"(lmean)); }
            if constexpr (s == LNS + 1 && n == 4) { lmean = add_x16(lmean + dppf<0x128>(lmean)) * (1.0f / 1024.0f); asm volatile("" : "+v"(lmean)); }
            if constexpr (s == LNS + 2 && n == 1) {
              const float m2 = asf(__builtin_amdgcn_readlane(__builtin_bit_cast(unsigned, lmean), 32));
              xs[0] = ln_rd(fmaxf(__builtin_fmaf(-lmean, lmean, m2), 0.f));
              asm volatile("" : "+v"(xs));
            }
            if constexpr (s == LNS + 2 && n == 5) {
              if (l == 0) st16(u32x4{__builtin_bit_cast(unsigned, (float)xs[0]), __builtin_bit_cast(unsigned, -rbf1(lmean)), T[0], T[1]}, rs, OFF_AB + (p * 256 + h * 8 + w) * 16, 0);
            }
            if constexpr (s == POI && n == 1) {
              if (w == 1) {
                const unsigned prb = 2 * p + (h >> 4), pct = h & 15;
                unsigned pz1 = POISON;
                asm volatile("" : "+v"(pz1));
                const u32x4 pz = {pz1, pz1, pz1, pz1};
                st16(pz, rs, OFF_STAT + (prb * 16 + pct) * 2048 + l * 16, 0);
                st16(pz, rs, OFF_STAT + (prb * 16 + pct) * 2048 + 1024 + l * 16, 0);
                if (l < 16) st16(pz, rs, OFF_CA + prb * 4096 + pct * 256 + l * 16, 0);
                else if (l == 16 && pct < RKEY) st4(pz1, rs, KEY_AT(0, pct) + prb * SKEY, 0);
              }
            }
            if constexpr (s == POI + 4 && n == 1) { if (w == 1 && l < RX) st8(T, rs, F_XN + (p * RX + l) * 256 + h * 8, 0); }
          } else {
            if constexpr ((s == 0 && n == 0) || (s == 2 && n == 2)) asm volatile("buffer_load_dwordx2 %0, %1, %2, 0 offen sc1" : "=v"(cxn) : "v"(X1_OFF()), "s"(rs) : "memory");
            if constexpr ((s == 1 && n == 3) || (s == 4 && n == 0)) {
              wait_vm<vm_at(1, s, n) - (s == 1 ? vm_at(1, 0, 1) : vm_at(1, 2, 3))>();
              asm volatile("" : "+v"(cxn)::"memory");
              xok |= __builtin_amdgcn_ballot_w64(cxn[0] == T[0] && cxn[1] == T[1]);
            }
#define IMG_OFF() (F_X1 + ((((h >> 3) * 2 + ((l >> 2) & 1)) * RX1 + (xcd >> 1) % RX1) * 32 + (l & 1) * 16 + (h & 7) * 2 + ((l >> 1) & 1)) * SX1)
            if constexpr (s == 0 && n == 2) { asm volatile("" : "=v"(cx1)); if (w == 1) asm volatile("buffer_load_dwordx2 %0, %1, %2, 0 offen sc1" : "=v"(cx1) : "v"(IMG_OFF()), "s"(rs) : "memory"); }
            if constexpr (s == 2 && n == 4) {
              if (w == 1) {
                wait_vm<vm_at(1, 2, 4) - vm_at(1, 0, 3)>();
                asm volatile("" : "+v"(cx1)::"memory");
                if (!__all(cx1[0] == T[0] && cx1[1] == T[1])) poll_flags(rs, IMG_OFF(), 0, T);
              }
            }
            if constexpr (ar >= 0 && (ar & 7) == 0) x1_need(3ull << (((rot + ar / 8) & 7) * 2));
            const unsigned R = (rb * 4 + (w & 3)) * 32 + (w >> 2) * 16 + (l & 15), so = OFF_STAT + rb * 32768;
            if constexpr (s == (SPL < 0 ? 0 : SPL) && n == (SPL < 0 ? 1 : 4)) {
#pragma unroll
              for (int i = 0; i < 4; ++i) ld16h<0>(fx[i], rs, (4 * g + i) * 2048 + (R & 127) * 16, so);
            }
            if constexpr (s == SPL + 2 && n == 1) {
              wait_vm<vm_at(1, s, n) - (SPL < 0 ? vm_at(1, 0, 2) : vm_at(1, SPL, 5))>();
              PINV4(fx);
              fin = fin_key(rs, T, g * 8192 + (R & 127) * 16, so, smem + L_SKEY, l, w, R, wsum, fx);
              count_in(2);
            }
            if constexpr (s == SPL + 3 && n == 0) qc = __hip_atomic_load(qcnt + 2, __ATOMIC_RELAXED, __HIP_MEMORY_SCOPE_WORKGROUP);
            if constexpr (s == SPL + 3 && n == 1) { count_wait(2, 8); fin_cand(rs, smem + L_SKEY, 0, g, rb, ct, fin, v); STAMPAT(51); }
            if constexpr (s == SPL + 3 && n == 3) {
              fin_x2(fin, v);
              x2p[0] = pack8v(v); x2p[1] = pack8v(v + 4);
              asm volatile("" : "+v"(x2p[0]), "+v"(x2p[1]));
            }
            if constexpr (s == SPL + 3 && n == 5) {
              if ((w & 3) * 2 + (w >> 2) == ct >> 1) {
                const float f = 1.0f + fin.rd, rd2 = ln_rd(fin.var * f * f), mb2 = rbf1(__builtin_fmaf(fin.mean - fin.mb, fin.rd, fin.mean));
                if (g == 0 && ((l >> 3) & 1) == (ct & 1))
                  st16(u32x4{__builtin_bit_cast(unsigned, f * rd2), __builtin_bit_cast(unsigned, -__builtin_fmaf(fin.mb, fin.rd, mb2) * __builtin_amdgcn_rcpf(f)), T[0], T[1]}, rs, OFF_AB + 32768 + R * 16, 0);
              }
            }
          }
          if constexpr (IT == 1 && ar >= 0) A_LOAD1(ar / 2, ar & 1);
          if constexpr (!IT && s == CSL && n == 4) { asm volatile("" : "=v"(fx[0])); if (w < 6) ld16h<0>(fx[0], rs, l * 16, OFF_CS + (h * 6 + w) * 1024); }
          if constexpr (!IT && s == CSS && n == 2) {
            if (w < 6) {
              wait_vm<SINCE(CSL)>();
              asm volatile("" : "+v"(fx[0])::"memory");
              tag_wait(rs, T, l * 16, OFF_CS + (h * 6 + w) * 1024, fx[0]);
              const f32x2 c = {sum_hi8(asf(fx[0][0])), sum_hi8(asf(fx[0][1]))};
              if (l < 8) *(LDS_AS f32x2*)(smem + L_CSL + w * 64 + l * 8) = c;
            }
          }
          constexpr int ABL = abl(IT), ABS = abs_(IT);
          if constexpr (s == ABL && n == 4) ld16h<0>(bv[0], rs, (l & 31) * 16, AB_OFF());
          if constexpr (s == ABS && n == 2) {
            wait_vm<SINCE(ABL)>();
            asm volatile("" : "+v"(bv[0])::"memory");
            tag_wait(rs, T, (l & 31) * 16, AB_OFF(), bv[0]);
            const unsigned t0 = bv[0][0], t1 = bv[0][1];
            ((LDS_AS unsigned*)(smem + L_ABW + rt * 2048))[l] = l < 32 ? t0 : t1;
          }
          if constexpr (s == ABS && n == 3) {
#pragma unroll
            for (int a = 0; a < 2; ++a) {
              const LDS_AS unsigned* aw = (const LDS_AS unsigned*)(smem + L_ABW + rt * 2048) + 16 * a;
              aq[a] = aw[l & 15]; bq[a] = aw[32 + (l & 15)];
              av[a] = *(const LDS_AS u32x4*)(aw + 4 * g); bv[a] = *(const LDS_AS u32x4*)(aw + 32 + 4 * g);
            }
          }
          if constexpr (s == ABS && n == 5) {
#pragma unroll
            for (int a = 0; a < 2; ++a) { b2[a] = f32x2{asf(bq[a]), asf(bq[a])}; asm volatile("" : "+v"(b2[a])); }
          }
          if constexpr (s == ABS + 1) {
            constexpr int k = (n + 4) % 6;
            if constexpr (k < 4) cs4[k & 1] = *(const LDS_AS f32x4*)(smem + L_CSL + k * 64 + g * 16);
            else cs4[k & 1][0] = *(const LDS_AS float*)(smem + L_CSL + k * 64 + (l & 15) * 4);
          }
          if constexpr ((s == ABS + 1 && n > 0) || (s == ABS + 2 && n == 0)) {
            constexpr int k = (n + 3) % 6;
            if constexpr (k < 4) {
#pragma unroll
              for (int a = 0; a < 2; ++a) acc[a][k] = __builtin_elementwise_fma(cs4[k & 1], f32x4{b2[a][0], b2[a][1], b2[a][0], b2[a][1]}, acc[a][k]);
            } else {
              f32x2 c2 = {cs4[k & 1][0], cs4[k & 1][0]};
              asm volatile("" : "+v"(c2));
#pragma unroll
              for (int a = 0; a < 2; ++a) acc[a][k] = __builtin_elementwise_fma(f32x4{c2[0], c2[1], c2[0], c2[1]}, __builtin_bit_cast(f32x4, bv[a]), acc[a][k]);
            }
          }
          if constexpr (IT == 0) {
            if constexpr (s == CSD && n < 3) fx[n] = frag0(SLOT((s >> 2) + PO()), 3 * w + n);
            if constexpr (s == CSD + 1 && n == 1) {
              unsigned one2 = 0x3F803F80u;
              asm volatile("" : "+v"(one2));
              const u32x4 o4 = {one2, one2, one2, one2};
              const f32x4 z4 = {0.f, 0.f, 0.f, 0.f};
              sa = mfma16(fx[0], o4, z4); sb = mfma16(fx[1], o4, z4); sc = mfma16(fx[2], o4, z4);
            }
            if constexpr (s == CSD + 2 && n == 1) {
              const int c = l & 15;
              const f32x4 t0 = sa, t1 = sb, t2 = sc, t = c == 0 ? +t0 : c == 1 ? +t1 : +t2;
              if (c < 3) *(LDS_AS f32x4*)(smem + L_CSP + (w * 3 + c) * 64 + g * 16) = t;
              count_in(2);
            }
            if constexpr (s == CSD + 4 && n == 1) {
              if (w == 2) {
                qc = 0;
                count_wait(2, 8);
                const LDS_AS f32x2* cp = (const LDS_AS f32x2*)(smem + L_CSP) + min(l, 47);
                const f32x2 t = (cp[0] + cp[48]) + (cp[96] + cp[144]);
                if (l < 48) st16(u32x4{__builtin_bit_cast(unsigned, (float)t[0]), __builtin_bit_cast(unsigned, (float)t[1]), T[0], T[1]}, rs, (l >> 3) * 1024 + (l & 7) * 16, OFF_CS + h * 6144 + p * 128);
              }
            }
            if constexpr (s >= 29 && (n & 1)) keep[3 * (s - 29) + (n >> 1)] = frag0(SLOT(s - 24), 3 * w + (n >> 1));
            if constexpr (s == (TS0 < TEX ? TS0 : TEX) - 2 && n == 1) {
              ARGP(pwg, 5); ARGP(pwu, 6);
              const int t = tid < 288 ? tid - 64 : tid - 288, go = (bi * 32 + t / 7) * F_ + xcd * 448 + (t % 7) * 64;
              tsrc = tid < 64 ? pwo + bid * 4096 + tid * 64 : tid < 288 ? pwg + go : pwu + go;
              asm volatile("" : "+v"(tsrc));
            }
            if constexpr (s >= TS0 && s < TS0 + TSN + TSD * (7 >> TSG) && n == 4) {
              if (tstep == s && ((TM0 >> w) & 1)) TOUCH(tsrc);
            }
            if constexpr (s == TEX && n == 5) {
              const GAS bf16* t2 = ((TM1 >> w) & 1) ? tsrc : (const GAS bf16*)x + p * 262144;
              if ((NTM & 1) && w > 0) asm volatile("s_mov_b32 m0, %0\n\ts_nop 0\n\tglobal_load_lds_dword %1, off nt" ::"s"(lbase + L_FXL), "v"(t2) : "memory");
              else TOUCH(t2);
            }
          } else {
            if constexpr (s >= 27 && s < 30 && (n == 0 || n == 3)) { const unsigned wov = WO_VOFF(); WO_PIECE(4 + 2 * (s - 27) + n / 3); }
            if constexpr (WON && s == WOS && n == 4) st16(wof, rs, (ct * 128 + rb * 8 + w) * 1024 + l * 16, OFF_WOI);
            if constexpr (WON && s == 19 && n == 1) { if (tid == 64) st8(T, rs, (ct * 16 + rb) * 256, F_WOI); }
            if constexpr (WON && s == 23 && n == 4) { asm volatile("" : "=v"(cx1)); if (w == 2) ld8h(cx1, rs, (l & 15) * 256, F_WOI + ct * 4096); }
            if constexpr (WON && s == 26 && n == 3) {
              if (w == 2) {
                wait_vm<SINCE(23)>();
                asm volatile("" : "+v"(cx1)::"memory");
                if (!__all(cx1[0] == T[0] && cx1[1] == T[1])) poll_flags(rs, (l & 15) * 256, F_WOI + ct * 4096, T);
              }
            }
            if constexpr (s == FKL && n == 4) { asm volatile("" : "=v"(kv)); if (w == 0) ld4h<0>(kv, rs, (l & 15) * SKEY, KEY_AT(0, KREP())); }
            if constexpr (s == FSL && n == 1) {
              asm volatile("" : "=v"(fx[0]), "=v"(fx[1]));
              if (w == 0) {
                wait_vm<SINCE(FKL)>();
                asm volatile("" : "+v"(kv)::"memory");
                STAMPAT(52);
                const unsigned k_ = key_wait(rs, T, l, 0, KREP(), kv);
                STAMPAT(53);
                if (l == 0) *(LDS_AS unsigned*)(smem + L_SKEY + 32) = k_;
                fkey = k_;
                ld16h<0>(fx[0], rs, (l >> 3) * 256 + (l & 7) * 16, cand_off(k_, 0)); ld16h<2048>(fx[1], rs, (l >> 3) * 256 + (l & 7) * 16, cand_off(k_, 0));
              }
            }
            if constexpr (s == FS0 - 1 && n == FSN) {
              if (w == 0) {
                wait_vm<vm_at(1, s, n) - vm_at(1, FSL, 2)>();
                asm volatile("" : "+v"(fx[0]), "+v"(fx[1])::"memory");
                word_wait<2>(rs, T, (l >> 3) * 256 + (l & 7) * 16, cand_off(fkey, 0), fx);
                *(LDS_AS u32x4*)(smem + L_ROW + l * 16) = fx[0]; *(LDS_AS u32x4*)(smem + L_ROW + 1024 + l * 16) = fx[1];
                count_in(0);
              }
            }
            if constexpr (s == FS0 - 1 && n == 1) {
              asm volatile("" : "=v"(dfr[0]), "=v"(dfr[1]), "=v"(dfr[2]), "=v"(dfr[3]), "=v"(dfr[4]), "=v"(dfr[5]), "=v"(dfr[6]), "=v"(dfr[7]));
              if (ffn_on) {
#pragma unroll
                for (int i = 0; i < 8; ++i) dfr[i] = trread(smem, L_DN + w * 4096 + g * 1024 + ((l & 15) >> 2) * 256 + i * 32 + (l & 3) * 8);
              }
            }
            if constexpr (s == FS0) {
              if constexpr (n == 0) qc = __hip_atomic_load(qcnt, __ATOMIC_RELAXED, __HIP_MEMORY_SCOPE_WORKGROUP);
              if constexpr (n == 1) {
                count_wait(0, 1);
                kv = *(const LDS_AS unsigned*)(smem + L_SKEY + 32);
#pragma unroll
                for (int i = 0; i < 4; ++i) fx[i] = *(const LDS_AS u32x4*)(smem + L_ROW + w * 256 + i * 64 + g * 16);
              }
              if constexpr (n == 2) {
                fkey = __builtin_amdgcn_readfirstlane(kv);
                sel0 = key_row(fkey);
                aff = (int)(sel0 >> 8) == p;
                asm volatile("" : "=v"(sa), "=v"(sb));
                if (ffn_on) ffn_gu(gv, uv, fx, sa, sb);
              }
              if constexpr (n == 4) {
                if (ffn_on) {
                  ffn_gu_store(smem, l, w, sa, sb);
                  count_in(1);
                }
              }
            }
            if constexpr (s == FS0D) {
              if constexpr (n == 0) qc = __hip_atomic_load(qcnt + 1, __ATOMIC_RELAXED, __HIP_MEMORY_SCOPE_WORKGROUP);
              if constexpr (n == 1) { asm volatile("" : "=v"(sa)); if (ffn_on) { count_wait(1, 8); sa = ffn_d_read(smem, l); } }
              if constexpr (n == 2) { asm volatile("" : "=v"(hh)); if (ffn_on) hh = ffn_d_h(sa); }
              if constexpr (n == 4) { asm volatile("" : "=v"(sa)); if (ffn_on) sa = ffn_d_all(dfr, hh); }
              if constexpr (n == 5) { if (ffn_on) ffn_d_store(rs, l, w, 0, slice, sa); STAMPAT(54); }
            }
            if constexpr (s == FS1 && n == 4) {
              DEF4(fx);
              asm volatile("" : "=v"(cxn), "=v"(cx1));
              if (!ffn_on) {
                const unsigned G = xcd * 4 + bi - 28;
#pragma unroll
                for (int k = 0; k < 4; ++k) ld16h<0>(fx[k], rs, RED_VO(k), RED_SO(0, G));
                ld8h(cxn, rs, RED_JJ(), cand_off(fkey, 0) + XO(G)); ld8h(cx1, rs, RED_JJ() + 128, cand_off(fkey, 0) + XO(G));
              }
            }
            if constexpr (s == FS1S && n == 2) {
              if (!ffn_on) {
                wait_vm<SINCE(FS1)>();
                PINV4(fx);
                asm volatile("" : "+v"(cxn), "+v"(cx1)::"memory");
                STAMPAT(55);
                red_sum(rs, smem, T, tid, w, 0, xcd * 4 + bi - 28, fkey, fx, cxn, cx1);
                STAMPAT(56);
                BAR();
                if (w == 0) {
                  const u32x2 ov = red_final(smem, l, fkey, cxn, cx1);
                  if (l < 8) st8(unpoison2(ov), rs, OFF_XNEW + (xcd * 4 + bi - 28) * 256 + ((l & 3) * 2 + (l >> 2)) * 8, 0);
                }
              }
            }
            if constexpr (s == FXA && n == 4) { asm volatile("" : "=v"(fx[0])); if (w == 1) ld16h<0>(fx[0], rs, ((l >> 2) & 3) * 256 + (l & 3) * 16, XP_OFF()); }
            if constexpr (s == FXB - 1 && n == FSN) {
              if (w == 1) {
                wait_vm<SINCE(FXA)>();
                asm volatile("" : "+v"(fx[0])::"memory");
                word_wait<1>(rs, T, ((l >> 2) & 3) * 256 + (l & 3) * 16, XP_OFF(), fx);
                if (l < 16) *(LDS_AS u32x4*)(smem + L_ROW + l * 16) = fx[0];
                count_in(0);
              }
            }
            if constexpr (s == FXB) {
#define FX_FRAG(i) (*(const LDS_AS u32x4*)(size_t)(bo[0][0] + (1 + PO()) * STAGE + (6 * (i) + w) * 1024))
              if constexpr (n == 0) { STAMPAT(58); qc = __hip_atomic_load(qcnt, __ATOMIC_RELAXED, __HIP_MEMORY_SCOPE_WORKGROUP); }
              if constexpr (n == 1) {
                DEF4(fx); DEF4(df);
                if (w < 6) {
                  count_wait(0, 2);
#pragma unroll
                  for (int i = 0; i < 4; ++i) { fx[i] = *(const LDS_AS u32x4*)(smem + L_ROW + i * 64 + g * 16); df[i] = FX_FRAG(i); }
                }
              }
              if constexpr (n == 2) { asm volatile("" : "=v"(sa), "=v"(sb)); if (w < 6) dot_mma(fx); }
              if constexpr (n == 4) { if (w < 6) dot_store(OFF_FX); STAMPAT(57); }
            }
            if constexpr (s == FXL && n == 4) {
              asm volatile("" : "=v"(fx[0]), "=v"(fx[1]));
              if (aff && w < 4) { ld16h<0>(fx[0], rs, FP_VO(), FP_OFF(0)); ld16h<0>(fx[1], rs, FP_VO(), FP_OFF(1)); }
            }
            if constexpr (s == FXS && n == 4) {
              if (aff && w < 4) {
                STAMPAT(59);
                wait_vm<0>();
                asm volatile("" : "+v"(fx[0]), "+v"(fx[1])::"memory");
                if (!FX_OK(2)) {
                  unsigned spins = 0;
                  do {
                    if (spin_fail(spins, rs, T)) break;
                    fx[0] = ld16(rs, FP_VO(), FP_OFF(0)); fx[1] = ld16(rs, FP_VO(), FP_OFF(1));
                  } while (!FX_OK(2));
                }
                STAMPAT(60);
                if (w == 3) {
                  f32x2 nx[8];
                  unpack8v(fx[0], nx); unpack8v(fx[1], nx + 4);
                  const f32x2 sm = ((nx[0] + nx[1]) + (nx[2] + nx[3])) + ((nx[4] + nx[5]) + (nx[6] + nx[7]));
                  const float mean = sum64(hadd(sm)) * (1.0f / 1024.0f);
                  const f32x2 m2 = {mean, mean};
                  f32x2 q = {0.f, 0.f};
#pragma unroll
                  for (int j = 0; j < 8; ++j) { const f32x2 d = nx[j] - m2; q = __builtin_elementwise_fma(d, d, q); }
                  const float rd = ln_rd(sum64(hadd(q)) * (1.0f / 1024.0f));
                  if (l == 0) *(LDS_AS f32x2*)(smem + L_FXL + 384) = f32x2{rd, -rbf1(mean)};
                } else {
                  tile_sum(fx[0], L_FXL);
                }
                count_in(3);
              }
            }
          }
        };
        static_for<32>([&](auto sc_) __attribute__((always_inline)) {
          constexpr int s = decltype(sc_)::value;
          if constexpr (s > 0) PIN_ACC();
          if constexpr (QPR && s == QPR) { if (w >= 4) __builtin_amdgcn_s_setprio(1); }
          if constexpr (IT == 1) wait_vm<vm_at(IT, s, 0) - vm_a(IT, s)>();
          pin_w<s % WA>(af[s % WA][0], af[s % WA][1]);
          VSTEP(s);
          SB();
          static_for<6>([&](auto nc_) __attribute__((always_inline)) {
            constexpr int n = decltype(nc_)::value;
            if constexpr (s == 0) { acc[0][n] = f32x4{0.f, 0.f, 0.f, 0.f}; acc[1][n] = acc[0][n]; }
            constexpr bool on = s < QL || n >= 2;
            if constexpr (on) acc[0][n] = n < 4 ? mfma16(bfr[n].v, af[s % WA][0], acc[0][n]) : mfma16(af[s % WA][0], bfr[n].v, acc[0][n]);
            SB();
            if constexpr (n == 0) { QKV_RDB(s, 5); }
            else if constexpr (s + 1 < 32 && (s + 1 < QL || n > 2)) QKV_RDB(s + 1, n - 1);
            SB();
            if constexpr (on) acc[1][n] = n < 4 ? mfma16(bfr[n].v, af[s % WA][1], acc[1][n]) : mfma16(af[s % WA][1], bfr[n].v, acc[1][n]);
            SB();
            if constexpr (s + 1 < 32 && (s + 1 < QL || n >= 2)) QKV_RDA(s + 1, n);
            SB();
            gap(sc_, nc_);
            GSTEP(s, n);
            SB();
          });
        });
        PIN_ACC();
        VSTEP(32);
        STAMPAT(18);
        VSTEP(33);
        frag_t qw[32 - QL][2];
        static_for<32 - QL>([&](auto c_) __attribute__((always_inline)) {
          constexpr int s = QL + decltype(c_)::value;
          QKV_RDA(s, 0); QKV_RDB(s, 0); QKV_RDA(s, 1); QKV_RDB(s, 1);
          qw[s - QL][0] = bfr[0]; qw[s - QL][1] = bfr[1];
        });
        SB();
        const bool own = aff && (int)((sel0 >> 5) & 7) == rt;
        const bool m0 = !(sel0 & 16), mq = (l & 15) == (int)(sel0 & 15), mv = g == (int)((sel0 & 15) >> 2);
        auto patch_qk = [&](int n) __attribute__((always_inline)) {
          f32x4 t = *(const LDS_AS f32x4*)(smem + L_FXL + n * 64 + g * 16);
          const f32x4 c = *(const LDS_AS f32x4*)(smem + L_CSL + n * 64 + g * 16);
          const float nb = *(const LDS_AS float*)(smem + L_FXL + 388);
#pragma unroll
          for (int e = 0; e < 4; ++e) { t[e] = __builtin_fmaf(c[e], nb, t[e]); asm volatile("" : "+v"(t[e])); }
          acc[0][n] = (mq && m0) ? t : acc[0][n];
          acc[1][n] = (mq && !m0) ? t : acc[1][n];
        };
        if (own) {
          while (__hip_atomic_load(qcnt + 3, __ATOMIC_RELAXED, __HIP_MEMORY_SCOPE_WORKGROUP) < 4u) {}
          asm volatile("" ::: "memory");
          patch_qk(2); patch_qk(3);
          const f32x2 nab = *(const LDS_AS f32x2*)(smem + L_FXL + 384);
          const LDS_AS float* fl_ = (const LDS_AS float*)(smem + L_FXL + 256) + (l & 15);
          const LDS_AS float* cl_ = (const LDS_AS float*)(smem + L_CSL + 256) + (l & 15);
          const float t4 = __builtin_fmaf(cl_[0], nab[1], fl_[0]), t5 = __builtin_fmaf(cl_[16], nab[1], fl_[16]);
          const unsigned na = __builtin_bit_cast(unsigned, (float)nab[0]);
#pragma unroll
          for (int a = 0; a < 2; ++a) {
            const bool ma = a ? !m0 : m0;
            aq[a] = (mq && ma) ? na : aq[a];
#pragma unroll
            for (int e = 0; e < 4; ++e) {
              const bool me = mv && ma && e == (int)(sel0 & 3);
              acc[a][4][e] = me ? t4 : acc[a][4][e]; acc[a][5][e] = me ? t5 : acc[a][5][e];
              av[a][e] = me ? na : av[a][e];
            }
          }
        }
        constexpr float C2 = 1.4426950408889634f * 0.17677669529663687f;
#pragma unroll
        for (int a = 0; a < 2; ++a) {
          f32x2 a2 = {asf(aq[a]), asf(aq[a])};
          qs2[a] = f32x2{asf(aq[a]) * C2, asf(aq[a]) * C2};
          asm volatile("" : "+v"(a2), "+v"(qs2[a]));
          acc[a][2] *= f32x4{a2[0], a2[1], a2[0], a2[1]}; acc[a][3] *= f32x4{a2[0], a2[1], a2[0], a2[1]};
          acc[a][4] *= __builtin_bit_cast(f32x4, av[a]); acc[a][5] *= __builtin_bit_cast(f32x4, av[a]);
        }
        k0 = pack8(acc[0][2], acc[0][3]); k1 = pack8(acc[1][2], acc[1][3]); v0 = pack8(acc[0][4], acc[1][4]); v1 = pack8(acc[0][5], acc[1][5]);
        const unsigned kvo = OFF_KV + IT * (8u << 20) + (p * 32 + h) * 32768 + rt * 4096;
        if (p < 7) { st16(k0, rs, l * 16, kvo); st16(k1, rs, l * 16 + 1024, kvo); st16(v0, rs, l * 16 + 2048, kvo); st16(v1, rs, l * 16 + 3072, kvo); }
        char* ownp = smem + (8 + rt) * 2048 + l * 16;
        *(u32x4*)(ownp + L_K) = k0;
        if (rt < 7) *(u32x4*)(ownp + L_K + 1024) = k1;
        *(u32x4*)(ownp + L_V) = v0; *(u32x4*)(ownp + L_V + 1024) = v1;
        SB();
        static_for<32 - QL>([&](auto c_) __attribute__((always_inline)) {
          constexpr int s = QL + decltype(c_)::value;
#pragma unroll
          for (int n = 0; n < 2; ++n) { acc[0][n] = mfma16(qw[s - QL][n].v, af[s % WA][0], acc[0][n]); acc[1][n] = mfma16(qw[s - QL][n].v, af[s % WA][1], acc[1][n]); }
        });
        if (own) { patch_qk(0); patch_qk(1); }
      };
      qkv_loop(it_c);
    }
#undef A_LOAD1
#undef QKV_RDA
#undef QKV_RDB
#undef QKV_BO
    STAMPAT(2); STAMPAT(21);
    {
      IDS();
      const GAS bf16 *pwo, *pwg, *pwu;
      asm volatile("s_load_dwordx2 %0, %3, 24\n\ts_load_dwordx2 %1, %3, 40\n\ts_load_dwordx2 %2, %3, 48" : "=&s"(pwo), "=&s"(pwg), "=&s"(pwu) : "s"(ka));
      const int ql = l & 15;
      const f32x4 z4 = {0.f, 0.f, 0.f, 0.f}, ninf = {-INFINITY, -INFINITY, -INFINITY, -INFINITY};
      LDS_AS unsigned* rdy = (LDS_AS unsigned*)(smem + L_RDY);
      const unsigned rtag = T[0] + it;
      u32x4 qf[2];
      f32x4 cn[2], oa[2][2], sum[2], S[2][2];
      bool told = false;
      auto announce = [&](bool now) __attribute__((always_inline)) {
        if (!told) {
          unsigned st;
          asm volatile("s_getreg_b32 %0, hwreg(HW_REG_IB_STS)" : "=s"(st)::"memory");
          if (now || !(st & 0xC0000Fu)) {
            DRAIN();
            if (l == 0) st8(T, rs, rt * SKW, F_KV + (it * 256 + p * 32 + h) * SKV);
            told = true;
            STAMPAT(34);
          }
        }
      };
      unsigned one2 = 0x3F803F80u;
      asm volatile("" : "+v"(one2));
      const u32x4 ones = {one2, one2, one2, one2};
      auto ex8 = [&](f32x4 a, f32x4 b) __attribute__((always_inline)) {
        return u32x4{pack2(__builtin_amdgcn_exp2f(a[0]), __builtin_amdgcn_exp2f(a[1])), pack2(__builtin_amdgcn_exp2f(a[2]), __builtin_amdgcn_exp2f(a[3])),
                     pack2(__builtin_amdgcn_exp2f(b[0]), __builtin_amdgcn_exp2f(b[1])), pack2(__builtin_amdgcn_exp2f(b[2]), __builtin_amdgcn_exp2f(b[3]))};
      };
      {
#pragma unroll
        for (int a = 0; a < 2; ++a) {
          const f32x4 c4 = {qs2[a][0], qs2[a][1], qs2[a][0], qs2[a][1]};
          qf[a] = pack8(acc[a][0] * c4, acc[a][1] * c4);
        }
        f32x4 mk;
#pragma unroll
        for (int e = 0; e < 4; ++e) mk[e] = (4 * g + e <= ql) ? 0.f : -INFINITY;
        const f32x4 d0 = mfma16(k0, qf[0], mk), d1 = mfma16(k1, qf[1], mk), d2 = mfma16(k0, qf[1], z4);
        const float m0 = -max_x32(max_x16(fmaxf(fmaxf(d0[0], d0[1]), fmaxf(d0[2], d0[3]))));
        const float m1 = -max_x32(max_x16(fmaxf(fmaxf(d1[0], d1[1]), fmaxf(d1[2], d1[3]))));
        cn[0] = f32x4{m0, m0, m0, m0}; cn[1] = f32x4{m1, m1, m1, m1};
        asm volatile("" : "+v"(cn[0]), "+v"(cn[1]));
        announce(false);
        const f32x4 e0 = d0 + cn[0];
        const u32x4 p0 = {pack2(__builtin_amdgcn_exp2f(e0[0]), __builtin_amdgcn_exp2f(e0[1])), pack2(__builtin_amdgcn_exp2f(e0[2]), __builtin_amdgcn_exp2f(e0[3])), 0u, 0u};
        const u32x4 p1 = ex8(d2 + cn[1], d1 + cn[1]);
        oa[0][0] = mfma16(v0, p0, z4); oa[0][1] = mfma16(v1, p0, z4); sum[0] = mfma16(ones, p0, z4);
        oa[1][0] = mfma16(v0, p1, z4); oa[1][1] = mfma16(v1, p1, z4); sum[1] = mfma16(ones, p1, z4);
      }
      const char* kb_ = smem + L_K + l * 16;
      const char* vb_ = smem + L_V + l * 16;
      const unsigned wov = WO_VOFF(), fnv = (l >> 1) * (F_ * 2) + (l & 1) * 16;
      asm volatile("" : "+v"(oa[0][0]), "+v"(oa[0][1]), "+v"(oa[1][0]), "+v"(oa[1][1]), "+v"(sum[0]), "+v"(sum[1]));
      const unsigned pw = __builtin_amdgcn_readfirstlane(it * 256 + p * 32 + h - 32), pfo = F_KV + pw * SKV + rt * SKW;
      const bool fetcher = rt < 4 && p > 0;
      announce(false);
      if (QPR) __builtin_amdgcn_s_setprio(0);
      STAMPAT(32);
      BAR();
      asm volatile("" : "+s"(pwo), "+s"(pwg), "+s"(pwu));
      STAMPAT(33);
#define KV_FETCH(gd)                                                                                                          \
  {                                                                                                                           \
    const unsigned pvo = OFF_KV + pw * 32768 + (gd) * 4096;                                                                   \
    dma16s<0>(rs, l * 16, pvo, lbase + L_K + (gd) * 2048); dma16s<1024>(rs, l * 16, pvo, lbase + L_K + (gd) * 2048);           \
    dma16s<0>(rs, l * 16, pvo + 2048, lbase + L_V + (gd) * 2048); dma16s<1024>(rs, l * 16, pvo + 2048, lbase + L_V + (gd) * 2048); \
  }
      auto fetch = [&]() __attribute__((always_inline)) {
        poll_flags(rs, KVJ ? (l & 1) * 4 * SKW : 0, pfo, T);
        KV_FETCH(rt);
        if (KVJ) { KV_FETCH(rt + 4); if (l == 0) rdy[8 + rt] = rtag; }
        STAMPAT(35);
      };
      announce(false);
      const bool first = fetcher && told;
      if (first) fetch();
#define LDS16(a_) (*(const LDS_AS u32x4*)(size_t)(a_))
      u32x4 kf[2], vf[2], p0, p1;
#define AE(a, i) { S[a][(i) >> 2][(i) & 3] = __builtin_amdgcn_exp2f(S[a][(i) >> 2][(i) & 3]); asm volatile("" : "+v"(S[a][(i) >> 2][(i) & 3])); SB(); }
#define AC(a, P, j) { P[j] = pack2(S[a][(j) >> 1][2 * ((j) & 1)], S[a][(j) >> 1][2 * ((j) & 1) + 1]); asm volatile("" : "+v"(P[j])); SB(); }
#define PHASE(a, P, M1, M2, L2, M3, M4, L4, M5)                                                                               \
  AE(a, 0) AE(a, 1) M1; SB(); AE(a, 2) AE(a, 3) M2; SB(); L2; SB(); AC(a, P, 0) AC(a, P, 1) AE(a, 4) M3; SB(); AE(a, 5) AE(a, 6) M4; SB(); \
  L4; SB(); AE(a, 7) AC(a, P, 2) M5; SB(); AC(a, P, 3)
#define PV(a, d, P) oa[a][d] = mfma16(vf[d], P, oa[a][d])
#define RS(a, P) sum[a] = mfma16(ones, P, sum[a])
#define SC(a, t, c) S[a][t] = mfma16(kf[t], qf[a], c)
#define K_AT(ad) (kf[0] = LDS16(ad), kf[1] = LDS16((ad) + 1024))
#define V_AT(ad) (vf[0] = LDS16((ad) + L_V), vf[1] = LDS16((ad) + L_V + 1024))
#define A_FIRST(ad, c00, c01, c10, c11, KN) { K_AT(ad); V_AT(ad); SB(); SC(0, 0, c00); SC(0, 1, c01); SB(); PHASE(0, p0, SC(1, 0, c10), SC(1, 1, c11), KN, , , , ) }
#define A_NEXT(ad, c00, c01, c10, c11, KN)                                                                                        \
  { PHASE(1, p1, PV(0, 0, p0), PV(0, 1, p0), , SC(0, 0, c00), SC(0, 1, c01), , RS(0, p0))                                     \
    PHASE(0, p0, PV(1, 0, p1), PV(1, 1, p1), V_AT(ad), SC(1, 0, c10), SC(1, 1, c11), KN, RS(1, p1)) }
#define A_LAST() { PHASE(1, p1, PV(0, 0, p0), PV(0, 1, p0), , RS(0, p0), , , ) SB(); PV(1, 0, p1); PV(1, 1, p1); RS(1, p1); }
      int nw = 0;
      auto piece = [&]() __attribute__((always_inline)) {
        if constexpr (it == 0) { if (ffn_on && nw < 8) { FFN_DMA(nw); } ++nw; }
      };
#define XN_OFF() (F_XN + (((rt - 6) * 4 + g) * RX + bi % RX) * 256 + (l & 15) * 16)
      u32x4 fxn;
      asm volatile("" : "=v"(fxn));
      unsigned ad = lbase + l * 16 + (6 + rt) * 2048;
      asm volatile("" : "+v"(ad));
      if (rt > 0) {
        A_FIRST(ad + 2048, cn[0], cn[0], cn[1], cn[1], K_AT(ad));
#pragma unroll 1
        for (int i = rt - 1; i > 0; --i) {
          ad -= 2048;
          asm volatile("" : "+v"(ad));
          if (PCE && told && rt >= 4) piece();
          announce(false);
          A_NEXT(ad + 2048, cn[0], cn[0], cn[1], cn[1], K_AT(ad));
        }
        announce(false);
        A_LAST();
      }
      announce(true);
      STAMPAT(26);
      {
        f32x4 mk;
#pragma unroll
        for (int e = 0; e < 4; ++e) mk[e] = (4 * g + e > ql) ? 0.f : -INFINITY;
        f32x4 cm0 = cn[0] + mk, cm1 = cn[1] + mk, cmi = ninf;
        asm volatile("" : "+v"(cm0), "+v"(cm1), "+v"(cmi));
        if (rt >= 4) {
          if (p > 0) {
            while (!__all(__hip_atomic_load(rdy + 8 + (l & 3), __ATOMIC_RELAXED, __HIP_MEMORY_SCOPE_WORKGROUP) == rtag)) __builtin_amdgcn_s_sleep(1);
            asm volatile("" ::: "memory");
          }
          if constexpr (it == 0) { for (; nw < 8;) piece(); }
          static_for<it ? 12 : 8>([&](auto c_) __attribute__((always_inline)) { constexpr int i = decltype(c_)::value; WO_PIECE_W(15 - i / 2, 2 * (rt & 3) + (i & 1)); });
          if (it == 0 && rt >= 6) ld16h<0>(fxn, rs, XN_OFF(), 0);
        } else if (p > 0) {
          if (!first) fetch();
          if (KVJ) {
            DRAIN();
            if (l < 2) rdy[rt + 4 * l] = rtag;
            if constexpr (it == 0) { for (; nw <= rt;) piece(); }
          } else {
            const u32x2 f2 = ld8(rs, 0, pfo + 4 * SKW);
            const bool up = __all(f2[0] == T[0] && f2[1] == T[1]);
            DRAIN();
            if (l == 0) rdy[rt] = rtag;
            if (!up) poll_flags(rs, 0, pfo + 4 * SKW, T);
            KV_FETCH(rt + 4);
            if (l == 0) rdy[8 + rt] = rtag;
            told = false;
          }
        }
        STAMPAT(3);
        if (p > 0) {
          auto landed = [&](bool now) __attribute__((always_inline)) {
            if (!told) {
              unsigned st;
              asm volatile("s_getreg_b32 %0, hwreg(HW_REG_IB_STS)" : "=s"(st)::"memory");
              if (now || !(st & 0xC0000Fu)) {
                DRAIN();
                if (l == 0) rdy[rt + 4] = rtag;
                told = true;
              }
            }
          };
          unsigned have = 0;
          auto need = [&](int gq) __attribute__((always_inline)) {
            if (__builtin_expect(gq < 8 && !((have >> gq) & 1), 0)) {
              do {
                const unsigned t = __hip_atomic_load(rdy + (l & 7), __ATOMIC_RELAXED, __HIP_MEMORY_SCOPE_WORKGROUP);
                have = (unsigned)__builtin_amdgcn_ballot_w64(t == rtag);
              } while (!((have >> gq) & 1));
              asm volatile("" ::: "memory");
            }
          };
          ad = lbase + l * 16 + rt * 2048;
          asm volatile("" : "+v"(ad));
          need(rt);
          K_AT(ad); V_AT(ad); SB(); SC(0, 0, cm0); SC(0, 1, cn[0]); SB();
          PHASE(0, p0, SC(1, 0, cmi), SC(1, 1, cm1), , , , , )
          if (!KVJ) landed(false);
          need(rt + 1);
          K_AT(ad + 2048);
#pragma unroll 1
          for (int gq = rt + 1; gq < 8; ++gq) {
            ad += 2048;
            asm volatile("" : "+v"(ad));
            if (KVJ) piece();
            else { landed(gq >= rt + KVD); if (told) { piece(); if (PC2) piece(); } }
            need(gq + 1);
            A_NEXT(ad, cn[0], cn[0], cn[1], cn[1], K_AT(ad + 2048));
          }
          A_LAST();
        }
        if constexpr (it == 0) { for (; nw < 8;) piece(); }
      }
      STAMPAT(25);
      {
        float big = 0.f;
#pragma unroll
        for (int a = 0; a < 2; ++a)
#pragma unroll
          for (int d = 0; d < 2; ++d)
            big = __builtin_elementwise_maximum(__builtin_elementwise_maximum(big, __builtin_elementwise_maximum(fabsf(oa[a][d][0]), fabsf(oa[a][d][1]))),
                                                __builtin_elementwise_maximum(fabsf(oa[a][d][2]), fabsf(oa[a][d][3])));
        bool ok = sum[0][0] >= 0.5f && sum[0][0] < 0x1p64f && sum[1][0] >= 0.5f && sum[1][0] < 0x1p64f && big < INFINITY;
        if (!__all(ok)) {
#pragma unroll
          for (int a = 0; a < 2; ++a) {
            const int qs = 2 * rt + a, qq = 256 + 16 * qs + ql, klo = max(qq - 255, p > 0 ? 0 : 256);
            auto score = [&](int t) __attribute__((always_inline)) {
              f32x4 s = mfma16(t == 31 ? k1 : *(const u32x4*)(kb_ + t * 1024), qf[a], z4);
#pragma unroll
              for (int e = 0; e < 4; ++e) { const int kk = 16 * t + 4 * g + e; s[e] = (kk <= qq && kk >= klo) ? s[e] : -INFINITY; }
              return s;
            };
            float mx = -INFINITY;
#pragma unroll 1
            for (int t = p > 0 ? qs : 16; t <= qs + 16; ++t) { const f32x4 s = score(t); mx = fmaxf(fmaxf(mx, fmaxf(s[0], s[1])), fmaxf(s[2], s[3])); }
            mx = max_x32(max_x16(mx));
            oa[a][0] = z4; oa[a][1] = z4; sum[a] = z4;
#pragma unroll 1
            for (int G = p > 0 ? qs >> 1 : 8; G <= (qs + 16) >> 1; ++G) {
              const f32x4 s0 = score(2 * G), s1 = score(2 * G + 1);
              f32x4 e0, e1;
#pragma unroll
              for (int e = 0; e < 4; ++e) { e0[e] = __builtin_amdgcn_exp2f(s0[e] - mx); e1[e] = __builtin_amdgcn_exp2f(s1[e] - mx); }
              const u32x4 pf = pack8(e0, e1);
              oa[a][0] = mfma16(*(const u32x4*)(vb_ + G * 2048), pf, oa[a][0]);
              oa[a][1] = mfma16(*(const u32x4*)(vb_ + G * 2048 + 1024), pf, oa[a][1]);
              sum[a] = mfma16(ones, pf, sum[a]);
            }
          }
        }
      }
#pragma unroll
      for (int a = 0; a < 2; ++a) {
        const float inv = __builtin_amdgcn_rcpf(sum[a][0]);
        oa[a][0] *= inv; oa[a][1] *= inv;
      }
      const unsigned oo = OFF_O + it * (4u << 20) + ((p * 8 + rt) * 64 + h * 2) * 1024;
      st16(pack8(oa[0][0], oa[0][1]), rs, l * 16, oo);
      st16(pack8(oa[1][0], oa[1][1]), rs, l * 16 + 1024, oo);
      STAMPAT(4);
      if constexpr (it == 0) {
        wait_vm<2>();
        asm volatile("" : "+v"(fxn)::"memory");
        if (rt >= 6 && !__all(fxn[0] == T[0] && fxn[1] == T[1] && fxn[2] == T[0] && fxn[3] == T[1])) poll_flags2(rs, XN_OFF(), T);
        DEF4(gv); DEF4(uv);
        if (ffn_on) {
          const unsigned ro = 65536 + w * 1024 + (4 * g + ((l & 15) >> 2)) * 32 + (l & 3) * 8;
#pragma unroll
          for (int i = 0; i < 4; ++i) {
            frag_t f;
            f.hf.a = trread(smem, ro + i * 16384); f.hf.b = trread(smem, ro + i * 16384 + 512);
            gv[i] = f.v;
            f.hf.a = trread(smem, ro + i * 16384 + 8192); f.hf.b = trread(smem, ro + i * 16384 + 8704);
            uv[i] = f.v;
          }
        }
        PINV4(gv); PINV4(uv);
        if (!OFE) { WO_PIECE(11); WO_PIECE(10); WO_PIECE(9); WO_PIECE(8); WO_PIECE(7); WO_PIECE(6); WO_PIECE(5); WO_PIECE(4); }
      }
      BAR();
      if constexpr (it == 1) { WO_PIECE(3); WO_PIECE(2); WO_PIECE(1); WO_PIECE(0); }
      wait_vm<it ? 4 : OFE ? 0 : 8>();
      asm volatile("s_barrier" ::: "memory");
      if (tid == 0) st8(T, rs, h * SO, F_O + (it * 8 + p) * 32 * SO);
      if constexpr (it == 0) {
        if (OFE) { WO_PIECE(11); WO_PIECE(10); WO_PIECE(9); WO_PIECE(8); WO_PIECE(7); WO_PIECE(6); WO_PIECE(5); WO_PIECE(4); }
        WO_PIECE(3); WO_PIECE(2); WO_PIECE(1); WO_PIECE(0);
      }
    }
    STAMPAT(5);
    IDS();
    ARGP(pwr, 4); ARGP(pwd, 7); ARGP(pout, 8); ARGP(px, 0);
    const int kh = w >> 2, rt2 = rb * 4 + (w & 3);
    const unsigned R = rt2 * 32 + kh * 16 + (l & 15);
    const unsigned to = (rt2 * 64 + ct * 4 + kh) * 1024 + l * 16;
    const unsigned rmo = ct * 64 + ((g & 1) ? 12 + 4 * g : 4 * g);
    f32x2 wv[8];
    {
      u32x4 wa[2], wrp[2], res[2];
#pragma unroll
      for (int j = 0; j < 2; ++j) {
        wrp[j] = *(const GAS u32x4*)(pwr + rmo + 32 * j);
        if constexpr (it == 0) wa[j] = *(const GAS u32x4*)(pwr + l * 8 + j * 512);
        if constexpr (it == 0) res[j] = *(const GAS u32x4*)(px + R * 1024 + rmo + 32 * j);
        else res[j] = ld16(rs, OFF_XNEW + (2 * ct + j) * 256 + g * 16, 0);
      }
      SB();
      unsigned bt[4];
#pragma unroll
      for (int n = 0; n < 4; ++n) {
        bt[n] = lbase + 32768 + 65536 + kh * 4096 + (4 * g + ((l & 15) >> 2)) * 128 + (l & 3) * 8 + ((n ^ (2 * (g & 1) + ((l & 15) >> 3))) << 5);
        asm volatile("" : "+v"(bt[n]));
      }
      if (w == 0) poll_flags(rs, (l & 31) * SO, F_O + (it * 8 + (rb >> 1)) * 32 * SO, T);
      asm volatile("s_barrier" ::: "memory");
      STAMPAT(6);
      auto prep = [&]() __attribute__((always_inline)) {
        if constexpr (it == 0) {
          f32x2 t[8];
          unpack8v(wa[0], t); unpack8v(wa[1], t + 4);
          const f32x2 s = ((t[0] + t[1]) + (t[2] + t[3])) + ((t[4] + t[5]) + (t[6] + t[7]));
          wsum = sum64(hadd(s));
        }
        unpack8v(trade16(wrp[0]), wv); unpack8v(trade16(wrp[1]), wv + 4);
        if constexpr (it == 0) { res[0] = trade16(res[0]); res[1] = trade16(res[1]); }
        else { res[0] = R == sel0 ? res[0] : x2p[0]; res[1] = R == sel0 ? res[1] : x2p[1]; }
        unpack8v(res[0], v + 0); unpack8v(res[1], v + 4);
        asm volatile("" : "+v"(v[0]), "+v"(v[1]), "+v"(v[2]), "+v"(v[3]), "+v"(v[4]), "+v"(v[5]), "+v"(v[6]), "+v"(v[7]));
        asm volatile("" : "+v"(wsum), "+v"(wv[0]), "+v"(wv[1]), "+v"(wv[2]), "+v"(wv[3]), "+v"(wv[4]), "+v"(wv[5]), "+v"(wv[6]), "+v"(wv[7]));
      };
      if constexpr (it == 1 && WON) {
        const unsigned abase = OFF_O + (4u << 20) + (rt2 * 64 + kh) * 1024;
        u32x4 af[W2B], bfr[W2L + 1][4];
        unsigned b2[2] = {lbase + 32768 + l * 16, lbase + 98304 + l * 16};
        asm volatile("" : "+v"(b2[0]), "+v"(b2[1]));
#define O_LOAD(s_) af[(s_) % W2B] = ld16(rs, l * 16 + (~(s_) & 1) * 2048, abase + ((31 - (s_)) >> 1) * 4096)
#define WO_RD(s_, n) bfr[(s_) % (W2L + 1)][n] = *(const LDS_AS u32x4*)(size_t)(b2[(31 - (s_)) >> 4] + ((31 - (s_)) & 15) * 4096 + (n) * 1024)
#pragma unroll
        for (int s = 0; s < W2R; ++s) O_LOAD(s);
        SB();
        prep();
        SB();
        f32x4 ac[4];
#pragma unroll
        for (int n = 0; n < 4; ++n) ac[n] = f32x4{v[2 * n][0], v[2 * n][1], v[2 * n + 1][0], v[2 * n + 1][1]};
#pragma unroll
        for (int n = 0; n < 4 * W2L; ++n) WO_RD(n >> 2, n & 3);
#pragma unroll
        for (int s = 0; s < 32; ++s) {
          if (s == W2P && w >= 4) __builtin_amdgcn_s_setprio(1);
          if (s == 24 - W2L) asm volatile("s_barrier" ::: "memory");
          if (s < W2B - W2R) O_LOAD(W2R + 2 * s);
          SB();
#pragma unroll
          for (int n = 0; n < 4; ++n) {
            ac[n] = mfma16(bfr[s % (W2L + 1)][n], af[s % W2B], ac[n]);
            if (s + W2L < 32) WO_RD(s + W2L, n);
            SB();
          }
          if (s < W2B - W2R) O_LOAD(W2R + 2 * s + 1);
          else if (s + W2B < 32) O_LOAD(s + W2B);
          SB();
        }
        __builtin_amdgcn_s_setprio(0);
#undef WO_RD
#undef O_LOAD
        STAMPAT(7);
#pragma unroll
        for (int n = 0; n < 4; ++n) { v[2 * n] = f32x2{ac[n][0], ac[n][1]}; v[2 * n + 1] = f32x2{ac[n][2], ac[n][3]}; }
      } else {
      const unsigned abase = OFF_O + it * (4u << 20) + (rt2 * 64 + 32 * kh) * 1024;
      const unsigned av0 = l * 16 + kh * 1024, av1 = l * 16 + (kh ^ 1) * 1024;
      u32x4 af[WB][2];
#define O_LOAD(s_) { af[(s_) % WB][0] = ld16(rs, av0 + (~(s_) & 1) * 2048, abase + ((15 - (s_)) >> 1) * 4096); af[(s_) % WB][1] = ld16(rs, av1 + (~(s_) & 1) * 2048, abase + ((15 - (s_)) >> 1) * 4096); }
#pragma unroll
      for (int s = 0; s < WR; ++s) O_LOAD(s);
      SB();
      prep();
      SB();
      f32x4 ac[2][4];
#pragma unroll
      for (int a = 0; a < 2; ++a)
#pragma unroll
        for (int n = 0; n < 4; ++n) ac[a][n] = f32x4{0.f, 0.f, 0.f, 0.f};
      u32x4 bfr[WLA + 1][4];
#define WO_RD(buf, s, n)                                                                             \
  {                                                                                                  \
    const unsigned o_ = bt[n] + ((15 - (s)) & 7) * 8192;     \
    frag_t f_;                                                                                       \
    f_.hf.a = trabs(o_); f_.hf.b = trabs(o_ + 2048);                                                 \
    bfr[buf][n] = f_.v;                                                                              \
  }
#pragma unroll
      for (int n = 0; n < 4 * WLA; ++n) WO_RD(n >> 2, n >> 2, n & 3);
#pragma unroll
      for (int s = 0; s < 16; ++s) {
        const int cur = s % (WLA + 1);
        if (WPR && s == WPR && w >= 4) __builtin_amdgcn_s_setprio(1);
        asm volatile("" : "+{v[176:179]}"(ac[0][0]), "+{v[180:183]}"(ac[0][1]), "+{v[184:187]}"(ac[0][2]), "+{v[188:191]}"(ac[0][3]),
                          "+{v[192:195]}"(ac[1][0]), "+{v[196:199]}"(ac[1][1]), "+{v[200:203]}"(ac[1][2]), "+{v[204:207]}"(ac[1][3]));
        if (s == (it == 0 ? 4 : 12) - WLA) asm volatile("s_barrier" ::: "memory");
        if (s == 8 - WLA) {
#pragma unroll
          for (int n = 0; n < 4; ++n) { bt[n] -= 65536; asm volatile("" : "+v"(bt[n])); }
        }
        if (it == 0 && s == WIS) {
#pragma unroll
          for (int i = 0; i < 3; ++i) {
            if (PO()) st16(keep[3 + i], rs, l * 16 + i * 1024, OFF_WI + (h * 8 + ((ROT0 + 6) & 7)) * STAGE + w * 3072);
            else st16(keep[i], rs, l * 16 + i * 1024, OFF_WI + (h * 8 + ((ROT0 + 5) & 7)) * STAGE + w * 3072);
          }
        }
        if (it == 0 && s == WPS) {
          unsigned pz1 = POISON;
          asm volatile("" : "+v"(pz1));
          const u32x4 pz = {pz1, pz1, pz1, pz1};
          st16(pz, rs, OFF_PART + (tid >> 8) * (1u << 20) + bid * 4096 + (tid & 255) * 16, 0);
          if (w < 2) st16(pz, rs, OFF_STAT + 524288 + (rb * 16 + ct) * 2048 + tid * 16, 0);
          if (w == 4 && l < 40) st16(pz, rs, l < 16 ? OFF_CA + 65536 + rb * 4096 + ct * 256 + l * 16 : OFF_FX + (h * 8 + p) * 512 + (l - 16) * 16, 0);
          if (w == 6 && l < (ct < (int)RKEY ? 3 : 2)) st4(pz1, rs, l < 2 ? OFF_XNEW + (bid >> 3) * 256 + (bid & 7) * 8 + l * 4 : KEY_AT(1, ct) + rb * SKEY, 0);
        }
        if (s < WB - WR) O_LOAD(WR + 2 * s < 16 ? WR + 2 * s : 0);
        SB();
#pragma unroll
        for (int n = 0; n < 4; ++n) {
          ac[0][n] = mfma16(bfr[cur][n], af[s % WB][0], ac[0][n]);
          ac[1][n] = mfma16(bfr[cur][n], af[s % WB][1], ac[1][n]);
          if (s + WLA < 16) WO_RD((s + WLA) % (WLA + 1), s + WLA, n);
          SB();
        }
        if (s < WB - WR) { O_LOAD(WR + 2 * s + 1); }
        else if (s + WB < 16) { O_LOAD(s + WB); }
        SB();
      }
      if (WPR) __builtin_amdgcn_s_setprio(0);
#undef WO_RD
#undef O_LOAD
      STAMPAT(7);
      if constexpr (it == 0 && WON) {
        const int ks = 2 * rb + kh;
        const unsigned o_ = lbase + 32768 + (ks & 15) * 8192 + (ks >> 4) * 4096 + (4 * g + ((l & 15) >> 2)) * 128 + (l & 3) * 8 + (((w & 3) ^ (2 * (g & 1) + ((l & 15) >> 3))) << 5);
        frag_t f_;
        f_.hf.a = trabs(o_); f_.hf.b = trabs(o_ + 2048);
        wof = f_.v;
        asm volatile("" : "+v"(wof));
      }
#define XCH_AT(v) ((v) == 7 ? (rb == 7 ? 139264 : 155648) : (v) * 4096)
#pragma unroll
      for (int n = 0; n < 4; ++n) *(f32x4*)(smem + XCH_AT(w) + n * 1024 + l * 16) = ac[1][n];
#pragma unroll
      for (int n = 0; n < 4; ++n) { v[2 * n] += f32x2{ac[0][n][0], ac[0][n][1]}; v[2 * n + 1] += f32x2{ac[0][n][2], ac[0][n][3]}; }
      BAR();
#pragma unroll
      for (int n = 0; n < 4; ++n) {
        const f32x4 o = *(const f32x4*)(smem + XCH_AT(w ^ 4) + n * 1024 + l * 16);
        v[2 * n] += f32x2{o[0], o[1]}; v[2 * n + 1] += f32x2{o[2], o[3]};
      }
      }
    }
    unsigned crow = 0;
    {
      if (it == 0) { st16(pack8v(v), rs, to, OFF_XN + (4u << 20)); st16(pack8v(v + 4), rs, to + 2048, OFF_XN + (4u << 20)); }
      SB();
      const f32x2 s = ((v[0] + v[1]) + (v[2] + v[3])) + ((v[4] + v[5]) + (v[6] + v[7]));
      f32x2 dp = v[0] * wv[0], q = v[0] * v[0];
#pragma unroll
      for (int j = 1; j < 8; ++j) { dp = __builtin_elementwise_fma(v[j], wv[j], dp); q = __builtin_elementwise_fma(v[j], v[j], q); }
      float mt = hadd(s), qq = hadd(q), dot = hadd(dp);
      add_g3(mt, qq, dot);
      const unsigned so = OFF_STAT + it * 524288 + rb * 32768, lrow = R & 127;
      if (it == 0) wait_vm<2>();
      if (g == 0) st16(unpoison4(__builtin_bit_cast(u32x4, f32x4{mt, qq, dot, 0.f})), rs, ct * 2048 + lrow * 16, so);
      SB();
      if (it == 0) wait_vm<1>();
      if constexpr (it == 0) {
        volatile LDS_AS u32x2* in = (volatile LDS_AS u32x2*)(smem + L_XC);
        if (l == 0) in[w] = T;
        const u32x2 t = in[l & 7];
        if (__all(t[0] == T[0] && t[1] == T[1]) && l < (int)RX1) st8(T, rs, (l * 32 + (rb & 1) * 16 + ct) * SX1, F_X1 + (rb >> 1) * RX1 * 32 * SX1);
      }
      STAMPAT(8);
      if (it == 0) {
        if (ffn_on) { WD_DMA(); }
#pragma unroll
        for (int j = 0; j < 9; ++j) *(u32x4*)(smem + L_RING + (j / 3) * STAGE + (w * 3 + j % 3) * 1024 + l * 16) = keep[j];
      }
      STAMPAT(16);
      if constexpr (it == 0) {
        if (w == 0) poll_flags(rs, ((l & 2) * 8 + ((ROT0 + 5) & 7) * 2 + (l & 1)) * SX1, F_X1 + (p * RX1 + (xcd >> 1) % RX1) * 32 * SX1, T);
        if (tid == 192) *(LDS_AS u32x4*)(smem + L_QC) = u32x4{0u, 0u, 0u, 0u};
        BAR();
        PRE_LOAD(1, ROT0 + 5, 0, NPRE);
        STAMPAT(9);
      } else {
        __builtin_amdgcn_s_sleep(SDIR);
        u32x4 sp[4];
#pragma unroll
        for (int i = 0; i < 4; ++i) sp[i] = ld16(rs, (4 * g + i) * 2048 + lrow * 16, so);
        const RowFin r = fin_key(rs, T, g * 8192 + lrow * 16, so, smem + L_XC, l, w, R, wsum, sp);
        STAMPAT(9);
        BAR();
        crow = fin_cand(rs, smem + L_XC, 1, g, rb, ct, r, v);
        STAMPAT(10);
        fin_x2(r, v);
      }
    }
    STAMPAT(11);
    if (it == 1) {
      u32x4 op[2];
      {
        char* tp = smem + L_OUT + w * 2048;
        const unsigned c0 = (g & 1) * 2 + (g >> 1), r = l & 15;
        *(u32x4*)(tp + r * 128 + ((c0 ^ (r & 7)) << 4)) = trade16(pack8v(v));
        *(u32x4*)(tp + r * 128 + (((c0 + 4) ^ (r & 7)) << 4)) = trade16(pack8v(v + 4));
#pragma unroll
        for (int j = 0; j < 2; ++j) op[j] = *(const u32x4*)(tp + j * 1024 + (l >> 3) * 128 + (((l ^ (l >> 3)) & 7) << 4));
      }
      const unsigned Ro = R - (l & 15) + (l >> 3), oo = (Ro * 1024 + ct * 64) * 2 + (l & 7) * 16;
#define OUT_STORE(c0, c1)                                                                           \
  {                                                                                                 \
    if (c0) __builtin_amdgcn_raw_buffer_store_b128(op[0], rout, oo, 0, OAUX);                        \
    if (c1) __builtin_amdgcn_raw_buffer_store_b128(op[1], rout, oo + 16384, 0, OAUX);                \
  }
      if (OUTE) OUT_STORE(Ro != crow, Ro + 8 != crow);
      if (w == 0) {
        __builtin_amdgcn_s_sleep(GSL);
        u32x4 xr[4];
        unsigned cur = 0, best;
        asm volatile("" : "=v"(xr[0]), "=v"(xr[1]));
        for (unsigned spins = 0;;) {
          const unsigned k_ = key_load(rs, l, 1, KREP());
          const bool all = __all(k_ != POISON);
          best = __builtin_amdgcn_readfirstlane(umax16(k_ == POISON ? 0u : k_));
          if (best != cur && ffn_on) {
            cur = best;
            ld16h<0>(xr[0], rs, (l >> 3) * 256 + (l & 7) * 16, cand_off(cur, 1)); ld16h<2048>(xr[1], rs, (l >> 3) * 256 + (l & 7) * 16, cand_off(cur, 1));
          }
          if (all || spin_fail(spins, rs, T)) break;
        }
        STAMPAT(17);
        if (l == 0) *(LDS_AS unsigned*)(smem + L_SKEY + 32) = best;
        asm volatile("s_waitcnt vmcnt(0)" : "+v"(xr[0]), "+v"(xr[1])::"memory");
        if (ffn_on) {
          word_wait<2>(rs, T, (l >> 3) * 256 + (l & 7) * 16, cand_off(best, 1), xr);
          *(LDS_AS u32x4*)(smem + L_ROW + l * 16) = xr[0]; *(LDS_AS u32x4*)(smem + L_ROW + 1024 + l * 16) = xr[1];
        }
      }
      BAR();
      const unsigned key = __builtin_amdgcn_readfirstlane(*(LDS_AS unsigned*)(smem + L_SKEY + 32)), krow = key_row(key);
      STAMPAT(12);
      if (ffn_on) {
        u32x4 xv[4];
#pragma unroll
        for (int i = 0; i < 4; ++i) xv[i] = *(const LDS_AS u32x4*)(smem + L_ROW + w * 256 + i * 64 + g * 16);
        f32x4 ga, ua;
        ffn_gu(gv, uv, xv, ga, ua);
        ffn_gu_store(smem, l, w, ga, ua);
        BAR();
        ffn_d_store(rs, l, w, 1, slice, ffn_d_all(dfr, ffn_d_h(ffn_d_read(smem, l))));
        STAMPAT(13);
        SB();
        if (OSL) __builtin_amdgcn_s_sleep(OSL);
      }
      if (OUTE) OUT_STORE(Ro == crow && Ro != krow, Ro + 8 == crow && Ro + 8 != krow)
      else OUT_STORE(Ro != krow, Ro + 8 != krow);
      if (!ffn_on) {
        const unsigned G = xcd * 4 + bi - 28;
        if (G == 0 && tid == 0) st8(T, rs, F_EP, 0);
        u32x4 pv[4];
        u32x2 cxn, cx1;
        __builtin_amdgcn_s_sleep(RSL);
        red_load(rs, tid, 1, G, key, pv, cxn, cx1);
        red_sum(rs, smem, T, tid, w, 1, G, key, pv, cxn, cx1);
        STAMPAT(14);
        BAR();
        if (w == 0) {
          const u32x2 ov = red_final(smem, l, key, cxn, cx1);
          if (l < 8) __builtin_amdgcn_raw_buffer_store_b64(ov, rout, (krow * 1024 + G * 32 + l * 4) * 2, 0, 0);
        }
      }
#undef OUT_STORE
    }
    STAMPAT(15); STAMPAT(23);
  };
  layer(std::integral_constant<int, 0>{});
  layer(std::integral_constant<int, 1>{});
}

extern "C" void kernel_launch(void* const* d_in, const int* in_sizes, int n_in, void* d_out, int out_size,
                              void* d_ws, size_t ws_size, hipStream_t stream) {
  (void)hipFuncSetAttribute((const void*)mega_kernel, hipFuncAttributeMaxDynamicSharedMemorySize, LDS_BYTES);
  mega_kernel<<<256, 512, LDS_BYTES, stream>>>((const bf16*)d_in[0], (char*)d_ws, (const bf16*)d_in[1], (const bf16*)d_in[2], (const bf16*)d_in[4],
                                               (const bf16*)d_in[5], (const bf16*)d_in[6], (const bf16*)d_in[7], (bf16*)d_out);
}
```

```cpp
#include <hip/hip_runtime.h>
#include <stdint.h>
#include <utility>

typedef __bf16 bf16;
typedef __bf16 bf16x8 __attribute__((ext_vector_type(8)));
typedef __bf16 bf16x2 __attribute__((ext_vector_type(2)));
typedef float f32x4 __attribute__((ext_vector_type(4)));
typedef float f32x2 __attribute__((ext_vector_type(2)));
typedef short s16x4 __attribute__((ext_vector_type(4)));
typedef unsigned int u32x4 __attribute__((ext_vector_type(4)));
typedef unsigned int u32x2 __attribute__((ext_vector_type(2)));
#define LDS_AS __attribute__((address_space(3)))
#define GAS __attribute__((address_space(1)))
typedef GAS unsigned gu32;
typedef GAS unsigned long long gu64;
#define RLX_AGENT __ATOMIC_RELAXED, __HIP_MEMORY_SCOPE_AGENT
typedef __amdgpu_buffer_rsrc_t rsrc_t;

constexpr int F_ = 3584;
constexpr int SC1 = 16;

constexpr int RX = 4;
constexpr unsigned F_EP = 0, F_TMO = 128;
constexpr unsigned F_XN = 4096;
#define FLG 256, 1024, 8, 64, 4, 256, 4
constexpr unsigned FLV[7] = {FLG};
constexpr unsigned SO = FLV[0], SKV = FLV[1], SKW = FLV[2], SX1 = FLV[3], RX1 = FLV[4], SKEY = FLV[5], RKEY = FLV[6];
constexpr unsigned F_O = 8u << 20;
constexpr unsigned F_KV = 11u << 20;
constexpr unsigned F_WOI = F_O + 262144;
constexpr unsigned F_X1 = 9u << 20;
constexpr unsigned OFF_KEY = 10u << 20;
#define KREP() (RKEY == 16 ? bid & 15 : (bid & 7) * RKEY / 8)
#define KEY_AT(L, rep) (OFF_KEY + ((L) * RKEY + (rep)) * (SKEY < 8 ? 128 : 16 * SKEY))
constexpr unsigned OFF_AB = 2u << 20;
constexpr unsigned OFF_XNEW = 262144;
constexpr unsigned OFF_CS = OFF_AB + 65536;
constexpr unsigned OFF_FX = 393216;
constexpr unsigned OFF_CA = 524288;
constexpr unsigned OFF_STAT = 1u << 20;
constexpr unsigned OFF_PART = 3u << 20;
constexpr unsigned OFF_XN = 8u << 20;
constexpr unsigned OFF_O = 16u << 20;
constexpr unsigned OFF_KV = 32u << 20;
constexpr unsigned OFF_WI = 52u << 20;
constexpr unsigned OFF_WOI = 58u << 20;
constexpr unsigned WS_BYTES = 60u << 20;
constexpr unsigned POISON = 0xFFFFFFFFu;

constexpr int L_ABW = 16640, L_CSP = 17408, L_CSL = 31872, L_FXL = 32256, L_K = 0, L_V = 32768, L_RING = 65536, L_XCH = 0, L_DN = 32768, L_RDY = 31744, L_QC = 31808, LDS_BYTES = 163840;
constexpr int STAGE = 24576;
#define SLOT(t) ((t) & 3)

template <typename F, int... I> __device__ __forceinline__ void static_for_impl(F&& f, std::integer_sequence<int, I...>) {
  (f(std::integral_constant<int, I>{}), ...);
}
template <int N, typename F> __device__ __forceinline__ void static_for(F&& f) { static_for_impl(f, std::make_integer_sequence<int, N>{}); }
__device__ __forceinline__ unsigned pack2(float a, float b) {
  f32x2 v = {a, b};
  return __builtin_bit_cast(unsigned, __builtin_convertvector(v, bf16x2));
}
__device__ __forceinline__ float asf(unsigned u) { return __builtin_bit_cast(float, u); }
__device__ __forceinline__ float lo16(unsigned u) { return __builtin_bit_cast(float, u << 16); }
__device__ __forceinline__ float hi16(unsigned u) { return __builtin_bit_cast(float, u & 0xffff0000u); }
__device__ __forceinline__ u32x4 pack8(const f32x4 a, const f32x4 b) {
  u32x4 o;
  o[0] = pack2(a[0], a[1]); o[1] = pack2(a[2], a[3]); o[2] = pack2(b[0], b[1]); o[3] = pack2(b[2], b[3]);
  return o;
}
__device__ __forceinline__ f32x4 mfma16(u32x4 a, u32x4 b, f32x4 c) {
  return __builtin_amdgcn_mfma_f32_16x16x32_bf16(__builtin_bit_cast(bf16x8, a), __builtin_bit_cast(bf16x8, b), c, 0, 0, 0);
}
__device__ __forceinline__ f32x4 mfma16k16(s16x4 a, s16x4 b, f32x4 c) { return __builtin_amdgcn_mfma_f32_16x16x16bf16_1k(a, b, c, 0, 0, 0); }
template <int CTRL> __device__ __forceinline__ float dppf(float v) {
  return __builtin_bit_cast(float, __builtin_amdgcn_update_dpp(0, __builtin_bit_cast(int, v), CTRL, 0xf, 0xf, false));
}
template <int CTRL> __device__ __forceinline__ unsigned dppu(unsigned v) {
  return (unsigned)__builtin_amdgcn_update_dpp(0, (int)v, CTRL, 0xf, 0xf, false);
}
__device__ __forceinline__ float add_x16(float v) {
  auto r = __builtin_amdgcn_permlane16_swap(__builtin_bit_cast(unsigned, v), __builtin_bit_cast(unsigned, v), false, false);
  return __builtin_bit_cast(float, (unsigned)r[0]) + __builtin_bit_cast(float, (unsigned)r[1]);
}
__device__ __forceinline__ float add_x32(float v) {
  auto r = __builtin_amdgcn_permlane32_swap(__builtin_bit_cast(unsigned, v), __builtin_bit_cast(unsigned, v), false, false);
  return __builtin_bit_cast(float, (unsigned)r[0]) + __builtin_bit_cast(float, (unsigned)r[1]);
}
__device__ __forceinline__ float max_x16(float v) {
  auto r = __builtin_amdgcn_permlane16_swap(__builtin_bit_cast(unsigned, v), __builtin_bit_cast(unsigned, v), false, false);
  return fmaxf(__builtin_bit_cast(float, (unsigned)r[0]), __builtin_bit_cast(float, (unsigned)r[1]));
}
__device__ __forceinline__ float max_x32(float v) {
  auto r = __builtin_amdgcn_permlane32_swap(__builtin_bit_cast(unsigned, v), __builtin_bit_cast(unsigned, v), false, false);
  return fmaxf(__builtin_bit_cast(float, (unsigned)r[0]), __builtin_bit_cast(float, (unsigned)r[1]));
}
__device__ __forceinline__ unsigned umax_x16(unsigned v) {
  auto r = __builtin_amdgcn_permlane16_swap(v, v, false, false);
  return max((unsigned)r[0], (unsigned)r[1]);
}
__device__ __forceinline__ unsigned umax_x32(unsigned v) {
  auto r = __builtin_amdgcn_permlane32_swap(v, v, false, false);
  return max((unsigned)r[0], (unsigned)r[1]);
}
__device__ __forceinline__ float sum_hi8(float v) {
  v += dppf<0x128>(v);
  v = add_x16(v);
  return add_x32(v);
}
__device__ __forceinline__ float sum64(float v) {
  v += dppf<0xB1>(v);
  v += dppf<0x4E>(v);
  v += dppf<0x124>(v);
  v += dppf<0x128>(v);
  v = add_x16(v);
  return add_x32(v);
}

#define BAR() asm volatile("s_waitcnt lgkmcnt(0)\n\ts_barrier" ::: "memory")
#define DRAIN() asm volatile("s_waitcnt vmcnt(0)" ::: "memory")
#define SB() __builtin_amdgcn_sched_barrier(0)
template <int N> __device__ __forceinline__ void wait_vm() { asm volatile("s_waitcnt vmcnt(%0)" ::"n"(N) : "memory"); }

__device__ __forceinline__ void dma16(rsrc_t rs, unsigned voff, unsigned soff, unsigned lds) {
  asm volatile("s_mov_b32 m0, %0\n\ts_nop 0\n\tbuffer_load_dwordx4 %1, %2, %3 offen lds" ::"s"(lds), "v"(voff), "s"(rs), "s"(soff) : "memory");
}
#define NTM 2
#define OAUX 16
template <int NT> __device__ __forceinline__ void dma16n(rsrc_t rs, unsigned voff, unsigned soff, unsigned lds) {
  if constexpr (NT) asm volatile("s_mov_b32 m0, %0\n\ts_nop 0\n\tbuffer_load_dwordx4 %1, %2, %3 offen nt lds" ::"s"(lds), "v"(voff), "s"(rs), "s"(soff) : "memory");
  else dma16(rs, voff, soff, lds);
}
template <int OFF> __device__ __forceinline__ void dma16s(rsrc_t rs, unsigned voff, unsigned soff, unsigned lds) {
  asm volatile("s_mov_b32 m0, %0\n\ts_nop 0\n\tbuffer_load_dwordx4 %1, %2, %3 offen offset:%4 sc1 lds" ::"s"(lds), "v"(voff), "s"(rs), "s"(soff), "n"(OFF) : "memory");
}
__device__ __forceinline__ float rbf1(float x) { return __builtin_bit_cast(float, pack2(0.f, x)); }
__device__ __forceinline__ void vswap(unsigned& a, unsigned& b) { asm volatile("v_swap_b32 %0, %1" : "+v"(a), "+v"(b)); }
__device__ __forceinline__ void vswap(float& a, float& b) { asm volatile("v_swap_b32 %0, %1" : "+v"(a), "+v"(b)); }
template <typename V, int N> __device__ __forceinline__ void vswapn(V& a, V& b) {
#pragma unroll
  for (int i = 0; i < N; ++i) { auto x = a[i], y = b[i]; vswap(x, y); a[i] = x; b[i] = y; }
}
__device__ __forceinline__ u32x4 ld16(rsrc_t rs, unsigned voff, unsigned soff) { return __builtin_amdgcn_raw_buffer_load_b128(rs, voff, soff, SC1); }
template <int OFF> __device__ __forceinline__ void ld16h(u32x4& d, rsrc_t rs, unsigned voff, unsigned soff) {
  asm volatile("buffer_load_dwordx4 %0, %1, %2, %3 offen offset:%4 sc1" : "=v"(d) : "v"(voff), "s"(rs), "s"(soff), "n"(OFF) : "memory");
}
template <int I, int OFF> __device__ __forceinline__ void ld16w(u32x4& d, rsrc_t rs, unsigned voff, unsigned soff) {
  if constexpr (I == 0) asm volatile("buffer_load_dwordx4 %0, %1, %2, %3 offen offset:%4 sc1" : "={v[128:131]}"(d) : "v"(voff), "s"(rs), "s"(soff), "n"(OFF) : "memory");
  else if constexpr (I == 1) asm volatile("buffer_load_dwordx4 %0, %1, %2, %3 offen offset:%4 sc1" : "={v[132:135]}"(d) : "v"(voff), "s"(rs), "s"(soff), "n"(OFF) : "memory");
  else if constexpr (I == 2) asm volatile("buffer_load_dwordx4 %0, %1, %2, %3 offen offset:%4 sc1" : "={v[136:139]}"(d) : "v"(voff), "s"(rs), "s"(soff), "n"(OFF) : "memory");
  else if constexpr (I == 3) asm volatile("buffer_load_dwordx4 %0, %1, %2, %3 offen offset:%4 sc1" : "={v[140:143]}"(d) : "v"(voff), "s"(rs), "s"(soff), "n"(OFF) : "memory");
  else if constexpr (I == 4) asm volatile("buffer_load_dwordx4 %0, %1, %2, %3 offen offset:%4 sc1" : "={v[144:147]}"(d) : "v"(voff), "s"(rs), "s"(soff), "n"(OFF) : "memory");
  else if constexpr (I == 5) asm volatile("buffer_load_dwordx4 %0, %1, %2, %3 offen offset:%4 sc1" : "={v[148:151]}"(d) : "v"(voff), "s"(rs), "s"(soff), "n"(OFF) : "memory");
  else if constexpr (I == 6) asm volatile("buffer_load_dwordx4 %0, %1, %2, %3 offen offset:%4 sc1" : "={v[152:155]}"(d) : "v"(voff), "s"(rs), "s"(soff), "n"(OFF) : "memory");
  else if constexpr (I == 7) asm volatile("buffer_load_dwordx4 %0, %1, %2, %3 offen offset:%4 sc1" : "={v[156:159]}"(d) : "v"(voff), "s"(rs), "s"(soff), "n"(OFF) : "memory");
  else if constexpr (I == 8) asm volatile("buffer_load_dwordx4 %0, %1, %2, %3 offen offset:%4 sc1" : "={v[160:163]}"(d) : "v"(voff), "s"(rs), "s"(soff), "n"(OFF) : "memory");
  else if constexpr (I == 9) asm volatile("buffer_load_dwordx4 %0, %1, %2, %3 offen offset:%4 sc1" : "={v[164:167]}"(d) : "v"(voff), "s"(rs), "s"(soff), "n"(OFF) : "memory");
  else if constexpr (I == 10) asm volatile("buffer_load_dwordx4 %0, %1, %2, %3 offen offset:%4 sc1" : "={v[168:171]}"(d) : "v"(voff), "s"(rs), "s"(soff), "n"(OFF) : "memory");
  else if constexpr (I == 11) asm volatile("buffer_load_dwordx4 %0, %1, %2, %3 offen offset:%4 sc1" : "={v[172:175]}"(d) : "v"(voff), "s"(rs), "s"(soff), "n"(OFF) : "memory");
}
template <int K> __device__ __forceinline__ void pin_w(u32x4& a, u32x4& b) {
  if constexpr (K == 0) asm volatile("" : "+{v[128:131]}"(a), "+{v[132:135]}"(b)::"memory");
  else if constexpr (K == 1) asm volatile("" : "+{v[136:139]}"(a), "+{v[140:143]}"(b)::"memory");
  else if constexpr (K == 2) asm volatile("" : "+{v[144:147]}"(a), "+{v[148:151]}"(b)::"memory");
  else if constexpr (K == 3) asm volatile("" : "+{v[152:155]}"(a), "+{v[156:159]}"(b)::"memory");
  else if constexpr (K == 4) asm volatile("" : "+{v[160:163]}"(a), "+{v[164:167]}"(b)::"memory");
  else if constexpr (K == 5) asm volatile("" : "+{v[168:171]}"(a), "+{v[172:175]}"(b)::"memory");
}
__device__ __forceinline__ void ld8h(u32x2& d, rsrc_t rs, unsigned voff, unsigned soff) {
  asm volatile("buffer_load_dwordx2 %0, %1, %2, %3 offen sc1" : "=v"(d) : "v"(voff), "s"(rs), "s"(soff) : "memory");
}
template <int OFF> __device__ __forceinline__ void ld4h(unsigned& d, rsrc_t rs, unsigned voff, unsigned soff) {
  asm volatile("buffer_load_dword %0, %1, %2, %3 offen offset:%4 sc1" : "=v"(d) : "v"(voff), "s"(rs), "s"(soff), "n"(OFF) : "memory");
}
__device__ __forceinline__ u32x2 ld8(rsrc_t rs, unsigned voff, unsigned soff) { return __builtin_amdgcn_raw_buffer_load_b64(rs, voff, soff, SC1); }
__device__ __forceinline__ void st16(u32x4 v, rsrc_t rs, unsigned voff, unsigned soff) { __builtin_amdgcn_raw_buffer_store_b128(v, rs, voff + soff, 0, SC1); }
__device__ __forceinline__ void st8(u32x2 v, rsrc_t rs, unsigned voff, unsigned soff) { __builtin_amdgcn_raw_buffer_store_b64(v, rs, voff, soff, SC1); }

__device__ __forceinline__ unsigned ld4(rsrc_t rs, unsigned voff, unsigned soff) { return __builtin_amdgcn_raw_buffer_load_b32(rs, voff, soff, SC1); }
__device__ __forceinline__ void st4(unsigned v, rsrc_t rs, unsigned voff, unsigned soff) { __builtin_amdgcn_raw_buffer_store_b32(v, rs, voff, soff, SC1); }

constexpr unsigned kSpinCap = 1u << 16;
__device__ __forceinline__ bool spin_fail(unsigned& spins, rsrc_t rs, u32x2 T) {
  asm volatile("" ::: "memory");
  ++spins;
  if (spins > kSpinCap) { st8(T, rs, 0, F_TMO); return true; }
  if ((spins & 255u) == 0u) {
    const u32x2 v = ld8(rs, 0, F_TMO);
    if (v[0] == T[0] && v[1] == T[1]) return true;
  }
  __builtin_amdgcn_s_sleep(3);
  return false;
}
__device__ __forceinline__ void poll_flags2(rsrc_t rs, unsigned voff, u32x2 T) {
  for (unsigned spins = 0;;) {
    const u32x4 v = ld16(rs, voff, 0);
    if (__all(v[0] == T[0] && v[1] == T[1] && v[2] == T[0] && v[3] == T[1])) break;
    if (spin_fail(spins, rs, T)) break;
  }
  asm volatile("" ::: "memory");
  __builtin_amdgcn_fence(__ATOMIC_ACQUIRE, "wavefront");
}
__device__ __forceinline__ void tag_wait(rsrc_t rs, u32x2 T, unsigned voff, unsigned soff, u32x4& v) {
  if (__builtin_expect(!__all(v[2] == T[0] && v[3] == T[1]), 0)) {
    unsigned spins = 0;
    do {
      if (spin_fail(spins, rs, T)) break;
      v = ld16(rs, voff, soff);
    } while (!__all(v[2] == T[0] && v[3] == T[1]));
    asm volatile("s_waitcnt vmcnt(0)" : "+v"(v)::"memory");
  }
}
template <int N> __device__ __forceinline__ void word_wait(rsrc_t rs, u32x2 T, unsigned voff, unsigned soff, u32x4 (&v)[4]) {
#define WW_OK() __all(max(max(max(v[0][0], v[0][1]), max(v[0][2], v[0][3])), max(max(v[N - 1][0], v[N - 1][1]), max(v[N - 1][2], v[N - 1][3]))) != POISON)
  if (__builtin_expect(!WW_OK(), 0)) {
    unsigned spins = 0;
    do {
      if (spin_fail(spins, rs, T)) break;
#pragma unroll
      for (int i = 0; i < N; ++i) v[i] = ld16(rs, voff + i * 2048, soff);
    } while (!WW_OK());
    asm volatile("s_waitcnt vmcnt(0)" : "+v"(v[0]), "+v"(v[N - 1])::"memory");
  }
#undef WW_OK
}
__device__ __forceinline__ void poll_flags(rsrc_t rs, unsigned voff, unsigned soff, u32x2 T) {
  for (unsigned spins = 0;;) {
    const u32x2 v = ld8(rs, voff, soff);
    if (__all(v[0] == T[0] && v[1] == T[1])) break;
    if (spin_fail(spins, rs, T)) break;
  }
  asm volatile("" ::: "memory");
  __builtin_amdgcn_fence(__ATOMIC_ACQUIRE, "wavefront");
}

__device__ __forceinline__ unsigned toff(unsigned R, unsigned ks, unsigned g) {
  return (((R >> 5) * 64 + ks * 2 + ((R >> 4) & 1)) << 10) + ((g * 16 + (R & 15)) << 4);
}
__device__ __forceinline__ void unpack8v(const u32x4 u, f32x2* v) {
#pragma unroll
  for (int j = 0; j < 4; ++j) v[j] = f32x2{lo16(u[j]), hi16(u[j])};
}
__device__ __forceinline__ u32x4 pack8v(const f32x2* v) {
  u32x4 o;
#pragma unroll
  for (int j = 0; j < 4; ++j) o[j] = pack2(v[j][0], v[j][1]);
  return o;
}
__device__ __forceinline__ f32x2 rbf1v(f32x2 v) { return f32x2{rbf1(v[0]), rbf1(v[1])}; }
__device__ __forceinline__ float ln_rd(float var) { return __builtin_amdgcn_rcpf(rbf1(__builtin_amdgcn_sqrtf(rbf1(rbf1(var) + 1e-5f)))); }
template <int N> __device__ __forceinline__ void ln_apply(float mb, float rd, const f32x2* v, f32x2* y) {
  const f32x2 mb2 = {mb, mb}, rd2 = {rd, rd};
#pragma unroll
  for (int j = 0; j < N; ++j) y[j] = rbf1v(rbf1v(v[j] - mb2) * rd2);
}
__device__ __forceinline__ float hadd(f32x2 v) {
  float a = v[0];
  asm volatile("" : "+v"(a));
  return a + v[1];
}
__device__ __forceinline__ float add_g(float v) { return add_x32(add_x16(v)); }
__device__ __forceinline__ void add_g3(float& x, float& y, float& z) {
  auto a = __builtin_amdgcn_permlane16_swap(__builtin_bit_cast(unsigned, x), __builtin_bit_cast(unsigned, y), false, false);
  auto b = __builtin_amdgcn_permlane16_swap(__builtin_bit_cast(unsigned, z), __builtin_bit_cast(unsigned, z), false, false);
  const float r = asf(a[0]) + asf(a[1]), t = asf(b[0]) + asf(b[1]);
  auto c = __builtin_amdgcn_permlane32_swap(__builtin_bit_cast(unsigned, r), __builtin_bit_cast(unsigned, r), false, false);
  auto d = __builtin_amdgcn_permlane32_swap(__builtin_bit_cast(unsigned, t), __builtin_bit_cast(unsigned, t), false, false);
  const float u = asf(c[0]) + asf(c[1]);
  z = asf(d[0]) + asf(d[1]);
  auto e = __builtin_amdgcn_permlane16_swap(__builtin_bit_cast(unsigned, u), __builtin_bit_cast(unsigned, u), false, false);
  x = asf(e[0]); y = asf(e[1]);
}
__device__ __forceinline__ u32x4 trade16(u32x4 v) {
  auto a = __builtin_amdgcn_permlane16_swap(v[0], v[2], false, false);
  auto b = __builtin_amdgcn_permlane16_swap(v[1], v[3], false, false);
  return u32x4{(unsigned)a[0], (unsigned)b[0], (unsigned)a[1], (unsigned)b[1]};
}
__device__ __forceinline__ unsigned umax4(u32x4 v) { return max(max(v[0], v[1]), max(v[2], v[3])); }
__device__ __forceinline__ u32x2 unpoison2(u32x2 v) { return u32x2{min(v[0], POISON - 1), min(v[1], POISON - 1)}; }
__device__ __forceinline__ u32x4 unpoison4(u32x4 v) {
  return u32x4{min(v[0], POISON - 1), min(v[1], POISON - 1), min(v[2], POISON - 1), min(v[3], POISON - 1)};
}

typedef union { struct { s16x4 a, b; } hf; u32x4 v; } frag_t;
__device__ __forceinline__ s16x4 trread(const char* smem, unsigned off) {
  return __builtin_amdgcn_ds_read_tr16_b64_v4i16((LDS_AS s16x4*)(smem + off));
}
__device__ __forceinline__ s16x4 trabs(unsigned addr) { return __builtin_amdgcn_ds_read_tr16_b64_v4i16((LDS_AS s16x4*)(size_t)addr); }

constexpr int WA = 6;
#define WOP 4, 3, 6, 8, 1, 4, 0
constexpr int WOV[7] = {WOP};
constexpr int WLA = WOV[4], WPR = WOV[5], QPR = WOV[6];
constexpr int WB = WOV[0], WR = WOV[1], WIS = WOV[2], WPS = WOV[3];
#define CH 0, 7, 9, 10, 16, 19, 24, 26, 30, 31, 5, 2
constexpr int CHV[12] = {CH};
constexpr int FKL = CHV[10], FSN = CHV[11];
constexpr int SPL = CHV[0];
constexpr int FSL = CHV[1], FS0 = CHV[2], FS0D = CHV[3], FS1 = CHV[4], FS1S = CHV[5];
constexpr int FXA = CHV[6], FXB = CHV[7], FXL = CHV[8], FXS = CHV[9];
#define ROTM 7
#define ROT0 (p & ROTM)
#define PO() (p & 7 & ~ROTM)
#define L0S 22, 25, 8, 0, 24, 28, 1, 2, 4, 14
constexpr int L0V[10] = {L0S};
constexpr int CSL = L0V[0], CSS = L0V[1], CSD = L0V[2];
constexpr int abl(int it) { return it ? 25 : L0V[4]; }
constexpr int abs_(int it) { return it ? 27 : L0V[5]; }
constexpr int W2S = L0V[6], WSH = L0V[7];
static_assert(ROTM == 7 || (CSD & 3) == 3, "two tiles must be there");
#define HLPW 0
#define OFE 1
#define LNP 0
constexpr int PRE0 = 12 + (LNP ? 3 : 0);
constexpr int LNL = L0V[3], LNS = L0V[8], POI = L0V[9];
#define SYNCB 3
#define SPINS 0
#define KVJ 1
#define KVD 2
#define PC2 1
#define PCE 1
#define TCH 40, 6, 0, 26, 0xFF, 0, 1
constexpr int TCV[7] = {TCH};
constexpr int TS0 = TCV[0], TSN = TCV[1], TM0 = TCV[2], TEX = TCV[3], TM1 = TCV[4], TSD = TCV[5], TSG = TCV[6];
constexpr int QL = 26;
#define SLP 18, 4, 28, 3, 0, 0
constexpr int SLV[6] = {SLP};
constexpr int OUTE = SLV[4], OSL = SLV[5];
constexpr int SDIR = SLV[0], GSL = SLV[1], RSL = SLV[2], KSL = SLV[3];
constexpr int tp0(int it) { return it ? 3 : 3 + WSH; }
constexpr bool tile_piece(int it, int s) { return s >= tp0(it) && ((s - tp0(it)) >> 2) + 3 < 8 && ((s - tp0(it)) & 3) < 3; }
#define NPRE 3
#define QAD 4
#define WO2 1, 5, 4, 2, 8
constexpr int W2V[5] = {WO2};
constexpr int WOS = 12;
static_assert(WOS + QAD < 18, "covered by the operand waits in front of the barrier of step 18");
constexpr int WON = W2V[0], W2B = W2V[1], W2R = W2V[2], W2L = W2V[3], W2P = W2V[4];
constexpr int a_req(int s, int n) {
  int r = 2 * NPRE;
  for (int u = 1; u < 192; u += 2) {
    const bool go = r < 64 && (r >> 1) <= u / 6 + QAD && (r < 8 || u >= 9);
    if (u == 6 * s + n) return go ? r : -1;
    r += go;
  }
  return -1;
}
constexpr int vm_n(int it, int s, int n) {
  int c = it ? (a_req(s, n) >= 0) + (s == 0 && n == 0) : (s + 5 < 32 && (n == 2 || n == 3));
  if (n == 0) c += tile_piece(it, s);
  if (n == 4) c += (s == abl(it));
  if (!it) c += (s >= W2S && s < W2S + 3 && n == 4) + (LNP == 0 && s == LNL && n == 5 ? 3 : 0) + (s == 0 && n < 2 ? 2 : 0) + (s == LNS + 2 && n == 5) + (s == TEX && n == 5);
  else c += (s == 2 && n == 2) + (s == (SPL < 0 ? 0 : SPL) && n == (SPL < 0 ? 1 : 4) ? 4 : 0) + (s >= 27 && s < 30 && (n == 0 || n == 3)) + (WON && s == WOS && n == 4);
  return c;
}
constexpr int vm_at(int it, int s, int n) {
  int c = it ? 2 * NPRE : PRE0;
  for (int u = 0; u < 6 * s + n; ++u) c += vm_n(it, u / 6, u % 6);
  return c;
}
constexpr int vm_a(int it, int k) {
  if (k < NPRE) return 2 * k + 2;
  for (int u = 0; u < 192; ++u)
    if (a_req(u / 6, u % 6) == 2 * k + 1) return vm_at(it, (u + 1) / 6, (u + 1) % 6);
  return 0;
}
constexpr int vm_tile(int it, int t) { return t >= 3 ? vm_at(it, 4 * t - 10 + tp0(it), 1) : t == 1 ? 12 + (LNP >= 2 ? 3 : 0) : vm_at(0, W2S + 2, 5); }
constexpr int vm_unit(int u) { return u < 3 ? 5 + 2 * u + (LNP == 3 || (LNP == 2 && u == 2) ? 3 : 0) : u < 5 ? vm_at(0, 0, u - 2) : vm_at(0, u - 5, 4); }
constexpr int unit_slot(int u) { return u % 3 == 0 ? 0 : 16384 + (u % 3) * 16384; }

constexpr int L_XC = LDS_BYTES - 64, L_SKEY = 32704, L_ROW = 1024, L_OUT = 65536, L_SRED = 4096, L_SFIN = 20480;
__device__ __forceinline__ unsigned key_load(rsrc_t rs, int l, int L, int rep) { return ld4(rs, (l & 15) * SKEY, KEY_AT(L, rep)); }
__device__ __forceinline__ unsigned umax16(unsigned k) {
  k = max(k, dppu<0xB1>(k));
  k = max(k, dppu<0x4E>(k));
  k = max(k, dppu<0x141>(k));
  return max(k, dppu<0x140>(k));
}
__device__ __forceinline__ unsigned key_row(unsigned key) { return (0xffffu - (key & 0xffffu)) & 2047u; }
__device__ __forceinline__ unsigned key_wait(rsrc_t rs, u32x2 T, int l, int L, int rep, unsigned kv) {
  if (!__all(kv != POISON)) {
    unsigned spins = 0;
    do {
      if (spin_fail(spins, rs, T)) break;
      if (L == 0) __builtin_amdgcn_s_sleep(KSL);
      kv = key_load(rs, l, L, rep);
    } while (!__all(kv != POISON));
  }
  return __builtin_amdgcn_readfirstlane(umax16(kv));
}
__device__ __forceinline__ unsigned cand_off(unsigned key, int L) { return OFF_CA + L * 65536 + (key_row(key) >> 7) * 4096; }
#define XO(i) (((i) >> 1) * 256 + ((i) & 1) * 64)
template <int S1, int S2> __device__ __forceinline__ void ffn_xwait(rsrc_t rs, u32x2 T, unsigned xo, int l, u32x4 (&xv)[4]) {
#define XV_OK() __all(max(max(umax4(xv[0]), umax4(xv[1])), max(umax4(xv[2]), umax4(xv[3]))) != POISON)
  if (!XV_OK()) {
    unsigned spins = 0;
    do {
      if (spin_fail(spins, rs, T)) break;
#pragma unroll
      for (int i = 0; i < 4; ++i) xv[i] = ld16(rs, (l >> 4) * 16 + (i & 1) * S1 + (i >> 1) * S2, xo);
    } while (!XV_OK());
  }
#undef XV_OK
}
__device__ __forceinline__ void ffn_gu(const u32x4 (&gv)[4], const u32x4 (&uv)[4], const u32x4 (&xv)[4], f32x4& ga, f32x4& ua) {
  ga = mfma16(gv[0], xv[0], f32x4{0.f, 0.f, 0.f, 0.f}); ua = mfma16(uv[0], xv[0], f32x4{0.f, 0.f, 0.f, 0.f});
#pragma unroll
  for (int i = 1; i < 4; ++i) { ga = mfma16(gv[i], xv[i], ga); ua = mfma16(uv[i], xv[i], ua); }
}
__device__ __forceinline__ void ffn_gu_check(rsrc_t rs, u32x2 T, unsigned xo, int l, const u32x4 (&gv)[4], const u32x4 (&uv)[4], u32x4 (&xv)[4], f32x4& ga, f32x4& ua) {
  if (__builtin_expect(__any(ga[0] != ga[0]), 0)) {
    ffn_xwait<64, 256>(rs, T, xo, l, xv);
    ffn_gu(gv, uv, xv, ga, ua);
  }
}
__device__ __forceinline__ void ffn_gu_store(char* smem, int l, int w, f32x4 ga, f32x4 ua) {
  LDS_AS f32x4* sred = (LDS_AS f32x4*)(smem + L_SRED);
  if ((l & 15) == 0) { sred[w * 8 + (l >> 4)] = ga; sred[w * 8 + 4 + (l >> 4)] = ua; }
}
__device__ __forceinline__ f32x4 ffn_d_read(char* smem, int l) {
  const LDS_AS float* t = (const LDS_AS float*)(smem + L_SRED) + (l & 8) * 16 + (l & 4) * 4 + (l >> 4) * 4 + (l & 3);
  return f32x4{t[0], t[32], t[64], t[96]};
}
__device__ __forceinline__ u32x2 ffn_d_h(f32x4 t) {
  float x = t[0] + t[1];
  asm volatile("" : "+v"(x));
  x += t[2] + t[3];
  x += dppf<0x128>(x);
  const float y = dppf<0x124>(x);
  const bool up = __builtin_amdgcn_inverse_ballot_w64(0xF0F0F0F0F0F0F0F0ull);
  const float gg = rbf1(up ? y : x), uu = rbf1(up ? x : y);
  const float h = rbf1(gg * __builtin_amdgcn_rcpf(1.0f + __expf(-gg))) * uu;
  const unsigned p = pack2(h, dppf<0xB1>(h));
  return u32x2{dppu<0x00>(p), dppu<0xAA>(p)};
}
template <int t> __device__ __forceinline__ f32x4 ffn_d_mma(s16x4 d, u32x2 hh, f32x4 y) {
  const bool me = __builtin_amdgcn_inverse_ballot_w64(0x0001000100010001ull << t);
  return mfma16k16(d, __builtin_bit_cast(s16x4, u32x2{me ? hh[0] : 0u, me ? hh[1] : 0u}), y);
}
__device__ __forceinline__ f32x4 ffn_d_all(const s16x4 (&dfr)[8], u32x2 hh) {
  f32x4 a = {0.f, 0.f, 0.f, 0.f}, b = a;
  a = ffn_d_mma<0>(dfr[0], hh, a); b = ffn_d_mma<1>(dfr[1], hh, b); a = ffn_d_mma<2>(dfr[2], hh, a); b = ffn_d_mma<3>(dfr[3], hh, b);
  a = ffn_d_mma<4>(dfr[4], hh, a); b = ffn_d_mma<5>(dfr[5], hh, b); a = ffn_d_mma<6>(dfr[6], hh, a); b = ffn_d_mma<7>(dfr[7], hh, b);
  return a + b;
}
__device__ __forceinline__ void ffn_d_store(rsrc_t rs, int l, int w, int L, int slice, f32x4 y) {
  const unsigned c = l & 15;
  if (c < 8) st16(unpoison4(__builtin_bit_cast(u32x4, y)), rs, (2 * w + (c >> 2)) * 65536 + slice * 256 + (c & 3) * 64 + (l >> 4) * 16, OFF_PART + L * (1u << 20));
}
#define RED_VO(k) ((min(tid + 512 * (k), 1791) >> 3) * 256 + (tid & 7) * 16)
#define RED_SO(L, G) (OFF_PART + (L) * (1u << 20) + ((G) >> 1) * 65536 + ((G) & 1) * 128)
#define RED_JJ() ((tid & 3) * 16 + (tid & 4) * 2)
__device__ __forceinline__ void red_load(rsrc_t rs, int tid, int L, unsigned G, unsigned key, u32x4 (&pv)[4], u32x2& cxn, u32x2& cx1) {
#pragma unroll
  for (int k = 0; k < 4; ++k) pv[k] = ld16(rs, RED_VO(k), RED_SO(L, G));
  cxn = ld8(rs, RED_JJ(), cand_off(key, L) + XO(G));
  cx1 = ld8(rs, RED_JJ() + 128, cand_off(key, L) + XO(G));
}
__device__ __forceinline__ void red_sum(rsrc_t rs, char* smem, u32x2 T, int tid, int w, int L, unsigned G, unsigned key,
                                        u32x4 (&pv)[4], u32x2& cxn, u32x2& cx1) {
#define PV_OK() __all(max(max(max(umax4(pv[0]), umax4(pv[1])), max(umax4(pv[2]), umax4(pv[3]))), max(max(cxn[0], cxn[1]), max(cx1[0], cx1[1]))) != POISON)
  if (!PV_OK()) {
    unsigned spins = 0;
    do {
      if (spin_fail(spins, rs, T)) break;
#pragma unroll
      for (int k = 0; k < 4; ++k)
        if (umax4(pv[k]) == POISON) pv[k] = ld16(rs, RED_VO(k), RED_SO(L, G));
      if (max(max(cxn[0], cxn[1]), max(cx1[0], cx1[1])) == POISON) { cxn = ld8(rs, RED_JJ(), cand_off(key, L) + XO(G)); cx1 = ld8(rs, RED_JJ() + 128, cand_off(key, L) + XO(G)); }
    } while (!PV_OK());
  }
#undef PV_OK
  f32x4 s = (__builtin_bit_cast(f32x4, pv[0]) + __builtin_bit_cast(f32x4, pv[1])) + __builtin_bit_cast(f32x4, pv[2]);
  if (w < 4) s += __builtin_bit_cast(f32x4, pv[3]);
  ((LDS_AS f32x4*)(smem + L_SFIN))[tid] = s;
}
__device__ __forceinline__ u32x2 red_final(char* smem, int l, unsigned key, u32x2 cxn, u32x2 cx1) {
  const LDS_AS f32x4* sf = (const LDS_AS f32x4*)(smem + L_SFIN) + l;
  f32x4 s = ((sf[0] + sf[64]) + (sf[128] + sf[192])) + ((sf[256] + sf[320]) + (sf[384] + sf[448]));
#pragma unroll
  for (int j = 0; j < 4; ++j) s[j] = sum_hi8(s[j]);
  const unsigned k16 = key >> 16;
  const float rmax = __builtin_bit_cast(float, ((k16 & 0x8000u) ? (k16 & 0x7fffu) : (~k16 & 0xffffu)) << 16);
  const float sig = rbf1(__builtin_amdgcn_rcpf(1.0f + __expf(-rmax)));
  const float xn4[4] = {lo16(cxn[0]), hi16(cxn[0]), lo16(cxn[1]), hi16(cxn[1])};
  const float x14[4] = {lo16(cx1[0]), hi16(cx1[0]), lo16(cx1[1]), hi16(cx1[1])};
  float o[4];
#pragma unroll
  for (int j = 0; j < 4; ++j) o[j] = x14[j] + rbf1(xn4[j] + rbf1(rbf1(s[j]) * sig));
  return u32x2{pack2(o[0], o[1]), pack2(o[2], o[3])};
}

struct RowFin { float mean, var, mb, rd; unsigned key; };
__device__ __forceinline__ void fin_wait(rsrc_t rs, u32x2 T, unsigned voff, unsigned so, u32x4 (&sp)[4]) {
#define ST_OK() __all(max(max(umax4(sp[0]), umax4(sp[1])), max(umax4(sp[2]), umax4(sp[3]))) != POISON)
  if (!ST_OK()) {
    unsigned spins = 0;
    do {
      if (spin_fail(spins, rs, T)) break;
#pragma unroll
      for (int i = 0; i < 4; ++i) sp[i] = ld16(rs, voff + i * 2048, so);
    } while (!ST_OK());
  }
#undef ST_OK
}
__device__ __forceinline__ RowFin fin_key(rsrc_t rs, u32x2 T, unsigned voff, unsigned so, char* skey, int l, int w, unsigned R, float wsum, u32x4 (&sp)[4]) {
  RowFin r;
#define ST_SUM() ((__builtin_bit_cast(f32x4, sp[0]) + __builtin_bit_cast(f32x4, sp[1])) + (__builtin_bit_cast(f32x4, sp[2]) + __builtin_bit_cast(f32x4, sp[3])))
  f32x4 sf = ST_SUM();
  const float z = (sf[0] + sf[1]) + sf[2];
  if (__any(z != z)) {
    fin_wait(rs, T, voff, so, sp);
    sf = ST_SUM();
  }
#undef ST_SUM
  float s0 = sf[0], s1 = sf[1], s2 = sf[2];
  add_g3(s0, s1, s2);
  r.mean = s0 * (1.0f / 1024.0f);
  r.var = fmaxf(__builtin_fmaf(-r.mean, r.mean, s1 * (1.0f / 1024.0f)), 0.f);
  r.mb = rbf1(r.mean); r.rd = ln_rd(r.var);
  const unsigned rb16 = pack2((s2 - r.mb * wsum) * r.rd, 0.f) & 0xffffu;
  r.key = min((((rb16 & 0x8000u) ? (~rb16 & 0xffffu) : (rb16 | 0x8000u)) << 16) | (0xffffu - R), POISON - 1);
  const unsigned wk = umax16(r.key);
  if (l == 0) ((LDS_AS unsigned*)skey)[w] = wk;
  return r;
}
__device__ __forceinline__ unsigned fin_cand(rsrc_t rs, char* skey, int L, int g, int rb, int ct, const RowFin& r, const f32x2 (&v)[8]) {
  const u32x4 k0 = *(const LDS_AS u32x4*)skey, k1 = *(const LDS_AS u32x4*)(skey + 16);
  const unsigned best = max(umax4(k0), umax4(k1));
  if (r.key == best) {
    const unsigned co = L * 65536 + rb * 4096 + ct * 256 + g * 16;
    f32x2 t[8], rd2 = {r.rd, r.rd}, of2 = {-r.mb * r.rd, -r.mb * r.rd};
    asm volatile("" : "+v"(rd2), "+v"(of2));
#pragma unroll
    for (int j = 0; j < 8; ++j) t[j] = __builtin_elementwise_fma(v[j], rd2, of2);
    st16(unpoison4(pack8v(t)), rs, co, OFF_CA); st16(unpoison4(pack8v(t + 4)), rs, co + 64, OFF_CA);
    if (g == 0 && ct < (int)RKEY) st4(best, rs, KEY_AT(L, ct) + rb * SKEY, 0);
    st16(unpoison4(pack8v(v)), rs, co + 128, OFF_CA); st16(unpoison4(pack8v(v + 4)), rs, co + 192, OFF_CA);
  }
  return key_row(best);
}
__device__ __forceinline__ void fin_x2(const RowFin& r, f32x2 (&v)[8]) {
  f32x2 sc2 = {1.0f + r.rd, 1.0f + r.rd}, of2 = {-r.mb * r.rd, -r.mb * r.rd};
  asm volatile("" : "+v"(sc2), "+v"(of2));
#pragma unroll
  for (int j = 0; j < 8; ++j) v[j] = __builtin_elementwise_fma(v[j], sc2, of2);
}

extern "C" __device__ unsigned long long dispatch_id() __asm("llvm.amdgcn.dispatch.id");

extern "C" __global__ __launch_bounds__(512, 2) void mega_kernel(const bf16* x, char* ws, const bf16* wqkv, const bf16* wo, const bf16* wr,
                                                                 const bf16* wg, const bf16* wu, const bf16* wd, bf16* out) {
  extern __shared__ __attribute__((aligned(16))) char smem[];
  const int w0_ = __builtin_amdgcn_readfirstlane(threadIdx.x >> 6);
  const unsigned lbase = (unsigned)(size_t)(LDS_AS char*)smem;
  {
    unsigned long long pc;
    unsigned len;
    asm volatile("s_getpc_b64 %0\n\ts_mov_b32 %1, .Ltext_end-mega_kernel\n\t.subsection 1\n.Ltext_end:\n\t.subsection 0" : "=s"(pc), "=s"(len));
    const unsigned o = ((blockIdx.x >> 3) * 64 + (threadIdx.x & 63)) * 128;
    if (w0_ == 7 && o < len)
      asm volatile("s_mov_b32 m0, %0\n\ts_nop 0\n\tglobal_load_lds_dword %1, %2" ::"s"(lbase + L_FXL), "v"(o), "s"(pc & ~127ull) : "memory");
  }
  asm volatile("" : "+s"(x), "+s"(ws), "+s"(wqkv));
#define IDS()                                                                                         \
  int tid = threadIdx.x, w = w0_, bid = blockIdx.x;                                                   \
  asm volatile("" : "+v"(tid));                                                                       \
  asm volatile("" : "+s"(w));                                                                         \
  asm volatile("" : "+s"(bid));                                                                       \
  const int l = tid & 63, xcd = bid & 7, bi = bid >> 3;                                               \
  const int p = (xcd & 1) * 4 + (bi & 3), h = (xcd >> 1) * 8 + (bi >> 2);         \
  const int rb = 2 * xcd + (bi & 1), ct = bi >> 1;                               \
  const int slice = xcd * 28 + bi;                                                   \
  const bool ffn_on = bi < 28;                                                                        \
  const int g = l >> 4;                                                                               \
  const int rt = w < 4 ? w : 11 - w;          \
  KARGS();                                                                                            \
  (void)p; (void)h; (void)rb; (void)ct; (void)slice; (void)ffn_on; (void)g; (void)rt;
  const rsrc_t rs = __builtin_amdgcn_make_buffer_rsrc((void*)ws, 0, WS_BYTES, 0x00020000);
  typedef const void* const __attribute__((address_space(4)))* karg_t;
#define KARGS() karg_t ka = (karg_t)__builtin_amdgcn_kernarg_segment_ptr(); asm volatile("" : "+s"(ka))
#define ARGP(name, i) const GAS bf16* name = (const GAS bf16*)ka[i]; asm volatile("" : "+s"(name))
#define RSRC(ptr, bytes) __builtin_amdgcn_make_buffer_rsrc((void*)(ptr), 0, bytes, 0x00020000)
#define rwq RSRC(pwq, 1024 * 3072 * 2)
#define rwo RSRC(pwo, 1024 * 1024 * 2)
#define rwg RSRC(pwg, 1024 * F_ * 2)
#define rwu RSRC(pwu, 1024 * F_ * 2)
#define rwd RSRC(pwd, 1024 * F_ * 2)
#define rout RSRC(pout, 2048 * 1024 * 2)

#define WQ_VOFF() ((l >> 2) * 6144 + (((l & 3) ^ ((((l >> 4) ^ (l >> 5)) & 1) << 1)) << 4))
#define WQ_PIECE(t, j, dst) dma16(rwq, wqv, ((((t) + rot) & 7) * 128 + ((j) & 7) * 16) * 6144 + ((j) >> 3) * 2048 + h * 64, lbase + (dst) + (j) * 1024)
#define WQ_BOFF(dt) ((8 * g + ((l & 15) >> 2)) * 64 + (l & 3) * 8 + (((dt) ^ (g & 1)) << 5))
#define WQ_POFF() ((4 * g + ((l & 15) >> 2)) * 64 + (l & 3) * 8 + (((g ^ (g >> 1)) & 1) << 5))
#define X_UNIT1(u, j) dma16(rx, xv_, (unsigned)(p * 256 + rt * 32 + ((u) & 1) * 16 + (j) * 8) * 2048 + ((((u) >> 2) + rot) & 7) * 256 + (((u) >> 1) & 1) * 128, lbase + rt * 2048 + unit_slot(u) + (j) * 1024)
#define X_VOFF() ((l >> 3) * 2048 + (((l & 7) ^ ((l >> 3) & 6)) << 4))
#define X_ROFF(hs) (lbase + rt * 2048 + (l & 15) * 128 + (((4 * (hs) + g) ^ (l & 6)) << 4))
#define TOUCH_TO(ptr, dst) asm volatile("s_mov_b32 m0, %0\n\ts_nop 0\n\tglobal_load_lds_dword %1, off" ::"s"(lbase + (dst)), "v"(ptr) : "memory")
#define TOUCH(ptr) TOUCH_TO(ptr, L_FXL)
#define WO_PIECE_W(j, w_)                                                                                                                 \
  {                                                                                                                                       \
    if constexpr (it == 1 && WON) dma16s<0>(rs, l * 16, OFF_WOI + ct * 131072 + (j) * 8192 + (w_) * 1024, lbase + 32768 + (j) * 8192 + (w_) * 1024); \
    else dma16(rwo, wov, (((w_) >> 2) * 64 + ((w_) & 3) + 4 * (j)) * 16384 + ct * 128, lbase + 32768 + (j) * 8192 + (w_) * 1024);           \
  }
#define WO_PIECE(j) WO_PIECE_W(j, w)
#define WO_VOFF() ((l >> 3) * 2048 + (((((l & 7) >> 1) ^ ((l >> 4) & 3)) * 2 + (l & 1)) << 4))
#define WD_DMA() _Pragma("unroll") for (int i = 0; i < 4; ++i) WD_DMA1(i)
#define WD_DMA1(i)                                                                                   \
      dma16n<(NTM >> 1) & 1>(rwd, (l >> 4) * 2048 + (l & 15) * 16, (unsigned)(slice * 16 + 4 * (i)) * 2048 + w * 256, lbase + L_DN + w * 4096 + (i) * 1024)
#define FFN_DMA(m) dma16n<(NTM >> 2) & 1>((m) & 1 ? rwu : rwg, fnv, (unsigned)(((4 * w + ((m) >> 1)) * 32) * F_ + slice * 16) * 2, lbase + 65536 + (m) * 8192 + w * 1024)
  u32x4 gv[4], uv[4];
  s16x4 dfr[8];
  u32x4 keep[9];
  u32x4 pre[4][2];
#define PRE_LOAD(it_, rot_, s0, s1)                                                                  \
  static_for<(s1) - (s0)>([&](auto c_) __attribute__((always_inline)) {                              \
    constexpr int s = (s0) + decltype(c_)::value;                                                    \
    const unsigned ab_ = OFF_XN + (it_) * (4u << 20) + (((rot_) + (s >> 2)) & 7) * 8192 + ((s >> 1) & 1) * 4096; \
    ld16w<2 * s, (s & 1) * 2048>(pre[s][0], rs, l * 16, ab_ + (p * 8 + rt) * 65536);                         \
    ld16w<2 * s + 1, (s & 1) * 2048 + 1024>(pre[s][1], rs, l * 16, ab_ + (p * 8 + rt) * 65536);                 \
  })
  u32x2 T;
  u32x2 ep;
  u32x4 xl[2];
#define LN_LOAD()                                                                                                                                   \
  {                                                                                                                                                 \
    asm volatile("buffer_load_dwordx4 %0, %1, %2, %3 offen" : "=v"(xl[0]) : "v"(l * 16), "s"(rx), "s"((p * 256 + h * 8 + w) * 2048) : "memory");      \
    asm volatile("buffer_load_dwordx4 %0, %1, %2, %3 offen offset:1024" : "=v"(xl[1]) : "v"(l * 16), "s"(rx), "s"((p * 256 + h * 8 + w) * 2048) : "memory"); \
    asm volatile("buffer_load_dwordx2 %0, off, %1, 0 sc1" : "=v"(ep) : "s"(rs) : "memory");                                                         \
  }
  const GAS bf16* tsrc;
  {
    IDS();
    const int it = 0, rot = ROT0;
    (void)it;
    const bf16* pwq = wqkv;
    const rsrc_t rx = RSRC(x, 2048 * 1024 * 2);
    const unsigned wqv = WQ_VOFF(), xv_ = X_VOFF();
    if (tid == 0) *(LDS_AS u32x4*)(smem + L_QC) = u32x4{0u, 0u, 0u, 0u};
    if (w == HLPW && l < 24) TOUCH((const GAS bf16*)wqkv + (bid * 24 + l) * 512 + ((3 - (unsigned)((size_t)wqkv >> 7)) & 7) * 64);
    if (LNP == 3) LN_LOAD();
#pragma unroll
    for (int i = 0; i < 3; ++i) WQ_PIECE(0, 3 * w + i, L_RING);
    X_UNIT1(0, 0); X_UNIT1(0, 1); X_UNIT1(1, 0); X_UNIT1(1, 1);
    if (LNP == 2) LN_LOAD();
    BAR();
    X_UNIT1(2, 0); X_UNIT1(2, 1);
#pragma unroll
    for (int i = 0; i < 3; ++i) WQ_PIECE(1, 3 * w + i, L_RING + STAGE);
    if (LNP == 1) LN_LOAD();
  }
  unsigned sel0 = 0x7FFFFFFFu;
  f32x2 v[8];
  u32x4 x2p[2], wof;
  float wsum;

  auto layer = [&](auto it_c) __attribute__((always_inline)) {
    constexpr int it = decltype(it_c)::value;
    f32x4 acc[2][6];
    f32x2 qs2[2];
    u32x4 k0, k1, v0, v1;
    {
      IDS();
      bool aff = false;
      unsigned aq[2];
      u32x4 av[2];
#define PIN_ACC() asm volatile("" : "+{v[176:179]}"(acc[0][0]), "+{v[180:183]}"(acc[0][1]), "+{v[184:187]}"(acc[0][2]), "+{v[188:191]}"(acc[0][3]), "+{v[192:195]}"(acc[0][4]), "+{v[196:199]}"(acc[0][5]), \
                               "+{v[200:203]}"(acc[1][0]), "+{v[204:207]}"(acc[1][1]), "+{v[208:211]}"(acc[1][2]), "+{v[212:215]}"(acc[1][3]), "+{v[216:219]}"(acc[1][4]), "+{v[220:223]}"(acc[1][5]))
#define PINV4(x) asm volatile("" : "+v"(x[0]), "+v"(x[1]), "+v"(x[2]), "+v"(x[3])::"memory")
      auto qkv_loop = [&](auto it_c) __attribute__((always_inline)) {
        constexpr int IT = decltype(it_c)::value;
        const unsigned abase = OFF_XN + IT * (4u << 20) + (p * 8 + rt) * 65536;
        const int rot = ROT0 + 5 * IT;
        unsigned fkey = 0, kv, bq[2];
        u32x4 fx[4], bv[2], af[WA][2];
        u32x2 cxn, cx1, hh;
        f32x4 sa, sb, cs4[2];
        f32x2 b2[2];
#define AB_OFF() (OFF_AB + IT * 32768 + (p * 8 + rt) * 512)
#define A_LOAD1(k, a) ld16w<(k) % WA * 2 + (a), ((k) & 1) * 2048 + (a) * 1024>(af[(k) % WA][a], rs, l * 16, abase + ((((k) >> 2) + rot) & 7) * 8192 + (((k) >> 1) & 1) * 4096)
        if constexpr (IT == 1) {
#pragma unroll
          for (int s = 0; s < NPRE; ++s) { af[s][0] = pre[s][0]; af[s][1] = pre[s][1]; }
        }
        const bf16* pwq = wqkv;
        ARGP(pwo, 3);
        const rsrc_t rx = RSRC(x, 2048 * 1024 * 2);
        unsigned xv_ = X_VOFF(), xr[2] = {X_ROFF(0), X_ROFF(1)};
        if constexpr (IT == 0) asm volatile("" : "+v"(xv_), "+v"(xr[0]), "+v"(xr[1]));
#define X_READ(u) { af[((u) & ~1) % WA][(u) & 1] = *(const LDS_AS u32x4*)(size_t)(xr[0] + unit_slot(u)); af[((u) | 1) % WA][(u) & 1] = *(const LDS_AS u32x4*)(size_t)(xr[1] + unit_slot(u)); }
        const int tstep = TS0 + bid % TSN + TSD * (w >> TSG);
        const unsigned wqv = WQ_VOFF();
#define QKV_DMA1(t, i)                                                                            \
  {                                                                                               \
    if constexpr (IT == 0) WQ_PIECE(t, 3 * w + (i), L_RING + SLOT(t) * STAGE);                    \
    else dma16s<(i) * 1024>(rs, l * 16, OFF_WI + (h * 8 + (((t) + rot) & 7)) * STAGE + w * 3072, lbase + L_RING + SLOT(t) * STAGE + w * 3072); \
  }
        unsigned bo[2][2];
#pragma unroll
        for (int dt = 0; dt < 2 - IT; ++dt) {
          bo[dt][0] = lbase + L_RING + (IT ? l * 16 : WQ_BOFF(dt)); bo[dt][1] = bo[dt][0] + 2 * STAGE;
          asm volatile("" : "+v"(bo[dt][0]), "+v"(bo[dt][1]));
        }
        auto frag0 = [&](int sl, int f) __attribute__((always_inline)) {
          unsigned pb = lbase + L_RING + WQ_POFF();
          asm volatile("" : "+v"(pb));
          const unsigned o = (pb ^ ((f & 1) << 5)) + sl * STAGE + ((f % 6) >> 1) * 8192 + (f / 6) * 2048;
          frag_t t;
          t.hf.a = trabs(o); t.hf.b = trabs(o + 1024);
          return t.v;
        };
        u32x4 df[4];
        auto dot_mma = [&](const u32x4 (&xf)[4]) __attribute__((always_inline)) {
          sa = mfma16(df[0], xf[0], f32x4{0.f, 0.f, 0.f, 0.f}); sb = mfma16(df[1], xf[1], f32x4{0.f, 0.f, 0.f, 0.f});
          sa = mfma16(df[2], xf[2], sa); sb = mfma16(df[3], xf[3], sb);
        };
        auto dot_store = [&](unsigned dst) __attribute__((always_inline)) {
          if ((l & 15) == 0) st16(unpoison4(__builtin_bit_cast(u32x4, sa + sb)), rs, g * 16, dst + (h * 8 + p) * 512 + w * 64);
        };
        auto tile_sum = [&](u32x4 pt, int dst) __attribute__((always_inline)) {
          f32x4 c = __builtin_bit_cast(f32x4, pt);
#pragma unroll
          for (int e = 0; e < 4; ++e) { c[e] += dppf<0x124>(c[e]); c[e] += dppf<0x128>(c[e]); c[e] = add_x16(c[e]); }
          if ((l & 28) == 0) *(LDS_AS f32x4*)(smem + dst + (w * 2 + (l >> 5)) * 64 + (l & 3) * 16) = c;
        };
        frag_t bfr[6];
#define QKV_BO(s, n, x) (bo[((n) ^ (x)) & 1][SLOT((s) >> 2) >> 1] + (SLOT((s) >> 2) & 1) * STAGE + ((n) >> 1) * 8192 + ((s) & 3) * 2048 + (x) * 256)
#define QKV_RDA(s, n)                                                                             \
  {                                                                                               \
    if constexpr (IT == 0) bfr[n].hf.a = trabs(QKV_BO(s, n, 0));                                  \
    else bfr[n].v = *(const LDS_AS u32x4*)(size_t)(bo[0][SLOT((s) >> 2) >> 1] + (SLOT((s) >> 2) & 1) * STAGE + (((s) & 3) * 6 + (n)) * 1024); \
  }
#define QKV_RDB(s, n) { if constexpr (IT == 0) bfr[n].hf.b = trabs(QKV_BO(s, n, 1)); }
#define XP_OFF() (OFF_XNEW + ((rot + 5 + PO()) & 7) * 1024)
#define FP_VO() (w == 3 ? (l >> 2) * 256 + (l & 3) * 16 : ((l >> 2) & 7) * 512 + (l >> 5) * 64 + (l & 3) * 16)
#define FP_OFF(i) (w == 3 ? OFF_XNEW + (i) * 4096 : OFF_FX + h * 4096 + w * 128)
#define FX_OK(n) __all(((n) == 4 ? max(max(umax4(fx[0]), umax4(fx[1])), max(umax4(fx[2]), umax4(fx[3]))) : max(umax4(fx[0]), umax4(fx[1]))) != POISON)
#define SINCE(s0) (vm_at(IT, s, n) - vm_at(IT, s0, 5))
#define DEF4(x) asm volatile("" : "=v"(x[0]), "=v"(x[1]), "=v"(x[2]), "=v"(x[3]))
        LDS_AS unsigned* qcnt = (LDS_AS unsigned*)(smem + L_QC);
        unsigned qc;
        unsigned qadr = lbase + L_QC, qone = 1;
        asm volatile("" : "+v"(qadr), "+v"(qone));
#define count_in(i) asm volatile("s_mov_b64 exec, 1\n\tds_add_u32 %0, %1 offset:%2\n\ts_mov_b64 exec, -1" ::"v"(qadr), "v"(qone), "n"(4 * (i)) : "memory")
        auto count_wait = [&](int i, unsigned want) __attribute__((always_inline)) {
          if (__builtin_expect((unsigned)__builtin_amdgcn_readfirstlane(qc) < want, 0)) {
            while ((unsigned)__builtin_amdgcn_readfirstlane(__hip_atomic_load(qcnt + i, __ATOMIC_RELAXED, __HIP_MEMORY_SCOPE_WORKGROUP)) < want) { if (SPINS) __builtin_amdgcn_s_sleep(SPINS); }
          }
          asm volatile("" ::: "memory");
        };
        if constexpr (IT == 0) {
          wait_vm<PRE0 - vm_unit(1)>();
          X_READ(0); X_READ(1);
          asm volatile("s_barrier" ::: "memory");
        }
#pragma unroll
        for (int n = 0; n < 6; ++n) { QKV_RDA(0, n); if (n < 5) QKV_RDB(0, n); }
        f32x2 lv[4], xs, xq;
        float lmean;
        f32x4 sc;
#define X1_OFF() (F_X1 + ((p * RX1 + (xcd >> 1) % RX1) * 32 + (rt >> 2) * 16 + (l & 15)) * SX1)
        unsigned long long xok = 0;
        RowFin fin;
        auto x1_need = [&](unsigned long long m) __attribute__((always_inline)) {
          if (__builtin_expect((xok & m) != m, 0)) {
            for (unsigned spins = 0;;) {
              const u32x2 f = ld8(rs, X1_OFF(), 0);
              xok = __builtin_amdgcn_ballot_w64(f[0] == T[0] && f[1] == T[1]);
              if ((xok & m) == m) break;
              if (spin_fail(spins, rs, T)) { xok = ~0ull; break; }
              __builtin_amdgcn_s_sleep(KSL);
            }
            asm volatile("" ::: "memory");
          }
        };
        auto gap = [&](auto sc_, auto nc_) __attribute__((always_inline)) {
          constexpr int s = decltype(sc_)::value, n = decltype(nc_)::value, ar = a_req(s, n);
          if constexpr ((SYNCB >> IT) & 1) {
            constexpr int tc = (s + 2) / 4;
            if constexpr (tc >= 1 && tc < 8 && s == 4 * tc - 2 && n == 5) {
              if constexpr (!IT || tc >= 3) wait_vm<vm_at(IT, s, n) - vm_tile(IT, tc)>();
              if constexpr (tc == 7 && !IT) asm volatile("s_waitcnt lgkmcnt(4)" ::: "memory");
              asm volatile("s_barrier" ::: "memory");
            }
          } else {
            constexpr int ts = (s + 3) / 4, tc = (s + 2) / 4, t0 = 1;
            if constexpr (ts >= 3 && ts < 8 && s == 4 * ts - 3 && n == 2) { wait_vm<vm_at(IT, s, n) - vm_tile(IT, ts)>(); count_in(0); }
            if constexpr (!IT && tc >= t0 && tc < 3 && s == 4 * tc - 2 && n == 1) { wait_vm<vm_at(IT, s, n) - vm_tile(IT, tc)>(); count_in(0); }
            if constexpr (IT && ts >= t0 && ts < 3 && s == 4 * ts - 3 && n == 2) count_in(0);
            if constexpr (tc >= t0 && tc < 8 && s == 4 * tc - 2) {
              if constexpr (n == 3) qc = __hip_atomic_load(qcnt, __ATOMIC_RELAXED, __HIP_MEMORY_SCOPE_WORKGROUP);
              if constexpr (n == 5) count_wait(0, 8 * (tc + 1 - IT));
            }
          }
          if constexpr (IT == 0 && n == 0 && s + 2 < 32) { wait_vm<vm_at(0, s, 0) - vm_unit(s + 2)>(); X_READ(s + 2); SB(); }
          if constexpr (n == 0 && tile_piece(IT, s)) QKV_DMA1(((s - tp0(IT)) >> 2) + 3, (s - tp0(IT)) & 3);
          if constexpr (IT == 0) {
            if constexpr ((n == 2 || n == 3) && s + 5 < 32) {
              if constexpr (n == 2) asm volatile("s_waitcnt lgkmcnt(%0)" ::"n"(s + 1 < QL ? 4 : 1) : "memory");
              X_UNIT1(s + 5, n - 2);
            }
            if constexpr (s >= W2S && s < W2S + 3 && n == 4) QKV_DMA1(2, s - W2S);
            if constexpr (LNP == 0 && s == LNL && n == 5) LN_LOAD();
            if constexpr (s == 0 && n < 2) { X_UNIT1(3 + n, 0); X_UNIT1(3 + n, 1); }
            if constexpr (s == LNS && n == 1) {
              if constexpr (LNP == 0) wait_vm<vm_at(0, s, n) - vm_at(0, LNL + 1, 0)>();
              asm volatile("" : "+v"(ep), "+v"(xl[0]), "+v"(xl[1])::"memory");
              const unsigned long long e = (((unsigned long long)(unsigned)__builtin_amdgcn_readfirstlane(ep[1]) << 32) | (unsigned)__builtin_amdgcn_readfirstlane(ep[0])) + 1ull + dispatch_id() * 0x9E3779B97F4A7C16ull;
              T = u32x2{(unsigned)e, (unsigned)(e >> 32)};
              unpack8v(xl[0], lv);
              xs = (lv[0] + lv[1]) + (lv[2] + lv[3]);
              xq = __builtin_elementwise_fma(lv[0], lv[0], lv[1] * lv[1]) + __builtin_elementwise_fma(lv[2], lv[2], lv[3] * lv[3]);
              asm volatile("" : "+v"(xs), "+v"(xq));
            }
            if constexpr (s == LNS && n == 4) {
              unpack8v(xl[1], lv);
              xs += (lv[0] + lv[1]) + (lv[2] + lv[3]);
              xq += __builtin_elementwise_fma(lv[0], lv[0], lv[1] * lv[1]) + __builtin_elementwise_fma(lv[2], lv[2], lv[3] * lv[3]);
              auto t = __builtin_amdgcn_permlane32_swap(__builtin_bit_cast(unsigned, hadd(xs)), __builtin_bit_cast(unsigned, hadd(xq)), false, false);
              lmean = asf(t[0]) + asf(t[1]);
              asm volatile("" : "+v"(lmean));
            }
            if constexpr (s == LNS + 1 && n == 1) { lmean += dppf<0xB1>(lmean); lmean += dppf<0x4E>(lmean); lmean += dppf<0x124>(lmean); asm volatile("" : "+v"(lmean)); }
            if constexpr (s == LNS + 1 && n == 4) { lmean = add_x16(lmean + dppf<0x128>(lmean)) * (1.0f / 1024.0f); asm volatile("" : "+v"(lmean)); }
            if constexpr (s == LNS + 2 && n == 1) {
              const float m2 = asf(__builtin_amdgcn_readlane(__builtin_bit_cast(unsigned, lmean), 32));
              xs[0] = ln_rd(fmaxf(__builtin_fmaf(-lmean, lmean, m2), 0.f));
              asm volatile("" : "+v"(xs));
            }
            if constexpr (s == LNS + 2 && n == 5) {
              if (l == 0) st16(u32x4{__builtin_bit_cast(unsigned, (float)xs[0]), __builtin_bit_cast(unsigned, -rbf1(lmean)), T[0], T[1]}, rs, OFF_AB + (p * 256 + h * 8 + w) * 16, 0);
            }
            if constexpr (s == POI && n == 1) {
              if (w == 1) {
                const unsigned prb = 2 * p + (h >> 4), pct = h & 15;
                unsigned pz1 = POISON;
                asm volatile("" : "+v"(pz1));
                const u32x4 pz = {pz1, pz1, pz1, pz1};
                st16(pz, rs, OFF_STAT + (prb * 16 + pct) * 2048 + l * 16, 0);
                st16(pz, rs, OFF_STAT + (prb * 16 + pct) * 2048 + 1024 + l * 16, 0);
                if (l < 16) st16(pz, rs, OFF_CA + prb * 4096 + pct * 256 + l * 16, 0);
                else if (l == 16 && pct < RKEY) st4(pz1, rs, KEY_AT(0, pct) + prb * SKEY, 0);
              }
            }
            if constexpr (s == POI + 4 && n == 1) { if (w == 1 && l < RX) st8(T, rs, F_XN + (p * RX + l) * 256 + h * 8, 0); }
          } else {
            if constexpr ((s == 0 && n == 0) || (s == 2 && n == 2)) asm volatile("buffer_load_dwordx2 %0, %1, %2, 0 offen sc1" : "=v"(cxn) : "v"(X1_OFF()), "s"(rs) : "memory");
            if constexpr ((s == 1 && n == 3) || (s == 4 && n == 0)) {
              wait_vm<vm_at(1, s, n) - (s == 1 ? vm_at(1, 0, 1) : vm_at(1, 2, 3))>();
              asm volatile("" : "+v"(cxn)::"memory");
              xok |= __builtin_amdgcn_ballot_w64(cxn[0] == T[0] && cxn[1] == T[1]);
            }
#define IMG_OFF() (F_X1 + ((((h >> 3) * 2 + ((l >> 2) & 1)) * RX1 + (xcd >> 1) % RX1) * 32 + (l & 1) * 16 + (h & 7) * 2 + ((l >> 1) & 1)) * SX1)
            if constexpr (s == 0 && n == 2) { asm volatile("" : "=v"(cx1)); if (w == 1) asm volatile("buffer_load_dwordx2 %0, %1, %2, 0 offen sc1" : "=v"(cx1) : "v"(IMG_OFF()), "s"(rs) : "memory"); }
            if constexpr (s == 2 && n == 4) {
              if (w == 1) {
                wait_vm<vm_at(1, 2, 4) - vm_at(1, 0, 3)>();
                asm volatile("" : "+v"(cx1)::"memory");
                if (!__all(cx1[0] == T[0] && cx1[1] == T[1])) poll_flags(rs, IMG_OFF(), 0, T);
              }
            }
            if constexpr (ar >= 0 && (ar & 7) == 0) x1_need(3ull << (((rot + ar / 8) & 7) * 2));
            const unsigned R = (rb * 4 + (w & 3)) * 32 + (w >> 2) * 16 + (l & 15), so = OFF_STAT + rb * 32768;
            if constexpr (s == (SPL < 0 ? 0 : SPL) && n == (SPL < 0 ? 1 : 4)) {
#pragma unroll
              for (int i = 0; i < 4; ++i) ld16h<0>(fx[i], rs, (4 * g + i) * 2048 + (R & 127) * 16, so);
            }
            if constexpr (s == SPL + 2 && n == 1) {
              wait_vm<vm_at(1, s, n) - (SPL < 0 ? vm_at(1, 0, 2) : vm_at(1, SPL, 5))>();
              PINV4(fx);
              fin = fin_key(rs, T, g * 8192 + (R & 127) * 16, so, smem + L_SKEY, l, w, R, wsum, fx);
              count_in(2);
            }
            if constexpr (s == SPL + 3 && n == 0) qc = __hip_atomic_load(qcnt + 2, __ATOMIC_RELAXED, __HIP_MEMORY_SCOPE_WORKGROUP);
            if constexpr (s == SPL + 3 && n == 1) { count_wait(2, 8); fin_cand(rs, smem + L_SKEY, 0, g, rb, ct, fin, v); }
            if constexpr (s == SPL + 3 && n == 3) {
              fin_x2(fin, v);
              x2p[0] = pack8v(v); x2p[1] = pack8v(v + 4);
              asm volatile("" : "+v"(x2p[0]), "+v"(x2p[1]));
            }
            if constexpr (s == SPL + 3 && n == 5) {
              if ((w & 3) * 2 + (w >> 2) == ct >> 1) {
                const float f = 1.0f + fin.rd, rd2 = ln_rd(fin.var * f * f), mb2 = rbf1(__builtin_fmaf(fin.mean - fin.mb, fin.rd, fin.mean));
                if (g == 0 && ((l >> 3) & 1) == (ct & 1))
                  st16(u32x4{__builtin_bit_cast(unsigned, f * rd2), __builtin_bit_cast(unsigned, -__builtin_fmaf(fin.mb, fin.rd, mb2) * __builtin_amdgcn_rcpf(f)), T[0], T[1]}, rs, OFF_AB + 32768 + R * 16, 0);
              }
            }
          }
          if constexpr (IT == 1 && ar >= 0) A_LOAD1(ar / 2, ar & 1);
          if constexpr (!IT && s == CSL && n == 4) { asm volatile("" : "=v"(fx[0])); if (w < 6) ld16h<0>(fx[0], rs, l * 16, OFF_CS + (h * 6 + w) * 1024); }
          if constexpr (!IT && s == CSS && n == 2) {
            if (w < 6) {
              wait_vm<SINCE(CSL)>();
              asm volatile("" : "+v"(fx[0])::"memory");
              tag_wait(rs, T, l * 16, OFF_CS + (h * 6 + w) * 1024, fx[0]);
              const f32x2 c = {sum_hi8(asf(fx[0][0])), sum_hi8(asf(fx[0][1]))};
              if (l < 8) *(LDS_AS f32x2*)(smem + L_CSL + w * 64 + l * 8) = c;
            }
          }
          constexpr int ABL = abl(IT), ABS = abs_(IT);
          if constexpr (s == ABL && n == 4) ld16h<0>(bv[0], rs, (l & 31) * 16, AB_OFF());
          if constexpr (s == ABS && n == 2) {
            wait_vm<SINCE(ABL)>();
            asm volatile("" : "+v"(bv[0])::"memory");
            tag_wait(rs, T, (l & 31) * 16, AB_OFF(), bv[0]);
            const unsigned t0 = bv[0][0], t1 = bv[0][1];
            ((LDS_AS unsigned*)(smem + L_ABW + rt * 2048))[l] = l < 32 ? t0 : t1;
          }
          if constexpr (s == ABS && n == 3) {
#pragma unroll
            for (int a = 0; a < 2; ++a) {
              const LDS_AS unsigned* aw = (const LDS_AS unsigned*)(smem + L_ABW + rt * 2048) + 16 * a;
              aq[a] = aw[l & 15]; bq[a] = aw[32 + (l & 15)];
              av[a] = *(const LDS_AS u32x4*)(aw + 4 * g); bv[a] = *(const LDS_AS u32x4*)(aw + 32 + 4 * g);
            }
          }
          if constexpr (s == ABS && n == 5) {
#pragma unroll
            for (int a = 0; a < 2; ++a) { b2[a] = f32x2{asf(bq[a]), asf(bq[a])}; asm volatile("" : "+v"(b2[a])); }
          }
          if constexpr (s == ABS + 1) {
            constexpr int k = (n + 4) % 6;
            if constexpr (k < 4) cs4[k & 1] = *(const LDS_AS f32x4*)(smem + L_CSL + k * 64 + g * 16);
            else cs4[k & 1][0] = *(const LDS_AS float*)(smem + L_CSL + k * 64 + (l & 15) * 4);
          }
          if constexpr ((s == ABS + 1 && n > 0) || (s == ABS + 2 && n == 0)) {
            constexpr int k = (n + 3) % 6;
            if constexpr (k < 4) {
#pragma unroll
              for (int a = 0; a < 2; ++a) acc[a][k] = __builtin_elementwise_fma(cs4[k & 1], f32x4{b2[a][0], b2[a][1], b2[a][0], b2[a][1]}, acc[a][k]);
            } else {
              f32x2 c2 = {cs4[k & 1][0], cs4[k & 1][0]};
              asm volatile("" : "+v"(c2));
#pragma unroll
              for (int a = 0; a < 2; ++a) acc[a][k] = __builtin_elementwise_fma(f32x4{c2[0], c2[1], c2[0], c2[1]}, __builtin_bit_cast(f32x4, bv[a]), acc[a][k]);
            }
          }
          if constexpr (IT == 0) {
            if constexpr (s == CSD && n < 3) fx[n] = frag0(SLOT((s >> 2) + PO()), 3 * w + n);
            if constexpr (s == CSD + 1 && n == 1) {
              unsigned one2 = 0x3F803F80u;
              asm volatile("" : "+v"(one2));
              const u32x4 o4 = {one2, one2, one2, one2};
              const f32x4 z4 = {0.f, 0.f, 0.f, 0.f};
              sa = mfma16(fx[0], o4, z4); sb = mfma16(fx[1], o4, z4); sc = mfma16(fx[2], o4, z4);
            }
            if constexpr (s == CSD + 2 && n == 1) {
              const int c = l & 15;
              const f32x4 t0 = sa, t1 = sb, t2 = sc, t = c == 0 ? +t0 : c == 1 ? +t1 : +t2;
              if (c < 3) *(LDS_AS f32x4*)(smem + L_CSP + (w * 3 + c) * 64 + g * 16) = t;
              count_in(2);
            }
            if constexpr (s == CSD + 4 && n == 1) {
              if (w == 2) {
                qc = 0;
                count_wait(2, 8);
                const LDS_AS f32x2* cp = (const LDS_AS f32x2*)(smem + L_CSP) + min(l, 47);
                const f32x2 t = (cp[0] + cp[48]) + (cp[96] + cp[144]);
                if (l < 48) st16(u32x4{__builtin_bit_cast(unsigned, (float)t[0]), __builtin_bit_cast(unsigned, (float)t[1]), T[0], T[1]}, rs, (l >> 3) * 1024 + (l & 7) * 16, OFF_CS + h * 6144 + p * 128);
              }
            }
            if constexpr (s >= 29 && (n & 1)) keep[3 * (s - 29) + (n >> 1)] = frag0(SLOT(s - 24), 3 * w + (n >> 1));
            if constexpr (s == (TS0 < TEX ? TS0 : TEX) - 2 && n == 1) {
              ARGP(pwg, 5); ARGP(pwu, 6);
              const int t = tid < 288 ? tid - 64 : tid - 288, go = (bi * 32 + t / 7) * F_ + xcd * 448 + (t % 7) * 64;
              tsrc = tid < 64 ? pwo + bid * 4096 + tid * 64 : tid < 288 ? pwg + go : pwu + go;
              asm volatile("" : "+v"(tsrc));
            }
            if constexpr (s >= TS0 && s < TS0 + TSN + TSD * (7 >> TSG) && n == 4) {
              if (tstep == s && ((TM0 >> w) & 1)) TOUCH(tsrc);
            }
            if constexpr (s == TEX && n == 5) {
              const GAS bf16* t2 = ((TM1 >> w) & 1) ? tsrc : (const GAS bf16*)x + p * 262144;
              if ((NTM & 1) && w > 0) asm volatile("s_mov_b32 m0, %0\n\ts_nop 0\n\tglobal_load_lds_dword %1, off nt" ::"s"(lbase + L_FXL), "v"(t2) : "memory");
              else TOUCH(t2);
            }
          } else {
            if constexpr (s >= 27 && s < 30 && (n == 0 || n == 3)) { const unsigned wov = WO_VOFF(); WO_PIECE(4 + 2 * (s - 27) + n / 3); }
            if constexpr (WON && s == WOS && n == 4) st16(wof, rs, (ct * 128 + rb * 8 + w) * 1024 + l * 16, OFF_WOI);
            if constexpr (WON && s == 19 && n == 1) { if (tid == 64) st8(T, rs, (ct * 16 + rb) * 256, F_WOI); }
            if constexpr (WON && s == 23 && n == 4) { asm volatile("" : "=v"(cx1)); if (w == 2) ld8h(cx1, rs, (l & 15) * 256, F_WOI + ct * 4096); }
            if constexpr (WON && s == 26 && n == 3) {
              if (w == 2) {
                wait_vm<SINCE(23)>();
                asm volatile("" : "+v"(cx1)::"memory");
                if (!__all(cx1[0] == T[0] && cx1[1] == T[1])) poll_flags(rs, (l & 15) * 256, F_WOI + ct * 4096, T);
              }
            }
            if constexpr (s == FKL && n == 4) { asm volatile("" : "=v"(kv)); if (w == 0) ld4h<0>(kv, rs, (l & 15) * SKEY, KEY_AT(0, KREP())); }
            if constexpr (s == FSL && n == 1) {
              asm volatile("" : "=v"(fx[0]), "=v"(fx[1]));
              if (w == 0) {
                wait_vm<SINCE(FKL)>();
                asm volatile("" : "+v"(kv)::"memory");
                const unsigned k_ = key_wait(rs, T, l, 0, KREP(), kv);
                if (l == 0) *(LDS_AS unsigned*)(smem + L_SKEY + 32) = k_;
                fkey = k_;
                ld16h<0>(fx[0], rs, (l >> 3) * 256 + (l & 7) * 16, cand_off(k_, 0)); ld16h<2048>(fx[1], rs, (l >> 3) * 256 + (l & 7) * 16, cand_off(k_, 0));
              }
            }
            if constexpr (s == FS0 - 1 && n == FSN) {
              if (w == 0) {
                wait_vm<vm_at(1, s, n) - vm_at(1, FSL, 2)>();
                asm volatile("" : "+v"(fx[0]), "+v"(fx[1])::"memory");
                word_wait<2>(rs, T, (l >> 3) * 256 + (l & 7) * 16, cand_off(fkey, 0), fx);
                *(LDS_AS u32x4*)(smem + L_ROW + l * 16) = fx[0]; *(LDS_AS u32x4*)(smem + L_ROW + 1024 + l * 16) = fx[1];
                count_in(0);
              }
            }
            if constexpr (s == FS0 - 1 && n == 1) {
              asm volatile("" : "=v"(dfr[0]), "=v"(dfr[1]), "=v"(dfr[2]), "=v"(dfr[3]), "=v"(dfr[4]), "=v"(dfr[5]), "=v"(dfr[6]), "=v"(dfr[7]));
              if (ffn_on) {
#pragma unroll
                for (int i = 0; i < 8; ++i) dfr[i] = trread(smem, L_DN + w * 4096 + g * 1024 + ((l & 15) >> 2) * 256 + i * 32 + (l & 3) * 8);
              }
            }
            if constexpr (s == FS0) {
              if constexpr (n == 0) qc = __hip_atomic_load(qcnt, __ATOMIC_RELAXED, __HIP_MEMORY_SCOPE_WORKGROUP);
              if constexpr (n == 1) {
                count_wait(0, 1);
                kv = *(const LDS_AS unsigned*)(smem + L_SKEY + 32);
#pragma unroll
                for (int i = 0; i < 4; ++i) fx[i] = *(const LDS_AS u32x4*)(smem + L_ROW + w * 256 + i * 64 + g * 16);
              }
              if constexpr (n == 2) {
                fkey = __builtin_amdgcn_readfirstlane(kv);
                sel0 = key_row(fkey);
                aff = (int)(sel0 >> 8) == p;
                asm volatile("" : "=v"(sa), "=v"(sb));
                if (ffn_on) ffn_gu(gv, uv, fx, sa, sb);
              }
              if constexpr (n == 4) {
                if (ffn_on) {
                  ffn_gu_store(smem, l, w, sa, sb);
                  count_in(1);
                }
              }
            }
            if constexpr (s == FS0D) {
              if constexpr (n == 0) qc = __hip_atomic_load(qcnt + 1, __ATOMIC_RELAXED, __HIP_MEMORY_SCOPE_WORKGROUP);
              if constexpr (n == 1) { asm volatile("" : "=v"(sa)); if (ffn_on) { count_wait(1, 8); sa = ffn_d_read(smem, l); } }
              if constexpr (n == 2) { asm volatile("" : "=v"(hh)); if (ffn_on) hh = ffn_d_h(sa); }
              if constexpr (n == 4) { asm volatile("" : "=v"(sa)); if (ffn_on) sa = ffn_d_all(dfr, hh); }
              if constexpr (n == 5) { if (ffn_on) ffn_d_store(rs, l, w, 0, slice, sa); }
            }
            if constexpr (s == FS1 && n == 4) {
              DEF4(fx);
              asm volatile("" : "=v"(cxn), "=v"(cx1));
              if (!ffn_on) {
                const unsigned G = xcd * 4 + bi - 28;
#pragma unroll
                for (int k = 0; k < 4; ++k) ld16h<0>(fx[k], rs, RED_VO(k), RED_SO(0, G));
                ld8h(cxn, rs, RED_JJ(), cand_off(fkey, 0) + XO(G)); ld8h(cx1, rs, RED_JJ() + 128, cand_off(fkey, 0) + XO(G));
              }
            }
            if constexpr (s == FS1S && n == 2) {
              if (!ffn_on) {
                wait_vm<SINCE(FS1)>();
                PINV4(fx);
                asm volatile("" : "+v"(cxn), "+v"(cx1)::"memory");
                red_sum(rs, smem, T, tid, w, 0, xcd * 4 + bi - 28, fkey, fx, cxn, cx1);
                BAR();
                if (w == 0) {
                  const u32x2 ov = red_final(smem, l, fkey, cxn, cx1);
                  if (l < 8) st8(unpoison2(ov), rs, OFF_XNEW + (xcd * 4 + bi - 28) * 256 + ((l & 3) * 2 + (l >> 2)) * 8, 0);
                }
              }
            }
            if constexpr (s == FXA && n == 4) { asm volatile("" : "=v"(fx[0])); if (w == 1) ld16h<0>(fx[0], rs, ((l >> 2) & 3) * 256 + (l & 3) * 16, XP_OFF()); }
            if constexpr (s == FXB - 1 && n == FSN) {
              if (w == 1) {
                wait_vm<SINCE(FXA)>();
                asm volatile("" : "+v"(fx[0])::"memory");
                word_wait<1>(rs, T, ((l >> 2) & 3) * 256 + (l & 3) * 16, XP_OFF(), fx);
                if (l < 16) *(LDS_AS u32x4*)(smem + L_ROW + l * 16) = fx[0];
                count_in(0);
              }
            }
            if constexpr (s == FXB) {
#define FX_FRAG(i) (*(const LDS_AS u32x4*)(size_t)(bo[0][0] + (1 + PO()) * STAGE + (6 * (i) + w) * 1024))
              if constexpr (n == 0) { qc = __hip_atomic_load(qcnt, __ATOMIC_RELAXED, __HIP_MEMORY_SCOPE_WORKGROUP); }
              if constexpr (n == 1) {
                DEF4(fx); DEF4(df);
                if (w < 6) {
                  count_wait(0, 2);
#pragma unroll
                  for (int i = 0; i < 4; ++i) { fx[i] = *(const LDS_AS u32x4*)(smem + L_ROW + i * 64 + g * 16); df[i] = FX_FRAG(i); }
                }
              }
              if constexpr (n == 2) { asm volatile("" : "=v"(sa), "=v"(sb)); if (w < 6) dot_mma(fx); }
              if constexpr (n == 4) { if (w < 6) dot_store(OFF_FX); }
            }
            if constexpr (s == FXL && n == 4) {
              asm volatile("" : "=v"(fx[0]), "=v"(fx[1]));
              if (aff && w < 4) { ld16h<0>(fx[0], rs, FP_VO(), FP_OFF(0)); ld16h<0>(fx[1], rs, FP_VO(), FP_OFF(1)); }
            }
            if constexpr (s == FXS && n == 4) {
              if (aff && w < 4) {
                wait_vm<0>();
                asm volatile("" : "+v"(fx[0]), "+v"(fx[1])::"memory");
                if (!FX_OK(2)) {
                  unsigned spins = 0;
                  do {
                    if (spin_fail(spins, rs, T)) break;
                    fx[0] = ld16(rs, FP_VO(), FP_OFF(0)); fx[1] = ld16(rs, FP_VO(), FP_OFF(1));
                  } while (!FX_OK(2));
                }
                if (w == 3) {
                  f32x2 nx[8];
                  unpack8v(fx[0], nx); unpack8v(fx[1], nx + 4);
                  const f32x2 sm = ((nx[0] + nx[1]) + (nx[2] + nx[3])) + ((nx[4] + nx[5]) + (nx[6] + nx[7]));
                  const float mean = sum64(hadd(sm)) * (1.0f / 1024.0f);
                  const f32x2 m2 = {mean, mean};
                  f32x2 q = {0.f, 0.f};
#pragma unroll
                  for (int j = 0; j < 8; ++j) { const f32x2 d = nx[j] - m2; q = __builtin_elementwise_fma(d, d, q); }
                  const float rd = ln_rd(sum64(hadd(q)) * (1.0f / 1024.0f));
                  if (l == 0) *(LDS_AS f32x2*)(smem + L_FXL + 384) = f32x2{rd, -rbf1(mean)};
                } else {
                  tile_sum(fx[0], L_FXL);
                }
                count_in(3);
              }
            }
          }
        };
        static_for<32>([&](auto sc_) __attribute__((always_inline)) {
          constexpr int s = decltype(sc_)::value;
          if constexpr (s > 0) PIN_ACC();
          if constexpr (QPR && s == QPR) { if (w >= 4) __builtin_amdgcn_s_setprio(1); }
          if constexpr (IT == 1) wait_vm<vm_at(IT, s, 0) - vm_a(IT, s)>();
          pin_w<s % WA>(af[s % WA][0], af[s % WA][1]);
          SB();
          static_for<6>([&](auto nc_) __attribute__((always_inline)) {
            constexpr int n = decltype(nc_)::value;
            if constexpr (s == 0) { acc[0][n] = f32x4{0.f, 0.f, 0.f, 0.f}; acc[1][n] = acc[0][n]; }
            constexpr bool on = s < QL || n >= 2;
            if constexpr (on) acc[0][n] = n < 4 ? mfma16(bfr[n].v, af[s % WA][0], acc[0][n]) : mfma16(af[s % WA][0], bfr[n].v, acc[0][n]);
            SB();
            if constexpr (n == 0) { QKV_RDB(s, 5); }
            else if constexpr (s + 1 < 32 && (s + 1 < QL || n > 2)) QKV_RDB(s + 1, n - 1);
            SB();
            if constexpr (on) acc[1][n] = n < 4 ? mfma16(bfr[n].v, af[s % WA][1], acc[1][n]) : mfma16(af[s % WA][1], bfr[n].v, acc[1][n]);
            SB();
            if constexpr (s + 1 < 32 && (s + 1 < QL || n >= 2)) QKV_RDA(s + 1, n);
            SB();
            gap(sc_, nc_);
            SB();
          });
        });
        PIN_ACC();
        frag_t qw[32 - QL][2];
        static_for<32 - QL>([&](auto c_) __attribute__((always_inline)) {
          constexpr int s = QL + decltype(c_)::value;
          QKV_RDA(s, 0); QKV_RDB(s, 0); QKV_RDA(s, 1); QKV_RDB(s, 1);
          qw[s - QL][0] = bfr[0]; qw[s - QL][1] = bfr[1];
        });
        SB();
        const bool own = aff && (int)((sel0 >> 5) & 7) == rt;
        const bool m0 = !(sel0 & 16), mq = (l & 15) == (int)(sel0 & 15), mv = g == (int)((sel0 & 15) >> 2);
        auto patch_qk = [&](int n) __attribute__((always_inline)) {
          f32x4 t = *(const LDS_AS f32x4*)(smem + L_FXL + n * 64 + g * 16);
          const f32x4 c = *(const LDS_AS f32x4*)(smem + L_CSL + n * 64 + g * 16);
          const float nb = *(const LDS_AS float*)(smem + L_FXL + 388);
#pragma unroll
          for (int e = 0; e < 4; ++e) { t[e] = __builtin_fmaf(c[e], nb, t[e]); asm volatile("" : "+v"(t[e])); }
          acc[0][n] = (mq && m0) ? t : acc[0][n];
          acc[1][n] = (mq && !m0) ? t : acc[1][n];
        };
        if (own) {
          while (__hip_atomic_load(qcnt + 3, __ATOMIC_RELAXED, __HIP_MEMORY_SCOPE_WORKGROUP) < 4u) {}
          asm volatile("" ::: "memory");
          patch_qk(2); patch_qk(3);
          const f32x2 nab = *(const LDS_AS f32x2*)(smem + L_FXL + 384);
          const LDS_AS float* fl_ = (const LDS_AS float*)(smem + L_FXL + 256) + (l & 15);
          const LDS_AS float* cl_ = (const LDS_AS float*)(smem + L_CSL + 256) + (l & 15);
          const float t4 = __builtin_fmaf(cl_[0], nab[1], fl_[0]), t5 = __builtin_fmaf(cl_[16], nab[1], fl_[16]);
          const unsigned na = __builtin_bit_cast(unsigned, (float)nab[0]);
#pragma unroll
          for (int a = 0; a < 2; ++a) {
            const bool ma = a ? !m0 : m0;
            aq[a] = (mq && ma) ? na : aq[a];
#pragma unroll
            for (int e = 0; e < 4; ++e) {
              const bool me = mv && ma && e == (int)(sel0 & 3);
              acc[a][4][e] = me ? t4 : acc[a][4][e]; acc[a][5][e] = me ? t5 : acc[a][5][e];
              av[a][e] = me ? na : av[a][e];
            }
          }
        }
        constexpr float C2 = 1.4426950408889634f * 0.17677669529663687f;
#pragma unroll
        for (int a = 0; a < 2; ++a) {
          f32x2 a2 = {asf(aq[a]), asf(aq[a])};
          qs2[a] = f32x2{asf(aq[a]) * C2, asf(aq[a]) * C2};
          asm volatile("" : "+v"(a2), "+v"(qs2[a]));
          acc[a][2] *= f32x4{a2[0], a2[1], a2[0], a2[1]}; acc[a][3] *= f32x4{a2[0], a2[1], a2[0], a2[1]};
          acc[a][4] *= __builtin_bit_cast(f32x4, av[a]); acc[a][5] *= __builtin_bit_cast(f32x4, av[a]);
        }
        k0 = pack8(acc[0][2], acc[0][3]); k1 = pack8(acc[1][2], acc[1][3]); v0 = pack8(acc[0][4], acc[1][4]); v1 = pack8(acc[0][5], acc[1][5]);
        const unsigned kvo = OFF_KV + IT * (8u << 20) + (p * 32 + h) * 32768 + rt * 4096;
        if (p < 7) { st16(k0, rs, l * 16, kvo); st16(k1, rs, l * 16 + 1024, kvo); st16(v0, rs, l * 16 + 2048, kvo); st16(v1, rs, l * 16 + 3072, kvo); }
        char* ownp = smem + (8 + rt) * 2048 + l * 16;
        *(u32x4*)(ownp + L_K) = k0;
        if (rt < 7) *(u32x4*)(ownp + L_K + 1024) = k1;
        *(u32x4*)(ownp + L_V) = v0; *(u32x4*)(ownp + L_V + 1024) = v1;
        SB();
        static_for<32 - QL>([&](auto c_) __attribute__((always_inline)) {
          constexpr int s = QL + decltype(c_)::value;
#pragma unroll
          for (int n = 0; n < 2; ++n) { acc[0][n] = mfma16(qw[s - QL][n].v, af[s % WA][0], acc[0][n]); acc[1][n] = mfma16(qw[s - QL][n].v, af[s % WA][1], acc[1][n]); }
        });
        if (own) { patch_qk(0); patch_qk(1); }
      };
      qkv_loop(it_c);
    }
#undef A_LOAD1
#undef QKV_RDA
#undef QKV_RDB
#undef QKV_BO
    {
      IDS();
      const GAS bf16 *pwo, *pwg, *pwu;
      asm volatile("s_load_dwordx2 %0, %3, 24\n\ts_load_dwordx2 %1, %3, 40\n\ts_load_dwordx2 %2, %3, 48" : "=&s"(pwo), "=&s"(pwg), "=&s"(pwu) : "s"(ka));
      const int ql = l & 15;
      const f32x4 z4 = {0.f, 0.f, 0.f, 0.f}, ninf = {-INFINITY, -INFINITY, -INFINITY, -INFINITY};
      LDS_AS unsigned* rdy = (LDS_AS unsigned*)(smem + L_RDY);
      const unsigned rtag = T[0] + it;
      u32x4 qf[2];
      f32x4 cn[2], oa[2][2], sum[2], S[2][2];
      bool told = false;
      auto announce = [&](bool now) __attribute__((always_inline)) {
        if (!told) {
          unsigned st;
          asm volatile("s_getreg_b32 %0, hwreg(HW_REG_IB_STS)" : "=s"(st)::"memory");
          if (now || !(st & 0xC0000Fu)) {
            DRAIN();
            if (l == 0) st8(T, rs, rt * SKW, F_KV + (it * 256 + p * 32 + h) * SKV);
            told = true;
          }
        }
      };
      unsigned one2 = 0x3F803F80u;
      asm volatile("" : "+v"(one2));
      const u32x4 ones = {one2, one2, one2, one2};
      auto ex8 = [&](f32x4 a, f32x4 b) __attribute__((always_inline)) {
        return u32x4{pack2(__builtin_amdgcn_exp2f(a[0]), __builtin_amdgcn_exp2f(a[1])), pack2(__builtin_amdgcn_exp2f(a[2]), __builtin_amdgcn_exp2f(a[3])),
                     pack2(__builtin_amdgcn_exp2f(b[0]), __builtin_amdgcn_exp2f(b[1])), pack2(__builtin_amdgcn_exp2f(b[2]), __builtin_amdgcn_exp2f(b[3]))};
      };
      {
#pragma unroll
        for (int a = 0; a < 2; ++a) {
          const f32x4 c4 = {qs2[a][0], qs2[a][1], qs2[a][0], qs2[a][1]};
          qf[a] = pack8(acc[a][0] * c4, acc[a][1] * c4);
        }
        f32x4 mk;
#pragma unroll
        for (int e = 0; e < 4; ++e) mk[e] = (4 * g + e <= ql) ? 0.f : -INFINITY;
        const f32x4 d0 = mfma16(k0, qf[0], mk), d1 = mfma16(k1, qf[1], mk), d2 = mfma16(k0, qf[1], z4);
        const float m0 = -max_x32(max_x16(fmaxf(fmaxf(d0[0], d0[1]), fmaxf(d0[2], d0[3]))));
        const float m1 = -max_x32(max_x16(fmaxf(fmaxf(d1[0], d1[1]), fmaxf(d1[2], d1[3]))));
        cn[0] = f32x4{m0, m0, m0, m0}; cn[1] = f32x4{m1, m1, m1, m1};
        asm volatile("" : "+v"(cn[0]), "+v"(cn[1]));
        announce(false);
        const f32x4 e0 = d0 + cn[0];
        const u32x4 p0 = {pack2(__builtin_amdgcn_exp2f(e0[0]), __builtin_amdgcn_exp2f(e0[1])), pack2(__builtin_amdgcn_exp2f(e0[2]), __builtin_amdgcn_exp2f(e0[3])), 0u, 0u};
        const u32x4 p1 = ex8(d2 + cn[1], d1 + cn[1]);
        oa[0][0] = mfma16(v0, p0, z4); oa[0][1] = mfma16(v1, p0, z4); sum[0] = mfma16(ones, p0, z4);
        oa[1][0] = mfma16(v0, p1, z4); oa[1][1] = mfma16(v1, p1, z4); sum[1] = mfma16(ones, p1, z4);
      }
      const char* kb_ = smem + L_K + l * 16;
      const char* vb_ = smem + L_V + l * 16;
      const unsigned wov = WO_VOFF(), fnv = (l >> 1) * (F_ * 2) + (l & 1) * 16;
      asm volatile("" : "+v"(oa[0][0]), "+v"(oa[0][1]), "+v"(oa[1][0]), "+v"(oa[1][1]), "+v"(sum[0]), "+v"(sum[1]));
      const unsigned pw = __builtin_amdgcn_readfirstlane(it * 256 + p * 32 + h - 32), pfo = F_KV + pw * SKV + rt * SKW;
      const bool fetcher = rt < 4 && p > 0;
      announce(false);
      if (QPR) __builtin_amdgcn_s_setprio(0);
      BAR();
      asm volatile("" : "+s"(pwo), "+s"(pwg), "+s"(pwu));
#define KV_FETCH(gd)                                                                                                          \
  {                                                                                                                           \
    const unsigned pvo = OFF_KV + pw * 32768 + (gd) * 4096;                                                                   \
    dma16s<0>(rs, l * 16, pvo, lbase + L_K + (gd) * 2048); dma16s<1024>(rs, l * 16, pvo, lbase + L_K + (gd) * 2048);           \
    dma16s<0>(rs, l * 16, pvo + 2048, lbase + L_V + (gd) * 2048); dma16s<1024>(rs, l * 16, pvo + 2048, lbase + L_V + (gd) * 2048); \
  }
      auto fetch = [&]() __attribute__((always_inline)) {
        poll_flags(rs, KVJ ? (l & 1) * 4 * SKW : 0, pfo, T);
        KV_FETCH(rt);
        if (KVJ) { KV_FETCH(rt + 4); if (l == 0) rdy[8 + rt] = rtag; }
      };
      announce(false);
      const bool first = fetcher && told;
      if (first) fetch();
#define LDS16(a_) (*(const LDS_AS u32x4*)(size_t)(a_))
      u32x4 kf[2], vf[2], p0, p1;
#define AE(a, i) { S[a][(i) >> 2][(i) & 3] = __builtin_amdgcn_exp2f(S[a][(i) >> 2][(i) & 3]); asm volatile("" : "+v"(S[a][(i) >> 2][(i) & 3])); SB(); }
#define AC(a, P, j) { P[j] = pack2(S[a][(j) >> 1][2 * ((j) & 1)], S[a][(j) >> 1][2 * ((j) & 1) + 1]); asm volatile("" : "+v"(P[j])); SB(); }
#define PHASE(a, P, M1, M2, L2, M3, M4, L4, M5)                                                                               \
  AE(a, 0) AE(a, 1) M1; SB(); AE(a, 2) AE(a, 3) M2; SB(); L2; SB(); AC(a, P, 0) AC(a, P, 1) AE(a, 4) M3; SB(); AE(a, 5) AE(a, 6) M4; SB(); \
  L4; SB(); AE(a, 7) AC(a, P, 2) M5; SB(); AC(a, P, 3)
#define PV(a, d, P) oa[a][d] = mfma16(vf[d], P, oa[a][d])
#define RS(a, P) sum[a] = mfma16(ones, P, sum[a])
#define SC(a, t, c) S[a][t] = mfma16(kf[t], qf[a], c)
#define K_AT(ad) (kf[0] = LDS16(ad), kf[1] = LDS16((ad) + 1024))
#define V_AT(ad) (vf[0] = LDS16((ad) + L_V), vf[1] = LDS16((ad) + L_V + 1024))
#define A_FIRST(ad, c00, c01, c10, c11, KN) { K_AT(ad); V_AT(ad); SB(); SC(0, 0, c00); SC(0, 1, c01); SB(); PHASE(0, p0, SC(1, 0, c10), SC(1, 1, c11), KN, , , , ) }
#define A_NEXT(ad, c00, c01, c10, c11, KN)                                                                                        \
  { PHASE(1, p1, PV(0, 0, p0), PV(0, 1, p0), , SC(0, 0, c00), SC(0, 1, c01), , RS(0, p0))                                     \
    PHASE(0, p0, PV(1, 0, p1), PV(1, 1, p1), V_AT(ad), SC(1, 0, c10), SC(1, 1, c11), KN, RS(1, p1)) }
#define A_LAST() { PHASE(1, p1, PV(0, 0, p0), PV(0, 1, p0), , RS(0, p0), , , ) SB(); PV(1, 0, p1); PV(1, 1, p1); RS(1, p1); }
      int nw = 0;
      auto piece = [&]() __attribute__((always_inline)) {
        if constexpr (it == 0) { if (ffn_on && nw < 8) { FFN_DMA(nw); } ++nw; }
      };
#define XN_OFF() (F_XN + (((rt - 6) * 4 + g) * RX + bi % RX) * 256 + (l & 15) * 16)
      u32x4 fxn;
      asm volatile("" : "=v"(fxn));
      unsigned ad = lbase + l * 16 + (6 + rt) * 2048;
      asm volatile("" : "+v"(ad));
      if (rt > 0) {
        A_FIRST(ad + 2048, cn[0], cn[0], cn[1], cn[1], K_AT(ad));
#pragma unroll 1
        for (int i = rt - 1; i > 0; --i) {
          ad -= 2048;
          asm volatile("" : "+v"(ad));
          if (PCE && told && rt >= 4) piece();
          announce(false);
          A_NEXT(ad + 2048, cn[0], cn[0], cn[1], cn[1], K_AT(ad));
        }
        announce(false);
        A_LAST();
      }
      announce(true);
      {
        f32x4 mk;
#pragma unroll
        for (int e = 0; e < 4; ++e) mk[e] = (4 * g + e > ql) ? 0.f : -INFINITY;
        f32x4 cm0 = cn[0] + mk, cm1 = cn[1] + mk, cmi = ninf;
        asm volatile("" : "+v"(cm0), "+v"(cm1), "+v"(cmi));
        if (rt >= 4) {
          if (p > 0) {
            while (!__all(__hip_atomic_load(rdy + 8 + (l & 3), __ATOMIC_RELAXED, __HIP_MEMORY_SCOPE_WORKGROUP) == rtag)) __builtin_amdgcn_s_sleep(1);
            asm volatile("" ::: "memory");
          }
          if constexpr (it == 0) { for (; nw < 8;) piece(); }
          static_for<it ? 12 : 8>([&](auto c_) __attribute__((always_inline)) { constexpr int i = decltype(c_)::value; WO_PIECE_W(15 - i / 2, 2 * (rt & 3) + (i & 1)); });
          if (it == 0 && rt >= 6) ld16h<0>(fxn, rs, XN_OFF(), 0);
        } else if (p > 0) {
          if (!first) fetch();
          if (KVJ) {
            DRAIN();
            if (l < 2) rdy[rt + 4 * l] = rtag;
            if constexpr (it == 0) { for (; nw <= rt;) piece(); }
          } else {
            const u32x2 f2 = ld8(rs, 0, pfo + 4 * SKW);
            const bool up = __all(f2[0] == T[0] && f2[1] == T[1]);
            DRAIN();
            if (l == 0) rdy[rt] = rtag;
            if (!up) poll_flags(rs, 0, pfo + 4 * SKW, T);
            KV_FETCH(rt + 4);
            if (l == 0) rdy[8 + rt] = rtag;
            told = false;
          }
        }
        if (p > 0) {
          auto landed = [&](bool now) __attribute__((always_inline)) {
            if (!told) {
              unsigned st;
              asm volatile("s_getreg_b32 %0, hwreg(HW_REG_IB_STS)" : "=s"(st)::"memory");
              if (now || !(st & 0xC0000Fu)) {
                DRAIN();
                if (l == 0) rdy[rt + 4] = rtag;
                told = true;
              }
            }
          };
          unsigned have = 0;
          auto need = [&](int gq) __attribute__((always_inline)) {
            if (__builtin_expect(gq < 8 && !((have >> gq) & 1), 0)) {
              do {
                const unsigned t = __hip_atomic_load(rdy + (l & 7), __ATOMIC_RELAXED, __HIP_MEMORY_SCOPE_WORKGROUP);
                have = (unsigned)__builtin_amdgcn_ballot_w64(t == rtag);
              } while (!((have >> gq) & 1));
              asm volatile("" ::: "memory");
            }
          };
          ad = lbase + l * 16 + rt * 2048;
          asm volatile("" : "+v"(ad));
          need(rt);
          K_AT(ad); V_AT(ad); SB(); SC(0, 0, cm0); SC(0, 1, cn[0]); SB();
          PHASE(0, p0, SC(1, 0, cmi), SC(1, 1, cm1), , , , , )
          if (!KVJ) landed(false);
          need(rt + 1);
          K_AT(ad + 2048);
#pragma unroll 1
          for (int gq = rt + 1; gq < 8; ++gq) {
            ad += 2048;
            asm volatile("" : "+v"(ad));
            if (KVJ) piece();
            else { landed(gq >= rt + KVD); if (told) { piece(); if (PC2) piece(); } }
            need(gq + 1);
            A_NEXT(ad, cn[0], cn[0], cn[1], cn[1], K_AT(ad + 2048));
          }
          A_LAST();
        }
        if constexpr (it == 0) { for (; nw < 8;) piece(); }
      }
      {
        float big = 0.f;
#pragma unroll
        for (int a = 0; a < 2; ++a)
#pragma unroll
          for (int d = 0; d < 2; ++d)
            big = __builtin_elementwise_maximum(__builtin_elementwise_maximum(big, __builtin_elementwise_maximum(fabsf(oa[a][d][0]), fabsf(oa[a][d][1]))),
                                                __builtin_elementwise_maximum(fabsf(oa[a][d][2]), fabsf(oa[a][d][3])));
        bool ok = sum[0][0] >= 0.5f && sum[0][0] < 0x1p64f && sum[1][0] >= 0.5f && sum[1][0] < 0x1p64f && big < INFINITY;
        if (!__all(ok)) {
#pragma unroll
          for (int a = 0; a < 2; ++a) {
            const int qs = 2 * rt + a, qq = 256 + 16 * qs + ql, klo = max(qq - 255, p > 0 ? 0 : 256);
            auto score = [&](int t) __attribute__((always_inline)) {
              f32x4 s = mfma16(t == 31 ? k1 : *(const u32x4*)(kb_ + t * 1024), qf[a], z4);
#pragma unroll
              for (int e = 0; e < 4; ++e) { const int kk = 16 * t + 4 * g + e; s[e] = (kk <= qq && kk >= klo) ? s[e] : -INFINITY; }
              return s;
            };
            float mx = -INFINITY;
#pragma unroll 1
            for (int t = p > 0 ? qs : 16; t <= qs + 16; ++t) { const f32x4 s = score(t); mx = fmaxf(fmaxf(mx, fmaxf(s[0], s[1])), fmaxf(s[2], s[3])); }
            mx = max_x32(max_x16(mx));
            oa[a][0] = z4; oa[a][1] = z4; sum[a] = z4;
#pragma unroll 1
            for (int G = p > 0 ? qs >> 1 : 8; G <= (qs + 16) >> 1; ++G) {
              const f32x4 s0 = score(2 * G), s1 = score(2 * G + 1);
              f32x4 e0, e1;
#pragma unroll
              for (int e = 0; e < 4; ++e) { e0[e] = __builtin_amdgcn_exp2f(s0[e] - mx); e1[e] = __builtin_amdgcn_exp2f(s1[e] - mx); }
              const u32x4 pf = pack8(e0, e1);
              oa[a][0] = mfma16(*(const u32x4*)(vb_ + G * 2048), pf, oa[a][0]);
              oa[a][1] = mfma16(*(const u32x4*)(vb_ + G * 2048 + 1024), pf, oa[a][1]);
              sum[a] = mfma16(ones, pf, sum[a]);
            }
          }
        }
      }
#pragma unroll
      for (int a = 0; a < 2; ++a) {
        const float inv = __builtin_amdgcn_rcpf(sum[a][0]);
        oa[a][0] *= inv; oa[a][1] *= inv;
      }
      const unsigned oo = OFF_O + it * (4u << 20) + ((p * 8 + rt) * 64 + h * 2) * 1024;
      st16(pack8(oa[0][0], oa[0][1]), rs, l * 16, oo);
      st16(pack8(oa[1][0], oa[1][1]), rs, l * 16 + 1024, oo);
      if constexpr (it == 0) {
        wait_vm<2>();
        asm volatile("" : "+v"(fxn)::"memory");
        if (rt >= 6 && !__all(fxn[0] == T[0] && fxn[1] == T[1] && fxn[2] == T[0] && fxn[3] == T[1])) poll_flags2(rs, XN_OFF(), T);
        DEF4(gv); DEF4(uv);
        if (ffn_on) {
          const unsigned ro = 65536 + w * 1024 + (4 * g + ((l & 15) >> 2)) * 32 + (l & 3) * 8;
#pragma unroll
          for (int i = 0; i < 4; ++i) {
            frag_t f;
            f.hf.a = trread(smem, ro + i * 16384); f.hf.b = trread(smem, ro + i * 16384 + 512);
            gv[i] = f.v;
            f.hf.a = trread(smem, ro + i * 16384 + 8192); f.hf.b = trread(smem, ro + i * 16384 + 8704);
            uv[i] = f.v;
          }
        }
        PINV4(gv); PINV4(uv);
        if (!OFE) { WO_PIECE(11); WO_PIECE(10); WO_PIECE(9); WO_PIECE(8); WO_PIECE(7); WO_PIECE(6); WO_PIECE(5); WO_PIECE(4); }
      }
      BAR();
      if constexpr (it == 1) { WO_PIECE(3); WO_PIECE(2); WO_PIECE(1); WO_PIECE(0); }
      wait_vm<it ? 4 : OFE ? 0 : 8>();
      asm volatile("s_barrier" ::: "memory");
      if (tid == 0) st8(T, rs, h * SO, F_O + (it * 8 + p) * 32 * SO);
      if constexpr (it == 0) {
        if (OFE) { WO_PIECE(11); WO_PIECE(10); WO_PIECE(9); WO_PIECE(8); WO_PIECE(7); WO_PIECE(6); WO_PIECE(5); WO_PIECE(4); }
        WO_PIECE(3); WO_PIECE(2); WO_PIECE(1); WO_PIECE(0);
      }
    }
    IDS();
    ARGP(pwr, 4); ARGP(pwd, 7); ARGP(pout, 8); ARGP(px, 0);
    const int kh = w >> 2, rt2 = rb * 4 + (w & 3);
    const unsigned R = rt2 * 32 + kh * 16 + (l & 15);
    const unsigned to = (rt2 * 64 + ct * 4 + kh) * 1024 + l * 16;
    const unsigned rmo = ct * 64 + ((g & 1) ? 12 + 4 * g : 4 * g);
    f32x2 wv[8];
    {
      u32x4 wa[2], wrp[2], res[2];
#pragma unroll
      for (int j = 0; j < 2; ++j) {
        wrp[j] = *(const GAS u32x4*)(pwr + rmo + 32 * j);
        if constexpr (it == 0) wa[j] = *(const GAS u32x4*)(pwr + l * 8 + j * 512);
        if constexpr (it == 0) res[j] = *(const GAS u32x4*)(px + R * 1024 + rmo + 32 * j);
        else res[j] = ld16(rs, OFF_XNEW + (2 * ct + j) * 256 + g * 16, 0);
      }
      SB();
      unsigned bt[4];
#pragma unroll
      for (int n = 0; n < 4; ++n) {
        bt[n] = lbase + 32768 + 65536 + kh * 4096 + (4 * g + ((l & 15) >> 2)) * 128 + (l & 3) * 8 + ((n ^ (2 * (g & 1) + ((l & 15) >> 3))) << 5);
        asm volatile("" : "+v"(bt[n]));
      }
      if (w == 0) poll_flags(rs, (l & 31) * SO, F_O + (it * 8 + (rb >> 1)) * 32 * SO, T);
      asm volatile("s_barrier" ::: "memory");
      auto prep = [&]() __attribute__((always_inline)) {
        if constexpr (it == 0) {
          f32x2 t[8];
          unpack8v(wa[0], t); unpack8v(wa[1], t + 4);
          const f32x2 s = ((t[0] + t[1]) + (t[2] + t[3])) + ((t[4] + t[5]) + (t[6] + t[7]));
          wsum = sum64(hadd(s));
        }
        unpack8v(trade16(wrp[0]), wv); unpack8v(trade16(wrp[1]), wv + 4);
        if constexpr (it == 0) { res[0] = trade16(res[0]); res[1] = trade16(res[1]); }
        else { res[0] = R == sel0 ? res[0] : x2p[0]; res[1] = R == sel0 ? res[1] : x2p[1]; }
        unpack8v(res[0], v + 0); unpack8v(res[1], v + 4);
        asm volatile("" : "+v"(v[0]), "+v"(v[1]), "+v"(v[2]), "+v"(v[3]), "+v"(v[4]), "+v"(v[5]), "+v"(v[6]), "+v"(v[7]));
        asm volatile("" : "+v"(wsum), "+v"(wv[0]), "+v"(wv[1]), "+v"(wv[2]), "+v"(wv[3]), "+v"(wv[4]), "+v"(wv[5]), "+v"(wv[6]), "+v"(wv[7]));
      };
      if constexpr (it == 1 && WON) {
        const unsigned abase = OFF_O + (4u << 20) + (rt2 * 64 + kh) * 1024;
        u32x4 af[W2B], bfr[W2L + 1][4];
        unsigned b2[2] = {lbase + 32768 + l * 16, lbase + 98304 + l * 16};
        asm volatile("" : "+v"(b2[0]), "+v"(b2[1]));
#define O_LOAD(s_) af[(s_) % W2B] = ld16(rs, l * 16 + (~(s_) & 1) * 2048, abase + ((31 - (s_)) >> 1) * 4096)
#define WO_RD(s_, n) bfr[(s_) % (W2L + 1)][n] = *(const LDS_AS u32x4*)(size_t)(b2[(31 - (s_)) >> 4] + ((31 - (s_)) & 15) * 4096 + (n) * 1024)
#pragma unroll
        for (int s = 0; s < W2R; ++s) O_LOAD(s);
        SB();
        prep();
        SB();
        f32x4 ac[4];
#pragma unroll
        for (int n = 0; n < 4; ++n) ac[n] = f32x4{v[2 * n][0], v[2 * n][1], v[2 * n + 1][0], v[2 * n + 1][1]};
#pragma unroll
        for (int n = 0; n < 4 * W2L; ++n) WO_RD(n >> 2, n & 3);
#pragma unroll
        for (int s = 0; s < 32; ++s) {
          if (s == W2P && w >= 4) __builtin_amdgcn_s_setprio(1);
          if (s == 24 - W2L) asm volatile("s_barrier" ::: "memory");
          if (s < W2B - W2R) O_LOAD(W2R + 2 * s);
          SB();
#pragma unroll
          for (int n = 0; n < 4; ++n) {
            ac[n] = mfma16(bfr[s % (W2L + 1)][n], af[s % W2B], ac[n]);
            if (s + W2L < 32) WO_RD(s + W2L, n);
            SB();
          }
          if (s < W2B - W2R) O_LOAD(W2R + 2 * s + 1);
          else if (s + W2B < 32) O_LOAD(s + W2B);
          SB();
        }
        __builtin_amdgcn_s_setprio(0);
#undef WO_RD
#undef O_LOAD
#pragma unroll
        for (int n = 0; n < 4; ++n) { v[2 * n] = f32x2{ac[n][0], ac[n][1]}; v[2 * n + 1] = f32x2{ac[n][2], ac[n][3]}; }
      } else {
      const unsigned abase = OFF_O + it * (4u << 20) + (rt2 * 64 + 32 * kh) * 1024;
      const unsigned av0 = l * 16 + kh * 1024, av1 = l * 16 + (kh ^ 1) * 1024;
      u32x4 af[WB][2];
#define O_LOAD(s_) { af[(s_) % WB][0] = ld16(rs, av0 + (~(s_) & 1) * 2048, abase + ((15 - (s_)) >> 1) * 4096); af[(s_) % WB][1] = ld16(rs, av1 + (~(s_) & 1) * 2048, abase + ((15 - (s_)) >> 1) * 4096); }
#pragma unroll
      for (int s = 0; s < WR; ++s) O_LOAD(s);
      SB();
      prep();
      SB();
      f32x4 ac[2][4];
#pragma unroll
      for (int a = 0; a < 2; ++a)
#pragma unroll
        for (int n = 0; n < 4; ++n) ac[a][n] = f32x4{0.f, 0.f, 0.f, 0.f};
      u32x4 bfr[WLA + 1][4];
#define WO_RD(buf, s, n)                                                                             \
  {                                                                                                  \
    const unsigned o_ = bt[n] + ((15 - (s)) & 7) * 8192;     \
    frag_t f_;                                                                                       \
    f_.hf.a = trabs(o_); f_.hf.b = trabs(o_ + 2048);                                                 \
    bfr[buf][n] = f_.v;                                                                              \
  }
#pragma unroll
      for (int n = 0; n < 4 * WLA; ++n) WO_RD(n >> 2, n >> 2, n & 3);
#pragma unroll
      for (int s = 0; s < 16; ++s) {
        const int cur = s % (WLA + 1);
        if (WPR && s == WPR && w >= 4) __builtin_amdgcn_s_setprio(1);
        asm volatile("" : "+{v[176:179]}"(ac[0][0]), "+{v[180:183]}"(ac[0][1]), "+{v[184:187]}"(ac[0][2]), "+{v[188:191]}"(ac[0][3]),
                          "+{v[192:195]}"(ac[1][0]), "+{v[196:199]}"(ac[1][1]), "+{v[200:203]}"(ac[1][2]), "+{v[204:207]}"(ac[1][3]));
        if (s == (it == 0 ? 4 : 12) - WLA) asm volatile("s_barrier" ::: "memory");
        if (s == 8 - WLA) {
#pragma unroll
          for (int n = 0; n < 4; ++n) { bt[n] -= 65536; asm volatile("" : "+v"(bt[n])); }
        }
        if (it == 0 && s == WIS) {
#pragma unroll
          for (int i = 0; i < 3; ++i) {
            if (PO()) st16(keep[3 + i], rs, l * 16 + i * 1024, OFF_WI + (h * 8 + ((ROT0 + 6) & 7)) * STAGE + w * 3072);
            else st16(keep[i], rs, l * 16 + i * 1024, OFF_WI + (h * 8 + ((ROT0 + 5) & 7)) * STAGE + w * 3072);
          }
        }
        if (it == 0 && s == WPS) {
          unsigned pz1 = POISON;
          asm volatile("" : "+v"(pz1));
          const u32x4 pz = {pz1, pz1, pz1, pz1};
          st16(pz, rs, OFF_PART + (tid >> 8) * (1u << 20) + bid * 4096 + (tid & 255) * 16, 0);
          if (w < 2) st16(pz, rs, OFF_STAT + 524288 + (rb * 16 + ct) * 2048 + tid * 16, 0);
          if (w == 4 && l < 40) st16(pz, rs, l < 16 ? OFF_CA + 65536 + rb * 4096 + ct * 256 + l * 16 : OFF_FX + (h * 8 + p) * 512 + (l - 16) * 16, 0);
          if (w == 6 && l < (ct < (int)RKEY ? 3 : 2)) st4(pz1, rs, l < 2 ? OFF_XNEW + (bid >> 3) * 256 + (bid & 7) * 8 + l * 4 : KEY_AT(1, ct) + rb * SKEY, 0);
        }
        if (s < WB - WR) O_LOAD(WR + 2 * s < 16 ? WR + 2 * s : 0);
        SB();
#pragma unroll
        for (int n = 0; n < 4; ++n) {
          ac[0][n] = mfma16(bfr[cur][n], af[s % WB][0], ac[0][n]);
          ac[1][n] = mfma16(bfr[cur][n], af[s % WB][1], ac[1][n]);
          if (s + WLA < 16) WO_RD((s + WLA) % (WLA + 1), s + WLA, n);
          SB();
        }
        if (s < WB - WR) { O_LOAD(WR + 2 * s + 1); }
        else if (s + WB < 16) { O_LOAD(s + WB); }
        SB();
      }
      if (WPR) __builtin_amdgcn_s_setprio(0);
#undef WO_RD
#undef O_LOAD
      if constexpr (it == 0 && WON) {
        const int ks = 2 * rb + kh;
        const unsigned o_ = lbase + 32768 + (ks & 15) * 8192 + (ks >> 4) * 4096 + (4 * g + ((l & 15) >> 2)) * 128 + (l & 3) * 8 + (((w & 3) ^ (2 * (g & 1) + ((l & 15) >> 3))) << 5);
        frag_t f_;
        f_.hf.a = trabs(o_); f_.hf.b = trabs(o_ + 2048);
        wof = f_.v;
        asm volatile("" : "+v"(wof));
      }
#define XCH_AT(v) ((v) == 7 ? (rb == 7 ? 139264 : 155648) : (v) * 4096)
#pragma unroll
      for (int n = 0; n < 4; ++n) *(f32x4*)(smem + XCH_AT(w) + n * 1024 + l * 16) = ac[1][n];
#pragma unroll
      for (int n = 0; n < 4; ++n) { v[2 * n] += f32x2{ac[0][n][0], ac[0][n][1]}; v[2 * n + 1] += f32x2{ac[0][n][2], ac[0][n][3]}; }
      BAR();
#pragma unroll
      for (int n = 0; n < 4; ++n) {
        const f32x4 o = *(const f32x4*)(smem + XCH_AT(w ^ 4) + n * 1024 + l * 16);
        v[2 * n] += f32x2{o[0], o[1]}; v[2 * n + 1] += f32x2{o[2], o[3]};
      }
      }
    }
    unsigned crow = 0;
    {
      if (it == 0) { st16(pack8v(v), rs, to, OFF_XN + (4u << 20)); st16(pack8v(v + 4), rs, to + 2048, OFF_XN + (4u << 20)); }
      SB();
      const f32x2 s = ((v[0] + v[1]) + (v[2] + v[3])) + ((v[4] + v[5]) + (v[6] + v[7]));
      f32x2 dp = v[0] * wv[0], q = v[0] * v[0];
#pragma unroll
      for (int j = 1; j < 8; ++j) { dp = __builtin_elementwise_fma(v[j], wv[j], dp); q = __builtin_elementwise_fma(v[j], v[j], q); }
      float mt = hadd(s), qq = hadd(q), dot = hadd(dp);
      add_g3(mt, qq, dot);
      const unsigned so = OFF_STAT + it * 524288 + rb * 32768, lrow = R & 127;
      if (it == 0) wait_vm<2>();
      if (g == 0) st16(unpoison4(__builtin_bit_cast(u32x4, f32x4{mt, qq, dot, 0.f})), rs, ct * 2048 + lrow * 16, so);
      SB();
      if (it == 0) wait_vm<1>();
      if constexpr (it == 0) {
        volatile LDS_AS u32x2* in = (volatile LDS_AS u32x2*)(smem + L_XC);
        if (l == 0) in[w] = T;
        const u32x2 t = in[l & 7];
        if (__all(t[0] == T[0] && t[1] == T[1]) && l < (int)RX1) st8(T, rs, (l * 32 + (rb & 1) * 16 + ct) * SX1, F_X1 + (rb >> 1) * RX1 * 32 * SX1);
      }
      if (it == 0) {
        if (ffn_on) { WD_DMA(); }
#pragma unroll
        for (int j = 0; j < 9; ++j) *(u32x4*)(smem + L_RING + (j / 3) * STAGE + (w * 3 + j % 3) * 1024 + l * 16) = keep[j];
      }
      if constexpr (it == 0) {
        if (w == 0) poll_flags(rs, ((l & 2) * 8 + ((ROT0 + 5) & 7) * 2 + (l & 1)) * SX1, F_X1 + (p * RX1 + (xcd >> 1) % RX1) * 32 * SX1, T);
        if (tid == 192) *(LDS_AS u32x4*)(smem + L_QC) = u32x4{0u, 0u, 0u, 0u};
        BAR();
        PRE_LOAD(1, ROT0 + 5, 0, NPRE);
      } else {
        __builtin_amdgcn_s_sleep(SDIR);
        u32x4 sp[4];
#pragma unroll
        for (int i = 0; i < 4; ++i) sp[i] = ld16(rs, (4 * g + i) * 2048 + lrow * 16, so);
        const RowFin r = fin_key(rs, T, g * 8192 + lrow * 16, so, smem + L_XC, l, w, R, wsum, sp);
        BAR();
        crow = fin_cand(rs, smem + L_XC, 1, g, rb, ct, r, v);
        fin_x2(r, v);
      }
    }
    if (it == 1) {
      u32x4 op[2];
      {
        char* tp = smem + L_OUT + w * 2048;
        const unsigned c0 = (g & 1) * 2 + (g >> 1), r = l & 15;
        *(u32x4*)(tp + r * 128 + ((c0 ^ (r & 7)) << 4)) = trade16(pack8v(v));
        *(u32x4*)(tp + r * 128 + (((c0 + 4) ^ (r & 7)) << 4)) = trade16(pack8v(v + 4));
#pragma unroll
        for (int j = 0; j < 2; ++j) op[j] = *(const u32x4*)(tp + j * 1024 + (l >> 3) * 128 + (((l ^ (l >> 3)) & 7) << 4));
      }
      const unsigned Ro = R - (l & 15) + (l >> 3), oo = (Ro * 1024 + ct * 64) * 2 + (l & 7) * 16;
#define OUT_STORE(c0, c1)                                                                           \
  {                                                                                                 \
    if (c0) __builtin_amdgcn_raw_buffer_store_b128(op[0], rout, oo, 0, OAUX);                        \
    if (c1) __builtin_amdgcn_raw_buffer_store_b128(op[1], rout, oo + 16384, 0, OAUX);                \
  }
      if (OUTE) OUT_STORE(Ro != crow, Ro + 8 != crow);
      if (w == 0) {
        __builtin_amdgcn_s_sleep(GSL);
        u32x4 xr[4];
        unsigned cur = 0, best;
        asm volatile("" : "=v"(xr[0]), "=v"(xr[1]));
        for (unsigned spins = 0;;) {
          const unsigned k_ = key_load(rs, l, 1, KREP());
          const bool all = __all(k_ != POISON);
          best = __builtin_amdgcn_readfirstlane(umax16(k_ == POISON ? 0u : k_));
          if (best != cur && ffn_on) {
            cur = best;
            ld16h<0>(xr[0], rs, (l >> 3) * 256 + (l & 7) * 16, cand_off(cur, 1)); ld16h<2048>(xr[1], rs, (l >> 3) * 256 + (l & 7) * 16, cand_off(cur, 1));
          }
          if (all || spin_fail(spins, rs, T)) break;
        }
        if (l == 0) *(LDS_AS unsigned*)(smem + L_SKEY + 32) = best;
        asm volatile("s_waitcnt vmcnt(0)" : "+v"(xr[0]), "+v"(xr[1])::"memory");
        if (ffn_on) {
          word_wait<2>(rs, T, (l >> 3) * 256 + (l & 7) * 16, cand_off(best, 1), xr);
          *(LDS_AS u32x4*)(smem + L_ROW + l * 16) = xr[0]; *(LDS_AS u32x4*)(smem + L_ROW + 1024 + l * 16) = xr[1];
        }
      }
      BAR();
      const unsigned key = __builtin_amdgcn_readfirstlane(*(LDS_AS unsigned*)(smem + L_SKEY + 32)), krow = key_row(key);
      if (ffn_on) {
        u32x4 xv[4];
#pragma unroll
        for (int i = 0; i < 4; ++i) xv[i] = *(const LDS_AS u32x4*)(smem + L_ROW + w * 256 + i * 64 + g * 16);
        f32x4 ga, ua;
        ffn_gu(gv, uv, xv, ga, ua);
        ffn_gu_store(smem, l, w, ga, ua);
        BAR();
        ffn_d_store(rs, l, w, 1, slice, ffn_d_all(dfr, ffn_d_h(ffn_d_read(smem, l))));
        SB();
        if (OSL) __builtin_amdgcn_s_sleep(OSL);
      }
      if (OUTE) OUT_STORE(Ro == crow && Ro != krow, Ro + 8 == crow && Ro + 8 != krow)
      else OUT_STORE(Ro != krow, Ro + 8 != krow);
      if (!ffn_on) {
        const unsigned G = xcd * 4 + bi - 28;
        if (G == 0 && tid == 0) st8(T, rs, F_EP, 0);
        u32x4 pv[4];
        u32x2 cxn, cx1;
        __builtin_amdgcn_s_sleep(RSL);
        red_load(rs, tid, 1, G, key, pv, cxn, cx1);
        red_sum(rs, smem, T, tid, w, 1, G, key, pv, cxn, cx1);
        BAR();
        if (w == 0) {
          const u32x2 ov = red_final(smem, l, key, cxn, cx1);
          if (l < 8) __builtin_amdgcn_raw_buffer_store_b64(ov, rout, (krow * 1024 + G * 32 + l * 4) * 2, 0, 0);
        }
      }
#undef OUT_STORE
    }
  };
  layer(std::integral_constant<int, 0>{});
  layer(std::integral_constant<int, 1>{});
}

extern "C" void kernel_launch(void* const* d_in, const int* in_sizes, int n_in, void* d_out, int out_size,
                              void* d_ws, size_t ws_size, hipStream_t stream) {
  (void)hipFuncSetAttribute((const void*)mega_kernel, hipFuncAttributeMaxDynamicSharedMemorySize, LDS_BYTES);
  mega_kernel<<<256, 512, LDS_BYTES, stream>>>((const bf16*)d_in[0], (char*)d_ws, (const bf16*)d_in[1], (const bf16*)d_in[2], (const bf16*)d_in[4],
                                               (const bf16*)d_in[5], (const bf16*)d_in[6], (const bf16*)d_in[7], (bf16*)d_out);
}
```

```cpp
#include <hip/hip_runtime.h>
#include <stdint.h>
#include <utility>

typedef __bf16 bf16;
typedef __bf16 bf16x8 __attribute__((ext_vector_type(8)));
typedef __bf16 bf16x2 __attribute__((ext_vector_type(2)));
typedef float f32x4 __attribute__((ext_vector_type(4)));
typedef float f32x2 __attribute__((ext_vector_type(2)));
typedef short s16x4 __attribute__((ext_vector_type(4)));
typedef unsigned int u32x4 __attribute__((ext_vector_type(4)));
typedef unsigned int u32x2 __attribute__((ext_vector_type(2)));
#define LDS_AS __attribute__((address_space(3)))
#define GAS __attribute__((address_space(1)))
typedef __amdgpu_buffer_rsrc_t rsrc_t;

constexpr int F_ = 3584;
constexpr int SC1 = 16;

constexpr int RX = 4;
constexpr unsigned F_EP = 0, F_TMO = 128;
constexpr unsigned F_XN = 4096;
#define FLG 256, 1024, 8, 64, 4, 256, 4
constexpr unsigned FLV[7] = {FLG};
constexpr unsigned SO = FLV[0], SKV = FLV[1], SKW = FLV[2], SX1 = FLV[3], RX1 = FLV[4], SKEY = FLV[5], RKEY = FLV[6];
constexpr unsigned F_O = 8u << 20;
constexpr unsigned F_KV = 11u << 20;
constexpr unsigned F_WOI = F_O + 262144;
constexpr unsigned F_X1 = 9u << 20;
constexpr unsigned OFF_KEY = 10u << 20;
#define KREP() (RKEY == 16 ? bid & 15 : (bid & 7) * RKEY / 8)
#define KEY_AT(L, rep) (OFF_KEY + ((L) * RKEY + (rep)) * (SKEY < 8 ? 128 : 16 * SKEY))
constexpr unsigned OFF_AB = 2u << 20;
constexpr unsigned OFF_XNEW = 262144;
constexpr unsigned OFF_CS = OFF_AB + 65536;
constexpr unsigned OFF_FX = 393216;
constexpr unsigned OFF_CA = 524288;
constexpr unsigned OFF_STAT = 1u << 20;
constexpr unsigned OFF_PART = 3u << 20;
constexpr unsigned OFF_XN = 8u << 20;
constexpr unsigned OFF_O = 16u << 20;
constexpr unsigned OFF_KV = 32u << 20;
constexpr unsigned OFF_WI = 52u << 20;
constexpr unsigned OFF_WOI = 58u << 20;
constexpr unsigned WS_BYTES = 60u << 20;
constexpr unsigned POISON = 0xFFFFFFFFu;

constexpr int L_ABW = 16640, L_CSP = 17408, L_CSL = 31872, L_FXL = 32256, L_K = 0, L_V = 32768, L_RING = 65536, L_DN = 32768, L_RDY = 31744, L_QC = 31808, LDS_BYTES = 163840;
constexpr int STAGE = 24576;
#define SLOT(t) ((t) & 3)

template <typename F, int... I> __device__ __forceinline__ void static_for_impl(F&& f, std::integer_sequence<int, I...>) {
  (f(std::integral_constant<int, I>{}), ...);
}
template <int N, typename F> __device__ __forceinline__ void static_for(F&& f) { static_for_impl(f, std::make_integer_sequence<int, N>{}); }
__device__ __forceinline__ unsigned pack2(float a, float b) {
  f32x2 v = {a, b};
  return __builtin_bit_cast(unsigned, __builtin_convertvector(v, bf16x2));
}
__device__ __forceinline__ float asf(unsigned u) { return __builtin_bit_cast(float, u); }
__device__ __forceinline__ float lo16(unsigned u) { return __builtin_bit_cast(float, u << 16); }
__device__ __forceinline__ float hi16(unsigned u) { return __builtin_bit_cast(float, u & 0xffff0000u); }
__device__ __forceinline__ u32x4 pack8(const f32x4 a, const f32x4 b) {
  u32x4 o;
  o[0] = pack2(a[0], a[1]); o[1] = pack2(a[2], a[3]); o[2] = pack2(b[0], b[1]); o[3] = pack2(b[2], b[3]);
  return o;
}
__device__ __forceinline__ f32x4 mfma16(u32x4 a, u32x4 b, f32x4 c) {
  return __builtin_amdgcn_mfma_f32_16x16x32_bf16(__builtin_bit_cast(bf16x8, a), __builtin_bit_cast(bf16x8, b), c, 0, 0, 0);
}
__device__ __forceinline__ f32x4 mfma16k16(s16x4 a, s16x4 b, f32x4 c) { return __builtin_amdgcn_mfma_f32_16x16x16bf16_1k(a, b, c, 0, 0, 0); }
template <int CTRL> __device__ __forceinline__ float dppf(float v) {
  return __builtin_bit_cast(float, __builtin_amdgcn_update_dpp(0, __builtin_bit_cast(int, v), CTRL, 0xf, 0xf, false));
}
template <int CTRL> __device__ __forceinline__ unsigned dppu(unsigned v) {
  return (unsigned)__builtin_amdgcn_update_dpp(0, (int)v, CTRL, 0xf, 0xf, false);
}
__device__ __forceinline__ float add_x16(float v) {
  auto r = __builtin_amdgcn_permlane16_swap(__builtin_bit_cast(unsigned, v), __builtin_bit_cast(unsigned, v), false, false);
  return __builtin_bit_cast(float, (unsigned)r[0]) + __builtin_bit_cast(float, (unsigned)r[1]);
}
__device__ __forceinline__ float add_x32(float v) {
  auto r = __builtin_amdgcn_permlane32_swap(__builtin_bit_cast(unsigned, v), __builtin_bit_cast(unsigned, v), false, false);
  return __builtin_bit_cast(float, (unsigned)r[0]) + __builtin_bit_cast(float, (unsigned)r[1]);
}
__device__ __forceinline__ float max_x16(float v) {
  auto r = __builtin_amdgcn_permlane16_swap(__builtin_bit_cast(unsigned, v), __builtin_bit_cast(unsigned, v), false, false);
  return fmaxf(__builtin_bit_cast(float, (unsigned)r[0]), __builtin_bit_cast(float, (unsigned)r[1]));
}
__device__ __forceinline__ float max_x32(float v) {
  auto r = __builtin_amdgcn_permlane32_swap(__builtin_bit_cast(unsigned, v), __builtin_bit_cast(unsigned, v), false, false);
  return fmaxf(__builtin_bit_cast(float, (unsigned)r[0]), __builtin_bit_cast(float, (unsigned)r[1]));
}
__device__ __forceinline__ float sum_hi8(float v) {
  v += dppf<0x128>(v);
  v = add_x16(v);
  return add_x32(v);
}
__device__ __forceinline__ float sum64(float v) {
  v += dppf<0xB1>(v);
  v += dppf<0x4E>(v);
  v += dppf<0x124>(v);
  v += dppf<0x128>(v);
  v = add_x16(v);
  return add_x32(v);
}

#define BAR() asm volatile("s_waitcnt lgkmcnt(0)\n\ts_barrier" ::: "memory")
#define DRAIN() asm volatile("s_waitcnt vmcnt(0)" ::: "memory")
#define SB() __builtin_amdgcn_sched_barrier(0)
template <int N> __device__ __forceinline__ void wait_vm() { asm volatile("s_waitcnt vmcnt(%0)" ::"n"(N) : "memory"); }

__device__ __forceinline__ void dma16(rsrc_t rs, unsigned voff, unsigned soff, unsigned lds) {
  asm volatile("s_mov_b32 m0, %0\n\ts_nop 0\n\tbuffer_load_dwordx4 %1, %2, %3 offen lds" ::"s"(lds), "v"(voff), "s"(rs), "s"(soff) : "memory");
}
__device__ __forceinline__ void dma16n(rsrc_t rs, unsigned voff, unsigned soff, unsigned lds) {
  asm volatile("s_mov_b32 m0, %0\n\ts_nop 0\n\tbuffer_load_dwordx4 %1, %2, %3 offen nt lds" ::"s"(lds), "v"(voff), "s"(rs), "s"(soff) : "memory");
}
template <int OFF> __device__ __forceinline__ void dma16s(rsrc_t rs, unsigned voff, unsigned soff, unsigned lds) {
  asm volatile("s_mov_b32 m0, %0\n\ts_nop 0\n\tbuffer_load_dwordx4 %1, %2, %3 offen offset:%4 sc1 lds" ::"s"(lds), "v"(voff), "s"(rs), "s"(soff), "n"(OFF) : "memory");
}
__device__ __forceinline__ float rbf1(float x) { return __builtin_bit_cast(float, pack2(0.f, x)); }
__device__ __forceinline__ u32x4 ld16(rsrc_t rs, unsigned voff, unsigned soff) { return __builtin_amdgcn_raw_buffer_load_b128(rs, voff, soff, SC1); }
template <int OFF> __device__ __forceinline__ void ld16h(u32x4& d, rsrc_t rs, unsigned voff, unsigned soff) {
  asm volatile("buffer_load_dwordx4 %0, %1, %2, %3 offen offset:%4 sc1" : "=v"(d) : "v"(voff), "s"(rs), "s"(soff), "n"(OFF) : "memory");
}
template <int I, int OFF> __device__ __forceinline__ void ld16w(u32x4& d, rsrc_t rs, unsigned voff, unsigned soff) {
  if constexpr (I == 0) asm volatile("buffer_load_dwordx4 %0, %1, %2, %3 offen offset:%4 sc1" : "={v[128:131]}"(d) : "v"(voff), "s"(rs), "s"(soff), "n"(OFF) : "memory");
  else if constexpr (I == 1) asm volatile("buffer_load_dwordx4 %0, %1, %2, %3 offen offset:%4 sc1" : "={v[132:135]}"(d) : "v"(voff), "s"(rs), "s"(soff), "n"(OFF) : "memory");
  else if constexpr (I == 2) asm volatile("buffer_load_dwordx4 %0, %1, %2, %3 offen offset:%4 sc1" : "={v[136:139]}"(d) : "v"(voff), "s"(rs), "s"(soff), "n"(OFF) : "memory");
  else if constexpr (I == 3) asm volatile("buffer_load_dwordx4 %0, %1, %2, %3 offen offset:%4 sc1" : "={v[140:143]}"(d) : "v"(voff), "s"(rs), "s"(soff), "n"(OFF) : "memory");
  else if constexpr (I == 4) asm volatile("buffer_load_dwordx4 %0, %1, %2, %3 offen offset:%4 sc1" : "={v[144:147]}"(d) : "v"(voff), "s"(rs), "s"(soff), "n"(OFF) : "memory");
  else if constexpr (I == 5) asm volatile("buffer_load_dwordx4 %0, %1, %2, %3 offen offset:%4 sc1" : "={v[148:151]}"(d) : "v"(voff), "s"(rs), "s"(soff), "n"(OFF) : "memory");
  else if constexpr (I == 6) asm volatile("buffer_load_dwordx4 %0, %1, %2, %3 offen offset:%4 sc1" : "={v[152:155]}"(d) : "v"(voff), "s"(rs), "s"(soff), "n"(OFF) : "memory");
  else if constexpr (I == 7) asm volatile("buffer_load_dwordx4 %0, %1, %2, %3 offen offset:%4 sc1" : "={v[156:159]}"(d) : "v"(voff), "s"(rs), "s"(soff), "n"(OFF) : "memory");
  else if constexpr (I == 8) asm volatile("buffer_load_dwordx4 %0, %1, %2, %3 offen offset:%4 sc1" : "={v[160:163]}"(d) : "v"(voff), "s"(rs), "s"(soff), "n"(OFF) : "memory");
  else if constexpr (I == 9) asm volatile("buffer_load_dwordx4 %0, %1, %2, %3 offen offset:%4 sc1" : "={v[164:167]}"(d) : "v"(voff), "s"(rs), "s"(soff), "n"(OFF) : "memory");
  else if constexpr (I == 10) asm volatile("buffer_load_dwordx4 %0, %1, %2, %3 offen offset:%4 sc1" : "={v[168:171]}"(d) : "v"(voff), "s"(rs), "s"(soff), "n"(OFF) : "memory");
  else if constexpr (I == 11) asm volatile("buffer_load_dwordx4 %0, %1, %2, %3 offen offset:%4 sc1" : "={v[172:175]}"(d) : "v"(voff), "s"(rs), "s"(soff), "n"(OFF) : "memory");
}
template <int K> __device__ __forceinline__ void pin_w(u32x4& a, u32x4& b) {
  if constexpr (K == 0) asm volatile("" : "+{v[128:131]}"(a), "+{v[132:135]}"(b)::"memory");
  else if constexpr (K == 1) asm volatile("" : "+{v[136:139]}"(a), "+{v[140:143]}"(b)::"memory");
  else if constexpr (K == 2) asm volatile("" : "+{v[144:147]}"(a), "+{v[148:151]}"(b)::"memory");
  else if constexpr (K == 3) asm volatile("" : "+{v[152:155]}"(a), "+{v[156:159]}"(b)::"memory");
  else if constexpr (K == 4) asm volatile("" : "+{v[160:163]}"(a), "+{v[164:167]}"(b)::"memory");
  else if constexpr (K == 5) asm volatile("" : "+{v[168:171]}"(a), "+{v[172:175]}"(b)::"memory");
}
__device__ __forceinline__ void ld8h(u32x2& d, rsrc_t rs, unsigned voff, unsigned soff) {
  asm volatile("buffer_load_dwordx2 %0, %1, %2, %3 offen sc1" : "=v"(d) : "v"(voff), "s"(rs), "s"(soff) : "memory");
}
template <int OFF> __device__ __forceinline__ void ld4h(unsigned& d, rsrc_t rs, unsigned voff, unsigned soff) {
  asm volatile("buffer_load_dword %0, %1, %2, %3 offen offset:%4 sc1" : "=v"(d) : "v"(voff), "s"(rs), "s"(soff), "n"(OFF) : "memory");
}
__device__ __forceinline__ u32x2 ld8(rsrc_t rs, unsigned voff, unsigned soff) { return __builtin_amdgcn_raw_buffer_load_b64(rs, voff, soff, SC1); }
__device__ __forceinline__ void st16(u32x4 v, rsrc_t rs, unsigned voff, unsigned soff) { __builtin_amdgcn_raw_buffer_store_b128(v, rs, voff + soff, 0, SC1); }
__device__ __forceinline__ void st8(u32x2 v, rsrc_t rs, unsigned voff, unsigned soff) { __builtin_amdgcn_raw_buffer_store_b64(v, rs, voff, soff, SC1); }

__device__ __forceinline__ unsigned ld4(rsrc_t rs, unsigned voff, unsigned soff) { return __builtin_amdgcn_raw_buffer_load_b32(rs, voff, soff, SC1); }
__device__ __forceinline__ void st4(unsigned v, rsrc_t rs, unsigned voff, unsigned soff) { __builtin_amdgcn_raw_buffer_store_b32(v, rs, voff, soff, SC1); }

constexpr unsigned kSpinCap = 1u << 16;
__device__ __forceinline__ bool spin_fail(unsigned& spins, rsrc_t rs, u32x2 T) {
  asm volatile("" ::: "memory");
  ++spins;
  if (spins > kSpinCap) { st8(T, rs, 0, F_TMO); return true; }
  if ((spins & 255u) == 0u) {
    const u32x2 v = ld8(rs, 0, F_TMO);
    if (v[0] == T[0] && v[1] == T[1]) return true;
  }
  __builtin_amdgcn_s_sleep(3);
  return false;
}
__device__ __forceinline__ void poll_flags2(rsrc_t rs, unsigned voff, u32x2 T) {
  for (unsigned spins = 0;;) {
    const u32x4 v = ld16(rs, voff, 0);
    if (__all(v[0] == T[0] && v[1] == T[1] && v[2] == T[0] && v[3] == T[1])) break;
    if (spin_fail(spins, rs, T)) break;
  }
  asm volatile("" ::: "memory");
  __builtin_amdgcn_fence(__ATOMIC_ACQUIRE, "wavefront");
}
__device__ __forceinline__ void tag_wait(rsrc_t rs, u32x2 T, unsigned voff, unsigned soff, u32x4& v) {
  if (__builtin_expect(!__all(v[2] == T[0] && v[3] == T[1]), 0)) {
    unsigned spins = 0;
    do {
      if (spin_fail(spins, rs, T)) break;
      v = ld16(rs, voff, soff);
    } while (!__all(v[2] == T[0] && v[3] == T[1]));
    asm volatile("s_waitcnt vmcnt(0)" : "+v"(v)::"memory");
  }
}
template <int N> __device__ __forceinline__ void word_wait(rsrc_t rs, u32x2 T, unsigned voff, unsigned soff, u32x4 (&v)[4]) {
#define WW_OK() __all(max(max(max(v[0][0], v[0][1]), max(v[0][2], v[0][3])), max(max(v[N - 1][0], v[N - 1][1]), max(v[N - 1][2], v[N - 1][3]))) != POISON)
  if (__builtin_expect(!WW_OK(), 0)) {
    unsigned spins = 0;
    do {
      if (spin_fail(spins, rs, T)) break;
#pragma unroll
      for (int i = 0; i < N; ++i) v[i] = ld16(rs, voff + i * 2048, soff);
    } while (!WW_OK());
    asm volatile("s_waitcnt vmcnt(0)" : "+v"(v[0]), "+v"(v[N - 1])::"memory");
  }
#undef WW_OK
}
__device__ __forceinline__ void poll_flags(rsrc_t rs, unsigned voff, unsigned soff, u32x2 T) {
  for (unsigned spins = 0;;) {
    const u32x2 v = ld8(rs, voff, soff);
    if (__all(v[0] == T[0] && v[1] == T[1])) break;
    if (spin_fail(spins, rs, T)) break;
  }
  asm volatile("" ::: "memory");
  __builtin_amdgcn_fence(__ATOMIC_ACQUIRE, "wavefront");
}

__device__ __forceinline__ void unpack8v(const u32x4 u, f32x2* v) {
#pragma unroll
  for (int j = 0; j < 4; ++j) v[j] = f32x2{lo16(u[j]), hi16(u[j])};
}
__device__ __forceinline__ u32x4 pack8v(const f32x2* v) {
  u32x4 o;
#pragma unroll
  for (int j = 0; j < 4; ++j) o[j] = pack2(v[j][0], v[j][1]);
  return o;
}
__device__ __forceinline__ float ln_rd(float var) { return __builtin_amdgcn_rcpf(rbf1(__builtin_amdgcn_sqrtf(rbf1(rbf1(var) + 1e-5f)))); }
__device__ __forceinline__ float hadd(f32x2 v) {
  float a = v[0];
  asm volatile("" : "+v"(a));
  return a + v[1];
}
__device__ __forceinline__ float add_g(float v) { return add_x32(add_x16(v)); }
__device__ __forceinline__ void add_g3(float& x, float& y, float& z) {
  auto a = __builtin_amdgcn_permlane16_swap(__builtin_bit_cast(unsigned, x), __builtin_bit_cast(unsigned, y), false, false);
  auto b = __builtin_amdgcn_permlane16_swap(__builtin_bit_cast(unsigned, z), __builtin_bit_cast(unsigned, z), false, false);
  const float r = asf(a[0]) + asf(a[1]), t = asf(b[0]) + asf(b[1]);
  auto c = __builtin_amdgcn_permlane32_swap(__builtin_bit_cast(unsigned, r), __builtin_bit_cast(unsigned, r), false, false);
  auto d = __builtin_amdgcn_permlane32_swap(__builtin_bit_cast(unsigned, t), __builtin_bit_cast(unsigned, t), false, false);
  const float u = asf(c[0]) + asf(c[1]);
  z = asf(d[0]) + asf(d[1]);
  auto e = __builtin_amdgcn_permlane16_swap(__builtin_bit_cast(unsigned, u), __builtin_bit_cast(unsigned, u), false, false);
  x = asf(e[0]); y = asf(e[1]);
}
__device__ __forceinline__ u32x4 trade16(u32x4 v) {
  auto a = __builtin_amdgcn_permlane16_swap(v[0], v[2], false, false);
  auto b = __builtin_amdgcn_permlane16_swap(v[1], v[3], false, false);
  return u32x4{(unsigned)a[0], (unsigned)b[0], (unsigned)a[1], (unsigned)b[1]};
}
__device__ __forceinline__ unsigned umax4(u32x4 v) { return max(max(v[0], v[1]), max(v[2], v[3])); }
__device__ __forceinline__ u32x2 unpoison2(u32x2 v) { return u32x2{min(v[0], POISON - 1), min(v[1], POISON - 1)}; }
__device__ __forceinline__ u32x4 unpoison4(u32x4 v) {
  return u32x4{min(v[0], POISON - 1), min(v[1], POISON - 1), min(v[2], POISON - 1), min(v[3], POISON - 1)};
}

typedef union { struct { s16x4 a, b; } hf; u32x4 v; } frag_t;
__device__ __forceinline__ s16x4 trread(const char* smem, unsigned off) {
  return __builtin_amdgcn_ds_read_tr16_b64_v4i16((LDS_AS s16x4*)(smem + off));
}
__device__ __forceinline__ s16x4 trabs(unsigned addr) { return __builtin_amdgcn_ds_read_tr16_b64_v4i16((LDS_AS s16x4*)(size_t)addr); }

constexpr int WA = 6;
#define WOP 4, 3, 6, 8, 1, 4, 0
constexpr int WOV[7] = {WOP};
constexpr int WLA = WOV[4], WPR = WOV[5], QPR = WOV[6];
constexpr int WB = WOV[0], WR = WOV[1], WIS = WOV[2], WPS = WOV[3];
#define CH 0, 7, 9, 10, 16, 19, 24, 26, 30, 31, 5, 2
constexpr int CHV[12] = {CH};
constexpr int FKL = CHV[10], FSN = CHV[11];
constexpr int SPL = CHV[0];
constexpr int FSL = CHV[1], FS0 = CHV[2], FS0D = CHV[3], FS1 = CHV[4], FS1S = CHV[5];
constexpr int FXA = CHV[6], FXB = CHV[7], FXL = CHV[8], FXS = CHV[9];
#define ROT0 p
#define L0S 22, 25, 8, 0, 24, 28, 1, 2, 4, 14
constexpr int L0V[10] = {L0S};
constexpr int CSL = L0V[0], CSS = L0V[1], CSD = L0V[2];
constexpr int abl(int it) { return it ? 25 : L0V[4]; }
constexpr int abs_(int it) { return it ? 27 : L0V[5]; }
constexpr int W2S = L0V[6], WSH = L0V[7];
constexpr int PRE0 = 12;
constexpr int LNL = L0V[3], LNS = L0V[8], POI = L0V[9];
#define TCH 22, 3, 0x07, 26, 0xF8, 0, 1
constexpr int TCV[7] = {TCH};
constexpr int TS0 = TCV[0], TSN = TCV[1], TM0 = TCV[2], TEX = TCV[3], TM1 = TCV[4], TSD = TCV[5], TSG = TCV[6];
constexpr int QL = 26;
#define SLP 18, 4, 28, 3, 0, 0
constexpr int SLV[6] = {SLP};
constexpr int SDIR = SLV[0], GSL = SLV[1], RSL = SLV[2], KSL = SLV[3];
constexpr int tp0(int it) { return it ? 3 : 3 + WSH; }
constexpr bool tile_piece(int it, int s) { return s >= tp0(it) && ((s - tp0(it)) >> 2) + 3 < 8 && ((s - tp0(it)) & 3) < 3; }
#define NPRE 3
#define QAD 4
#define WO2 1, 5, 4, 2, 8
constexpr int W2V[5] = {WO2};
constexpr int WOS = 12;
static_assert(WOS + QAD < 18, "covered by the operand waits in front of the barrier of step 18");
constexpr int WON = W2V[0], W2B = W2V[1], W2R = W2V[2], W2L = W2V[3], W2P = W2V[4];
constexpr int a_req(int s, int n) {
  int r = 2 * NPRE;
  for (int u = 1; u < 192; u += 2) {
    const bool go = r < 64 && (r >> 1) <= u / 6 + QAD && (r < 8 || u >= 9);
    if (u == 6 * s + n) return go ? r : -1;
    r += go;
  }
  return -1;
}
constexpr int vm_n(int it, int s, int n) {
  int c = it ? (a_req(s, n) >= 0) + (s == 0 && n == 0) : (s + 5 < 32 && (n == 2 || n == 3));
  if (n == 0) c += tile_piece(it, s);
  if (n == 4) c += (s == abl(it));
  if (!it) c += (s >= W2S && s < W2S + 3 && n == 4) + (s == LNL && n == 5 ? 3 : 0) + (s == 0 && n < 2 ? 2 : 0) + (s == LNS + 2 && n == 5) + (s == TEX && n == 5);
  else c += (s == 2 && n == 2) + (s == (SPL < 0 ? 0 : SPL) && n == (SPL < 0 ? 1 : 4) ? 4 : 0) + (s >= 27 && s < 30 && (n == 0 || n == 3)) + (WON && s == WOS && n == 4);
  return c;
}
constexpr int vm_at(int it, int s, int n) {
  int c = it ? 2 * NPRE : PRE0;
  for (int u = 0; u < 6 * s + n; ++u) c += vm_n(it, u / 6, u % 6);
  return c;
}
constexpr int vm_a(int it, int k) {
  if (k < NPRE) return 2 * k + 2;
  for (int u = 0; u < 192; ++u)
    if (a_req(u / 6, u % 6) == 2 * k + 1) return vm_at(it, (u + 1) / 6, (u + 1) % 6);
  return 0;
}
constexpr int vm_tile(int it, int t) { return t >= 3 ? vm_at(it, 4 * t - 10 + tp0(it), 1) : t == 1 ? 12 : vm_at(0, W2S + 2, 5); }
constexpr int vm_unit(int u) { return u < 3 ? 5 + 2 * u : u < 5 ? vm_at(0, 0, u - 2) : vm_at(0, u - 5, 4); }
constexpr int unit_slot(int u) { return u % 3 == 0 ? 0 : 16384 + (u % 3) * 16384; }

constexpr int L_XC = LDS_BYTES - 64, L_SKEY = 32704, L_ROW = 1024, L_OUT = 65536, L_SRED = 4096, L_SFIN = 20480;
__device__ __forceinline__ unsigned key_load(rsrc_t rs, int l, int L, int rep) { return ld4(rs, (l & 15) * SKEY, KEY_AT(L, rep)); }
__device__ __forceinline__ unsigned umax16(unsigned k) {
  k = max(k, dppu<0xB1>(k));
  k = max(k, dppu<0x4E>(k));
  k = max(k, dppu<0x141>(k));
  return max(k, dppu<0x140>(k));
}
__device__ __forceinline__ unsigned key_row(unsigned key) { return (0xffffu - (key & 0xffffu)) & 2047u; }
__device__ __forceinline__ unsigned key_wait(rsrc_t rs, u32x2 T, int l, int L, int rep, unsigned kv) {
  if (!__all(kv != POISON)) {
    unsigned spins = 0;
    do {
      if (spin_fail(spins, rs, T)) break;
      if (L == 0) __builtin_amdgcn_s_sleep(KSL);
      kv = key_load(rs, l, L, rep);
    } while (!__all(kv != POISON));
  }
  return __builtin_amdgcn_readfirstlane(umax16(kv));
}
__device__ __forceinline__ unsigned cand_off(unsigned key, int L) { return OFF_CA + L * 65536 + (key_row(key) >> 7) * 4096; }
#define XO(i) (((i) >> 1) * 256 + ((i) & 1) * 64)
__device__ __forceinline__ void ffn_gu(const u32x4 (&gv)[4], const u32x4 (&uv)[4], const u32x4 (&xv)[4], f32x4& ga, f32x4& ua) {
  ga = mfma16(gv[0], xv[0], f32x4{0.f, 0.f, 0.f, 0.f}); ua = mfma16(uv[0], xv[0], f32x4{0.f, 0.f, 0.f, 0.f});
#pragma unroll
  for (int i = 1; i < 4; ++i) { ga = mfma16(gv[i], xv[i], ga); ua = mfma16(uv[i], xv[i], ua); }
}
__device__ __forceinline__ void ffn_gu_store(char* smem, int l, int w, f32x4 ga, f32x4 ua) {
  LDS_AS f32x4* sred = (LDS_AS f32x4*)(smem + L_SRED);
  if ((l & 15) == 0) { sred[w * 8 + (l >> 4)] = ga; sred[w * 8 + 4 + (l >> 4)] = ua; }
}
__device__ __forceinline__ f32x4 ffn_d_read(char* smem, int l) {
  const LDS_AS float* t = (const LDS_AS float*)(smem + L_SRED) + (l & 8) * 16 + (l & 4) * 4 + (l >> 4) * 4 + (l & 3);
  return f32x4{t[0], t[32], t[64], t[96]};
}
__device__ __forceinline__ u32x2 ffn_d_h(f32x4 t) {
  float x = t[0] + t[1];
  asm volatile("" : "+v"(x));
  x += t[2] + t[3];
  x += dppf<0x128>(x);
  const float y = dppf<0x124>(x);
  const bool up = __builtin_amdgcn_inverse_ballot_w64(0xF0F0F0F0F0F0F0F0ull);
  const float gg = rbf1(up ? y : x), uu = rbf1(up ? x : y);
  const float h = rbf1(gg * __builtin_amdgcn_rcpf(1.0f + __expf(-gg))) * uu;
  const unsigned p = pack2(h, dppf<0xB1>(h));
  return u32x2{dppu<0x00>(p), dppu<0xAA>(p)};
}
template <int t> __device__ __forceinline__ f32x4 ffn_d_mma(s16x4 d, u32x2 hh, f32x4 y) {
  const bool me = __builtin_amdgcn_inverse_ballot_w64(0x0001000100010001ull << t);
  return mfma16k16(d, __builtin_bit_cast(s16x4, u32x2{me ? hh[0] : 0u, me ? hh[1] : 0u}), y);
}
__device__ __forceinline__ f32x4 ffn_d_all(const s16x4 (&dfr)[8], u32x2 hh) {
  f32x4 a = {0.f, 0.f, 0.f, 0.f}, b = a;
  a = ffn_d_mma<0>(dfr[0], hh, a); b = ffn_d_mma<1>(dfr[1], hh, b); a = ffn_d_mma<2>(dfr[2], hh, a); b = ffn_d_mma<3>(dfr[3], hh, b);
  a = ffn_d_mma<4>(dfr[4], hh, a); b = ffn_d_mma<5>(dfr[5], hh, b); a = ffn_d_mma<6>(dfr[6], hh, a); b = ffn_d_mma<7>(dfr[7], hh, b);
  return a + b;
}
__device__ __forceinline__ void ffn_d_store(rsrc_t rs, int l, int w, int L, int slice, f32x4 y) {
  const unsigned c = l & 15;
  if (c < 8) st16(unpoison4(__builtin_bit_cast(u32x4, y)), rs, (2 * w + (c >> 2)) * 65536 + slice * 256 + (c & 3) * 64 + (l >> 4) * 16, OFF_PART + L * (1u << 20));
}
#define RED_VO(k) ((min(tid + 512 * (k), 1791) >> 3) * 256 + (tid & 7) * 16)
#define RED_SO(L, G) (OFF_PART + (L) * (1u << 20) + ((G) >> 1) * 65536 + ((G) & 1) * 128)
#define RED_JJ() ((tid & 3) * 16 + (tid & 4) * 2)
__device__ __forceinline__ void red_load(rsrc_t rs, int tid, int L, unsigned G, unsigned key, u32x4 (&pv)[4], u32x2& cxn, u32x2& cx1) {
#pragma unroll
  for (int k = 0; k < 4; ++k) pv[k] = ld16(rs, RED_VO(k), RED_SO(L, G));
  cxn = ld8(rs, RED_JJ(), cand_off(key, L) + XO(G));
  cx1 = ld8(rs, RED_JJ() + 128, cand_off(key, L) + XO(G));
}
__device__ __forceinline__ void red_sum(rsrc_t rs, char* smem, u32x2 T, int tid, int w, int L, unsigned G, unsigned key,
                                        u32x4 (&pv)[4], u32x2& cxn, u32x2& cx1) {
#define PV_OK() __all(max(max(max(umax4(pv[0]), umax4(pv[1])), max(umax4(pv[2]), umax4(pv[3]))), max(max(cxn[0], cxn[1]), max(cx1[0], cx1[1]))) != POISON)
  if (!PV_OK()) {
    unsigned spins = 0;
    do {
      if (spin_fail(spins, rs, T)) break;
#pragma unroll
      for (int k = 0; k < 4; ++k)
        if (umax4(pv[k]) == POISON) pv[k] = ld16(rs, RED_VO(k), RED_SO(L, G));
      if (max(max(cxn[0], cxn[1]), max(cx1[0], cx1[1])) == POISON) { cxn = ld8(rs, RED_JJ(), cand_off(key, L) + XO(G)); cx1 = ld8(rs, RED_JJ() + 128, cand_off(key, L) + XO(G)); }
    } while (!PV_OK());
  }
#undef PV_OK
  f32x4 s = (__builtin_bit_cast(f32x4, pv[0]) + __builtin_bit_cast(f32x4, pv[1])) + __builtin_bit_cast(f32x4, pv[2]);
  if (w < 4) s += __builtin_bit_cast(f32x4, pv[3]);
#pragma unroll
  for (int j = 0; j < 4; ++j) s[j] = sum_hi8(s[j]);
  if ((tid & 63) < 8) ((LDS_AS f32x4*)(smem + L_SFIN))[w * 8 + (tid & 7)] = s;
}
__device__ __forceinline__ float red_gate(unsigned key) {
  const unsigned k16 = key >> 16;
  const float rmax = __builtin_bit_cast(float, ((k16 & 0x8000u) ? (k16 & 0x7fffu) : (~k16 & 0xffffu)) << 16);
  float sig = rbf1(__builtin_amdgcn_rcpf(1.0f + __expf(-rmax)));
  asm volatile("" : "+v"(sig));
  return sig;
}
__device__ __forceinline__ u32x2 red_final(char* smem, int l, float sig, u32x2 cxn, u32x2 cx1) {
  f32x4 s = ((const LDS_AS f32x4*)(smem + L_SFIN))[l];
#pragma unroll
  for (int j = 0; j < 4; ++j) s[j] = sum_hi8(s[j]);
  const float xn4[4] = {lo16(cxn[0]), hi16(cxn[0]), lo16(cxn[1]), hi16(cxn[1])};
  const float x14[4] = {lo16(cx1[0]), hi16(cx1[0]), lo16(cx1[1]), hi16(cx1[1])};
  float o[4];
#pragma unroll
  for (int j = 0; j < 4; ++j) o[j] = x14[j] + rbf1(xn4[j] + rbf1(rbf1(s[j]) * sig));
  return u32x2{pack2(o[0], o[1]), pack2(o[2], o[3])};
}

struct RowFin { float mean, var, mb, rd; unsigned key; };
__device__ __forceinline__ void fin_wait(rsrc_t rs, u32x2 T, unsigned voff, unsigned so, u32x4 (&sp)[4]) {
#define ST_OK() __all(max(max(umax4(sp[0]), umax4(sp[1])), max(umax4(sp[2]), umax4(sp[3]))) != POISON)
  if (!ST_OK()) {
    unsigned spins = 0;
    do {
      if (spin_fail(spins, rs, T)) break;
#pragma unroll
      for (int i = 0; i < 4; ++i) sp[i] = ld16(rs, voff + i * 2048, so);
    } while (!ST_OK());
  }
#undef ST_OK
}
__device__ __forceinline__ RowFin fin_key(rsrc_t rs, u32x2 T, unsigned voff, unsigned so, char* skey, int l, int w, unsigned R, float wsum, u32x4 (&sp)[4]) {
  RowFin r;
#define ST_SUM() ((__builtin_bit_cast(f32x4, sp[0]) + __builtin_bit_cast(f32x4, sp[1])) + (__builtin_bit_cast(f32x4, sp[2]) + __builtin_bit_cast(f32x4, sp[3])))
  f32x4 sf = ST_SUM();
  const float z = (sf[0] + sf[1]) + sf[2];
  if (__any(z != z)) {
    fin_wait(rs, T, voff, so, sp);
    sf = ST_SUM();
  }
#undef ST_SUM
  float s0 = sf[0], s1 = sf[1], s2 = sf[2];
  add_g3(s0, s1, s2);
  r.mean = s0 * (1.0f / 1024.0f);
  r.var = fmaxf(__builtin_fmaf(-r.mean, r.mean, s1 * (1.0f / 1024.0f)), 0.f);
  r.mb = rbf1(r.mean); r.rd = ln_rd(r.var);
  const unsigned rb16 = pack2((s2 - r.mb * wsum) * r.rd, 0.f) & 0xffffu;
  r.key = min((((rb16 & 0x8000u) ? (~rb16 & 0xffffu) : (rb16 | 0x8000u)) << 16) | (0xffffu - R), POISON - 1);
  const unsigned wk = umax16(r.key);
  if (l == 0) ((LDS_AS unsigned*)skey)[w] = wk;
  return r;
}
__device__ __forceinline__ void fin_cand(rsrc_t rs, char* skey, int L, int g, int rb, int ct, const RowFin& r, const f32x2 (&v)[8]) {
  const u32x4 k0 = *(const LDS_AS u32x4*)skey, k1 = *(const LDS_AS u32x4*)(skey + 16);
  const unsigned best = max(umax4(k0), umax4(k1));
  if (r.key == best) {
    const unsigned co = L * 65536 + rb * 4096 + ct * 256 + g * 16;
    f32x2 t[8], rd2 = {r.rd, r.rd}, of2 = {-r.mb * r.rd, -r.mb * r.rd};
    asm volatile("" : "+v"(rd2), "+v"(of2));
#pragma unroll
    for (int j = 0; j < 8; ++j) t[j] = __builtin_elementwise_fma(v[j], rd2, of2);
    st16(unpoison4(pack8v(t)), rs, co, OFF_CA); st16(unpoison4(pack8v(t + 4)), rs, co + 64, OFF_CA);
    if (g == 0 && ct < (int)RKEY) st4(best, rs, KEY_AT(L, ct) + rb * SKEY, 0);
    st16(unpoison4(pack8v(v)), rs, co + 128, OFF_CA); st16(unpoison4(pack8v(v + 4)), rs, co + 192, OFF_CA);
  }
}
__device__ __forceinline__ void fin_x2(const RowFin& r, f32x2 (&v)[8]) {
  f32x2 sc2 = {1.0f + r.rd, 1.0f + r.rd}, of2 = {-r.mb * r.rd, -r.mb * r.rd};
  asm volatile("" : "+v"(sc2), "+v"(of2));
#pragma unroll
  for (int j = 0; j < 8; ++j) v[j] = __builtin_elementwise_fma(v[j], sc2, of2);
}

extern "C" __device__ unsigned long long dispatch_id() __asm("llvm.amdgcn.dispatch.id");

extern "C" __global__ __launch_bounds__(512, 2) void mega_kernel(const bf16* x, char* ws, const bf16* wqkv, const bf16* wo, const bf16* wr,
                                                                 const bf16* wg, const bf16* wu, const bf16* wd, bf16* out) {
  extern __shared__ __attribute__((aligned(16))) char smem[];
  const int w0_ = __builtin_amdgcn_readfirstlane(threadIdx.x >> 6);
  const unsigned lbase = (unsigned)(size_t)(LDS_AS char*)smem;
  {
    unsigned long long pc;
    unsigned len;
    asm volatile("s_getpc_b64 %0\n\ts_mov_b32 %1, .Ltext_end-mega_kernel\n\t.subsection 1\n.Ltext_end:\n\t.subsection 0" : "=s"(pc), "=s"(len));
    const unsigned o = ((blockIdx.x >> 3) * 64 + (threadIdx.x & 63)) * 128;
    if (w0_ == 7 && o < len)
      asm volatile("s_mov_b32 m0, %0\n\ts_nop 0\n\tglobal_load_lds_dword %1, %2" ::"s"(lbase + L_FXL), "v"(o), "s"(pc & ~127ull) : "memory");
  }
  asm volatile("" : "+s"(x), "+s"(ws), "+s"(wqkv));
#define IDS()                                                                                         \
  int tid = threadIdx.x, w = w0_, bid = blockIdx.x;                                                   \
  asm volatile("" : "+v"(tid));                                                                       \
  asm volatile("" : "+s"(w));                                                                         \
  asm volatile("" : "+s"(bid));                                                                       \
  const int l = tid & 63, xcd = bid & 7, bi = bid >> 3;                                               \
  const int p = (xcd & 1) * 4 + (bi & 3), h = (xcd >> 1) * 8 + (bi >> 2);         \
  const int rb = 2 * xcd + (bi & 1), ct = bi >> 1;                               \
  const int slice = xcd * 28 + bi;                                                   \
  const bool ffn_on = bi < 28;                                                                        \
  const int g = l >> 4;                                                                               \
  const int rt = w < 4 ? w : 11 - w;          \
  KARGS();                                                                                            \
  (void)p; (void)h; (void)rb; (void)ct; (void)slice; (void)ffn_on; (void)g; (void)rt;
  const rsrc_t rs = __builtin_amdgcn_make_buffer_rsrc((void*)ws, 0, WS_BYTES, 0x00020000);
  typedef const void* const __attribute__((address_space(4)))* karg_t;
#define KARGS() karg_t ka = (karg_t)__builtin_amdgcn_kernarg_segment_ptr(); asm volatile("" : "+s"(ka))
#define ARGP(name, i) const GAS bf16* name = (const GAS bf16*)ka[i]; asm volatile("" : "+s"(name))
#define RSRC(ptr, bytes) __builtin_amdgcn_make_buffer_rsrc((void*)(ptr), 0, bytes, 0x00020000)
#define rwq RSRC(pwq, 1024 * 3072 * 2)
#define rwo RSRC(pwo, 1024 * 1024 * 2)
#define rwg RSRC(pwg, 1024 * F_ * 2)
#define rwu RSRC(pwu, 1024 * F_ * 2)
#define rwd RSRC(pwd, 1024 * F_ * 2)
#define rout RSRC(pout, 2048 * 1024 * 2)

#define WQ_VOFF() ((l >> 2) * 6144 + (((l & 3) ^ ((((l >> 4) ^ (l >> 5)) & 1) << 1)) << 4))
#define WQ_PIECE(t, j, dst) dma16(rwq, wqv, ((((t) + rot) & 7) * 128 + ((j) & 7) * 16) * 6144 + ((j) >> 3) * 2048 + h * 64, lbase + (dst) + (j) * 1024)
#define WQ_BOFF(dt) ((8 * g + ((l & 15) >> 2)) * 64 + (l & 3) * 8 + (((dt) ^ (g & 1)) << 5))
#define WQ_POFF() ((4 * g + ((l & 15) >> 2)) * 64 + (l & 3) * 8 + (((g ^ (g >> 1)) & 1) << 5))
#define X_UNIT1(u, j) dma16(rx, xv_, (unsigned)(p * 256 + rt * 32 + ((u) & 1) * 16 + (j) * 8) * 2048 + ((((u) >> 2) + rot) & 7) * 256 + (((u) >> 1) & 1) * 128, lbase + rt * 2048 + unit_slot(u) + (j) * 1024)
#define X_VOFF() ((l >> 3) * 2048 + (((l & 7) ^ ((l >> 3) & 6)) << 4))
#define X_ROFF(hs) (lbase + rt * 2048 + (l & 15) * 128 + (((4 * (hs) + g) ^ (l & 6)) << 4))
#define TOUCH_TO(ptr, dst) asm volatile("s_mov_b32 m0, %0\n\ts_nop 0\n\tglobal_load_lds_dword %1, off" ::"s"(lbase + (dst)), "v"(ptr) : "memory")
#define TOUCH(ptr) TOUCH_TO(ptr, L_FXL)
#define WO_PIECE_W(j, w_)                                                                                                                 \
  {                                                                                                                                       \
    if constexpr (it == 1 && WON) dma16s<0>(rs, l * 16, OFF_WOI + ct * 131072 + (j) * 8192 + (w_) * 1024, lbase + 32768 + (j) * 8192 + (w_) * 1024); \
    else dma16(rwo, wov, (((w_) >> 2) * 64 + ((w_) & 3) + 4 * (j)) * 16384 + ct * 128, lbase + 32768 + (j) * 8192 + (w_) * 1024);           \
  }
#define WO_PIECE(j) WO_PIECE_W(j, w)
#define WO_VOFF() ((l >> 3) * 2048 + (((((l & 7) >> 1) ^ ((l >> 4) & 3)) * 2 + (l & 1)) << 4))
#define WD_DMA() _Pragma("unroll") for (int i = 0; i < 4; ++i) WD_DMA1(i)
#define WD_DMA1(i)                                                                                   \
      dma16n(rwd, (l >> 4) * 2048 + (l & 15) * 16, (unsigned)(slice * 16 + 4 * (i)) * 2048 + w * 256, lbase + L_DN + w * 4096 + (i) * 1024)
#define FFN_DMA(m) dma16((m) & 1 ? rwu : rwg, fnv, (unsigned)(((4 * w + ((m) >> 1)) * 32) * F_ + slice * 16) * 2, lbase + 65536 + (m) * 8192 + w * 1024)
  u32x4 gv[4], uv[4];
  s16x4 dfr[8];
  u32x4 keep[9];
  u32x4 pre[4][2];
#define PRE_LOAD(it_, rot_, s0, s1)                                                                  \
  static_for<(s1) - (s0)>([&](auto c_) __attribute__((always_inline)) {                              \
    constexpr int s = (s0) + decltype(c_)::value;                                                    \
    const unsigned ab_ = OFF_XN + (it_) * (4u << 20) + (((rot_) + (s >> 2)) & 7) * 8192 + ((s >> 1) & 1) * 4096; \
    ld16w<2 * s, (s & 1) * 2048>(pre[s][0], rs, l * 16, ab_ + (p * 8 + rt) * 65536);                         \
    ld16w<2 * s + 1, (s & 1) * 2048 + 1024>(pre[s][1], rs, l * 16, ab_ + (p * 8 + rt) * 65536);                 \
  })
  u32x2 T;
  u32x2 ep;
  u32x4 xl[2];
#define LN_LOAD()                                                                                                                                   \
  {                                                                                                                                                 \
    asm volatile("buffer_load_dwordx4 %0, %1, %2, %3 offen" : "=v"(xl[0]) : "v"(l * 16), "s"(rx), "s"((p * 256 + h * 8 + w) * 2048) : "memory");      \
    asm volatile("buffer_load_dwordx4 %0, %1, %2, %3 offen offset:1024" : "=v"(xl[1]) : "v"(l * 16), "s"(rx), "s"((p * 256 + h * 8 + w) * 2048) : "memory"); \
    asm volatile("buffer_load_dwordx2 %0, off, %1, 0 sc1" : "=v"(ep) : "s"(rs) : "memory");                                                         \
  }
  const GAS bf16* tsrc;
  {
    IDS();
    const int it = 0, rot = ROT0;
    (void)it;
    const bf16* pwq = wqkv;
    const rsrc_t rx = RSRC(x, 2048 * 1024 * 2);
    const unsigned wqv = WQ_VOFF(), xv_ = X_VOFF();
    if (tid == 0) *(LDS_AS u32x4*)(smem + L_QC) = u32x4{0u, 0u, 0u, 0u};
    if (w == 0 && l < 24) TOUCH((const GAS bf16*)wqkv + (bid * 24 + l) * 512 + ((3 - (unsigned)((size_t)wqkv >> 7)) & 7) * 64);
#pragma unroll
    for (int i = 0; i < 3; ++i) WQ_PIECE(0, 3 * w + i, L_RING);
    X_UNIT1(0, 0); X_UNIT1(0, 1); X_UNIT1(1, 0); X_UNIT1(1, 1);
    BAR();
    X_UNIT1(2, 0); X_UNIT1(2, 1);
#pragma unroll
    for (int i = 0; i < 3; ++i) WQ_PIECE(1, 3 * w + i, L_RING + STAGE);
  }
  unsigned sel0 = 0x7FFFFFFFu;
  f32x2 v[8];
  u32x4 x2p[2], wof;
  float wsum;

  auto layer = [&](auto it_c) __attribute__((always_inline)) {
    constexpr int it = decltype(it_c)::value;
    f32x4 acc[2][6];
    f32x2 qs2[2];
    u32x4 k0, k1, v0, v1;
    {
      IDS();
      bool aff = false;
      unsigned aq[2];
      u32x4 av[2];
#define PIN_ACC() asm volatile("" : "+{v[176:179]}"(acc[0][0]), "+{v[180:183]}"(acc[0][1]), "+{v[184:187]}"(acc[0][2]), "+{v[188:191]}"(acc[0][3]), "+{v[192:195]}"(acc[0][4]), "+{v[196:199]}"(acc[0][5]), \
                               "+{v[200:203]}"(acc[1][0]), "+{v[204:207]}"(acc[1][1]), "+{v[208:211]}"(acc[1][2]), "+{v[212:215]}"(acc[1][3]), "+{v[216:219]}"(acc[1][4]), "+{v[220:223]}"(acc[1][5]))
#define PINV4(x) asm volatile("" : "+v"(x[0]), "+v"(x[1]), "+v"(x[2]), "+v"(x[3])::"memory")
      auto qkv_loop = [&](auto it_c) __attribute__((always_inline)) {
        constexpr int IT = decltype(it_c)::value;
        const unsigned abase = OFF_XN + IT * (4u << 20) + (p * 8 + rt) * 65536;
        const int rot = ROT0 + 5 * IT;
        unsigned fkey = 0, kv, bq[2];
        u32x4 fx[4], bv[2], af[WA][2];
        u32x2 cxn, cx1, hh;
        f32x4 sa, sb, cs4[2];
        f32x2 b2[2];
#define AB_OFF() (OFF_AB + IT * 32768 + (p * 8 + rt) * 512)
#define A_LOAD1(k, a) ld16w<(k) % WA * 2 + (a), ((k) & 1) * 2048 + (a) * 1024>(af[(k) % WA][a], rs, l * 16, abase + ((((k) >> 2) + rot) & 7) * 8192 + (((k) >> 1) & 1) * 4096)
        if constexpr (IT == 1) {
#pragma unroll
          for (int s = 0; s < NPRE; ++s) { af[s][0] = pre[s][0]; af[s][1] = pre[s][1]; }
        }
        const bf16* pwq = wqkv;
        ARGP(pwo, 3);
        const rsrc_t rx = RSRC(x, 2048 * 1024 * 2);
        unsigned xv_ = X_VOFF(), xr[2] = {X_ROFF(0), X_ROFF(1)};
        if constexpr (IT == 0) asm volatile("" : "+v"(xv_), "+v"(xr[0]), "+v"(xr[1]));
#define X_READ(u) { af[((u) & ~1) % WA][(u) & 1] = *(const LDS_AS u32x4*)(size_t)(xr[0] + unit_slot(u)); af[((u) | 1) % WA][(u) & 1] = *(const LDS_AS u32x4*)(size_t)(xr[1] + unit_slot(u)); }
        const int tstep = TS0 + bid % TSN + TSD * (w >> TSG);
        const unsigned wqv = WQ_VOFF();
#define QKV_DMA1(t, i)                                                                            \
  {                                                                                               \
    if constexpr (IT == 0) WQ_PIECE(t, 3 * w + (i), L_RING + SLOT(t) * STAGE);                    \
    else dma16s<(i) * 1024>(rs, l * 16, OFF_WI + (h * 8 + (((t) + rot) & 7)) * STAGE + w * 3072, lbase + L_RING + SLOT(t) * STAGE + w * 3072); \
  }
        unsigned bo[2][2];
#pragma unroll
        for (int dt = 0; dt < 2 - IT; ++dt) {
          bo[dt][0] = lbase + L_RING + (IT ? l * 16 : WQ_BOFF(dt)); bo[dt][1] = bo[dt][0] + 2 * STAGE;
          asm volatile("" : "+v"(bo[dt][0]), "+v"(bo[dt][1]));
        }
        auto frag0 = [&](int sl, int f) __attribute__((always_inline)) {
          unsigned pb = lbase + L_RING + WQ_POFF();
          asm volatile("" : "+v"(pb));
          const unsigned o = (pb ^ ((f & 1) << 5)) + sl * STAGE + ((f % 6) >> 1) * 8192 + (f / 6) * 2048;
          frag_t t;
          t.hf.a = trabs(o); t.hf.b = trabs(o + 1024);
          return t.v;
        };
        u32x4 df[4];
        auto dot_mma = [&](const u32x4 (&xf)[4]) __attribute__((always_inline)) {
          sa = mfma16(df[0], xf[0], f32x4{0.f, 0.f, 0.f, 0.f}); sb = mfma16(df[1], xf[1], f32x4{0.f, 0.f, 0.f, 0.f});
          sa = mfma16(df[2], xf[2], sa); sb = mfma16(df[3], xf[3], sb);
        };
        auto dot_store = [&](unsigned dst) __attribute__((always_inline)) {
          if ((l & 15) == 0) st16(unpoison4(__builtin_bit_cast(u32x4, sa + sb)), rs, g * 16, dst + (h * 8 + p) * 512 + w * 64);
        };
        auto tile_sum = [&](u32x4 pt, int dst) __attribute__((always_inline)) {
          f32x4 c = __builtin_bit_cast(f32x4, pt);
#pragma unroll
          for (int e = 0; e < 4; ++e) { c[e] += dppf<0x124>(c[e]); c[e] += dppf<0x128>(c[e]); c[e] = add_x16(c[e]); }
          if ((l & 28) == 0) *(LDS_AS f32x4*)(smem + dst + (w * 2 + (l >> 5)) * 64 + (l & 3) * 16) = c;
        };
        frag_t bfr[6];
#define QKV_BO(s, n, x) (bo[((n) ^ (x)) & 1][SLOT((s) >> 2) >> 1] + (SLOT((s) >> 2) & 1) * STAGE + ((n) >> 1) * 8192 + ((s) & 3) * 2048 + (x) * 256)
#define QKV_RDA(s, n)                                                                             \
  {                                                                                               \
    if constexpr (IT == 0) bfr[n].hf.a = trabs(QKV_BO(s, n, 0));                                  \
    else bfr[n].v = *(const LDS_AS u32x4*)(size_t)(bo[0][SLOT((s) >> 2) >> 1] + (SLOT((s) >> 2) & 1) * STAGE + (((s) & 3) * 6 + (n)) * 1024); \
  }
#define QKV_RDB(s, n) { if constexpr (IT == 0) bfr[n].hf.b = trabs(QKV_BO(s, n, 1)); }
#define XP_OFF() (OFF_XNEW + ((rot + 5) & 7) * 1024)
#define FP_VO() (w == 3 ? (l >> 2) * 256 + (l & 3) * 16 : ((l >> 2) & 7) * 512 + (l >> 5) * 64 + (l & 3) * 16)
#define FP_OFF(i) (w == 3 ? OFF_XNEW + (i) * 4096 : OFF_FX + h * 4096 + w * 128)
#define FX_OK(n) __all(((n) == 4 ? max(max(umax4(fx[0]), umax4(fx[1])), max(umax4(fx[2]), umax4(fx[3]))) : max(umax4(fx[0]), umax4(fx[1]))) != POISON)
#define SINCE(s0) (vm_at(IT, s, n) - vm_at(IT, s0, 5))
#define DEF4(x) asm volatile("" : "=v"(x[0]), "=v"(x[1]), "=v"(x[2]), "=v"(x[3]))
        LDS_AS unsigned* qcnt = (LDS_AS unsigned*)(smem + L_QC);
        unsigned qc;
        unsigned qadr = lbase + L_QC, qone = 1;
        asm volatile("" : "+v"(qadr), "+v"(qone));
#define count_in(i) asm volatile("s_mov_b64 exec, 1\n\tds_add_u32 %0, %1 offset:%2\n\ts_mov_b64 exec, -1" ::"v"(qadr), "v"(qone), "n"(4 * (i)) : "memory")
        auto count_wait = [&](int i, unsigned want) __attribute__((always_inline)) {
          if (__builtin_expect((unsigned)__builtin_amdgcn_readfirstlane(qc) < want, 0)) {
            while ((unsigned)__builtin_amdgcn_readfirstlane(__hip_atomic_load(qcnt + i, __ATOMIC_RELAXED, __HIP_MEMORY_SCOPE_WORKGROUP)) < want) {}
          }
          asm volatile("" ::: "memory");
        };
        if constexpr (IT == 0) {
          wait_vm<PRE0 - vm_unit(1)>();
          X_READ(0); X_READ(1);
          asm volatile("s_barrier" ::: "memory");
        }
#pragma unroll
        for (int n = 0; n < 6; ++n) { QKV_RDA(0, n); if (n < 5) QKV_RDB(0, n); }
        f32x2 lv[4], xs, xq;
        float lmean;
        f32x4 sc;
#define X1_OFF() (F_X1 + ((p * RX1 + (xcd >> 1) % RX1) * 32 + (rt >> 2) * 16 + (l & 15)) * SX1)
        unsigned long long xok = 0;
        RowFin fin;
        auto x1_need = [&](unsigned long long m) __attribute__((always_inline)) {
          if (__builtin_expect((xok & m) != m, 0)) {
            for (unsigned spins = 0;;) {
              const u32x2 f = ld8(rs, X1_OFF(), 0);
              xok = __builtin_amdgcn_ballot_w64(f[0] == T[0] && f[1] == T[1]);
              if ((xok & m) == m) break;
              if (spin_fail(spins, rs, T)) { xok = ~0ull; break; }
              __builtin_amdgcn_s_sleep(KSL);
            }
            asm volatile("" ::: "memory");
          }
        };
        auto gap = [&](auto sc_, auto nc_) __attribute__((always_inline)) {
          constexpr int s = decltype(sc_)::value, n = decltype(nc_)::value, ar = a_req(s, n);
          {
            constexpr int tc = (s + 2) / 4;
            if constexpr (tc >= 1 && tc < 8 && s == 4 * tc - 2 && n == 5) {
              if constexpr (!IT || tc >= 3) wait_vm<vm_at(IT, s, n) - vm_tile(IT, tc)>();
              if constexpr (tc == 7 && !IT) asm volatile("s_waitcnt lgkmcnt(4)" ::: "memory");
              asm volatile("s_barrier" ::: "memory");
            }
          }
          if constexpr (IT == 0 && n == 0 && s + 2 < 32) { wait_vm<vm_at(0, s, 0) - vm_unit(s + 2)>(); X_READ(s + 2); SB(); }
          if constexpr (n == 0 && tile_piece(IT, s)) QKV_DMA1(((s - tp0(IT)) >> 2) + 3, (s - tp0(IT)) & 3);
          if constexpr (IT == 0) {
            if constexpr ((n == 2 || n == 3) && s + 5 < 32) {
              if constexpr (n == 2) asm volatile("s_waitcnt lgkmcnt(%0)" ::"n"(s + 1 < QL ? 4 : 1) : "memory");
              X_UNIT1(s + 5, n - 2);
            }
            if constexpr (s >= W2S && s < W2S + 3 && n == 4) QKV_DMA1(2, s - W2S);
            if constexpr (s == LNL && n == 5) LN_LOAD();
            if constexpr (s == 0 && n < 2) { X_UNIT1(3 + n, 0); X_UNIT1(3 + n, 1); }
            if constexpr (s == LNS && n == 1) {
              wait_vm<vm_at(0, s, n) - vm_at(0, LNL + 1, 0)>();
              asm volatile("" : "+v"(ep), "+v"(xl[0]), "+v"(xl[1])::"memory");
              const unsigned long long e = (((unsigned long long)(unsigned)__builtin_amdgcn_readfirstlane(ep[1]) << 32) | (unsigned)__builtin_amdgcn_readfirstlane(ep[0])) + 1ull + dispatch_id() * 0x9E3779B97F4A7C16ull;
              T = u32x2{(unsigned)e, (unsigned)(e >> 32)};
              unpack8v(xl[0], lv);
              xs = (lv[0] + lv[1]) + (lv[2] + lv[3]);
              xq = __builtin_elementwise_fma(lv[0], lv[0], lv[1] * lv[1]) + __builtin_elementwise_fma(lv[2], lv[2], lv[3] * lv[3]);
              asm volatile("" : "+v"(xs), "+v"(xq));
            }
            if constexpr (s == LNS && n == 4) {
              unpack8v(xl[1], lv);
              xs += (lv[0] + lv[1]) + (lv[2] + lv[3]);
              xq += __builtin_elementwise_fma(lv[0], lv[0], lv[1] * lv[1]) + __builtin_elementwise_fma(lv[2], lv[2], lv[3] * lv[3]);
              auto t = __builtin_amdgcn_permlane32_swap(__builtin_bit_cast(unsigned, hadd(xs)), __builtin_bit_cast(unsigned, hadd(xq)), false, false);
              lmean = asf(t[0]) + asf(t[1]);
              asm volatile("" : "+v"(lmean));
            }
            if constexpr (s == LNS + 1 && n == 1) { lmean += dppf<0xB1>(lmean); lmean += dppf<0x4E>(lmean); lmean += dppf<0x124>(lmean); asm volatile("" : "+v"(lmean)); }
            if constexpr (s == LNS + 1 && n == 4) { lmean = add_x16(lmean + dppf<0x128>(lmean)) * (1.0f / 1024.0f); asm volatile("" : "+v"(lmean)); }
            if constexpr (s == LNS + 2 && n == 1) {
              const float m2 = asf(__builtin_amdgcn_readlane(__builtin_bit_cast(unsigned, lmean), 32));
              xs[0] = ln_rd(fmaxf(__builtin_fmaf(-lmean, lmean, m2), 0.f));
              asm volatile("" : "+v"(xs));
            }
            if constexpr (s == LNS + 2 && n == 5) {
              if (l == 0) st16(u32x4{__builtin_bit_cast(unsigned, (float)xs[0]), __builtin_bit_cast(unsigned, -rbf1(lmean)), T[0], T[1]}, rs, OFF_AB + (p * 256 + h * 8 + w) * 16, 0);
            }
            if constexpr (s == POI && n == 1) {
              if (w == 1) {
                const unsigned prb = 2 * p + (h >> 4), pct = h & 15;
                unsigned pz1 = POISON;
                asm volatile("" : "+v"(pz1));
                const u32x4 pz = {pz1, pz1, pz1, pz1};
                st16(pz, rs, OFF_STAT + (prb * 16 + pct) * 2048 + l * 16, 0);
                st16(pz, rs, OFF_STAT + (prb * 16 + pct) * 2048 + 1024 + l * 16, 0);
                if (l < 16) st16(pz, rs, OFF_CA + prb * 4096 + pct * 256 + l * 16, 0);
                else if (l == 16 && pct < RKEY) st4(pz1, rs, KEY_AT(0, pct) + prb * SKEY, 0);
              }
            }
            if constexpr (s == POI + 4 && n == 1) { if (w == 1 && l < RX) st8(T, rs, F_XN + (p * RX + l) * 256 + h * 8, 0); }
          } else {
            if constexpr ((s == 0 && n == 0) || (s == 2 && n == 2)) asm volatile("buffer_load_dwordx2 %0, %1, %2, 0 offen sc1" : "=v"(cxn) : "v"(X1_OFF()), "s"(rs) : "memory");
            if constexpr ((s == 1 && n == 3) || (s == 4 && n == 0)) {
              wait_vm<vm_at(1, s, n) - (s == 1 ? vm_at(1, 0, 1) : vm_at(1, 2, 3))>();
              asm volatile("" : "+v"(cxn)::"memory");
              xok |= __builtin_amdgcn_ballot_w64(cxn[0] == T[0] && cxn[1] == T[1]);
            }
#define IMG_OFF() (F_X1 + ((((h >> 3) * 2 + ((l >> 2) & 1)) * RX1 + (xcd >> 1) % RX1) * 32 + (l & 1) * 16 + (h & 7) * 2 + ((l >> 1) & 1)) * SX1)
            if constexpr (s == 0 && n == 2) { asm volatile("" : "=v"(cx1)); if (w == 1) asm volatile("buffer_load_dwordx2 %0, %1, %2, 0 offen sc1" : "=v"(cx1) : "v"(IMG_OFF()), "s"(rs) : "memory"); }
            if constexpr (s == 2 && n == 4) {
              if (w == 1) {
                wait_vm<vm_at(1, 2, 4) - vm_at(1, 0, 3)>();
                asm volatile("" : "+v"(cx1)::"memory");
                if (!__all(cx1[0] == T[0] && cx1[1] == T[1])) poll_flags(rs, IMG_OFF(), 0, T);
              }
            }
            if constexpr (ar >= 0 && (ar & 7) == 0) x1_need(3ull << (((rot + ar / 8) & 7) * 2));
            const unsigned R = (rb * 4 + (w & 3)) * 32 + (w >> 2) * 16 + (l & 15), so = OFF_STAT + rb * 32768;
            if constexpr (s == (SPL < 0 ? 0 : SPL) && n == (SPL < 0 ? 1 : 4)) {
#pragma unroll
              for (int i = 0; i < 4; ++i) ld16h<0>(fx[i], rs, (4 * g + i) * 2048 + (R & 127) * 16, so);
            }
            if constexpr (s == SPL + 2 && n == 1) {
              wait_vm<vm_at(1, s, n) - (SPL < 0 ? vm_at(1, 0, 2) : vm_at(1, SPL, 5))>();
              PINV4(fx);
              fin = fin_key(rs, T, g * 8192 + (R & 127) * 16, so, smem + L_SKEY, l, w, R, wsum, fx);
              count_in(2);
            }
            if constexpr (s == SPL + 3 && n == 0) qc = __hip_atomic_load(qcnt + 2, __ATOMIC_RELAXED, __HIP_MEMORY_SCOPE_WORKGROUP);
            if constexpr (s == SPL + 3 && n == 1) { count_wait(2, 8); fin_cand(rs, smem + L_SKEY, 0, g, rb, ct, fin, v); }
            if constexpr (s == SPL + 3 && n == 3) {
              fin_x2(fin, v);
              x2p[0] = pack8v(v); x2p[1] = pack8v(v + 4);
              asm volatile("" : "+v"(x2p[0]), "+v"(x2p[1]));
            }
            if constexpr (s == SPL + 3 && n == 5) {
              if ((w & 3) * 2 + (w >> 2) == ct >> 1) {
                const float f = 1.0f + fin.rd, rd2 = ln_rd(fin.var * f * f), mb2 = rbf1(__builtin_fmaf(fin.mean - fin.mb, fin.rd, fin.mean));
                if (g == 0 && ((l >> 3) & 1) == (ct & 1))
                  st16(u32x4{__builtin_bit_cast(unsigned, f * rd2), __builtin_bit_cast(unsigned, -__builtin_fmaf(fin.mb, fin.rd, mb2) * __builtin_amdgcn_rcpf(f)), T[0], T[1]}, rs, OFF_AB + 32768 + R * 16, 0);
              }
            }
          }
          if constexpr (IT == 1 && ar >= 0) A_LOAD1(ar / 2, ar & 1);
          if constexpr (!IT && s == CSL && n == 4) { asm volatile("" : "=v"(fx[0])); if (w < 6) ld16h<0>(fx[0], rs, l * 16, OFF_CS + (h * 6 + w) * 1024); }
          if constexpr (!IT && s == CSS && n == 2) {
            if (w < 6) {
              wait_vm<SINCE(CSL)>();
              asm volatile("" : "+v"(fx[0])::"memory");
              tag_wait(rs, T, l * 16, OFF_CS + (h * 6 + w) * 1024, fx[0]);
              const f32x2 c = {sum_hi8(asf(fx[0][0])), sum_hi8(asf(fx[0][1]))};
              if (l < 8) *(LDS_AS f32x2*)(smem + L_CSL + w * 64 + l * 8) = c;
            }
          }
          constexpr int ABL = abl(IT), ABS = abs_(IT);
          if constexpr (s == ABL && n == 4) ld16h<0>(bv[0], rs, (l & 31) * 16, AB_OFF());
          if constexpr (s == ABS && n == 2) {
            wait_vm<SINCE(ABL)>();
            asm volatile("" : "+v"(bv[0])::"memory");
            tag_wait(rs, T, (l & 31) * 16, AB_OFF(), bv[0]);
            const unsigned t0 = bv[0][0], t1 = bv[0][1];
            ((LDS_AS unsigned*)(smem + L_ABW + rt * 2048))[l] = l < 32 ? t0 : t1;
          }
          if constexpr (s == ABS && n == 3) {
#pragma unroll
            for (int a = 0; a < 2; ++a) {
              const LDS_AS unsigned* aw = (const LDS_AS unsigned*)(smem + L_ABW + rt * 2048) + 16 * a;
              aq[a] = aw[l & 15]; bq[a] = aw[32 + (l & 15)];
              av[a] = *(const LDS_AS u32x4*)(aw + 4 * g); bv[a] = *(const LDS_AS u32x4*)(aw + 32 + 4 * g);
            }
          }
          if constexpr (s == ABS && n == 5) {
#pragma unroll
            for (int a = 0; a < 2; ++a) { b2[a] = f32x2{asf(bq[a]), asf(bq[a])}; asm volatile("" : "+v"(b2[a])); }
          }
          if constexpr (s == ABS + 1) {
            constexpr int k = (n + 4) % 6;
            if constexpr (k < 4) cs4[k & 1] = *(const LDS_AS f32x4*)(smem + L_CSL + k * 64 + g * 16);
            else cs4[k & 1][0] = *(const LDS_AS float*)(smem + L_CSL + k * 64 + (l & 15) * 4);
          }
          if constexpr ((s == ABS + 1 && n > 0) || (s == ABS + 2 && n == 0)) {
            constexpr int k = (n + 3) % 6;
            if constexpr (k < 4) {
#pragma unroll
              for (int a = 0; a < 2; ++a) acc[a][k] = __builtin_elementwise_fma(cs4[k & 1], f32x4{b2[a][0], b2[a][1], b2[a][0], b2[a][1]}, acc[a][k]);
            } else {
              f32x2 c2 = {cs4[k & 1][0], cs4[k & 1][0]};
              asm volatile("" : "+v"(c2));
#pragma unroll
              for (int a = 0; a < 2; ++a) acc[a][k] = __builtin_elementwise_fma(f32x4{c2[0], c2[1], c2[0], c2[1]}, __builtin_bit_cast(f32x4, bv[a]), acc[a][k]);
            }
          }
          if constexpr (IT == 0) {
            if constexpr (s == CSD && n < 3) fx[n] = frag0(SLOT(s >> 2), 3 * w + n);
            if constexpr (s == CSD + 1 && n == 1) {
              unsigned one2 = 0x3F803F80u;
              asm volatile("" : "+v"(one2));
              const u32x4 o4 = {one2, one2, one2, one2};
              const f32x4 z4 = {0.f, 0.f, 0.f, 0.f};
              sa = mfma16(fx[0], o4, z4); sb = mfma16(fx[1], o4, z4); sc = mfma16(fx[2], o4, z4);
            }
            if constexpr (s == CSD + 2 && n == 1) {
              const int c = l & 15;
              const f32x4 t0 = sa, t1 = sb, t2 = sc, t = c == 0 ? +t0 : c == 1 ? +t1 : +t2;
              if (c < 3) *(LDS_AS f32x4*)(smem + L_CSP + (w * 3 + c) * 64 + g * 16) = t;
              count_in(2);
            }
            if constexpr (s == CSD + 4 && n == 1) {
              if (w == 2) {
                qc = 0;
                count_wait(2, 8);
                const LDS_AS f32x2* cp = (const LDS_AS f32x2*)(smem + L_CSP) + min(l, 47);
                const f32x2 t = (cp[0] + cp[48]) + (cp[96] + cp[144]);
                if (l < 48) st16(u32x4{__builtin_bit_cast(unsigned, (float)t[0]), __builtin_bit_cast(unsigned, (float)t[1]), T[0], T[1]}, rs, (l >> 3) * 1024 + (l & 7) * 16, OFF_CS + h * 6144 + p * 128);
              }
            }
            if constexpr (s >= 29 && (n & 1)) keep[3 * (s - 29) + (n >> 1)] = frag0(SLOT(s - 24), 3 * w + (n >> 1));
            if constexpr (s == (TS0 < TEX ? TS0 : TEX) - 2 && n == 1) {
              ARGP(pwg, 5); ARGP(pwu, 6);
              const int t = tid < 288 ? tid - 64 : tid - 288, go = (bi * 32 + t / 7) * F_ + xcd * 448 + (t % 7) * 64;
              tsrc = tid < 64 ? pwo + bid * 4096 + tid * 64 : tid < 288 ? pwg + go : pwu + go;
              asm volatile("" : "+v"(tsrc));
            }
            if constexpr (s >= TS0 && s < TS0 + TSN + TSD * (7 >> TSG) && n == 4) {
              if (tstep == s && ((TM0 >> w) & 1)) TOUCH(tsrc);
            }
            if constexpr (s == TEX && n == 5) {
              const GAS bf16* t2 = ((TM1 >> w) & 1) ? tsrc : (const GAS bf16*)x + p * 262144;
              TOUCH(t2);
            }
          } else {
            if constexpr (s >= 27 && s < 30 && (n == 0 || n == 3)) { const unsigned wov = WO_VOFF(); WO_PIECE(4 + 2 * (s - 27) + n / 3); }
            if constexpr (WON && s == WOS && n == 4) st16(wof, rs, (ct * 128 + rb * 8 + w) * 1024 + l * 16, OFF_WOI);
            if constexpr (WON && s == 19 && n == 1) { if (tid == 64) st8(T, rs, (ct * 16 + rb) * 256, F_WOI); }
            if constexpr (WON && s == 23 && n == 4) { asm volatile("" : "=v"(cx1)); if (w == 2) ld8h(cx1, rs, (l & 15) * 256, F_WOI + ct * 4096); }
            if constexpr (WON && s == 26 && n == 3) {
              if (w == 2) {
                wait_vm<SINCE(23)>();
                asm volatile("" : "+v"(cx1)::"memory");
                if (!__all(cx1[0] == T[0] && cx1[1] == T[1])) poll_flags(rs, (l & 15) * 256, F_WOI + ct * 4096, T);
              }
            }
            if constexpr (s == FKL && n == 4) { asm volatile("" : "=v"(kv)); if (w == 0) ld4h<0>(kv, rs, (l & 15) * SKEY, KEY_AT(0, KREP())); }
            if constexpr (s == FSL && n == 1) {
              asm volatile("" : "=v"(fx[0]), "=v"(fx[1]));
              if (w == 0) {
                wait_vm<SINCE(FKL)>();
                asm volatile("" : "+v"(kv)::"memory");
                const unsigned k_ = key_wait(rs, T, l, 0, KREP(), kv);
                if (l == 0) *(LDS_AS unsigned*)(smem + L_SKEY + 32) = k_;
                fkey = k_;
                ld16h<0>(fx[0], rs, (l >> 3) * 256 + (l & 7) * 16, cand_off(k_, 0)); ld16h<2048>(fx[1], rs, (l >> 3) * 256 + (l & 7) * 16, cand_off(k_, 0));
              }
            }
            if constexpr (s == FS0 - 1 && n == FSN) {
              if (w == 0) {
                wait_vm<vm_at(1, s, n) - vm_at(1, FSL, 2)>();
                asm volatile("" : "+v"(fx[0]), "+v"(fx[1])::"memory");
                word_wait<2>(rs, T, (l >> 3) * 256 + (l & 7) * 16, cand_off(fkey, 0), fx);
                *(LDS_AS u32x4*)(smem + L_ROW + l * 16) = fx[0]; *(LDS_AS u32x4*)(smem + L_ROW + 1024 + l * 16) = fx[1];
                count_in(0);
              }
            }
            if constexpr (s == FS0 - 1 && n == 1) {
              asm volatile("" : "=v"(dfr[0]), "=v"(dfr[1]), "=v"(dfr[2]), "=v"(dfr[3]), "=v"(dfr[4]), "=v"(dfr[5]), "=v"(dfr[6]), "=v"(dfr[7]));
              if (ffn_on) {
#pragma unroll
                for (int i = 0; i < 8; ++i) dfr[i] = trread(smem, L_DN + w * 4096 + g * 1024 + ((l & 15) >> 2) * 256 + i * 32 + (l & 3) * 8);
              }
            }
            if constexpr (s == FS0) {
              if constexpr (n == 0) qc = __hip_atomic_load(qcnt, __ATOMIC_RELAXED, __HIP_MEMORY_SCOPE_WORKGROUP);
              if constexpr (n == 1) {
                count_wait(0, 1);
                kv = *(const LDS_AS unsigned*)(smem + L_SKEY + 32);
#pragma unroll
                for (int i = 0; i < 4; ++i) fx[i] = *(const LDS_AS u32x4*)(smem + L_ROW + w * 256 + i * 64 + g * 16);
              }
              if constexpr (n == 2) {
                fkey = __builtin_amdgcn_readfirstlane(kv);
                sel0 = key_row(fkey);
                aff = (int)(sel0 >> 8) == p;
                asm volatile("" : "=v"(sa), "=v"(sb));
                if (ffn_on) ffn_gu(gv, uv, fx, sa, sb);
              }
              if constexpr (n == 4) {
                if (ffn_on) {
                  ffn_gu_store(smem, l, w, sa, sb);
                  count_in(1);
                }
              }
            }
            if constexpr (s == FS0D) {
              if constexpr (n == 0) qc = __hip_atomic_load(qcnt + 1, __ATOMIC_RELAXED, __HIP_MEMORY_SCOPE_WORKGROUP);
              if constexpr (n == 1) { asm volatile("" : "=v"(sa)); if (ffn_on) { count_wait(1, 8); sa = ffn_d_read(smem, l); } }
              if constexpr (n == 2) { asm volatile("" : "=v"(hh)); if (ffn_on) hh = ffn_d_h(sa); }
              if constexpr (n == 4) { asm volatile("" : "=v"(sa)); if (ffn_on) sa = ffn_d_all(dfr, hh); }
              if constexpr (n == 5) { if (ffn_on) ffn_d_store(rs, l, w, 0, slice, sa); }
            }
            if constexpr (s == FS1 && n == 4) {
              DEF4(fx);
              asm volatile("" : "=v"(cxn), "=v"(cx1));
              if (!ffn_on) {
                const unsigned G = xcd * 4 + bi - 28;
#pragma unroll
                for (int k = 0; k < 4; ++k) ld16h<0>(fx[k], rs, RED_VO(k), RED_SO(0, G));
                ld8h(cxn, rs, RED_JJ(), cand_off(fkey, 0) + XO(G)); ld8h(cx1, rs, RED_JJ() + 128, cand_off(fkey, 0) + XO(G));
              }
            }
            if constexpr (s == FS1S && n == 2) {
              if (!ffn_on) {
                wait_vm<SINCE(FS1)>();
                PINV4(fx);
                asm volatile("" : "+v"(cxn), "+v"(cx1)::"memory");
                red_sum(rs, smem, T, tid, w, 0, xcd * 4 + bi - 28, fkey, fx, cxn, cx1);
                BAR();
                if (w == 0) {
                  const u32x2 ov = red_final(smem, l, red_gate(fkey), cxn, cx1);
                  if (l < 8) st8(unpoison2(ov), rs, OFF_XNEW + (xcd * 4 + bi - 28) * 256 + ((l & 3) * 2 + (l >> 2)) * 8, 0);
                }
              }
            }
            if constexpr (s == FXA && n == 4) { asm volatile("" : "=v"(fx[0])); if (w == 1) ld16h<0>(fx[0], rs, ((l >> 2) & 3) * 256 + (l & 3) * 16, XP_OFF()); }
            if constexpr (s == FXB - 1 && n == FSN) {
              if (w == 1) {
                wait_vm<SINCE(FXA)>();
                asm volatile("" : "+v"(fx[0])::"memory");
                word_wait<1>(rs, T, ((l >> 2) & 3) * 256 + (l & 3) * 16, XP_OFF(), fx);
                if (l < 16) *(LDS_AS u32x4*)(smem + L_ROW + l * 16) = fx[0];
                count_in(0);
              }
            }
            if constexpr (s == FXB) {
#define FX_FRAG(i) (*(const LDS_AS u32x4*)(size_t)(bo[0][0] + STAGE + (6 * (i) + w) * 1024))
              if constexpr (n == 0) { qc = __hip_atomic_load(qcnt, __ATOMIC_RELAXED, __HIP_MEMORY_SCOPE_WORKGROUP); }
              if constexpr (n == 1) {
                DEF4(fx); DEF4(df);
                if (w < 6) {
                  count_wait(0, 2);
#pragma unroll
                  for (int i = 0; i < 4; ++i) { fx[i] = *(const LDS_AS u32x4*)(smem + L_ROW + i * 64 + g * 16); df[i] = FX_FRAG(i); }
                }
              }
              if constexpr (n == 2) { asm volatile("" : "=v"(sa), "=v"(sb)); if (w < 6) dot_mma(fx); }
              if constexpr (n == 4) { if (w < 6) dot_store(OFF_FX); }
            }
            if constexpr (s == FXL && n == 4) {
              asm volatile("" : "=v"(fx[0]), "=v"(fx[1]));
              if (aff && w < 4) { ld16h<0>(fx[0], rs, FP_VO(), FP_OFF(0)); ld16h<0>(fx[1], rs, FP_VO(), FP_OFF(1)); }
            }
            if constexpr (s == FXS && n == 4) {
              if (aff && w < 4) {
                wait_vm<0>();
                asm volatile("" : "+v"(fx[0]), "+v"(fx[1])::"memory");
                if (!FX_OK(2)) {
                  unsigned spins = 0;
                  do {
                    if (spin_fail(spins, rs, T)) break;
                    fx[0] = ld16(rs, FP_VO(), FP_OFF(0)); fx[1] = ld16(rs, FP_VO(), FP_OFF(1));
                  } while (!FX_OK(2));
                }
                if (w == 3) {
                  f32x2 nx[8];
                  unpack8v(fx[0], nx); unpack8v(fx[1], nx + 4);
                  const f32x2 sm = ((nx[0] + nx[1]) + (nx[2] + nx[3])) + ((nx[4] + nx[5]) + (nx[6] + nx[7]));
                  const float mean = sum64(hadd(sm)) * (1.0f / 1024.0f);
                  const f32x2 m2 = {mean, mean};
                  f32x2 q = {0.f, 0.f};
#pragma unroll
                  for (int j = 0; j < 8; ++j) { const f32x2 d = nx[j] - m2; q = __builtin_elementwise_fma(d, d, q); }
                  const float rd = ln_rd(sum64(hadd(q)) * (1.0f / 1024.0f));
                  if (l == 0) *(LDS_AS f32x2*)(smem + L_FXL + 384) = f32x2{rd, -rbf1(mean)};
                } else {
                  tile_sum(fx[0], L_FXL);
                }
                count_in(3);
              }
            }
          }
        };
        static_for<32>([&](auto sc_) __attribute__((always_inline)) {
          constexpr int s = decltype(sc_)::value;
          if constexpr (s > 0) PIN_ACC();
          if constexpr (QPR && s == QPR) { if (w >= 4) __builtin_amdgcn_s_setprio(1); }
          if constexpr (IT == 1) wait_vm<vm_at(IT, s, 0) - vm_a(IT, s)>();
          pin_w<s % WA>(af[s % WA][0], af[s % WA][1]);
          SB();
          static_for<6>([&](auto nc_) __attribute__((always_inline)) {
            constexpr int n = decltype(nc_)::value;
            if constexpr (s == 0) { acc[0][n] = f32x4{0.f, 0.f, 0.f, 0.f}; acc[1][n] = acc[0][n]; }
            constexpr bool on = s < QL || n >= 2;
            if constexpr (on) acc[0][n] = n < 4 ? mfma16(bfr[n].v, af[s % WA][0], acc[0][n]) : mfma16(af[s % WA][0], bfr[n].v, acc[0][n]);
            SB();
            if constexpr (n == 0) { QKV_RDB(s, 5); }
            else if constexpr (s + 1 < 32 && (s + 1 < QL || n > 2)) QKV_RDB(s + 1, n - 1);
            SB();
            if constexpr (on) acc[1][n] = n < 4 ? mfma16(bfr[n].v, af[s % WA][1], acc[1][n]) : mfma16(af[s % WA][1], bfr[n].v, acc[1][n]);
            SB();
            if constexpr (s + 1 < 32 && (s + 1 < QL || n >= 2)) QKV_RDA(s + 1, n);
            SB();
            gap(sc_, nc_);
            SB();
          });
        });
        PIN_ACC();
        frag_t qw[32 - QL][2];
        static_for<32 - QL>([&](auto c_) __attribute__((always_inline)) {
          constexpr int s = QL + decltype(c_)::value;
          QKV_RDA(s, 0); QKV_RDB(s, 0); QKV_RDA(s, 1); QKV_RDB(s, 1);
          qw[s - QL][0] = bfr[0]; qw[s - QL][1] = bfr[1];
        });
        SB();
        const bool own = aff && (int)((sel0 >> 5) & 7) == rt;
        const bool m0 = !(sel0 & 16), mq = (l & 15) == (int)(sel0 & 15), mv = g == (int)((sel0 & 15) >> 2);
        auto patch_qk = [&](int n) __attribute__((always_inline)) {
          f32x4 t = *(const LDS_AS f32x4*)(smem + L_FXL + n * 64 + g * 16);
          const f32x4 c = *(const LDS_AS f32x4*)(smem + L_CSL + n * 64 + g * 16);
          const float nb = *(const LDS_AS float*)(smem + L_FXL + 388);
#pragma unroll
          for (int e = 0; e < 4; ++e) { t[e] = __builtin_fmaf(c[e], nb, t[e]); asm volatile("" : "+v"(t[e])); }
          acc[0][n] = (mq && m0) ? t : acc[0][n];
          acc[1][n] = (mq && !m0) ? t : acc[1][n];
        };
        if (own) {
          while (__hip_atomic_load(qcnt + 3, __ATOMIC_RELAXED, __HIP_MEMORY_SCOPE_WORKGROUP) < 4u) {}
          asm volatile("" ::: "memory");
          patch_qk(2); patch_qk(3);
          const f32x2 nab = *(const LDS_AS f32x2*)(smem + L_FXL + 384);
          const LDS_AS float* fl_ = (const LDS_AS float*)(smem + L_FXL + 256) + (l & 15);
          const LDS_AS float* cl_ = (const LDS_AS float*)(smem + L_CSL + 256) + (l & 15);
          const float t4 = __builtin_fmaf(cl_[0], nab[1], fl_[0]), t5 = __builtin_fmaf(cl_[16], nab[1], fl_[16]);
          const unsigned na = __builtin_bit_cast(unsigned, (float)nab[0]);
#pragma unroll
          for (int a = 0; a < 2; ++a) {
            const bool ma = a ? !m0 : m0;
            aq[a] = (mq && ma) ? na : aq[a];
#pragma unroll
            for (int e = 0; e < 4; ++e) {
              const bool me = mv && ma && e == (int)(sel0 & 3);
              acc[a][4][e] = me ? t4 : acc[a][4][e]; acc[a][5][e] = me ? t5 : acc[a][5][e];
              av[a][e] = me ? na : av[a][e];
            }
          }
        }
        constexpr float C2 = 1.4426950408889634f * 0.17677669529663687f;
#pragma unroll
        for (int a = 0; a < 2; ++a) {
          f32x2 a2 = {asf(aq[a]), asf(aq[a])};
          qs2[a] = f32x2{asf(aq[a]) * C2, asf(aq[a]) * C2};
          asm volatile("" : "+v"(a2), "+v"(qs2[a]));
          acc[a][2] *= f32x4{a2[0], a2[1], a2[0], a2[1]}; acc[a][3] *= f32x4{a2[0], a2[1], a2[0], a2[1]};
          acc[a][4] *= __builtin_bit_cast(f32x4, av[a]); acc[a][5] *= __builtin_bit_cast(f32x4, av[a]);
        }
        k0 = pack8(acc[0][2], acc[0][3]); k1 = pack8(acc[1][2], acc[1][3]); v0 = pack8(acc[0][4], acc[1][4]); v1 = pack8(acc[0][5], acc[1][5]);
        const unsigned kvo = OFF_KV + IT * (8u << 20) + (p * 32 + h) * 32768 + rt * 4096;
        if (p < 7) { st16(k0, rs, l * 16, kvo); st16(k1, rs, l * 16 + 1024, kvo); st16(v0, rs, l * 16 + 2048, kvo); st16(v1, rs, l * 16 + 3072, kvo); }
        char* ownp = smem + (8 + rt) * 2048 + l * 16;
        *(u32x4*)(ownp + L_K) = k0;
        if (rt < 7) *(u32x4*)(ownp + L_K + 1024) = k1;
        *(u32x4*)(ownp + L_V) = v0; *(u32x4*)(ownp + L_V + 1024) = v1;
        SB();
        static_for<32 - QL>([&](auto c_) __attribute__((always_inline)) {
          constexpr int s = QL + decltype(c_)::value;
#pragma unroll
          for (int n = 0; n < 2; ++n) { acc[0][n] = mfma16(qw[s - QL][n].v, af[s % WA][0], acc[0][n]); acc[1][n] = mfma16(qw[s - QL][n].v, af[s % WA][1], acc[1][n]); }
        });
        if (own) { patch_qk(0); patch_qk(1); }
      };
      qkv_loop(it_c);
    }
#undef A_LOAD1
#undef QKV_RDA
#undef QKV_RDB
#undef QKV_BO
    {
      IDS();
      const GAS bf16 *pwo, *pwg, *pwu;
      asm volatile("s_load_dwordx2 %0, %3, 24\n\ts_load_dwordx2 %1, %3, 40\n\ts_load_dwordx2 %2, %3, 48" : "=&s"(pwo), "=&s"(pwg), "=&s"(pwu) : "s"(ka));
      const int ql = l & 15;
      const f32x4 z4 = {0.f, 0.f, 0.f, 0.f}, ninf = {-INFINITY, -INFINITY, -INFINITY, -INFINITY};
      LDS_AS unsigned* rdy = (LDS_AS unsigned*)(smem + L_RDY);
      const unsigned rtag = T[0] + it;
      u32x4 qf[2];
      f32x4 cn[2], oa[2][2], sum[2], S[2][2];
      bool told = false;
      auto announce = [&](bool now) __attribute__((always_inline)) {
        if (!told) {
          unsigned st;
          asm volatile("s_getreg_b32 %0, hwreg(HW_REG_IB_STS)" : "=s"(st)::"memory");
          if (now || !(st & 0xC0000Fu)) {
            DRAIN();
            if (l == 0) st8(T, rs, rt * SKW, F_KV + (it * 256 + p * 32 + h) * SKV);
            told = true;
          }
        }
      };
      unsigned one2 = 0x3F803F80u;
      asm volatile("" : "+v"(one2));
      const u32x4 ones = {one2, one2, one2, one2};
      auto ex8 = [&](f32x4 a, f32x4 b) __attribute__((always_inline)) {
        return u32x4{pack2(__builtin_amdgcn_exp2f(a[0]), __builtin_amdgcn_exp2f(a[1])), pack2(__builtin_amdgcn_exp2f(a[2]), __builtin_amdgcn_exp2f(a[3])),
                     pack2(__builtin_amdgcn_exp2f(b[0]), __builtin_amdgcn_exp2f(b[1])), pack2(__builtin_amdgcn_exp2f(b[2]), __builtin_amdgcn_exp2f(b[3]))};
      };
      {
#pragma unroll
        for (int a = 0; a < 2; ++a) {
          const f32x4 c4 = {qs2[a][0], qs2[a][1], qs2[a][0], qs2[a][1]};
          qf[a] = pack8(acc[a][0] * c4, acc[a][1] * c4);
        }
        f32x4 mk;
#pragma unroll
        for (int e = 0; e < 4; ++e) mk[e] = (4 * g + e <= ql) ? 0.f : -INFINITY;
        const f32x4 d0 = mfma16(k0, qf[0], mk), d1 = mfma16(k1, qf[1], mk), d2 = mfma16(k0, qf[1], z4);
        const float m0 = -max_x32(max_x16(fmaxf(fmaxf(d0[0], d0[1]), fmaxf(d0[2], d0[3]))));
        const float m1 = -max_x32(max_x16(fmaxf(fmaxf(d1[0], d1[1]), fmaxf(d1[2], d1[3]))));
        cn[0] = f32x4{m0, m0, m0, m0}; cn[1] = f32x4{m1, m1, m1, m1};
        asm volatile("" : "+v"(cn[0]), "+v"(cn[1]));
        announce(false);
        const f32x4 e0 = d0 + cn[0];
        const u32x4 p0 = {pack2(__builtin_amdgcn_exp2f(e0[0]), __builtin_amdgcn_exp2f(e0[1])), pack2(__builtin_amdgcn_exp2f(e0[2]), __builtin_amdgcn_exp2f(e0[3])), 0u, 0u};
        const u32x4 p1 = ex8(d2 + cn[1], d1 + cn[1]);
        oa[0][0] = mfma16(v0, p0, z4); oa[0][1] = mfma16(v1, p0, z4); sum[0] = mfma16(ones, p0, z4);
        oa[1][0] = mfma16(v0, p1, z4); oa[1][1] = mfma16(v1, p1, z4); sum[1] = mfma16(ones, p1, z4);
      }
      const char* kb_ = smem + L_K + l * 16;
      const char* vb_ = smem + L_V + l * 16;
      const unsigned wov = WO_VOFF(), fnv = (l >> 1) * (F_ * 2) + (l & 1) * 16;
      asm volatile("" : "+v"(oa[0][0]), "+v"(oa[0][1]), "+v"(oa[1][0]), "+v"(oa[1][1]), "+v"(sum[0]), "+v"(sum[1]));
      const unsigned pw = __builtin_amdgcn_readfirstlane(it * 256 + p * 32 + h - 32), pfo = F_KV + pw * SKV + rt * SKW;
      const bool fetcher = rt < 4 && p > 0;
      announce(false);
      if (QPR) __builtin_amdgcn_s_setprio(0);
      BAR();
      asm volatile("" : "+s"(pwo), "+s"(pwg), "+s"(pwu));
#define KV_FETCH(gd)                                                                                                          \
  {                                                                                                                           \
    const unsigned pvo = OFF_KV + pw * 32768 + (gd) * 4096;                                                                   \
    dma16s<0>(rs, l * 16, pvo, lbase + L_K + (gd) * 2048); dma16s<1024>(rs, l * 16, pvo, lbase + L_K + (gd) * 2048);           \
    dma16s<0>(rs, l * 16, pvo + 2048, lbase + L_V + (gd) * 2048); dma16s<1024>(rs, l * 16, pvo + 2048, lbase + L_V + (gd) * 2048); \
  }
      auto fetch = [&]() __attribute__((always_inline)) {
        poll_flags(rs, (l & 1) * 4 * SKW, pfo, T);
        KV_FETCH(rt); KV_FETCH(rt + 4);
        if (l == 0) rdy[8 + rt] = rtag;
      };
      announce(false);
      const bool first = fetcher && told;
      if (first) fetch();
#define LDS16(a_) (*(const LDS_AS u32x4*)(size_t)(a_))
      u32x4 kf[2], vf[2], p0, p1;
#define AE(a, i) { S[a][(i) >> 2][(i) & 3] = __builtin_amdgcn_exp2f(S[a][(i) >> 2][(i) & 3]); asm volatile("" : "+v"(S[a][(i) >> 2][(i) & 3])); SB(); }
#define AC(a, P, j) { P[j] = pack2(S[a][(j) >> 1][2 * ((j) & 1)], S[a][(j) >> 1][2 * ((j) & 1) + 1]); asm volatile("" : "+v"(P[j])); SB(); }
#define PHASE(a, P, M1, M2, L2, M3, M4, L4, M5)                                                                               \
  AE(a, 0) AE(a, 1) M1; SB(); AE(a, 2) AE(a, 3) M2; SB(); L2; SB(); AC(a, P, 0) AC(a, P, 1) AE(a, 4) M3; SB(); AE(a, 5) AE(a, 6) M4; SB(); \
  L4; SB(); AE(a, 7) AC(a, P, 2) M5; SB(); AC(a, P, 3)
#define PV(a, d, P) oa[a][d] = mfma16(vf[d], P, oa[a][d])
#define RS(a, P) sum[a] = mfma16(ones, P, sum[a])
#define SC(a, t, c) S[a][t] = mfma16(kf[t], qf[a], c)
#define K_AT(ad) (kf[0] = LDS16(ad), kf[1] = LDS16((ad) + 1024))
#define V_AT(ad) (vf[0] = LDS16((ad) + L_V), vf[1] = LDS16((ad) + L_V + 1024))
#define A_FIRST(ad, c00, c01, c10, c11, KN) { K_AT(ad); V_AT(ad); SB(); SC(0, 0, c00); SC(0, 1, c01); SB(); PHASE(0, p0, SC(1, 0, c10), SC(1, 1, c11), KN, , , , ) }
#define A_NEXT(ad, c00, c01, c10, c11, KN)                                                                                        \
  { PHASE(1, p1, PV(0, 0, p0), PV(0, 1, p0), , SC(0, 0, c00), SC(0, 1, c01), , RS(0, p0))                                     \
    PHASE(0, p0, PV(1, 0, p1), PV(1, 1, p1), V_AT(ad), SC(1, 0, c10), SC(1, 1, c11), KN, RS(1, p1)) }
#define A_LAST() { PHASE(1, p1, PV(0, 0, p0), PV(0, 1, p0), , RS(0, p0), , , ) SB(); PV(1, 0, p1); PV(1, 1, p1); RS(1, p1); }
      int nw = 0;
      auto piece = [&]() __attribute__((always_inline)) {
        if constexpr (it == 0) { if (ffn_on && nw < 8) { FFN_DMA(nw); } ++nw; }
      };
#define XN_OFF() (F_XN + (((rt - 6) * 4 + g) * RX + bi % RX) * 256 + (l & 15) * 16)
      u32x4 fxn;
      asm volatile("" : "=v"(fxn));
      unsigned ad = lbase + l * 16 + (6 + rt) * 2048;
      asm volatile("" : "+v"(ad));
      if (rt > 0) {
        A_FIRST(ad + 2048, cn[0], cn[0], cn[1], cn[1], K_AT(ad));
#pragma unroll 1
        for (int i = rt - 1; i > 0; --i) {
          ad -= 2048;
          asm volatile("" : "+v"(ad));
          if (told && rt >= 4) piece();
          announce(false);
          A_NEXT(ad + 2048, cn[0], cn[0], cn[1], cn[1], K_AT(ad));
        }
        announce(false);
        A_LAST();
      }
      announce(true);
      {
        f32x4 mk;
#pragma unroll
        for (int e = 0; e < 4; ++e) mk[e] = (4 * g + e > ql) ? 0.f : -INFINITY;
        f32x4 cm0 = cn[0] + mk, cm1 = cn[1] + mk, cmi = ninf;
        asm volatile("" : "+v"(cm0), "+v"(cm1), "+v"(cmi));
        if (rt >= 4) {
          if (p > 0) {
            while (!__all(__hip_atomic_load(rdy + 8 + (l & 3), __ATOMIC_RELAXED, __HIP_MEMORY_SCOPE_WORKGROUP) == rtag)) __builtin_amdgcn_s_sleep(1);
            asm volatile("" ::: "memory");
          }
          if constexpr (it == 0) { for (; nw < 8;) piece(); }
          static_for<it ? 12 : 8>([&](auto c_) __attribute__((always_inline)) { constexpr int i = decltype(c_)::value; WO_PIECE_W(15 - i / 2, 2 * (rt & 3) + (i & 1)); });
          if (it == 0 && rt >= 6) ld16h<0>(fxn, rs, XN_OFF(), 0);
        } else if (p > 0) {
          if (!first) fetch();
          DRAIN();
          if (l < 2) rdy[rt + 4 * l] = rtag;
          if constexpr (it == 0) { for (; nw <= rt;) piece(); }
        }
        if (p > 0) {
          unsigned have = 0;
          auto need = [&](int gq) __attribute__((always_inline)) {
            if (__builtin_expect(gq < 8 && !((have >> gq) & 1), 0)) {
              do {
                const unsigned t = __hip_atomic_load(rdy + (l & 7), __ATOMIC_RELAXED, __HIP_MEMORY_SCOPE_WORKGROUP);
                have = (unsigned)__builtin_amdgcn_ballot_w64(t == rtag);
              } while (!((have >> gq) & 1));
              asm volatile("" ::: "memory");
            }
          };
          ad = lbase + l * 16 + rt * 2048;
          asm volatile("" : "+v"(ad));
          need(rt);
          K_AT(ad); V_AT(ad); SB(); SC(0, 0, cm0); SC(0, 1, cn[0]); SB();
          PHASE(0, p0, SC(1, 0, cmi), SC(1, 1, cm1), , , , , )
          need(rt + 1);
          K_AT(ad + 2048);
#pragma unroll 1
          for (int gq = rt + 1; gq < 8; ++gq) {
            ad += 2048;
            asm volatile("" : "+v"(ad));
            piece();
            need(gq + 1);
            A_NEXT(ad, cn[0], cn[0], cn[1], cn[1], K_AT(ad + 2048));
          }
          A_LAST();
        }
        if constexpr (it == 0) { for (; nw < 8;) piece(); }
      }
      {
        float big = 0.f;
#pragma unroll
        for (int a = 0; a < 2; ++a)
#pragma unroll
          for (int d = 0; d < 2; ++d)
            big = __builtin_elementwise_maximum(__builtin_elementwise_maximum(big, __builtin_elementwise_maximum(fabsf(oa[a][d][0]), fabsf(oa[a][d][1]))),
                                                __builtin_elementwise_maximum(fabsf(oa[a][d][2]), fabsf(oa[a][d][3])));
        bool ok = sum[0][0] >= 0.5f && sum[0][0] < 0x1p64f && sum[1][0] >= 0.5f && sum[1][0] < 0x1p64f && big < INFINITY;
        if (!__all(ok)) {
#pragma unroll
          for (int a = 0; a < 2; ++a) {
            const int qs = 2 * rt + a, qq = 256 + 16 * qs + ql, klo = max(qq - 255, p > 0 ? 0 : 256);
            auto score = [&](int t) __attribute__((always_inline)) {
              f32x4 s = mfma16(t == 31 ? k1 : *(const u32x4*)(kb_ + t * 1024), qf[a], z4);
#pragma unroll
              for (int e = 0; e < 4; ++e) { const int kk = 16 * t + 4 * g + e; s[e] = (kk <= qq && kk >= klo) ? s[e] : -INFINITY; }
              return s;
            };
            float mx = -INFINITY;
#pragma unroll 1
            for (int t = p > 0 ? qs : 16; t <= qs + 16; ++t) { const f32x4 s = score(t); mx = fmaxf(fmaxf(mx, fmaxf(s[0], s[1])), fmaxf(s[2], s[3])); }
            mx = max_x32(max_x16(mx));
            oa[a][0] = z4; oa[a][1] = z4; sum[a] = z4;
#pragma unroll 1
            for (int G = p > 0 ? qs >> 1 : 8; G <= (qs + 16) >> 1; ++G) {
              const f32x4 s0 = score(2 * G), s1 = score(2 * G + 1);
              f32x4 e0, e1;
#pragma unroll
              for (int e = 0; e < 4; ++e) { e0[e] = __builtin_amdgcn_exp2f(s0[e] - mx); e1[e] = __builtin_amdgcn_exp2f(s1[e] - mx); }
              const u32x4 pf = pack8(e0, e1);
              oa[a][0] = mfma16(*(const u32x4*)(vb_ + G * 2048), pf, oa[a][0]);
              oa[a][1] = mfma16(*(const u32x4*)(vb_ + G * 2048 + 1024), pf, oa[a][1]);
              sum[a] = mfma16(ones, pf, sum[a]);
            }
          }
        }
      }
#pragma unroll
      for (int a = 0; a < 2; ++a) {
        const float inv = __builtin_amdgcn_rcpf(sum[a][0]);
        oa[a][0] *= inv; oa[a][1] *= inv;
      }
      const unsigned oo = OFF_O + it * (4u << 20) + ((p * 8 + rt) * 64 + h * 2) * 1024;
      st16(pack8(oa[0][0], oa[0][1]), rs, l * 16, oo);
      st16(pack8(oa[1][0], oa[1][1]), rs, l * 16 + 1024, oo);
      if constexpr (it == 0) {
        wait_vm<2>();
        asm volatile("" : "+v"(fxn)::"memory");
        if (rt >= 6 && !__all(fxn[0] == T[0] && fxn[1] == T[1] && fxn[2] == T[0] && fxn[3] == T[1])) poll_flags2(rs, XN_OFF(), T);
        DEF4(gv); DEF4(uv);
        if (ffn_on) {
          const unsigned ro = 65536 + w * 1024 + (4 * g + ((l & 15) >> 2)) * 32 + (l & 3) * 8;
#pragma unroll
          for (int i = 0; i < 4; ++i) {
            frag_t f;
            f.hf.a = trread(smem, ro + i * 16384); f.hf.b = trread(smem, ro + i * 16384 + 512);
            gv[i] = f.v;
            f.hf.a = trread(smem, ro + i * 16384 + 8192); f.hf.b = trread(smem, ro + i * 16384 + 8704);
            uv[i] = f.v;
          }
        }
        PINV4(gv); PINV4(uv);
      }
      BAR();
      if constexpr (it == 1) { WO_PIECE(3); WO_PIECE(2); WO_PIECE(1); WO_PIECE(0); }
      wait_vm<it ? 4 : 0>();
      asm volatile("s_barrier" ::: "memory");
      if (tid == 0) st8(T, rs, h * SO, F_O + (it * 8 + p) * 32 * SO);
      if constexpr (it == 0) {
        WO_PIECE(11); WO_PIECE(10); WO_PIECE(9); WO_PIECE(8); WO_PIECE(7); WO_PIECE(6); WO_PIECE(5); WO_PIECE(4);
        WO_PIECE(3); WO_PIECE(2); WO_PIECE(1); WO_PIECE(0);
      }
    }
    IDS();
    ARGP(pwr, 4); ARGP(pwd, 7); ARGP(pout, 8); ARGP(px, 0);
    const int kh = w >> 2, rt2 = rb * 4 + (w & 3);
    const unsigned R = rt2 * 32 + kh * 16 + (l & 15);
    const unsigned to = (rt2 * 64 + ct * 4 + kh) * 1024 + l * 16;
    const unsigned rmo = ct * 64 + ((g & 1) ? 12 + 4 * g : 4 * g);
    f32x2 wv[8];
    {
      u32x4 wa[2], wrp[2], res[2];
#pragma unroll
      for (int j = 0; j < 2; ++j) {
        wrp[j] = *(const GAS u32x4*)(pwr + rmo + 32 * j);
        if constexpr (it == 0) wa[j] = *(const GAS u32x4*)(pwr + l * 8 + j * 512);
        if constexpr (it == 0) res[j] = *(const GAS u32x4*)(px + R * 1024 + rmo + 32 * j);
        else res[j] = ld16(rs, OFF_XNEW + (2 * ct + j) * 256 + g * 16, 0);
      }
      SB();
      unsigned bt[4];
#pragma unroll
      for (int n = 0; n < 4; ++n) {
        bt[n] = lbase + 32768 + 65536 + kh * 4096 + (4 * g + ((l & 15) >> 2)) * 128 + (l & 3) * 8 + ((n ^ (2 * (g & 1) + ((l & 15) >> 3))) << 5);
        asm volatile("" : "+v"(bt[n]));
      }
      if (w == 0) poll_flags(rs, (l & 31) * SO, F_O + (it * 8 + (rb >> 1)) * 32 * SO, T);
      asm volatile("s_barrier" ::: "memory");
      auto prep = [&]() __attribute__((always_inline)) {
        if constexpr (it == 0) {
          f32x2 t[8];
          unpack8v(wa[0], t); unpack8v(wa[1], t + 4);
          const f32x2 s = ((t[0] + t[1]) + (t[2] + t[3])) + ((t[4] + t[5]) + (t[6] + t[7]));
          wsum = sum64(hadd(s));
        }
        unpack8v(trade16(wrp[0]), wv); unpack8v(trade16(wrp[1]), wv + 4);
        if constexpr (it == 0) { res[0] = trade16(res[0]); res[1] = trade16(res[1]); }
        else { res[0] = R == sel0 ? res[0] : x2p[0]; res[1] = R == sel0 ? res[1] : x2p[1]; }
        unpack8v(res[0], v + 0); unpack8v(res[1], v + 4);
        asm volatile("" : "+v"(v[0]), "+v"(v[1]), "+v"(v[2]), "+v"(v[3]), "+v"(v[4]), "+v"(v[5]), "+v"(v[6]), "+v"(v[7]));
        asm volatile("" : "+v"(wsum), "+v"(wv[0]), "+v"(wv[1]), "+v"(wv[2]), "+v"(wv[3]), "+v"(wv[4]), "+v"(wv[5]), "+v"(wv[6]), "+v"(wv[7]));
      };
      if constexpr (it == 1 && WON) {
        const unsigned abase = OFF_O + (4u << 20) + (rt2 * 64 + kh) * 1024;
        u32x4 af[W2B], bfr[W2L + 1][4];
        unsigned b2[2] = {lbase + 32768 + l * 16, lbase + 98304 + l * 16};
        asm volatile("" : "+v"(b2[0]), "+v"(b2[1]));
#define O_LOAD(s_) af[(s_) % W2B] = ld16(rs, l * 16 + (~(s_) & 1) * 2048, abase + ((31 - (s_)) >> 1) * 4096)
#define WO_RD(s_, n) bfr[(s_) % (W2L + 1)][n] = *(const LDS_AS u32x4*)(size_t)(b2[(31 - (s_)) >> 4] + ((31 - (s_)) & 15) * 4096 + (n) * 1024)
#pragma unroll
        for (int s = 0; s < W2R; ++s) O_LOAD(s);
        SB();
        prep();
        SB();
        f32x4 ac[4];
#pragma unroll
        for (int n = 0; n < 4; ++n) ac[n] = f32x4{v[2 * n][0], v[2 * n][1], v[2 * n + 1][0], v[2 * n + 1][1]};
#pragma unroll
        for (int n = 0; n < 4 * W2L; ++n) WO_RD(n >> 2, n & 3);
#pragma unroll
        for (int s = 0; s < 32; ++s) {
          if (s == W2P && w >= 4) __builtin_amdgcn_s_setprio(1);
          if (s == 24 - W2L) asm volatile("s_barrier" ::: "memory");
          if (s < W2B - W2R) O_LOAD(W2R + 2 * s);
          SB();
#pragma unroll
          for (int n = 0; n < 4; ++n) {
            ac[n] = mfma16(bfr[s % (W2L + 1)][n], af[s % W2B], ac[n]);
            if (s + W2L < 32) WO_RD(s + W2L, n);
            SB();
          }
          if (s < W2B - W2R) O_LOAD(W2R + 2 * s + 1);
          else if (s + W2B < 32) O_LOAD(s + W2B);
          SB();
        }
        __builtin_amdgcn_s_setprio(0);
#undef WO_RD
#undef O_LOAD
#pragma unroll
        for (int n = 0; n < 4; ++n) { v[2 * n] = f32x2{ac[n][0], ac[n][1]}; v[2 * n + 1] = f32x2{ac[n][2], ac[n][3]}; }
      } else {
      const unsigned abase = OFF_O + it * (4u << 20) + (rt2 * 64 + 32 * kh) * 1024;
      const unsigned av0 = l * 16 + kh * 1024, av1 = l * 16 + (kh ^ 1) * 1024;
      u32x4 af[WB][2];
#define O_LOAD(s_) { af[(s_) % WB][0] = ld16(rs, av0 + (~(s_) & 1) * 2048, abase + ((15 - (s_)) >> 1) * 4096); af[(s_) % WB][1] = ld16(rs, av1 + (~(s_) & 1) * 2048, abase + ((15 - (s_)) >> 1) * 4096); }
#pragma unroll
      for (int s = 0; s < WR; ++s) O_LOAD(s);
      SB();
      prep();
      SB();
      f32x4 ac[2][4];
#pragma unroll
      for (int a = 0; a < 2; ++a)
#pragma unroll
        for (int n = 0; n < 4; ++n) ac[a][n] = f32x4{0.f, 0.f, 0.f, 0.f};
      u32x4 bfr[WLA + 1][4];
#define WO_RD(buf, s, n)                                                                             \
  {                                                                                                  \
    const unsigned o_ = bt[n] + ((15 - (s)) & 7) * 8192;     \
    frag_t f_;                                                                                       \
    f_.hf.a = trabs(o_); f_.hf.b = trabs(o_ + 2048);                                                 \
    bfr[buf][n] = f_.v;                                                                              \
  }
#pragma unroll
      for (int n = 0; n < 4 * WLA; ++n) WO_RD(n >> 2, n >> 2, n & 3);
#pragma unroll
      for (int s = 0; s < 16; ++s) {
        const int cur = s % (WLA + 1);
        if (WPR && s == WPR && w >= 4) __builtin_amdgcn_s_setprio(1);
        asm volatile("" : "+{v[176:179]}"(ac[0][0]), "+{v[180:183]}"(ac[0][1]), "+{v[184:187]}"(ac[0][2]), "+{v[188:191]}"(ac[0][3]),
                          "+{v[192:195]}"(ac[1][0]), "+{v[196:199]}"(ac[1][1]), "+{v[200:203]}"(ac[1][2]), "+{v[204:207]}"(ac[1][3]));
        if (s == (it == 0 ? 4 : 12) - WLA) asm volatile("s_barrier" ::: "memory");
        if (s == 8 - WLA) {
#pragma unroll
          for (int n = 0; n < 4; ++n) { bt[n] -= 65536; asm volatile("" : "+v"(bt[n])); }
        }
        if (it == 0 && s == WIS) {
#pragma unroll
          for (int i = 0; i < 3; ++i) st16(keep[i], rs, l * 16 + i * 1024, OFF_WI + (h * 8 + ((ROT0 + 5) & 7)) * STAGE + w * 3072);
        }
        if (it == 0 && s == WPS) {
          unsigned pz1 = POISON;
          asm volatile("" : "+v"(pz1));
          const u32x4 pz = {pz1, pz1, pz1, pz1};
          st16(pz, rs, OFF_PART + (tid >> 8) * (1u << 20) + bid * 4096 + (tid & 255) * 16, 0);
          if (w < 2) st16(pz, rs, OFF_STAT + 524288 + (rb * 16 + ct) * 2048 + tid * 16, 0);
          if (w == 4 && l < 40) st16(pz, rs, l < 16 ? OFF_CA + 65536 + rb * 4096 + ct * 256 + l * 16 : OFF_FX + (h * 8 + p) * 512 + (l - 16) * 16, 0);
          if (w == 6 && l < (ct < (int)RKEY ? 3 : 2)) st4(pz1, rs, l < 2 ? OFF_XNEW + (bid >> 3) * 256 + (bid & 7) * 8 + l * 4 : KEY_AT(1, ct) + rb * SKEY, 0);
        }
        if (s < WB - WR) O_LOAD(WR + 2 * s < 16 ? WR + 2 * s : 0);
        SB();
#pragma unroll
        for (int n = 0; n < 4; ++n) {
          ac[0][n] = mfma16(bfr[cur][n], af[s % WB][0], ac[0][n]);
          ac[1][n] = mfma16(bfr[cur][n], af[s % WB][1], ac[1][n]);
          if (s + WLA < 16) WO_RD((s + WLA) % (WLA + 1), s + WLA, n);
          SB();
        }
        if (s < WB - WR) { O_LOAD(WR + 2 * s + 1); }
        else if (s + WB < 16) { O_LOAD(s + WB); }
        SB();
      }
      if (WPR) __builtin_amdgcn_s_setprio(0);
#undef WO_RD
#undef O_LOAD
      if constexpr (it == 0 && WON) {
        const int ks = 2 * rb + kh;
        const unsigned o_ = lbase + 32768 + (ks & 15) * 8192 + (ks >> 4) * 4096 + (4 * g + ((l & 15) >> 2)) * 128 + (l & 3) * 8 + (((w & 3) ^ (2 * (g & 1) + ((l & 15) >> 3))) << 5);
        frag_t f_;
        f_.hf.a = trabs(o_); f_.hf.b = trabs(o_ + 2048);
        wof = f_.v;
        asm volatile("" : "+v"(wof));
      }
#define XCH_AT(v) ((v) == 7 ? (rb == 7 ? 139264 : 155648) : (v) * 4096)
#pragma unroll
      for (int n = 0; n < 4; ++n) *(f32x4*)(smem + XCH_AT(w) + n * 1024 + l * 16) = ac[1][n];
#pragma unroll
      for (int n = 0; n < 4; ++n) { v[2 * n] += f32x2{ac[0][n][0], ac[0][n][1]}; v[2 * n + 1] += f32x2{ac[0][n][2], ac[0][n][3]}; }
      BAR();
#pragma unroll
      for (int n = 0; n < 4; ++n) {
        const f32x4 o = *(const f32x4*)(smem + XCH_AT(w ^ 4) + n * 1024 + l * 16);
        v[2 * n] += f32x2{o[0], o[1]}; v[2 * n + 1] += f32x2{o[2], o[3]};
      }
      }
    }
    {
      if (it == 0) { st16(pack8v(v), rs, to, OFF_XN + (4u << 20)); st16(pack8v(v + 4), rs, to + 2048, OFF_XN + (4u << 20)); }
      SB();
      const f32x2 s = ((v[0] + v[1]) + (v[2] + v[3])) + ((v[4] + v[5]) + (v[6] + v[7]));
      f32x2 dp = v[0] * wv[0], q = v[0] * v[0];
#pragma unroll
      for (int j = 1; j < 8; ++j) { dp = __builtin_elementwise_fma(v[j], wv[j], dp); q = __builtin_elementwise_fma(v[j], v[j], q); }
      float mt = hadd(s), qq = hadd(q), dot = hadd(dp);
      add_g3(mt, qq, dot);
      const unsigned so = OFF_STAT + it * 524288 + rb * 32768, lrow = R & 127;
      if (it == 0) wait_vm<2>();
      if (g == 0) st16(unpoison4(__builtin_bit_cast(u32x4, f32x4{mt, qq, dot, 0.f})), rs, ct * 2048 + lrow * 16, so);
      SB();
      if (it == 0) wait_vm<1>();
      if constexpr (it == 0) {
        volatile LDS_AS u32x2* in = (volatile LDS_AS u32x2*)(smem + L_XC);
        if (l == 0) in[w] = T;
        const u32x2 t = in[l & 7];
        if (__all(t[0] == T[0] && t[1] == T[1]) && l < (int)RX1) st8(T, rs, (l * 32 + (rb & 1) * 16 + ct) * SX1, F_X1 + (rb >> 1) * RX1 * 32 * SX1);
      }
      if (it == 0) {
        if (ffn_on) { WD_DMA(); }
#pragma unroll
        for (int j = 0; j < 9; ++j) *(u32x4*)(smem + L_RING + (j / 3) * STAGE + (w * 3 + j % 3) * 1024 + l * 16) = keep[j];
      }
      if constexpr (it == 0) {
        if (w == 0) poll_flags(rs, ((l & 2) * 8 + ((ROT0 + 5) & 7) * 2 + (l & 1)) * SX1, F_X1 + (p * RX1 + (xcd >> 1) % RX1) * 32 * SX1, T);
        if (tid == 192) *(LDS_AS u32x4*)(smem + L_QC) = u32x4{0u, 0u, 0u, 0u};
        BAR();
        PRE_LOAD(1, ROT0 + 5, 0, NPRE);
      } else {
        __builtin_amdgcn_s_sleep(SDIR);
        u32x4 sp[4];
#pragma unroll
        for (int i = 0; i < 4; ++i) sp[i] = ld16(rs, (4 * g + i) * 2048 + lrow * 16, so);
        const RowFin r = fin_key(rs, T, g * 8192 + lrow * 16, so, smem + L_XC, l, w, R, wsum, sp);
        BAR();
        fin_cand(rs, smem + L_XC, 1, g, rb, ct, r, v);
        fin_x2(r, v);
      }
    }
    if (it == 1) {
      u32x4 op[2];
      {
        char* tp = smem + L_OUT + w * 2048;
        const unsigned c0 = (g & 1) * 2 + (g >> 1), r = l & 15;
        *(u32x4*)(tp + r * 128 + ((c0 ^ (r & 7)) << 4)) = trade16(pack8v(v));
        *(u32x4*)(tp + r * 128 + (((c0 + 4) ^ (r & 7)) << 4)) = trade16(pack8v(v + 4));
#pragma unroll
        for (int j = 0; j < 2; ++j) op[j] = *(const u32x4*)(tp + j * 1024 + (l >> 3) * 128 + (((l ^ (l >> 3)) & 7) << 4));
      }
      const unsigned Ro = R - (l & 15) + (l >> 3), oo = (Ro * 1024 + ct * 64) * 2 + (l & 7) * 16;
#define OUT_STORE(c0, c1)                                                                           \
  {                                                                                                 \
    if (c0) __builtin_amdgcn_raw_buffer_store_b128(op[0], rout, oo, 0, SC1);                        \
    if (c1) __builtin_amdgcn_raw_buffer_store_b128(op[1], rout, oo + 16384, 0, SC1);                \
  }
      if (w == 0) {
        __builtin_amdgcn_s_sleep(GSL);
        u32x4 xr[4];
        unsigned cur = 0, best;
        asm volatile("" : "=v"(xr[0]), "=v"(xr[1]));
        for (unsigned spins = 0;;) {
          const unsigned k_ = key_load(rs, l, 1, KREP());
          const bool all = __all(k_ != POISON);
          best = __builtin_amdgcn_readfirstlane(umax16(k_ == POISON ? 0u : k_));
          if (best != cur && ffn_on) {
            cur = best;
            ld16h<0>(xr[0], rs, (l >> 3) * 256 + (l & 7) * 16, cand_off(cur, 1)); ld16h<2048>(xr[1], rs, (l >> 3) * 256 + (l & 7) * 16, cand_off(cur, 1));
          }
          if (all || spin_fail(spins, rs, T)) break;
        }
        if (l == 0) *(LDS_AS unsigned*)(smem + L_SKEY + 32) = best;
        asm volatile("s_waitcnt vmcnt(0)" : "+v"(xr[0]), "+v"(xr[1])::"memory");
        if (ffn_on) {
          word_wait<2>(rs, T, (l >> 3) * 256 + (l & 7) * 16, cand_off(best, 1), xr);
          *(LDS_AS u32x4*)(smem + L_ROW + l * 16) = xr[0]; *(LDS_AS u32x4*)(smem + L_ROW + 1024 + l * 16) = xr[1];
        }
      }
      BAR();
      const unsigned key = __builtin_amdgcn_readfirstlane(*(LDS_AS unsigned*)(smem + L_SKEY + 32)), krow = key_row(key);
      if (ffn_on) {
        u32x4 xv[4];
#pragma unroll
        for (int i = 0; i < 4; ++i) xv[i] = *(const LDS_AS u32x4*)(smem + L_ROW + w * 256 + i * 64 + g * 16);
        f32x4 ga, ua;
        ffn_gu(gv, uv, xv, ga, ua);
        ffn_gu_store(smem, l, w, ga, ua);
        BAR();
        ffn_d_store(rs, l, w, 1, slice, ffn_d_all(dfr, ffn_d_h(ffn_d_read(smem, l))));
        SB();
      }
      OUT_STORE(Ro != krow, Ro + 8 != krow);
      if (!ffn_on) {
        const unsigned G = xcd * 4 + bi - 28;
        if (G == 0 && tid == 0) st8(T, rs, F_EP, 0);
        u32x4 pv[4];
        u32x2 cxn, cx1;
        const float sig = red_gate(key);
        __builtin_amdgcn_s_sleep(RSL);
        red_load(rs, tid, 1, G, key, pv, cxn, cx1);
        red_sum(rs, smem, T, tid, w, 1, G, key, pv, cxn, cx1);
        BAR();
        if (w == 0) {
          const u32x2 ov = red_final(smem, l, sig, cxn, cx1);
          if (l < 8) __builtin_amdgcn_raw_buffer_store_b64(ov, rout, (krow * 1024 + G * 32 + l * 4) * 2, 0, 0);
        }
      }
#undef OUT_STORE
    }
  };
  layer(std::integral_constant<int, 0>{});
  layer(std::integral_constant<int, 1>{});
}

extern "C" void kernel_launch(void* const* d_in, const int* in_sizes, int n_in, void* d_out, int out_size,
                              void* d_ws, size_t ws_size, hipStream_t stream) {
  (void)hipFuncSetAttribute((const void*)mega_kernel, hipFuncAttributeMaxDynamicSharedMemorySize, LDS_BYTES);
  mega_kernel<<<256, 512, LDS_BYTES, stream>>>((const bf16*)d_in[0], (char*)d_ws, (const bf16*)d_in[1], (const bf16*)d_in[2], (const bf16*)d_in[4],
                                               (const bf16*)d_in[5], (const bf16*)d_in[6], (const bf16*)d_in[7], (bf16*)d_out);
}
```

```cpp
#include <hip/hip_runtime.h>
#include <stdint.h>
#include <utility>

typedef __bf16 bf16;
typedef __bf16 bf16x8 __attribute__((ext_vector_type(8)));
typedef __bf16 bf16x2 __attribute__((ext_vector_type(2)));
typedef float f32x4 __attribute__((ext_vector_type(4)));
typedef float f32x2 __attribute__((ext_vector_type(2)));
typedef short s16x4 __attribute__((ext_vector_type(4)));
typedef unsigned int u32x4 __attribute__((ext_vector_type(4)));
typedef unsigned int u32x2 __attribute__((ext_vector_type(2)));
#define LDS_AS __attribute__((address_space(3)))
#define GAS __attribute__((address_space(1)))
typedef __amdgpu_buffer_rsrc_t rsrc_t;

constexpr int F_ = 3584;
constexpr int SC1 = 16;

constexpr int RX = 4;
constexpr unsigned F_EP = 0, F_TMO = 128;
constexpr unsigned F_XN = 4096;
#define FLG 256, 1024, 8, 64, 4, 256, 4
constexpr unsigned FLV[7] = {FLG};
constexpr unsigned SO = FLV[0], SKV = FLV[1], SKW = FLV[2], SX1 = FLV[3], RX1 = FLV[4], SKEY = FLV[5], RKEY = FLV[6];
constexpr unsigned F_O = 8u << 20;
constexpr unsigned F_KV = 11u << 20;
constexpr unsigned F_WOI = F_O + 262144;
constexpr unsigned F_X1 = 9u << 20;
constexpr unsigned OFF_KEY = 10u << 20;
#define KREP() (RKEY == 16 ? bid & 15 : (bid & 7) * RKEY / 8)
#define KEY_AT(L, rep) (OFF_KEY + ((L) * RKEY + (rep)) * (SKEY < 8 ? 128 : 16 * SKEY))
constexpr unsigned OFF_AB = 2u << 20;
constexpr unsigned OFF_XNEW = 262144;
constexpr unsigned OFF_CS = OFF_AB + 65536;
constexpr unsigned OFF_FX = 393216;
constexpr unsigned OFF_CA = 524288;
constexpr unsigned OFF_STAT = 1u << 20;
constexpr unsigned OFF_PART = 3u << 20;
constexpr unsigned OFF_XN = 8u << 20;
constexpr unsigned OFF_O = 16u << 20;
constexpr unsigned OFF_KV = 32u << 20;
constexpr unsigned OFF_WI = 52u << 20;
constexpr unsigned OFF_WOI = 58u << 20;
constexpr unsigned WS_BYTES = 60u << 20;
constexpr unsigned POISON = 0xFFFFFFFFu;

constexpr int L_ABW = 16640, L_CSP = 17408, L_CSL = 31872, L_FXL = 32256, L_K = 0, L_V = 32768, L_RING = 65536, L_DN = 32768, L_RDY = 31744, L_QC = 31808, LDS_BYTES = 163840;
constexpr int STAGE = 24576;
#define SLOT(t) ((t) & 3)

template <typename F, int... I> __device__ __forceinline__ void static_for_impl(F&& f, std::integer_sequence<int, I...>) {
  (f(std::integral_constant<int, I>{}), ...);
}
template <int N, typename F> __device__ __forceinline__ void static_for(F&& f) { static_for_impl(f, std::make_integer_sequence<int, N>{}); }
__device__ __forceinline__ unsigned pack2(float a, float b) {
  f32x2 v = {a, b};
  return __builtin_bit_cast(unsigned, __builtin_convertvector(v, bf16x2));
}
__device__ __forceinline__ float asf(unsigned u) { return __builtin_bit_cast(float, u); }
__device__ __forceinline__ float lo16(unsigned u) { return __builtin_bit_cast(float, u << 16); }
__device__ __forceinline__ float hi16(unsigned u) { return __builtin_bit_cast(float, u & 0xffff0000u); }
__device__ __forceinline__ u32x4 pack8(const f32x4 a, const f32x4 b) {
  u32x4 o;
  o[0] = pack2(a[0], a[1]); o[1] = pack2(a[2], a[3]); o[2] = pack2(b[0], b[1]); o[3] = pack2(b[2], b[3]);
  return o;
}
__device__ __forceinline__ f32x4 mfma16(u32x4 a, u32x4 b, f32x4 c) {
  return __builtin_amdgcn_mfma_f32_16x16x32_bf16(__builtin_bit_cast(bf16x8, a), __builtin_bit_cast(bf16x8, b), c, 0, 0, 0);
}
__device__ __forceinline__ f32x4 mfma16k16(s16x4 a, s16x4 b, f32x4 c) { return __builtin_amdgcn_mfma_f32_16x16x16bf16_1k(a, b, c, 0, 0, 0); }
template <int CTRL> __device__ __forceinline__ float dppf(float v) {
  return __builtin_bit_cast(float, __builtin_amdgcn_update_dpp(0, __builtin_bit_cast(int, v), CTRL, 0xf, 0xf, false));
}
template <int CTRL> __device__ __forceinline__ unsigned dppu(unsigned v) {
  return (unsigned)__builtin_amdgcn_update_dpp(0, (int)v, CTRL, 0xf, 0xf, false);
}
__device__ __forceinline__ float add_x16(float v) {
  auto r = __builtin_amdgcn_permlane16_swap(__builtin_bit_cast(unsigned, v), __builtin_bit_cast(unsigned, v), false, false);
  return __builtin_bit_cast(float, (unsigned)r[0]) + __builtin_bit_cast(float, (unsigned)r[1]);
}
__device__ __forceinline__ float add_x32(float v) {
  auto r = __builtin_amdgcn_permlane32_swap(__builtin_bit_cast(unsigned, v), __builtin_bit_cast(unsigned, v), false, false);
  return __builtin_bit_cast(float, (unsigned)r[0]) + __builtin_bit_cast(float, (unsigned)r[1]);
}
__device__ __forceinline__ float max_x16(float v) {
  auto r = __builtin_amdgcn_permlane16_swap(__builtin_bit_cast(unsigned, v), __builtin_bit_cast(unsigned, v), false, false);
  return fmaxf(__builtin_bit_cast(float, (unsigned)r[0]), __builtin_bit_cast(float, (unsigned)r[1]));
}
__device__ __forceinline__ float max_x32(float v) {
  auto r = __builtin_amdgcn_permlane32_swap(__builtin_bit_cast(unsigned, v), __builtin_bit_cast(unsigned, v), false, false);
  return fmaxf(__builtin_bit_cast(float, (unsigned)r[0]), __builtin_bit_cast(float, (unsigned)r[1]));
}
__device__ __forceinline__ float sum_hi8(float v) {
  v += dppf<0x128>(v);
  v = add_x16(v);
  return add_x32(v);
}
__device__ __forceinline__ float sum64(float v) {
  v += dppf<0xB1>(v);
  v += dppf<0x4E>(v);
  v += dppf<0x124>(v);
  v += dppf<0x128>(v);
  v = add_x16(v);
  return add_x32(v);
}

#define BAR() asm volatile("s_waitcnt lgkmcnt(0)\n\ts_barrier" ::: "memory")
#define DRAIN() asm volatile("s_waitcnt vmcnt(0)" ::: "memory")
#define SB() __builtin_amdgcn_sched_barrier(0)
template <int N> __device__ __forceinline__ void wait_vm() { asm volatile("s_waitcnt vmcnt(%0)" ::"n"(N) : "memory"); }

__device__ __forceinline__ void dma16(rsrc_t rs, unsigned voff, unsigned soff, unsigned lds) {
  asm volatile("s_mov_b32 m0, %0\n\ts_nop 0\n\tbuffer_load_dwordx4 %1, %2, %3 offen lds" ::"s"(lds), "v"(voff), "s"(rs), "s"(soff) : "memory");
}
__device__ __forceinline__ void dma16n(rsrc_t rs, unsigned voff, unsigned soff, unsigned lds) {
  asm volatile("s_mov_b32 m0, %0\n\ts_nop 0\n\tbuffer_load_dwordx4 %1, %2, %3 offen nt lds" ::"s"(lds), "v"(voff), "s"(rs), "s"(soff) : "memory");
}
template <int OFF> __device__ __forceinline__ void dma16s(rsrc_t rs, unsigned voff, unsigned soff, unsigned lds) {
  asm volatile("s_mov_b32 m0, %0\n\ts_nop 0\n\tbuffer_load_dwordx4 %1, %2, %3 offen offset:%4 sc1 lds" ::"s"(lds), "v"(voff), "s"(rs), "s"(soff), "n"(OFF) : "memory");
}
__device__ __forceinline__ float rbf1(float x) { return __builtin_bit_cast(float, pack2(0.f, x)); }
__device__ __forceinline__ u32x4 ld16(rsrc_t rs, unsigned voff, unsigned soff) { return __builtin_amdgcn_raw_buffer_load_b128(rs, voff, soff, SC1); }
template <int OFF> __device__ __forceinline__ void ld16h(u32x4& d, rsrc_t rs, unsigned voff, unsigned soff) {
  asm volatile("buffer_load_dwordx4 %0, %1, %2, %3 offen offset:%4 sc1" : "=v"(d) : "v"(voff), "s"(rs), "s"(soff), "n"(OFF) : "memory");
}
template <int I, int OFF> __device__ __forceinline__ void ld16w(u32x4& d, rsrc_t rs, unsigned voff, unsigned soff) {
  if constexpr (I == 0) asm volatile("buffer_load_dwordx4 %0, %1, %2, %3 offen offset:%4 sc1" : "={v[128:131]}"(d) : "v"(voff), "s"(rs), "s"(soff), "n"(OFF) : "memory");
  else if constexpr (I == 1) asm volatile("buffer_load_dwordx4 %0, %1, %2, %3 offen offset:%4 sc1" : "={v[132:135]}"(d) : "v"(voff), "s"(rs), "s"(soff), "n"(OFF) : "memory");
  else if constexpr (I == 2) asm volatile("buffer_load_dwordx4 %0, %1, %2, %3 offen offset:%4 sc1" : "={v[136:139]}"(d) : "v"(voff), "s"(rs), "s"(soff), "n"(OFF) : "memory");
  else if constexpr (I == 3) asm volatile("buffer_load_dwordx4 %0, %1, %2, %3 offen offset:%4 sc1" : "={v[140:143]}"(d) : "v"(voff), "s"(rs), "s"(soff), "n"(OFF) : "memory");
  else if constexpr (I == 4) asm volatile("buffer_load_dwordx4 %0, %1, %2, %3 offen offset:%4 sc1" : "={v[144:147]}"(d) : "v"(voff), "s"(rs), "s"(soff), "n"(OFF) : "memory");
  else if constexpr (I == 5) asm volatile("buffer_load_dwordx4 %0, %1, %2, %3 offen offset:%4 sc1" : "={v[148:151]}"(d) : "v"(voff), "s"(rs), "s"(soff), "n"(OFF) : "memory");
  else if constexpr (I == 6) asm volatile("buffer_load_dwordx4 %0, %1, %2, %3 offen offset:%4 sc1" : "={v[152:155]}"(d) : "v"(voff), "s"(rs), "s"(soff), "n"(OFF) : "memory");
  else if constexpr (I == 7) asm volatile("buffer_load_dwordx4 %0, %1, %2, %3 offen offset:%4 sc1" : "={v[156:159]}"(d) : "v"(voff), "s"(rs), "s"(soff), "n"(OFF) : "memory");
  else if constexpr (I == 8) asm volatile("buffer_load_dwordx4 %0, %1, %2, %3 offen offset:%4 sc1" : "={v[160:163]}"(d) : "v"(voff), "s"(rs), "s"(soff), "n"(OFF) : "memory");
  else if constexpr (I == 9) asm volatile("buffer_load_dwordx4 %0, %1, %2, %3 offen offset:%4 sc1" : "={v[164:167]}"(d) : "v"(voff), "s"(rs), "s"(soff), "n"(OFF) : "memory");
  else if constexpr (I == 10) asm volatile("buffer_load_dwordx4 %0, %1, %2, %3 offen offset:%4 sc1" : "={v[168:171]}"(d) : "v"(voff), "s"(rs), "s"(soff), "n"(OFF) : "memory");
  else if constexpr (I == 11) asm volatile("buffer_load_dwordx4 %0, %1, %2, %3 offen offset:%4 sc1" : "={v[172:175]}"(d) : "v"(voff), "s"(rs), "s"(soff), "n"(OFF) : "memory");
}
template <int K> __device__ __forceinline__ void pin_w(u32x4& a, u32x4& b) {
  if constexpr (K == 0) asm volatile("" : "+{v[128:131]}"(a), "+{v[132:135]}"(b)::"memory");
  else if constexpr (K == 1) asm volatile("" : "+{v[136:139]}"(a), "+{v[140:143]}"(b)::"memory");
  else if constexpr (K == 2) asm volatile("" : "+{v[144:147]}"(a), "+{v[148:151]}"(b)::"memory");
  else if constexpr (K == 3) asm volatile("" : "+{v[152:155]}"(a), "+{v[156:159]}"(b)::"memory");
  else if constexpr (K == 4) asm volatile("" : "+{v[160:163]}"(a), "+{v[164:167]}"(b)::"memory");
  else if constexpr (K == 5) asm volatile("" : "+{v[168:171]}"(a), "+{v[172:175]}"(b)::"memory");
}
__device__ __forceinline__ void ld8h(u32x2& d, rsrc_t rs, unsigned voff, unsigned soff) {
  asm volatile("buffer_load_dwordx2 %0, %1, %2, %3 offen sc1" : "=v"(d) : "v"(voff), "s"(rs), "s"(soff) : "memory");
}
template <int OFF> __device__ __forceinline__ void ld4h(unsigned& d, rsrc_t rs, unsigned voff, unsigned soff) {
  asm volatile("buffer_load_dword %0, %1, %2, %3 offen offset:%4 sc1" : "=v"(d) : "v"(voff), "s"(rs), "s"(soff), "n"(OFF) : "memory");
}
__device__ __forceinline__ u32x2 ld8(rsrc_t rs, unsigned voff, unsigned soff) { return __builtin_amdgcn_raw_buffer_load_b64(rs, voff, soff, SC1); }
__device__ __forceinline__ void st16(u32x4 v, rsrc_t rs, unsigned voff, unsigned soff) { __builtin_amdgcn_raw_buffer_store_b128(v, rs, voff + soff, 0, SC1); }
__device__ __forceinline__ void st8(u32x2 v, rsrc_t rs, unsigned voff, unsigned soff) { __builtin_amdgcn_raw_buffer_store_b64(v, rs, voff, soff, SC1); }

__device__ __forceinline__ unsigned ld4(rsrc_t rs, unsigned voff, unsigned soff) { return __builtin_amdgcn_raw_buffer_load_b32(rs, voff, soff, SC1); }
__device__ __forceinline__ void st4(unsigned v, rsrc_t rs, unsigned voff, unsigned soff) { __builtin_amdgcn_raw_buffer_store_b32(v, rs, voff, soff, SC1); }

constexpr unsigned kSpinCap = 1u << 16;
__device__ __forceinline__ bool spin_fail(unsigned& spins, rsrc_t rs, u32x2 T) {
  asm volatile("" ::: "memory");
  ++spins;
  if (spins > kSpinCap) { st8(T, rs, 0, F_TMO); return true; }
  if ((spins & 255u) == 0u) {
    const u32x2 v = ld8(rs, 0, F_TMO);
    if (v[0] == T[0] && v[1] == T[1]) return true;
  }
  __builtin_amdgcn_s_sleep(3);
  return false;
}
__device__ __forceinline__ void poll_flags2(rsrc_t rs, unsigned voff, u32x2 T) {
  for (unsigned spins = 0;;) {
    const u32x4 v = ld16(rs, voff, 0);
    if (__all(v[0] == T[0] && v[1] == T[1] && v[2] == T[0] && v[3] == T[1])) break;
    if (spin_fail(spins, rs, T)) break;
  }
  asm volatile("" ::: "memory");
  __builtin_amdgcn_fence(__ATOMIC_ACQUIRE, "wavefront");
}
__device__ __forceinline__ void tag_wait(rsrc_t rs, u32x2 T, unsigned voff, unsigned soff, u32x4& v) {
  if (__builtin_expect(!__all(v[2] == T[0] && v[3] == T[1]), 0)) {
    unsigned spins = 0;
    do {
      if (spin_fail(spins, rs, T)) break;
      v = ld16(rs, voff, soff);
    } while (!__all(v[2] == T[0] && v[3] == T[1]));
    asm volatile("s_waitcnt vmcnt(0)" : "+v"(v)::"memory");
  }
}
template <int N> __device__ __forceinline__ void word_wait(rsrc_t rs, u32x2 T, unsigned voff, unsigned soff, u32x4 (&v)[4]) {
#define WW_OK() __all(max(max(max(v[0][0], v[0][1]), max(v[0][2], v[0][3])), max(max(v[N - 1][0], v[N - 1][1]), max(v[N - 1][2], v[N - 1][3]))) != POISON)
  if (__builtin_expect(!WW_OK(), 0)) {
    unsigned spins = 0;
    do {
      if (spin_fail(spins, rs, T)) break;
#pragma unroll
      for (int i = 0; i < N; ++i) v[i] = ld16(rs, voff + i * 2048, soff);
    } while (!WW_OK());
    asm volatile("s_waitcnt vmcnt(0)" : "+v"(v[0]), "+v"(v[N - 1])::"memory");
  }
#undef WW_OK
}
__device__ __forceinline__ void poll_flags(rsrc_t rs, unsigned voff, unsigned soff, u32x2 T) {
  for (unsigned spins = 0;;) {
    const u32x2 v = ld8(rs, voff, soff);
    if (__all(v[0] == T[0] && v[1] == T[1])) break;
    if (spin_fail(spins, rs, T)) break;
  }
  asm volatile("" ::: "memory");
  __builtin_amdgcn_fence(__ATOMIC_ACQUIRE, "wavefront");
}

__device__ __forceinline__ void unpack8v(const u32x4 u, f32x2* v) {
#pragma unroll
  for (int j = 0; j < 4; ++j) v[j] = f32x2{lo16(u[j]), hi16(u[j])};
}
__device__ __forceinline__ u32x4 pack8v(const f32x2* v) {
  u32x4 o;
#pragma unroll
  for (int j = 0; j < 4; ++j) o[j] = pack2(v[j][0], v[j][1]);
  return o;
}
__device__ __forceinline__ float ln_rd(float var) { return __builtin_amdgcn_rcpf(rbf1(__builtin_amdgcn_sqrtf(rbf1(rbf1(var) + 1e-5f)))); }
__device__ __forceinline__ float hadd(f32x2 v) {
  float a = v[0];
  asm volatile("" : "+v"(a));
  return a + v[1];
}
__device__ __forceinline__ float add_g(float v) { return add_x32(add_x16(v)); }
__device__ __forceinline__ void add_g3(float& x, float& y, float& z) {
  auto a = __builtin_amdgcn_permlane16_swap(__builtin_bit_cast(unsigned, x), __builtin_bit_cast(unsigned, y), false, false);
  auto b = __builtin_amdgcn_permlane16_swap(__builtin_bit_cast(unsigned, z), __builtin_bit_cast(unsigned, z), false, false);
  const float r = asf(a[0]) + asf(a[1]), t = asf(b[0]) + asf(b[1]);
  auto c = __builtin_amdgcn_permlane32_swap(__builtin_bit_cast(unsigned, r), __builtin_bit_cast(unsigned, r), false, false);
  auto d = __builtin_amdgcn_permlane32_swap(__builtin_bit_cast(unsigned, t), __builtin_bit_cast(unsigned, t), false, false);
  const float u = asf(c[0]) + asf(c[1]);
  z = asf(d[0]) + asf(d[1]);
  auto e = __builtin_amdgcn_permlane16_swap(__builtin_bit_cast(unsigned, u), __builtin_bit_cast(unsigned, u), false, false);
  x = asf(e[0]); y = asf(e[1]);
}
__device__ __forceinline__ u32x4 trade16(u32x4 v) {
  auto a = __builtin_amdgcn_permlane16_swap(v[0], v[2], false, false);
  auto b = __builtin_amdgcn_permlane16_swap(v[1], v[3], false, false);
  return u32x4{(unsigned)a[0], (unsigned)b[0], (unsigned)a[1], (unsigned)b[1]};
}
__device__ __forceinline__ unsigned umax4(u32x4 v) { return max(max(v[0], v[1]), max(v[2], v[3])); }
__device__ __forceinline__ u32x2 unpoison2(u32x2 v) { return u32x2{min(v[0], POISON - 1), min(v[1], POISON - 1)}; }
__device__ __forceinline__ u32x4 unpoison4(u32x4 v) {
  return u32x4{min(v[0], POISON - 1), min(v[1], POISON - 1), min(v[2], POISON - 1), min(v[3], POISON - 1)};
}

typedef union { struct { s16x4 a, b; } hf; u32x4 v; } frag_t;
__device__ __forceinline__ s16x4 trread(const char* smem, unsigned off) {
  return __builtin_amdgcn_ds_read_tr16_b64_v4i16((LDS_AS s16x4*)(smem + off));
}
__device__ __forceinline__ s16x4 trabs(unsigned addr) { return __builtin_amdgcn_ds_read_tr16_b64_v4i16((LDS_AS s16x4*)(size_t)addr); }

constexpr int WA = 6;
#define WOP 4, 3, 6, 8, 1, 4, 0
constexpr int WOV[7] = {WOP};
constexpr int WLA = WOV[4], WPR = WOV[5], QPR = WOV[6];
constexpr int WB = WOV[0], WR = WOV[1], WIS = WOV[2], WPS = WOV[3];
#define CH 0, 7, 9, 10, 16, 19, 24, 26, 30, 31, 5, 2
constexpr int CHV[12] = {CH};
constexpr int FKL = CHV[10], FSN = CHV[11];
constexpr int SPL = CHV[0];
constexpr int FSL = CHV[1], FS0 = CHV[2], FS0D = CHV[3], FS1 = CHV[4], FS1S = CHV[5];
constexpr int FXA = CHV[6], FXB = CHV[7], FXL = CHV[8], FXS = CHV[9];
#define ROT0 p
#define L0S 22, 25, 8, 0, 24, 28, 1, 2, 4, 14
constexpr int L0V[10] = {L0S};
constexpr int CSL = L0V[0], CSS = L0V[1], CSD = L0V[2];
constexpr int abl(int it) { return it ? 25 : L0V[4]; }
constexpr int abs_(int it) { return it ? 27 : L0V[5]; }
constexpr int W2S = L0V[6], WSH = L0V[7];
constexpr int PRE0 = 12;
constexpr int LNL = L0V[3], LNS = L0V[8], POI = L0V[9];
#define TCH 22, 3, 0x07, 26, 0xF8, 0, 1
constexpr int TCV[7] = {TCH};
constexpr int TS0 = TCV[0], TSN = TCV[1], TM0 = TCV[2], TEX = TCV[3], TM1 = TCV[4], TSD = TCV[5], TSG = TCV[6];
constexpr int QL = 26;
#define SLP 18, 4, 28, 3, 0, 0
constexpr int SLV[6] = {SLP};
constexpr int SDIR = SLV[0], GSL = SLV[1], RSL = SLV[2], KSL = SLV[3];
constexpr int tp0(int it) { return it ? 3 : 3 + WSH; }
constexpr bool tile_piece(int it, int s) { return s >= tp0(it) && ((s - tp0(it)) >> 2) + 3 < 8 && ((s - tp0(it)) & 3) < 3; }
#define NPRE 3
#define QAD 4
#define WO2 1, 5, 4, 2, 8
constexpr int W2V[5] = {WO2};
constexpr int WOS = 12;
static_assert(WOS + QAD < 18, "covered by the operand waits in front of the barrier of step 18");
constexpr int WON = W2V[0], W2B = W2V[1], W2R = W2V[2], W2L = W2V[3], W2P = W2V[4];
constexpr int a_req(int s, int n) {
  int r = 2 * NPRE;
  for (int u = 1; u < 192; u += 2) {
    const bool go = r < 64 && (r >> 1) <= u / 6 + QAD && (r < 8 || u >= 9);
    if (u == 6 * s + n) return go ? r : -1;
    r += go;
  }
  return -1;
}
constexpr int vm_n(int it, int s, int n) {
  int c = it ? (a_req(s, n) >= 0) + (s == 0 && n == 0) : (s + 5 < 32 && (n == 2 || n == 3));
  if (n == 0) c += tile_piece(it, s);
  if (n == 4) c += (s == abl(it));
  if (!it) c += (s >= W2S && s < W2S + 3 && n == 4) + (s == LNL && n == 5 ? 3 : 0) + (s == 0 && n < 2 ? 2 : 0) + (s == LNS + 2 && n == 5) + (s == TEX && n == 5);
  else c += (s == 2 && n == 2) + (s == (SPL < 0 ? 0 : SPL) && n == (SPL < 0 ? 1 : 4) ? 4 : 0) + (s >= 27 && s < 30 && (n == 0 || n == 3)) + (WON && s == WOS && n == 4);
  return c;
}
constexpr int vm_at(int it, int s, int n) {
  int c = it ? 2 * NPRE : PRE0;
  for (int u = 0; u < 6 * s + n; ++u) c += vm_n(it, u / 6, u % 6);
  return c;
}
constexpr int vm_a(int it, int k) {
  if (k < NPRE) return 2 * k + 2;
  for (int u = 0; u < 192; ++u)
    if (a_req(u / 6, u % 6) == 2 * k + 1) return vm_at(it, (u + 1) / 6, (u + 1) % 6);
  return 0;
}
constexpr int vm_tile(int it, int t) { return t >= 3 ? vm_at(it, 4 * t - 10 + tp0(it), 1) : t == 1 ? 12 : vm_at(0, W2S + 2, 5); }
constexpr int vm_unit(int u) { return u < 3 ? 5 + 2 * u : u < 5 ? vm_at(0, 0, u - 2) : vm_at(0, u - 5, 4); }
constexpr int unit_slot(int u) { return u % 3 == 0 ? 0 : 16384 + (u % 3) * 16384; }

constexpr int L_XC = LDS_BYTES - 64, L_SKEY = 32704, L_ROW = 1024, L_OUT = 65536, L_SRED = 4096, L_SFIN = 20480;
__device__ __forceinline__ unsigned key_load(rsrc_t rs, int l, int L, int rep) { return ld4(rs, (l & 15) * SKEY, KEY_AT(L, rep)); }
__device__ __forceinline__ unsigned umax16(unsigned k) {
  k = max(k, dppu<0xB1>(k));
  k = max(k, dppu<0x4E>(k));
  k = max(k, dppu<0x141>(k));
  return max(k, dppu<0x140>(k));
}
__device__ __forceinline__ unsigned key_row(unsigned key) { return (0xffffu - (key & 0xffffu)) & 2047u; }
__device__ __forceinline__ unsigned key_wait(rsrc_t rs, u32x2 T, int l, int L, int rep, unsigned kv) {
  if (!__all(kv != POISON)) {
    unsigned spins = 0;
    do {
      if (spin_fail(spins, rs, T)) break;
      if (L == 0) __builtin_amdgcn_s_sleep(KSL);
      kv = key_load(rs, l, L, rep);
    } while (!__all(kv != POISON));
  }
  return __builtin_amdgcn_readfirstlane(umax16(kv));
}
__device__ __forceinline__ unsigned cand_off(unsigned key, int L) { return OFF_CA + L * 65536 + (key_row(key) >> 7) * 4096; }
#define XO(i) (((i) >> 1) * 256 + ((i) & 1) * 64)
__device__ __forceinline__ void ffn_gu(const u32x4 (&gv)[4], const u32x4 (&uv)[4], const u32x4 (&xv)[4], f32x4& ga, f32x4& ua) {
  ga = mfma16(gv[0], xv[0], f32x4{0.f, 0.f, 0.f, 0.f}); ua = mfma16(uv[0], xv[0], f32x4{0.f, 0.f, 0.f, 0.f});
#pragma unroll
  for (int i = 1; i < 4; ++i) { ga = mfma16(gv[i], xv[i], ga); ua = mfma16(uv[i], xv[i], ua); }
}
__device__ __forceinline__ void ffn_gu_store(char* smem, int l, int w, f32x4 ga, f32x4 ua) {
  LDS_AS f32x4* sred = (LDS_AS f32x4*)(smem + L_SRED);
  if ((l & 15) == 0) { sred[w * 8 + (l >> 4)] = ga; sred[w * 8 + 4 + (l >> 4)] = ua; }
}
__device__ __forceinline__ f32x4 ffn_d_read(char* smem, int l) {
  const LDS_AS float* t = (const LDS_AS float*)(smem + L_SRED) + (l & 8) * 16 + (l & 4) * 4 + (l >> 4) * 4 + (l & 3);
  return f32x4{t[0], t[32], t[64], t[96]};
}
__device__ __forceinline__ u32x2 ffn_d_h(f32x4 t) {
  float x = t[0] + t[1];
  asm volatile("" : "+v"(x));
  x += t[2] + t[3];
  x += dppf<0x128>(x);
  const float y = dppf<0x124>(x);
  const bool up = __builtin_amdgcn_inverse_ballot_w64(0xF0F0F0F0F0F0F0F0ull);
  const float gg = rbf1(up ? y : x), uu = rbf1(up ? x : y);
  const float h = rbf1(gg * __builtin_amdgcn_rcpf(1.0f + __expf(-gg))) * uu;
  const unsigned p = pack2(h, dppf<0xB1>(h));
  return u32x2{dppu<0x00>(p), dppu<0xAA>(p)};
}
template <int t> __device__ __forceinline__ f32x4 ffn_d_mma(s16x4 d, u32x2 hh, f32x4 y) {
  const bool me = __builtin_amdgcn_inverse_ballot_w64(0x0001000100010001ull << t);
  return mfma16k16(d, __builtin_bit_cast(s16x4, u32x2{me ? hh[0] : 0u, me ? hh[1] : 0u}), y);
}
__device__ __forceinline__ f32x4 ffn_d_all(const s16x4 (&dfr)[8], u32x2 hh) {
  f32x4 a = {0.f, 0.f, 0.f, 0.f}, b = a;
  a = ffn_d_mma<0>(dfr[0], hh, a); b = ffn_d_mma<1>(dfr[1], hh, b); a = ffn_d_mma<2>(dfr[2], hh, a); b = ffn_d_mma<3>(dfr[3], hh, b);
  a = ffn_d_mma<4>(dfr[4], hh, a); b = ffn_d_mma<5>(dfr[5], hh, b); a = ffn_d_mma<6>(dfr[6], hh, a); b = ffn_d_mma<7>(dfr[7], hh, b);
  return a + b;
}
__device__ __forceinline__ void ffn_d_store(rsrc_t rs, int l, int w, int L, int slice, f32x4 y) {
  const unsigned c = l & 15;
  if (c < 8) st16(unpoison4(__builtin_bit_cast(u32x4, y)), rs, (2 * w + (c >> 2)) * 65536 + slice * 256 + (c & 3) * 64 + (l >> 4) * 16, OFF_PART + L * (1u << 20));
}
#define RED_VO(k) ((min(tid + 512 * (k), 1791) >> 3) * 256 + (tid & 7) * 16)
#define RED_SO(L, G) (OFF_PART + (L) * (1u << 20) + ((G) >> 1) * 65536 + ((G) & 1) * 128)
#define RED_JJ() ((tid & 3) * 16 + (tid & 4) * 2)
__device__ __forceinline__ void red_load(rsrc_t rs, int tid, int L, unsigned G, unsigned key, u32x4 (&pv)[4], u32x2& cxn, u32x2& cx1) {
#pragma unroll
  for (int k = 0; k < 4; ++k) pv[k] = ld16(rs, RED_VO(k), RED_SO(L, G));
  cxn = ld8(rs, RED_JJ(), cand_off(key, L) + XO(G));
  cx1 = ld8(rs, RED_JJ() + 128, cand_off(key, L) + XO(G));
}
__device__ __forceinline__ void red_sum(rsrc_t rs, char* smem, u32x2 T, int tid, int w, int L, unsigned G, unsigned key,
                                        u32x4 (&pv)[4], u32x2& cxn, u32x2& cx1) {
#define PV_OK() __all(max(max(max(umax4(pv[0]), umax4(pv[1])), max(umax4(pv[2]), umax4(pv[3]))), max(max(cxn[0], cxn[1]), max(cx1[0], cx1[1]))) != POISON)
  if (!PV_OK()) {
    unsigned spins = 0;
    do {
      if (spin_fail(spins, rs, T)) break;
#pragma unroll
      for (int k = 0; k < 4; ++k)
        if (umax4(pv[k]) == POISON) pv[k] = ld16(rs, RED_VO(k), RED_SO(L, G));
      if (max(max(cxn[0], cxn[1]), max(cx1[0], cx1[1])) == POISON) { cxn = ld8(rs, RED_JJ(), cand_off(key, L) + XO(G)); cx1 = ld8(rs, RED_JJ() + 128, cand_off(key, L) + XO(G)); }
    } while (!PV_OK());
  }
#undef PV_OK
  f32x4 s = (__builtin_bit_cast(f32x4, pv[0]) + __builtin_bit_cast(f32x4, pv[1])) + __builtin_bit_cast(f32x4, pv[2]);
  if (w < 4) s += __builtin_bit_cast(f32x4, pv[3]);
#pragma unroll
  for (int j = 0; j < 4; ++j) s[j] = sum_hi8(s[j]);
  if ((tid & 63) < 8) ((LDS_AS f32x4*)(smem + L_SFIN))[w * 8 + (tid & 7)] = s;
}
__device__ __forceinline__ float red_gate(unsigned key) {
  const unsigned k16 = key >> 16;
  const float rmax = __builtin_bit_cast(float, ((k16 & 0x8000u) ? (k16 & 0x7fffu) : (~k16 & 0xffffu)) << 16);
  float sig = rbf1(__builtin_amdgcn_rcpf(1.0f + __expf(-rmax)));
  asm volatile("" : "+v"(sig));
  return sig;
}
__device__ __forceinline__ u32x2 red_final(char* smem, int l, float sig, u32x2 cxn, u32x2 cx1) {
  f32x4 s = ((const LDS_AS f32x4*)(smem + L_SFIN))[l];
#pragma unroll
  for (int j = 0; j < 4; ++j) s[j] = sum_hi8(s[j]);
  const float xn4[4] = {lo16(cxn[0]), hi16(cxn[0]), lo16(cxn[1]), hi16(cxn[1])};
  const float x14[4] = {lo16(cx1[0]), hi16(cx1[0]), lo16(cx1[1]), hi16(cx1[1])};
  float o[4];
#pragma unroll
  for (int j = 0; j < 4; ++j) o[j] = x14[j] + rbf1(xn4[j] + rbf1(rbf1(s[j]) * sig));
  return u32x2{pack2(o[0], o[1]), pack2(o[2], o[3])};
}

struct RowFin { float mean, var, mb, rd; unsigned key; };
__device__ __forceinline__ void fin_wait(rsrc_t rs, u32x2 T, unsigned voff, unsigned so, u32x4 (&sp)[4]) {
#define ST_OK() __all(max(max(umax4(sp[0]), umax4(sp[1])), max(umax4(sp[2]), umax4(sp[3]))) != POISON)
  if (!ST_OK()) {
    unsigned spins = 0;
    do {
      if (spin_fail(spins, rs, T)) break;
#pragma unroll
      for (int i = 0; i < 4; ++i) sp[i] = ld16(rs, voff + i * 2048, so);
    } while (!ST_OK());
  }
#undef ST_OK
}
__device__ __forceinline__ RowFin fin_key(rsrc_t rs, u32x2 T, unsigned voff, unsigned so, char* skey, int l, int w, unsigned R, float wsum, u32x4 (&sp)[4]) {
  RowFin r;
#define ST_SUM() ((__builtin_bit_cast(f32x4, sp[0]) + __builtin_bit_cast(f32x4, sp[1])) + (__builtin_bit_cast(f32x4, sp[2]) + __builtin_bit_cast(f32x4, sp[3])))
  f32x4 sf = ST_SUM();
  const float z = (sf[0] + sf[1]) + sf[2];
  if (__any(z != z)) {
    fin_wait(rs, T, voff, so, sp);
    sf = ST_SUM();
  }
#undef ST_SUM
  float s0 = sf[0], s1 = sf[1], s2 = sf[2];
  add_g3(s0, s1, s2);
  r.mean = s0 * (1.0f / 1024.0f);
  r.var = fmaxf(__builtin_fmaf(-r.mean, r.mean, s1 * (1.0f / 1024.0f)), 0.f);
  r.mb = rbf1(r.mean); r.rd = ln_rd(r.var);
  const unsigned rb16 = pack2((s2 - r.mb * wsum) * r.rd, 0.f) & 0xffffu;
  r.key = min((((rb16 & 0x8000u) ? (~rb16 & 0xffffu) : (rb16 | 0x8000u)) << 16) | (0xffffu - R), POISON - 1);
  const unsigned wk = umax16(r.key);
  if (l == 0) ((LDS_AS unsigned*)skey)[w] = wk;
  return r;
}
__device__ __forceinline__ void fin_cand(rsrc_t rs, char* skey, int L, int g, int rb, int ct, const RowFin& r, const f32x2 (&v)[8]) {
  const u32x4 k0 = *(const LDS_AS u32x4*)skey, k1 = *(const LDS_AS u32x4*)(skey + 16);
  const unsigned best = max(umax4(k0), umax4(k1));
  if (r.key == best) {
    const unsigned co = L * 65536 + rb * 4096 + ct * 256 + g * 16;
    f32x2 t[8], rd2 = {r.rd, r.rd}, of2 = {-r.mb * r.rd, -r.mb * r.rd};
    asm volatile("" : "+v"(rd2), "+v"(of2));
#pragma unroll
    for (int j = 0; j < 8; ++j) t[j] = __builtin_elementwise_fma(v[j], rd2, of2);
    st16(unpoison4(pack8v(t)), rs, co, OFF_CA); st16(unpoison4(pack8v(t + 4)), rs, co + 64, OFF_CA);
    if (g == 0 && ct < (int)RKEY) st4(best, rs, KEY_AT(L, ct) + rb * SKEY, 0);
    st16(unpoison4(pack8v(v)), rs, co + 128, OFF_CA); st16(unpoison4(pack8v(v + 4)), rs, co + 192, OFF_CA);
  }
}
__device__ __forceinline__ void fin_x2(const RowFin& r, f32x2 (&v)[8]) {
  f32x2 sc2 = {1.0f + r.rd, 1.0f + r.rd}, of2 = {-r.mb * r.rd, -r.mb * r.rd};
  asm volatile("" : "+v"(sc2), "+v"(of2));
#pragma unroll
  for (int j = 0; j < 8; ++j) v[j] = __builtin_elementwise_fma(v[j], sc2, of2);
}

extern "C" __device__ unsigned long long dispatch_id() __asm("llvm.amdgcn.dispatch.id");

extern "C" __global__ __launch_bounds__(512, 2) void mega_kernel(const bf16* x, char* ws, const bf16* wqkv, const bf16* wo, const bf16* wr,
                                                                 const bf16* wg, const bf16* wu, const bf16* wd, bf16* out) {
  extern __shared__ __attribute__((aligned(16))) char smem[];
  const int w0_ = __builtin_amdgcn_readfirstlane(threadIdx.x >> 6);
  const unsigned lbase = (unsigned)(size_t)(LDS_AS char*)smem;
  {
    unsigned long long pc;
    unsigned len;
    asm volatile("s_getpc_b64 %0\n\ts_mov_b32 %1, .Ltext_end-mega_kernel\n\t.subsection 1\n.Ltext_end:\n\t.subsection 0" : "=s"(pc), "=s"(len));
    const unsigned o = ((blockIdx.x >> 3) * 64 + (threadIdx.x & 63)) * 128;
    if (w0_ == 7 && o < len)
      asm volatile("s_mov_b32 m0, %0\n\ts_nop 0\n\tglobal_load_lds_dword %1, %2" ::"s"(lbase + L_FXL), "v"(o), "s"(pc & ~127ull) : "memory");
  }
  asm volatile("" : "+s"(x), "+s"(ws), "+s"(wqkv));
#define IDS()                                                                                         \
  int tid = threadIdx.x, w = w0_, bid = blockIdx.x;                                                   \
  asm volatile("" : "+v"(tid));                                                                       \
  asm volatile("" : "+s"(w));                                                                         \
  asm volatile("" : "+s"(bid));                                                                       \
  const int l = tid & 63, xcd = bid & 7, bi = bid >> 3;                                               \
  const int p = (xcd & 1) * 4 + (bi & 3), h = (xcd >> 1) * 8 + (bi >> 2);         \
  const int rb = 2 * p + ((h >> 3) & 1), ct = (h >> 4) * 8 + (h & 7);       \
  const int slice = xcd * 28 + bi;                                                   \
  const bool ffn_on = bi < 28;                                                                        \
  const int g = l >> 4;                                                                               \
  const int rt = w < 4 ? w : 11 - w;          \
  KARGS();                                                                                            \
  (void)p; (void)h; (void)rb; (void)ct; (void)slice; (void)ffn_on; (void)g; (void)rt;
  const rsrc_t rs = __builtin_amdgcn_make_buffer_rsrc((void*)ws, 0, WS_BYTES, 0x00020000);
  typedef const void* const __attribute__((address_space(4)))* karg_t;
#define KARGS() karg_t ka = (karg_t)__builtin_amdgcn_kernarg_segment_ptr(); asm volatile("" : "+s"(ka))
#define ARGP(name, i) const GAS bf16* name = (const GAS bf16*)ka[i]; asm volatile("" : "+s"(name))
#define RSRC(ptr, bytes) __builtin_amdgcn_make_buffer_rsrc((void*)(ptr), 0, bytes, 0x00020000)
#define rwq RSRC(pwq, 1024 * 3072 * 2)
#define rwo RSRC(pwo, 1024 * 1024 * 2)
#define rwg RSRC(pwg, 1024 * F_ * 2)
#define rwu RSRC(pwu, 1024 * F_ * 2)
#define rwd RSRC(pwd, 1024 * F_ * 2)
#define rout RSRC(pout, 2048 * 1024 * 2)

#define WQ_VOFF() ((l >> 2) * 6144 + (((l & 3) ^ ((((l >> 4) ^ (l >> 5)) & 1) << 1)) << 4))
#define WQ_PIECE(t, j, dst) dma16(rwq, wqv, ((((t) + rot) & 7) * 128 + ((j) & 7) * 16) * 6144 + ((j) >> 3) * 2048 + h * 64, lbase + (dst) + (j) * 1024)
#define WQ_BOFF(dt) ((8 * g + ((l & 15) >> 2)) * 64 + (l & 3) * 8 + (((dt) ^ (g & 1)) << 5))
#define WQ_POFF() ((4 * g + ((l & 15) >> 2)) * 64 + (l & 3) * 8 + (((g ^ (g >> 1)) & 1) << 5))
#define X_UNIT1(u, j) dma16(rx, xv_, (unsigned)(p * 256 + rt * 32 + ((u) & 1) * 16 + (j) * 8) * 2048 + ((((u) >> 2) + rot) & 7) * 256 + (((u) >> 1) & 1) * 128, lbase + rt * 2048 + unit_slot(u) + (j) * 1024)
#define X_VOFF() ((l >> 3) * 2048 + (((l & 7) ^ ((l >> 3) & 6)) << 4))
#define X_ROFF(hs) (lbase + rt * 2048 + (l & 15) * 128 + (((4 * (hs) + g) ^ (l & 6)) << 4))
#define TOUCH_TO(ptr, dst) asm volatile("s_mov_b32 m0, %0\n\ts_nop 0\n\tglobal_load_lds_dword %1, off" ::"s"(lbase + (dst)), "v"(ptr) : "memory")
#define TOUCH(ptr) TOUCH_TO(ptr, L_FXL)
#define WO_PIECE_W(j, w_)                                                                                                                 \
  {                                                                                                                                       \
    if constexpr (it == 1 && WON) dma16s<0>(rs, l * 16, OFF_WOI + ct * 131072 + (j) * 8192 + (w_) * 1024, lbase + 32768 + (j) * 8192 + (w_) * 1024); \
    else dma16(rwo, wov, (((w_) >> 2) * 64 + ((w_) & 3) + 4 * (j)) * 16384 + ct * 128, lbase + 32768 + (j) * 8192 + (w_) * 1024);           \
  }
#define WO_PIECE(j) WO_PIECE_W(j, w)
#define WO_VOFF() ((l >> 3) * 2048 + (((((l & 7) >> 1) ^ ((l >> 4) & 3)) * 2 + (l & 1)) << 4))
#define WD_DMA() _Pragma("unroll") for (int i = 0; i < 4; ++i) WD_DMA1(i)
#define WD_DMA1(i)                                                                                   \
      dma16n(rwd, (l >> 4) * 2048 + (((l & 15) * 16) ^ ((l >> 4) * 32) ^ (((i) & 1) * 128)), (unsigned)(slice * 16 + 4 * (i)) * 2048 + w * 256, lbase + L_DN + w * 4096 + (i) * 1024)
#define FFN_DMA(m) dma16((m) & 1 ? rwu : rwg, fnv, (unsigned)(((4 * w + ((m) >> 1)) * 32) * F_ + slice * 16) * 2, lbase + 65536 + (m) * 8192 + w * 1024)
  u32x4 gv[4], uv[4];
  s16x4 dfr[8];
  u32x4 keep[9];
  u32x4 pre[4][2];
#define PRE_LOAD(it_, rot_, s0, s1)                                                                  \
  static_for<(s1) - (s0)>([&](auto c_) __attribute__((always_inline)) {                              \
    constexpr int s = (s0) + decltype(c_)::value;                                                    \
    const unsigned ab_ = OFF_XN + (it_) * (4u << 20) + (((rot_) + (s >> 2)) & 7) * 8192 + ((s >> 1) & 1) * 4096; \
    ld16w<2 * s, (s & 1) * 2048>(pre[s][0], rs, l * 16, ab_ + (p * 8 + rt) * 65536);                         \
    ld16w<2 * s + 1, (s & 1) * 2048 + 1024>(pre[s][1], rs, l * 16, ab_ + (p * 8 + rt) * 65536);                 \
  })
  u32x2 T;
  u32x2 ep;
  u32x4 xl[2];
#define LN_LOAD()                                                                                                                                   \
  {                                                                                                                                                 \
    asm volatile("buffer_load_dwordx4 %0, %1, %2, %3 offen" : "=v"(xl[0]) : "v"(l * 16), "s"(rx), "s"((p * 256 + h * 8 + w) * 2048) : "memory");      \
    asm volatile("buffer_load_dwordx4 %0, %1, %2, %3 offen offset:1024" : "=v"(xl[1]) : "v"(l * 16), "s"(rx), "s"((p * 256 + h * 8 + w) * 2048) : "memory"); \
    asm volatile("buffer_load_dwordx2 %0, off, %1, 0 sc1" : "=v"(ep) : "s"(rs) : "memory");                                                         \
  }
  const GAS bf16* tsrc;
  {
    IDS();
    const int it = 0, rot = ROT0;
    (void)it;
    const bf16* pwq = wqkv;
    const rsrc_t rx = RSRC(x, 2048 * 1024 * 2);
    const unsigned wqv = WQ_VOFF(), xv_ = X_VOFF();
    if (tid == 0) *(LDS_AS u32x4*)(smem + L_QC) = u32x4{0u, 0u, 0u, 0u};
    if (w == 0 && l < 24) TOUCH((const GAS bf16*)wqkv + (bid * 24 + l) * 512 + ((3 - (unsigned)((size_t)wqkv >> 7)) & 7) * 64);
#pragma unroll
    for (int i = 0; i < 3; ++i) WQ_PIECE(0, 3 * w + i, L_RING);
    X_UNIT1(0, 0); X_UNIT1(0, 1); X_UNIT1(1, 0); X_UNIT1(1, 1);
    BAR();
    X_UNIT1(2, 0); X_UNIT1(2, 1);
#pragma unroll
    for (int i = 0; i < 3; ++i) WQ_PIECE(1, 3 * w + i, L_RING + STAGE);
  }
  unsigned sel0 = 0x7FFFFFFFu;
  f32x2 v[8];
  u32x4 x2p[2], wof;
  float wsum;

  auto layer = [&](auto it_c) __attribute__((always_inline)) {
    constexpr int it = decltype(it_c)::value;
    f32x4 acc[2][6];
    f32x2 qs2[2];
    u32x4 k0, k1, v0, v1;
    {
      IDS();
      bool aff = false;
      unsigned aq[2];
      u32x4 av[2];
#define PIN_ACC() asm volatile("" : "+{v[176:179]}"(acc[0][0]), "+{v[180:183]}"(acc[0][1]), "+{v[184:187]}"(acc[0][2]), "+{v[188:191]}"(acc[0][3]), "+{v[192:195]}"(acc[0][4]), "+{v[196:199]}"(acc[0][5]), \
                               "+{v[200:203]}"(acc[1][0]), "+{v[204:207]}"(acc[1][1]), "+{v[208:211]}"(acc[1][2]), "+{v[212:215]}"(acc[1][3]), "+{v[216:219]}"(acc[1][4]), "+{v[220:223]}"(acc[1][5]))
#define PINV4(x) asm volatile("" : "+v"(x[0]), "+v"(x[1]), "+v"(x[2]), "+v"(x[3])::"memory")
      auto qkv_loop = [&](auto it_c) __attribute__((always_inline)) {
        constexpr int IT = decltype(it_c)::value;
        const unsigned abase = OFF_XN + IT * (4u << 20) + (p * 8 + rt) * 65536;
        const int rot = ROT0 + 5 * IT;
        unsigned fkey = 0, kv, bq[2];
        u32x4 fx[4], bv[2], af[WA][2];
        u32x2 cxn, cx1, hh;
        f32x4 sa, sb, cs4[2];
        f32x2 b2[2];
#define AB_OFF() (OFF_AB + IT * 32768 + (p * 8 + rt) * 512)
#define A_LOAD1(k, a) ld16w<(k) % WA * 2 + (a), ((k) & 1) * 2048 + (a) * 1024>(af[(k) % WA][a], rs, l * 16, abase + ((((k) >> 2) + rot) & 7) * 8192 + (((k) >> 1) & 1) * 4096)
        if constexpr (IT == 1) {
#pragma unroll
          for (int s = 0; s < NPRE; ++s) { af[s][0] = pre[s][0]; af[s][1] = pre[s][1]; }
        }
        const bf16* pwq = wqkv;
        ARGP(pwo, 3);
        const rsrc_t rx = RSRC(x, 2048 * 1024 * 2);
        unsigned xv_ = X_VOFF(), xr[2] = {X_ROFF(0), X_ROFF(1)};
        if constexpr (IT == 0) asm volatile("" : "+v"(xv_), "+v"(xr[0]), "+v"(xr[1]));
#define X_READ(u) { af[((u) & ~1) % WA][(u) & 1] = *(const LDS_AS u32x4*)(size_t)(xr[0] + unit_slot(u)); af[((u) | 1) % WA][(u) & 1] = *(const LDS_AS u32x4*)(size_t)(xr[1] + unit_slot(u)); }
        const int tstep = TS0 + bid % TSN + TSD * (w >> TSG);
        const unsigned wqv = WQ_VOFF();
#define QKV_DMA1(t, i)                                                                            \
  {                                                                                               \
    if constexpr (IT == 0) WQ_PIECE(t, 3 * w + (i), L_RING + SLOT(t) * STAGE);                    \
    else dma16s<(i) * 1024>(rs, l * 16, OFF_WI + (h * 8 + (((t) + rot) & 7)) * STAGE + w * 3072, lbase + L_RING + SLOT(t) * STAGE + w * 3072); \
  }
        unsigned bo[2][2];
#pragma unroll
        for (int dt = 0; dt < 2 - IT; ++dt) {
          bo[dt][0] = lbase + L_RING + (IT ? l * 16 : WQ_BOFF(dt)); bo[dt][1] = bo[dt][0] + 2 * STAGE;
          asm volatile("" : "+v"(bo[dt][0]), "+v"(bo[dt][1]));
        }
        auto frag0 = [&](int sl, int f) __attribute__((always_inline)) {
          unsigned pb = lbase + L_RING + WQ_POFF();
          asm volatile("" : "+v"(pb));
          const unsigned o = (pb ^ ((f & 1) << 5)) + sl * STAGE + ((f % 6) >> 1) * 8192 + (f / 6) * 2048;
          frag_t t;
          t.hf.a = trabs(o); t.hf.b = trabs(o + 1024);
          return t.v;
        };
        u32x4 df[4];
        auto dot_mma = [&](const u32x4 (&xf)[4]) __attribute__((always_inline)) {
          sa = mfma16(df[0], xf[0], f32x4{0.f, 0.f, 0.f, 0.f}); sb = mfma16(df[1], xf[1], f32x4{0.f, 0.f, 0.f, 0.f});
          sa = mfma16(df[2], xf[2], sa); sb = mfma16(df[3], xf[3], sb);
        };
        auto dot_store = [&](unsigned dst) __attribute__((always_inline)) {
          if ((l & 15) == 0) st16(unpoison4(__builtin_bit_cast(u32x4, sa + sb)), rs, g * 16, dst + (h * 8 + p) * 512 + w * 64);
        };
        auto tile_sum = [&](u32x4 pt, int dst) __attribute__((always_inline)) {
          f32x4 c = __builtin_bit_cast(f32x4, pt);
#pragma unroll
          for (int e = 0; e < 4; ++e) { c[e] += dppf<0x124>(c[e]); c[e] += dppf<0x128>(c[e]); c[e] = add_x16(c[e]); }
          if ((l & 28) == 0) *(LDS_AS f32x4*)(smem + dst + (w * 2 + (l >> 5)) * 64 + (l & 3) * 16) = c;
        };
        frag_t bfr[6];
#define QKV_BO(s, n, x) (bo[((n) ^ (x)) & 1][SLOT((s) >> 2) >> 1] + (SLOT((s) >> 2) & 1) * STAGE + ((n) >> 1) * 8192 + ((s) & 3) * 2048 + (x) * 256)
#define QKV_RDA(s, n)                                                                             \
  {                                                                                               \
    if constexpr (IT == 0) bfr[n].hf.a = trabs(QKV_BO(s, n, 0));                                  \
    else bfr[n].v = *(const LDS_AS u32x4*)(size_t)(bo[0][SLOT((s) >> 2) >> 1] + (SLOT((s) >> 2) & 1) * STAGE + (((s) & 3) * 6 + (n)) * 1024); \
  }
#define QKV_RDB(s, n) { if constexpr (IT == 0) bfr[n].hf.b = trabs(QKV_BO(s, n, 1)); }
#define XP_OFF() (OFF_XNEW + ((rot + 5) & 7) * 1024)
#define FP_VO() (w == 3 ? (l >> 2) * 256 + (l & 3) * 16 : ((l >> 2) & 7) * 512 + (l >> 5) * 64 + (l & 3) * 16)
#define FP_OFF(i) (w == 3 ? OFF_XNEW + (i) * 4096 : OFF_FX + h * 4096 + w * 128)
#define FX_OK(n) __all(((n) == 4 ? max(max(umax4(fx[0]), umax4(fx[1])), max(umax4(fx[2]), umax4(fx[3]))) : max(umax4(fx[0]), umax4(fx[1]))) != POISON)
#define SINCE(s0) (vm_at(IT, s, n) - vm_at(IT, s0, 5))
#define DEF4(x) asm volatile("" : "=v"(x[0]), "=v"(x[1]), "=v"(x[2]), "=v"(x[3]))
        LDS_AS unsigned* qcnt = (LDS_AS unsigned*)(smem + L_QC);
        unsigned qc;
        unsigned qadr = lbase + L_QC, qone = 1;
        asm volatile("" : "+v"(qadr), "+v"(qone));
#define count_in(i) asm volatile("s_mov_b64 exec, 1\n\tds_add_u32 %0, %1 offset:%2\n\ts_mov_b64 exec, -1" ::"v"(qadr), "v"(qone), "n"(4 * (i)) : "memory")
        auto count_wait = [&](int i, unsigned want) __attribute__((always_inline)) {
          if (__builtin_expect((unsigned)__builtin_amdgcn_readfirstlane(qc) < want, 0)) {
            while ((unsigned)__builtin_amdgcn_readfirstlane(__hip_atomic_load(qcnt + i, __ATOMIC_RELAXED, __HIP_MEMORY_SCOPE_WORKGROUP)) < want) {}
          }
          asm volatile("" ::: "memory");
        };
        if constexpr (IT == 0) {
          wait_vm<PRE0 - vm_unit(1)>();
          X_READ(0); X_READ(1);
          asm volatile("s_barrier" ::: "memory");
        }
#pragma unroll
        for (int n = 0; n < 6; ++n) { QKV_RDA(0, n); if (n < 5) QKV_RDB(0, n); }
        f32x2 lv[4], xs, xq;
        float lmean;
        f32x4 sc;
#define X1_OFF() (F_X1 + ((p * RX1 + (xcd >> 1) % RX1) * 32 + (rt >> 2) * 16 + (l & 15)) * SX1)
        unsigned long long xok = 0;
        RowFin fin;
        auto x1_need = [&](unsigned long long m) __attribute__((always_inline)) {
          if (__builtin_expect((xok & m) != m, 0)) {
            for (unsigned spins = 0;;) {
              const u32x2 f = ld8(rs, X1_OFF(), 0);
              xok = __builtin_amdgcn_ballot_w64(f[0] == T[0] && f[1] == T[1]);
              if ((xok & m) == m) break;
              if (spin_fail(spins, rs, T)) { xok = ~0ull; break; }
              __builtin_amdgcn_s_sleep(KSL);
            }
            asm volatile("" ::: "memory");
          }
        };
        auto gap = [&](auto sc_, auto nc_) __attribute__((always_inline)) {
          constexpr int s = decltype(sc_)::value, n = decltype(nc_)::value, ar = a_req(s, n);
          {
            constexpr int tc = (s + 2) / 4;
            if constexpr (tc >= 1 && tc < 8 && s == 4 * tc - 2 && n == 5) {
              if constexpr (!IT || tc >= 3) wait_vm<vm_at(IT, s, n) - vm_tile(IT, tc)>();
              if constexpr (tc == 7 && !IT) asm volatile("s_waitcnt lgkmcnt(4)" ::: "memory");
              asm volatile("s_barrier" ::: "memory");
            }
          }
          if constexpr (IT == 0 && n == 0 && s + 2 < 32) { wait_vm<vm_at(0, s, 0) - vm_unit(s + 2)>(); X_READ(s + 2); SB(); }
          if constexpr (n == 0 && tile_piece(IT, s)) QKV_DMA1(((s - tp0(IT)) >> 2) + 3, (s - tp0(IT)) & 3);
          if constexpr (IT == 0) {
            if constexpr ((n == 2 || n == 3) && s + 5 < 32) {
              if constexpr (n == 2) asm volatile("s_waitcnt lgkmcnt(%0)" ::"n"(s + 1 < QL ? 4 : 1) : "memory");
              X_UNIT1(s + 5, n - 2);
            }
            if constexpr (s >= W2S && s < W2S + 3 && n == 4) QKV_DMA1(2, s - W2S);
            if constexpr (s == LNL && n == 5) LN_LOAD();
            if constexpr (s == 0 && n < 2) { X_UNIT1(3 + n, 0); X_UNIT1(3 + n, 1); }
            if constexpr (s == LNS && n == 1) {
              wait_vm<vm_at(0, s, n) - vm_at(0, LNL + 1, 0)>();
              asm volatile("" : "+v"(ep), "+v"(xl[0]), "+v"(xl[1])::"memory");
              const unsigned long long e = (((unsigned long long)(unsigned)__builtin_amdgcn_readfirstlane(ep[1]) << 32) | (unsigned)__builtin_amdgcn_readfirstlane(ep[0])) + 1ull + dispatch_id() * 0x9E3779B97F4A7C16ull;
              T = u32x2{(unsigned)e, (unsigned)(e >> 32)};
              unpack8v(xl[0], lv);
              xs = (lv[0] + lv[1]) + (lv[2] + lv[3]);
              xq = __builtin_elementwise_fma(lv[0], lv[0], lv[1] * lv[1]) + __builtin_elementwise_fma(lv[2], lv[2], lv[3] * lv[3]);
              asm volatile("" : "+v"(xs), "+v"(xq));
            }
            if constexpr (s == LNS && n == 4) {
              unpack8v(xl[1], lv);
              xs += (lv[0] + lv[1]) + (lv[2] + lv[3]);
              xq += __builtin_elementwise_fma(lv[0], lv[0], lv[1] * lv[1]) + __builtin_elementwise_fma(lv[2], lv[2], lv[3] * lv[3]);
              auto t = __builtin_amdgcn_permlane32_swap(__builtin_bit_cast(unsigned, hadd(xs)), __builtin_bit_cast(unsigned, hadd(xq)), false, false);
              lmean = asf(t[0]) + asf(t[1]);
              asm volatile("" : "+v"(lmean));
            }
            if constexpr (s == LNS + 1 && n == 1) { lmean += dppf<0xB1>(lmean); lmean += dppf<0x4E>(lmean); lmean += dppf<0x124>(lmean); asm volatile("" : "+v"(lmean)); }
            if constexpr (s == LNS + 1 && n == 4) { lmean = add_x16(lmean + dppf<0x128>(lmean)) * (1.0f / 1024.0f); asm volatile("" : "+v"(lmean)); }
            if constexpr (s == LNS + 2 && n == 1) {
              const float m2 = asf(__builtin_amdgcn_readlane(__builtin_bit_cast(unsigned, lmean), 32));
              xs[0] = ln_rd(fmaxf(__builtin_fmaf(-lmean, lmean, m2), 0.f));
              asm volatile("" : "+v"(xs));
            }
            if constexpr (s == LNS + 2 && n == 5) {
              if (l == 0) st16(u32x4{__builtin_bit_cast(unsigned, (float)xs[0]), __builtin_bit_cast(unsigned, -rbf1(lmean)), T[0], T[1]}, rs, OFF_AB + (p * 256 + h * 8 + w) * 16, 0);
            }
            if constexpr (s == POI && n == 1) {
              if (w == 1) {
                const unsigned prb = 2 * p + (h >> 4), pct = h & 15;
                unsigned pz1 = POISON;
                asm volatile("" : "+v"(pz1));
                const u32x4 pz = {pz1, pz1, pz1, pz1};
                st16(pz, rs, OFF_STAT + (prb * 16 + pct) * 2048 + l * 16, 0);
                st16(pz, rs, OFF_STAT + (prb * 16 + pct) * 2048 + 1024 + l * 16, 0);
                if (l < 16) st16(pz, rs, OFF_CA + prb * 4096 + pct * 256 + l * 16, 0);
                else if (l == 16 && pct < RKEY) st4(pz1, rs, KEY_AT(0, pct) + prb * SKEY, 0);
              }
            }
            if constexpr (s == POI + 4 && n == 1) { if (w == 1 && l < RX) st8(T, rs, F_XN + (p * RX + l) * 256 + h * 8, 0); }
          } else {
            if constexpr ((s == 0 && n == 0) || (s == 2 && n == 2)) asm volatile("buffer_load_dwordx2 %0, %1, %2, 0 offen sc1" : "=v"(cxn) : "v"(X1_OFF()), "s"(rs) : "memory");
            if constexpr ((s == 1 && n == 3) || (s == 4 && n == 0)) {
              wait_vm<vm_at(1, s, n) - (s == 1 ? vm_at(1, 0, 1) : vm_at(1, 2, 3))>();
              asm volatile("" : "+v"(cxn)::"memory");
              xok |= __builtin_amdgcn_ballot_w64(cxn[0] == T[0] && cxn[1] == T[1]);
            }
#define IMG_OFF() (F_X1 + (((l & 7) * RX1 + (xcd >> 1) % RX1) * 32 + (rb & 1) * 16 + ct) * SX1)
            if constexpr (s == 0 && n == 2) { asm volatile("" : "=v"(cx1)); if (w == 1) asm volatile("buffer_load_dwordx2 %0, %1, %2, 0 offen sc1" : "=v"(cx1) : "v"(IMG_OFF()), "s"(rs) : "memory"); }
            if constexpr (s == 2 && n == 4) {
              if (w == 1) {
                wait_vm<vm_at(1, 2, 4) - vm_at(1, 0, 3)>();
                asm volatile("" : "+v"(cx1)::"memory");
                if (!__all(cx1[0] == T[0] && cx1[1] == T[1])) poll_flags(rs, IMG_OFF(), 0, T);
              }
            }
            if constexpr (ar >= 0 && (ar & 7) == 0) x1_need(3ull << (((rot + ar / 8) & 7) * 2));
            const unsigned R = (rb * 4 + (w & 3)) * 32 + (w >> 2) * 16 + (l & 15), so = OFF_STAT + rb * 32768;
            if constexpr (s == (SPL < 0 ? 0 : SPL) && n == (SPL < 0 ? 1 : 4)) {
#pragma unroll
              for (int i = 0; i < 4; ++i) ld16h<0>(fx[i], rs, (4 * g + i) * 2048 + (R & 127) * 16, so);
            }
            if constexpr (s == SPL + 2 && n == 1) {
              wait_vm<vm_at(1, s, n) - (SPL < 0 ? vm_at(1, 0, 2) : vm_at(1, SPL, 5))>();
              PINV4(fx);
              fin = fin_key(rs, T, g * 8192 + (R & 127) * 16, so, smem + L_SKEY, l, w, R, wsum, fx);
              count_in(2);
            }
            if constexpr (s == SPL + 3 && n == 0) qc = __hip_atomic_load(qcnt + 2, __ATOMIC_RELAXED, __HIP_MEMORY_SCOPE_WORKGROUP);
            if constexpr (s == SPL + 3 && n == 1) { count_wait(2, 8); fin_cand(rs, smem + L_SKEY, 0, g, rb, ct, fin, v); }
            if constexpr (s == SPL + 3 && n == 3) {
              fin_x2(fin, v);
              x2p[0] = pack8v(v); x2p[1] = pack8v(v + 4);
              asm volatile("" : "+v"(x2p[0]), "+v"(x2p[1]));
            }
            if constexpr (s == SPL + 3 && n == 5) {
              if ((w & 3) * 2 + (w >> 2) == ct >> 1) {
                const float f = 1.0f + fin.rd, rd2 = ln_rd(fin.var * f * f), mb2 = rbf1(__builtin_fmaf(fin.mean - fin.mb, fin.rd, fin.mean));
                if (g == 0 && ((l >> 3) & 1) == (ct & 1))
                  st16(u32x4{__builtin_bit_cast(unsigned, f * rd2), __builtin_bit_cast(unsigned, -__builtin_fmaf(fin.mb, fin.rd, mb2) * __builtin_amdgcn_rcpf(f)), T[0], T[1]}, rs, OFF_AB + 32768 + R * 16, 0);
              }
            }
          }
          if constexpr (IT == 1 && ar >= 0) A_LOAD1(ar / 2, ar & 1);
          if constexpr (!IT && s == CSL && n == 4) { asm volatile("" : "=v"(fx[0])); if (w < 6) ld16h<0>(fx[0], rs, l * 16, OFF_CS + (h * 6 + w) * 1024); }
          if constexpr (!IT && s == CSS && n == 2) {
            if (w < 6) {
              wait_vm<SINCE(CSL)>();
              asm volatile("" : "+v"(fx[0])::"memory");
              tag_wait(rs, T, l * 16, OFF_CS + (h * 6 + w) * 1024, fx[0]);
              const f32x2 c = {sum_hi8(asf(fx[0][0])), sum_hi8(asf(fx[0][1]))};
              if (l < 8) *(LDS_AS f32x2*)(smem + L_CSL + w * 64 + l * 8) = c;
            }
          }
          constexpr int ABL = abl(IT), ABS = abs_(IT);
          if constexpr (s == ABL && n == 4) ld16h<0>(bv[0], rs, (l & 31) * 16, AB_OFF());
          if constexpr (s == ABS && n == 2) {
            wait_vm<SINCE(ABL)>();
            asm volatile("" : "+v"(bv[0])::"memory");
            tag_wait(rs, T, (l & 31) * 16, AB_OFF(), bv[0]);
            const unsigned t0 = bv[0][0], t1 = bv[0][1];
            ((LDS_AS unsigned*)(smem + L_ABW + rt * 2048))[l] = l < 32 ? t0 : t1;
          }
          if constexpr (s == ABS && n == 3) {
#pragma unroll
            for (int a = 0; a < 2; ++a) {
              const LDS_AS unsigned* aw = (const LDS_AS unsigned*)(smem + L_ABW + rt * 2048) + 16 * a;
              aq[a] = aw[l & 15]; bq[a] = aw[32 + (l & 15)];
              av[a] = *(const LDS_AS u32x4*)(aw + 4 * g); bv[a] = *(const LDS_AS u32x4*)(aw + 32 + 4 * g);
            }
          }
          if constexpr (s == ABS && n == 5) {
#pragma unroll
            for (int a = 0; a < 2; ++a) { b2[a] = f32x2{asf(bq[a]), asf(bq[a])}; asm volatile("" : "+v"(b2[a])); }
          }
          if constexpr (s == ABS + 1) {
            constexpr int k = (n + 4) % 6;
            if constexpr (k < 4) cs4[k & 1] = *(const LDS_AS f32x4*)(smem + L_CSL + k * 64 + g * 16);
            else cs4[k & 1][0] = *(const LDS_AS float*)(smem + L_CSL + k * 64 + (l & 15) * 4);
          }
          if constexpr ((s == ABS + 1 && n > 0) || (s == ABS + 2 && n == 0)) {
            constexpr int k = (n + 3) % 6;
            if constexpr (k < 4) {
#pragma unroll
              for (int a = 0; a < 2; ++a) acc[a][k] = __builtin_elementwise_fma(cs4[k & 1], f32x4{b2[a][0], b2[a][1], b2[a][0], b2[a][1]}, acc[a][k]);
            } else {
              f32x2 c2 = {cs4[k & 1][0], cs4[k & 1][0]};
              asm volatile("" : "+v"(c2));
#pragma unroll
              for (int a = 0; a < 2; ++a) acc[a][k] = __builtin_elementwise_fma(f32x4{c2[0], c2[1], c2[0], c2[1]}, __builtin_bit_cast(f32x4, bv[a]), acc[a][k]);
            }
          }
          if constexpr (IT == 0) {
            if constexpr (s == CSD && n < 3) fx[n] = frag0(SLOT(s >> 2), 3 * w + n);
            if constexpr (s == CSD + 1 && n == 1) {
              unsigned one2 = 0x3F803F80u;
              asm volatile("" : "+v"(one2));
              const u32x4 o4 = {one2, one2, one2, one2};
              const f32x4 z4 = {0.f, 0.f, 0.f, 0.f};
              sa = mfma16(fx[0], o4, z4); sb = mfma16(fx[1], o4, z4); sc = mfma16(fx[2], o4, z4);
            }
            if constexpr (s == CSD + 2 && n == 1) {
              const int c = l & 15;
              const f32x4 t0 = sa, t1 = sb, t2 = sc, t = c == 0 ? +t0 : c == 1 ? +t1 : +t2;
              if (c < 3) *(LDS_AS f32x4*)(smem + L_CSP + (w * 3 + c) * 64 + g * 16) = t;
              count_in(2);
            }
            if constexpr (s == CSD + 4 && n == 1) {
              if (w == 2) {
                qc = 0;
                count_wait(2, 8);
                const LDS_AS f32x2* cp = (const LDS_AS f32x2*)(smem + L_CSP) + min(l, 47);
                const f32x2 t = (cp[0] + cp[48]) + (cp[96] + cp[144]);
                if (l < 48) st16(u32x4{__builtin_bit_cast(unsigned, (float)t[0]), __builtin_bit_cast(unsigned, (float)t[1]), T[0], T[1]}, rs, (l >> 3) * 1024 + (l & 7) * 16, OFF_CS + h * 6144 + p * 128);
              }
            }
            if constexpr (s >= 29 && (n & 1)) keep[3 * (s - 29) + (n >> 1)] = frag0(SLOT(s - 24), 3 * w + (n >> 1));
            if constexpr (s == (TS0 < TEX ? TS0 : TEX) - 2 && n == 1) {
              ARGP(pwg, 5); ARGP(pwu, 6);
              const int t = tid < 288 ? tid - 64 : tid - 288, go = (bi * 32 + t / 7) * F_ + xcd * 448 + (t % 7) * 64;
              tsrc = tid < 64 ? pwo + bid * 4096 + tid * 64 : tid < 288 ? pwg + go : pwu + go;
              asm volatile("" : "+v"(tsrc));
            }
            if constexpr (s >= TS0 && s < TS0 + TSN + TSD * (7 >> TSG) && n == 4) {
              if (tstep == s && ((TM0 >> w) & 1)) TOUCH(tsrc);
            }
            if constexpr (s == TEX && n == 5) {
              const GAS bf16* t2 = ((TM1 >> w) & 1) ? tsrc : (const GAS bf16*)x + p * 262144;
              TOUCH(t2);
            }
          } else {
            if constexpr (s >= 27 && s < 30 && (n == 0 || n == 3)) { const unsigned wov = WO_VOFF(); WO_PIECE(4 + 2 * (s - 27) + n / 3); }
            if constexpr (WON && s == WOS && n == 4) st16(wof, rs, (ct * 128 + rb * 8 + w) * 1024 + l * 16, OFF_WOI);
            if constexpr (WON && s == 19 && n == 1) { if (tid == 64) st8(T, rs, (ct * 16 + rb) * 256, F_WOI); }
            if constexpr (WON && s == 23 && n == 4) { asm volatile("" : "=v"(cx1)); if (w == 2) ld8h(cx1, rs, (l & 15) * 256, F_WOI + ct * 4096); }
            if constexpr (WON && s == 26 && n == 3) {
              if (w == 2) {
                wait_vm<SINCE(23)>();
                asm volatile("" : "+v"(cx1)::"memory");
                if (!__all(cx1[0] == T[0] && cx1[1] == T[1])) poll_flags(rs, (l & 15) * 256, F_WOI + ct * 4096, T);
              }
            }
            if constexpr (s == FKL && n == 4) { asm volatile("" : "=v"(kv)); if (w == 0) ld4h<0>(kv, rs, (l & 15) * SKEY, KEY_AT(0, KREP())); }
            if constexpr (s == FSL && n == 1) {
              asm volatile("" : "=v"(fx[0]), "=v"(fx[1]));
              if (w == 0) {
                wait_vm<SINCE(FKL)>();
                asm volatile("" : "+v"(kv)::"memory");
                const unsigned k_ = key_wait(rs, T, l, 0, KREP(), kv);
                if (l == 0) *(LDS_AS unsigned*)(smem + L_SKEY + 32) = k_;
                fkey = k_;
                ld16h<0>(fx[0], rs, (l >> 3) * 256 + (l & 7) * 16, cand_off(k_, 0)); ld16h<2048>(fx[1], rs, (l >> 3) * 256 + (l & 7) * 16, cand_off(k_, 0));
              }
            }
            if constexpr (s == FS0 - 1 && n == FSN) {
              if (w == 0) {
                wait_vm<vm_at(1, s, n) - vm_at(1, FSL, 2)>();
                asm volatile("" : "+v"(fx[0]), "+v"(fx[1])::"memory");
                word_wait<2>(rs, T, (l >> 3) * 256 + (l & 7) * 16, cand_off(fkey, 0), fx);
                *(LDS_AS u32x4*)(smem + L_ROW + l * 16) = fx[0]; *(LDS_AS u32x4*)(smem + L_ROW + 1024 + l * 16) = fx[1];
                count_in(0);
              }
            }
            if constexpr (s == FS0 - 1 && n == 1) {
              asm volatile("" : "=v"(dfr[0]), "=v"(dfr[1]), "=v"(dfr[2]), "=v"(dfr[3]), "=v"(dfr[4]), "=v"(dfr[5]), "=v"(dfr[6]), "=v"(dfr[7]));
              if (ffn_on) {
#pragma unroll
                for (int i = 0; i < 8; ++i) dfr[i] = trread(smem, L_DN + w * 4096 + g * 1024 + ((l & 15) >> 2) * 256 + ((i ^ ((l >> 2) & 7)) * 32) + (l & 3) * 8);
              }
            }
            if constexpr (s == FS0) {
              if constexpr (n == 0) qc = __hip_atomic_load(qcnt, __ATOMIC_RELAXED, __HIP_MEMORY_SCOPE_WORKGROUP);
              if constexpr (n == 1) {
                count_wait(0, 1);
                kv = *(const LDS_AS unsigned*)(smem + L_SKEY + 32);
#pragma unroll
                for (int i = 0; i < 4; ++i) fx[i] = *(const LDS_AS u32x4*)(smem + L_ROW + w * 256 + i * 64 + g * 16);
              }
              if constexpr (n == 2) {
                fkey = __builtin_amdgcn_readfirstlane(kv);
                sel0 = key_row(fkey);
                aff = (int)(sel0 >> 8) == p;
                asm volatile("" : "=v"(sa), "=v"(sb));
                if (ffn_on) ffn_gu(gv, uv, fx, sa, sb);
              }
              if constexpr (n == 4) {
                if (ffn_on) {
                  ffn_gu_store(smem, l, w, sa, sb);
                  count_in(1);
                }
              }
            }
            if constexpr (s == FS0D) {
              if constexpr (n == 0) qc = __hip_atomic_load(qcnt + 1, __ATOMIC_RELAXED, __HIP_MEMORY_SCOPE_WORKGROUP);
              if constexpr (n == 1) { asm volatile("" : "=v"(sa)); if (ffn_on) { count_wait(1, 8); sa = ffn_d_read(smem, l); } }
              if constexpr (n == 2) { asm volatile("" : "=v"(hh)); if (ffn_on) hh = ffn_d_h(sa); }
              if constexpr (n == 4) { asm volatile("" : "=v"(sa)); if (ffn_on) sa = ffn_d_all(dfr, hh); }
              if constexpr (n == 5) { if (ffn_on) ffn_d_store(rs, l, w, 0, slice, sa); }
            }
            if constexpr (s == FS1 && n == 4) {
              DEF4(fx);
              asm volatile("" : "=v"(cxn), "=v"(cx1));
              if (!ffn_on) {
                const unsigned G = xcd * 4 + bi - 28;
#pragma unroll
                for (int k = 0; k < 4; ++k) ld16h<0>(fx[k], rs, RED_VO(k), RED_SO(0, G));
                ld8h(cxn, rs, RED_JJ(), cand_off(fkey, 0) + XO(G)); ld8h(cx1, rs, RED_JJ() + 128, cand_off(fkey, 0) + XO(G));
              }
            }
            if constexpr (s == FS1S && n == 2) {
              if (!ffn_on) {
                wait_vm<SINCE(FS1)>();
                PINV4(fx);
                asm volatile("" : "+v"(cxn), "+v"(cx1)::"memory");
                red_sum(rs, smem, T, tid, w, 0, xcd * 4 + bi - 28, fkey, fx, cxn, cx1);
                BAR();
                if (w == 0) {
                  const u32x2 ov = red_final(smem, l, red_gate(fkey), cxn, cx1);
                  if (l < 8) st8(unpoison2(ov), rs, OFF_XNEW + (xcd * 4 + bi - 28) * 256 + ((l & 3) * 2 + (l >> 2)) * 8, 0);
                }
              }
            }
            if constexpr (s == FXA && n == 4) { asm volatile("" : "=v"(fx[0])); if (w == 1) ld16h<0>(fx[0], rs, ((l >> 2) & 3) * 256 + (l & 3) * 16, XP_OFF()); }
            if constexpr (s == FXB - 1 && n == FSN) {
              if (w == 1) {
                wait_vm<SINCE(FXA)>();
                asm volatile("" : "+v"(fx[0])::"memory");
                word_wait<1>(rs, T, ((l >> 2) & 3) * 256 + (l & 3) * 16, XP_OFF(), fx);
                if (l < 16) *(LDS_AS u32x4*)(smem + L_ROW + l * 16) = fx[0];
                count_in(0);
              }
            }
            if constexpr (s == FXB) {
#define FX_FRAG(i) (*(const LDS_AS u32x4*)(size_t)(bo[0][0] + STAGE + (6 * (i) + w) * 1024))
              if constexpr (n == 0) { qc = __hip_atomic_load(qcnt, __ATOMIC_RELAXED, __HIP_MEMORY_SCOPE_WORKGROUP); }
              if constexpr (n == 1) {
                DEF4(fx); DEF4(df);
                if (w < 6) {
                  count_wait(0, 2);
#pragma unroll
                  for (int i = 0; i < 4; ++i) { fx[i] = *(const LDS_AS u32x4*)(smem + L_ROW + i * 64 + g * 16); df[i] = FX_FRAG(i); }
                }
              }
              if constexpr (n == 2) { asm volatile("" : "=v"(sa), "=v"(sb)); if (w < 6) dot_mma(fx); }
              if constexpr (n == 4) { if (w < 6) dot_store(OFF_FX); }
            }
            if constexpr (s == FXL && n == 4) {
              asm volatile("" : "=v"(fx[0]), "=v"(fx[1]));
              if (aff && w < 4) { ld16h<0>(fx[0], rs, FP_VO(), FP_OFF(0)); ld16h<0>(fx[1], rs, FP_VO(), FP_OFF(1)); }
            }
            if constexpr (s == FXS && n == 4) {
              if (aff && w < 4) {
                wait_vm<0>();
                asm volatile("" : "+v"(fx[0]), "+v"(fx[1])::"memory");
                if (!FX_OK(2)) {
                  unsigned spins = 0;
                  do {
                    if (spin_fail(spins, rs, T)) break;
                    fx[0] = ld16(rs, FP_VO(), FP_OFF(0)); fx[1] = ld16(rs, FP_VO(), FP_OFF(1));
                  } while (!FX_OK(2));
                }
                if (w == 3) {
                  f32x2 nx[8];
                  unpack8v(fx[0], nx); unpack8v(fx[1], nx + 4);
                  const f32x2 sm = ((nx[0] + nx[1]) + (nx[2] + nx[3])) + ((nx[4] + nx[5]) + (nx[6] + nx[7]));
                  const float mean = sum64(hadd(sm)) * (1.0f / 1024.0f);
                  const f32x2 m2 = {mean, mean};
                  f32x2 q = {0.f, 0.f};
#pragma unroll
                  for (int j = 0; j < 8; ++j) { const f32x2 d = nx[j] - m2; q = __builtin_elementwise_fma(d, d, q); }
                  const float rd = ln_rd(sum64(hadd(q)) * (1.0f / 1024.0f));
                  if (l == 0) *(LDS_AS f32x2*)(smem + L_FXL + 384) = f32x2{rd, -rbf1(mean)};
                } else {
                  tile_sum(fx[0], L_FXL);
                }
                count_in(3);
              }
            }
          }
        };
        static_for<32>([&](auto sc_) __attribute__((always_inline)) {
          constexpr int s = decltype(sc_)::value;
          if constexpr (s > 0) PIN_ACC();
          if constexpr (QPR && s == QPR) { if (w >= 4) __builtin_amdgcn_s_setprio(1); }
          if constexpr (IT == 1) wait_vm<vm_at(IT, s, 0) - vm_a(IT, s)>();
          pin_w<s % WA>(af[s % WA][0], af[s % WA][1]);
          SB();
          static_for<6>([&](auto nc_) __attribute__((always_inline)) {
            constexpr int n = decltype(nc_)::value;
            if constexpr (s == 0) { acc[0][n] = f32x4{0.f, 0.f, 0.f, 0.f}; acc[1][n] = acc[0][n]; }
            constexpr bool on = s < QL || n >= 2;
            if constexpr (on) acc[0][n] = n < 4 ? mfma16(bfr[n].v, af[s % WA][0], acc[0][n]) : mfma16(af[s % WA][0], bfr[n].v, acc[0][n]);
            SB();
            if constexpr (n == 0) { QKV_RDB(s, 5); }
            else if constexpr (s + 1 < 32 && (s + 1 < QL || n > 2)) QKV_RDB(s + 1, n - 1);
            SB();
            if constexpr (on) acc[1][n] = n < 4 ? mfma16(bfr[n].v, af[s % WA][1], acc[1][n]) : mfma16(af[s % WA][1], bfr[n].v, acc[1][n]);
            SB();
            if constexpr (s + 1 < 32 && (s + 1 < QL || n >= 2)) QKV_RDA(s + 1, n);
            SB();
            gap(sc_, nc_);
            SB();
          });
        });
        PIN_ACC();
        frag_t qw[32 - QL][2];
        static_for<32 - QL>([&](auto c_) __attribute__((always_inline)) {
          constexpr int s = QL + decltype(c_)::value;
          QKV_RDA(s, 0); QKV_RDB(s, 0); QKV_RDA(s, 1); QKV_RDB(s, 1);
          qw[s - QL][0] = bfr[0]; qw[s - QL][1] = bfr[1];
        });
        SB();
        const bool own = aff && (int)((sel0 >> 5) & 7) == rt;
        const bool m0 = !(sel0 & 16), mq = (l & 15) == (int)(sel0 & 15), mv = g == (int)((sel0 & 15) >> 2);
        auto patch_qk = [&](int n) __attribute__((always_inline)) {
          f32x4 t = *(const LDS_AS f32x4*)(smem + L_FXL + n * 64 + g * 16);
          const f32x4 c = *(const LDS_AS f32x4*)(smem + L_CSL + n * 64 + g * 16);
          const float nb = *(const LDS_AS float*)(smem + L_FXL + 388);
#pragma unroll
          for (int e = 0; e < 4; ++e) { t[e] = __builtin_fmaf(c[e], nb, t[e]); asm volatile("" : "+v"(t[e])); }
          acc[0][n] = (mq && m0) ? t : acc[0][n];
          acc[1][n] = (mq && !m0) ? t : acc[1][n];
        };
        if (own) {
          while (__hip_atomic_load(qcnt + 3, __ATOMIC_RELAXED, __HIP_MEMORY_SCOPE_WORKGROUP) < 4u) {}
          asm volatile("" ::: "memory");
          patch_qk(2); patch_qk(3);
          const f32x2 nab = *(const LDS_AS f32x2*)(smem + L_FXL + 384);
          const LDS_AS float* fl_ = (const LDS_AS float*)(smem + L_FXL + 256) + (l & 15);
          const LDS_AS float* cl_ = (const LDS_AS float*)(smem + L_CSL + 256) + (l & 15);
          const float t4 = __builtin_fmaf(cl_[0], nab[1], fl_[0]), t5 = __builtin_fmaf(cl_[16], nab[1], fl_[16]);
          const unsigned na = __builtin_bit_cast(unsigned, (float)nab[0]);
#pragma unroll
          for (int a = 0; a < 2; ++a) {
            const bool ma = a ? !m0 : m0;
            aq[a] = (mq && ma) ? na : aq[a];
#pragma unroll
            for (int e = 0; e < 4; ++e) {
              const bool me = mv && ma && e == (int)(sel0 & 3);
              acc[a][4][e] = me ? t4 : acc[a][4][e]; acc[a][5][e] = me ? t5 : acc[a][5][e];
              av[a][e] = me ? na : av[a][e];
            }
          }
        }
        constexpr float C2 = 1.4426950408889634f * 0.17677669529663687f;
#pragma unroll
        for (int a = 0; a < 2; ++a) {
          f32x2 a2 = {asf(aq[a]), asf(aq[a])};
          qs2[a] = f32x2{asf(aq[a]) * C2, asf(aq[a]) * C2};
          asm volatile("" : "+v"(a2), "+v"(qs2[a]));
          acc[a][2] *= f32x4{a2[0], a2[1], a2[0], a2[1]}; acc[a][3] *= f32x4{a2[0], a2[1], a2[0], a2[1]};
          acc[a][4] *= __builtin_bit_cast(f32x4, av[a]); acc[a][5] *= __builtin_bit_cast(f32x4, av[a]);
        }
        k0 = pack8(acc[0][2], acc[0][3]); k1 = pack8(acc[1][2], acc[1][3]); v0 = pack8(acc[0][4], acc[1][4]); v1 = pack8(acc[0][5], acc[1][5]);
        const unsigned kvo = OFF_KV + IT * (8u << 20) + (p * 32 + h) * 32768 + rt * 4096;
        if (p < 7) { st16(k0, rs, l * 16, kvo); st16(k1, rs, l * 16 + 1024, kvo); st16(v0, rs, l * 16 + 2048, kvo); st16(v1, rs, l * 16 + 3072, kvo); }
        char* ownp = smem + (8 + rt) * 2048 + l * 16;
        *(u32x4*)(ownp + L_K) = k0;
        if (rt < 7) *(u32x4*)(ownp + L_K + 1024) = k1;
        *(u32x4*)(ownp + L_V) = v0; *(u32x4*)(ownp + L_V + 1024) = v1;
        SB();
        static_for<32 - QL>([&](auto c_) __attribute__((always_inline)) {
          constexpr int s = QL + decltype(c_)::value;
#pragma unroll
          for (int n = 0; n < 2; ++n) { acc[0][n] = mfma16(qw[s - QL][n].v, af[s % WA][0], acc[0][n]); acc[1][n] = mfma16(qw[s - QL][n].v, af[s % WA][1], acc[1][n]); }
        });
        if (own) { patch_qk(0); patch_qk(1); }
      };
      qkv_loop(it_c);
    }
#undef A_LOAD1
#undef QKV_RDA
#undef QKV_RDB
#undef QKV_BO
    {
      IDS();
      const GAS bf16 *pwo, *pwg, *pwu;
      asm volatile("s_load_dwordx2 %0, %3, 24\n\ts_load_dwordx2 %1, %3, 40\n\ts_load_dwordx2 %2, %3, 48" : "=&s"(pwo), "=&s"(pwg), "=&s"(pwu) : "s"(ka));
      const int ql = l & 15;
      const f32x4 z4 = {0.f, 0.f, 0.f, 0.f}, ninf = {-INFINITY, -INFINITY, -INFINITY, -INFINITY};
      LDS_AS unsigned* rdy = (LDS_AS unsigned*)(smem + L_RDY);
      const unsigned rtag = T[0] + it;
      u32x4 qf[2];
      f32x4 cn[2], oa[2][2], sum[2], S[2][2];
      bool told = false;
      auto announce = [&](bool now) __attribute__((always_inline)) {
        if (!told) {
          unsigned st;
          asm volatile("s_getreg_b32 %0, hwreg(HW_REG_IB_STS)" : "=s"(st)::"memory");
          if (now || !(st & 0xC0000Fu)) {
            DRAIN();
            if (l == 0) st8(T, rs, rt * SKW, F_KV + (it * 256 + p * 32 + h) * SKV);
            told = true;
          }
        }
      };
      unsigned one2 = 0x3F803F80u;
      asm volatile("" : "+v"(one2));
      const u32x4 ones = {one2, one2, one2, one2};
      auto ex8 = [&](f32x4 a, f32x4 b) __attribute__((always_inline)) {
        return u32x4{pack2(__builtin_amdgcn_exp2f(a[0]), __builtin_amdgcn_exp2f(a[1])), pack2(__builtin_amdgcn_exp2f(a[2]), __builtin_amdgcn_exp2f(a[3])),
                     pack2(__builtin_amdgcn_exp2f(b[0]), __builtin_amdgcn_exp2f(b[1])), pack2(__builtin_amdgcn_exp2f(b[2]), __builtin_amdgcn_exp2f(b[3]))};
      };
      {
#pragma unroll
        for (int a = 0; a < 2; ++a) {
          const f32x4 c4 = {qs2[a][0], qs2[a][1], qs2[a][0], qs2[a][1]};
          qf[a] = pack8(acc[a][0] * c4, acc[a][1] * c4);
        }
        f32x4 mk;
#pragma unroll
        for (int e = 0; e < 4; ++e) mk[e] = (4 * g + e <= ql) ? 0.f : -INFINITY;
        const f32x4 d0 = mfma16(k0, qf[0], mk), d1 = mfma16(k1, qf[1], mk), d2 = mfma16(k0, qf[1], z4);
        const float m0 = -max_x32(max_x16(fmaxf(fmaxf(d0[0], d0[1]), fmaxf(d0[2], d0[3]))));
        const float m1 = -max_x32(max_x16(fmaxf(fmaxf(d1[0], d1[1]), fmaxf(d1[2], d1[3]))));
        cn[0] = f32x4{m0, m0, m0, m0}; cn[1] = f32x4{m1, m1, m1, m1};
        asm volatile("" : "+v"(cn[0]), "+v"(cn[1]));
        announce(false);
        const f32x4 e0 = d0 + cn[0];
        const u32x4 p0 = {pack2(__builtin_amdgcn_exp2f(e0[0]), __builtin_amdgcn_exp2f(e0[1])), pack2(__builtin_amdgcn_exp2f(e0[2]), __builtin_amdgcn_exp2f(e0[3])), 0u, 0u};
        const u32x4 p1 = ex8(d2 + cn[1], d1 + cn[1]);
        oa[0][0] = mfma16(v0, p0, z4); oa[0][1] = mfma16(v1, p0, z4); sum[0] = mfma16(ones, p0, z4);
        oa[1][0] = mfma16(v0, p1, z4); oa[1][1] = mfma16(v1, p1, z4); sum[1] = mfma16(ones, p1, z4);
      }
      const char* kb_ = smem + L_K + l * 16;
      const char* vb_ = smem + L_V + l * 16;
      const unsigned wov = WO_VOFF(), fnv = (l >> 1) * (F_ * 2) + (l & 1) * 16;
      asm volatile("" : "+v"(oa[0][0]), "+v"(oa[0][1]), "+v"(oa[1][0]), "+v"(oa[1][1]), "+v"(sum[0]), "+v"(sum[1]));
      const unsigned pw = __builtin_amdgcn_readfirstlane(it * 256 + p * 32 + h - 32), pfo = F_KV + pw * SKV + rt * SKW;
      const bool fetcher = rt < 4 && p > 0;
      announce(false);
      if (QPR) __builtin_amdgcn_s_setprio(0);
      BAR();
      asm volatile("" : "+s"(pwo), "+s"(pwg), "+s"(pwu));
#define KV_FETCH(gd)                                                                                                          \
  {                                                                                                                           \
    const unsigned pvo = OFF_KV + pw * 32768 + (gd) * 4096;                                                                   \
    dma16s<0>(rs, l * 16, pvo, lbase + L_K + (gd) * 2048); dma16s<1024>(rs, l * 16, pvo, lbase + L_K + (gd) * 2048);           \
    dma16s<0>(rs, l * 16, pvo + 2048, lbase + L_V + (gd) * 2048); dma16s<1024>(rs, l * 16, pvo + 2048, lbase + L_V + (gd) * 2048); \
  }
      auto fetch = [&]() __attribute__((always_inline)) {
        poll_flags(rs, (l & 1) * 4 * SKW, pfo, T);
        KV_FETCH(rt); KV_FETCH(rt + 4);
        if (l == 0) rdy[8 + rt] = rtag;
      };
      announce(false);
      const bool first = fetcher && told;
      if (first) fetch();
#define LDS16(a_) (*(const LDS_AS u32x4*)(size_t)(a_))
      u32x4 kf[2], vf[2], p0, p1;
#define AE(a, i) { S[a][(i) >> 2][(i) & 3] = __builtin_amdgcn_exp2f(S[a][(i) >> 2][(i) & 3]); asm volatile("" : "+v"(S[a][(i) >> 2][(i) & 3])); SB(); }
#define AC(a, P, j) { P[j] = pack2(S[a][(j) >> 1][2 * ((j) & 1)], S[a][(j) >> 1][2 * ((j) & 1) + 1]); asm volatile("" : "+v"(P[j])); SB(); }
#define PHASE(a, P, M1, M2, L2, M3, M4, L4, M5)                                                                               \
  AE(a, 0) AE(a, 1) M1; SB(); AE(a, 2) AE(a, 3) M2; SB(); L2; SB(); AC(a, P, 0) AC(a, P, 1) AE(a, 4) M3; SB(); AE(a, 5) AE(a, 6) M4; SB(); \
  L4; SB(); AE(a, 7) AC(a, P, 2) M5; SB(); AC(a, P, 3)
#define PV(a, d, P) oa[a][d] = mfma16(vf[d], P, oa[a][d])
#define RS(a, P) sum[a] = mfma16(ones, P, sum[a])
#define SC(a, t, c) S[a][t] = mfma16(kf[t], qf[a], c)
#define K_AT(ad) (kf[0] = LDS16(ad), kf[1] = LDS16((ad) + 1024))
#define V_AT(ad) (vf[0] = LDS16((ad) + L_V), vf[1] = LDS16((ad) + L_V + 1024))
#define A_FIRST(ad, c00, c01, c10, c11, KN) { K_AT(ad); V_AT(ad); SB(); SC(0, 0, c00); SC(0, 1, c01); SB(); PHASE(0, p0, SC(1, 0, c10), SC(1, 1, c11), KN, , , , ) }
#define A_NEXT(ad, c00, c01, c10, c11, KN)                                                                                        \
  { PHASE(1, p1, PV(0, 0, p0), PV(0, 1, p0), , SC(0, 0, c00), SC(0, 1, c01), , RS(0, p0))                                     \
    PHASE(0, p0, PV(1, 0, p1), PV(1, 1, p1), V_AT(ad), SC(1, 0, c10), SC(1, 1, c11), KN, RS(1, p1)) }
#define A_LAST() { PHASE(1, p1, PV(0, 0, p0), PV(0, 1, p0), , RS(0, p0), , , ) SB(); PV(1, 0, p1); PV(1, 1, p1); RS(1, p1); }
      int nw = 0;
      auto piece = [&]() __attribute__((always_inline)) {
        if constexpr (it == 0) { if (ffn_on && nw < 8) { FFN_DMA(nw); } ++nw; }
      };
#define XN_OFF() (F_XN + (((rt - 6) * 4 + g) * RX + bi % RX) * 256 + (l & 15) * 16)
      u32x4 fxn;
      asm volatile("" : "=v"(fxn));
      unsigned ad = lbase + l * 16 + (6 + rt) * 2048;
      asm volatile("" : "+v"(ad));
      if (rt > 0) {
        A_FIRST(ad + 2048, cn[0], cn[0], cn[1], cn[1], K_AT(ad));
#pragma unroll 1
        for (int i = rt - 1; i > 0; --i) {
          ad -= 2048;
          asm volatile("" : "+v"(ad));
          if (told && rt >= 4) piece();
          announce(false);
          A_NEXT(ad + 2048, cn[0], cn[0], cn[1], cn[1], K_AT(ad));
        }
        announce(false);
        A_LAST();
      }
      announce(true);
      {
        f32x4 mk;
#pragma unroll
        for (int e = 0; e < 4; ++e) mk[e] = (4 * g + e > ql) ? 0.f : -INFINITY;
        f32x4 cm0 = cn[0] + mk, cm1 = cn[1] + mk, cmi = ninf;
        asm volatile("" : "+v"(cm0), "+v"(cm1), "+v"(cmi));
        if (rt >= 4) {
          if (p > 0) {
            while (!__all(__hip_atomic_load(rdy + 8 + (l & 3), __ATOMIC_RELAXED, __HIP_MEMORY_SCOPE_WORKGROUP) == rtag)) __builtin_amdgcn_s_sleep(1);
            asm volatile("" ::: "memory");
          }
          if constexpr (it == 0) { for (; nw < 8;) piece(); }
          static_for<it ? 12 : 8>([&](auto c_) __attribute__((always_inline)) { constexpr int i = decltype(c_)::value; WO_PIECE_W(15 - i / 2, 2 * (rt & 3) + (i & 1)); });
          if (it == 0 && rt >= 6) ld16h<0>(fxn, rs, XN_OFF(), 0);
        } else if (p > 0) {
          if (!first) fetch();
          DRAIN();
          if (l < 2) rdy[rt + 4 * l] = rtag;
          if constexpr (it == 0) { for (; nw <= rt;) piece(); }
        }
        if (p > 0) {
          unsigned have = 0;
          auto need = [&](int gq) __attribute__((always_inline)) {
            if (__builtin_expect(gq < 8 && !((have >> gq) & 1), 0)) {
              do {
                const unsigned t = __hip_atomic_load(rdy + (l & 7), __ATOMIC_RELAXED, __HIP_MEMORY_SCOPE_WORKGROUP);
                have = (unsigned)__builtin_amdgcn_ballot_w64(t == rtag);
              } while (!((have >> gq) & 1));
              asm volatile("" ::: "memory");
            }
          };
          ad = lbase + l * 16 + rt * 2048;
          asm volatile("" : "+v"(ad));
          need(rt);
          K_AT(ad); V_AT(ad); SB(); SC(0, 0, cm0); SC(0, 1, cn[0]); SB();
          PHASE(0, p0, SC(1, 0, cmi), SC(1, 1, cm1), , , , , )
          need(rt + 1);
          K_AT(ad + 2048);
#pragma unroll 1
          for (int gq = rt + 1; gq < 8; ++gq) {
            ad += 2048;
            asm volatile("" : "+v"(ad));
            piece();
            need(gq + 1);
            A_NEXT(ad, cn[0], cn[0], cn[1], cn[1], K_AT(ad + 2048));
          }
          A_LAST();
        }
        if constexpr (it == 0) { for (; nw < 8;) piece(); }
      }
      auto o_store = [&]() __attribute__((always_inline)) {
        const unsigned oo = OFF_O + it * (4u << 20) + ((p * 8 + rt) * 64 + h * 2) * 1024;
#pragma unroll
        for (int a = 0; a < 2; ++a) {
          f32x2 inv = {__builtin_amdgcn_rcpf(sum[a][0]), 0.f};
          inv[1] = inv[0];
          asm volatile("" : "+v"(inv));
          const f32x4 i4 = {inv[0], inv[1], inv[0], inv[1]};
          st16(pack8(oa[a][0] * i4, oa[a][1] * i4), rs, l * 16 + a * 1024, oo);
        }
      };
      {
        float big = 0.f;
#pragma unroll
        for (int a = 0; a < 2; ++a)
#pragma unroll
          for (int d = 0; d < 2; ++d)
            big = __builtin_elementwise_maximum(__builtin_elementwise_maximum(big, __builtin_elementwise_maximum(fabsf(oa[a][d][0]), fabsf(oa[a][d][1]))),
                                                __builtin_elementwise_maximum(fabsf(oa[a][d][2]), fabsf(oa[a][d][3])));
        bool ok = sum[0][0] >= 0.5f && sum[0][0] < 0x1p64f && sum[1][0] >= 0.5f && sum[1][0] < 0x1p64f && big < INFINITY;
        if (!__all(ok)) {
#pragma unroll
          for (int a = 0; a < 2; ++a) {
            const int qs = 2 * rt + a, qq = 256 + 16 * qs + ql, klo = max(qq - 255, p > 0 ? 0 : 256);
            auto score = [&](int t) __attribute__((always_inline)) {
              f32x4 s = mfma16(t == 31 ? k1 : *(const u32x4*)(kb_ + t * 1024), qf[a], z4);
#pragma unroll
              for (int e = 0; e < 4; ++e) { const int kk = 16 * t + 4 * g + e; s[e] = (kk <= qq && kk >= klo) ? s[e] : -INFINITY; }
              return s;
            };
            float mx = -INFINITY;
#pragma unroll 1
            for (int t = p > 0 ? qs : 16; t <= qs + 16; ++t) { const f32x4 s = score(t); mx = fmaxf(fmaxf(mx, fmaxf(s[0], s[1])), fmaxf(s[2], s[3])); }
            mx = max_x32(max_x16(mx));
            oa[a][0] = z4; oa[a][1] = z4; sum[a] = z4;
#pragma unroll 1
            for (int G = p > 0 ? qs >> 1 : 8; G <= (qs + 16) >> 1; ++G) {
              const f32x4 s0 = score(2 * G), s1 = score(2 * G + 1);
              f32x4 e0, e1;
#pragma unroll
              for (int e = 0; e < 4; ++e) { e0[e] = __builtin_amdgcn_exp2f(s0[e] - mx); e1[e] = __builtin_amdgcn_exp2f(s1[e] - mx); }
              const u32x4 pf = pack8(e0, e1);
              oa[a][0] = mfma16(*(const u32x4*)(vb_ + G * 2048), pf, oa[a][0]);
              oa[a][1] = mfma16(*(const u32x4*)(vb_ + G * 2048 + 1024), pf, oa[a][1]);
              sum[a] = mfma16(ones, pf, sum[a]);
            }
          }
        }
      }
      o_store();
      if constexpr (it == 0) {
        wait_vm<2>();
        asm volatile("" : "+v"(fxn)::"memory");
        if (rt >= 6 && !__all(fxn[0] == T[0] && fxn[1] == T[1] && fxn[2] == T[0] && fxn[3] == T[1])) poll_flags2(rs, XN_OFF(), T);
        DEF4(gv); DEF4(uv);
        if (ffn_on) {
          const unsigned ro = 65536 + w * 1024 + (4 * g + ((l & 15) >> 2)) * 32 + (l & 3) * 8;
#pragma unroll
          for (int i = 0; i < 4; ++i) {
            frag_t f;
            f.hf.a = trread(smem, ro + i * 16384); f.hf.b = trread(smem, ro + i * 16384 + 512);
            gv[i] = f.v;
            f.hf.a = trread(smem, ro + i * 16384 + 8192); f.hf.b = trread(smem, ro + i * 16384 + 8704);
            uv[i] = f.v;
          }
        }
        PINV4(gv); PINV4(uv);
      }
      BAR();
      if constexpr (it == 1) { WO_PIECE(3); WO_PIECE(2); WO_PIECE(1); WO_PIECE(0); }
      wait_vm<it ? 4 : 0>();
      asm volatile("s_barrier" ::: "memory");
      if (tid == 0) st8(T, rs, h * SO, F_O + (it * 8 + p) * 32 * SO);
      if constexpr (it == 0) {
        WO_PIECE(11); WO_PIECE(10); WO_PIECE(9); WO_PIECE(8); WO_PIECE(7); WO_PIECE(6); WO_PIECE(5); WO_PIECE(4);
        WO_PIECE(3); WO_PIECE(2); WO_PIECE(1); WO_PIECE(0);
      }
    }
    IDS();
    ARGP(pwr, 4); ARGP(pwd, 7); ARGP(pout, 8); ARGP(px, 0);
    const int kh = w >> 2, rt2 = rb * 4 + (w & 3);
    const unsigned R = rt2 * 32 + kh * 16 + (l & 15);
    const unsigned to = (rt2 * 64 + ct * 4 + kh) * 1024 + l * 16;
    const unsigned rmo = ct * 64 + ((g & 1) ? 12 + 4 * g : 4 * g);
    f32x2 wv[8];
    {
      u32x4 wa[2], wrp[2], res[2];
#pragma unroll
      for (int j = 0; j < 2; ++j) {
        wrp[j] = *(const GAS u32x4*)(pwr + rmo + 32 * j);
        if constexpr (it == 0) wa[j] = *(const GAS u32x4*)(pwr + l * 8 + j * 512);
        if constexpr (it == 0) res[j] = *(const GAS u32x4*)(px + (R - (l & 15) + 8 * j + (l >> 3)) * 1024 + ct * 64 + (l & 7) * 8);
        else res[j] = ld16(rs, OFF_XNEW + (2 * ct + j) * 256 + g * 16, 0);
      }
      SB();
      unsigned bt[4];
#pragma unroll
      for (int n = 0; n < 4; ++n) {
        bt[n] = lbase + 32768 + 65536 + kh * 4096 + (4 * g + ((l & 15) >> 2)) * 128 + (l & 3) * 8 + ((n ^ (2 * (g & 1) + ((l & 15) >> 3))) << 5);
        asm volatile("" : "+v"(bt[n]));
      }
      if (w == 0) poll_flags(rs, (l & 31) * SO, F_O + (it * 8 + p) * 32 * SO, T);
      asm volatile("s_barrier" ::: "memory");
      auto prep = [&]() __attribute__((always_inline)) {
        if constexpr (it == 0) {
          f32x2 t[8];
          unpack8v(wa[0], t); unpack8v(wa[1], t + 4);
          const f32x2 s = ((t[0] + t[1]) + (t[2] + t[3])) + ((t[4] + t[5]) + (t[6] + t[7]));
          wsum = sum64(hadd(s));
        }
        unpack8v(trade16(wrp[0]), wv); unpack8v(trade16(wrp[1]), wv + 4);
        if constexpr (it == 0) {
          char* tp = smem + w * 2048;
          const unsigned c0 = (g & 1) * 2 + (g >> 1), r = l & 15;
#pragma unroll
          for (int j = 0; j < 2; ++j) *(u32x4*)(tp + j * 1024 + (l >> 3) * 128 + (((l ^ (l >> 3)) & 7) << 4)) = res[j];
          res[0] = trade16(*(const u32x4*)(tp + r * 128 + ((c0 ^ (r & 7)) << 4)));
          res[1] = trade16(*(const u32x4*)(tp + r * 128 + (((c0 + 4) ^ (r & 7)) << 4)));
        }
        else { res[0] = R == sel0 ? res[0] : x2p[0]; res[1] = R == sel0 ? res[1] : x2p[1]; }
        unpack8v(res[0], v + 0); unpack8v(res[1], v + 4);
        asm volatile("" : "+v"(v[0]), "+v"(v[1]), "+v"(v[2]), "+v"(v[3]), "+v"(v[4]), "+v"(v[5]), "+v"(v[6]), "+v"(v[7]));
        asm volatile("" : "+v"(wsum), "+v"(wv[0]), "+v"(wv[1]), "+v"(wv[2]), "+v"(wv[3]), "+v"(wv[4]), "+v"(wv[5]), "+v"(wv[6]), "+v"(wv[7]));
      };
      if constexpr (it == 1 && WON) {
        const unsigned abase = OFF_O + (4u << 20) + (rt2 * 64 + kh) * 1024;
        u32x4 af[W2B], bfr[W2L + 1][4];
        unsigned b2[2] = {lbase + 32768 + l * 16, lbase + 98304 + l * 16};
        asm volatile("" : "+v"(b2[0]), "+v"(b2[1]));
#define O_LOAD(s_) af[(s_) % W2B] = ld16(rs, l * 16 + (~(s_) & 1) * 2048, abase + ((31 - (s_)) >> 1) * 4096)
#define WO_RD(s_, n) bfr[(s_) % (W2L + 1)][n] = *(const LDS_AS u32x4*)(size_t)(b2[(31 - (s_)) >> 4] + ((31 - (s_)) & 15) * 4096 + (n) * 1024)
#pragma unroll
        for (int s = 0; s < W2R; ++s) O_LOAD(s);
        SB();
        prep();
        SB();
        f32x4 ac[4];
#pragma unroll
        for (int n = 0; n < 4; ++n) ac[n] = f32x4{v[2 * n][0], v[2 * n][1], v[2 * n + 1][0], v[2 * n + 1][1]};
#pragma unroll
        for (int n = 0; n < 4 * W2L; ++n) WO_RD(n >> 2, n & 3);
#pragma unroll
        for (int s = 0; s < 32; ++s) {
          if (s == W2P && w >= 4) __builtin_amdgcn_s_setprio(1);
          if (s == 24 - W2L) asm volatile("s_barrier" ::: "memory");
          if (s < W2B - W2R) O_LOAD(W2R + 2 * s);
          SB();
#pragma unroll
          for (int n = 0; n < 4; ++n) {
            ac[n] = mfma16(bfr[s % (W2L + 1)][n], af[s % W2B], ac[n]);
            if (s + W2L < 32) WO_RD(s + W2L, n);
            SB();
          }
          if (s < W2B - W2R) O_LOAD(W2R + 2 * s + 1);
          else if (s + W2B < 32) O_LOAD(s + W2B);
          SB();
        }
        __builtin_amdgcn_s_setprio(0);
#undef WO_RD
#undef O_LOAD
#pragma unroll
        for (int n = 0; n < 4; ++n) { v[2 * n] = f32x2{ac[n][0], ac[n][1]}; v[2 * n + 1] = f32x2{ac[n][2], ac[n][3]}; }
      } else {
      const unsigned abase = OFF_O + it * (4u << 20) + (rt2 * 64 + 32 * kh) * 1024;
      const unsigned av0 = l * 16 + kh * 1024, av1 = l * 16 + (kh ^ 1) * 1024;
      u32x4 af[WB][2];
#define O_LOAD(s_) { af[(s_) % WB][0] = ld16(rs, av0 + (~(s_) & 1) * 2048, abase + ((15 - (s_)) >> 1) * 4096); af[(s_) % WB][1] = ld16(rs, av1 + (~(s_) & 1) * 2048, abase + ((15 - (s_)) >> 1) * 4096); }
#pragma unroll
      for (int s = 0; s < WR; ++s) O_LOAD(s);
      SB();
      prep();
      SB();
      f32x4 ac[2][4];
#pragma unroll
      for (int a = 0; a < 2; ++a)
#pragma unroll
        for (int n = 0; n < 4; ++n) ac[a][n] = f32x4{0.f, 0.f, 0.f, 0.f};
      u32x4 bfr[WLA + 1][4];
#define WO_RD(buf, s, n)                                                                             \
  {                                                                                                  \
    const unsigned o_ = bt[n] + ((15 - (s)) & 7) * 8192;     \
    frag_t f_;                                                                                       \
    f_.hf.a = trabs(o_); f_.hf.b = trabs(o_ + 2048);                                                 \
    bfr[buf][n] = f_.v;                                                                              \
  }
#pragma unroll
      for (int n = 0; n < 4 * WLA; ++n) WO_RD(n >> 2, n >> 2, n & 3);
#pragma unroll
      for (int s = 0; s < 16; ++s) {
        const int cur = s % (WLA + 1);
        if (WPR && s == WPR && w >= 4) __builtin_amdgcn_s_setprio(1);
        asm volatile("" : "+{v[176:179]}"(ac[0][0]), "+{v[180:183]}"(ac[0][1]), "+{v[184:187]}"(ac[0][2]), "+{v[188:191]}"(ac[0][3]),
                          "+{v[192:195]}"(ac[1][0]), "+{v[196:199]}"(ac[1][1]), "+{v[200:203]}"(ac[1][2]), "+{v[204:207]}"(ac[1][3]));
        if (s == (it == 0 ? 4 : 12) - WLA) asm volatile("s_barrier" ::: "memory");
        if (s == 8 - WLA) {
#pragma unroll
          for (int n = 0; n < 4; ++n) { bt[n] -= 65536; asm volatile("" : "+v"(bt[n])); }
        }
        if (it == 0 && s == WIS) {
#pragma unroll
          for (int i = 0; i < 3; ++i) st16(keep[i], rs, l * 16 + i * 1024, OFF_WI + (h * 8 + ((ROT0 + 5) & 7)) * STAGE + w * 3072);
        }
        if (it == 0 && s == WPS) {
          unsigned pz1 = POISON;
          asm volatile("" : "+v"(pz1));
          const u32x4 pz = {pz1, pz1, pz1, pz1};
          st16(pz, rs, OFF_PART + (tid >> 8) * (1u << 20) + bid * 4096 + (tid & 255) * 16, 0);
          if (w < 2) st16(pz, rs, OFF_STAT + 524288 + (rb * 16 + ct) * 2048 + tid * 16, 0);
          if (w == 4 && l < 40) st16(pz, rs, l < 16 ? OFF_CA + 65536 + rb * 4096 + ct * 256 + l * 16 : OFF_FX + (h * 8 + p) * 512 + (l - 16) * 16, 0);
          if (w == 6 && l < (ct < (int)RKEY ? 3 : 2)) st4(pz1, rs, l < 2 ? OFF_XNEW + (bid >> 3) * 256 + (bid & 7) * 8 + l * 4 : KEY_AT(1, ct) + rb * SKEY, 0);
        }
        if (s < WB - WR) O_LOAD(WR + 2 * s < 16 ? WR + 2 * s : 0);
        SB();
#pragma unroll
        for (int n = 0; n < 4; ++n) {
          ac[0][n] = mfma16(bfr[cur][n], af[s % WB][0], ac[0][n]);
          ac[1][n] = mfma16(bfr[cur][n], af[s % WB][1], ac[1][n]);
          if (s + WLA < 16) WO_RD((s + WLA) % (WLA + 1), s + WLA, n);
          SB();
        }
        if (s < WB - WR) { O_LOAD(WR + 2 * s + 1); }
        else if (s + WB < 16) { O_LOAD(s + WB); }
        SB();
      }
      if (WPR) __builtin_amdgcn_s_setprio(0);
#undef WO_RD
#undef O_LOAD
      if constexpr (it == 0 && WON) {
        const int ks = 2 * rb + kh;
        const unsigned o_ = lbase + 32768 + (ks & 15) * 8192 + (ks >> 4) * 4096 + (4 * g + ((l & 15) >> 2)) * 128 + (l & 3) * 8 + (((w & 3) ^ (2 * (g & 1) + ((l & 15) >> 3))) << 5);
        frag_t f_;
        f_.hf.a = trabs(o_); f_.hf.b = trabs(o_ + 2048);
        wof = f_.v;
        asm volatile("" : "+v"(wof));
      }
#define XCH_AT(v) ((v) == 7 ? (rb == 7 ? 139264 : 155648) : (v) * 4096)
#pragma unroll
      for (int n = 0; n < 4; ++n) *(f32x4*)(smem + XCH_AT(w) + n * 1024 + l * 16) = ac[1][n];
#pragma unroll
      for (int n = 0; n < 4; ++n) { v[2 * n] += f32x2{ac[0][n][0], ac[0][n][1]}; v[2 * n + 1] += f32x2{ac[0][n][2], ac[0][n][3]}; }
      BAR();
#pragma unroll
      for (int n = 0; n < 4; ++n) {
        const f32x4 o = *(const f32x4*)(smem + XCH_AT(w ^ 4) + n * 1024 + l * 16);
        v[2 * n] += f32x2{o[0], o[1]}; v[2 * n + 1] += f32x2{o[2], o[3]};
      }
      }
    }
    {
      if (it == 0) { st16(pack8v(v), rs, to, OFF_XN + (4u << 20)); st16(pack8v(v + 4), rs, to + 2048, OFF_XN + (4u << 20)); }
      SB();
      const f32x2 s = ((v[0] + v[1]) + (v[2] + v[3])) + ((v[4] + v[5]) + (v[6] + v[7]));
      f32x2 dp = v[0] * wv[0], q = v[0] * v[0];
#pragma unroll
      for (int j = 1; j < 8; ++j) { dp = __builtin_elementwise_fma(v[j], wv[j], dp); q = __builtin_elementwise_fma(v[j], v[j], q); }
      float mt = hadd(s), qq = hadd(q), dot = hadd(dp);
      add_g3(mt, qq, dot);
      const unsigned so = OFF_STAT + it * 524288 + rb * 32768, lrow = R & 127;
      if (it == 0) wait_vm<2>();
      if (g == 0) st16(unpoison4(__builtin_bit_cast(u32x4, f32x4{mt, qq, dot, 0.f})), rs, ct * 2048 + lrow * 16, so);
      SB();
      if (it == 0) wait_vm<1>();
      if constexpr (it == 0) {
        volatile LDS_AS u32x2* in = (volatile LDS_AS u32x2*)(smem + L_XC);
        if (l == 0) in[w] = T;
        const u32x2 t = in[l & 7];
        if (__all(t[0] == T[0] && t[1] == T[1]) && l < (int)RX1) st8(T, rs, (l * 32 + (rb & 1) * 16 + ct) * SX1, F_X1 + (rb >> 1) * RX1 * 32 * SX1);
      }
      if (it == 0) {
        if (ffn_on) { WD_DMA(); }
#pragma unroll
        for (int j = 0; j < 9; ++j) *(u32x4*)(smem + L_RING + (j / 3) * STAGE + (w * 3 + j % 3) * 1024 + l * 16) = keep[j];
      }
      if constexpr (it == 0) {
        if (w == 0) poll_flags(rs, ((l & 2) * 8 + ((ROT0 + 5) & 7) * 2 + (l & 1)) * SX1, F_X1 + (p * RX1 + (xcd >> 1) % RX1) * 32 * SX1, T);
        if (tid == 192) *(LDS_AS u32x4*)(smem + L_QC) = u32x4{0u, 0u, 0u, 0u};
        BAR();
        PRE_LOAD(1, ROT0 + 5, 0, NPRE);
      } else {
        __builtin_amdgcn_s_sleep(SDIR);
        u32x4 sp[4];
#pragma unroll
        for (int i = 0; i < 4; ++i) sp[i] = ld16(rs, (4 * g + i) * 2048 + lrow * 16, so);
        const RowFin r = fin_key(rs, T, g * 8192 + lrow * 16, so, smem + L_XC, l, w, R, wsum, sp);
        BAR();
        fin_cand(rs, smem + L_XC, 1, g, rb, ct, r, v);
        fin_x2(r, v);
      }
    }
    if (it == 1) {
      u32x4 op[2];
      {
        char* tp = smem + L_OUT + w * 2048;
        const unsigned c0 = (g & 1) * 2 + (g >> 1), r = l & 15;
        *(u32x4*)(tp + r * 128 + ((c0 ^ (r & 7)) << 4)) = trade16(pack8v(v));
        *(u32x4*)(tp + r * 128 + (((c0 + 4) ^ (r & 7)) << 4)) = trade16(pack8v(v + 4));
#pragma unroll
        for (int j = 0; j < 2; ++j) op[j] = *(const u32x4*)(tp + j * 1024 + (l >> 3) * 128 + (((l ^ (l >> 3)) & 7) << 4));
      }
      const unsigned Ro = R - (l & 15) + (l >> 3), oo = (Ro * 1024 + ct * 64) * 2 + (l & 7) * 16;
#define OUT_STORE(c0, c1)                                                                           \
  {                                                                                                 \
    if (c0) __builtin_amdgcn_raw_buffer_store_b128(op[0], rout, oo, 0, SC1);                        \
    if (c1) __builtin_amdgcn_raw_buffer_store_b128(op[1], rout, oo + 16384, 0, SC1);                \
  }
      if (w == 0) {
        __builtin_amdgcn_s_sleep(GSL);
        u32x4 xr[4];
        unsigned cur = 0, best;
        asm volatile("" : "=v"(xr[0]), "=v"(xr[1]));
        for (unsigned spins = 0;;) {
          const unsigned k_ = key_load(rs, l, 1, KREP());
          const bool all = __all(k_ != POISON);
          best = __builtin_amdgcn_readfirstlane(umax16(k_ == POISON ? 0u : k_));
          if (best != cur && ffn_on) {
            cur = best;
            ld16h<0>(xr[0], rs, (l >> 3) * 256 + (l & 7) * 16, cand_off(cur, 1)); ld16h<2048>(xr[1], rs, (l >> 3) * 256 + (l & 7) * 16, cand_off(cur, 1));
          }
          if (all || spin_fail(spins, rs, T)) break;
        }
        if (l == 0) *(LDS_AS unsigned*)(smem + L_SKEY + 32) = best;
        asm volatile("s_waitcnt vmcnt(0)" : "+v"(xr[0]), "+v"(xr[1])::"memory");
        if (ffn_on) {
          word_wait<2>(rs, T, (l >> 3) * 256 + (l & 7) * 16, cand_off(best, 1), xr);
          *(LDS_AS u32x4*)(smem + L_ROW + l * 16) = xr[0]; *(LDS_AS u32x4*)(smem + L_ROW + 1024 + l * 16) = xr[1];
        }
      }
      BAR();
      const unsigned key = __builtin_amdgcn_readfirstlane(*(LDS_AS unsigned*)(smem + L_SKEY + 32)), krow = key_row(key);
      if (ffn_on) {
        u32x4 xv[4];
#pragma unroll
        for (int i = 0; i < 4; ++i) xv[i] = *(const LDS_AS u32x4*)(smem + L_ROW + w * 256 + i * 64 + g * 16);
        f32x4 ga, ua;
        ffn_gu(gv, uv, xv, ga, ua);
        ffn_gu_store(smem, l, w, ga, ua);
        BAR();
        ffn_d_store(rs, l, w, 1, slice, ffn_d_all(dfr, ffn_d_h(ffn_d_read(smem, l))));
        SB();
      }
      OUT_STORE(Ro != krow, Ro + 8 != krow);
      if (!ffn_on) {
        const unsigned G = xcd * 4 + bi - 28;
        if (G == 0 && tid == 0) st8(T, rs, F_EP, 0);
        u32x4 pv[4];
        u32x2 cxn, cx1;
        const float sig = red_gate(key);
        __builtin_amdgcn_s_sleep(RSL);
        red_load(rs, tid, 1, G, key, pv, cxn, cx1);
        red_sum(rs, smem, T, tid, w, 1, G, key, pv, cxn, cx1);
        BAR();
        if (w == 0) {
          const u32x2 ov = red_final(smem, l, sig, cxn, cx1);
          if (l < 8) __builtin_amdgcn_raw_buffer_store_b64(ov, rout, (krow * 1024 + G * 32 + l * 4) * 2, 0, 0);
        }
      }
#undef OUT_STORE
    }
  };
  layer(std::integral_constant<int, 0>{});
  layer(std::integral_constant<int, 1>{});
}

extern "C" void kernel_launch(void* const* d_in, const int* in_sizes, int n_in, void* d_out, int out_size,
                              void* d_ws, size_t ws_size, hipStream_t stream) {
  (void)hipFuncSetAttribute((const void*)mega_kernel, hipFuncAttributeMaxDynamicSharedMemorySize, LDS_BYTES);
  mega_kernel<<<256, 512, LDS_BYTES, stream>>>((const bf16*)d_in[0], (char*)d_ws, (const bf16*)d_in[1], (const bf16*)d_in[2], (const bf16*)d_in[4],
                                               (const bf16*)d_in[5], (const bf16*)d_in[6], (const bf16*)d_in[7], (bf16*)d_out);
}
```

```cpp
#include <hip/hip_runtime.h>
#include <stdint.h>
#include <utility>

typedef __bf16 bf16;
typedef __bf16 bf16x8 __attribute__((ext_vector_type(8)));
typedef __bf16 bf16x2 __attribute__((ext_vector_type(2)));
typedef float f32x4 __attribute__((ext_vector_type(4)));
typedef float f32x2 __attribute__((ext_vector_type(2)));
typedef short s16x4 __attribute__((ext_vector_type(4)));
typedef unsigned int u32x4 __attribute__((ext_vector_type(4)));
typedef unsigned int u32x2 __attribute__((ext_vector_type(2)));
#define LDS_AS __attribute__((address_space(3)))
#define GAS __attribute__((address_space(1)))
typedef __amdgpu_buffer_rsrc_t rsrc_t;

constexpr int F_ = 3584;
constexpr int SC1 = 16;

constexpr int RX = 4;
constexpr unsigned F_EP = 0, F_TMO = 128;
constexpr unsigned F_XN = 4096;
#define FLG 256, 1024, 8, 64, 4, 256, 4
constexpr unsigned FLV[7] = {FLG};
constexpr unsigned SO = FLV[0], SKV = FLV[1], SKW = FLV[2], SX1 = FLV[3], RX1 = FLV[4], SKEY = FLV[5], RKEY = FLV[6];
constexpr unsigned F_O = 8u << 20;
constexpr unsigned F_KV = 11u << 20;
constexpr unsigned F_WOI = F_O + 262144;
constexpr unsigned F_X1 = 9u << 20;
constexpr unsigned OFF_KEY = 10u << 20;
#define KREP() (RKEY == 16 ? bid & 15 : (bid & 7) * RKEY / 8)
#define KEY_AT(L, rep) (OFF_KEY + ((L) * RKEY + (rep)) * (SKEY < 8 ? 128 : 16 * SKEY))
constexpr unsigned OFF_AB = 2u << 20;
constexpr unsigned OFF_XNEW = 262144;
constexpr unsigned OFF_CS = OFF_AB + 65536;
constexpr unsigned OFF_FX = 393216;
constexpr unsigned OFF_CA = 524288;
constexpr unsigned OFF_STAT = 1u << 20;
constexpr unsigned OFF_PART = 3u << 20;
constexpr unsigned OFF_STAMP = 6u << 20;
constexpr unsigned OFF_XN = 8u << 20;
constexpr unsigned OFF_O = 16u << 20;
constexpr unsigned OFF_KV = 32u << 20;
constexpr unsigned OFF_WI = 52u << 20;
constexpr unsigned OFF_WOI = 58u << 20;
constexpr unsigned WS_BYTES = 60u << 20;
constexpr unsigned POISON = 0xFFFFFFFFu;

constexpr int L_ABW = 16640, L_CSP = 17408, L_CSL = 31872, L_FXL = 32256, L_K = 0, L_V = 32768, L_RING = 65536, L_DN = 32768, L_RDY = 31744, L_QC = 31808, LDS_BYTES = 163840;
constexpr int STAGE = 24576;
#define SLOT(t) ((t) & 3)

template <typename F, int... I> __device__ __forceinline__ void static_for_impl(F&& f, std::integer_sequence<int, I...>) {
  (f(std::integral_constant<int, I>{}), ...);
}
template <int N, typename F> __device__ __forceinline__ void static_for(F&& f) { static_for_impl(f, std::make_integer_sequence<int, N>{}); }
__device__ __forceinline__ unsigned pack2(float a, float b) {
  f32x2 v = {a, b};
  return __builtin_bit_cast(unsigned, __builtin_convertvector(v, bf16x2));
}
__device__ __forceinline__ float asf(unsigned u) { return __builtin_bit_cast(float, u); }
__device__ __forceinline__ float lo16(unsigned u) { return __builtin_bit_cast(float, u << 16); }
__device__ __forceinline__ float hi16(unsigned u) { return __builtin_bit_cast(float, u & 0xffff0000u); }
__device__ __forceinline__ u32x4 pack8(const f32x4 a, const f32x4 b) {
  u32x4 o;
  o[0] = pack2(a[0], a[1]); o[1] = pack2(a[2], a[3]); o[2] = pack2(b[0], b[1]); o[3] = pack2(b[2], b[3]);
  return o;
}
__device__ __forceinline__ f32x4 mfma16(u32x4 a, u32x4 b, f32x4 c) {
  return __builtin_amdgcn_mfma_f32_16x16x32_bf16(__builtin_bit_cast(bf16x8, a), __builtin_bit_cast(bf16x8, b), c, 0, 0, 0);
}
__device__ __forceinline__ f32x4 mfma16k16(s16x4 a, s16x4 b, f32x4 c) { return __builtin_amdgcn_mfma_f32_16x16x16bf16_1k(a, b, c, 0, 0, 0); }
template <int CTRL> __device__ __forceinline__ float dppf(float v) {
  return __builtin_bit_cast(float, __builtin_amdgcn_update_dpp(0, __builtin_bit_cast(int, v), CTRL, 0xf, 0xf, false));
}
template <int CTRL> __device__ __forceinline__ unsigned dppu(unsigned v) {
  return (unsigned)__builtin_amdgcn_update_dpp(0, (int)v, CTRL, 0xf, 0xf, false);
}
__device__ __forceinline__ float add_x16(float v) {
  auto r = __builtin_amdgcn_permlane16_swap(__builtin_bit_cast(unsigned, v), __builtin_bit_cast(unsigned, v), false, false);
  return __builtin_bit_cast(float, (unsigned)r[0]) + __builtin_bit_cast(float, (unsigned)r[1]);
}
__device__ __forceinline__ float add_x32(float v) {
  auto r = __builtin_amdgcn_permlane32_swap(__builtin_bit_cast(unsigned, v), __builtin_bit_cast(unsigned, v), false, false);
  return __builtin_bit_cast(float, (unsigned)r[0]) + __builtin_bit_cast(float, (unsigned)r[1]);
}
__device__ __forceinline__ float max_x16(float v) {
  auto r = __builtin_amdgcn_permlane16_swap(__builtin_bit_cast(unsigned, v), __builtin_bit_cast(unsigned, v), false, false);
  return fmaxf(__builtin_bit_cast(float, (unsigned)r[0]), __builtin_bit_cast(float, (unsigned)r[1]));
}
__device__ __forceinline__ float max_x32(float v) {
  auto r = __builtin_amdgcn_permlane32_swap(__builtin_bit_cast(unsigned, v), __builtin_bit_cast(unsigned, v), false, false);
  return fmaxf(__builtin_bit_cast(float, (unsigned)r[0]), __builtin_bit_cast(float, (unsigned)r[1]));
}
__device__ __forceinline__ float sum_hi8(float v) {
  v += dppf<0x128>(v);
  v = add_x16(v);
  return add_x32(v);
}
__device__ __forceinline__ float sum64(float v) {
  v += dppf<0xB1>(v);
  v += dppf<0x4E>(v);
  v += dppf<0x124>(v);
  v += dppf<0x128>(v);
  v = add_x16(v);
  return add_x32(v);
}

#define BAR() asm volatile("s_waitcnt lgkmcnt(0)\n\ts_barrier" ::: "memory")
#define DRAIN() asm volatile("s_waitcnt vmcnt(0)" ::: "memory")
#define SB() __builtin_amdgcn_sched_barrier(0)
template <int N> __device__ __forceinline__ void wait_vm() { asm volatile("s_waitcnt vmcnt(%0)" ::"n"(N) : "memory"); }

__device__ __forceinline__ void dma16(rsrc_t rs, unsigned voff, unsigned soff, unsigned lds) {
  asm volatile("s_mov_b32 m0, %0\n\ts_nop 0\n\tbuffer_load_dwordx4 %1, %2, %3 offen lds" ::"s"(lds), "v"(voff), "s"(rs), "s"(soff) : "memory");
}
__device__ __forceinline__ void dma16n(rsrc_t rs, unsigned voff, unsigned soff, unsigned lds) {
  asm volatile("s_mov_b32 m0, %0\n\ts_nop 0\n\tbuffer_load_dwordx4 %1, %2, %3 offen nt lds" ::"s"(lds), "v"(voff), "s"(rs), "s"(soff) : "memory");
}
template <int OFF> __device__ __forceinline__ void dma16s(rsrc_t rs, unsigned voff, unsigned soff, unsigned lds) {
  asm volatile("s_mov_b32 m0, %0\n\ts_nop 0\n\tbuffer_load_dwordx4 %1, %2, %3 offen offset:%4 sc1 lds" ::"s"(lds), "v"(voff), "s"(rs), "s"(soff), "n"(OFF) : "memory");
}
__device__ __forceinline__ float rbf1(float x) { return __builtin_bit_cast(float, pack2(0.f, x)); }
__device__ __forceinline__ u32x4 ld16(rsrc_t rs, unsigned voff, unsigned soff) { return __builtin_amdgcn_raw_buffer_load_b128(rs, voff, soff, SC1); }
template <int OFF> __device__ __forceinline__ void ld16h(u32x4& d, rsrc_t rs, unsigned voff, unsigned soff) {
  asm volatile("buffer_load_dwordx4 %0, %1, %2, %3 offen offset:%4 sc1" : "=v"(d) : "v"(voff), "s"(rs), "s"(soff), "n"(OFF) : "memory");
}
template <int I, int OFF> __device__ __forceinline__ void ld16w(u32x4& d, rsrc_t rs, unsigned voff, unsigned soff) {
  if constexpr (I == 0) asm volatile("buffer_load_dwordx4 %0, %1, %2, %3 offen offset:%4 sc1" : "={v[128:131]}"(d) : "v"(voff), "s"(rs), "s"(soff), "n"(OFF) : "memory");
  else if constexpr (I == 1) asm volatile("buffer_load_dwordx4 %0, %1, %2, %3 offen offset:%4 sc1" : "={v[132:135]}"(d) : "v"(voff), "s"(rs), "s"(soff), "n"(OFF) : "memory");
  else if constexpr (I == 2) asm volatile("buffer_load_dwordx4 %0, %1, %2, %3 offen offset:%4 sc1" : "={v[136:139]}"(d) : "v"(voff), "s"(rs), "s"(soff), "n"(OFF) : "memory");
  else if constexpr (I == 3) asm volatile("buffer_load_dwordx4 %0, %1, %2, %3 offen offset:%4 sc1" : "={v[140:143]}"(d) : "v"(voff), "s"(rs), "s"(soff), "n"(OFF) : "memory");
  else if constexpr (I == 4) asm volatile("buffer_load_dwordx4 %0, %1, %2, %3 offen offset:%4 sc1" : "={v[144:147]}"(d) : "v"(voff), "s"(rs), "s"(soff), "n"(OFF) : "memory");
  else if constexpr (I == 5) asm volatile("buffer_load_dwordx4 %0, %1, %2, %3 offen offset:%4 sc1" : "={v[148:151]}"(d) : "v"(voff), "s"(rs), "s"(soff), "n"(OFF) : "memory");
  else if constexpr (I == 6) asm volatile("buffer_load_dwordx4 %0, %1, %2, %3 offen offset:%4 sc1" : "={v[152:155]}"(d) : "v"(voff), "s"(rs), "s"(soff), "n"(OFF) : "memory");
  else if constexpr (I == 7) asm volatile("buffer_load_dwordx4 %0, %1, %2, %3 offen offset:%4 sc1" : "={v[156:159]}"(d) : "v"(voff), "s"(rs), "s"(soff), "n"(OFF) : "memory");
  else if constexpr (I == 8) asm volatile("buffer_load_dwordx4 %0, %1, %2, %3 offen offset:%4 sc1" : "={v[160:163]}"(d) : "v"(voff), "s"(rs), "s"(soff), "n"(OFF) : "memory");
  else if constexpr (I == 9) asm volatile("buffer_load_dwordx4 %0, %1, %2, %3 offen offset:%4 sc1" : "={v[164:167]}"(d) : "v"(voff), "s"(rs), "s"(soff), "n"(OFF) : "memory");
  else if constexpr (I == 10) asm volatile("buffer_load_dwordx4 %0, %1, %2, %3 offen offset:%4 sc1" : "={v[168:171]}"(d) : "v"(voff), "s"(rs), "s"(soff), "n"(OFF) : "memory");
  else if constexpr (I == 11) asm volatile("buffer_load_dwordx4 %0, %1, %2, %3 offen offset:%4 sc1" : "={v[172:175]}"(d) : "v"(voff), "s"(rs), "s"(soff), "n"(OFF) : "memory");
}
template <int K> __device__ __forceinline__ void pin_w(u32x4& a, u32x4& b) {
  if constexpr (K == 0) asm volatile("" : "+{v[128:131]}"(a), "+{v[132:135]}"(b)::"memory");
  else if constexpr (K == 1) asm volatile("" : "+{v[136:139]}"(a), "+{v[140:143]}"(b)::"memory");
  else if constexpr (K == 2) asm volatile("" : "+{v[144:147]}"(a), "+{v[148:151]}"(b)::"memory");
  else if constexpr (K == 3) asm volatile("" : "+{v[152:155]}"(a), "+{v[156:159]}"(b)::"memory");
  else if constexpr (K == 4) asm volatile("" : "+{v[160:163]}"(a), "+{v[164:167]}"(b)::"memory");
  else if constexpr (K == 5) asm volatile("" : "+{v[168:171]}"(a), "+{v[172:175]}"(b)::"memory");
}
__device__ __forceinline__ void ld8h(u32x2& d, rsrc_t rs, unsigned voff, unsigned soff) {
  asm volatile("buffer_load_dwordx2 %0, %1, %2, %3 offen sc1" : "=v"(d) : "v"(voff), "s"(rs), "s"(soff) : "memory");
}
template <int OFF> __device__ __forceinline__ void ld4h(unsigned& d, rsrc_t rs, unsigned voff, unsigned soff) {
  asm volatile("buffer_load_dword %0, %1, %2, %3 offen offset:%4 sc1" : "=v"(d) : "v"(voff), "s"(rs), "s"(soff), "n"(OFF) : "memory");
}
__device__ __forceinline__ u32x2 ld8(rsrc_t rs, unsigned voff, unsigned soff) { return __builtin_amdgcn_raw_buffer_load_b64(rs, voff, soff, SC1); }
__device__ __forceinline__ void st16(u32x4 v, rsrc_t rs, unsigned voff, unsigned soff) { __builtin_amdgcn_raw_buffer_store_b128(v, rs, voff + soff, 0, SC1); }
__device__ __forceinline__ void st8(u32x2 v, rsrc_t rs, unsigned voff, unsigned soff) { __builtin_amdgcn_raw_buffer_store_b64(v, rs, voff, soff, SC1); }

__device__ __forceinline__ unsigned ld4(rsrc_t rs, unsigned voff, unsigned soff) { return __builtin_amdgcn_raw_buffer_load_b32(rs, voff, soff, SC1); }
__device__ __forceinline__ void st4(unsigned v, rsrc_t rs, unsigned voff, unsigned soff) { __builtin_amdgcn_raw_buffer_store_b32(v, rs, voff, soff, SC1); }

constexpr unsigned kSpinCap = 1u << 16;
__device__ __forceinline__ bool spin_fail(unsigned& spins, rsrc_t rs, u32x2 T) {
  asm volatile("" ::: "memory");
  ++spins;
  if (spins > kSpinCap) { st8(T, rs, 0, F_TMO); return true; }
  if ((spins & 255u) == 0u) {
    const u32x2 v = ld8(rs, 0, F_TMO);
    if (v[0] == T[0] && v[1] == T[1]) return true;
  }
  __builtin_amdgcn_s_sleep(3);
  return false;
}
__device__ __forceinline__ void poll_flags2(rsrc_t rs, unsigned voff, u32x2 T) {
  for (unsigned spins = 0;;) {
    const u32x4 v = ld16(rs, voff, 0);
    if (__all(v[0] == T[0] && v[1] == T[1] && v[2] == T[0] && v[3] == T[1])) break;
    if (spin_fail(spins, rs, T)) break;
  }
  asm volatile("" ::: "memory");
  __builtin_amdgcn_fence(__ATOMIC_ACQUIRE, "wavefront");
}
__device__ __forceinline__ void tag_wait(rsrc_t rs, u32x2 T, unsigned voff, unsigned soff, u32x4& v) {
  if (__builtin_expect(!__all(v[2] == T[0] && v[3] == T[1]), 0)) {
    unsigned spins = 0;
    do {
      if (spin_fail(spins, rs, T)) break;
      v = ld16(rs, voff, soff);
    } while (!__all(v[2] == T[0] && v[3] == T[1]));
    asm volatile("s_waitcnt vmcnt(0)" : "+v"(v)::"memory");
  }
}
template <int N> __device__ __forceinline__ void word_wait(rsrc_t rs, u32x2 T, unsigned voff, unsigned soff, u32x4 (&v)[4]) {
#define WW_OK() __all(max(max(max(v[0][0], v[0][1]), max(v[0][2], v[0][3])), max(max(v[N - 1][0], v[N - 1][1]), max(v[N - 1][2], v[N - 1][3]))) != POISON)
  if (__builtin_expect(!WW_OK(), 0)) {
    unsigned spins = 0;
    do {
      if (spin_fail(spins, rs, T)) break;
#pragma unroll
      for (int i = 0; i < N; ++i) v[i] = ld16(rs, voff + i * 2048, soff);
    } while (!WW_OK());
    asm volatile("s_waitcnt vmcnt(0)" : "+v"(v[0]), "+v"(v[N - 1])::"memory");
  }
#undef WW_OK
}
__device__ __forceinline__ void poll_flags(rsrc_t rs, unsigned voff, unsigned soff, u32x2 T) {
  for (unsigned spins = 0;;) {
    const u32x2 v = ld8(rs, voff, soff);
    if (__all(v[0] == T[0] && v[1] == T[1])) break;
    if (spin_fail(spins, rs, T)) break;
  }
  asm volatile("" ::: "memory");
  __builtin_amdgcn_fence(__ATOMIC_ACQUIRE, "wavefront");
}

#define STAMP_ON(i) true
#define STAMP_VAL(i) (((i) == 23 || (i) == 22 || (i) == 21) ? __builtin_amdgcn_s_memtime() : __builtin_amdgcn_s_memrealtime())
#define STAMPAT(i)

#define GSTEP(s, n)
#define VSTEP(i)

__device__ __forceinline__ void unpack8v(const u32x4 u, f32x2* v) {
#pragma unroll
  for (int j = 0; j < 4; ++j) v[j] = f32x2{lo16(u[j]), hi16(u[j])};
}
__device__ __forceinline__ u32x4 pack8v(const f32x2* v) {
  u32x4 o;
#pragma unroll
  for (int j = 0; j < 4; ++j) o[j] = pack2(v[j][0], v[j][1]);
  return o;
}
__device__ __forceinline__ float ln_rd(float var) { return __builtin_amdgcn_rcpf(rbf1(__builtin_amdgcn_sqrtf(rbf1(rbf1(var) + 1e-5f)))); }
__device__ __forceinline__ float hadd(f32x2 v) {
  float a = v[0];
  asm volatile("" : "+v"(a));
  return a + v[1];
}
__device__ __forceinline__ float add_g(float v) { return add_x32(add_x16(v)); }
__device__ __forceinline__ void add_g3(float& x, float& y, float& z) {
  auto a = __builtin_amdgcn_permlane16_swap(__builtin_bit_cast(unsigned, x), __builtin_bit_cast(unsigned, y), false, false);
  auto b = __builtin_amdgcn_permlane16_swap(__builtin_bit_cast(unsigned, z), __builtin_bit_cast(unsigned, z), false, false);
  const float r = asf(a[0]) + asf(a[1]), t = asf(b[0]) + asf(b[1]);
  auto c = __builtin_amdgcn_permlane32_swap(__builtin_bit_cast(unsigned, r), __builtin_bit_cast(unsigned, r), false, false);
  auto d = __builtin_amdgcn_permlane32_swap(__builtin_bit_cast(unsigned, t), __builtin_bit_cast(unsigned, t), false, false);
  const float u = asf(c[0]) + asf(c[1]);
  z = asf(d[0]) + asf(d[1]);
  auto e = __builtin_amdgcn_permlane16_swap(__builtin_bit_cast(unsigned, u), __builtin_bit_cast(unsigned, u), false, false);
  x = asf(e[0]); y = asf(e[1]);
}
__device__ __forceinline__ u32x4 trade16(u32x4 v) {
  auto a = __builtin_amdgcn_permlane16_swap(v[0], v[2], false, false);
  auto b = __builtin_amdgcn_permlane16_swap(v[1], v[3], false, false);
  return u32x4{(unsigned)a[0], (unsigned)b[0], (unsigned)a[1], (unsigned)b[1]};
}
__device__ __forceinline__ unsigned umax4(u32x4 v) { return max(max(v[0], v[1]), max(v[2], v[3])); }
__device__ __forceinline__ u32x2 unpoison2(u32x2 v) { return u32x2{min(v[0], POISON - 1), min(v[1], POISON - 1)}; }
__device__ __forceinline__ u32x4 unpoison4(u32x4 v) {
  return u32x4{min(v[0], POISON - 1), min(v[1], POISON - 1), min(v[2], POISON - 1), min(v[3], POISON - 1)};
}

typedef union { struct { s16x4 a, b; } hf; u32x4 v; } frag_t;
__device__ __forceinline__ s16x4 trread(const char* smem, unsigned off) {
  return __builtin_amdgcn_ds_read_tr16_b64_v4i16((LDS_AS s16x4*)(smem + off));
}
__device__ __forceinline__ s16x4 trabs(unsigned addr) { return __builtin_amdgcn_ds_read_tr16_b64_v4i16((LDS_AS s16x4*)(size_t)addr); }

constexpr int WA = 6;
#define WOP 4, 3, 6, 8, 1, 4, 0
constexpr int WOV[7] = {WOP};
constexpr int WLA = WOV[4], WPR = WOV[5], QPR = WOV[6];
constexpr int WB = WOV[0], WR = WOV[1], WIS = WOV[2], WPS = WOV[3];
#define CH 0, 7, 9, 10, 16, 19, 24, 26, 30, 31, 5, 2
constexpr int CHV[12] = {CH};
constexpr int FKL = CHV[10], FSN = CHV[11];
constexpr int SPL = CHV[0];
constexpr int FSL = CHV[1], FS0 = CHV[2], FS0D = CHV[3], FS1 = CHV[4], FS1S = CHV[5];
constexpr int FXA = CHV[6], FXB = CHV[7], FXL = CHV[8], FXS = CHV[9];
#define ROT0 p
#define L0S 22, 25, 8, 0, 24, 28, 1, 2, 4, 14
constexpr int L0V[10] = {L0S};
constexpr int CSL = L0V[0], CSS = L0V[1], CSD = L0V[2];
constexpr int abl(int it) { return it ? 25 : L0V[4]; }
constexpr int abs_(int it) { return it ? 27 : L0V[5]; }
constexpr int W2S = L0V[6], WSH = L0V[7];
constexpr int PRE0 = 12;
constexpr int LNL = L0V[3], LNS = L0V[8], POI = L0V[9];
#define TCH 22, 3, 0x07, 26, 0xF8, 0, 1
constexpr int TCV[7] = {TCH};
constexpr int TS0 = TCV[0], TSN = TCV[1], TM0 = TCV[2], TEX = TCV[3], TM1 = TCV[4], TSD = TCV[5], TSG = TCV[6];
constexpr int QL = 26;
#define SLP 18, 4, 28, 3, 0, 0
constexpr int SLV[6] = {SLP};
constexpr int SDIR = SLV[0], GSL = SLV[1], RSL = SLV[2], KSL = SLV[3];
constexpr int tp0(int it) { return it ? 3 : 3 + WSH; }
constexpr bool tile_piece(int it, int s) { return s >= tp0(it) && ((s - tp0(it)) >> 2) + 3 < 8 && ((s - tp0(it)) & 3) < 3; }
#define NPRE 3
#define QAD 4
#define WO2 1, 5, 4, 2, 8
constexpr int W2V[5] = {WO2};
constexpr int WOS = 12;
static_assert(WOS + QAD < 18, "covered by the operand waits in front of the barrier of step 18");
constexpr int WON = W2V[0], W2B = W2V[1], W2R = W2V[2], W2L = W2V[3], W2P = W2V[4];
constexpr int a_req(int s, int n) {
  int r = 2 * NPRE;
  for (int u = 1; u < 192; u += 2) {
    const bool go = r < 64 && (r >> 1) <= u / 6 + QAD && (r < 8 || u >= 9);
    if (u == 6 * s + n) return go ? r : -1;
    r += go;
  }
  return -1;
}
constexpr int vm_n(int it, int s, int n) {
  int c = it ? (a_req(s, n) >= 0) + (s == 0 && n == 0) : (s + 5 < 32 && (n == 2 || n == 3));
  if (n == 0) c += tile_piece(it, s);
  if (n == 4) c += (s == abl(it));
  if (!it) c += (s >= W2S && s < W2S + 3 && n == 4) + (s == LNL && n == 5 ? 3 : 0) + (s == 0 && n < 2 ? 2 : 0) + (s == LNS + 2 && n == 5) + (s == TEX && n == 5);
  else c += (s == 2 && n == 2) + (s == (SPL < 0 ? 0 : SPL) && n == (SPL < 0 ? 1 : 4) ? 4 : 0) + (s >= 27 && s < 30 && (n == 0 || n == 3)) + (WON && s == WOS && n == 4);
  return c;
}
constexpr int vm_at(int it, int s, int n) {
  int c = it ? 2 * NPRE : PRE0;
  for (int u = 0; u < 6 * s + n; ++u) c += vm_n(it, u / 6, u % 6);
  return c;
}
constexpr int vm_a(int it, int k) {
  if (k < NPRE) return 2 * k + 2;
  for (int u = 0; u < 192; ++u)
    if (a_req(u / 6, u % 6) == 2 * k + 1) return vm_at(it, (u + 1) / 6, (u + 1) % 6);
  return 0;
}
constexpr int vm_tile(int it, int t) { return t >= 3 ? vm_at(it, 4 * t - 10 + tp0(it), 1) : t == 1 ? 12 : vm_at(0, W2S + 2, 5); }
constexpr int vm_unit(int u) { return u < 3 ? 5 + 2 * u : u < 5 ? vm_at(0, 0, u - 2) : vm_at(0, u - 5, 4); }
constexpr int unit_slot(int u) { return u % 3 == 0 ? 0 : 16384 + (u % 3) * 16384; }

constexpr int L_XC = LDS_BYTES - 64, L_SKEY = 32704, L_ROW = 1024, L_OUT = 65536, L_SRED = 4096, L_SFIN = 20480;
__device__ __forceinline__ unsigned key_load(rsrc_t rs, int l, int L, int rep) { return ld4(rs, (l & 15) * SKEY, KEY_AT(L, rep)); }
__device__ __forceinline__ unsigned umax16(unsigned k) {
  k = max(k, dppu<0xB1>(k));
  k = max(k, dppu<0x4E>(k));
  k = max(k, dppu<0x141>(k));
  return max(k, dppu<0x140>(k));
}
__device__ __forceinline__ unsigned key_row(unsigned key) { return (0xffffu - (key & 0xffffu)) & 2047u; }
__device__ __forceinline__ unsigned key_wait(rsrc_t rs, u32x2 T, int l, int L, int rep, unsigned kv) {
  if (!__all(kv != POISON)) {
    unsigned spins = 0;
    do {
      if (spin_fail(spins, rs, T)) break;
      if (L == 0) __builtin_amdgcn_s_sleep(KSL);
      kv = key_load(rs, l, L, rep);
    } while (!__all(kv != POISON));
  }
  return __builtin_amdgcn_readfirstlane(umax16(kv));
}
__device__ __forceinline__ unsigned cand_off(unsigned key, int L) { return OFF_CA + L * 65536 + (key_row(key) >> 7) * 4096; }
#define XO(i) (((i) >> 1) * 256 + ((i) & 1) * 64)
__device__ __forceinline__ void ffn_gu(const u32x4 (&gv)[4], const u32x4 (&uv)[4], const u32x4 (&xv)[4], f32x4& ga, f32x4& ua) {
  ga = mfma16(gv[0], xv[0], f32x4{0.f, 0.f, 0.f, 0.f}); ua = mfma16(uv[0], xv[0], f32x4{0.f, 0.f, 0.f, 0.f});
#pragma unroll
  for (int i = 1; i < 4; ++i) { ga = mfma16(gv[i], xv[i], ga); ua = mfma16(uv[i], xv[i], ua); }
}
__device__ __forceinline__ void ffn_gu_store(char* smem, int l, int w, f32x4 ga, f32x4 ua) {
  LDS_AS f32x4* sred = (LDS_AS f32x4*)(smem + L_SRED);
  if ((l & 15) == 0) { sred[w * 8 + (l >> 4)] = ga; sred[w * 8 + 4 + (l >> 4)] = ua; }
}
__device__ __forceinline__ f32x4 ffn_d_read(char* smem, int l) {
  const LDS_AS float* t = (const LDS_AS float*)(smem + L_SRED) + (l & 8) * 16 + (l & 4) * 4 + (l >> 4) * 4 + (l & 3);
  return f32x4{t[0], t[32], t[64], t[96]};
}
__device__ __forceinline__ u32x2 ffn_d_h(f32x4 t) {
  float x = t[0] + t[1];
  asm volatile("" : "+v"(x));
  x += t[2] + t[3];
  x += dppf<0x128>(x);
  const float y = dppf<0x124>(x);
  const bool up = __builtin_amdgcn_inverse_ballot_w64(0xF0F0F0F0F0F0F0F0ull);
  const float gg = rbf1(up ? y : x), uu = rbf1(up ? x : y);
  const float h = rbf1(gg * __builtin_amdgcn_rcpf(1.0f + __expf(-gg))) * uu;
  const unsigned p = pack2(h, dppf<0xB1>(h));
  return u32x2{dppu<0x00>(p), dppu<0xAA>(p)};
}
template <int t> __device__ __forceinline__ f32x4 ffn_d_mma(s16x4 d, u32x2 hh, f32x4 y) {
  const bool me = __builtin_amdgcn_inverse_ballot_w64(0x0001000100010001ull << t);
  return mfma16k16(d, __builtin_bit_cast(s16x4, u32x2{me ? hh[0] : 0u, me ? hh[1] : 0u}), y);
}
__device__ __forceinline__ f32x4 ffn_d_all(const s16x4 (&dfr)[8], u32x2 hh) {
  f32x4 a = {0.f, 0.f, 0.f, 0.f}, b = a;
  a = ffn_d_mma<0>(dfr[0], hh, a); b = ffn_d_mma<1>(dfr[1], hh, b); a = ffn_d_mma<2>(dfr[2], hh, a); b = ffn_d_mma<3>(dfr[3], hh, b);
  a = ffn_d_mma<4>(dfr[4], hh, a); b = ffn_d_mma<5>(dfr[5], hh, b); a = ffn_d_mma<6>(dfr[6], hh, a); b = ffn_d_mma<7>(dfr[7], hh, b);
  return a + b;
}
__device__ __forceinline__ void ffn_d_store(rsrc_t rs, int l, int w, int L, int slice, f32x4 y) {
  const unsigned c = l & 15;
  if (c < 8) st16(unpoison4(__builtin_bit_cast(u32x4, y)), rs, (2 * w + (c >> 2)) * 65536 + slice * 256 + (c & 3) * 64 + (l >> 4) * 16, OFF_PART + L * (1u << 20));
}
#define RED_VO(k) ((min(tid + 512 * (k), 1791) >> 3) * 256 + (tid & 7) * 16)
#define RED_SO(L, G) (OFF_PART + (L) * (1u << 20) + ((G) >> 1) * 65536 + ((G) & 1) * 128)
#define RED_JJ() ((tid & 3) * 16 + (tid & 4) * 2)
__device__ __forceinline__ void red_load(rsrc_t rs, int tid, int L, unsigned G, unsigned key, u32x4 (&pv)[4], u32x2& cxn, u32x2& cx1) {
#pragma unroll
  for (int k = 0; k < 4; ++k) pv[k] = ld16(rs, RED_VO(k), RED_SO(L, G));
  cxn = ld8(rs, RED_JJ(), cand_off(key, L) + XO(G));
  cx1 = ld8(rs, RED_JJ() + 128, cand_off(key, L) + XO(G));
}
__device__ __forceinline__ void red_sum(rsrc_t rs, char* smem, u32x2 T, int tid, int w, int L, unsigned G, unsigned key,
                                        u32x4 (&pv)[4], u32x2& cxn, u32x2& cx1) {
#define PV_OK() __all(max(max(max(umax4(pv[0]), umax4(pv[1])), max(umax4(pv[2]), umax4(pv[3]))), max(max(cxn[0], cxn[1]), max(cx1[0], cx1[1]))) != POISON)
  if (!PV_OK()) {
    unsigned spins = 0;
    do {
      if (spin_fail(spins, rs, T)) break;
#pragma unroll
      for (int k = 0; k < 4; ++k)
        if (umax4(pv[k]) == POISON) pv[k] = ld16(rs, RED_VO(k), RED_SO(L, G));
      if (max(max(cxn[0], cxn[1]), max(cx1[0], cx1[1])) == POISON) { cxn = ld8(rs, RED_JJ(), cand_off(key, L) + XO(G)); cx1 = ld8(rs, RED_JJ() + 128, cand_off(key, L) + XO(G)); }
    } while (!PV_OK());
  }
#undef PV_OK
  f32x4 s = (__builtin_bit_cast(f32x4, pv[0]) + __builtin_bit_cast(f32x4, pv[1])) + __builtin_bit_cast(f32x4, pv[2]);
  if (w < 4) s += __builtin_bit_cast(f32x4, pv[3]);
#pragma unroll
  for (int j = 0; j < 4; ++j) s[j] = sum_hi8(s[j]);
  if ((tid & 63) < 8) ((LDS_AS f32x4*)(smem + L_SFIN))[w * 8 + (tid & 7)] = s;
}
__device__ __forceinline__ float red_gate(unsigned key) {
  const unsigned k16 = key >> 16;
  const float rmax = __builtin_bit_cast(float, ((k16 & 0x8000u) ? (k16 & 0x7fffu) : (~k16 & 0xffffu)) << 16);
  float sig = rbf1(__builtin_amdgcn_rcpf(1.0f + __expf(-rmax)));
  asm volatile("" : "+v"(sig));
  return sig;
}
__device__ __forceinline__ u32x2 red_final(char* smem, int l, float sig, u32x2 cxn, u32x2 cx1) {
  f32x4 s = ((const LDS_AS f32x4*)(smem + L_SFIN))[l];
#pragma unroll
  for (int j = 0; j < 4; ++j) s[j] = sum_hi8(s[j]);
  const float xn4[4] = {lo16(cxn[0]), hi16(cxn[0]), lo16(cxn[1]), hi16(cxn[1])};
  const float x14[4] = {lo16(cx1[0]), hi16(cx1[0]), lo16(cx1[1]), hi16(cx1[1])};
  float o[4];
#pragma unroll
  for (int j = 0; j < 4; ++j) o[j] = x14[j] + rbf1(xn4[j] + rbf1(rbf1(s[j]) * sig));
  return u32x2{pack2(o[0], o[1]), pack2(o[2], o[3])};
}

struct RowFin { float mean, var, mb, rd; unsigned key; };
__device__ __forceinline__ void fin_wait(rsrc_t rs, u32x2 T, unsigned voff, unsigned so, u32x4 (&sp)[4]) {
#define ST_OK() __all(max(max(umax4(sp[0]), umax4(sp[1])), max(umax4(sp[2]), umax4(sp[3]))) != POISON)
  if (!ST_OK()) {
    unsigned spins = 0;
    do {
      if (spin_fail(spins, rs, T)) break;
#pragma unroll
      for (int i = 0; i < 4; ++i) sp[i] = ld16(rs, voff + i * 2048, so);
    } while (!ST_OK());
  }
#undef ST_OK
}
__device__ __forceinline__ RowFin fin_key(rsrc_t rs, u32x2 T, unsigned voff, unsigned so, char* skey, int l, int w, unsigned R, float wsum, u32x4 (&sp)[4]) {
  RowFin r;
#define ST_SUM() ((__builtin_bit_cast(f32x4, sp[0]) + __builtin_bit_cast(f32x4, sp[1])) + (__builtin_bit_cast(f32x4, sp[2]) + __builtin_bit_cast(f32x4, sp[3])))
  f32x4 sf = ST_SUM();
  const float z = (sf[0] + sf[1]) + sf[2];
  if (__any(z != z)) {
    fin_wait(rs, T, voff, so, sp);
    sf = ST_SUM();
  }
#undef ST_SUM
  float s0 = sf[0], s1 = sf[1], s2 = sf[2];
  add_g3(s0, s1, s2);
  r.mean = s0 * (1.0f / 1024.0f);
  r.var = fmaxf(__builtin_fmaf(-r.mean, r.mean, s1 * (1.0f / 1024.0f)), 0.f);
  r.mb = rbf1(r.mean); r.rd = ln_rd(r.var);
  const unsigned rb16 = pack2((s2 - r.mb * wsum) * r.rd, 0.f) & 0xffffu;
  r.key = min((((rb16 & 0x8000u) ? (~rb16 & 0xffffu) : (rb16 | 0x8000u)) << 16) | (0xffffu - R), POISON - 1);
  const unsigned wk = umax16(r.key);
  if (l == 0) ((LDS_AS unsigned*)skey)[w] = wk;
  return r;
}
__device__ __forceinline__ void fin_cand(rsrc_t rs, char* skey, int L, int g, int rb, int ct, const RowFin& r, const f32x2 (&v)[8]) {
  const u32x4 k0 = *(const LDS_AS u32x4*)skey, k1 = *(const LDS_AS u32x4*)(skey + 16);
  const unsigned best = max(umax4(k0), umax4(k1));
  if (r.key == best) {
    const unsigned co = L * 65536 + rb * 4096 + ct * 256 + g * 16;
    f32x2 t[8], rd2 = {r.rd, r.rd}, of2 = {-r.mb * r.rd, -r.mb * r.rd};
    asm volatile("" : "+v"(rd2), "+v"(of2));
#pragma unroll
    for (int j = 0; j < 8; ++j) t[j] = __builtin_elementwise_fma(v[j], rd2, of2);
    st16(unpoison4(pack8v(t)), rs, co, OFF_CA); st16(unpoison4(pack8v(t + 4)), rs, co + 64, OFF_CA);
    if (g == 0 && ct < (int)RKEY) st4(best, rs, KEY_AT(L, ct) + rb * SKEY, 0);
    st16(unpoison4(pack8v(v)), rs, co + 128, OFF_CA); st16(unpoison4(pack8v(v + 4)), rs, co + 192, OFF_CA);
  }
}
__device__ __forceinline__ void fin_x2(const RowFin& r, f32x2 (&v)[8]) {
  f32x2 sc2 = {1.0f + r.rd, 1.0f + r.rd}, of2 = {-r.mb * r.rd, -r.mb * r.rd};
  asm volatile("" : "+v"(sc2), "+v"(of2));
#pragma unroll
  for (int j = 0; j < 8; ++j) v[j] = __builtin_elementwise_fma(v[j], sc2, of2);
}

extern "C" __device__ unsigned long long dispatch_id() __asm("llvm.amdgcn.dispatch.id");

extern "C" __global__ __launch_bounds__(512, 2) void mega_kernel(const bf16* x, char* ws, const bf16* wqkv, const bf16* wo, const bf16* wr,
                                                                 const bf16* wg, const bf16* wu, const bf16* wd, bf16* out) {
  extern __shared__ __attribute__((aligned(16))) char smem[];
  const int w0_ = __builtin_amdgcn_readfirstlane(threadIdx.x >> 6);
  const unsigned lbase = (unsigned)(size_t)(LDS_AS char*)smem;
  {
    unsigned long long pc;
    unsigned len;
    asm volatile("s_getpc_b64 %0\n\ts_mov_b32 %1, .Ltext_end-mega_kernel\n\t.subsection 1\n.Ltext_end:\n\t.subsection 0" : "=s"(pc), "=s"(len));
    const unsigned o = ((blockIdx.x >> 3) * 64 + (threadIdx.x & 63)) * 128;
    if (w0_ == 7 && o < len)
      asm volatile("s_mov_b32 m0, %0\n\ts_nop 0\n\tglobal_load_lds_dword %1, %2" ::"s"(lbase + L_FXL), "v"(o), "s"(pc & ~127ull) : "memory");
  }
  asm volatile("" : "+s"(x), "+s"(ws), "+s"(wqkv));
#define IDS()                                                                                         \
  int tid = threadIdx.x, w = w0_, bid = blockIdx.x;                                                   \
  asm volatile("" : "+v"(tid));                                                                       \
  asm volatile("" : "+s"(w));                                                                         \
  asm volatile("" : "+s"(bid));                                                                       \
  const int l = tid & 63, xcd = bid & 7, bi = bid >> 3;                                               \
  const int p = (xcd & 1) * 4 + (bi & 3), h = (xcd >> 1) * 8 + (bi >> 2);         \
  const int rb = 2 * p + ((h >> 3) & 1), ct = (h >> 4) * 8 + (h & 7);       \
  const int slice = xcd * 28 + bi;                                                   \
  const bool ffn_on = bi < 28;                                                                        \
  const int g = l >> 4;                                                                               \
  const int rt = w < 4 ? w : 11 - w;          \
  KARGS();                                                                                            \
  (void)p; (void)h; (void)rb; (void)ct; (void)slice; (void)ffn_on; (void)g; (void)rt;
  const rsrc_t rs = __builtin_amdgcn_make_buffer_rsrc((void*)ws, 0, WS_BYTES, 0x00020000);
  typedef const void* const __attribute__((address_space(4)))* karg_t;
#define KARGS() karg_t ka = (karg_t)__builtin_amdgcn_kernarg_segment_ptr(); asm volatile("" : "+s"(ka))
#define ARGP(name, i) const GAS bf16* name = (const GAS bf16*)ka[i]; asm volatile("" : "+s"(name))
#define RSRC(ptr, bytes) __builtin_amdgcn_make_buffer_rsrc((void*)(ptr), 0, bytes, 0x00020000)
#define rwq RSRC(pwq, 1024 * 3072 * 2)
#define rwo RSRC(pwo, 1024 * 1024 * 2)
#define rwg RSRC(pwg, 1024 * F_ * 2)
#define rwu RSRC(pwu, 1024 * F_ * 2)
#define rwd RSRC(pwd, 1024 * F_ * 2)
#define rout RSRC(pout, 2048 * 1024 * 2)

#define WQ_VOFF() ((l >> 2) * 6144 + (((l & 3) ^ ((((l >> 4) ^ (l >> 5)) & 1) << 1)) << 4))
#define WQ_PIECE(t, j, dst) dma16(rwq, wqv, ((((t) + rot) & 7) * 128 + ((j) & 7) * 16) * 6144 + ((j) >> 3) * 2048 + h * 64, lbase + (dst) + (j) * 1024)
#define WQ_BOFF(dt) ((8 * g + ((l & 15) >> 2)) * 64 + (l & 3) * 8 + (((dt) ^ (g & 1)) << 5))
#define WQ_POFF() ((4 * g + ((l & 15) >> 2)) * 64 + (l & 3) * 8 + (((g ^ (g >> 1)) & 1) << 5))
#define X_UNIT1(u, j) dma16(rx, xv_, (unsigned)(p * 256 + rt * 32 + ((u) & 1) * 16 + (j) * 8) * 2048 + ((((u) >> 2) + rot) & 7) * 256 + (((u) >> 1) & 1) * 128, lbase + rt * 2048 + unit_slot(u) + (j) * 1024)
#define X_VOFF() ((l >> 3) * 2048 + (((l & 7) ^ ((l >> 3) & 6)) << 4))
#define X_ROFF(hs) (lbase + rt * 2048 + (l & 15) * 128 + (((4 * (hs) + g) ^ (l & 6)) << 4))
#define TOUCH_TO(ptr, dst) asm volatile("s_mov_b32 m0, %0\n\ts_nop 0\n\tglobal_load_lds_dword %1, off" ::"s"(lbase + (dst)), "v"(ptr) : "memory")
#define TOUCH(ptr) TOUCH_TO(ptr, L_FXL)
#define WO_PIECE_W(j, w_)                                                                                                                 \
  {                                                                                                                                       \
    if constexpr (it == 1 && WON) dma16s<0>(rs, l * 16, OFF_WOI + ct * 131072 + (j) * 8192 + (w_) * 1024, lbase + 32768 + (j) * 8192 + (w_) * 1024); \
    else dma16(rwo, wov, (((w_) >> 2) * 64 + ((w_) & 3) + 4 * (j)) * 16384 + ct * 128, lbase + 32768 + (j) * 8192 + (w_) * 1024);           \
  }
#define WO_PIECE(j) WO_PIECE_W(j, w)
#define WO_VOFF() ((l >> 3) * 2048 + (((((l & 7) >> 1) ^ ((l >> 4) & 3)) * 2 + (l & 1)) << 4))
#define WD_DMA() _Pragma("unroll") for (int i = 0; i < 4; ++i) WD_DMA1(i)
#define WD_DMA1(i)                                                                                   \
      dma16n(rwd, (l >> 4) * 2048 + (((l & 15) * 16) ^ ((l >> 4) * 32) ^ (((i) & 1) * 128)), (unsigned)(slice * 16 + 4 * (i)) * 2048 + w * 256, lbase + L_DN + w * 4096 + (i) * 1024)
#define FFN_DMA(m) dma16((m) & 1 ? rwu : rwg, fnv, (unsigned)(((4 * w + ((m) >> 1)) * 32) * F_ + slice * 16) * 2, lbase + 65536 + (m) * 8192 + w * 1024)
  u32x4 gv[4], uv[4];
  s16x4 dfr[8];
  u32x4 keep[9];
  u32x4 pre[4][2];
#define PRE_LOAD(it_, rot_, s0, s1)                                                                  \
  static_for<(s1) - (s0)>([&](auto c_) __attribute__((always_inline)) {                              \
    constexpr int s = (s0) + decltype(c_)::value;                                                    \
    const unsigned ab_ = OFF_XN + (it_) * (4u << 20) + (((rot_) + (s >> 2)) & 7) * 8192 + ((s >> 1) & 1) * 4096; \
    ld16w<2 * s, (s & 1) * 2048>(pre[s][0], rs, l * 16, ab_ + (p * 8 + rt) * 65536);                         \
    ld16w<2 * s + 1, (s & 1) * 2048 + 1024>(pre[s][1], rs, l * 16, ab_ + (p * 8 + rt) * 65536);                 \
  })
  u32x2 T;
  u32x2 ep;
  u32x4 xl[2];
#define LN_LOAD()                                                                                                                                   \
  {                                                                                                                                                 \
    asm volatile("buffer_load_dwordx4 %0, %1, %2, %3 offen" : "=v"(xl[0]) : "v"(l * 16), "s"(rx), "s"((p * 256 + h * 8 + w) * 2048) : "memory");      \
    asm volatile("buffer_load_dwordx4 %0, %1, %2, %3 offen offset:1024" : "=v"(xl[1]) : "v"(l * 16), "s"(rx), "s"((p * 256 + h * 8 + w) * 2048) : "memory"); \
    asm volatile("buffer_load_dwordx2 %0, off, %1, 0 sc1" : "=v"(ep) : "s"(rs) : "memory");                                                         \
  }
  const GAS bf16* tsrc;
  {
    IDS();
    const int it = 0, rot = ROT0;
    (void)it;
    STAMPAT(0);
    const bf16* pwq = wqkv;
    const rsrc_t rx = RSRC(x, 2048 * 1024 * 2);
    const unsigned wqv = WQ_VOFF(), xv_ = X_VOFF();
    if (tid == 0) *(LDS_AS u32x4*)(smem + L_QC) = u32x4{0u, 0u, 0u, 0u};
    if (w == 0 && l < 24) TOUCH((const GAS bf16*)wqkv + (bid * 24 + l) * 512 + ((3 - (unsigned)((size_t)wqkv >> 7)) & 7) * 64);
#pragma unroll
    for (int i = 0; i < 3; ++i) WQ_PIECE(0, 3 * w + i, L_RING);
    STAMPAT(41);
    X_UNIT1(0, 0); X_UNIT1(0, 1); X_UNIT1(1, 0); X_UNIT1(1, 1);
    STAMPAT(42);
    BAR();
    STAMPAT(43);
    X_UNIT1(2, 0); X_UNIT1(2, 1);
#pragma unroll
    for (int i = 0; i < 3; ++i) WQ_PIECE(1, 3 * w + i, L_RING + STAGE);
    STAMPAT(44);
    STAMPAT(31);
  }
  unsigned sel0 = 0x7FFFFFFFu;
  f32x2 v[8];
  u32x4 x2p[2], wof;
  float wsum;

  auto layer = [&](auto it_c) __attribute__((always_inline)) {
    constexpr int it = decltype(it_c)::value;
    STAMPAT(1); STAMPAT(22);
    f32x4 acc[2][6];
    f32x2 qs2[2];
    u32x4 k0, k1, v0, v1;
    {
      IDS();
      bool aff = false;
      unsigned aq[2];
      u32x4 av[2];
#define PIN_ACC() asm volatile("" : "+{v[176:179]}"(acc[0][0]), "+{v[180:183]}"(acc[0][1]), "+{v[184:187]}"(acc[0][2]), "+{v[188:191]}"(acc[0][3]), "+{v[192:195]}"(acc[0][4]), "+{v[196:199]}"(acc[0][5]), \
                               "+{v[200:203]}"(acc[1][0]), "+{v[204:207]}"(acc[1][1]), "+{v[208:211]}"(acc[1][2]), "+{v[212:215]}"(acc[1][3]), "+{v[216:219]}"(acc[1][4]), "+{v[220:223]}"(acc[1][5]))
#define PINV4(x) asm volatile("" : "+v"(x[0]), "+v"(x[1]), "+v"(x[2]), "+v"(x[3])::"memory")
      auto qkv_loop = [&](auto it_c) __attribute__((always_inline)) {
        constexpr int IT = decltype(it_c)::value;
        const unsigned abase = OFF_XN + IT * (4u << 20) + (p * 8 + rt) * 65536;
        const int rot = ROT0 + 5 * IT;
        unsigned fkey = 0, kv, bq[2];
        u32x4 fx[4], bv[2], af[WA][2];
        u32x2 cxn, cx1, hh;
        f32x4 sa, sb, cs4[2];
        f32x2 b2[2];
#define AB_OFF() (OFF_AB + IT * 32768 + (p * 8 + rt) * 512)
#define A_LOAD1(k, a) ld16w<(k) % WA * 2 + (a), ((k) & 1) * 2048 + (a) * 1024>(af[(k) % WA][a], rs, l * 16, abase + ((((k) >> 2) + rot) & 7) * 8192 + (((k) >> 1) & 1) * 4096)
        if constexpr (IT == 1) {
#pragma unroll
          for (int s = 0; s < NPRE; ++s) { af[s][0] = pre[s][0]; af[s][1] = pre[s][1]; }
        }
        const bf16* pwq = wqkv;
        ARGP(pwo, 3);
        const rsrc_t rx = RSRC(x, 2048 * 1024 * 2);
        unsigned xv_ = X_VOFF(), xr[2] = {X_ROFF(0), X_ROFF(1)};
        if constexpr (IT == 0) asm volatile("" : "+v"(xv_), "+v"(xr[0]), "+v"(xr[1]));
#define X_READ(u) { af[((u) & ~1) % WA][(u) & 1] = *(const LDS_AS u32x4*)(size_t)(xr[0] + unit_slot(u)); af[((u) | 1) % WA][(u) & 1] = *(const LDS_AS u32x4*)(size_t)(xr[1] + unit_slot(u)); }
        const int tstep = TS0 + bid % TSN + TSD * (w >> TSG);
        const unsigned wqv = WQ_VOFF();
#define QKV_DMA1(t, i)                                                                            \
  {                                                                                               \
    if constexpr (IT == 0) WQ_PIECE(t, 3 * w + (i), L_RING + SLOT(t) * STAGE);                    \
    else dma16s<(i) * 1024>(rs, l * 16, OFF_WI + (h * 8 + (((t) + rot) & 7)) * STAGE + w * 3072, lbase + L_RING + SLOT(t) * STAGE + w * 3072); \
  }
        unsigned bo[2][2];
#pragma unroll
        for (int dt = 0; dt < 2 - IT; ++dt) {
          bo[dt][0] = lbase + L_RING + (IT ? l * 16 : WQ_BOFF(dt)); bo[dt][1] = bo[dt][0] + 2 * STAGE;
          asm volatile("" : "+v"(bo[dt][0]), "+v"(bo[dt][1]));
        }
        auto frag0 = [&](int sl, int f) __attribute__((always_inline)) {
          unsigned pb = lbase + L_RING + WQ_POFF();
          asm volatile("" : "+v"(pb));
          const unsigned o = (pb ^ ((f & 1) << 5)) + sl * STAGE + ((f % 6) >> 1) * 8192 + (f / 6) * 2048;
          frag_t t;
          t.hf.a = trabs(o); t.hf.b = trabs(o + 1024);
          return t.v;
        };
        u32x4 df[4];
        auto dot_mma = [&](const u32x4 (&xf)[4]) __attribute__((always_inline)) {
          sa = mfma16(df[0], xf[0], f32x4{0.f, 0.f, 0.f, 0.f}); sb = mfma16(df[1], xf[1], f32x4{0.f, 0.f, 0.f, 0.f});
          sa = mfma16(df[2], xf[2], sa); sb = mfma16(df[3], xf[3], sb);
        };
        auto dot_store = [&](unsigned dst) __attribute__((always_inline)) {
          if ((l & 15) == 0) st16(unpoison4(__builtin_bit_cast(u32x4, sa + sb)), rs, g * 16, dst + (h * 8 + p) * 512 + w * 64);
        };
        auto tile_sum = [&](u32x4 pt, int dst) __attribute__((always_inline)) {
          f32x4 c = __builtin_bit_cast(f32x4, pt);
#pragma unroll
          for (int e = 0; e < 4; ++e) { c[e] += dppf<0x124>(c[e]); c[e] += dppf<0x128>(c[e]); c[e] = add_x16(c[e]); }
          if ((l & 28) == 0) *(LDS_AS f32x4*)(smem + dst + (w * 2 + (l >> 5)) * 64 + (l & 3) * 16) = c;
        };
        frag_t bfr[6];
#define QKV_BO(s, n, x) (bo[((n) ^ (x)) & 1][SLOT((s) >> 2) >> 1] + (SLOT((s) >> 2) & 1) * STAGE + ((n) >> 1) * 8192 + ((s) & 3) * 2048 + (x) * 256)
#define QKV_RDA(s, n)                                                                             \
  {                                                                                               \
    if constexpr (IT == 0) bfr[n].hf.a = trabs(QKV_BO(s, n, 0));                                  \
    else bfr[n].v = *(const LDS_AS u32x4*)(size_t)(bo[0][SLOT((s) >> 2) >> 1] + (SLOT((s) >> 2) & 1) * STAGE + (((s) & 3) * 6 + (n)) * 1024); \
  }
#define QKV_RDB(s, n) { if constexpr (IT == 0) bfr[n].hf.b = trabs(QKV_BO(s, n, 1)); }
#define XP_OFF() (OFF_XNEW + ((rot + 5) & 7) * 1024)
#define FP_VO() (w == 3 ? (l >> 2) * 256 + (l & 3) * 16 : ((l >> 2) & 7) * 512 + (l >> 5) * 64 + (l & 3) * 16)
#define FP_OFF(i) (w == 3 ? OFF_XNEW + (i) * 4096 : OFF_FX + h * 4096 + w * 128)
#define FX_OK(n) __all(((n) == 4 ? max(max(umax4(fx[0]), umax4(fx[1])), max(umax4(fx[2]), umax4(fx[3]))) : max(umax4(fx[0]), umax4(fx[1]))) != POISON)
#define SINCE(s0) (vm_at(IT, s, n) - vm_at(IT, s0, 5))
#define DEF4(x) asm volatile("" : "=v"(x[0]), "=v"(x[1]), "=v"(x[2]), "=v"(x[3]))
        LDS_AS unsigned* qcnt = (LDS_AS unsigned*)(smem + L_QC);
        unsigned qc;
        unsigned qadr = lbase + L_QC, qone = 1;
        asm volatile("" : "+v"(qadr), "+v"(qone));
#define count_in(i) asm volatile("s_mov_b64 exec, 1\n\tds_add_u32 %0, %1 offset:%2\n\ts_mov_b64 exec, -1" ::"v"(qadr), "v"(qone), "n"(4 * (i)) : "memory")
        auto count_wait = [&](int i, unsigned want) __attribute__((always_inline)) {
          if (__builtin_expect((unsigned)__builtin_amdgcn_readfirstlane(qc) < want, 0)) {
            while ((unsigned)__builtin_amdgcn_readfirstlane(__hip_atomic_load(qcnt + i, __ATOMIC_RELAXED, __HIP_MEMORY_SCOPE_WORKGROUP)) < want) {}
          }
          asm volatile("" ::: "memory");
        };
        if constexpr (IT == 0) {
          wait_vm<PRE0 - vm_unit(1)>();
          STAMPAT(46);
          X_READ(0); X_READ(1);
          asm volatile("s_barrier" ::: "memory");
        }
        STAMPAT(39);
#pragma unroll
        for (int n = 0; n < 6; ++n) { QKV_RDA(0, n); if (n < 5) QKV_RDB(0, n); }
        f32x2 lv[4], xs, xq;
        float lmean;
        f32x4 sc;
#define X1_OFF() (F_X1 + ((p * RX1 + (xcd >> 1) % RX1) * 32 + (rt >> 2) * 16 + (l & 15)) * SX1)
        unsigned long long xok = 0;
        RowFin fin;
        auto x1_need = [&](unsigned long long m) __attribute__((always_inline)) {
          if (__builtin_expect((xok & m) != m, 0)) {
            for (unsigned spins = 0;;) {
              const u32x2 f = ld8(rs, X1_OFF(), 0);
              xok = __builtin_amdgcn_ballot_w64(f[0] == T[0] && f[1] == T[1]);
              if ((xok & m) == m) break;
              if (spin_fail(spins, rs, T)) { xok = ~0ull; break; }
              __builtin_amdgcn_s_sleep(KSL);
            }
            asm volatile("" ::: "memory");
          }
        };
        auto gap = [&](auto sc_, auto nc_) __attribute__((always_inline)) {
          constexpr int s = decltype(sc_)::value, n = decltype(nc_)::value, ar = a_req(s, n);
          {
            constexpr int tc = (s + 2) / 4;
            if constexpr (tc >= 1 && tc < 8 && s == 4 * tc - 2 && n == 5) {
              if constexpr (!IT || tc >= 3) wait_vm<vm_at(IT, s, n) - vm_tile(IT, tc)>();
              if constexpr (tc == 7 && !IT) asm volatile("s_waitcnt lgkmcnt(4)" ::: "memory");
              asm volatile("s_barrier" ::: "memory");
            }
          }
          if constexpr (IT == 0 && n == 0 && s + 2 < 32) { wait_vm<vm_at(0, s, 0) - vm_unit(s + 2)>(); X_READ(s + 2); SB(); }
          if constexpr (n == 0 && tile_piece(IT, s)) QKV_DMA1(((s - tp0(IT)) >> 2) + 3, (s - tp0(IT)) & 3);
          if constexpr (IT == 0) {
            if constexpr ((n == 2 || n == 3) && s + 5 < 32) {
              if constexpr (n == 2) asm volatile("s_waitcnt lgkmcnt(%0)" ::"n"(s + 1 < QL ? 4 : 1) : "memory");
              X_UNIT1(s + 5, n - 2);
            }
            if constexpr (s >= W2S && s < W2S + 3 && n == 4) QKV_DMA1(2, s - W2S);
            if constexpr (s == LNL && n == 5) LN_LOAD();
            if constexpr (s == 0 && n < 2) { X_UNIT1(3 + n, 0); X_UNIT1(3 + n, 1); }
            if constexpr (s == LNS && n == 1) {
              wait_vm<vm_at(0, s, n) - vm_at(0, LNL + 1, 0)>();
              asm volatile("" : "+v"(ep), "+v"(xl[0]), "+v"(xl[1])::"memory");
              const unsigned long long e = (((unsigned long long)(unsigned)__builtin_amdgcn_readfirstlane(ep[1]) << 32) | (unsigned)__builtin_amdgcn_readfirstlane(ep[0])) + 1ull + dispatch_id() * 0x9E3779B97F4A7C16ull;
              T = u32x2{(unsigned)e, (unsigned)(e >> 32)};
              unpack8v(xl[0], lv);
              xs = (lv[0] + lv[1]) + (lv[2] + lv[3]);
              xq = __builtin_elementwise_fma(lv[0], lv[0], lv[1] * lv[1]) + __builtin_elementwise_fma(lv[2], lv[2], lv[3] * lv[3]);
              asm volatile("" : "+v"(xs), "+v"(xq));
            }
            if constexpr (s == LNS && n == 4) {
              unpack8v(xl[1], lv);
              xs += (lv[0] + lv[1]) + (lv[2] + lv[3]);
              xq += __builtin_elementwise_fma(lv[0], lv[0], lv[1] * lv[1]) + __builtin_elementwise_fma(lv[2], lv[2], lv[3] * lv[3]);
              auto t = __builtin_amdgcn_permlane32_swap(__builtin_bit_cast(unsigned, hadd(xs)), __builtin_bit_cast(unsigned, hadd(xq)), false, false);
              lmean = asf(t[0]) + asf(t[1]);
              asm volatile("" : "+v"(lmean));
            }
            if constexpr (s == LNS + 1 && n == 1) { lmean += dppf<0xB1>(lmean); lmean += dppf<0x4E>(lmean); lmean += dppf<0x124>(lmean); asm volatile("" : "+v"(lmean)); }
            if constexpr (s == LNS + 1 && n == 4) { lmean = add_x16(lmean + dppf<0x128>(lmean)) * (1.0f / 1024.0f); asm volatile("" : "+v"(lmean)); }
            if constexpr (s == LNS + 2 && n == 1) {
              const float m2 = asf(__builtin_amdgcn_readlane(__builtin_bit_cast(unsigned, lmean), 32));
              xs[0] = ln_rd(fmaxf(__builtin_fmaf(-lmean, lmean, m2), 0.f));
              asm volatile("" : "+v"(xs));
            }
            if constexpr (s == LNS + 2 && n == 5) {
              if (l == 0) st16(u32x4{__builtin_bit_cast(unsigned, (float)xs[0]), __builtin_bit_cast(unsigned, -rbf1(lmean)), T[0], T[1]}, rs, OFF_AB + (p * 256 + h * 8 + w) * 16, 0);
            }
            if constexpr (s == POI && n == 1) {
              if (w == 1) {
                const unsigned prb = 2 * p + (h >> 4), pct = h & 15;
                unsigned pz1 = POISON;
                asm volatile("" : "+v"(pz1));
                const u32x4 pz = {pz1, pz1, pz1, pz1};
                st16(pz, rs, OFF_STAT + (prb * 16 + pct) * 2048 + l * 16, 0);
                st16(pz, rs, OFF_STAT + (prb * 16 + pct) * 2048 + 1024 + l * 16, 0);
                if (l < 16) st16(pz, rs, OFF_CA + prb * 4096 + pct * 256 + l * 16, 0);
                else if (l == 16 && pct < RKEY) st4(pz1, rs, KEY_AT(0, pct) + prb * SKEY, 0);
              }
            }
            if constexpr (s == POI + 4 && n == 1) { if (w == 1 && l < RX) st8(T, rs, F_XN + (p * RX + l) * 256 + h * 8, 0); }
          } else {
            if constexpr ((s == 0 && n == 0) || (s == 2 && n == 2)) asm volatile("buffer_load_dwordx2 %0, %1, %2, 0 offen sc1" : "=v"(cxn) : "v"(X1_OFF()), "s"(rs) : "memory");
            if constexpr ((s == 1 && n == 3) || (s == 4 && n == 0)) {
              wait_vm<vm_at(1, s, n) - (s == 1 ? vm_at(1, 0, 1) : vm_at(1, 2, 3))>();
              asm volatile("" : "+v"(cxn)::"memory");
              xok |= __builtin_amdgcn_ballot_w64(cxn[0] == T[0] && cxn[1] == T[1]);
            }
#define IMG_OFF() (F_X1 + (((l & 7) * RX1 + (xcd >> 1) % RX1) * 32 + (rb & 1) * 16 + ct) * SX1)
            if constexpr (s == 0 && n == 2) { asm volatile("" : "=v"(cx1)); if (w == 1) asm volatile("buffer_load_dwordx2 %0, %1, %2, 0 offen sc1" : "=v"(cx1) : "v"(IMG_OFF()), "s"(rs) : "memory"); }
            if constexpr (s == 2 && n == 4) {
              if (w == 1) {
                wait_vm<vm_at(1, 2, 4) - vm_at(1, 0, 3)>();
                asm volatile("" : "+v"(cx1)::"memory");
                if (!__all(cx1[0] == T[0] && cx1[1] == T[1])) poll_flags(rs, IMG_OFF(), 0, T);
              }
            }
            if constexpr (ar >= 0 && (ar & 7) == 0) x1_need(3ull << (((rot + ar / 8) & 7) * 2));
            const unsigned R = (rb * 4 + (w & 3)) * 32 + (w >> 2) * 16 + (l & 15), so = OFF_STAT + rb * 32768;
            if constexpr (s == (SPL < 0 ? 0 : SPL) && n == (SPL < 0 ? 1 : 4)) {
#pragma unroll
              for (int i = 0; i < 4; ++i) ld16h<0>(fx[i], rs, (4 * g + i) * 2048 + (R & 127) * 16, so);
            }
            if constexpr (s == SPL + 2 && n == 1) {
              wait_vm<vm_at(1, s, n) - (SPL < 0 ? vm_at(1, 0, 2) : vm_at(1, SPL, 5))>();
              PINV4(fx);
              fin = fin_key(rs, T, g * 8192 + (R & 127) * 16, so, smem + L_SKEY, l, w, R, wsum, fx);
              count_in(2);
            }
            if constexpr (s == SPL + 3 && n == 0) qc = __hip_atomic_load(qcnt + 2, __ATOMIC_RELAXED, __HIP_MEMORY_SCOPE_WORKGROUP);
            if constexpr (s == SPL + 3 && n == 1) { count_wait(2, 8); fin_cand(rs, smem + L_SKEY, 0, g, rb, ct, fin, v); STAMPAT(51); }
            if constexpr (s == SPL + 3 && n == 3) {
              fin_x2(fin, v);
              x2p[0] = pack8v(v); x2p[1] = pack8v(v + 4);
              asm volatile("" : "+v"(x2p[0]), "+v"(x2p[1]));
            }
            if constexpr (s == SPL + 3 && n == 5) {
              if ((w & 3) * 2 + (w >> 2) == ct >> 1) {
                const float f = 1.0f + fin.rd, rd2 = ln_rd(fin.var * f * f), mb2 = rbf1(__builtin_fmaf(fin.mean - fin.mb, fin.rd, fin.mean));
                if (g == 0 && ((l >> 3) & 1) == (ct & 1))
                  st16(u32x4{__builtin_bit_cast(unsigned, f * rd2), __builtin_bit_cast(unsigned, -__builtin_fmaf(fin.mb, fin.rd, mb2) * __builtin_amdgcn_rcpf(f)), T[0], T[1]}, rs, OFF_AB + 32768 + R * 16, 0);
              }
            }
          }
          if constexpr (IT == 1 && ar >= 0) A_LOAD1(ar / 2, ar & 1);
          if constexpr (!IT && s == CSL && n == 4) { asm volatile("" : "=v"(fx[0])); if (w < 6) ld16h<0>(fx[0], rs, l * 16, OFF_CS + (h * 6 + w) * 1024); }
          if constexpr (!IT && s == CSS && n == 2) {
            if (w < 6) {
              wait_vm<SINCE(CSL)>();
              asm volatile("" : "+v"(fx[0])::"memory");
              tag_wait(rs, T, l * 16, OFF_CS + (h * 6 + w) * 1024, fx[0]);
              const f32x2 c = {sum_hi8(asf(fx[0][0])), sum_hi8(asf(fx[0][1]))};
              if (l < 8) *(LDS_AS f32x2*)(smem + L_CSL + w * 64 + l * 8) = c;
            }
          }
          constexpr int ABL = abl(IT), ABS = abs_(IT);
          if constexpr (s == ABL && n == 4) ld16h<0>(bv[0], rs, (l & 31) * 16, AB_OFF());
          if constexpr (s == ABS && n == 2) {
            wait_vm<SINCE(ABL)>();
            asm volatile("" : "+v"(bv[0])::"memory");
            tag_wait(rs, T, (l & 31) * 16, AB_OFF(), bv[0]);
            const unsigned t0 = bv[0][0], t1 = bv[0][1];
            ((LDS_AS unsigned*)(smem + L_ABW + rt * 2048))[l] = l < 32 ? t0 : t1;
          }
          if constexpr (s == ABS && n == 3) {
#pragma unroll
            for (int a = 0; a < 2; ++a) {
              const LDS_AS unsigned* aw = (const LDS_AS unsigned*)(smem + L_ABW + rt * 2048) + 16 * a;
              aq[a] = aw[l & 15]; bq[a] = aw[32 + (l & 15)];
              av[a] = *(const LDS_AS u32x4*)(aw + 4 * g); bv[a] = *(const LDS_AS u32x4*)(aw + 32 + 4 * g);
            }
          }
          if constexpr (s == ABS && n == 5) {
#pragma unroll
            for (int a = 0; a < 2; ++a) { b2[a] = f32x2{asf(bq[a]), asf(bq[a])}; asm volatile("" : "+v"(b2[a])); }
          }
          if constexpr (s == ABS + 1) {
            constexpr int k = (n + 4) % 6;
            if constexpr (k < 4) cs4[k & 1] = *(const LDS_AS f32x4*)(smem + L_CSL + k * 64 + g * 16);
            else cs4[k & 1][0] = *(const LDS_AS float*)(smem + L_CSL + k * 64 + (l & 15) * 4);
          }
          if constexpr ((s == ABS + 1 && n > 0) || (s == ABS + 2 && n == 0)) {
            constexpr int k = (n + 3) % 6;
            if constexpr (k < 4) {
#pragma unroll
              for (int a = 0; a < 2; ++a) acc[a][k] = __builtin_elementwise_fma(cs4[k & 1], f32x4{b2[a][0], b2[a][1], b2[a][0], b2[a][1]}, acc[a][k]);
            } else {
              f32x2 c2 = {cs4[k & 1][0], cs4[k & 1][0]};
              asm volatile("" : "+v"(c2));
#pragma unroll
              for (int a = 0; a < 2; ++a) acc[a][k] = __builtin_elementwise_fma(f32x4{c2[0], c2[1], c2[0], c2[1]}, __builtin_bit_cast(f32x4, bv[a]), acc[a][k]);
            }
          }
          if constexpr (IT == 0) {
            if constexpr (s == CSD && n < 3) fx[n] = frag0(SLOT(s >> 2), 3 * w + n);
            if constexpr (s == CSD + 1 && n == 1) {
              unsigned one2 = 0x3F803F80u;
              asm volatile("" : "+v"(one2));
              const u32x4 o4 = {one2, one2, one2, one2};
              const f32x4 z4 = {0.f, 0.f, 0.f, 0.f};
              sa = mfma16(fx[0], o4, z4); sb = mfma16(fx[1], o4, z4); sc = mfma16(fx[2], o4, z4);
            }
            if constexpr (s == CSD + 2 && n == 1) {
              const int c = l & 15;
              const f32x4 t0 = sa, t1 = sb, t2 = sc, t = c == 0 ? +t0 : c == 1 ? +t1 : +t2;
              if (c < 3) *(LDS_AS f32x4*)(smem + L_CSP + (w * 3 + c) * 64 + g * 16) = t;
              count_in(2);
            }
            if constexpr (s == CSD + 4 && n == 1) {
              if (w == 2) {
                qc = 0;
                count_wait(2, 8);
                const LDS_AS f32x2* cp = (const LDS_AS f32x2*)(smem + L_CSP) + min(l, 47);
                const f32x2 t = (cp[0] + cp[48]) + (cp[96] + cp[144]);
                if (l < 48) st16(u32x4{__builtin_bit_cast(unsigned, (float)t[0]), __builtin_bit_cast(unsigned, (float)t[1]), T[0], T[1]}, rs, (l >> 3) * 1024 + (l & 7) * 16, OFF_CS + h * 6144 + p * 128);
              }
            }
            if constexpr (s >= 29 && (n & 1)) keep[3 * (s - 29) + (n >> 1)] = frag0(SLOT(s - 24), 3 * w + (n >> 1));
            if constexpr (s == (TS0 < TEX ? TS0 : TEX) - 2 && n == 1) {
              ARGP(pwg, 5); ARGP(pwu, 6);
              const int t = tid < 288 ? tid - 64 : tid - 288, go = (bi * 32 + t / 7) * F_ + xcd * 448 + (t % 7) * 64;
              tsrc = tid < 64 ? pwo + bid * 4096 + tid * 64 : tid < 288 ? pwg + go : pwu + go;
              asm volatile("" : "+v"(tsrc));
            }
            if constexpr (s >= TS0 && s < TS0 + TSN + TSD * (7 >> TSG) && n == 4) {
              if (tstep == s && ((TM0 >> w) & 1)) TOUCH(tsrc);
            }
            if constexpr (s == TEX && n == 5) {
              const GAS bf16* t2 = ((TM1 >> w) & 1) ? tsrc : (const GAS bf16*)x + p * 262144;
              TOUCH(t2);
            }
          } else {
            if constexpr (s >= 27 && s < 30 && (n == 0 || n == 3)) { const unsigned wov = WO_VOFF(); WO_PIECE(4 + 2 * (s - 27) + n / 3); }
            if constexpr (WON && s == WOS && n == 4) st16(wof, rs, (ct * 128 + rb * 8 + w) * 1024 + l * 16, OFF_WOI);
            if constexpr (WON && s == 19 && n == 1) { if (tid == 64) st8(T, rs, (ct * 16 + rb) * 256, F_WOI); }
            if constexpr (WON && s == 23 && n == 4) { asm volatile("" : "=v"(cx1)); if (w == 2) ld8h(cx1, rs, (l & 15) * 256, F_WOI + ct * 4096); }
            if constexpr (WON && s == 26 && n == 3) {
              if (w == 2) {
                wait_vm<SINCE(23)>();
                asm volatile("" : "+v"(cx1)::"memory");
                if (!__all(cx1[0] == T[0] && cx1[1] == T[1])) poll_flags(rs, (l & 15) * 256, F_WOI + ct * 4096, T);
              }
            }
            if constexpr (s == FKL && n == 4) { asm volatile("" : "=v"(kv)); if (w == 0) ld4h<0>(kv, rs, (l & 15) * SKEY, KEY_AT(0, KREP())); }
            if constexpr (s == FSL && n == 1) {
              asm volatile("" : "=v"(fx[0]), "=v"(fx[1]));
              if (w == 0) {
                wait_vm<SINCE(FKL)>();
                asm volatile("" : "+v"(kv)::"memory");
                STAMPAT(52);
                const unsigned k_ = key_wait(rs, T, l, 0, KREP(), kv);
                STAMPAT(53);
                if (l == 0) *(LDS_AS unsigned*)(smem + L_SKEY + 32) = k_;
                fkey = k_;
                ld16h<0>(fx[0], rs, (l >> 3) * 256 + (l & 7) * 16, cand_off(k_, 0)); ld16h<2048>(fx[1], rs, (l >> 3) * 256 + (l & 7) * 16, cand_off(k_, 0));
              }
            }
            if constexpr (s == FS0 - 1 && n == FSN) {
              if (w == 0) {
                wait_vm<vm_at(1, s, n) - vm_at(1, FSL, 2)>();
                asm volatile("" : "+v"(fx[0]), "+v"(fx[1])::"memory");
                word_wait<2>(rs, T, (l >> 3) * 256 + (l & 7) * 16, cand_off(fkey, 0), fx);
                *(LDS_AS u32x4*)(smem + L_ROW + l * 16) = fx[0]; *(LDS_AS u32x4*)(smem + L_ROW + 1024 + l * 16) = fx[1];
                count_in(0);
              }
            }
            if constexpr (s == FS0 - 1 && n == 1) {
              asm volatile("" : "=v"(dfr[0]), "=v"(dfr[1]), "=v"(dfr[2]), "=v"(dfr[3]), "=v"(dfr[4]), "=v"(dfr[5]), "=v"(dfr[6]), "=v"(dfr[7]));
              if (ffn_on) {
#pragma unroll
                for (int i = 0; i < 8; ++i) dfr[i] = trread(smem, L_DN + w * 4096 + g * 1024 + ((l & 15) >> 2) * 256 + ((i ^ ((l >> 2) & 7)) * 32) + (l & 3) * 8);
              }
            }
            if constexpr (s == FS0) {
              if constexpr (n == 0) qc = __hip_atomic_load(qcnt, __ATOMIC_RELAXED, __HIP_MEMORY_SCOPE_WORKGROUP);
              if constexpr (n == 1) {
                count_wait(0, 1);
                kv = *(const LDS_AS unsigned*)(smem + L_SKEY + 32);
#pragma unroll
                for (int i = 0; i < 4; ++i) fx[i] = *(const LDS_AS u32x4*)(smem + L_ROW + w * 256 + i * 64 + g * 16);
              }
              if constexpr (n == 2) {
                fkey = __builtin_amdgcn_readfirstlane(kv);
                sel0 = key_row(fkey);
                aff = (int)(sel0 >> 8) == p;
                asm volatile("" : "=v"(sa), "=v"(sb));
                if (ffn_on) ffn_gu(gv, uv, fx, sa, sb);
              }
              if constexpr (n == 4) {
                if (ffn_on) {
                  ffn_gu_store(smem, l, w, sa, sb);
                  count_in(1);
                }
              }
            }
            if constexpr (s == FS0D) {
              if constexpr (n == 0) qc = __hip_atomic_load(qcnt + 1, __ATOMIC_RELAXED, __HIP_MEMORY_SCOPE_WORKGROUP);
              if constexpr (n == 1) { asm volatile("" : "=v"(sa)); if (ffn_on) { count_wait(1, 8); sa = ffn_d_read(smem, l); } }
              if constexpr (n == 2) { asm volatile("" : "=v"(hh)); if (ffn_on) hh = ffn_d_h(sa); }
              if constexpr (n == 4) { asm volatile("" : "=v"(sa)); if (ffn_on) sa = ffn_d_all(dfr, hh); }
              if constexpr (n == 5) { if (ffn_on) ffn_d_store(rs, l, w, 0, slice, sa); STAMPAT(54); }
            }
            if constexpr (s == FS1 && n == 4) {
              DEF4(fx);
              asm volatile("" : "=v"(cxn), "=v"(cx1));
              if (!ffn_on) {
                const unsigned G = xcd * 4 + bi - 28;
#pragma unroll
                for (int k = 0; k < 4; ++k) ld16h<0>(fx[k], rs, RED_VO(k), RED_SO(0, G));
                ld8h(cxn, rs, RED_JJ(), cand_off(fkey, 0) + XO(G)); ld8h(cx1, rs, RED_JJ() + 128, cand_off(fkey, 0) + XO(G));
              }
            }
            if constexpr (s == FS1S && n == 2) {
              if (!ffn_on) {
                wait_vm<SINCE(FS1)>();
                PINV4(fx);
                asm volatile("" : "+v"(cxn), "+v"(cx1)::"memory");
                STAMPAT(55);
                red_sum(rs, smem, T, tid, w, 0, xcd * 4 + bi - 28, fkey, fx, cxn, cx1);
                STAMPAT(56);
                BAR();
                if (w == 0) {
                  const u32x2 ov = red_final(smem, l, red_gate(fkey), cxn, cx1);
                  if (l < 8) st8(unpoison2(ov), rs, OFF_XNEW + (xcd * 4 + bi - 28) * 256 + ((l & 3) * 2 + (l >> 2)) * 8, 0);
                }
              }
            }
            if constexpr (s == FXA && n == 4) { asm volatile("" : "=v"(fx[0])); if (w == 1) ld16h<0>(fx[0], rs, ((l >> 2) & 3) * 256 + (l & 3) * 16, XP_OFF()); }
            if constexpr (s == FXB - 1 && n == FSN) {
              if (w == 1) {
                wait_vm<SINCE(FXA)>();
                asm volatile("" : "+v"(fx[0])::"memory");
                word_wait<1>(rs, T, ((l >> 2) & 3) * 256 + (l & 3) * 16, XP_OFF(), fx);
                if (l < 16) *(LDS_AS u32x4*)(smem + L_ROW + l * 16) = fx[0];
                count_in(0);
              }
            }
            if constexpr (s == FXB) {
#define FX_FRAG(i) (*(const LDS_AS u32x4*)(size_t)(bo[0][0] + STAGE + (6 * (i) + w) * 1024))
              if constexpr (n == 0) { STAMPAT(58); qc = __hip_atomic_load(qcnt, __ATOMIC_RELAXED, __HIP_MEMORY_SCOPE_WORKGROUP); }
              if constexpr (n == 1) {
                DEF4(fx); DEF4(df);
                if (w < 6) {
                  count_wait(0, 2);
#pragma unroll
                  for (int i = 0; i < 4; ++i) { fx[i] = *(const LDS_AS u32x4*)(smem + L_ROW + i * 64 + g * 16); df[i] = FX_FRAG(i); }
                }
              }
              if constexpr (n == 2) { asm volatile("" : "=v"(sa), "=v"(sb)); if (w < 6) dot_mma(fx); }
              if constexpr (n == 4) { if (w < 6) dot_store(OFF_FX); STAMPAT(57); }
            }
            if constexpr (s == FXL && n == 4) {
              asm volatile("" : "=v"(fx[0]), "=v"(fx[1]));
              if (aff && w < 4) { ld16h<0>(fx[0], rs, FP_VO(), FP_OFF(0)); ld16h<0>(fx[1], rs, FP_VO(), FP_OFF(1)); }
            }
            if constexpr (s == FXS && n == 4) {
              if (aff && w < 4) {
                STAMPAT(59);
                wait_vm<0>();
                asm volatile("" : "+v"(fx[0]), "+v"(fx[1])::"memory");
                if (!FX_OK(2)) {
                  unsigned spins = 0;
                  do {
                    if (spin_fail(spins, rs, T)) break;
                    fx[0] = ld16(rs, FP_VO(), FP_OFF(0)); fx[1] = ld16(rs, FP_VO(), FP_OFF(1));
                  } while (!FX_OK(2));
                }
                STAMPAT(60);
                if (w == 3) {
                  f32x2 nx[8];
                  unpack8v(fx[0], nx); unpack8v(fx[1], nx + 4);
                  const f32x2 sm = ((nx[0] + nx[1]) + (nx[2] + nx[3])) + ((nx[4] + nx[5]) + (nx[6] + nx[7]));
                  const float mean = sum64(hadd(sm)) * (1.0f / 1024.0f);
                  const f32x2 m2 = {mean, mean};
                  f32x2 q = {0.f, 0.f};
#pragma unroll
                  for (int j = 0; j < 8; ++j) { const f32x2 d = nx[j] - m2; q = __builtin_elementwise_fma(d, d, q); }
                  const float rd = ln_rd(sum64(hadd(q)) * (1.0f / 1024.0f));
                  if (l == 0) *(LDS_AS f32x2*)(smem + L_FXL + 384) = f32x2{rd, -rbf1(mean)};
                } else {
                  tile_sum(fx[0], L_FXL);
                }
                count_in(3);
              }
            }
          }
        };
        static_for<32>([&](auto sc_) __attribute__((always_inline)) {
          constexpr int s = decltype(sc_)::value;
          if constexpr (s > 0) PIN_ACC();
          if constexpr (QPR && s == QPR) { if (w >= 4) __builtin_amdgcn_s_setprio(1); }
          if constexpr (IT == 1) wait_vm<vm_at(IT, s, 0) - vm_a(IT, s)>();
          pin_w<s % WA>(af[s % WA][0], af[s % WA][1]);
          VSTEP(s);
          SB();
          static_for<6>([&](auto nc_) __attribute__((always_inline)) {
            constexpr int n = decltype(nc_)::value;
            if constexpr (s == 0) { acc[0][n] = f32x4{0.f, 0.f, 0.f, 0.f}; acc[1][n] = acc[0][n]; }
            constexpr bool on = s < QL || n >= 2;
            if constexpr (on) acc[0][n] = n < 4 ? mfma16(bfr[n].v, af[s % WA][0], acc[0][n]) : mfma16(af[s % WA][0], bfr[n].v, acc[0][n]);
            SB();
            if constexpr (n == 0) { QKV_RDB(s, 5); }
            else if constexpr (s + 1 < 32 && (s + 1 < QL || n > 2)) QKV_RDB(s + 1, n - 1);
            SB();
            if constexpr (on) acc[1][n] = n < 4 ? mfma16(bfr[n].v, af[s % WA][1], acc[1][n]) : mfma16(af[s % WA][1], bfr[n].v, acc[1][n]);
            SB();
            if constexpr (s + 1 < 32 && (s + 1 < QL || n >= 2)) QKV_RDA(s + 1, n);
            SB();
            gap(sc_, nc_);
            GSTEP(s, n);
            SB();
          });
        });
        PIN_ACC();
        VSTEP(32);
        STAMPAT(18);
        VSTEP(33);
        frag_t qw[32 - QL][2];
        static_for<32 - QL>([&](auto c_) __attribute__((always_inline)) {
          constexpr int s = QL + decltype(c_)::value;
          QKV_RDA(s, 0); QKV_RDB(s, 0); QKV_RDA(s, 1); QKV_RDB(s, 1);
          qw[s - QL][0] = bfr[0]; qw[s - QL][1] = bfr[1];
        });
        SB();
        const bool own = aff && (int)((sel0 >> 5) & 7) == rt;
        const bool m0 = !(sel0 & 16), mq = (l & 15) == (int)(sel0 & 15), mv = g == (int)((sel0 & 15) >> 2);
        auto patch_qk = [&](int n) __attribute__((always_inline)) {
          f32x4 t = *(const LDS_AS f32x4*)(smem + L_FXL + n * 64 + g * 16);
          const f32x4 c = *(const LDS_AS f32x4*)(smem + L_CSL + n * 64 + g * 16);
          const float nb = *(const LDS_AS float*)(smem + L_FXL + 388);
#pragma unroll
          for (int e = 0; e < 4; ++e) { t[e] = __builtin_fmaf(c[e], nb, t[e]); asm volatile("" : "+v"(t[e])); }
          acc[0][n] = (mq && m0) ? t : acc[0][n];
          acc[1][n] = (mq && !m0) ? t : acc[1][n];
        };
        if (own) {
          while (__hip_atomic_load(qcnt + 3, __ATOMIC_RELAXED, __HIP_MEMORY_SCOPE_WORKGROUP) < 4u) {}
          asm volatile("" ::: "memory");
          patch_qk(2); patch_qk(3);
          const f32x2 nab = *(const LDS_AS f32x2*)(smem + L_FXL + 384);
          const LDS_AS float* fl_ = (const LDS_AS float*)(smem + L_FXL + 256) + (l & 15);
          const LDS_AS float* cl_ = (const LDS_AS float*)(smem + L_CSL + 256) + (l & 15);
          const float t4 = __builtin_fmaf(cl_[0], nab[1], fl_[0]), t5 = __builtin_fmaf(cl_[16], nab[1], fl_[16]);
          const unsigned na = __builtin_bit_cast(unsigned, (float)nab[0]);
#pragma unroll
          for (int a = 0; a < 2; ++a) {
            const bool ma = a ? !m0 : m0;
            aq[a] = (mq && ma) ? na : aq[a];
#pragma unroll
            for (int e = 0; e < 4; ++e) {
              const bool me = mv && ma && e == (int)(sel0 & 3);
              acc[a][4][e] = me ? t4 : acc[a][4][e]; acc[a][5][e] = me ? t5 : acc[a][5][e];
              av[a][e] = me ? na : av[a][e];
            }
          }
        }
        constexpr float C2 = 1.4426950408889634f * 0.17677669529663687f;
#pragma unroll
        for (int a = 0; a < 2; ++a) {
          f32x2 a2 = {asf(aq[a]), asf(aq[a])};
          qs2[a] = f32x2{asf(aq[a]) * C2, asf(aq[a]) * C2};
          asm volatile("" : "+v"(a2), "+v"(qs2[a]));
          acc[a][2] *= f32x4{a2[0], a2[1], a2[0], a2[1]}; acc[a][3] *= f32x4{a2[0], a2[1], a2[0], a2[1]};
          acc[a][4] *= __builtin_bit_cast(f32x4, av[a]); acc[a][5] *= __builtin_bit_cast(f32x4, av[a]);
        }
        k0 = pack8(acc[0][2], acc[0][3]); k1 = pack8(acc[1][2], acc[1][3]); v0 = pack8(acc[0][4], acc[1][4]); v1 = pack8(acc[0][5], acc[1][5]);
        const unsigned kvo = OFF_KV + IT * (8u << 20) + (p * 32 + h) * 32768 + rt * 4096;
        if (p < 7) { st16(k0, rs, l * 16, kvo); st16(k1, rs, l * 16 + 1024, kvo); st16(v0, rs, l * 16 + 2048, kvo); st16(v1, rs, l * 16 + 3072, kvo); }
        char* ownp = smem + (8 + rt) * 2048 + l * 16;
        *(u32x4*)(ownp + L_K) = k0;
        if (rt < 7) *(u32x4*)(ownp + L_K + 1024) = k1;
        *(u32x4*)(ownp + L_V) = v0; *(u32x4*)(ownp + L_V + 1024) = v1;
        SB();
        static_for<32 - QL>([&](auto c_) __attribute__((always_inline)) {
          constexpr int s = QL + decltype(c_)::value;
#pragma unroll
          for (int n = 0; n < 2; ++n) { acc[0][n] = mfma16(qw[s - QL][n].v, af[s % WA][0], acc[0][n]); acc[1][n] = mfma16(qw[s - QL][n].v, af[s % WA][1], acc[1][n]); }
        });
        if (own) { patch_qk(0); patch_qk(1); }
      };
      qkv_loop(it_c);
    }
#undef A_LOAD1
#undef QKV_RDA
#undef QKV_RDB
#undef QKV_BO
    STAMPAT(2); STAMPAT(21);
    {
      IDS();
      const GAS bf16 *pwo, *pwg, *pwu;
      asm volatile("s_load_dwordx2 %0, %3, 24\n\ts_load_dwordx2 %1, %3, 40\n\ts_load_dwordx2 %2, %3, 48" : "=&s"(pwo), "=&s"(pwg), "=&s"(pwu) : "s"(ka));
      const int ql = l & 15;
      const f32x4 z4 = {0.f, 0.f, 0.f, 0.f}, ninf = {-INFINITY, -INFINITY, -INFINITY, -INFINITY};
      LDS_AS unsigned* rdy = (LDS_AS unsigned*)(smem + L_RDY);
      const unsigned rtag = T[0] + it;
      u32x4 qf[2];
      f32x4 cn[2], oa[2][2], sum[2], S[2][2];
      bool told = false;
      auto announce = [&](bool now) __attribute__((always_inline)) {
        if (!told) {
          unsigned st;
          asm volatile("s_getreg_b32 %0, hwreg(HW_REG_IB_STS)" : "=s"(st)::"memory");
          if (now || !(st & 0xC0000Fu)) {
            DRAIN();
            if (l == 0) st8(T, rs, rt * SKW, F_KV + (it * 256 + p * 32 + h) * SKV);
            told = true;
            STAMPAT(34);
          }
        }
      };
      unsigned one2 = 0x3F803F80u;
      asm volatile("" : "+v"(one2));
      const u32x4 ones = {one2, one2, one2, one2};
      auto ex8 = [&](f32x4 a, f32x4 b) __attribute__((always_inline)) {
        return u32x4{pack2(__builtin_amdgcn_exp2f(a[0]), __builtin_amdgcn_exp2f(a[1])), pack2(__builtin_amdgcn_exp2f(a[2]), __builtin_amdgcn_exp2f(a[3])),
                     pack2(__builtin_amdgcn_exp2f(b[0]), __builtin_amdgcn_exp2f(b[1])), pack2(__builtin_amdgcn_exp2f(b[2]), __builtin_amdgcn_exp2f(b[3]))};
      };
      {
#pragma unroll
        for (int a = 0; a < 2; ++a) {
          const f32x4 c4 = {qs2[a][0], qs2[a][1], qs2[a][0], qs2[a][1]};
          qf[a] = pack8(acc[a][0] * c4, acc[a][1] * c4);
        }
        f32x4 mk;
#pragma unroll
        for (int e = 0; e < 4; ++e) mk[e] = (4 * g + e <= ql) ? 0.f : -INFINITY;
        const f32x4 d0 = mfma16(k0, qf[0], mk), d1 = mfma16(k1, qf[1], mk), d2 = mfma16(k0, qf[1], z4);
        const float m0 = -max_x32(max_x16(fmaxf(fmaxf(d0[0], d0[1]), fmaxf(d0[2], d0[3]))));
        const float m1 = -max_x32(max_x16(fmaxf(fmaxf(d1[0], d1[1]), fmaxf(d1[2], d1[3]))));
        cn[0] = f32x4{m0, m0, m0, m0}; cn[1] = f32x4{m1, m1, m1, m1};
        asm volatile("" : "+v"(cn[0]), "+v"(cn[1]));
        announce(false);
        const f32x4 e0 = d0 + cn[0];
        const u32x4 p0 = {pack2(__builtin_amdgcn_exp2f(e0[0]), __builtin_amdgcn_exp2f(e0[1])), pack2(__builtin_amdgcn_exp2f(e0[2]), __builtin_amdgcn_exp2f(e0[3])), 0u, 0u};
        const u32x4 p1 = ex8(d2 + cn[1], d1 + cn[1]);
        oa[0][0] = mfma16(v0, p0, z4); oa[0][1] = mfma16(v1, p0, z4); sum[0] = mfma16(ones, p0, z4);
        oa[1][0] = mfma16(v0, p1, z4); oa[1][1] = mfma16(v1, p1, z4); sum[1] = mfma16(ones, p1, z4);
      }
      const char* kb_ = smem + L_K + l * 16;
      const char* vb_ = smem + L_V + l * 16;
      const unsigned wov = WO_VOFF(), fnv = (l >> 1) * (F_ * 2) + (l & 1) * 16;
      asm volatile("" : "+v"(oa[0][0]), "+v"(oa[0][1]), "+v"(oa[1][0]), "+v"(oa[1][1]), "+v"(sum[0]), "+v"(sum[1]));
      const unsigned pw = __builtin_amdgcn_readfirstlane(it * 256 + p * 32 + h - 32), pfo = F_KV + pw * SKV + rt * SKW;
      const bool fetcher = rt < 4 && p > 0;
      announce(false);
      if (QPR) __builtin_amdgcn_s_setprio(0);
      STAMPAT(32);
      BAR();
      asm volatile("" : "+s"(pwo), "+s"(pwg), "+s"(pwu));
      STAMPAT(33);
#define KV_FETCH(gd)                                                                                                          \
  {                                                                                                                           \
    const unsigned pvo = OFF_KV + pw * 32768 + (gd) * 4096;                                                                   \
    dma16s<0>(rs, l * 16, pvo, lbase + L_K + (gd) * 2048); dma16s<1024>(rs, l * 16, pvo, lbase + L_K + (gd) * 2048);           \
    dma16s<0>(rs, l * 16, pvo + 2048, lbase + L_V + (gd) * 2048); dma16s<1024>(rs, l * 16, pvo + 2048, lbase + L_V + (gd) * 2048); \
  }
      auto fetch = [&]() __attribute__((always_inline)) {
        poll_flags(rs, (l & 1) * 4 * SKW, pfo, T);
        KV_FETCH(rt); KV_FETCH(rt + 4);
        if (l == 0) rdy[8 + rt] = rtag;
        STAMPAT(35);
      };
      announce(false);
      const bool first = fetcher && told;
      if (first) fetch();
#define LDS16(a_) (*(const LDS_AS u32x4*)(size_t)(a_))
      u32x4 kf[2], vf[2], p0, p1;
#define AE(a, i) { S[a][(i) >> 2][(i) & 3] = __builtin_amdgcn_exp2f(S[a][(i) >> 2][(i) & 3]); asm volatile("" : "+v"(S[a][(i) >> 2][(i) & 3])); SB(); }
#define AC(a, P, j) { P[j] = pack2(S[a][(j) >> 1][2 * ((j) & 1)], S[a][(j) >> 1][2 * ((j) & 1) + 1]); asm volatile("" : "+v"(P[j])); SB(); }
#define PHASE(a, P, M1, M2, L2, M3, M4, L4, M5)                                                                               \
  AE(a, 0) AE(a, 1) M1; SB(); AE(a, 2) AE(a, 3) M2; SB(); L2; SB(); AC(a, P, 0) AC(a, P, 1) AE(a, 4) M3; SB(); AE(a, 5) AE(a, 6) M4; SB(); \
  L4; SB(); AE(a, 7) AC(a, P, 2) M5; SB(); AC(a, P, 3)
#define PV(a, d, P) oa[a][d] = mfma16(vf[d], P, oa[a][d])
#define RS(a, P) sum[a] = mfma16(ones, P, sum[a])
#define SC(a, t, c) S[a][t] = mfma16(kf[t], qf[a], c)
#define K_AT(ad) (kf[0] = LDS16(ad), kf[1] = LDS16((ad) + 1024))
#define V_AT(ad) (vf[0] = LDS16((ad) + L_V), vf[1] = LDS16((ad) + L_V + 1024))
#define A_FIRST(ad, c00, c01, c10, c11, KN) { K_AT(ad); V_AT(ad); SB(); SC(0, 0, c00); SC(0, 1, c01); SB(); PHASE(0, p0, SC(1, 0, c10), SC(1, 1, c11), KN, , , , ) }
#define A_NEXT(ad, c00, c01, c10, c11, KN)                                                                                        \
  { PHASE(1, p1, PV(0, 0, p0), PV(0, 1, p0), , SC(0, 0, c00), SC(0, 1, c01), , RS(0, p0))                                     \
    PHASE(0, p0, PV(1, 0, p1), PV(1, 1, p1), V_AT(ad), SC(1, 0, c10), SC(1, 1, c11), KN, RS(1, p1)) }
#define A_LAST() { PHASE(1, p1, PV(0, 0, p0), PV(0, 1, p0), , RS(0, p0), , , ) SB(); PV(1, 0, p1); PV(1, 1, p1); RS(1, p1); }
      int nw = 0;
      auto piece = [&]() __attribute__((always_inline)) {
        if constexpr (it == 0) { if (ffn_on && nw < 8) { FFN_DMA(nw); } ++nw; }
      };
#define XN_OFF() (F_XN + (((rt - 6) * 4 + g) * RX + bi % RX) * 256 + (l & 15) * 16)
      u32x4 fxn;
      asm volatile("" : "=v"(fxn));
      unsigned ad = lbase + l * 16 + (6 + rt) * 2048;
      asm volatile("" : "+v"(ad));
      if (rt > 0) {
        A_FIRST(ad + 2048, cn[0], cn[0], cn[1], cn[1], K_AT(ad));
#pragma unroll 1
        for (int i = rt - 1; i > 0; --i) {
          ad -= 2048;
          asm volatile("" : "+v"(ad));
          if (told && rt >= 4) piece();
          announce(false);
          A_NEXT(ad + 2048, cn[0], cn[0], cn[1], cn[1], K_AT(ad));
        }
        announce(false);
        A_LAST();
      }
      announce(true);
      STAMPAT(26);
      {
        f32x4 mk;
#pragma unroll
        for (int e = 0; e < 4; ++e) mk[e] = (4 * g + e > ql) ? 0.f : -INFINITY;
        f32x4 cm0 = cn[0] + mk, cm1 = cn[1] + mk, cmi = ninf;
        asm volatile("" : "+v"(cm0), "+v"(cm1), "+v"(cmi));
        if (rt >= 4) {
          if (p > 0) {
            while (!__all(__hip_atomic_load(rdy + 8 + (l & 3), __ATOMIC_RELAXED, __HIP_MEMORY_SCOPE_WORKGROUP) == rtag)) __builtin_amdgcn_s_sleep(1);
            asm volatile("" ::: "memory");
          }
          if constexpr (it == 0) { for (; nw < 8;) piece(); }
          static_for<it ? 12 : 8>([&](auto c_) __attribute__((always_inline)) { constexpr int i = decltype(c_)::value; WO_PIECE_W(15 - i / 2, 2 * (rt & 3) + (i & 1)); });
          if (it == 0 && rt >= 6) ld16h<0>(fxn, rs, XN_OFF(), 0);
        } else if (p > 0) {
          if (!first) fetch();
          DRAIN();
          if (l < 2) rdy[rt + 4 * l] = rtag;
          if constexpr (it == 0) { for (; nw <= rt;) piece(); }
        }
        STAMPAT(3);
        if (p > 0) {
          unsigned have = 0;
          auto need = [&](int gq) __attribute__((always_inline)) {
            if (__builtin_expect(gq < 8 && !((have >> gq) & 1), 0)) {
              do {
                const unsigned t = __hip_atomic_load(rdy + (l & 7), __ATOMIC_RELAXED, __HIP_MEMORY_SCOPE_WORKGROUP);
                have = (unsigned)__builtin_amdgcn_ballot_w64(t == rtag);
              } while (!((have >> gq) & 1));
              asm volatile("" ::: "memory");
            }
          };
          ad = lbase + l * 16 + rt * 2048;
          asm volatile("" : "+v"(ad));
          need(rt);
          K_AT(ad); V_AT(ad); SB(); SC(0, 0, cm0); SC(0, 1, cn[0]); SB();
          PHASE(0, p0, SC(1, 0, cmi), SC(1, 1, cm1), , , , , )
          need(rt + 1);
          K_AT(ad + 2048);
#pragma unroll 1
          for (int gq = rt + 1; gq < 8; ++gq) {
            ad += 2048;
            asm volatile("" : "+v"(ad));
            piece();
            need(gq + 1);
            A_NEXT(ad, cn[0], cn[0], cn[1], cn[1], K_AT(ad + 2048));
          }
          A_LAST();
        }
        if constexpr (it == 0) { for (; nw < 8;) piece(); }
      }
      STAMPAT(25);
      auto o_store = [&]() __attribute__((always_inline)) {
        const unsigned oo = OFF_O + it * (4u << 20) + ((p * 8 + rt) * 64 + h * 2) * 1024;
#pragma unroll
        for (int a = 0; a < 2; ++a) {
          f32x2 inv = {__builtin_amdgcn_rcpf(sum[a][0]), 0.f};
          inv[1] = inv[0];
          asm volatile("" : "+v"(inv));
          const f32x4 i4 = {inv[0], inv[1], inv[0], inv[1]};
          st16(pack8(oa[a][0] * i4, oa[a][1] * i4), rs, l * 16 + a * 1024, oo);
        }
      };
      {
        float big = 0.f;
#pragma unroll
        for (int a = 0; a < 2; ++a)
#pragma unroll
          for (int d = 0; d < 2; ++d)
            big = __builtin_elementwise_maximum(__builtin_elementwise_maximum(big, __builtin_elementwise_maximum(fabsf(oa[a][d][0]), fabsf(oa[a][d][1]))),
                                                __builtin_elementwise_maximum(fabsf(oa[a][d][2]), fabsf(oa[a][d][3])));
        bool ok = sum[0][0] >= 0.5f && sum[0][0] < 0x1p64f && sum[1][0] >= 0.5f && sum[1][0] < 0x1p64f && big < INFINITY;
        if (!__all(ok)) {
#pragma unroll
          for (int a = 0; a < 2; ++a) {
            const int qs = 2 * rt + a, qq = 256 + 16 * qs + ql, klo = max(qq - 255, p > 0 ? 0 : 256);
            auto score = [&](int t) __attribute__((always_inline)) {
              f32x4 s = mfma16(t == 31 ? k1 : *(const u32x4*)(kb_ + t * 1024), qf[a], z4);
#pragma unroll
              for (int e = 0; e < 4; ++e) { const int kk = 16 * t + 4 * g + e; s[e] = (kk <= qq && kk >= klo) ? s[e] : -INFINITY; }
              return s;
            };
            float mx = -INFINITY;
#pragma unroll 1
            for (int t = p > 0 ? qs : 16; t <= qs + 16; ++t) { const f32x4 s = score(t); mx = fmaxf(fmaxf(mx, fmaxf(s[0], s[1])), fmaxf(s[2], s[3])); }
            mx = max_x32(max_x16(mx));
            oa[a][0] = z4; oa[a][1] = z4; sum[a] = z4;
#pragma unroll 1
            for (int G = p > 0 ? qs >> 1 : 8; G <= (qs + 16) >> 1; ++G) {
              const f32x4 s0 = score(2 * G), s1 = score(2 * G + 1);
              f32x4 e0, e1;
#pragma unroll
              for (int e = 0; e < 4; ++e) { e0[e] = __builtin_amdgcn_exp2f(s0[e] - mx); e1[e] = __builtin_amdgcn_exp2f(s1[e] - mx); }
              const u32x4 pf = pack8(e0, e1);
              oa[a][0] = mfma16(*(const u32x4*)(vb_ + G * 2048), pf, oa[a][0]);
              oa[a][1] = mfma16(*(const u32x4*)(vb_ + G * 2048 + 1024), pf, oa[a][1]);
              sum[a] = mfma16(ones, pf, sum[a]);
            }
          }
        }
      }
      o_store();
      STAMPAT(4);
      if constexpr (it == 0) {
        wait_vm<2>();
        asm volatile("" : "+v"(fxn)::"memory");
        if (rt >= 6 && !__all(fxn[0] == T[0] && fxn[1] == T[1] && fxn[2] == T[0] && fxn[3] == T[1])) poll_flags2(rs, XN_OFF(), T);
        DEF4(gv); DEF4(uv);
        if (ffn_on) {
          const unsigned ro = 65536 + w * 1024 + (4 * g + ((l & 15) >> 2)) * 32 + (l & 3) * 8;
#pragma unroll
          for (int i = 0; i < 4; ++i) {
            frag_t f;
            f.hf.a = trread(smem, ro + i * 16384); f.hf.b = trread(smem, ro + i * 16384 + 512);
            gv[i] = f.v;
            f.hf.a = trread(smem, ro + i * 16384 + 8192); f.hf.b = trread(smem, ro + i * 16384 + 8704);
            uv[i] = f.v;
          }
        }
        PINV4(gv); PINV4(uv);
      }
      BAR();
      if constexpr (it == 1) { WO_PIECE(3); WO_PIECE(2); WO_PIECE(1); WO_PIECE(0); }
      wait_vm<it ? 4 : 0>();
      asm volatile("s_barrier" ::: "memory");
      if (tid == 0) st8(T, rs, h * SO, F_O + (it * 8 + p) * 32 * SO);
      if constexpr (it == 0) {
        WO_PIECE(11); WO_PIECE(10); WO_PIECE(9); WO_PIECE(8); WO_PIECE(7); WO_PIECE(6); WO_PIECE(5); WO_PIECE(4);
        WO_PIECE(3); WO_PIECE(2); WO_PIECE(1); WO_PIECE(0);
      }
    }
    STAMPAT(5);
    IDS();
    ARGP(pwr, 4); ARGP(pwd, 7); ARGP(pout, 8); ARGP(px, 0);
    const int kh = w >> 2, rt2 = rb * 4 + (w & 3);
    const unsigned R = rt2 * 32 + kh * 16 + (l & 15);
    const unsigned to = (rt2 * 64 + ct * 4 + kh) * 1024 + l * 16;
    const unsigned rmo = ct * 64 + ((g & 1) ? 12 + 4 * g : 4 * g);
    f32x2 wv[8];
    {
      u32x4 wa[2], wrp[2], res[2];
#pragma unroll
      for (int j = 0; j < 2; ++j) {
        wrp[j] = *(const GAS u32x4*)(pwr + rmo + 32 * j);
        if constexpr (it == 0) wa[j] = *(const GAS u32x4*)(pwr + l * 8 + j * 512);
        if constexpr (it == 0) res[j] = *(const GAS u32x4*)(px + (R - (l & 15) + 8 * j + (l >> 3)) * 1024 + ct * 64 + (l & 7) * 8);
        else res[j] = ld16(rs, OFF_XNEW + (2 * ct + j) * 256 + g * 16, 0);
      }
      SB();
      unsigned bt[4];
#pragma unroll
      for (int n = 0; n < 4; ++n) {
        bt[n] = lbase + 32768 + 65536 + kh * 4096 + (4 * g + ((l & 15) >> 2)) * 128 + (l & 3) * 8 + ((n ^ (2 * (g & 1) + ((l & 15) >> 3))) << 5);
        asm volatile("" : "+v"(bt[n]));
      }
      if (w == 0) poll_flags(rs, (l & 31) * SO, F_O + (it * 8 + p) * 32 * SO, T);
      asm volatile("s_barrier" ::: "memory");
      STAMPAT(6);
      auto prep = [&]() __attribute__((always_inline)) {
        if constexpr (it == 0) {
          f32x2 t[8];
          unpack8v(wa[0], t); unpack8v(wa[1], t + 4);
          const f32x2 s = ((t[0] + t[1]) + (t[2] + t[3])) + ((t[4] + t[5]) + (t[6] + t[7]));
          wsum = sum64(hadd(s));
        }
        unpack8v(trade16(wrp[0]), wv); unpack8v(trade16(wrp[1]), wv + 4);
        if constexpr (it == 0) {
          char* tp = smem + w * 2048;
          const unsigned c0 = (g & 1) * 2 + (g >> 1), r = l & 15;
#pragma unroll
          for (int j = 0; j < 2; ++j) *(u32x4*)(tp + j * 1024 + (l >> 3) * 128 + (((l ^ (l >> 3)) & 7) << 4)) = res[j];
          res[0] = trade16(*(const u32x4*)(tp + r * 128 + ((c0 ^ (r & 7)) << 4)));
          res[1] = trade16(*(const u32x4*)(tp + r * 128 + (((c0 + 4) ^ (r & 7)) << 4)));
        }
        else { res[0] = R == sel0 ? res[0] : x2p[0]; res[1] = R == sel0 ? res[1] : x2p[1]; }
        unpack8v(res[0], v + 0); unpack8v(res[1], v + 4);
        asm volatile("" : "+v"(v[0]), "+v"(v[1]), "+v"(v[2]), "+v"(v[3]), "+v"(v[4]), "+v"(v[5]), "+v"(v[6]), "+v"(v[7]));
        asm volatile("" : "+v"(wsum), "+v"(wv[0]), "+v"(wv[1]), "+v"(wv[2]), "+v"(wv[3]), "+v"(wv[4]), "+v"(wv[5]), "+v"(wv[6]), "+v"(wv[7]));
      };
      if constexpr (it == 1 && WON) {
        const unsigned abase = OFF_O + (4u << 20) + (rt2 * 64 + kh) * 1024;
        u32x4 af[W2B], bfr[W2L + 1][4];
        unsigned b2[2] = {lbase + 32768 + l * 16, lbase + 98304 + l * 16};
        asm volatile("" : "+v"(b2[0]), "+v"(b2[1]));
#define O_LOAD(s_) af[(s_) % W2B] = ld16(rs, l * 16 + (~(s_) & 1) * 2048, abase + ((31 - (s_)) >> 1) * 4096)
#define WO_RD(s_, n) bfr[(s_) % (W2L + 1)][n] = *(const LDS_AS u32x4*)(size_t)(b2[(31 - (s_)) >> 4] + ((31 - (s_)) & 15) * 4096 + (n) * 1024)
#pragma unroll
        for (int s = 0; s < W2R; ++s) O_LOAD(s);
        SB();
        prep();
        SB();
        f32x4 ac[4];
#pragma unroll
        for (int n = 0; n < 4; ++n) ac[n] = f32x4{v[2 * n][0], v[2 * n][1], v[2 * n + 1][0], v[2 * n + 1][1]};
#pragma unroll
        for (int n = 0; n < 4 * W2L; ++n) WO_RD(n >> 2, n & 3);
#pragma unroll
        for (int s = 0; s < 32; ++s) {
          if (s == W2P && w >= 4) __builtin_amdgcn_s_setprio(1);
          if (s == 24 - W2L) asm volatile("s_barrier" ::: "memory");
          if (s < W2B - W2R) O_LOAD(W2R + 2 * s);
          SB();
#pragma unroll
          for (int n = 0; n < 4; ++n) {
            ac[n] = mfma16(bfr[s % (W2L + 1)][n], af[s % W2B], ac[n]);
            if (s + W2L < 32) WO_RD(s + W2L, n);
            SB();
          }
          if (s < W2B - W2R) O_LOAD(W2R + 2 * s + 1);
          else if (s + W2B < 32) O_LOAD(s + W2B);
          SB();
        }
        __builtin_amdgcn_s_setprio(0);
#undef WO_RD
#undef O_LOAD
        STAMPAT(7);
#pragma unroll
        for (int n = 0; n < 4; ++n) { v[2 * n] = f32x2{ac[n][0], ac[n][1]}; v[2 * n + 1] = f32x2{ac[n][2], ac[n][3]}; }
      } else {
      const unsigned abase = OFF_O + it * (4u << 20) + (rt2 * 64 + 32 * kh) * 1024;
      const unsigned av0 = l * 16 + kh * 1024, av1 = l * 16 + (kh ^ 1) * 1024;
      u32x4 af[WB][2];
#define O_LOAD(s_) { af[(s_) % WB][0] = ld16(rs, av0 + (~(s_) & 1) * 2048, abase + ((15 - (s_)) >> 1) * 4096); af[(s_) % WB][1] = ld16(rs, av1 + (~(s_) & 1) * 2048, abase + ((15 - (s_)) >> 1) * 4096); }
#pragma unroll
      for (int s = 0; s < WR; ++s) O_LOAD(s);
      SB();
      prep();
      SB();
      f32x4 ac[2][4];
#pragma unroll
      for (int a = 0; a < 2; ++a)
#pragma unroll
        for (int n = 0; n < 4; ++n) ac[a][n] = f32x4{0.f, 0.f, 0.f, 0.f};
      u32x4 bfr[WLA + 1][4];
#define WO_RD(buf, s, n)                                                                             \
  {                                                                                                  \
    const unsigned o_ = bt[n] + ((15 - (s)) & 7) * 8192;     \
    frag_t f_;                                                                                       \
    f_.hf.a = trabs(o_); f_.hf.b = trabs(o_ + 2048);                                                 \
    bfr[buf][n] = f_.v;                                                                              \
  }
#pragma unroll
      for (int n = 0; n < 4 * WLA; ++n) WO_RD(n >> 2, n >> 2, n & 3);
#pragma unroll
      for (int s = 0; s < 16; ++s) {
        const int cur = s % (WLA + 1);
        if (WPR && s == WPR && w >= 4) __builtin_amdgcn_s_setprio(1);
        asm volatile("" : "+{v[176:179]}"(ac[0][0]), "+{v[180:183]}"(ac[0][1]), "+{v[184:187]}"(ac[0][2]), "+{v[188:191]}"(ac[0][3]),
                          "+{v[192:195]}"(ac[1][0]), "+{v[196:199]}"(ac[1][1]), "+{v[200:203]}"(ac[1][2]), "+{v[204:207]}"(ac[1][3]));
        if (s == (it == 0 ? 4 : 12) - WLA) asm volatile("s_barrier" ::: "memory");
        if (s == 8 - WLA) {
#pragma unroll
          for (int n = 0; n < 4; ++n) { bt[n] -= 65536; asm volatile("" : "+v"(bt[n])); }
        }
        if (it == 0 && s == WIS) {
#pragma unroll
          for (int i = 0; i < 3; ++i) st16(keep[i], rs, l * 16 + i * 1024, OFF_WI + (h * 8 + ((ROT0 + 5) & 7)) * STAGE + w * 3072);
        }
        if (it == 0 && s == WPS) {
          unsigned pz1 = POISON;
          asm volatile("" : "+v"(pz1));
          const u32x4 pz = {pz1, pz1, pz1, pz1};
          st16(pz, rs, OFF_PART + (tid >> 8) * (1u << 20) + bid * 4096 + (tid & 255) * 16, 0);
          if (w < 2) st16(pz, rs, OFF_STAT + 524288 + (rb * 16 + ct) * 2048 + tid * 16, 0);
          if (w == 4 && l < 40) st16(pz, rs, l < 16 ? OFF_CA + 65536 + rb * 4096 + ct * 256 + l * 16 : OFF_FX + (h * 8 + p) * 512 + (l - 16) * 16, 0);
          if (w == 6 && l < (ct < (int)RKEY ? 3 : 2)) st4(pz1, rs, l < 2 ? OFF_XNEW + (bid >> 3) * 256 + (bid & 7) * 8 + l * 4 : KEY_AT(1, ct) + rb * SKEY, 0);
        }
        if (s < WB - WR) O_LOAD(WR + 2 * s < 16 ? WR + 2 * s : 0);
        SB();
#pragma unroll
        for (int n = 0; n < 4; ++n) {
          ac[0][n] = mfma16(bfr[cur][n], af[s % WB][0], ac[0][n]);
          ac[1][n] = mfma16(bfr[cur][n], af[s % WB][1], ac[1][n]);
          if (s + WLA < 16) WO_RD((s + WLA) % (WLA + 1), s + WLA, n);
          SB();
        }
        if (s < WB - WR) { O_LOAD(WR + 2 * s + 1); }
        else if (s + WB < 16) { O_LOAD(s + WB); }
        SB();
      }
      if (WPR) __builtin_amdgcn_s_setprio(0);
#undef WO_RD
#undef O_LOAD
      STAMPAT(7);
      if constexpr (it == 0 && WON) {
        const int ks = 2 * rb + kh;
        const unsigned o_ = lbase + 32768 + (ks & 15) * 8192 + (ks >> 4) * 4096 + (4 * g + ((l & 15) >> 2)) * 128 + (l & 3) * 8 + (((w & 3) ^ (2 * (g & 1) + ((l & 15) >> 3))) << 5);
        frag_t f_;
        f_.hf.a = trabs(o_); f_.hf.b = trabs(o_ + 2048);
        wof = f_.v;
        asm volatile("" : "+v"(wof));
      }
#define XCH_AT(v) ((v) == 7 ? (rb == 7 ? 139264 : 155648) : (v) * 4096)
#pragma unroll
      for (int n = 0; n < 4; ++n) *(f32x4*)(smem + XCH_AT(w) + n * 1024 + l * 16) = ac[1][n];
#pragma unroll
      for (int n = 0; n < 4; ++n) { v[2 * n] += f32x2{ac[0][n][0], ac[0][n][1]}; v[2 * n + 1] += f32x2{ac[0][n][2], ac[0][n][3]}; }
      BAR();
#pragma unroll
      for (int n = 0; n < 4; ++n) {
        const f32x4 o = *(const f32x4*)(smem + XCH_AT(w ^ 4) + n * 1024 + l * 16);
        v[2 * n] += f32x2{o[0], o[1]}; v[2 * n + 1] += f32x2{o[2], o[3]};
      }
      }
    }
    {
      if (it == 0) { st16(pack8v(v), rs, to, OFF_XN + (4u << 20)); st16(pack8v(v + 4), rs, to + 2048, OFF_XN + (4u << 20)); }
      SB();
      const f32x2 s = ((v[0] + v[1]) + (v[2] + v[3])) + ((v[4] + v[5]) + (v[6] + v[7]));
      f32x2 dp = v[0] * wv[0], q = v[0] * v[0];
#pragma unroll
      for (int j = 1; j < 8; ++j) { dp = __builtin_elementwise_fma(v[j], wv[j], dp); q = __builtin_elementwise_fma(v[j], v[j], q); }
      float mt = hadd(s), qq = hadd(q), dot = hadd(dp);
      add_g3(mt, qq, dot);
      const unsigned so = OFF_STAT + it * 524288 + rb * 32768, lrow = R & 127;
      if (it == 0) wait_vm<2>();
      if (g == 0) st16(unpoison4(__builtin_bit_cast(u32x4, f32x4{mt, qq, dot, 0.f})), rs, ct * 2048 + lrow * 16, so);
      SB();
      if (it == 0) wait_vm<1>();
      if constexpr (it == 0) {
        volatile LDS_AS u32x2* in = (volatile LDS_AS u32x2*)(smem + L_XC);
        if (l == 0) in[w] = T;
        const u32x2 t = in[l & 7];
        if (__all(t[0] == T[0] && t[1] == T[1]) && l < (int)RX1) st8(T, rs, (l * 32 + (rb & 1) * 16 + ct) * SX1, F_X1 + (rb >> 1) * RX1 * 32 * SX1);
      }
      STAMPAT(8);
      if (it == 0) {
        if (ffn_on) { WD_DMA(); }
#pragma unroll
        for (int j = 0; j < 9; ++j) *(u32x4*)(smem + L_RING + (j / 3) * STAGE + (w * 3 + j % 3) * 1024 + l * 16) = keep[j];
      }
      STAMPAT(16);
      if constexpr (it == 0) {
        if (w == 0) poll_flags(rs, ((l & 2) * 8 + ((ROT0 + 5) & 7) * 2 + (l & 1)) * SX1, F_X1 + (p * RX1 + (xcd >> 1) % RX1) * 32 * SX1, T);
        if (tid == 192) *(LDS_AS u32x4*)(smem + L_QC) = u32x4{0u, 0u, 0u, 0u};
        BAR();
        PRE_LOAD(1, ROT0 + 5, 0, NPRE);
        STAMPAT(9);
      } else {
        __builtin_amdgcn_s_sleep(SDIR);
        u32x4 sp[4];
#pragma unroll
        for (int i = 0; i < 4; ++i) sp[i] = ld16(rs, (4 * g + i) * 2048 + lrow * 16, so);
        const RowFin r = fin_key(rs, T, g * 8192 + lrow * 16, so, smem + L_XC, l, w, R, wsum, sp);
        STAMPAT(9);
        BAR();
        fin_cand(rs, smem + L_XC, 1, g, rb, ct, r, v);
        STAMPAT(10);
        fin_x2(r, v);
      }
    }
    STAMPAT(11);
    if (it == 1) {
      u32x4 op[2];
      {
        char* tp = smem + L_OUT + w * 2048;
        const unsigned c0 = (g & 1) * 2 + (g >> 1), r = l & 15;
        *(u32x4*)(tp + r * 128 + ((c0 ^ (r & 7)) << 4)) = trade16(pack8v(v));
        *(u32x4*)(tp + r * 128 + (((c0 + 4) ^ (r & 7)) << 4)) = trade16(pack8v(v + 4));
#pragma unroll
        for (int j = 0; j < 2; ++j) op[j] = *(const u32x4*)(tp + j * 1024 + (l >> 3) * 128 + (((l ^ (l >> 3)) & 7) << 4));
      }
      const unsigned Ro = R - (l & 15) + (l >> 3), oo = (Ro * 1024 + ct * 64) * 2 + (l & 7) * 16;
#define OUT_STORE(c0, c1)                                                                           \
  {                                                                                                 \
    if (c0) __builtin_amdgcn_raw_buffer_store_b128(op[0], rout, oo, 0, SC1);                        \
    if (c1) __builtin_amdgcn_raw_buffer_store_b128(op[1], rout, oo + 16384, 0, SC1);                \
  }
      if (w == 0) {
        __builtin_amdgcn_s_sleep(GSL);
        u32x4 xr[4];
        unsigned cur = 0, best;
        asm volatile("" : "=v"(xr[0]), "=v"(xr[1]));
        for (unsigned spins = 0;;) {
          const unsigned k_ = key_load(rs, l, 1, KREP());
          const bool all = __all(k_ != POISON);
          best = __builtin_amdgcn_readfirstlane(umax16(k_ == POISON ? 0u : k_));
          if (best != cur && ffn_on) {
            cur = best;
            ld16h<0>(xr[0], rs, (l >> 3) * 256 + (l & 7) * 16, cand_off(cur, 1)); ld16h<2048>(xr[1], rs, (l >> 3) * 256 + (l & 7) * 16, cand_off(cur, 1));
          }
          if (all || spin_fail(spins, rs, T)) break;
        }
        STAMPAT(17);
        if (l == 0) *(LDS_AS unsigned*)(smem + L_SKEY + 32) = best;
        asm volatile("s_waitcnt vmcnt(0)" : "+v"(xr[0]), "+v"(xr[1])::"memory");
        if (ffn_on) {
          word_wait<2>(rs, T, (l >> 3) * 256 + (l & 7) * 16, cand_off(best, 1), xr);
          *(LDS_AS u32x4*)(smem + L_ROW + l * 16) = xr[0]; *(LDS_AS u32x4*)(smem + L_ROW + 1024 + l * 16) = xr[1];
        }
      }
      BAR();
      const unsigned key = __builtin_amdgcn_readfirstlane(*(LDS_AS unsigned*)(smem + L_SKEY + 32)), krow = key_row(key);
      STAMPAT(12);
      if (ffn_on) {
        u32x4 xv[4];
#pragma unroll
        for (int i = 0; i < 4; ++i) xv[i] = *(const LDS_AS u32x4*)(smem + L_ROW + w * 256 + i * 64 + g * 16);
        f32x4 ga, ua;
        ffn_gu(gv, uv, xv, ga, ua);
        ffn_gu_store(smem, l, w, ga, ua);
        BAR();
        ffn_d_store(rs, l, w, 1, slice, ffn_d_all(dfr, ffn_d_h(ffn_d_read(smem, l))));
        STAMPAT(13);
        SB();
      }
      OUT_STORE(Ro != krow, Ro + 8 != krow);
      if (!ffn_on) {
        const unsigned G = xcd * 4 + bi - 28;
        if (G == 0 && tid == 0) st8(T, rs, F_EP, 0);
        u32x4 pv[4];
        u32x2 cxn, cx1;
        const float sig = red_gate(key);
        __builtin_amdgcn_s_sleep(RSL);
        red_load(rs, tid, 1, G, key, pv, cxn, cx1);
        red_sum(rs, smem, T, tid, w, 1, G, key, pv, cxn, cx1);
        STAMPAT(14);
        BAR();
        if (w == 0) {
          const u32x2 ov = red_final(smem, l, sig, cxn, cx1);
          if (l < 8) __builtin_amdgcn_raw_buffer_store_b64(ov, rout, (krow * 1024 + G * 32 + l * 4) * 2, 0, 0);
        }
      }
#undef OUT_STORE
    }
    STAMPAT(15); STAMPAT(23);
  };
  layer(std::integral_constant<int, 0>{});
  layer(std::integral_constant<int, 1>{});
}

extern "C" void kernel_launch(void* const* d_in, const int* in_sizes, int n_in, void* d_out, int out_size,
                              void* d_ws, size_t ws_size, hipStream_t stream) {
  (void)hipFuncSetAttribute((const void*)mega_kernel, hipFuncAttributeMaxDynamicSharedMemorySize, LDS_BYTES);
  mega_kernel<<<256, 512, LDS_BYTES, stream>>>((const bf16*)d_in[0], (char*)d_ws, (const bf16*)d_in[1], (const bf16*)d_in[2], (const bf16*)d_in[4],
                                               (const bf16*)d_in[5], (const bf16*)d_in[6], (const bf16*)d_in[7], (bf16*)d_out);
}
```

```cpp
#include <hip/hip_runtime.h>
#include <stdint.h>
#include <utility>

typedef __bf16 bf16;
typedef __bf16 bf16x8 __attribute__((ext_vector_type(8)));
typedef __bf16 bf16x2 __attribute__((ext_vector_type(2)));
typedef float f32x4 __attribute__((ext_vector_type(4)));
typedef float f32x2 __attribute__((ext_vector_type(2)));
typedef short s16x4 __attribute__((ext_vector_type(4)));
typedef unsigned int u32x4 __attribute__((ext_vector_type(4)));
typedef unsigned int u32x2 __attribute__((ext_vector_type(2)));
#define LDS_AS __attribute__((address_space(3)))
#define GAS __attribute__((address_space(1)))
typedef __amdgpu_buffer_rsrc_t rsrc_t;

constexpr int F_ = 3584;
constexpr int SC1 = 16;

constexpr int RX = 4;
constexpr unsigned F_EP = 0, F_TMO = 128;
constexpr unsigned F_XN = 4096;
#define FLG 256, 1024, 8, 64, 4, 256, 4
constexpr unsigned FLV[7] = {FLG};
constexpr unsigned SO = FLV[0], SKV = FLV[1], SKW = FLV[2], SX1 = FLV[3], RX1 = FLV[4], SKEY = FLV[5], RKEY = FLV[6];
constexpr unsigned F_O = 8u << 20;
constexpr unsigned F_KV = 11u << 20;
constexpr unsigned F_WOI = F_O + 262144;
constexpr unsigned F_X1 = 9u << 20;
constexpr unsigned OFF_KEY = 10u << 20;
#define KREP() (RKEY == 16 ? bid & 15 : (bid & 7) * RKEY / 8)
#define KEY_AT(L, rep) (OFF_KEY + ((L) * RKEY + (rep)) * (SKEY < 8 ? 128 : 16 * SKEY))
constexpr unsigned OFF_AB = 2u << 20;
constexpr unsigned OFF_XNEW = 262144;
constexpr unsigned OFF_CS = OFF_AB + 65536;
constexpr unsigned OFF_FX = 393216;
constexpr unsigned OFF_CA = 524288;
constexpr unsigned OFF_STAT = 1u << 20;
constexpr unsigned OFF_PART = 3u << 20;
constexpr unsigned OFF_XN = 8u << 20;
constexpr unsigned OFF_O = 16u << 20;
constexpr unsigned OFF_KV = 32u << 20;
constexpr unsigned OFF_WI = 52u << 20;
constexpr unsigned OFF_WOI = 58u << 20;
constexpr unsigned WS_BYTES = 60u << 20;
constexpr unsigned POISON = 0xFFFFFFFFu;

constexpr int L_ABW = 16640, L_CSP = 17408, L_CSL = 31872, L_FXL = 32256, L_K = 0, L_V = 32768, L_RING = 65536, L_DN = 32768, L_RDY = 31744, L_QC = 31808, LDS_BYTES = 163840;
constexpr int STAGE = 24576;
#define SLOT(t) ((t) & 3)

template <typename F, int... I> __device__ __forceinline__ void static_for_impl(F&& f, std::integer_sequence<int, I...>) {
  (f(std::integral_constant<int, I>{}), ...);
}
template <int N, typename F> __device__ __forceinline__ void static_for(F&& f) { static_for_impl(f, std::make_integer_sequence<int, N>{}); }
__device__ __forceinline__ unsigned pack2(float a, float b) {
  f32x2 v = {a, b};
  return __builtin_bit_cast(unsigned, __builtin_convertvector(v, bf16x2));
}
__device__ __forceinline__ float asf(unsigned u) { return __builtin_bit_cast(float, u); }
__device__ __forceinline__ float lo16(unsigned u) { return __builtin_bit_cast(float, u << 16); }
__device__ __forceinline__ float hi16(unsigned u) { return __builtin_bit_cast(float, u & 0xffff0000u); }
__device__ __forceinline__ u32x4 pack8(const f32x4 a, const f32x4 b) {
  u32x4 o;
  o[0] = pack2(a[0], a[1]); o[1] = pack2(a[2], a[3]); o[2] = pack2(b[0], b[1]); o[3] = pack2(b[2], b[3]);
  return o;
}
__device__ __forceinline__ f32x4 mfma16(u32x4 a, u32x4 b, f32x4 c) {
  return __builtin_amdgcn_mfma_f32_16x16x32_bf16(__builtin_bit_cast(bf16x8, a), __builtin_bit_cast(bf16x8, b), c, 0, 0, 0);
}
__device__ __forceinline__ f32x4 mfma16k16(s16x4 a, s16x4 b, f32x4 c) { return __builtin_amdgcn_mfma_f32_16x16x16bf16_1k(a, b, c, 0, 0, 0); }
template <int CTRL> __device__ __forceinline__ float dppf(float v) {
  return __builtin_bit_cast(float, __builtin_amdgcn_update_dpp(0, __builtin_bit_cast(int, v), CTRL, 0xf, 0xf, false));
}
template <int CTRL> __device__ __forceinline__ unsigned dppu(unsigned v) {
  return (unsigned)__builtin_amdgcn_update_dpp(0, (int)v, CTRL, 0xf, 0xf, false);
}
__device__ __forceinline__ float add_x16(float v) {
  auto r = __builtin_amdgcn_permlane16_swap(__builtin_bit_cast(unsigned, v), __builtin_bit_cast(unsigned, v), false, false);
  return __builtin_bit_cast(float, (unsigned)r[0]) + __builtin_bit_cast(float, (unsigned)r[1]);
}
__device__ __forceinline__ float add_x32(float v) {
  auto r = __builtin_amdgcn_permlane32_swap(__builtin_bit_cast(unsigned, v), __builtin_bit_cast(unsigned, v), false, false);
  return __builtin_bit_cast(float, (unsigned)r[0]) + __builtin_bit_cast(float, (unsigned)r[1]);
}
__device__ __forceinline__ float max_x16(float v) {
  auto r = __builtin_amdgcn_permlane16_swap(__builtin_bit_cast(unsigned, v), __builtin_bit_cast(unsigned, v), false, false);
  return fmaxf(__builtin_bit_cast(float, (unsigned)r[0]), __builtin_bit_cast(float, (unsigned)r[1]));
}
__device__ __forceinline__ float max_x32(float v) {
  auto r = __builtin_amdgcn_permlane32_swap(__builtin_bit_cast(unsigned, v), __builtin_bit_cast(unsigned, v), false, false);
  return fmaxf(__builtin_bit_cast(float, (unsigned)r[0]), __builtin_bit_cast(float, (unsigned)r[1]));
}
__device__ __forceinline__ float sum_hi8(float v) {
  v += dppf<0x128>(v);
  v = add_x16(v);
  return add_x32(v);
}
__device__ __forceinline__ float sum64(float v) {
  v += dppf<0xB1>(v);
  v += dppf<0x4E>(v);
  v += dppf<0x124>(v);
  v += dppf<0x128>(v);
  v = add_x16(v);
  return add_x32(v);
}

#define BAR() asm volatile("s_waitcnt lgkmcnt(0)\n\ts_barrier" ::: "memory")
#define DRAIN() asm volatile("s_waitcnt vmcnt(0)" ::: "memory")
#define SB() __builtin_amdgcn_sched_barrier(0)
template <int N> __device__ __forceinline__ void wait_vm() { asm volatile("s_waitcnt vmcnt(%0)" ::"n"(N) : "memory"); }

__device__ __forceinline__ void dma16(rsrc_t rs, unsigned voff, unsigned soff, unsigned lds) {
  asm volatile("s_mov_b32 m0, %0\n\ts_nop 0\n\tbuffer_load_dwordx4 %1, %2, %3 offen lds" ::"s"(lds), "v"(voff), "s"(rs), "s"(soff) : "memory");
}
__device__ __forceinline__ void dma16n(rsrc_t rs, unsigned voff, unsigned soff, unsigned lds) {
  asm volatile("s_mov_b32 m0, %0\n\ts_nop 0\n\tbuffer_load_dwordx4 %1, %2, %3 offen nt lds" ::"s"(lds), "v"(voff), "s"(rs), "s"(soff) : "memory");
}
template <int OFF> __device__ __forceinline__ void dma16s(rsrc_t rs, unsigned voff, unsigned soff, unsigned lds) {
  asm volatile("s_mov_b32 m0, %0\n\ts_nop 0\n\tbuffer_load_dwordx4 %1, %2, %3 offen offset:%4 sc1 lds" ::"s"(lds), "v"(voff), "s"(rs), "s"(soff), "n"(OFF) : "memory");
}
__device__ __forceinline__ float rbf1(float x) { return __builtin_bit_cast(float, pack2(0.f, x)); }
__device__ __forceinline__ u32x4 ld16(rsrc_t rs, unsigned voff, unsigned soff) { return __builtin_amdgcn_raw_buffer_load_b128(rs, voff, soff, SC1); }
template <int OFF> __device__ __forceinline__ void ld16h(u32x4& d, rsrc_t rs, unsigned voff, unsigned soff) {
  asm volatile("buffer_load_dwordx4 %0, %1, %2, %3 offen offset:%4 sc1" : "=v"(d) : "v"(voff), "s"(rs), "s"(soff), "n"(OFF) : "memory");
}
template <int I, int OFF> __device__ __forceinline__ void ld16w(u32x4& d, rsrc_t rs, unsigned voff, unsigned soff) {
  if constexpr (I == 0) asm volatile("buffer_load_dwordx4 %0, %1, %2, %3 offen offset:%4 sc1" : "={v[128:131]}"(d) : "v"(voff), "s"(rs), "s"(soff), "n"(OFF) : "memory");
  else if constexpr (I == 1) asm volatile("buffer_load_dwordx4 %0, %1, %2, %3 offen offset:%4 sc1" : "={v[132:135]}"(d) : "v"(voff), "s"(rs), "s"(soff), "n"(OFF) : "memory");
  else if constexpr (I == 2) asm volatile("buffer_load_dwordx4 %0, %1, %2, %3 offen offset:%4 sc1" : "={v[136:139]}"(d) : "v"(voff), "s"(rs), "s"(soff), "n"(OFF) : "memory");
  else if constexpr (I == 3) asm volatile("buffer_load_dwordx4 %0, %1, %2, %3 offen offset:%4 sc1" : "={v[140:143]}"(d) : "v"(voff), "s"(rs), "s"(soff), "n"(OFF) : "memory");
  else if constexpr (I == 4) asm volatile("buffer_load_dwordx4 %0, %1, %2, %3 offen offset:%4 sc1" : "={v[144:147]}"(d) : "v"(voff), "s"(rs), "s"(soff), "n"(OFF) : "memory");
  else if constexpr (I == 5) asm volatile("buffer_load_dwordx4 %0, %1, %2, %3 offen offset:%4 sc1" : "={v[148:151]}"(d) : "v"(voff), "s"(rs), "s"(soff), "n"(OFF) : "memory");
  else if constexpr (I == 6) asm volatile("buffer_load_dwordx4 %0, %1, %2, %3 offen offset:%4 sc1" : "={v[152:155]}"(d) : "v"(voff), "s"(rs), "s"(soff), "n"(OFF) : "memory");
  else if constexpr (I == 7) asm volatile("buffer_load_dwordx4 %0, %1, %2, %3 offen offset:%4 sc1" : "={v[156:159]}"(d) : "v"(voff), "s"(rs), "s"(soff), "n"(OFF) : "memory");
  else if constexpr (I == 8) asm volatile("buffer_load_dwordx4 %0, %1, %2, %3 offen offset:%4 sc1" : "={v[160:163]}"(d) : "v"(voff), "s"(rs), "s"(soff), "n"(OFF) : "memory");
  else if constexpr (I == 9) asm volatile("buffer_load_dwordx4 %0, %1, %2, %3 offen offset:%4 sc1" : "={v[164:167]}"(d) : "v"(voff), "s"(rs), "s"(soff), "n"(OFF) : "memory");
  else if constexpr (I == 10) asm volatile("buffer_load_dwordx4 %0, %1, %2, %3 offen offset:%4 sc1" : "={v[168:171]}"(d) : "v"(voff), "s"(rs), "s"(soff), "n"(OFF) : "memory");
  else if constexpr (I == 11) asm volatile("buffer_load_dwordx4 %0, %1, %2, %3 offen offset:%4 sc1" : "={v[172:175]}"(d) : "v"(voff), "s"(rs), "s"(soff), "n"(OFF) : "memory");
}
template <int K> __device__ __forceinline__ void pin_w(u32x4& a, u32x4& b) {
  if constexpr (K == 0) asm volatile("" : "+{v[128:131]}"(a), "+{v[132:135]}"(b)::"memory");
  else if constexpr (K == 1) asm volatile("" : "+{v[136:139]}"(a), "+{v[140:143]}"(b)::"memory");
  else if constexpr (K == 2) asm volatile("" : "+{v[144:147]}"(a), "+{v[148:151]}"(b)::"memory");
  else if constexpr (K == 3) asm volatile("" : "+{v[152:155]}"(a), "+{v[156:159]}"(b)::"memory");
  else if constexpr (K == 4) asm volatile("" : "+{v[160:163]}"(a), "+{v[164:167]}"(b)::"memory");
  else if constexpr (K == 5) asm volatile("" : "+{v[168:171]}"(a), "+{v[172:175]}"(b)::"memory");
}
__device__ __forceinline__ void ld8h(u32x2& d, rsrc_t rs, unsigned voff, unsigned soff) {
  asm volatile("buffer_load_dwordx2 %0, %1, %2, %3 offen sc1" : "=v"(d) : "v"(voff), "s"(rs), "s"(soff) : "memory");
}
template <int OFF> __device__ __forceinline__ void ld4h(unsigned& d, rsrc_t rs, unsigned voff, unsigned soff) {
  asm volatile("buffer_load_dword %0, %1, %2, %3 offen offset:%4 sc1" : "=v"(d) : "v"(voff), "s"(rs), "s"(soff), "n"(OFF) : "memory");
}
__device__ __forceinline__ u32x2 ld8(rsrc_t rs, unsigned voff, unsigned soff) { return __builtin_amdgcn_raw_buffer_load_b64(rs, voff, soff, SC1); }
__device__ __forceinline__ void st16(u32x4 v, rsrc_t rs, unsigned voff, unsigned soff) { __builtin_amdgcn_raw_buffer_store_b128(v, rs, voff + soff, 0, SC1); }
__device__ __forceinline__ void st8(u32x2 v, rsrc_t rs, unsigned voff, unsigned soff) { __builtin_amdgcn_raw_buffer_store_b64(v, rs, voff, soff, SC1); }

__device__ __forceinline__ unsigned ld4(rsrc_t rs, unsigned voff, unsigned soff) { return __builtin_amdgcn_raw_buffer_load_b32(rs, voff, soff, SC1); }
__device__ __forceinline__ void st4(unsigned v, rsrc_t rs, unsigned voff, unsigned soff) { __builtin_amdgcn_raw_buffer_store_b32(v, rs, voff, soff, SC1); }

constexpr unsigned kSpinCap = 1u << 16;
__device__ __forceinline__ bool spin_fail(unsigned& spins, rsrc_t rs, u32x2 T) {
  asm volatile("" ::: "memory");
  ++spins;
  if (spins > kSpinCap) { st8(T, rs, 0, F_TMO); return true; }
  if ((spins & 255u) == 0u) {
    const u32x2 v = ld8(rs, 0, F_TMO);
    if (v[0] == T[0] && v[1] == T[1]) return true;
  }
  __builtin_amdgcn_s_sleep(3);
  return false;
}
__device__ __forceinline__ void poll_flags2(rsrc_t rs, unsigned voff, u32x2 T) {
  for (unsigned spins = 0;;) {
    const u32x4 v = ld16(rs, voff, 0);
    if (__all(v[0] == T[0] && v[1] == T[1] && v[2] == T[0] && v[3] == T[1])) break;
    if (spin_fail(spins, rs, T)) break;
  }
  asm volatile("" ::: "memory");
  __builtin_amdgcn_fence(__ATOMIC_ACQUIRE, "wavefront");
}
__device__ __forceinline__ void tag_wait(rsrc_t rs, u32x2 T, unsigned voff, unsigned soff, u32x4& v) {
  if (__builtin_expect(!__all(v[2] == T[0] && v[3] == T[1]), 0)) {
    unsigned spins = 0;
    do {
      if (spin_fail(spins, rs, T)) break;
      v = ld16(rs, voff, soff);
    } while (!__all(v[2] == T[0] && v[3] == T[1]));
    asm volatile("s_waitcnt vmcnt(0)" : "+v"(v)::"memory");
  }
}
template <int N> __device__ __forceinline__ void word_wait(rsrc_t rs, u32x2 T, unsigned voff, unsigned soff, u32x4 (&v)[4]) {
#define WW_OK() __all(max(max(max(v[0][0], v[0][1]), max(v[0][2], v[0][3])), max(max(v[N - 1][0], v[N - 1][1]), max(v[N - 1][2], v[N - 1][3]))) != POISON)
  if (__builtin_expect(!WW_OK(), 0)) {
    unsigned spins = 0;
    do {
      if (spin_fail(spins, rs, T)) break;
#pragma unroll
      for (int i = 0; i < N; ++i) v[i] = ld16(rs, voff + i * 2048, soff);
    } while (!WW_OK());
    asm volatile("s_waitcnt vmcnt(0)" : "+v"(v[0]), "+v"(v[N - 1])::"memory");
  }
#undef WW_OK
}
__device__ __forceinline__ void poll_flags(rsrc_t rs, unsigned voff, unsigned soff, u32x2 T) {
  for (unsigned spins = 0;;) {
    const u32x2 v = ld8(rs, voff, soff);
    if (__all(v[0] == T[0] && v[1] == T[1])) break;
    if (spin_fail(spins, rs, T)) break;
  }
  asm volatile("" ::: "memory");
  __builtin_amdgcn_fence(__ATOMIC_ACQUIRE, "wavefront");
}

__device__ __forceinline__ void unpack8v(const u32x4 u, f32x2* v) {
#pragma unroll
  for (int j = 0; j < 4; ++j) v[j] = f32x2{lo16(u[j]), hi16(u[j])};
}
__device__ __forceinline__ u32x4 pack8v(const f32x2* v) {
  u32x4 o;
#pragma unroll
  for (int j = 0; j < 4; ++j) o[j] = pack2(v[j][0], v[j][1]);
  return o;
}
__device__ __forceinline__ float ln_rd(float var) { return __builtin_amdgcn_rcpf(rbf1(__builtin_amdgcn_sqrtf(rbf1(rbf1(var) + 1e-5f)))); }
__device__ __forceinline__ float hadd(f32x2 v) {
  float a = v[0];
  asm volatile("" : "+v"(a));
  return a + v[1];
}
__device__ __forceinline__ float add_g(float v) { return add_x32(add_x16(v)); }
__device__ __forceinline__ void add_g3(float& x, float& y, float& z) {
  auto a = __builtin_amdgcn_permlane16_swap(__builtin_bit_cast(unsigned, x), __builtin_bit_cast(unsigned, y), false, false);
  auto b = __builtin_amdgcn_permlane16_swap(__builtin_bit_cast(unsigned, z), __builtin_bit_cast(unsigned, z), false, false);
  const float r = asf(a[0]) + asf(a[1]), t = asf(b[0]) + asf(b[1]);
  auto c = __builtin_amdgcn_permlane32_swap(__builtin_bit_cast(unsigned, r), __builtin_bit_cast(unsigned, r), false, false);
  auto d = __builtin_amdgcn_permlane32_swap(__builtin_bit_cast(unsigned, t), __builtin_bit_cast(unsigned, t), false, false);
  const float u = asf(c[0]) + asf(c[1]);
  z = asf(d[0]) + asf(d[1]);
  auto e = __builtin_amdgcn_permlane16_swap(__builtin_bit_cast(unsigned, u), __builtin_bit_cast(unsigned, u), false, false);
  x = asf(e[0]); y = asf(e[1]);
}
__device__ __forceinline__ u32x4 trade16(u32x4 v) {
  auto a = __builtin_amdgcn_permlane16_swap(v[0], v[2], false, false);
  auto b = __builtin_amdgcn_permlane16_swap(v[1], v[3], false, false);
  return u32x4{(unsigned)a[0], (unsigned)b[0], (unsigned)a[1], (unsigned)b[1]};
}
__device__ __forceinline__ unsigned umax4(u32x4 v) { return max(max(v[0], v[1]), max(v[2], v[3])); }
__device__ __forceinline__ u32x2 unpoison2(u32x2 v) { return u32x2{min(v[0], POISON - 1), min(v[1], POISON - 1)}; }
__device__ __forceinline__ u32x4 unpoison4(u32x4 v) {
  return u32x4{min(v[0], POISON - 1), min(v[1], POISON - 1), min(v[2], POISON - 1), min(v[3], POISON - 1)};
}

typedef union { struct { s16x4 a, b; } hf; u32x4 v; } frag_t;
__device__ __forceinline__ s16x4 trread(const char* smem, unsigned off) {
  return __builtin_amdgcn_ds_read_tr16_b64_v4i16((LDS_AS s16x4*)(smem + off));
}
__device__ __forceinline__ s16x4 trabs(unsigned addr) { return __builtin_amdgcn_ds_read_tr16_b64_v4i16((LDS_AS s16x4*)(size_t)addr); }

constexpr int WA = 6;
#define WOP 4, 3, 6, 8, 1, 4, 0
constexpr int WOV[7] = {WOP};
constexpr int WLA = WOV[4], WPR = WOV[5], QPR = WOV[6];
constexpr int WB = WOV[0], WR = WOV[1], WIS = WOV[2], WPS = WOV[3];
#define CH 0, 6, 8, 9, 15, 18, 23, 25, 29, 31, 4, 4
constexpr int CHV[12] = {CH};
constexpr int FKL = CHV[10], FSN = CHV[11];
constexpr int SPL = CHV[0];
constexpr int FSL = CHV[1], FS0 = CHV[2], FS0D = CHV[3], FS1 = CHV[4], FS1S = CHV[5];
constexpr int FXA = CHV[6], FXB = CHV[7], FXL = CHV[8], FXS = CHV[9];
#define ROT0 p
#define L0S 22, 25, 8, 0, 24, 28, 1, 2, 4, 14
constexpr int L0V[10] = {L0S};
constexpr int CSL = L0V[0], CSS = L0V[1], CSD = L0V[2];
constexpr int abl(int it) { return it ? 25 : L0V[4]; }
constexpr int abs_(int it) { return it ? 27 : L0V[5]; }
constexpr int W2S = L0V[6], WSH = L0V[7];
constexpr int PRE0 = 12;
constexpr int LNL = L0V[3], LNS = L0V[8], POI = L0V[9];
#define TCH 22, 3, 0x07, 26, 0xF8, 0, 1
constexpr int TCV[7] = {TCH};
constexpr int TS0 = TCV[0], TSN = TCV[1], TM0 = TCV[2], TEX = TCV[3], TM1 = TCV[4], TSD = TCV[5], TSG = TCV[6];
constexpr int QL = 26;
#define SLP 18, 4, 28, 3, 0, 0
constexpr int SLV[6] = {SLP};
constexpr int SDIR = SLV[0], GSL = SLV[1], RSL = SLV[2], KSL = SLV[3];
constexpr int tp0(int it) { return it ? 3 : 3 + WSH; }
constexpr bool tile_piece(int it, int s) { return s >= tp0(it) && ((s - tp0(it)) >> 2) + 3 < 8 && ((s - tp0(it)) & 3) < 3; }
#define NPRE 3
#define QAD 4
#define WO2 1, 5, 4, 2, 8
constexpr int W2V[5] = {WO2};
constexpr int WOS = 12;
static_assert(WOS + QAD < 18, "covered by the operand waits in front of the barrier of step 18");
constexpr int WON = W2V[0], W2B = W2V[1], W2R = W2V[2], W2L = W2V[3], W2P = W2V[4];
constexpr int a_req(int s, int n) {
  int r = 2 * NPRE;
  for (int u = 1; u < 192; u += 2) {
    const bool go = r < 64 && (r >> 1) <= u / 6 + QAD && (r < 8 || u >= 9);
    if (u == 6 * s + n) return go ? r : -1;
    r += go;
  }
  return -1;
}
constexpr int vm_n(int it, int s, int n) {
  int c = it ? (a_req(s, n) >= 0) + (s == 0 && n == 0) : (s + 5 < 32 && (n == 2 || n == 3));
  if (n == 0) c += tile_piece(it, s);
  if (n == 4) c += (s == abl(it));
  if (!it) c += (s >= W2S && s < W2S + 3 && n == 4) + (s == LNL && n == 5 ? 3 : 0) + (s == 0 && n < 2 ? 2 : 0) + (s == LNS + 2 && n == 5) + (s == TEX && n == 5);
  else c += (s == 2 && n == 2) + (s == (SPL < 0 ? 0 : SPL) && n == (SPL < 0 ? 1 : 4) ? 4 : 0) + (s >= 27 && s < 30 && (n == 0 || n == 3)) + (WON && s == WOS && n == 4);
  return c;
}
constexpr int vm_at(int it, int s, int n) {
  int c = it ? 2 * NPRE : PRE0;
  for (int u = 0; u < 6 * s + n; ++u) c += vm_n(it, u / 6, u % 6);
  return c;
}
constexpr int vm_a(int it, int k) {
  if (k < NPRE) return 2 * k + 2;
  for (int u = 0; u < 192; ++u)
    if (a_req(u / 6, u % 6) == 2 * k + 1) return vm_at(it, (u + 1) / 6, (u + 1) % 6);
  return 0;
}
constexpr int vm_tile(int it, int t) { return t >= 3 ? vm_at(it, 4 * t - 10 + tp0(it), 1) : t == 1 ? 12 : vm_at(0, W2S + 2, 5); }
constexpr int vm_unit(int u) { return u < 3 ? 5 + 2 * u : u < 5 ? vm_at(0, 0, u - 2) : vm_at(0, u - 5, 4); }
constexpr int unit_slot(int u) { return u % 3 == 0 ? 0 : 16384 + (u % 3) * 16384; }

constexpr int L_XC = LDS_BYTES - 64, L_SKEY = 32704, L_ROW = 1024, L_OUT = 65536, L_SRED = 4096, L_SFIN = 20480;
__device__ __forceinline__ unsigned key_load(rsrc_t rs, int l, int L, int rep) { return ld4(rs, (l & 15) * SKEY, KEY_AT(L, rep)); }
__device__ __forceinline__ unsigned umax16(unsigned k) {
  k = max(k, dppu<0xB1>(k));
  k = max(k, dppu<0x4E>(k));
  k = max(k, dppu<0x141>(k));
  return max(k, dppu<0x140>(k));
}
__device__ __forceinline__ unsigned key_row(unsigned key) { return (0xffffu - (key & 0xffffu)) & 2047u; }
__device__ __forceinline__ unsigned key_wait(rsrc_t rs, u32x2 T, int l, int L, int rep, unsigned kv) {
  if (!__all(kv != POISON)) {
    unsigned spins = 0;
    do {
      if (spin_fail(spins, rs, T)) break;
      if (L == 0) __builtin_amdgcn_s_sleep(KSL);
      kv = key_load(rs, l, L, rep);
    } while (!__all(kv != POISON));
  }
  return __builtin_amdgcn_readfirstlane(umax16(kv));
}
__device__ __forceinline__ unsigned cand_off(unsigned key, int L) { return OFF_CA + L * 65536 + (key_row(key) >> 7) * 4096; }
#define XO(i) (((i) >> 1) * 256 + ((i) & 1) * 64)
__device__ __forceinline__ void ffn_gu(const u32x4 (&gv)[4], const u32x4 (&uv)[4], const u32x4 (&xv)[4], f32x4& ga, f32x4& ua) {
  ga = mfma16(gv[0], xv[0], f32x4{0.f, 0.f, 0.f, 0.f}); ua = mfma16(uv[0], xv[0], f32x4{0.f, 0.f, 0.f, 0.f});
#pragma unroll
  for (int i = 1; i < 4; ++i) { ga = mfma16(gv[i], xv[i], ga); ua = mfma16(uv[i], xv[i], ua); }
}
__device__ __forceinline__ void ffn_gu_store(char* smem, int l, int w, f32x4 ga, f32x4 ua) {
  LDS_AS f32x4* sred = (LDS_AS f32x4*)(smem + L_SRED);
  if ((l & 15) == 0) { sred[w * 8 + (l >> 4)] = ga; sred[w * 8 + 4 + (l >> 4)] = ua; }
}
__device__ __forceinline__ f32x4 ffn_d_read(char* smem, int l) {
  const LDS_AS float* t = (const LDS_AS float*)(smem + L_SRED) + (l & 8) * 16 + (l & 4) * 4 + (l >> 4) * 4 + (l & 3);
  return f32x4{t[0], t[32], t[64], t[96]};
}
__device__ __forceinline__ u32x2 ffn_d_h(f32x4 t) {
  float x = t[0] + t[1];
  asm volatile("" : "+v"(x));
  x += t[2] + t[3];
  x += dppf<0x128>(x);
  const float y = dppf<0x124>(x);
  const bool up = __builtin_amdgcn_inverse_ballot_w64(0xF0F0F0F0F0F0F0F0ull);
  const float gg = rbf1(up ? y : x), uu = rbf1(up ? x : y);
  const float h = rbf1(gg * __builtin_amdgcn_rcpf(1.0f + __expf(-gg))) * uu;
  const unsigned p = pack2(h, dppf<0xB1>(h));
  return u32x2{dppu<0x00>(p), dppu<0xAA>(p)};
}
template <int t> __device__ __forceinline__ f32x4 ffn_d_mma(s16x4 d, u32x2 hh, f32x4 y) {
  const bool me = __builtin_amdgcn_inverse_ballot_w64(0x0001000100010001ull << t);
  return mfma16k16(d, __builtin_bit_cast(s16x4, u32x2{me ? hh[0] : 0u, me ? hh[1] : 0u}), y);
}
__device__ __forceinline__ f32x4 ffn_d_all(const s16x4 (&dfr)[8], u32x2 hh) {
  f32x4 a = {0.f, 0.f, 0.f, 0.f}, b = a;
  a = ffn_d_mma<0>(dfr[0], hh, a); b = ffn_d_mma<1>(dfr[1], hh, b); a = ffn_d_mma<2>(dfr[2], hh, a); b = ffn_d_mma<3>(dfr[3], hh, b);
  a = ffn_d_mma<4>(dfr[4], hh, a); b = ffn_d_mma<5>(dfr[5], hh, b); a = ffn_d_mma<6>(dfr[6], hh, a); b = ffn_d_mma<7>(dfr[7], hh, b);
  return a + b;
}
__device__ __forceinline__ void ffn_d_store(rsrc_t rs, int l, int w, int L, int slice, f32x4 y) {
  const unsigned c = l & 15;
  if (c < 8) st16(unpoison4(__builtin_bit_cast(u32x4, y)), rs, (2 * w + (c >> 2)) * 65536 + slice * 256 + (c & 3) * 64 + (l >> 4) * 16, OFF_PART + L * (1u << 20));
}
#define RED_VO(k) ((min(tid + 512 * (k), 1791) >> 3) * 256 + (tid & 7) * 16)
#define RED_SO(L, G) (OFF_PART + (L) * (1u << 20) + ((G) >> 1) * 65536 + ((G) & 1) * 128)
#define RED_JJ() ((tid & 3) * 16 + (tid & 4) * 2)
__device__ __forceinline__ void red_load(rsrc_t rs, int tid, int L, unsigned G, unsigned key, u32x4 (&pv)[4], u32x2& cxn, u32x2& cx1) {
#pragma unroll
  for (int k = 0; k < 4; ++k) pv[k] = ld16(rs, RED_VO(k), RED_SO(L, G));
  cxn = ld8(rs, RED_JJ(), cand_off(key, L) + XO(G));
  cx1 = ld8(rs, RED_JJ() + 128, cand_off(key, L) + XO(G));
}
__device__ __forceinline__ void red_sum(rsrc_t rs, char* smem, u32x2 T, int tid, int w, int L, unsigned G, unsigned key,
                                        u32x4 (&pv)[4], u32x2& cxn, u32x2& cx1) {
#define PV_OK() __all(max(max(max(umax4(pv[0]), umax4(pv[1])), max(umax4(pv[2]), umax4(pv[3]))), max(max(cxn[0], cxn[1]), max(cx1[0], cx1[1]))) != POISON)
  if (!PV_OK()) {
    unsigned spins = 0;
    do {
      if (spin_fail(spins, rs, T)) break;
#pragma unroll
      for (int k = 0; k < 4; ++k)
        if (umax4(pv[k]) == POISON) pv[k] = ld16(rs, RED_VO(k), RED_SO(L, G));
      if (max(max(cxn[0], cxn[1]), max(cx1[0], cx1[1])) == POISON) { cxn = ld8(rs, RED_JJ(), cand_off(key, L) + XO(G)); cx1 = ld8(rs, RED_JJ() + 128, cand_off(key, L) + XO(G)); }
    } while (!PV_OK());
  }
#undef PV_OK
  f32x4 s = (__builtin_bit_cast(f32x4, pv[0]) + __builtin_bit_cast(f32x4, pv[1])) + __builtin_bit_cast(f32x4, pv[2]);
  if (w < 4) s += __builtin_bit_cast(f32x4, pv[3]);
#pragma unroll
  for (int j = 0; j < 4; ++j) s[j] = sum_hi8(s[j]);
  if ((tid & 63) < 8) ((LDS_AS f32x4*)(smem + L_SFIN))[w * 8 + (tid & 7)] = s;
}
__device__ __forceinline__ float red_gate(unsigned key) {
  const unsigned k16 = key >> 16;
  const float rmax = __builtin_bit_cast(float, ((k16 & 0x8000u) ? (k16 & 0x7fffu) : (~k16 & 0xffffu)) << 16);
  float sig = rbf1(__builtin_amdgcn_rcpf(1.0f + __expf(-rmax)));
  asm volatile("" : "+v"(sig));
  return sig;
}
__device__ __forceinline__ u32x2 red_final(char* smem, int l, float sig, u32x2 cxn, u32x2 cx1) {
  f32x4 s = ((const LDS_AS f32x4*)(smem + L_SFIN))[l];
#pragma unroll
  for (int j = 0; j < 4; ++j) s[j] = sum_hi8(s[j]);
  const float xn4[4] = {lo16(cxn[0]), hi16(cxn[0]), lo16(cxn[1]), hi16(cxn[1])};
  const float x14[4] = {lo16(cx1[0]), hi16(cx1[0]), lo16(cx1[1]), hi16(cx1[1])};
  float o[4];
#pragma unroll
  for (int j = 0; j < 4; ++j) o[j] = x14[j] + rbf1(xn4[j] + rbf1(rbf1(s[j]) * sig));
  return u32x2{pack2(o[0], o[1]), pack2(o[2], o[3])};
}

struct RowFin { float mean, var, mb, rd; unsigned key; };
__device__ __forceinline__ void fin_wait(rsrc_t rs, u32x2 T, unsigned voff, unsigned so, u32x4 (&sp)[4]) {
#define ST_OK() __all(max(max(umax4(sp[0]), umax4(sp[1])), max(umax4(sp[2]), umax4(sp[3]))) != POISON)
  if (!ST_OK()) {
    unsigned spins = 0;
    do {
      if (spin_fail(spins, rs, T)) break;
#pragma unroll
      for (int i = 0; i < 4; ++i) sp[i] = ld16(rs, voff + i * 2048, so);
    } while (!ST_OK());
  }
#undef ST_OK
}
__device__ __forceinline__ RowFin fin_key(rsrc_t rs, u32x2 T, unsigned voff, unsigned so, char* skey, int l, int w, unsigned R, float wsum, u32x4 (&sp)[4]) {
  RowFin r;
#define ST_SUM() ((__builtin_bit_cast(f32x4, sp[0]) + __builtin_bit_cast(f32x4, sp[1])) + (__builtin_bit_cast(f32x4, sp[2]) + __builtin_bit_cast(f32x4, sp[3])))
  f32x4 sf = ST_SUM();
  const float z = (sf[0] + sf[1]) + sf[2];
  if (__any(z != z)) {
    fin_wait(rs, T, voff, so, sp);
    sf = ST_SUM();
  }
#undef ST_SUM
  float s0 = sf[0], s1 = sf[1], s2 = sf[2];
  add_g3(s0, s1, s2);
  r.mean = s0 * (1.0f / 1024.0f);
  r.var = fmaxf(__builtin_fmaf(-r.mean, r.mean, s1 * (1.0f / 1024.0f)), 0.f);
  r.mb = rbf1(r.mean); r.rd = ln_rd(r.var);
  const unsigned rb16 = pack2((s2 - r.mb * wsum) * r.rd, 0.f) & 0xffffu;
  r.key = min((((rb16 & 0x8000u) ? (~rb16 & 0xffffu) : (rb16 | 0x8000u)) << 16) | (0xffffu - R), POISON - 1);
  const unsigned wk = umax16(r.key);
  if (l == 0) ((LDS_AS unsigned*)skey)[w] = wk;
  return r;
}
__device__ __forceinline__ void fin_cand(rsrc_t rs, char* skey, int L, int g, int rb, int ct, const RowFin& r, const f32x2 (&v)[8]) {
  const u32x4 k0 = *(const LDS_AS u32x4*)skey, k1 = *(const LDS_AS u32x4*)(skey + 16);
  const unsigned best = max(umax4(k0), umax4(k1));
  if (r.key == best) {
    const unsigned co = L * 65536 + rb * 4096 + ct * 256 + g * 16;
    f32x2 t[8], rd2 = {r.rd, r.rd}, of2 = {-r.mb * r.rd, -r.mb * r.rd};
    asm volatile("" : "+v"(rd2), "+v"(of2));
#pragma unroll
    for (int j = 0; j < 8; ++j) t[j] = __builtin_elementwise_fma(v[j], rd2, of2);
    st16(unpoison4(pack8v(t)), rs, co, OFF_CA); st16(unpoison4(pack8v(t + 4)), rs, co + 64, OFF_CA);
    if (g == 0 && ct < (int)RKEY) st4(best, rs, KEY_AT(L, ct) + rb * SKEY, 0);
    st16(unpoison4(pack8v(v)), rs, co + 128, OFF_CA); st16(unpoison4(pack8v(v + 4)), rs, co + 192, OFF_CA);
  }
}
__device__ __forceinline__ void fin_x2(const RowFin& r, f32x2 (&v)[8]) {
  f32x2 sc2 = {1.0f + r.rd, 1.0f + r.rd}, of2 = {-r.mb * r.rd, -r.mb * r.rd};
  asm volatile("" : "+v"(sc2), "+v"(of2));
#pragma unroll
  for (int j = 0; j < 8; ++j) v[j] = __builtin_elementwise_fma(v[j], sc2, of2);
}

extern "C" __device__ unsigned long long dispatch_id() __asm("llvm.amdgcn.dispatch.id");

extern "C" __global__ __launch_bounds__(512, 2) void mega_kernel(const bf16* x, char* ws, const bf16* wqkv, const bf16* wo, const bf16* wr,
                                                                 const bf16* wg, const bf16* wu, const bf16* wd, bf16* out) {
  extern __shared__ __attribute__((aligned(16))) char smem[];
  const int w0_ = __builtin_amdgcn_readfirstlane(threadIdx.x >> 6);
  const unsigned lbase = (unsigned)(size_t)(LDS_AS char*)smem;
  {
    unsigned long long pc;
    unsigned len;
    asm volatile("s_getpc_b64 %0\n\ts_mov_b32 %1, .Ltext_end-mega_kernel\n\t.subsection 1\n.Ltext_end:\n\t.subsection 0" : "=s"(pc), "=s"(len));
    const unsigned o = ((blockIdx.x >> 3) * 64 + (threadIdx.x & 63)) * 128;
    if (w0_ == 7 && o < len)
      asm volatile("s_mov_b32 m0, %0\n\ts_nop 0\n\tglobal_load_lds_dword %1, %2" ::"s"(lbase + L_FXL), "v"(o), "s"(pc & ~127ull) : "memory");
  }
  asm volatile("" : "+s"(x), "+s"(ws), "+s"(wqkv));
#define IDS()                                                                                         \
  int tid = threadIdx.x, w = w0_, bid = blockIdx.x;                                                   \
  asm volatile("" : "+v"(tid));                                                                       \
  asm volatile("" : "+s"(w));                                                                         \
  asm volatile("" : "+s"(bid));                                                                       \
  const int l = tid & 63, xcd = bid & 7, bi = bid >> 3;                                               \
  const int p = (xcd & 1) * 4 + (bi & 3), h = (xcd >> 1) * 8 + (bi >> 2);         \
  const int rb = 2 * p + ((h >> 3) & 1), ct = (h >> 4) * 8 + (h & 7);       \
  const int slice = xcd * 28 + bi;                                                   \
  const bool ffn_on = bi < 28;                                                                        \
  const int g = l >> 4;                                                                               \
  const int rt = w < 4 ? w : 11 - w;          \
  KARGS();                                                                                            \
  (void)p; (void)h; (void)rb; (void)ct; (void)slice; (void)ffn_on; (void)g; (void)rt;
  const rsrc_t rs = __builtin_amdgcn_make_buffer_rsrc((void*)ws, 0, WS_BYTES, 0x00020000);
  typedef const void* const __attribute__((address_space(4)))* karg_t;
#define KARGS() karg_t ka = (karg_t)__builtin_amdgcn_kernarg_segment_ptr(); asm volatile("" : "+s"(ka))
#define ARGP(name, i) const GAS bf16* name = (const GAS bf16*)ka[i]; asm volatile("" : "+s"(name))
#define RSRC(ptr, bytes) __builtin_amdgcn_make_buffer_rsrc((void*)(ptr), 0, bytes, 0x00020000)
#define rwq RSRC(pwq, 1024 * 3072 * 2)
#define rwo RSRC(pwo, 1024 * 1024 * 2)
#define rwg RSRC(pwg, 1024 * F_ * 2)
#define rwu RSRC(pwu, 1024 * F_ * 2)
#define rwd RSRC(pwd, 1024 * F_ * 2)
#define rout RSRC(pout, 2048 * 1024 * 2)

#define WQ_VOFF() ((l >> 2) * 6144 + (((l & 3) ^ ((((l >> 4) ^ (l >> 5)) & 1) << 1)) << 4))
#define WQ_PIECE(t, j, dst) dma16(rwq, wqv, ((((t) + rot) & 7) * 128 + ((j) & 7) * 16) * 6144 + ((j) >> 3) * 2048 + h * 64, lbase + (dst) + (j) * 1024)
#define WQ_BOFF(dt) ((8 * g + ((l & 15) >> 2)) * 64 + (l & 3) * 8 + (((dt) ^ (g & 1)) << 5))
#define WQ_POFF() ((4 * g + ((l & 15) >> 2)) * 64 + (l & 3) * 8 + (((g ^ (g >> 1)) & 1) << 5))
#define X_UNIT1(u, j) dma16(rx, xv_, (unsigned)(p * 256 + rt * 32 + ((u) & 1) * 16 + (j) * 8) * 2048 + ((((u) >> 2) + rot) & 7) * 256 + (((u) >> 1) & 1) * 128, lbase + rt * 2048 + unit_slot(u) + (j) * 1024)
#define X_VOFF() ((l >> 3) * 2048 + (((l & 7) ^ ((l >> 3) & 6)) << 4))
#define X_ROFF(hs) (lbase + rt * 2048 + (l & 15) * 128 + (((4 * (hs) + g) ^ (l & 6)) << 4))
#define TOUCH_TO(ptr, dst) asm volatile("s_mov_b32 m0, %0\n\ts_nop 0\n\tglobal_load_lds_dword %1, off" ::"s"(lbase + (dst)), "v"(ptr) : "memory")
#define TOUCH(ptr) TOUCH_TO(ptr, L_FXL)
#define WO_PIECE_W(j, w_)                                                                                                                 \
  {                                                                                                                                       \
    if constexpr (it == 1 && WON) dma16s<0>(rs, l * 16, OFF_WOI + ct * 131072 + (j) * 8192 + (w_) * 1024, lbase + 32768 + (j) * 8192 + (w_) * 1024); \
    else dma16(rwo, wov, (((w_) >> 2) * 64 + ((w_) & 3) + 4 * (j)) * 16384 + ct * 128, lbase + 32768 + (j) * 8192 + (w_) * 1024);           \
  }
#define WO_PIECE(j) WO_PIECE_W(j, w)
#define WO_VOFF() ((l >> 3) * 2048 + (((((l & 7) >> 1) ^ ((l >> 4) & 3)) * 2 + (l & 1)) << 4))
#define WD_DMA() _Pragma("unroll") for (int i = 0; i < 4; ++i) WD_DMA1(i)
#define WD_DMA1(i)                                                                                   \
      dma16n(rwd, (l >> 4) * 2048 + (((l & 15) * 16) ^ ((l >> 4) * 32) ^ (((i) & 1) * 128)), (unsigned)(slice * 16 + 4 * (i)) * 2048 + w * 256, lbase + L_DN + w * 4096 + (i) * 1024)
#define FFN_DMA(m) dma16((m) & 1 ? rwu : rwg, fnv, (unsigned)(((4 * w + ((m) >> 1)) * 32) * F_ + slice * 16) * 2, lbase + 65536 + (m) * 8192 + w * 1024)
  u32x4 gv[4], uv[4];
  s16x4 dfr[8];
  u32x4 keep[9];
  u32x4 pre[4][2];
#define PRE_LOAD(it_, rot_, s0, s1)                                                                  \
  static_for<(s1) - (s0)>([&](auto c_) __attribute__((always_inline)) {                              \
    constexpr int s = (s0) + decltype(c_)::value;                                                    \
    const unsigned ab_ = OFF_XN + (it_) * (4u << 20) + (((rot_) + (s >> 2)) & 7) * 8192 + ((s >> 1) & 1) * 4096; \
    ld16w<2 * s, (s & 1) * 2048>(pre[s][0], rs, l * 16, ab_ + (p * 8 + rt) * 65536);                         \
    ld16w<2 * s + 1, (s & 1) * 2048 + 1024>(pre[s][1], rs, l * 16, ab_ + (p * 8 + rt) * 65536);                 \
  })
  u32x2 T;
  u32x2 ep;
  u32x4 xl[2];
#define LN_LOAD()                                                                                                                                   \
  {                                                                                                                                                 \
    asm volatile("buffer_load_dwordx4 %0, %1, %2, %3 offen" : "=v"(xl[0]) : "v"(l * 16), "s"(rx), "s"((p * 256 + h * 8 + w) * 2048) : "memory");      \
    asm volatile("buffer_load_dwordx4 %0, %1, %2, %3 offen offset:1024" : "=v"(xl[1]) : "v"(l * 16), "s"(rx), "s"((p * 256 + h * 8 + w) * 2048) : "memory"); \
    asm volatile("buffer_load_dwordx2 %0, off, %1, 0 sc1" : "=v"(ep) : "s"(rs) : "memory");                                                         \
  }
  const GAS bf16* tsrc;
  {
    IDS();
    const int it = 0, rot = ROT0;
    (void)it;
    const bf16* pwq = wqkv;
    const rsrc_t rx = RSRC(x, 2048 * 1024 * 2);
    const unsigned wqv = WQ_VOFF(), xv_ = X_VOFF();
    if (tid == 0) *(LDS_AS u32x4*)(smem + L_QC) = u32x4{0u, 0u, 0u, 0u};
    if (w == 0 && l < 24) TOUCH((const GAS bf16*)wqkv + (bid * 24 + l) * 512 + ((3 - (unsigned)((size_t)wqkv >> 7)) & 7) * 64);
#pragma unroll
    for (int i = 0; i < 3; ++i) WQ_PIECE(0, 3 * w + i, L_RING);
    X_UNIT1(0, 0); X_UNIT1(0, 1); X_UNIT1(1, 0); X_UNIT1(1, 1);
    BAR();
    X_UNIT1(2, 0); X_UNIT1(2, 1);
#pragma unroll
    for (int i = 0; i < 3; ++i) WQ_PIECE(1, 3 * w + i, L_RING + STAGE);
  }
  unsigned sel0 = 0x7FFFFFFFu;
  f32x2 v[8];
  u32x4 x2p[2], wof;
  float wsum;

  auto layer = [&](auto it_c) __attribute__((always_inline)) {
    constexpr int it = decltype(it_c)::value;
    f32x4 acc[2][6];
    f32x2 qs2[2];
    u32x4 k0, k1, v0, v1;
    {
      IDS();
      bool aff = false;
      unsigned aq[2];
      u32x4 av[2];
#define PIN_ACC() asm volatile("" : "+{v[176:179]}"(acc[0][0]), "+{v[180:183]}"(acc[0][1]), "+{v[184:187]}"(acc[0][2]), "+{v[188:191]}"(acc[0][3]), "+{v[192:195]}"(acc[0][4]), "+{v[196:199]}"(acc[0][5]), \
                               "+{v[200:203]}"(acc[1][0]), "+{v[204:207]}"(acc[1][1]), "+{v[208:211]}"(acc[1][2]), "+{v[212:215]}"(acc[1][3]), "+{v[216:219]}"(acc[1][4]), "+{v[220:223]}"(acc[1][5]))
#define PINV4(x) asm volatile("" : "+v"(x[0]), "+v"(x[1]), "+v"(x[2]), "+v"(x[3])::"memory")
      auto qkv_loop = [&](auto it_c) __attribute__((always_inline)) {
        constexpr int IT = decltype(it_c)::value;
        const unsigned abase = OFF_XN + IT * (4u << 20) + (p * 8 + rt) * 65536;
        const int rot = ROT0 + 5 * IT;
        unsigned fkey = 0, kv, bq[2];
        u32x4 fx[4], bv[2], af[WA][2];
        u32x2 cxn, cx1, hh;
        f32x4 sa, sb, cs4[2];
        f32x2 b2[2];
#define AB_OFF() (OFF_AB + IT * 32768 + (p * 8 + rt) * 512)
#define A_LOAD1(k, a) ld16w<(k) % WA * 2 + (a), ((k) & 1) * 2048 + (a) * 1024>(af[(k) % WA][a], rs, l * 16, abase + ((((k) >> 2) + rot) & 7) * 8192 + (((k) >> 1) & 1) * 4096)
        if constexpr (IT == 1) {
#pragma unroll
          for (int s = 0; s < NPRE; ++s) { af[s][0] = pre[s][0]; af[s][1] = pre[s][1]; }
        }
        const bf16* pwq = wqkv;
        ARGP(pwo, 3);
        const rsrc_t rx = RSRC(x, 2048 * 1024 * 2);
        unsigned xv_ = X_VOFF(), xr[2] = {X_ROFF(0), X_ROFF(1)};
        if constexpr (IT == 0) asm volatile("" : "+v"(xv_), "+v"(xr[0]), "+v"(xr[1]));
#define X_READ(u) { af[((u) & ~1) % WA][(u) & 1] = *(const LDS_AS u32x4*)(size_t)(xr[0] + unit_slot(u)); af[((u) | 1) % WA][(u) & 1] = *(const LDS_AS u32x4*)(size_t)(xr[1] + unit_slot(u)); }
        const int tstep = TS0 + bid % TSN + TSD * (w >> TSG);
        const unsigned wqv = WQ_VOFF();
#define QKV_DMA1(t, i)                                                                            \
  {                                                                                               \
    if constexpr (IT == 0) WQ_PIECE(t, 3 * w + (i), L_RING + SLOT(t) * STAGE);                    \
    else dma16s<(i) * 1024>(rs, l * 16, OFF_WI + (h * 8 + (((t) + rot) & 7)) * STAGE + w * 3072, lbase + L_RING + SLOT(t) * STAGE + w * 3072); \
  }
        unsigned bo[2][2];
#pragma unroll
        for (int dt = 0; dt < 2 - IT; ++dt) {
          bo[dt][0] = lbase + L_RING + (IT ? l * 16 : WQ_BOFF(dt)); bo[dt][1] = bo[dt][0] + 2 * STAGE;
          asm volatile("" : "+v"(bo[dt][0]), "+v"(bo[dt][1]));
        }
        auto frag0 = [&](int sl, int f) __attribute__((always_inline)) {
          unsigned pb = lbase + L_RING + WQ_POFF();
          asm volatile("" : "+v"(pb));
          const unsigned o = (pb ^ ((f & 1) << 5)) + sl * STAGE + ((f % 6) >> 1) * 8192 + (f / 6) * 2048;
          frag_t t;
          t.hf.a = trabs(o); t.hf.b = trabs(o + 1024);
          return t.v;
        };
        u32x4 df[4];
        auto dot_mma = [&](const u32x4 (&xf)[4]) __attribute__((always_inline)) {
          sa = mfma16(df[0], xf[0], f32x4{0.f, 0.f, 0.f, 0.f}); sb = mfma16(df[1], xf[1], f32x4{0.f, 0.f, 0.f, 0.f});
          sa = mfma16(df[2], xf[2], sa); sb = mfma16(df[3], xf[3], sb);
        };
        auto dot_store = [&](unsigned dst) __attribute__((always_inline)) {
          if ((l & 15) == 0) st16(unpoison4(__builtin_bit_cast(u32x4, sa + sb)), rs, g * 16, dst + (h * 8 + p) * 512 + w * 64);
        };
        auto tile_sum = [&](u32x4 pt, int dst) __attribute__((always_inline)) {
          f32x4 c = __builtin_bit_cast(f32x4, pt);
#pragma unroll
          for (int e = 0; e < 4; ++e) { c[e] += dppf<0x124>(c[e]); c[e] += dppf<0x128>(c[e]); c[e] = add_x16(c[e]); }
          if ((l & 28) == 0) *(LDS_AS f32x4*)(smem + dst + (w * 2 + (l >> 5)) * 64 + (l & 3) * 16) = c;
        };
        frag_t bfr[6];
#define QKV_BO(s, n, x) (bo[((n) ^ (x)) & 1][SLOT((s) >> 2) >> 1] + (SLOT((s) >> 2) & 1) * STAGE + ((n) >> 1) * 8192 + ((s) & 3) * 2048 + (x) * 256)
#define QKV_RDA(s, n)                                                                             \
  {                                                                                               \
    if constexpr (IT == 0) bfr[n].hf.a = trabs(QKV_BO(s, n, 0));                                  \
    else bfr[n].v = *(const LDS_AS u32x4*)(size_t)(bo[0][SLOT((s) >> 2) >> 1] + (SLOT((s) >> 2) & 1) * STAGE + (((s) & 3) * 6 + (n)) * 1024); \
  }
#define QKV_RDB(s, n) { if constexpr (IT == 0) bfr[n].hf.b = trabs(QKV_BO(s, n, 1)); }
#define XP_OFF() (OFF_XNEW + ((rot + 5) & 7) * 1024)
#define FP_VO() (w == 3 ? (l >> 2) * 256 + (l & 3) * 16 : ((l >> 2) & 7) * 512 + (l >> 5) * 64 + (l & 3) * 16)
#define FP_OFF(i) (w == 3 ? OFF_XNEW + (i) * 4096 : OFF_FX + h * 4096 + w * 128)
#define FX_OK(n) __all(((n) == 4 ? max(max(umax4(fx[0]), umax4(fx[1])), max(umax4(fx[2]), umax4(fx[3]))) : max(umax4(fx[0]), umax4(fx[1]))) != POISON)
#define SINCE(s0) (vm_at(IT, s, n) - vm_at(IT, s0, 5))
#define DEF4(x) asm volatile("" : "=v"(x[0]), "=v"(x[1]), "=v"(x[2]), "=v"(x[3]))
        LDS_AS unsigned* qcnt = (LDS_AS unsigned*)(smem + L_QC);
        unsigned qc;
        unsigned qadr = lbase + L_QC, qone = 1;
        asm volatile("" : "+v"(qadr), "+v"(qone));
#define count_in(i) asm volatile("s_mov_b64 exec, 1\n\tds_add_u32 %0, %1 offset:%2\n\ts_mov_b64 exec, -1" ::"v"(qadr), "v"(qone), "n"(4 * (i)) : "memory")
        auto count_wait = [&](int i, unsigned want) __attribute__((always_inline)) {
          if (__builtin_expect((unsigned)__builtin_amdgcn_readfirstlane(qc) < want, 0)) {
            while ((unsigned)__builtin_amdgcn_readfirstlane(__hip_atomic_load(qcnt + i, __ATOMIC_RELAXED, __HIP_MEMORY_SCOPE_WORKGROUP)) < want) {}
          }
          asm volatile("" ::: "memory");
        };
        if constexpr (IT == 0) {
          wait_vm<PRE0 - vm_unit(1)>();
          X_READ(0); X_READ(1);
          asm volatile("s_barrier" ::: "memory");
        }
#pragma unroll
        for (int n = 0; n < 6; ++n) { QKV_RDA(0, n); if (n < 5) QKV_RDB(0, n); }
        f32x2 lv[4], xs, xq;
        float lmean;
        f32x4 sc;
#define X1_OFF() (F_X1 + ((p * RX1 + (xcd >> 1) % RX1) * 32 + (rt >> 2) * 16 + (l & 15)) * SX1)
        unsigned long long xok = 0;
        RowFin fin;
        auto x1_need = [&](unsigned long long m) __attribute__((always_inline)) {
          if (__builtin_expect((xok & m) != m, 0)) {
            for (unsigned spins = 0;;) {
              const u32x2 f = ld8(rs, X1_OFF(), 0);
              xok = __builtin_amdgcn_ballot_w64(f[0] == T[0] && f[1] == T[1]);
              if ((xok & m) == m) break;
              if (spin_fail(spins, rs, T)) { xok = ~0ull; break; }
              __builtin_amdgcn_s_sleep(KSL);
            }
            asm volatile("" ::: "memory");
          }
        };
        auto gap = [&](auto sc_, auto nc_) __attribute__((always_inline)) {
          constexpr int s = decltype(sc_)::value, n = decltype(nc_)::value, ar = a_req(s, n);
          {
            constexpr int tc = (s + 2) / 4;
            if constexpr (tc >= 1 && tc < 8 && s == 4 * tc - 2 && n == 5) {
              if constexpr (!IT || tc >= 3) wait_vm<vm_at(IT, s, n) - vm_tile(IT, tc)>();
              if constexpr (tc == 7 && !IT) asm volatile("s_waitcnt lgkmcnt(4)" ::: "memory");
              asm volatile("s_barrier" ::: "memory");
            }
          }
          if constexpr (IT == 0 && n == 0 && s + 2 < 32) { wait_vm<vm_at(0, s, 0) - vm_unit(s + 2)>(); X_READ(s + 2); SB(); }
          if constexpr (n == 0 && tile_piece(IT, s)) QKV_DMA1(((s - tp0(IT)) >> 2) + 3, (s - tp0(IT)) & 3);
          if constexpr (IT == 0) {
            if constexpr ((n == 2 || n == 3) && s + 5 < 32) {
              if constexpr (n == 2) asm volatile("s_waitcnt lgkmcnt(%0)" ::"n"(s + 1 < QL ? 4 : 1) : "memory");
              X_UNIT1(s + 5, n - 2);
            }
            if constexpr (s >= W2S && s < W2S + 3 && n == 4) QKV_DMA1(2, s - W2S);
            if constexpr (s == LNL && n == 5) LN_LOAD();
            if constexpr (s == 0 && n < 2) { X_UNIT1(3 + n, 0); X_UNIT1(3 + n, 1); }
            if constexpr (s == LNS && n == 1) {
              wait_vm<vm_at(0, s, n) - vm_at(0, LNL + 1, 0)>();
              asm volatile("" : "+v"(ep), "+v"(xl[0]), "+v"(xl[1])::"memory");
              const unsigned long long e = (((unsigned long long)(unsigned)__builtin_amdgcn_readfirstlane(ep[1]) << 32) | (unsigned)__builtin_amdgcn_readfirstlane(ep[0])) + 1ull + dispatch_id() * 0x9E3779B97F4A7C16ull;
              T = u32x2{(unsigned)e, (unsigned)(e >> 32)};
              unpack8v(xl[0], lv);
              xs = (lv[0] + lv[1]) + (lv[2] + lv[3]);
              xq = __builtin_elementwise_fma(lv[0], lv[0], lv[1] * lv[1]) + __builtin_elementwise_fma(lv[2], lv[2], lv[3] * lv[3]);
              asm volatile("" : "+v"(xs), "+v"(xq));
            }
            if constexpr (s == LNS && n == 4) {
              unpack8v(xl[1], lv);
              xs += (lv[0] + lv[1]) + (lv[2] + lv[3]);
              xq += __builtin_elementwise_fma(lv[0], lv[0], lv[1] * lv[1]) + __builtin_elementwise_fma(lv[2], lv[2], lv[3] * lv[3]);
              auto t = __builtin_amdgcn_permlane32_swap(__builtin_bit_cast(unsigned, hadd(xs)), __builtin_bit_cast(unsigned, hadd(xq)), false, false);
              lmean = asf(t[0]) + asf(t[1]);
              asm volatile("" : "+v"(lmean));
            }
            if constexpr (s == LNS + 1 && n == 1) { lmean += dppf<0xB1>(lmean); lmean += dppf<0x4E>(lmean); lmean += dppf<0x124>(lmean); asm volatile("" : "+v"(lmean)); }
            if constexpr (s == LNS + 1 && n == 4) { lmean = add_x16(lmean + dppf<0x128>(lmean)) * (1.0f / 1024.0f); asm volatile("" : "+v"(lmean)); }
            if constexpr (s == LNS + 2 && n == 1) {
              const float m2 = asf(__builtin_amdgcn_readlane(__builtin_bit_cast(unsigned, lmean), 32));
              xs[0] = ln_rd(fmaxf(__builtin_fmaf(-lmean, lmean, m2), 0.f));
              asm volatile("" : "+v"(xs));
            }
            if constexpr (s == LNS + 2 && n == 5) {
              if (l == 0) st16(u32x4{__builtin_bit_cast(unsigned, (float)xs[0]), __builtin_bit_cast(unsigned, -rbf1(lmean)), T[0], T[1]}, rs, OFF_AB + (p * 256 + h * 8 + w) * 16, 0);
            }
            if constexpr (s == POI && n == 1) {
              if (w == 1) {
                const unsigned prb = 2 * p + (h >> 4), pct = h & 15;
                unsigned pz1 = POISON;
                asm volatile("" : "+v"(pz1));
                const u32x4 pz = {pz1, pz1, pz1, pz1};
                st16(pz, rs, OFF_STAT + (prb * 16 + pct) * 2048 + l * 16, 0);
                st16(pz, rs, OFF_STAT + (prb * 16 + pct) * 2048 + 1024 + l * 16, 0);
                if (l < 16) st16(pz, rs, OFF_CA + prb * 4096 + pct * 256 + l * 16, 0);
                else if (l == 16 && pct < RKEY) st4(pz1, rs, KEY_AT(0, pct) + prb * SKEY, 0);
              }
            }
            if constexpr (s == POI + 4 && n == 1) { if (w == 1 && l < RX) st8(T, rs, F_XN + (p * RX + l) * 256 + h * 8, 0); }
          } else {
            if constexpr ((s == 0 && n == 0) || (s == 2 && n == 2)) asm volatile("buffer_load_dwordx2 %0, %1, %2, 0 offen sc1" : "=v"(cxn) : "v"(X1_OFF()), "s"(rs) : "memory");
            if constexpr ((s == 1 && n == 3) || (s == 4 && n == 0)) {
              wait_vm<vm_at(1, s, n) - (s == 1 ? vm_at(1, 0, 1) : vm_at(1, 2, 3))>();
              asm volatile("" : "+v"(cxn)::"memory");
              xok |= __builtin_amdgcn_ballot_w64(cxn[0] == T[0] && cxn[1] == T[1]);
            }
#define IMG_OFF() (F_X1 + (((l & 7) * RX1 + (xcd >> 1) % RX1) * 32 + (rb & 1) * 16 + ct) * SX1)
            if constexpr (s == 0 && n == 2) { asm volatile("" : "=v"(cx1)); if (w == 1) asm volatile("buffer_load_dwordx2 %0, %1, %2, 0 offen sc1" : "=v"(cx1) : "v"(IMG_OFF()), "s"(rs) : "memory"); }
            if constexpr (s == 2 && n == 4) {
              if (w == 1) {
                wait_vm<vm_at(1, 2, 4) - vm_at(1, 0, 3)>();
                asm volatile("" : "+v"(cx1)::"memory");
                if (!__all(cx1[0] == T[0] && cx1[1] == T[1])) poll_flags(rs, IMG_OFF(), 0, T);
              }
            }
            if constexpr (ar >= 0 && (ar & 7) == 0) x1_need(3ull << (((rot + ar / 8) & 7) * 2));
            const unsigned R = (rb * 4 + (w & 3)) * 32 + (w >> 2) * 16 + (l & 15), so = OFF_STAT + rb * 32768;
            if constexpr (s == (SPL < 0 ? 0 : SPL) && n == (SPL < 0 ? 1 : 4)) {
#pragma unroll
              for (int i = 0; i < 4; ++i) ld16h<0>(fx[i], rs, (4 * g + i) * 2048 + (R & 127) * 16, so);
            }
            if constexpr (s == SPL + 2 && n == 1) {
              wait_vm<vm_at(1, s, n) - (SPL < 0 ? vm_at(1, 0, 2) : vm_at(1, SPL, 5))>();
              PINV4(fx);
              fin = fin_key(rs, T, g * 8192 + (R & 127) * 16, so, smem + L_SKEY, l, w, R, wsum, fx);
              count_in(2);
            }
            if constexpr (s == SPL + 3 && n == 0) qc = __hip_atomic_load(qcnt + 2, __ATOMIC_RELAXED, __HIP_MEMORY_SCOPE_WORKGROUP);
            if constexpr (s == SPL + 3 && n == 1) { count_wait(2, 8); fin_cand(rs, smem + L_SKEY, 0, g, rb, ct, fin, v); }
            if constexpr (s == SPL + 3 && n == 3) {
              fin_x2(fin, v);
              x2p[0] = pack8v(v); x2p[1] = pack8v(v + 4);
              asm volatile("" : "+v"(x2p[0]), "+v"(x2p[1]));
            }
            if constexpr (s == SPL + 3 && n == 5) {
              if ((w & 3) * 2 + (w >> 2) == ct >> 1) {
                const float f = 1.0f + fin.rd, rd2 = ln_rd(fin.var * f * f), mb2 = rbf1(__builtin_fmaf(fin.mean - fin.mb, fin.rd, fin.mean));
                if (g == 0 && ((l >> 3) & 1) == (ct & 1))
                  st16(u32x4{__builtin_bit_cast(unsigned, f * rd2), __builtin_bit_cast(unsigned, -__builtin_fmaf(fin.mb, fin.rd, mb2) * __builtin_amdgcn_rcpf(f)), T[0], T[1]}, rs, OFF_AB + 32768 + R * 16, 0);
              }
            }
          }
          if constexpr (IT == 1 && ar >= 0) A_LOAD1(ar / 2, ar & 1);
          if constexpr (!IT && s == CSL && n == 4) { asm volatile("" : "=v"(fx[0])); if (w < 6) ld16h<0>(fx[0], rs, l * 16, OFF_CS + (h * 6 + w) * 1024); }
          if constexpr (!IT && s == CSS && n == 2) {
            if (w < 6) {
              wait_vm<SINCE(CSL)>();
              asm volatile("" : "+v"(fx[0])::"memory");
              tag_wait(rs, T, l * 16, OFF_CS + (h * 6 + w) * 1024, fx[0]);
              const f32x2 c = {sum_hi8(asf(fx[0][0])), sum_hi8(asf(fx[0][1]))};
              if (l < 8) *(LDS_AS f32x2*)(smem + L_CSL + w * 64 + l * 8) = c;
            }
          }
          constexpr int ABL = abl(IT), ABS = abs_(IT);
          if constexpr (s == ABL && n == 4) ld16h<0>(bv[0], rs, (l & 31) * 16, AB_OFF());
          if constexpr (s == ABS && n == 2) {
            wait_vm<SINCE(ABL)>();
            asm volatile("" : "+v"(bv[0])::"memory");
            tag_wait(rs, T, (l & 31) * 16, AB_OFF(), bv[0]);
            const unsigned t0 = bv[0][0], t1 = bv[0][1];
            ((LDS_AS unsigned*)(smem + L_ABW + rt * 2048))[l] = l < 32 ? t0 : t1;
          }
          if constexpr (s == ABS && n == 3) {
#pragma unroll
            for (int a = 0; a < 2; ++a) {
              const LDS_AS unsigned* aw = (const LDS_AS unsigned*)(smem + L_ABW + rt * 2048) + 16 * a;
              aq[a] = aw[l & 15]; bq[a] = aw[32 + (l & 15)];
              av[a] = *(const LDS_AS u32x4*)(aw + 4 * g); bv[a] = *(const LDS_AS u32x4*)(aw + 32 + 4 * g);
            }
          }
          if constexpr (s == ABS && n == 5) {
#pragma unroll
            for (int a = 0; a < 2; ++a) { b2[a] = f32x2{asf(bq[a]), asf(bq[a])}; asm volatile("" : "+v"(b2[a])); }
          }
          if constexpr (s == ABS + 1) {
            constexpr int k = (n + 4) % 6;
            if constexpr (k < 4) cs4[k & 1] = *(const LDS_AS f32x4*)(smem + L_CSL + k * 64 + g * 16);
            else cs4[k & 1][0] = *(const LDS_AS float*)(smem + L_CSL + k * 64 + (l & 15) * 4);
          }
          if constexpr ((s == ABS + 1 && n > 0) || (s == ABS + 2 && n == 0)) {
            constexpr int k = (n + 3) % 6;
            if constexpr (k < 4) {
#pragma unroll
              for (int a = 0; a < 2; ++a) acc[a][k] = __builtin_elementwise_fma(cs4[k & 1], f32x4{b2[a][0], b2[a][1], b2[a][0], b2[a][1]}, acc[a][k]);
            } else {
              f32x2 c2 = {cs4[k & 1][0], cs4[k & 1][0]};
              asm volatile("" : "+v"(c2));
#pragma unroll
              for (int a = 0; a < 2; ++a) acc[a][k] = __builtin_elementwise_fma(f32x4{c2[0], c2[1], c2[0], c2[1]}, __builtin_bit_cast(f32x4, bv[a]), acc[a][k]);
            }
          }
          if constexpr (IT == 0) {
            if constexpr (s == CSD && n < 3) fx[n] = frag0(SLOT(s >> 2), 3 * w + n);
            if constexpr (s == CSD + 1 && n == 1) {
              unsigned one2 = 0x3F803F80u;
              asm volatile("" : "+v"(one2));
              const u32x4 o4 = {one2, one2, one2, one2};
              const f32x4 z4 = {0.f, 0.f, 0.f, 0.f};
              sa = mfma16(fx[0], o4, z4); sb = mfma16(fx[1], o4, z4); sc = mfma16(fx[2], o4, z4);
            }
            if constexpr (s == CSD + 2 && n == 1) {
              const int c = l & 15;
              const f32x4 t0 = sa, t1 = sb, t2 = sc, t = c == 0 ? +t0 : c == 1 ? +t1 : +t2;
              if (c < 3) *(LDS_AS f32x4*)(smem + L_CSP + (w * 3 + c) * 64 + g * 16) = t;
              count_in(2);
            }
            if constexpr (s == CSD + 4 && n == 1) {
              if (w == 2) {
                qc = 0;
                count_wait(2, 8);
                const LDS_AS f32x2* cp = (const LDS_AS f32x2*)(smem + L_CSP) + min(l, 47);
                const f32x2 t = (cp[0] + cp[48]) + (cp[96] + cp[144]);
                if (l < 48) st16(u32x4{__builtin_bit_cast(unsigned, (float)t[0]), __builtin_bit_cast(unsigned, (float)t[1]), T[0], T[1]}, rs, (l >> 3) * 1024 + (l & 7) * 16, OFF_CS + h * 6144 + p * 128);
              }
            }
            if constexpr (s >= 29 && (n & 1)) keep[3 * (s - 29) + (n >> 1)] = frag0(SLOT(s - 24), 3 * w + (n >> 1));
            if constexpr (s == (TS0 < TEX ? TS0 : TEX) - 2 && n == 1) {
              ARGP(pwg, 5); ARGP(pwu, 6);
              const int t = tid < 288 ? tid - 64 : tid - 288, go = (bi * 32 + t / 7) * F_ + xcd * 448 + (t % 7) * 64;
              tsrc = tid < 64 ? pwo + bid * 4096 + tid * 64 : tid < 288 ? pwg + go : pwu + go;
              asm volatile("" : "+v"(tsrc));
            }
            if constexpr (s >= TS0 && s < TS0 + TSN + TSD * (7 >> TSG) && n == 4) {
              if (tstep == s && ((TM0 >> w) & 1)) TOUCH(tsrc);
            }
            if constexpr (s == TEX && n == 5) {
              const GAS bf16* t2 = ((TM1 >> w) & 1) ? tsrc : (const GAS bf16*)x + p * 262144;
              TOUCH(t2);
            }
          } else {
            if constexpr (s >= 27 && s < 30 && (n == 0 || n == 3)) { const unsigned wov = WO_VOFF(); WO_PIECE(4 + 2 * (s - 27) + n / 3); }
            if constexpr (WON && s == WOS && n == 4) st16(wof, rs, (ct * 128 + rb * 8 + w) * 1024 + l * 16, OFF_WOI);
            if constexpr (WON && s == 19 && n == 1) { if (tid == 64) st8(T, rs, (ct * 16 + rb) * 256, F_WOI); }
            if constexpr (WON && s == 23 && n == 4) { asm volatile("" : "=v"(cx1)); if (w == 2) ld8h(cx1, rs, (l & 15) * 256, F_WOI + ct * 4096); }
            if constexpr (WON && s == 26 && n == 3) {
              if (w == 2) {
                wait_vm<SINCE(23)>();
                asm volatile("" : "+v"(cx1)::"memory");
                if (!__all(cx1[0] == T[0] && cx1[1] == T[1])) poll_flags(rs, (l & 15) * 256, F_WOI + ct * 4096, T);
              }
            }
            if constexpr (s == FKL && n == 4) { asm volatile("" : "=v"(kv)); if (w == 0) ld4h<0>(kv, rs, (l & 15) * SKEY, KEY_AT(0, KREP())); }
            if constexpr (s == FSL && n == 1) {
              asm volatile("" : "=v"(fx[0]), "=v"(fx[1]));
              if (w == 0) {
                wait_vm<SINCE(FKL)>();
                asm volatile("" : "+v"(kv)::"memory");
                fkey = __builtin_amdgcn_readfirstlane(umax16(kv == POISON ? 0u : kv));
                ld4h<0>(kv, rs, (l & 15) * SKEY, KEY_AT(0, KREP()));
                ld16h<0>(fx[0], rs, (l >> 3) * 256 + (l & 7) * 16, cand_off(fkey, 0)); ld16h<2048>(fx[1], rs, (l >> 3) * 256 + (l & 7) * 16, cand_off(fkey, 0));
              }
            }
            if constexpr (s == FS0 - 1 && n == FSN) {
              if (w == 0) {
                wait_vm<vm_at(1, s, n) - vm_at(1, FSL, 2)>();
                asm volatile("" : "+v"(fx[0]), "+v"(fx[1]), "+v"(kv)::"memory");
                const unsigned k_ = key_wait(rs, T, l, 0, KREP(), kv);
                if (l == 0) *(LDS_AS unsigned*)(smem + L_SKEY + 32) = k_;
                if (k_ != fkey) fx[0][0] = POISON;
                word_wait<2>(rs, T, (l >> 3) * 256 + (l & 7) * 16, cand_off(k_, 0), fx);
                *(LDS_AS u32x4*)(smem + L_ROW + l * 16) = fx[0]; *(LDS_AS u32x4*)(smem + L_ROW + 1024 + l * 16) = fx[1];
                count_in(0);
              }
            }
            if constexpr (s == FS0 - 1 && n == 1) {
              asm volatile("" : "=v"(dfr[0]), "=v"(dfr[1]), "=v"(dfr[2]), "=v"(dfr[3]), "=v"(dfr[4]), "=v"(dfr[5]), "=v"(dfr[6]), "=v"(dfr[7]));
              if (ffn_on) {
#pragma unroll
                for (int i = 0; i < 8; ++i) dfr[i] = trread(smem, L_DN + w * 4096 + g * 1024 + ((l & 15) >> 2) * 256 + ((i ^ ((l >> 2) & 7)) * 32) + (l & 3) * 8);
              }
            }
            if constexpr (s == FS0) {
              if constexpr (n == 0) qc = __hip_atomic_load(qcnt, __ATOMIC_RELAXED, __HIP_MEMORY_SCOPE_WORKGROUP);
              if constexpr (n == 1) {
                count_wait(0, 1);
                kv = *(const LDS_AS unsigned*)(smem + L_SKEY + 32);
#pragma unroll
                for (int i = 0; i < 4; ++i) fx[i] = *(const LDS_AS u32x4*)(smem + L_ROW + w * 256 + i * 64 + g * 16);
              }
              if constexpr (n == 2) {
                fkey = __builtin_amdgcn_readfirstlane(kv);
                sel0 = key_row(fkey);
                aff = (int)(sel0 >> 8) == p;
                asm volatile("" : "=v"(sa), "=v"(sb));
                if (ffn_on) ffn_gu(gv, uv, fx, sa, sb);
              }
              if constexpr (n == 4) {
                if (ffn_on) {
                  ffn_gu_store(smem, l, w, sa, sb);
                  count_in(1);
                }
              }
            }
            if constexpr (s == FS0D) {
              if constexpr (n == 0) qc = __hip_atomic_load(qcnt + 1, __ATOMIC_RELAXED, __HIP_MEMORY_SCOPE_WORKGROUP);
              if constexpr (n == 1) { asm volatile("" : "=v"(sa)); if (ffn_on) { count_wait(1, 8); sa = ffn_d_read(smem, l); } }
              if constexpr (n == 2) { asm volatile("" : "=v"(hh)); if (ffn_on) hh = ffn_d_h(sa); }
              if constexpr (n == 4) { asm volatile("" : "=v"(sa)); if (ffn_on) sa = ffn_d_all(dfr, hh); }
              if constexpr (n == 5) { if (ffn_on) ffn_d_store(rs, l, w, 0, slice, sa); }
            }
            if constexpr (s == FS1 && n == 4) {
              DEF4(fx);
              asm volatile("" : "=v"(cxn), "=v"(cx1));
              if (!ffn_on) {
                const unsigned G = xcd * 4 + bi - 28;
#pragma unroll
                for (int k = 0; k < 4; ++k) ld16h<0>(fx[k], rs, RED_VO(k), RED_SO(0, G));
                ld8h(cxn, rs, RED_JJ(), cand_off(fkey, 0) + XO(G)); ld8h(cx1, rs, RED_JJ() + 128, cand_off(fkey, 0) + XO(G));
              }
            }
            if constexpr (s == FS1S && n == 2) {
              if (!ffn_on) {
                wait_vm<SINCE(FS1)>();
                PINV4(fx);
                asm volatile("" : "+v"(cxn), "+v"(cx1)::"memory");
                red_sum(rs, smem, T, tid, w, 0, xcd * 4 + bi - 28, fkey, fx, cxn, cx1);
                BAR();
                if (w == 0) {
                  const u32x2 ov = red_final(smem, l, red_gate(fkey), cxn, cx1);
                  if (l < 8) st8(unpoison2(ov), rs, OFF_XNEW + (xcd * 4 + bi - 28) * 256 + ((l & 3) * 2 + (l >> 2)) * 8, 0);
                }
              }
            }
            if constexpr (s == FXA && n == 4) { asm volatile("" : "=v"(fx[0])); if (w == 1) ld16h<0>(fx[0], rs, ((l >> 2) & 3) * 256 + (l & 3) * 16, XP_OFF()); }
            if constexpr (s == FXB - 1 && n == FSN) {
              if (w == 1) {
                wait_vm<SINCE(FXA)>();
                asm volatile("" : "+v"(fx[0])::"memory");
                word_wait<1>(rs, T, ((l >> 2) & 3) * 256 + (l & 3) * 16, XP_OFF(), fx);
                if (l < 16) *(LDS_AS u32x4*)(smem + L_ROW + l * 16) = fx[0];
                count_in(0);
              }
            }
            if constexpr (s == FXB) {
#define FX_FRAG(i) (*(const LDS_AS u32x4*)(size_t)(bo[0][0] + STAGE + (6 * (i) + w) * 1024))
              if constexpr (n == 0) { qc = __hip_atomic_load(qcnt, __ATOMIC_RELAXED, __HIP_MEMORY_SCOPE_WORKGROUP); }
              if constexpr (n == 1) {
                DEF4(fx); DEF4(df);
                if (w < 6) {
                  count_wait(0, 2);
#pragma unroll
                  for (int i = 0; i < 4; ++i) { fx[i] = *(const LDS_AS u32x4*)(smem + L_ROW + i * 64 + g * 16); df[i] = FX_FRAG(i); }
                }
              }
              if constexpr (n == 2) { asm volatile("" : "=v"(sa), "=v"(sb)); if (w < 6) dot_mma(fx); }
              if constexpr (n == 4) { if (w < 6) dot_store(OFF_FX); }
            }
            if constexpr (s == FXL && n == 4) {
              asm volatile("" : "=v"(fx[0]), "=v"(fx[1]));
              if (aff && w < 4) { ld16h<0>(fx[0], rs, FP_VO(), FP_OFF(0)); ld16h<0>(fx[1], rs, FP_VO(), FP_OFF(1)); }
            }
            if constexpr (s == FXS && n == 4) {
              if (aff && w < 4) {
                wait_vm<0>();
                asm volatile("" : "+v"(fx[0]), "+v"(fx[1])::"memory");
                if (!FX_OK(2)) {
                  unsigned spins = 0;
                  do {
                    if (spin_fail(spins, rs, T)) break;
                    fx[0] = ld16(rs, FP_VO(), FP_OFF(0)); fx[1] = ld16(rs, FP_VO(), FP_OFF(1));
                  } while (!FX_OK(2));
                }
                if (w == 3) {
                  f32x2 nx[8];
                  unpack8v(fx[0], nx); unpack8v(fx[1], nx + 4);
                  const f32x2 sm = ((nx[0] + nx[1]) + (nx[2] + nx[3])) + ((nx[4] + nx[5]) + (nx[6] + nx[7]));
                  const float mean = sum64(hadd(sm)) * (1.0f / 1024.0f);
                  const f32x2 m2 = {mean, mean};
                  f32x2 q = {0.f, 0.f};
#pragma unroll
                  for (int j = 0; j < 8; ++j) { const f32x2 d = nx[j] - m2; q = __builtin_elementwise_fma(d, d, q); }
                  const float rd = ln_rd(sum64(hadd(q)) * (1.0f / 1024.0f));
                  if (l == 0) *(LDS_AS f32x2*)(smem + L_FXL + 384) = f32x2{rd, -rbf1(mean)};
                } else {
                  tile_sum(fx[0], L_FXL);
                }
                count_in(3);
              }
            }
          }
        };
        static_for<32>([&](auto sc_) __attribute__((always_inline)) {
          constexpr int s = decltype(sc_)::value;
          if constexpr (s > 0) PIN_ACC();
          if constexpr (QPR && s == QPR) { if (w >= 4) __builtin_amdgcn_s_setprio(1); }
          if constexpr (IT == 1) wait_vm<vm_at(IT, s, 0) - vm_a(IT, s)>();
          pin_w<s % WA>(af[s % WA][0], af[s % WA][1]);
          SB();
          static_for<6>([&](auto nc_) __attribute__((always_inline)) {
            constexpr int n = decltype(nc_)::value;
            if constexpr (s == 0) { acc[0][n] = f32x4{0.f, 0.f, 0.f, 0.f}; acc[1][n] = acc[0][n]; }
            constexpr bool on = s < QL || n >= 2;
            if constexpr (on) acc[0][n] = n < 4 ? mfma16(bfr[n].v, af[s % WA][0], acc[0][n]) : mfma16(af[s % WA][0], bfr[n].v, acc[0][n]);
            SB();
            if constexpr (n == 0) { QKV_RDB(s, 5); }
            else if constexpr (s + 1 < 32 && (s + 1 < QL || n > 2)) QKV_RDB(s + 1, n - 1);
            SB();
            if constexpr (on) acc[1][n] = n < 4 ? mfma16(bfr[n].v, af[s % WA][1], acc[1][n]) : mfma16(af[s % WA][1], bfr[n].v, acc[1][n]);
            SB();
            if constexpr (s + 1 < 32 && (s + 1 < QL || n >= 2)) QKV_RDA(s + 1, n);
            SB();
            gap(sc_, nc_);
            SB();
          });
        });
        PIN_ACC();
        frag_t qw[32 - QL][2];
        static_for<32 - QL>([&](auto c_) __attribute__((always_inline)) {
          constexpr int s = QL + decltype(c_)::value;
          QKV_RDA(s, 0); QKV_RDB(s, 0); QKV_RDA(s, 1); QKV_RDB(s, 1);
          qw[s - QL][0] = bfr[0]; qw[s - QL][1] = bfr[1];
        });
        SB();
        const bool own = aff && (int)((sel0 >> 5) & 7) == rt;
        const bool m0 = !(sel0 & 16), mq = (l & 15) == (int)(sel0 & 15), mv = g == (int)((sel0 & 15) >> 2);
        auto patch_qk = [&](int n) __attribute__((always_inline)) {
          f32x4 t = *(const LDS_AS f32x4*)(smem + L_FXL + n * 64 + g * 16);
          const f32x4 c = *(const LDS_AS f32x4*)(smem + L_CSL + n * 64 + g * 16);
          const float nb = *(const LDS_AS float*)(smem + L_FXL + 388);
#pragma unroll
          for (int e = 0; e < 4; ++e) { t[e] = __builtin_fmaf(c[e], nb, t[e]); asm volatile("" : "+v"(t[e])); }
          acc[0][n] = (mq && m0) ? t : acc[0][n];
          acc[1][n] = (mq && !m0) ? t : acc[1][n];
        };
        if (own) {
          while (__hip_atomic_load(qcnt + 3, __ATOMIC_RELAXED, __HIP_MEMORY_SCOPE_WORKGROUP) < 4u) {}
          asm volatile("" ::: "memory");
          patch_qk(2); patch_qk(3);
          const f32x2 nab = *(const LDS_AS f32x2*)(smem + L_FXL + 384);
          const LDS_AS float* fl_ = (const LDS_AS float*)(smem + L_FXL + 256) + (l & 15);
          const LDS_AS float* cl_ = (const LDS_AS float*)(smem + L_CSL + 256) + (l & 15);
          const float t4 = __builtin_fmaf(cl_[0], nab[1], fl_[0]), t5 = __builtin_fmaf(cl_[16], nab[1], fl_[16]);
          const unsigned na = __builtin_bit_cast(unsigned, (float)nab[0]);
#pragma unroll
          for (int a = 0; a < 2; ++a) {
            const bool ma = a ? !m0 : m0;
            aq[a] = (mq && ma) ? na : aq[a];
#pragma unroll
            for (int e = 0; e < 4; ++e) {
              const bool me = mv && ma && e == (int)(sel0 & 3);
              acc[a][4][e] = me ? t4 : acc[a][4][e]; acc[a][5][e] = me ? t5 : acc[a][5][e];
              av[a][e] = me ? na : av[a][e];
            }
          }
        }
        constexpr float C2 = 1.4426950408889634f * 0.17677669529663687f;
#pragma unroll
        for (int a = 0; a < 2; ++a) {
          f32x2 a2 = {asf(aq[a]), asf(aq[a])};
          qs2[a] = f32x2{asf(aq[a]) * C2, asf(aq[a]) * C2};
          asm volatile("" : "+v"(a2), "+v"(qs2[a]));
          acc[a][2] *= f32x4{a2[0], a2[1], a2[0], a2[1]}; acc[a][3] *= f32x4{a2[0], a2[1], a2[0], a2[1]};
          acc[a][4] *= __builtin_bit_cast(f32x4, av[a]); acc[a][5] *= __builtin_bit_cast(f32x4, av[a]);
        }
        k0 = pack8(acc[0][2], acc[0][3]); k1 = pack8(acc[1][2], acc[1][3]); v0 = pack8(acc[0][4], acc[1][4]); v1 = pack8(acc[0][5], acc[1][5]);
        const unsigned kvo = OFF_KV + IT * (8u << 20) + (p * 32 + h) * 32768 + rt * 4096;
        if (p < 7) { st16(k0, rs, l * 16, kvo); st16(k1, rs, l * 16 + 1024, kvo); st16(v0, rs, l * 16 + 2048, kvo); st16(v1, rs, l * 16 + 3072, kvo); }
        char* ownp = smem + (8 + rt) * 2048 + l * 16;
        *(u32x4*)(ownp + L_K) = k0;
        if (rt < 7) *(u32x4*)(ownp + L_K + 1024) = k1;
        *(u32x4*)(ownp + L_V) = v0; *(u32x4*)(ownp + L_V + 1024) = v1;
        SB();
        static_for<32 - QL>([&](auto c_) __attribute__((always_inline)) {
          constexpr int s = QL + decltype(c_)::value;
#pragma unroll
          for (int n = 0; n < 2; ++n) { acc[0][n] = mfma16(qw[s - QL][n].v, af[s % WA][0], acc[0][n]); acc[1][n] = mfma16(qw[s - QL][n].v, af[s % WA][1], acc[1][n]); }
        });
        if (own) { patch_qk(0); patch_qk(1); }
      };
      qkv_loop(it_c);
    }
#undef A_LOAD1
#undef QKV_RDA
#undef QKV_RDB
#undef QKV_BO
    {
      IDS();
      const GAS bf16 *pwo, *pwg, *pwu;
      asm volatile("s_load_dwordx2 %0, %3, 24\n\ts_load_dwordx2 %1, %3, 40\n\ts_load_dwordx2 %2, %3, 48" : "=&s"(pwo), "=&s"(pwg), "=&s"(pwu) : "s"(ka));
      const int ql = l & 15;
      const f32x4 z4 = {0.f, 0.f, 0.f, 0.f}, ninf = {-INFINITY, -INFINITY, -INFINITY, -INFINITY};
      LDS_AS unsigned* rdy = (LDS_AS unsigned*)(smem + L_RDY);
      const unsigned rtag = T[0] + it;
      u32x4 qf[2];
      f32x4 cn[2], oa[2][2], sum[2], S[2][2];
      bool told = false;
      auto announce = [&](bool now) __attribute__((always_inline)) {
        if (!told) {
          unsigned st;
          asm volatile("s_getreg_b32 %0, hwreg(HW_REG_IB_STS)" : "=s"(st)::"memory");
          if (now || !(st & 0xC0000Fu)) {
            DRAIN();
            if (l == 0) st8(T, rs, rt * SKW, F_KV + (it * 256 + p * 32 + h) * SKV);
            told = true;
          }
        }
      };
      unsigned one2 = 0x3F803F80u;
      asm volatile("" : "+v"(one2));
      const u32x4 ones = {one2, one2, one2, one2};
      auto ex8 = [&](f32x4 a, f32x4 b) __attribute__((always_inline)) {
        return u32x4{pack2(__builtin_amdgcn_exp2f(a[0]), __builtin_amdgcn_exp2f(a[1])), pack2(__builtin_amdgcn_exp2f(a[2]), __builtin_amdgcn_exp2f(a[3])),
                     pack2(__builtin_amdgcn_exp2f(b[0]), __builtin_amdgcn_exp2f(b[1])), pack2(__builtin_amdgcn_exp2f(b[2]), __builtin_amdgcn_exp2f(b[3]))};
      };
      {
#pragma unroll
        for (int a = 0; a < 2; ++a) {
          const f32x4 c4 = {qs2[a][0], qs2[a][1], qs2[a][0], qs2[a][1]};
          qf[a] = pack8(acc[a][0] * c4, acc[a][1] * c4);
        }
        f32x4 mk;
#pragma unroll
        for (int e = 0; e < 4; ++e) mk[e] = (4 * g + e <= ql) ? 0.f : -INFINITY;
        const f32x4 d0 = mfma16(k0, qf[0], mk), d1 = mfma16(k1, qf[1], mk), d2 = mfma16(k0, qf[1], z4);
        const float m0 = -max_x32(max_x16(fmaxf(fmaxf(d0[0], d0[1]), fmaxf(d0[2], d0[3]))));
        const float m1 = -max_x32(max_x16(fmaxf(fmaxf(d1[0], d1[1]), fmaxf(d1[2], d1[3]))));
        cn[0] = f32x4{m0, m0, m0, m0}; cn[1] = f32x4{m1, m1, m1, m1};
        asm volatile("" : "+v"(cn[0]), "+v"(cn[1]));
        announce(false);
        const f32x4 e0 = d0 + cn[0];
        const u32x4 p0 = {pack2(__builtin_amdgcn_exp2f(e0[0]), __builtin_amdgcn_exp2f(e0[1])), pack2(__builtin_amdgcn_exp2f(e0[2]), __builtin_amdgcn_exp2f(e0[3])), 0u, 0u};
        const u32x4 p1 = ex8(d2 + cn[1], d1 + cn[1]);
        oa[0][0] = mfma16(v0, p0, z4); oa[0][1] = mfma16(v1, p0, z4); sum[0] = mfma16(ones, p0, z4);
        oa[1][0] = mfma16(v0, p1, z4); oa[1][1] = mfma16(v1, p1, z4); sum[1] = mfma16(ones, p1, z4);
      }
      const char* kb_ = smem + L_K + l * 16;
      const char* vb_ = smem + L_V + l * 16;
      const unsigned wov = WO_VOFF(), fnv = (l >> 1) * (F_ * 2) + (l & 1) * 16;
      asm volatile("" : "+v"(oa[0][0]), "+v"(oa[0][1]), "+v"(oa[1][0]), "+v"(oa[1][1]), "+v"(sum[0]), "+v"(sum[1]));
      const unsigned pw = __builtin_amdgcn_readfirstlane(it * 256 + p * 32 + h - 32), pfo = F_KV + pw * SKV + rt * SKW;
      const bool fetcher = rt < 4 && p > 0;
      announce(false);
      if (QPR) __builtin_amdgcn_s_setprio(0);
      BAR();
      asm volatile("" : "+s"(pwo), "+s"(pwg), "+s"(pwu));
#define KV_FETCH(gd)                                                                                                          \
  {                                                                                                                           \
    const unsigned pvo = OFF_KV + pw * 32768 + (gd) * 4096;                                                                   \
    dma16s<0>(rs, l * 16, pvo, lbase + L_K + (gd) * 2048); dma16s<1024>(rs, l * 16, pvo, lbase + L_K + (gd) * 2048);           \
    dma16s<0>(rs, l * 16, pvo + 2048, lbase + L_V + (gd) * 2048); dma16s<1024>(rs, l * 16, pvo + 2048, lbase + L_V + (gd) * 2048); \
  }
      auto fetch = [&]() __attribute__((always_inline)) {
        poll_flags(rs, (l & 1) * 4 * SKW, pfo, T);
        KV_FETCH(rt); KV_FETCH(rt + 4);
        if (l == 0) rdy[8 + rt] = rtag;
      };
      announce(false);
      const bool first = fetcher && told;
      if (first) fetch();
#define LDS16(a_) (*(const LDS_AS u32x4*)(size_t)(a_))
      u32x4 kf[2], vf[2], p0, p1;
#define AE(a, i) { S[a][(i) >> 2][(i) & 3] = __builtin_amdgcn_exp2f(S[a][(i) >> 2][(i) & 3]); asm volatile("" : "+v"(S[a][(i) >> 2][(i) & 3])); SB(); }
#define AC(a, P, j) { P[j] = pack2(S[a][(j) >> 1][2 * ((j) & 1)], S[a][(j) >> 1][2 * ((j) & 1) + 1]); asm volatile("" : "+v"(P[j])); SB(); }
#define PHASE(a, P, M1, M2, L2, M3, M4, L4, M5)                                                                               \
  AE(a, 0) AE(a, 1) M1; SB(); AE(a, 2) AE(a, 3) M2; SB(); L2; SB(); AC(a, P, 0) AC(a, P, 1) AE(a, 4) M3; SB(); AE(a, 5) AE(a, 6) M4; SB(); \
  L4; SB(); AE(a, 7) AC(a, P, 2) M5; SB(); AC(a, P, 3)
#define PV(a, d, P) oa[a][d] = mfma16(vf[d], P, oa[a][d])
#define RS(a, P) sum[a] = mfma16(ones, P, sum[a])
#define SC(a, t, c) S[a][t] = mfma16(kf[t], qf[a], c)
#define K_AT(ad) (kf[0] = LDS16(ad), kf[1] = LDS16((ad) + 1024))
#define V_AT(ad) (vf[0] = LDS16((ad) + L_V), vf[1] = LDS16((ad) + L_V + 1024))
#define A_FIRST(ad, c00, c01, c10, c11, KN) { K_AT(ad); V_AT(ad); SB(); SC(0, 0, c00); SC(0, 1, c01); SB(); PHASE(0, p0, SC(1, 0, c10), SC(1, 1, c11), KN, , , , ) }
#define A_NEXT(ad, c00, c01, c10, c11, KN)                                                                                        \
  { PHASE(1, p1, PV(0, 0, p0), PV(0, 1, p0), , SC(0, 0, c00), SC(0, 1, c01), , RS(0, p0))                                     \
    PHASE(0, p0, PV(1, 0, p1), PV(1, 1, p1), V_AT(ad), SC(1, 0, c10), SC(1, 1, c11), KN, RS(1, p1)) }
#define A_LAST() { PHASE(1, p1, PV(0, 0, p0), PV(0, 1, p0), , RS(0, p0), , , ) SB(); PV(1, 0, p1); PV(1, 1, p1); RS(1, p1); }
      int nw = 0;
      auto piece = [&]() __attribute__((always_inline)) {
        if constexpr (it == 0) { if (ffn_on && nw < 8) { FFN_DMA(nw); } ++nw; }
      };
#define XN_OFF() (F_XN + (((rt - 6) * 4 + g) * RX + bi % RX) * 256 + (l & 15) * 16)
      u32x4 fxn;
      asm volatile("" : "=v"(fxn));
      unsigned ad = lbase + l * 16 + (6 + rt) * 2048;
      asm volatile("" : "+v"(ad));
      if (rt > 0) {
        A_FIRST(ad + 2048, cn[0], cn[0], cn[1], cn[1], K_AT(ad));
#pragma unroll 1
        for (int i = rt - 1; i > 0; --i) {
          ad -= 2048;
          asm volatile("" : "+v"(ad));
          if (told && rt >= 4) piece();
          announce(false);
          A_NEXT(ad + 2048, cn[0], cn[0], cn[1], cn[1], K_AT(ad));
        }
        announce(false);
        A_LAST();
      }
      announce(true);
      {
        f32x4 mk;
#pragma unroll
        for (int e = 0; e < 4; ++e) mk[e] = (4 * g + e > ql) ? 0.f : -INFINITY;
        f32x4 cm0 = cn[0] + mk, cm1 = cn[1] + mk, cmi = ninf;
        asm volatile("" : "+v"(cm0), "+v"(cm1), "+v"(cmi));
        if (rt >= 4) {
          if (p > 0) {
            while (!__all(__hip_atomic_load(rdy + 8 + (l & 3), __ATOMIC_RELAXED, __HIP_MEMORY_SCOPE_WORKGROUP) == rtag)) __builtin_amdgcn_s_sleep(1);
            asm volatile("" ::: "memory");
          }
          if constexpr (it == 0) { for (; nw < 8;) piece(); }
          static_for<it ? 12 : 8>([&](auto c_) __attribute__((always_inline)) { constexpr int i = decltype(c_)::value; WO_PIECE_W(15 - i / 2, 2 * (rt & 3) + (i & 1)); });
          if (it == 0 && rt >= 6) ld16h<0>(fxn, rs, XN_OFF(), 0);
        } else if (p > 0) {
          if (!first) fetch();
          DRAIN();
          if (l < 2) rdy[rt + 4 * l] = rtag;
          if constexpr (it == 0) { for (; nw <= rt;) piece(); }
        }
        if (p > 0) {
          unsigned have = 0;
          auto need = [&](int gq) __attribute__((always_inline)) {
            if (__builtin_expect(gq < 8 && !((have >> gq) & 1), 0)) {
              do {
                const unsigned t = __hip_atomic_load(rdy + (l & 7), __ATOMIC_RELAXED, __HIP_MEMORY_SCOPE_WORKGROUP);
                have = (unsigned)__builtin_amdgcn_ballot_w64(t == rtag);
              } while (!((have >> gq) & 1));
              asm volatile("" ::: "memory");
            }
          };
          ad = lbase + l * 16 + rt * 2048;
          asm volatile("" : "+v"(ad));
          need(rt);
          K_AT(ad); V_AT(ad); SB(); SC(0, 0, cm0); SC(0, 1, cn[0]); SB();
          PHASE(0, p0, SC(1, 0, cmi), SC(1, 1, cm1), , , , , )
          need(rt + 1);
          K_AT(ad + 2048);
#pragma unroll 1
          for (int gq = rt + 1; gq < 8; ++gq) {
            ad += 2048;
            asm volatile("" : "+v"(ad));
            piece();
            need(gq + 1);
            A_NEXT(ad, cn[0], cn[0], cn[1], cn[1], K_AT(ad + 2048));
          }
          A_LAST();
        }
        if constexpr (it == 0) { for (; nw < 8;) piece(); }
      }
      auto o_store = [&]() __attribute__((always_inline)) {
        const unsigned oo = OFF_O + it * (4u << 20) + ((p * 8 + rt) * 64 + h * 2) * 1024;
#pragma unroll
        for (int a = 0; a < 2; ++a) {
          f32x2 inv = {__builtin_amdgcn_rcpf(sum[a][0]), 0.f};
          inv[1] = inv[0];
          asm volatile("" : "+v"(inv));
          const f32x4 i4 = {inv[0], inv[1], inv[0], inv[1]};
          st16(pack8(oa[a][0] * i4, oa[a][1] * i4), rs, l * 16 + a * 1024, oo);
        }
      };
      {
        float big = 0.f;
#pragma unroll
        for (int a = 0; a < 2; ++a)
#pragma unroll
          for (int d = 0; d < 2; ++d)
            big = __builtin_elementwise_maximum(__builtin_elementwise_maximum(big, __builtin_elementwise_maximum(fabsf(oa[a][d][0]), fabsf(oa[a][d][1]))),
                                                __builtin_elementwise_maximum(fabsf(oa[a][d][2]), fabsf(oa[a][d][3])));
        bool ok = sum[0][0] >= 0.5f && sum[0][0] < 0x1p64f && sum[1][0] >= 0.5f && sum[1][0] < 0x1p64f && big < INFINITY;
        if (!__all(ok)) {
#pragma unroll
          for (int a = 0; a < 2; ++a) {
            const int qs = 2 * rt + a, qq = 256 + 16 * qs + ql, klo = max(qq - 255, p > 0 ? 0 : 256);
            auto score = [&](int t) __attribute__((always_inline)) {
              f32x4 s = mfma16(t == 31 ? k1 : *(const u32x4*)(kb_ + t * 1024), qf[a], z4);
#pragma unroll
              for (int e = 0; e < 4; ++e) { const int kk = 16 * t + 4 * g + e; s[e] = (kk <= qq && kk >= klo) ? s[e] : -INFINITY; }
              return s;
            };
            float mx = -INFINITY;
#pragma unroll 1
            for (int t = p > 0 ? qs : 16; t <= qs + 16; ++t) { const f32x4 s = score(t); mx = fmaxf(fmaxf(mx, fmaxf(s[0], s[1])), fmaxf(s[2], s[3])); }
            mx = max_x32(max_x16(mx));
            oa[a][0] = z4; oa[a][1] = z4; sum[a] = z4;
#pragma unroll 1
            for (int G = p > 0 ? qs >> 1 : 8; G <= (qs + 16) >> 1; ++G) {
              const f32x4 s0 = score(2 * G), s1 = score(2 * G + 1);
              f32x4 e0, e1;
#pragma unroll
              for (int e = 0; e < 4; ++e) { e0[e] = __builtin_amdgcn_exp2f(s0[e] - mx); e1[e] = __builtin_amdgcn_exp2f(s1[e] - mx); }
              const u32x4 pf = pack8(e0, e1);
              oa[a][0] = mfma16(*(const u32x4*)(vb_ + G * 2048), pf, oa[a][0]);
              oa[a][1] = mfma16(*(const u32x4*)(vb_ + G * 2048 + 1024), pf, oa[a][1]);
              sum[a] = mfma16(ones, pf, sum[a]);
            }
          }
        }
      }
      o_store();
      if constexpr (it == 0) {
        wait_vm<2>();
        asm volatile("" : "+v"(fxn)::"memory");
        if (rt >= 6 && !__all(fxn[0] == T[0] && fxn[1] == T[1] && fxn[2] == T[0] && fxn[3] == T[1])) poll_flags2(rs, XN_OFF(), T);
        DEF4(gv); DEF4(uv);
        if (ffn_on) {
          const unsigned ro = 65536 + w * 1024 + (4 * g + ((l & 15) >> 2)) * 32 + (l & 3) * 8;
#pragma unroll
          for (int i = 0; i < 4; ++i) {
            frag_t f;
            f.hf.a = trread(smem, ro + i * 16384); f.hf.b = trread(smem, ro + i * 16384 + 512);
            gv[i] = f.v;
            f.hf.a = trread(smem, ro + i * 16384 + 8192); f.hf.b = trread(smem, ro + i * 16384 + 8704);
            uv[i] = f.v;
          }
        }
        PINV4(gv); PINV4(uv);
      }
      BAR();
      if constexpr (it == 1) { WO_PIECE(3); WO_PIECE(2); WO_PIECE(1); WO_PIECE(0); }
      wait_vm<it ? 4 : 0>();
      asm volatile("s_barrier" ::: "memory");
      if (tid == 0) st8(T, rs, h * SO, F_O + (it * 8 + p) * 32 * SO);
      if constexpr (it == 0) {
        WO_PIECE(11); WO_PIECE(10); WO_PIECE(9); WO_PIECE(8); WO_PIECE(7); WO_PIECE(6); WO_PIECE(5); WO_PIECE(4);
        WO_PIECE(3); WO_PIECE(2); WO_PIECE(1); WO_PIECE(0);
      }
    }
    IDS();
    ARGP(pwr, 4); ARGP(pwd, 7); ARGP(pout, 8); ARGP(px, 0);
    const int kh = w >> 2, rt2 = rb * 4 + (w & 3);
    const unsigned R = rt2 * 32 + kh * 16 + (l & 15);
    const unsigned to = (rt2 * 64 + ct * 4 + kh) * 1024 + l * 16;
    const unsigned rmo = ct * 64 + ((g & 1) ? 12 + 4 * g : 4 * g);
    f32x2 wv[8];
    {
      u32x4 wa[2], wrp[2], res[2];
#pragma unroll
      for (int j = 0; j < 2; ++j) {
        wrp[j] = *(const GAS u32x4*)(pwr + rmo + 32 * j);
        if constexpr (it == 0) wa[j] = *(const GAS u32x4*)(pwr + l * 8 + j * 512);
        if constexpr (it == 0) res[j] = *(const GAS u32x4*)(px + (R - (l & 15) + 8 * j + (l >> 3)) * 1024 + ct * 64 + (l & 7) * 8);
        else res[j] = ld16(rs, OFF_XNEW + (2 * ct + j) * 256 + g * 16, 0);
      }
      SB();
      unsigned bt[4];
#pragma unroll
      for (int n = 0; n < 4; ++n) {
        bt[n] = lbase + 32768 + 65536 + kh * 4096 + (4 * g + ((l & 15) >> 2)) * 128 + (l & 3) * 8 + ((n ^ (2 * (g & 1) + ((l & 15) >> 3))) << 5);
        asm volatile("" : "+v"(bt[n]));
      }
      if (w == 0) poll_flags(rs, (l & 31) * SO, F_O + (it * 8 + p) * 32 * SO, T);
      asm volatile("s_barrier" ::: "memory");
      auto prep = [&]() __attribute__((always_inline)) {
        if constexpr (it == 0) {
          f32x2 t[8];
          unpack8v(wa[0], t); unpack8v(wa[1], t + 4);
          const f32x2 s = ((t[0] + t[1]) + (t[2] + t[3])) + ((t[4] + t[5]) + (t[6] + t[7]));
          wsum = sum64(hadd(s));
        }
        unpack8v(trade16(wrp[0]), wv); unpack8v(trade16(wrp[1]), wv + 4);
        if constexpr (it == 0) {
          char* tp = smem + w * 2048;
          const unsigned c0 = (g & 1) * 2 + (g >> 1), r = l & 15;
#pragma unroll
          for (int j = 0; j < 2; ++j) *(u32x4*)(tp + j * 1024 + (l >> 3) * 128 + (((l ^ (l >> 3)) & 7) << 4)) = res[j];
          res[0] = trade16(*(const u32x4*)(tp + r * 128 + ((c0 ^ (r & 7)) << 4)));
          res[1] = trade16(*(const u32x4*)(tp + r * 128 + (((c0 + 4) ^ (r & 7)) << 4)));
        }
        else { res[0] = R == sel0 ? res[0] : x2p[0]; res[1] = R == sel0 ? res[1] : x2p[1]; }
        unpack8v(res[0], v + 0); unpack8v(res[1], v + 4);
        asm volatile("" : "+v"(v[0]), "+v"(v[1]), "+v"(v[2]), "+v"(v[3]), "+v"(v[4]), "+v"(v[5]), "+v"(v[6]), "+v"(v[7]));
        asm volatile("" : "+v"(wsum), "+v"(wv[0]), "+v"(wv[1]), "+v"(wv[2]), "+v"(wv[3]), "+v"(wv[4]), "+v"(wv[5]), "+v"(wv[6]), "+v"(wv[7]));
      };
      if constexpr (it == 1 && WON) {
        const unsigned abase = OFF_O + (4u << 20) + (rt2 * 64 + kh) * 1024;
        u32x4 af[W2B], bfr[W2L + 1][4];
        unsigned b2[2] = {lbase + 32768 + l * 16, lbase + 98304 + l * 16};
        asm volatile("" : "+v"(b2[0]), "+v"(b2[1]));
#define O_LOAD(s_) af[(s_) % W2B] = ld16(rs, l * 16 + (~(s_) & 1) * 2048, abase + ((31 - (s_)) >> 1) * 4096)
#define WO_RD(s_, n) bfr[(s_) % (W2L + 1)][n] = *(const LDS_AS u32x4*)(size_t)(b2[(31 - (s_)) >> 4] + ((31 - (s_)) & 15) * 4096 + (n) * 1024)
#pragma unroll
        for (int s = 0; s < W2R; ++s) O_LOAD(s);
        SB();
        prep();
        SB();
        f32x4 ac[4];
#pragma unroll
        for (int n = 0; n < 4; ++n) ac[n] = f32x4{v[2 * n][0], v[2 * n][1], v[2 * n + 1][0], v[2 * n + 1][1]};
#pragma unroll
        for (int n = 0; n < 4 * W2L; ++n) WO_RD(n >> 2, n & 3);
#pragma unroll
        for (int s = 0; s < 32; ++s) {
          if (s == W2P && w >= 4) __builtin_amdgcn_s_setprio(1);
          if (s == 24 - W2L) asm volatile("s_barrier" ::: "memory");
          if (s < W2B - W2R) O_LOAD(W2R + 2 * s);
          SB();
#pragma unroll
          for (int n = 0; n < 4; ++n) {
            ac[n] = mfma16(bfr[s % (W2L + 1)][n], af[s % W2B], ac[n]);
            if (s + W2L < 32) WO_RD(s + W2L, n);
            SB();
          }
          if (s < W2B - W2R) O_LOAD(W2R + 2 * s + 1);
          else if (s + W2B < 32) O_LOAD(s + W2B);
          SB();
        }
        __builtin_amdgcn_s_setprio(0);
#undef WO_RD
#undef O_LOAD
#pragma unroll
        for (int n = 0; n < 4; ++n) { v[2 * n] = f32x2{ac[n][0], ac[n][1]}; v[2 * n + 1] = f32x2{ac[n][2], ac[n][3]}; }
      } else {
      const unsigned abase = OFF_O + it * (4u << 20) + (rt2 * 64 + 32 * kh) * 1024;
      const unsigned av0 = l * 16 + kh * 1024, av1 = l * 16 + (kh ^ 1) * 1024;
      u32x4 af[WB][2];
#define O_LOAD(s_) { af[(s_) % WB][0] = ld16(rs, av0 + (~(s_) & 1) * 2048, abase + ((15 - (s_)) >> 1) * 4096); af[(s_) % WB][1] = ld16(rs, av1 + (~(s_) & 1) * 2048, abase + ((15 - (s_)) >> 1) * 4096); }
#pragma unroll
      for (int s = 0; s < WR; ++s) O_LOAD(s);
      SB();
      prep();
      SB();
      f32x4 ac[2][4];
#pragma unroll
      for (int a = 0; a < 2; ++a)
#pragma unroll
        for (int n = 0; n < 4; ++n) ac[a][n] = f32x4{0.f, 0.f, 0.f, 0.f};
      u32x4 bfr[WLA + 1][4];
#define WO_RD(buf, s, n)                                                                             \
  {                                                                                                  \
    const unsigned o_ = bt[n] + ((15 - (s)) & 7) * 8192;     \
    frag_t f_;                                                                                       \
    f_.hf.a = trabs(o_); f_.hf.b = trabs(o_ + 2048);                                                 \
    bfr[buf][n] = f_.v;                                                                              \
  }
#pragma unroll
      for (int n = 0; n < 4 * WLA; ++n) WO_RD(n >> 2, n >> 2, n & 3);
#pragma unroll
      for (int s = 0; s < 16; ++s) {
        const int cur = s % (WLA + 1);
        if (WPR && s == WPR && w >= 4) __builtin_amdgcn_s_setprio(1);
        asm volatile("" : "+{v[176:179]}"(ac[0][0]), "+{v[180:183]}"(ac[0][1]), "+{v[184:187]}"(ac[0][2]), "+{v[188:191]}"(ac[0][3]),
                          "+{v[192:195]}"(ac[1][0]), "+{v[196:199]}"(ac[1][1]), "+{v[200:203]}"(ac[1][2]), "+{v[204:207]}"(ac[1][3]));
        if (s == (it == 0 ? 4 : 12) - WLA) asm volatile("s_barrier" ::: "memory");
        if (s == 8 - WLA) {
#pragma unroll
          for (int n = 0; n < 4; ++n) { bt[n] -= 65536; asm volatile("" : "+v"(bt[n])); }
        }
        if (it == 0 && s == WIS) {
#pragma unroll
          for (int i = 0; i < 3; ++i) st16(keep[i], rs, l * 16 + i * 1024, OFF_WI + (h * 8 + ((ROT0 + 5) & 7)) * STAGE + w * 3072);
        }
        if (it == 0 && s == WPS) {
          unsigned pz1 = POISON;
          asm volatile("" : "+v"(pz1));
          const u32x4 pz = {pz1, pz1, pz1, pz1};
          st16(pz, rs, OFF_PART + (tid >> 8) * (1u << 20) + bid * 4096 + (tid & 255) * 16, 0);
          if (w < 2) st16(pz, rs, OFF_STAT + 524288 + (rb * 16 + ct) * 2048 + tid * 16, 0);
          if (w == 4 && l < 40) st16(pz, rs, l < 16 ? OFF_CA + 65536 + rb * 4096 + ct * 256 + l * 16 : OFF_FX + (h * 8 + p) * 512 + (l - 16) * 16, 0);
          if (w == 6 && l < (ct < (int)RKEY ? 3 : 2)) st4(pz1, rs, l < 2 ? OFF_XNEW + (bid >> 3) * 256 + (bid & 7) * 8 + l * 4 : KEY_AT(1, ct) + rb * SKEY, 0);
        }
        if (s < WB - WR) O_LOAD(WR + 2 * s < 16 ? WR + 2 * s : 0);
        SB();
#pragma unroll
        for (int n = 0; n < 4; ++n) {
          ac[0][n] = mfma16(bfr[cur][n], af[s % WB][0], ac[0][n]);
          ac[1][n] = mfma16(bfr[cur][n], af[s % WB][1], ac[1][n]);
          if (s + WLA < 16) WO_RD((s + WLA) % (WLA + 1), s + WLA, n);
          SB();
        }
        if (s < WB - WR) { O_LOAD(WR + 2 * s + 1); }
        else if (s + WB < 16) { O_LOAD(s + WB); }
        SB();
      }
      if (WPR) __builtin_amdgcn_s_setprio(0);
#undef WO_RD
#undef O_LOAD
      if constexpr (it == 0 && WON) {
        const int ks = 2 * rb + kh;
        const unsigned o_ = lbase + 32768 + (ks & 15) * 8192 + (ks >> 4) * 4096 + (4 * g + ((l & 15) >> 2)) * 128 + (l & 3) * 8 + (((w & 3) ^ (2 * (g & 1) + ((l & 15) >> 3))) << 5);
        frag_t f_;
        f_.hf.a = trabs(o_); f_.hf.b = trabs(o_ + 2048);
        wof = f_.v;
        asm volatile("" : "+v"(wof));
      }
#define XCH_AT(v) ((v) == 7 ? (rb == 7 ? 139264 : 155648) : (v) * 4096)
#pragma unroll
      for (int n = 0; n < 4; ++n) *(f32x4*)(smem + XCH_AT(w) + n * 1024 + l * 16) = ac[1][n];
#pragma unroll
      for (int n = 0; n < 4; ++n) { v[2 * n] += f32x2{ac[0][n][0], ac[0][n][1]}; v[2 * n + 1] += f32x2{ac[0][n][2], ac[0][n][3]}; }
      BAR();
#pragma unroll
      for (int n = 0; n < 4; ++n) {
        const f32x4 o = *(const f32x4*)(smem + XCH_AT(w ^ 4) + n * 1024 + l * 16);
        v[2 * n] += f32x2{o[0], o[1]}; v[2 * n + 1] += f32x2{o[2], o[3]};
      }
      }
    }
    {
      if (it == 0) { st16(pack8v(v), rs, to, OFF_XN + (4u << 20)); st16(pack8v(v + 4), rs, to + 2048, OFF_XN + (4u << 20)); }
      SB();
      const f32x2 s = ((v[0] + v[1]) + (v[2] + v[3])) + ((v[4] + v[5]) + (v[6] + v[7]));
      f32x2 dp = v[0] * wv[0], q = v[0] * v[0];
#pragma unroll
      for (int j = 1; j < 8; ++j) { dp = __builtin_elementwise_fma(v[j], wv[j], dp); q = __builtin_elementwise_fma(v[j], v[j], q); }
      float mt = hadd(s), qq = hadd(q), dot = hadd(dp);
      add_g3(mt, qq, dot);
      const unsigned so = OFF_STAT + it * 524288 + rb * 32768, lrow = R & 127;
      if (it == 0) wait_vm<2>();
      if (g == 0) st16(unpoison4(__builtin_bit_cast(u32x4, f32x4{mt, qq, dot, 0.f})), rs, ct * 2048 + lrow * 16, so);
      SB();
      if (it == 0) wait_vm<1>();
      if constexpr (it == 0) {
        volatile LDS_AS u32x2* in = (volatile LDS_AS u32x2*)(smem + L_XC);
        if (l == 0) in[w] = T;
        const u32x2 t = in[l & 7];
        if (__all(t[0] == T[0] && t[1] == T[1]) && l < (int)RX1) st8(T, rs, (l * 32 + (rb & 1) * 16 + ct) * SX1, F_X1 + (rb >> 1) * RX1 * 32 * SX1);
      }
      if (it == 0) {
        if (ffn_on) { WD_DMA(); }
#pragma unroll
        for (int j = 0; j < 9; ++j) *(u32x4*)(smem + L_RING + (j / 3) * STAGE + (w * 3 + j % 3) * 1024 + l * 16) = keep[j];
      }
      if constexpr (it == 0) {
        if (w == 0) poll_flags(rs, ((l & 2) * 8 + ((ROT0 + 5) & 7) * 2 + (l & 1)) * SX1, F_X1 + (p * RX1 + (xcd >> 1) % RX1) * 32 * SX1, T);
        if (tid == 192) *(LDS_AS u32x4*)(smem + L_QC) = u32x4{0u, 0u, 0u, 0u};
        BAR();
        PRE_LOAD(1, ROT0 + 5, 0, NPRE);
      } else {
        __builtin_amdgcn_s_sleep(SDIR);
        u32x4 sp[4];
#pragma unroll
        for (int i = 0; i < 4; ++i) sp[i] = ld16(rs, (4 * g + i) * 2048 + lrow * 16, so);
        const RowFin r = fin_key(rs, T, g * 8192 + lrow * 16, so, smem + L_XC, l, w, R, wsum, sp);
        BAR();
        fin_cand(rs, smem + L_XC, 1, g, rb, ct, r, v);
        fin_x2(r, v);
      }
    }
    if (it == 1) {
      u32x4 op[2];
      {
        char* tp = smem + L_OUT + w * 2048;
        const unsigned c0 = (g & 1) * 2 + (g >> 1), r = l & 15;
        *(u32x4*)(tp + r * 128 + ((c0 ^ (r & 7)) << 4)) = trade16(pack8v(v));
        *(u32x4*)(tp + r * 128 + (((c0 + 4) ^ (r & 7)) << 4)) = trade16(pack8v(v + 4));
#pragma unroll
        for (int j = 0; j < 2; ++j) op[j] = *(const u32x4*)(tp + j * 1024 + (l >> 3) * 128 + (((l ^ (l >> 3)) & 7) << 4));
      }
      const unsigned Ro = R - (l & 15) + (l >> 3), oo = (Ro * 1024 + ct * 64) * 2 + (l & 7) * 16;
#define OUT_STORE(c0, c1)                                                                           \
  {                                                                                                 \
    if (c0) __builtin_amdgcn_raw_buffer_store_b128(op[0], rout, oo, 0, SC1);                        \
    if (c1) __builtin_amdgcn_raw_buffer_store_b128(op[1], rout, oo + 16384, 0, SC1);                \
  }
      if (w == 0) {
        __builtin_amdgcn_s_sleep(GSL);
        u32x4 xr[4];
        unsigned cur = 0, best;
        asm volatile("" : "=v"(xr[0]), "=v"(xr[1]));
        for (unsigned spins = 0;;) {
          const unsigned k_ = key_load(rs, l, 1, KREP());
          const bool all = __all(k_ != POISON);
          best = __builtin_amdgcn_readfirstlane(umax16(k_ == POISON ? 0u : k_));
          if (best != cur && ffn_on) {
            cur = best;
            ld16h<0>(xr[0], rs, (l >> 3) * 256 + (l & 7) * 16, cand_off(cur, 1)); ld16h<2048>(xr[1], rs, (l >> 3) * 256 + (l & 7) * 16, cand_off(cur, 1));
          }
          if (all || spin_fail(spins, rs, T)) break;
        }
        if (l == 0) *(LDS_AS unsigned*)(smem + L_SKEY + 32) = best;
        asm volatile("s_waitcnt vmcnt(0)" : "+v"(xr[0]), "+v"(xr[1])::"memory");
        if (ffn_on) {
          word_wait<2>(rs, T, (l >> 3) * 256 + (l & 7) * 16, cand_off(best, 1), xr);
          *(LDS_AS u32x4*)(smem + L_ROW + l * 16) = xr[0]; *(LDS_AS u32x4*)(smem + L_ROW + 1024 + l * 16) = xr[1];
        }
      }
      BAR();
      const unsigned key = __builtin_amdgcn_readfirstlane(*(LDS_AS unsigned*)(smem + L_SKEY + 32)), krow = key_row(key);
      if (ffn_on) {
        u32x4 xv[4];
#pragma unroll
        for (int i = 0; i < 4; ++i) xv[i] = *(const LDS_AS u32x4*)(smem + L_ROW + w * 256 + i * 64 + g * 16);
        f32x4 ga, ua;
        ffn_gu(gv, uv, xv, ga, ua);
        ffn_gu_store(smem, l, w, ga, ua);
        BAR();
        ffn_d_store(rs, l, w, 1, slice, ffn_d_all(dfr, ffn_d_h(ffn_d_read(smem, l))));
        SB();
      }
      OUT_STORE(Ro != krow, Ro + 8 != krow);
      if (!ffn_on) {
        const unsigned G = xcd * 4 + bi - 28;
        if (G == 0 && tid == 0) st8(T, rs, F_EP, 0);
        u32x4 pv[4];
        u32x2 cxn, cx1;
        const float sig = red_gate(key);
        __builtin_amdgcn_s_sleep(RSL);
        red_load(rs, tid, 1, G, key, pv, cxn, cx1);
        red_sum(rs, smem, T, tid, w, 1, G, key, pv, cxn, cx1);
        BAR();
        if (w == 0) {
          const u32x2 ov = red_final(smem, l, sig, cxn, cx1);
          if (l < 8) __builtin_amdgcn_raw_buffer_store_b64(ov, rout, (krow * 1024 + G * 32 + l * 4) * 2, 0, 0);
        }
      }
#undef OUT_STORE
    }
  };
  layer(std::integral_constant<int, 0>{});
  layer(std::integral_constant<int, 1>{});
}

extern "C" void kernel_launch(void* const* d_in, const int* in_sizes, int n_in, void* d_out, int out_size,
                              void* d_ws, size_t ws_size, hipStream_t stream) {
  (void)hipFuncSetAttribute((const void*)mega_kernel, hipFuncAttributeMaxDynamicSharedMemorySize, LDS_BYTES);
  mega_kernel<<<256, 512, LDS_BYTES, stream>>>((const bf16*)d_in[0], (char*)d_ws, (const bf16*)d_in[1], (const bf16*)d_in[2], (const bf16*)d_in[4],
                                               (const bf16*)d_in[5], (const bf16*)d_in[6], (const bf16*)d_in[7], (bf16*)d_out);
}
```

```cpp
#include <hip/hip_runtime.h>
#include <stdint.h>
#include <utility>

typedef __bf16 bf16;
typedef __bf16 bf16x8 __attribute__((ext_vector_type(8)));
typedef __bf16 bf16x2 __attribute__((ext_vector_type(2)));
typedef float f32x4 __attribute__((ext_vector_type(4)));
typedef float f32x2 __attribute__((ext_vector_type(2)));
typedef short s16x4 __attribute__((ext_vector_type(4)));
typedef unsigned int u32x4 __attribute__((ext_vector_type(4)));
typedef unsigned int u32x2 __attribute__((ext_vector_type(2)));
#define LDS_AS __attribute__((address_space(3)))
#define GAS __attribute__((address_space(1)))
typedef __amdgpu_buffer_rsrc_t rsrc_t;

constexpr int F_ = 3584;
constexpr int SC1 = 16;

constexpr int RX = 4;
constexpr unsigned F_EP = 0, F_TMO = 128;
constexpr unsigned F_XN = 4096;
#define FLG 256, 1024, 8, 64, 4, 256, 4
constexpr unsigned FLV[7] = {FLG};
constexpr unsigned SO = FLV[0], SKV = FLV[1], SKW = FLV[2], SX1 = FLV[3], RX1 = FLV[4], SKEY = FLV[5], RKEY = FLV[6];
constexpr unsigned F_O = 8u << 20;
constexpr unsigned F_KV = 11u << 20;
constexpr unsigned F_IM = F_O + 524288;
constexpr unsigned F_WOI = F_O + 262144;
constexpr unsigned F_X1 = 9u << 20;
constexpr unsigned OFF_KEY = 10u << 20;
#define KREP() (RKEY == 16 ? bid & 15 : (bid & 7) * RKEY / 8)
#define KEY_AT(L, rep) (OFF_KEY + ((L) * RKEY + (rep)) * (SKEY < 8 ? 128 : 16 * SKEY))
constexpr unsigned OFF_AB = 2u << 20;
constexpr unsigned OFF_XNEW = 262144;
constexpr unsigned OFF_CS = OFF_AB + 65536;
constexpr unsigned OFF_FX = 393216;
constexpr unsigned OFF_CA = 524288;
constexpr unsigned OFF_STAT = 1u << 20;
constexpr unsigned OFF_PART = 3u << 20;
constexpr unsigned OFF_XN = 8u << 20;
constexpr unsigned OFF_O = 16u << 20;
constexpr unsigned OFF_KV = 32u << 20;
constexpr unsigned OFF_WI = 52u << 20;
constexpr unsigned OFF_WOI = 58u << 20;
constexpr unsigned WS_BYTES = 60u << 20;
constexpr unsigned POISON = 0xFFFFFFFFu;

constexpr int L_WOS = 20480, L_ABW = 16640, L_CSP = 17408, L_CSL = 31872, L_FXL = 32256, L_K = 0, L_V = 32768, L_RING = 65536, L_DN = 32768, L_RDY = 31744, L_QC = 31808, LDS_BYTES = 163840;
constexpr int STAGE = 24576;
#define SLOT(t) ((t) & 3)

template <typename F, int... I> __device__ __forceinline__ void static_for_impl(F&& f, std::integer_sequence<int, I...>) {
  (f(std::integral_constant<int, I>{}), ...);
}
template <int N, typename F> __device__ __forceinline__ void static_for(F&& f) { static_for_impl(f, std::make_integer_sequence<int, N>{}); }
__device__ __forceinline__ unsigned pack2(float a, float b) {
  f32x2 v = {a, b};
  return __builtin_bit_cast(unsigned, __builtin_convertvector(v, bf16x2));
}
__device__ __forceinline__ float asf(unsigned u) { return __builtin_bit_cast(float, u); }
__device__ __forceinline__ float lo16(unsigned u) { return __builtin_bit_cast(float, u << 16); }
__device__ __forceinline__ float hi16(unsigned u) { return __builtin_bit_cast(float, u & 0xffff0000u); }
__device__ __forceinline__ u32x4 pack8(const f32x4 a, const f32x4 b) {
  u32x4 o;
  o[0] = pack2(a[0], a[1]); o[1] = pack2(a[2], a[3]); o[2] = pack2(b[0], b[1]); o[3] = pack2(b[2], b[3]);
  return o;
}
__device__ __forceinline__ f32x4 mfma16(u32x4 a, u32x4 b, f32x4 c) {
  return __builtin_amdgcn_mfma_f32_16x16x32_bf16(__builtin_bit_cast(bf16x8, a), __builtin_bit_cast(bf16x8, b), c, 0, 0, 0);
}
__device__ __forceinline__ f32x4 mfma16k16(s16x4 a, s16x4 b, f32x4 c) { return __builtin_amdgcn_mfma_f32_16x16x16bf16_1k(a, b, c, 0, 0, 0); }
template <int CTRL> __device__ __forceinline__ float dppf(float v) {
  return __builtin_bit_cast(float, __builtin_amdgcn_update_dpp(0, __builtin_bit_cast(int, v), CTRL, 0xf, 0xf, false));
}
template <int CTRL> __device__ __forceinline__ unsigned dppu(unsigned v) {
  return (unsigned)__builtin_amdgcn_update_dpp(0, (int)v, CTRL, 0xf, 0xf, false);
}
__device__ __forceinline__ float add_x16(float v) {
  auto r = __builtin_amdgcn_permlane16_swap(__builtin_bit_cast(unsigned, v), __builtin_bit_cast(unsigned, v), false, false);
  return __builtin_bit_cast(float, (unsigned)r[0]) + __builtin_bit_cast(float, (unsigned)r[1]);
}
__device__ __forceinline__ float add_x32(float v) {
  auto r = __builtin_amdgcn_permlane32_swap(__builtin_bit_cast(unsigned, v), __builtin_bit_cast(unsigned, v), false, false);
  return __builtin_bit_cast(float, (unsigned)r[0]) + __builtin_bit_cast(float, (unsigned)r[1]);
}
__device__ __forceinline__ float max_x16(float v) {
  auto r = __builtin_amdgcn_permlane16_swap(__builtin_bit_cast(unsigned, v), __builtin_bit_cast(unsigned, v), false, false);
  return fmaxf(__builtin_bit_cast(float, (unsigned)r[0]), __builtin_bit_cast(float, (unsigned)r[1]));
}
__device__ __forceinline__ float max_x32(float v) {
  auto r = __builtin_amdgcn_permlane32_swap(__builtin_bit_cast(unsigned, v), __builtin_bit_cast(unsigned, v), false, false);
  return fmaxf(__builtin_bit_cast(float, (unsigned)r[0]), __builtin_bit_cast(float, (unsigned)r[1]));
}
__device__ __forceinline__ float sum_hi8(float v) {
  v += dppf<0x128>(v);
  v = add_x16(v);
  return add_x32(v);
}
__device__ __forceinline__ float sum64(float v) {
  v += dppf<0xB1>(v);
  v += dppf<0x4E>(v);
  v += dppf<0x124>(v);
  v += dppf<0x128>(v);
  v = add_x16(v);
  return add_x32(v);
}

#define BAR() asm volatile("s_waitcnt lgkmcnt(0)\n\ts_barrier" ::: "memory")
#define DRAIN() asm volatile("s_waitcnt vmcnt(0)" ::: "memory")
#define SB() __builtin_amdgcn_sched_barrier(0)
template <int N> __device__ __forceinline__ void wait_vm() { asm volatile("s_waitcnt vmcnt(%0)" ::"n"(N) : "memory"); }

__device__ __forceinline__ void dma16(rsrc_t rs, unsigned voff, unsigned soff, unsigned lds) {
  asm volatile("s_mov_b32 m0, %0\n\ts_nop 0\n\tbuffer_load_dwordx4 %1, %2, %3 offen lds" ::"s"(lds), "v"(voff), "s"(rs), "s"(soff) : "memory");
}
__device__ __forceinline__ void dma16n(rsrc_t rs, unsigned voff, unsigned soff, unsigned lds) {
  asm volatile("s_mov_b32 m0, %0\n\ts_nop 0\n\tbuffer_load_dwordx4 %1, %2, %3 offen nt lds" ::"s"(lds), "v"(voff), "s"(rs), "s"(soff) : "memory");
}
template <int OFF> __device__ __forceinline__ void dma16s(rsrc_t rs, unsigned voff, unsigned soff, unsigned lds) {
  asm volatile("s_mov_b32 m0, %0\n\ts_nop 0\n\tbuffer_load_dwordx4 %1, %2, %3 offen offset:%4 sc1 lds" ::"s"(lds), "v"(voff), "s"(rs), "s"(soff), "n"(OFF) : "memory");
}
__device__ __forceinline__ float rbf1(float x) { return __builtin_bit_cast(float, pack2(0.f, x)); }
__device__ __forceinline__ u32x4 ld16(rsrc_t rs, unsigned voff, unsigned soff) { return __builtin_amdgcn_raw_buffer_load_b128(rs, voff, soff, SC1); }
template <int OFF> __device__ __forceinline__ void ld16h(u32x4& d, rsrc_t rs, unsigned voff, unsigned soff) {
  asm volatile("buffer_load_dwordx4 %0, %1, %2, %3 offen offset:%4 sc1" : "=v"(d) : "v"(voff), "s"(rs), "s"(soff), "n"(OFF) : "memory");
}
template <int I, int OFF> __device__ __forceinline__ void ld16w(u32x4& d, rsrc_t rs, unsigned voff, unsigned soff) {
  if constexpr (I == 0) asm volatile("buffer_load_dwordx4 %0, %1, %2, %3 offen offset:%4 sc1" : "={v[128:131]}"(d) : "v"(voff), "s"(rs), "s"(soff), "n"(OFF) : "memory");
  else if constexpr (I == 1) asm volatile("buffer_load_dwordx4 %0, %1, %2, %3 offen offset:%4 sc1" : "={v[132:135]}"(d) : "v"(voff), "s"(rs), "s"(soff), "n"(OFF) : "memory");
  else if constexpr (I == 2) asm volatile("buffer_load_dwordx4 %0, %1, %2, %3 offen offset:%4 sc1" : "={v[136:139]}"(d) : "v"(voff), "s"(rs), "s"(soff), "n"(OFF) : "memory");
  else if constexpr (I == 3) asm volatile("buffer_load_dwordx4 %0, %1, %2, %3 offen offset:%4 sc1" : "={v[140:143]}"(d) : "v"(voff), "s"(rs), "s"(soff), "n"(OFF) : "memory");
  else if constexpr (I == 4) asm volatile("buffer_load_dwordx4 %0, %1, %2, %3 offen offset:%4 sc1" : "={v[144:147]}"(d) : "v"(voff), "s"(rs), "s"(soff), "n"(OFF) : "memory");
  else if constexpr (I == 5) asm volatile("buffer_load_dwordx4 %0, %1, %2, %3 offen offset:%4 sc1" : "={v[148:151]}"(d) : "v"(voff), "s"(rs), "s"(soff), "n"(OFF) : "memory");
  else if constexpr (I == 6) asm volatile("buffer_load_dwordx4 %0, %1, %2, %3 offen offset:%4 sc1" : "={v[152:155]}"(d) : "v"(voff), "s"(rs), "s"(soff), "n"(OFF) : "memory");
  else if constexpr (I == 7) asm volatile("buffer_load_dwordx4 %0, %1, %2, %3 offen offset:%4 sc1" : "={v[156:159]}"(d) : "v"(voff), "s"(rs), "s"(soff), "n"(OFF) : "memory");
  else if constexpr (I == 8) asm volatile("buffer_load_dwordx4 %0, %1, %2, %3 offen offset:%4 sc1" : "={v[160:163]}"(d) : "v"(voff), "s"(rs), "s"(soff), "n"(OFF) : "memory");
  else if constexpr (I == 9) asm volatile("buffer_load_dwordx4 %0, %1, %2, %3 offen offset:%4 sc1" : "={v[164:167]}"(d) : "v"(voff), "s"(rs), "s"(soff), "n"(OFF) : "memory");
  else if constexpr (I == 10) asm volatile("buffer_load_dwordx4 %0, %1, %2, %3 offen offset:%4 sc1" : "={v[168:171]}"(d) : "v"(voff), "s"(rs), "s"(soff), "n"(OFF) : "memory");
  else if constexpr (I == 11) asm volatile("buffer_load_dwordx4 %0, %1, %2, %3 offen offset:%4 sc1" : "={v[172:175]}"(d) : "v"(voff), "s"(rs), "s"(soff), "n"(OFF) : "memory");
}
template <int K> __device__ __forceinline__ void pin_w(u32x4& a, u32x4& b) {
  if constexpr (K == 0) asm volatile("" : "+{v[128:131]}"(a), "+{v[132:135]}"(b)::"memory");
  else if constexpr (K == 1) asm volatile("" : "+{v[136:139]}"(a), "+{v[140:143]}"(b)::"memory");
  else if constexpr (K == 2) asm volatile("" : "+{v[144:147]}"(a), "+{v[148:151]}"(b)::"memory");
  else if constexpr (K == 3) asm volatile("" : "+{v[152:155]}"(a), "+{v[156:159]}"(b)::"memory");
  else if constexpr (K == 4) asm volatile("" : "+{v[160:163]}"(a), "+{v[164:167]}"(b)::"memory");
  else if constexpr (K == 5) asm volatile("" : "+{v[168:171]}"(a), "+{v[172:175]}"(b)::"memory");
}
__device__ __forceinline__ void ld8h(u32x2& d, rsrc_t rs, unsigned voff, unsigned soff) {
  asm volatile("buffer_load_dwordx2 %0, %1, %2, %3 offen sc1" : "=v"(d) : "v"(voff), "s"(rs), "s"(soff) : "memory");
}
template <int OFF> __device__ __forceinline__ void ld4h(unsigned& d, rsrc_t rs, unsigned voff, unsigned soff) {
  asm volatile("buffer_load_dword %0, %1, %2, %3 offen offset:%4 sc1" : "=v"(d) : "v"(voff), "s"(rs), "s"(soff), "n"(OFF) : "memory");
}
__device__ __forceinline__ u32x2 ld8(rsrc_t rs, unsigned voff, unsigned soff) { return __builtin_amdgcn_raw_buffer_load_b64(rs, voff, soff, SC1); }
__device__ __forceinline__ void st16(u32x4 v, rsrc_t rs, unsigned voff, unsigned soff) { __builtin_amdgcn_raw_buffer_store_b128(v, rs, voff + soff, 0, SC1); }
__device__ __forceinline__ void st8(u32x2 v, rsrc_t rs, unsigned voff, unsigned soff) { __builtin_amdgcn_raw_buffer_store_b64(v, rs, voff, soff, SC1); }

__device__ __forceinline__ unsigned ld4(rsrc_t rs, unsigned voff, unsigned soff) { return __builtin_amdgcn_raw_buffer_load_b32(rs, voff, soff, SC1); }
__device__ __forceinline__ void st4(unsigned v, rsrc_t rs, unsigned voff, unsigned soff) { __builtin_amdgcn_raw_buffer_store_b32(v, rs, voff, soff, SC1); }

constexpr unsigned kSpinCap = 1u << 16;
__device__ __forceinline__ bool spin_fail(unsigned& spins, rsrc_t rs, u32x2 T) {
  asm volatile("" ::: "memory");
  ++spins;
  if (spins > kSpinCap) { st8(T, rs, 0, F_TMO); return true; }
  if ((spins & 255u) == 0u) {
    const u32x2 v = ld8(rs, 0, F_TMO);
    if (v[0] == T[0] && v[1] == T[1]) return true;
  }
  __builtin_amdgcn_s_sleep(3);
  return false;
}
__device__ __forceinline__ void poll_flags2(rsrc_t rs, unsigned voff, u32x2 T) {
  for (unsigned spins = 0;;) {
    const u32x4 v = ld16(rs, voff, 0);
    if (__all(v[0] == T[0] && v[1] == T[1] && v[2] == T[0] && v[3] == T[1])) break;
    if (spin_fail(spins, rs, T)) break;
  }
  asm volatile("" ::: "memory");
  __builtin_amdgcn_fence(__ATOMIC_ACQUIRE, "wavefront");
}
__device__ __forceinline__ void tag_wait(rsrc_t rs, u32x2 T, unsigned voff, unsigned soff, u32x4& v) {
  if (__builtin_expect(!__all(v[2] == T[0] && v[3] == T[1]), 0)) {
    unsigned spins = 0;
    do {
      if (spin_fail(spins, rs, T)) break;
      v = ld16(rs, voff, soff);
    } while (!__all(v[2] == T[0] && v[3] == T[1]));
    asm volatile("s_waitcnt vmcnt(0)" : "+v"(v)::"memory");
  }
}
template <int N> __device__ __forceinline__ void word_wait(rsrc_t rs, u32x2 T, unsigned voff, unsigned soff, u32x4 (&v)[4]) {
#define WW_OK() __all(max(max(max(v[0][0], v[0][1]), max(v[0][2], v[0][3])), max(max(v[N - 1][0], v[N - 1][1]), max(v[N - 1][2], v[N - 1][3]))) != POISON)
  if (__builtin_expect(!WW_OK(), 0)) {
    unsigned spins = 0;
    do {
      if (spin_fail(spins, rs, T)) break;
#pragma unroll
      for (int i = 0; i < N; ++i) v[i] = ld16(rs, voff + i * 2048, soff);
    } while (!WW_OK());
    asm volatile("s_waitcnt vmcnt(0)" : "+v"(v[0]), "+v"(v[N - 1])::"memory");
  }
#undef WW_OK
}
__device__ __forceinline__ void poll_flags(rsrc_t rs, unsigned voff, unsigned soff, u32x2 T) {
  for (unsigned spins = 0;;) {
    const u32x2 v = ld8(rs, voff, soff);
    if (__all(v[0] == T[0] && v[1] == T[1])) break;
    if (spin_fail(spins, rs, T)) break;
  }
  asm volatile("" ::: "memory");
  __builtin_amdgcn_fence(__ATOMIC_ACQUIRE, "wavefront");
}

__device__ __forceinline__ void unpack8v(const u32x4 u, f32x2* v) {
#pragma unroll
  for (int j = 0; j < 4; ++j) v[j] = f32x2{lo16(u[j]), hi16(u[j])};
}
__device__ __forceinline__ u32x4 pack8v(const f32x2* v) {
  u32x4 o;
#pragma unroll
  for (int j = 0; j < 4; ++j) o[j] = pack2(v[j][0], v[j][1]);
  return o;
}
__device__ __forceinline__ float ln_rd(float var) { return __builtin_amdgcn_rcpf(rbf1(__builtin_amdgcn_sqrtf(rbf1(rbf1(var) + 1e-5f)))); }
__device__ __forceinline__ float hadd(f32x2 v) {
  float a = v[0];
  asm volatile("" : "+v"(a));
  return a + v[1];
}
__device__ __forceinline__ float add_g(float v) { return add_x32(add_x16(v)); }
__device__ __forceinline__ void add_g3(float& x, float& y, float& z) {
  auto a = __builtin_amdgcn_permlane16_swap(__builtin_bit_cast(unsigned, x), __builtin_bit_cast(unsigned, y), false, false);
  auto b = __builtin_amdgcn_permlane16_swap(__builtin_bit_cast(unsigned, z), __builtin_bit_cast(unsigned, z), false, false);
  const float r = asf(a[0]) + asf(a[1]), t = asf(b[0]) + asf(b[1]);
  auto c = __builtin_amdgcn_permlane32_swap(__builtin_bit_cast(unsigned, r), __builtin_bit_cast(unsigned, r), false, false);
  auto d = __builtin_amdgcn_permlane32_swap(__builtin_bit_cast(unsigned, t), __builtin_bit_cast(unsigned, t), false, false);
  const float u = asf(c[0]) + asf(c[1]);
  z = asf(d[0]) + asf(d[1]);
  auto e = __builtin_amdgcn_permlane16_swap(__builtin_bit_cast(unsigned, u), __builtin_bit_cast(unsigned, u), false, false);
  x = asf(e[0]); y = asf(e[1]);
}
__device__ __forceinline__ u32x4 trade16(u32x4 v) {
  auto a = __builtin_amdgcn_permlane16_swap(v[0], v[2], false, false);
  auto b = __builtin_amdgcn_permlane16_swap(v[1], v[3], false, false);
  return u32x4{(unsigned)a[0], (unsigned)b[0], (unsigned)a[1], (unsigned)b[1]};
}
__device__ __forceinline__ unsigned umax4(u32x4 v) { return max(max(v[0], v[1]), max(v[2], v[3])); }
__device__ __forceinline__ u32x2 unpoison2(u32x2 v) { return u32x2{min(v[0], POISON - 1), min(v[1], POISON - 1)}; }
__device__ __forceinline__ u32x4 unpoison4(u32x4 v) {
  return u32x4{min(v[0], POISON - 1), min(v[1], POISON - 1), min(v[2], POISON - 1), min(v[3], POISON - 1)};
}

typedef union { struct { s16x4 a, b; } hf; u32x4 v; } frag_t;
__device__ __forceinline__ s16x4 trread(const char* smem, unsigned off) {
  return __builtin_amdgcn_ds_read_tr16_b64_v4i16((LDS_AS s16x4*)(smem + off));
}
__device__ __forceinline__ s16x4 trabs(unsigned addr) { return __builtin_amdgcn_ds_read_tr16_b64_v4i16((LDS_AS s16x4*)(size_t)addr); }

constexpr int WA = 6;
#define WPS 16
#define CH 0, 6, 8, 9, 15, 18, 23, 25, 29, 31, 4, 4
constexpr int CHV[12] = {CH};
constexpr int FKL = CHV[10], FSN = CHV[11];
constexpr int SPL = CHV[0];
constexpr int FSL = CHV[1], FS0 = CHV[2], FS0D = CHV[3], FS1 = CHV[4], FS1S = CHV[5];
constexpr int FXA = CHV[6], FXB = CHV[7], FXL = CHV[8], FXS = CHV[9];
#define ROT0 p
#define L0S 22, 25, 1, 0, 24, 28, 1, 2, 4, 14
constexpr int L0V[10] = {L0S};
constexpr int CSL = L0V[0], CSS = L0V[1], CSD = L0V[2];
constexpr int abl(int it) { return it ? 25 : L0V[4]; }
constexpr int abs_(int it) { return it ? 27 : L0V[5]; }
constexpr int W2S = L0V[6], WSH = L0V[7];
constexpr int PRE0 = 12;
constexpr int LNL = L0V[3], LNS = L0V[8], POI = L0V[9];
#define TCH 22, 3, 0x07, 26, 0xF8, 0, 1
constexpr int TCV[7] = {TCH};
constexpr int TS0 = TCV[0], TSN = TCV[1], TM0 = TCV[2], TEX = TCV[3], TM1 = TCV[4], TSD = TCV[5], TSG = TCV[6];
constexpr int QL = 26;
#define SLP 18, 4, 28, 3, 0, 0
constexpr int SLV[6] = {SLP};
constexpr int SDIR = SLV[0], GSL = SLV[1], RSL = SLV[2], KSL = SLV[3];
#define IME 5, 7, 12, 14, 1, 2
constexpr int IMV[6] = {IME};
constexpr int IMB = IMV[4], IMD = IMV[5];
constexpr int im_st(int s, int n) { return IMB ? (s == CSD + 1 && (n & 1) ? n >> 1 : -1) : (s > CSD && s <= CSD + 3 && n == 5 ? s - CSD - 1 : -1); }
constexpr int IMT = IMV[0], IMF = IMV[1], IML = IMV[2], IMK = IMV[3];
constexpr int tp0(int it) { return it ? 3 : 3 + WSH; }
static_assert(((IMF + 1) & ~3) - 2 >= CSD + (IMB ? 5 : 7) && (IMK & 3) == 2 && IMK < 4 * IMT - 12 + 3 + WSH + IMD && WSH + IMD <= 4, "a wait for a younger request and a barrier in between");
constexpr int tile_at(int it, int t) { return 4 * t - 12 + tp0(it) + (!it && t == IMT ? IMD : !it && t == IMT + 1 && IMD > 1 ? IMD - 1 : 0); }
constexpr int piece_tile(int it, int s) { for (int t = 3; t < 8; ++t) if (s >= tile_at(it, t) && s < tile_at(it, t) + 3) return t; return -1; }
constexpr bool tile_piece(int it, int s) { return piece_tile(it, s) >= 0; }
#define NPRE 3
#define QAD 4
#define WO2 5, 3, 2, 8
constexpr int W2V[4] = {WO2};
#define WOE 1, 7, 15, 19, 22
constexpr int WEV[5] = {WOE};
constexpr int WOL = WEV[0], WOS = WEV[1], WOF = WEV[2], WOC = WEV[3], WOK = WEV[4];
static_assert(((WOS + 1) & ~3) - 2 >= WOL + 4 && ((WOF + 1) & ~3) - 2 >= WOS + 5 && (WOK & 3) == 2, "a wait for a younger request and a barrier in between");
constexpr int W2B = W2V[0], W2R = W2V[1], W2L = W2V[2], W2P = W2V[3];
constexpr int a_req(int s, int n) {
  int r = 2 * NPRE;
  for (int u = 1; u < 192; u += 2) {
    const bool go = r < 64 && (r >> 1) <= u / 6 + QAD && (r < 8 || u >= 9);
    if (u == 6 * s + n) return go ? r : -1;
    r += go;
  }
  return -1;
}
constexpr int vm_n(int it, int s, int n) {
  int c = it ? (a_req(s, n) >= 0) + (s == 0 && n == 0) : (s + 5 < 32 && (n == 2 || n == 3));
  if (n == 0) c += tile_piece(it, s);
  if (n == 4) c += (s == abl(it));
  if (!it) c += (s >= W2S && s < W2S + 3 && n == 4) + (s == LNL && n == 5 ? 3 : 0) + (s == 0 && n < 2 ? 2 : 0) + (s == LNS + 2 && n == 5) + (s == TEX && n == 5) + (s == WOL && n == 5) + (s == WOS + 1 && n == 5) + (im_st(s, n) >= 0);
  else c += (s == 2 && n == 2) + (s == (SPL < 0 ? 0 : SPL) && n == (SPL < 0 ? 1 : 4) ? 4 : 0) + (s >= 27 && s < 30 && (n == 0 || n == 3));
  return c;
}
constexpr int vm_at(int it, int s, int n) {
  int c = it ? 2 * NPRE : PRE0;
  for (int u = 0; u < 6 * s + n; ++u) c += vm_n(it, u / 6, u % 6);
  return c;
}
constexpr int vm_a(int it, int k) {
  if (k < NPRE) return 2 * k + 2;
  for (int u = 0; u < 192; ++u)
    if (a_req(u / 6, u % 6) == 2 * k + 1) return vm_at(it, (u + 1) / 6, (u + 1) % 6);
  return 0;
}
constexpr int vm_tile(int it, int t) { return t >= 3 ? vm_at(it, tile_at(it, t) + 2, 1) : t == 1 ? 12 : vm_at(0, W2S + 2, 5); }
constexpr int vm_unit(int u) { return u < 3 ? 5 + 2 * u : u < 5 ? vm_at(0, 0, u - 2) : vm_at(0, u - 5, 4); }
constexpr int unit_slot(int u) { return u % 3 == 0 ? 0 : 16384 + (u % 3) * 16384; }

constexpr int L_XC = LDS_BYTES - 64, L_SKEY = 32704, L_ROW = 1024, L_OUT = 65536, L_SRED = 4096, L_SFIN = 20480;
__device__ __forceinline__ unsigned key_load(rsrc_t rs, int l, int L, int rep) { return ld4(rs, (l & 15) * SKEY, KEY_AT(L, rep)); }
__device__ __forceinline__ unsigned umax16(unsigned k) {
  k = max(k, dppu<0xB1>(k));
  k = max(k, dppu<0x4E>(k));
  k = max(k, dppu<0x141>(k));
  return max(k, dppu<0x140>(k));
}
__device__ __forceinline__ unsigned key_row(unsigned key) { return (0xffffu - (key & 0xffffu)) & 2047u; }
__device__ __forceinline__ unsigned key_wait(rsrc_t rs, u32x2 T, int l, int L, int rep, unsigned kv) {
  if (!__all(kv != POISON)) {
    unsigned spins = 0;
    do {
      if (spin_fail(spins, rs, T)) break;
      if (L == 0) __builtin_amdgcn_s_sleep(KSL);
      kv = key_load(rs, l, L, rep);
    } while (!__all(kv != POISON));
  }
  return __builtin_amdgcn_readfirstlane(umax16(kv));
}
__device__ __forceinline__ unsigned cand_off(unsigned key, int L) { return OFF_CA + L * 65536 + (key_row(key) >> 7) * 4096; }
#define XO(i) (((i) >> 1) * 256 + ((i) & 1) * 64)
__device__ __forceinline__ void ffn_gu(const u32x4 (&gv)[4], const u32x4 (&uv)[4], const u32x4 (&xv)[4], f32x4& ga, f32x4& ua) {
  ga = mfma16(gv[0], xv[0], f32x4{0.f, 0.f, 0.f, 0.f}); ua = mfma16(uv[0], xv[0], f32x4{0.f, 0.f, 0.f, 0.f});
#pragma unroll
  for (int i = 1; i < 4; ++i) { ga = mfma16(gv[i], xv[i], ga); ua = mfma16(uv[i], xv[i], ua); }
}
__device__ __forceinline__ void ffn_gu_store(char* smem, int l, int w, f32x4 ga, f32x4 ua) {
  LDS_AS f32x4* sred = (LDS_AS f32x4*)(smem + L_SRED);
  if ((l & 15) == 0) { sred[w * 8 + (l >> 4)] = ga; sred[w * 8 + 4 + (l >> 4)] = ua; }
}
__device__ __forceinline__ f32x4 ffn_d_read(char* smem, int l) {
  const LDS_AS float* t = (const LDS_AS float*)(smem + L_SRED) + (l & 8) * 16 + (l & 4) * 4 + (l >> 4) * 4 + (l & 3);
  return f32x4{t[0], t[32], t[64], t[96]};
}
__device__ __forceinline__ u32x2 ffn_d_h(f32x4 t) {
  float x = t[0] + t[1];
  asm volatile("" : "+v"(x));
  x += t[2] + t[3];
  x += dppf<0x128>(x);
  const float y = dppf<0x124>(x);
  const bool up = __builtin_amdgcn_inverse_ballot_w64(0xF0F0F0F0F0F0F0F0ull);
  const float gg = rbf1(up ? y : x), uu = rbf1(up ? x : y);
  const float h = rbf1(gg * __builtin_amdgcn_rcpf(1.0f + __expf(-gg))) * uu;
  const unsigned p = pack2(h, dppf<0xB1>(h));
  return u32x2{dppu<0x00>(p), dppu<0xAA>(p)};
}
template <int t> __device__ __forceinline__ f32x4 ffn_d_mma(s16x4 d, u32x2 hh, f32x4 y) {
  const bool me = __builtin_amdgcn_inverse_ballot_w64(0x0001000100010001ull << t);
  return mfma16k16(d, __builtin_bit_cast(s16x4, u32x2{me ? hh[0] : 0u, me ? hh[1] : 0u}), y);
}
__device__ __forceinline__ f32x4 ffn_d_all(const s16x4 (&dfr)[8], u32x2 hh) {
  f32x4 a = {0.f, 0.f, 0.f, 0.f}, b = a;
  a = ffn_d_mma<0>(dfr[0], hh, a); b = ffn_d_mma<1>(dfr[1], hh, b); a = ffn_d_mma<2>(dfr[2], hh, a); b = ffn_d_mma<3>(dfr[3], hh, b);
  a = ffn_d_mma<4>(dfr[4], hh, a); b = ffn_d_mma<5>(dfr[5], hh, b); a = ffn_d_mma<6>(dfr[6], hh, a); b = ffn_d_mma<7>(dfr[7], hh, b);
  return a + b;
}
__device__ __forceinline__ void ffn_d_store(rsrc_t rs, int l, int w, int L, int slice, f32x4 y) {
  const unsigned c = l & 15;
  if (c < 8) st16(unpoison4(__builtin_bit_cast(u32x4, y)), rs, (2 * w + (c >> 2)) * 65536 + slice * 256 + (c & 3) * 64 + (l >> 4) * 16, OFF_PART + L * (1u << 20));
}
#define RED_VO(k) ((min(tid + 512 * (k), 1791) >> 3) * 256 + (tid & 7) * 16)
#define RED_SO(L, G) (OFF_PART + (L) * (1u << 20) + ((G) >> 1) * 65536 + ((G) & 1) * 128)
#define RED_JJ() ((tid & 3) * 16 + (tid & 4) * 2)
__device__ __forceinline__ void red_load(rsrc_t rs, int tid, int L, unsigned G, unsigned key, u32x4 (&pv)[4], u32x2& cxn, u32x2& cx1) {
#pragma unroll
  for (int k = 0; k < 4; ++k) pv[k] = ld16(rs, RED_VO(k), RED_SO(L, G));
  cxn = ld8(rs, RED_JJ(), cand_off(key, L) + XO(G));
  cx1 = ld8(rs, RED_JJ() + 128, cand_off(key, L) + XO(G));
}
__device__ __forceinline__ void red_sum(rsrc_t rs, char* smem, u32x2 T, int tid, int w, int L, unsigned G, unsigned key,
                                        u32x4 (&pv)[4], u32x2& cxn, u32x2& cx1) {
#define PV_OK() __all(max(max(max(umax4(pv[0]), umax4(pv[1])), max(umax4(pv[2]), umax4(pv[3]))), max(max(cxn[0], cxn[1]), max(cx1[0], cx1[1]))) != POISON)
  if (!PV_OK()) {
    unsigned spins = 0;
    do {
      if (spin_fail(spins, rs, T)) break;
#pragma unroll
      for (int k = 0; k < 4; ++k)
        if (umax4(pv[k]) == POISON) pv[k] = ld16(rs, RED_VO(k), RED_SO(L, G));
      if (max(max(cxn[0], cxn[1]), max(cx1[0], cx1[1])) == POISON) { cxn = ld8(rs, RED_JJ(), cand_off(key, L) + XO(G)); cx1 = ld8(rs, RED_JJ() + 128, cand_off(key, L) + XO(G)); }
    } while (!PV_OK());
  }
#undef PV_OK
  f32x4 s = (__builtin_bit_cast(f32x4, pv[0]) + __builtin_bit_cast(f32x4, pv[1])) + __builtin_bit_cast(f32x4, pv[2]);
  if (w < 4) s += __builtin_bit_cast(f32x4, pv[3]);
#pragma unroll
  for (int j = 0; j < 4; ++j) s[j] = sum_hi8(s[j]);
  if ((tid & 63) < 8) ((LDS_AS f32x4*)(smem + L_SFIN))[w * 8 + (tid & 7)] = s;
}
__device__ __forceinline__ float red_gate(unsigned key) {
  const unsigned k16 = key >> 16;
  const float rmax = __builtin_bit_cast(float, ((k16 & 0x8000u) ? (k16 & 0x7fffu) : (~k16 & 0xffffu)) << 16);
  float sig = rbf1(__builtin_amdgcn_rcpf(1.0f + __expf(-rmax)));
  asm volatile("" : "+v"(sig));
  return sig;
}
__device__ __forceinline__ u32x2 red_final(char* smem, int l, float sig, u32x2 cxn, u32x2 cx1) {
  f32x4 s = ((const LDS_AS f32x4*)(smem + L_SFIN))[l];
#pragma unroll
  for (int j = 0; j < 4; ++j) s[j] = sum_hi8(s[j]);
  const float xn4[4] = {lo16(cxn[0]), hi16(cxn[0]), lo16(cxn[1]), hi16(cxn[1])};
  const float x14[4] = {lo16(cx1[0]), hi16(cx1[0]), lo16(cx1[1]), hi16(cx1[1])};
  float o[4];
#pragma unroll
  for (int j = 0; j < 4; ++j) o[j] = x14[j] + rbf1(xn4[j] + rbf1(rbf1(s[j]) * sig));
  return u32x2{pack2(o[0], o[1]), pack2(o[2], o[3])};
}

struct RowFin { float mean, var, mb, rd; unsigned key; };
__device__ __forceinline__ void fin_wait(rsrc_t rs, u32x2 T, unsigned voff, unsigned so, u32x4 (&sp)[4]) {
#define ST_OK() __all(max(max(umax4(sp[0]), umax4(sp[1])), max(umax4(sp[2]), umax4(sp[3]))) != POISON)
  if (!ST_OK()) {
    unsigned spins = 0;
    do {
      if (spin_fail(spins, rs, T)) break;
#pragma unroll
      for (int i = 0; i < 4; ++i) sp[i] = ld16(rs, voff + i * 2048, so);
    } while (!ST_OK());
  }
#undef ST_OK
}
__device__ __forceinline__ RowFin fin_key(rsrc_t rs, u32x2 T, unsigned voff, unsigned so, char* skey, int l, int w, unsigned R, float wsum, u32x4 (&sp)[4]) {
  RowFin r;
#define ST_SUM() ((__builtin_bit_cast(f32x4, sp[0]) + __builtin_bit_cast(f32x4, sp[1])) + (__builtin_bit_cast(f32x4, sp[2]) + __builtin_bit_cast(f32x4, sp[3])))
  f32x4 sf = ST_SUM();
  const float z = (sf[0] + sf[1]) + sf[2];
  if (__any(z != z)) {
    fin_wait(rs, T, voff, so, sp);
    sf = ST_SUM();
  }
#undef ST_SUM
  float s0 = sf[0], s1 = sf[1], s2 = sf[2];
  add_g3(s0, s1, s2);
  r.mean = s0 * (1.0f / 1024.0f);
  r.var = fmaxf(__builtin_fmaf(-r.mean, r.mean, s1 * (1.0f / 1024.0f)), 0.f);
  r.mb = rbf1(r.mean); r.rd = ln_rd(r.var);
  const unsigned rb16 = pack2((s2 - r.mb * wsum) * r.rd, 0.f) & 0xffffu;
  r.key = min((((rb16 & 0x8000u) ? (~rb16 & 0xffffu) : (rb16 | 0x8000u)) << 16) | (0xffffu - R), POISON - 1);
  const unsigned wk = umax16(r.key);
  if (l == 0) ((LDS_AS unsigned*)skey)[w] = wk;
  return r;
}
__device__ __forceinline__ void fin_cand(rsrc_t rs, char* skey, int L, int g, int rb, int ct, const RowFin& r, const f32x2 (&v)[8]) {
  const u32x4 k0 = *(const LDS_AS u32x4*)skey, k1 = *(const LDS_AS u32x4*)(skey + 16);
  const unsigned best = max(umax4(k0), umax4(k1));
  if (r.key == best) {
    const unsigned co = L * 65536 + rb * 4096 + ct * 256 + g * 16;
    f32x2 t[8], rd2 = {r.rd, r.rd}, of2 = {-r.mb * r.rd, -r.mb * r.rd};
    asm volatile("" : "+v"(rd2), "+v"(of2));
#pragma unroll
    for (int j = 0; j < 8; ++j) t[j] = __builtin_elementwise_fma(v[j], rd2, of2);
    st16(unpoison4(pack8v(t)), rs, co, OFF_CA); st16(unpoison4(pack8v(t + 4)), rs, co + 64, OFF_CA);
    if (g == 0 && ct < (int)RKEY) st4(best, rs, KEY_AT(L, ct) + rb * SKEY, 0);
    st16(unpoison4(pack8v(v)), rs, co + 128, OFF_CA); st16(unpoison4(pack8v(v + 4)), rs, co + 192, OFF_CA);
  }
}
__device__ __forceinline__ void fin_x2(const RowFin& r, f32x2 (&v)[8]) {
  f32x2 sc2 = {1.0f + r.rd, 1.0f + r.rd}, of2 = {-r.mb * r.rd, -r.mb * r.rd};
  asm volatile("" : "+v"(sc2), "+v"(of2));
#pragma unroll
  for (int j = 0; j < 8; ++j) v[j] = __builtin_elementwise_fma(v[j], sc2, of2);
}

extern "C" __device__ unsigned long long dispatch_id() __asm("llvm.amdgcn.dispatch.id");

extern "C" __global__ __launch_bounds__(512, 2) void mega_kernel(const bf16* x, char* ws, const bf16* wqkv, const bf16* wo, const bf16* wr,
                                                                 const bf16* wg, const bf16* wu, const bf16* wd, bf16* out) {
  extern __shared__ __attribute__((aligned(16))) char smem[];
  const int w0_ = __builtin_amdgcn_readfirstlane(threadIdx.x >> 6);
  const unsigned lbase = (unsigned)(size_t)(LDS_AS char*)smem;
  {
    unsigned long long pc;
    unsigned len;
    asm volatile("s_getpc_b64 %0\n\ts_mov_b32 %1, .Ltext_end-mega_kernel\n\t.subsection 1\n.Ltext_end:\n\t.subsection 0" : "=s"(pc), "=s"(len));
    const unsigned o = ((blockIdx.x >> 3) * 64 + (threadIdx.x & 63)) * 128;
    if (w0_ == 7 && o < len)
      asm volatile("s_mov_b32 m0, %0\n\ts_nop 0\n\tglobal_load_lds_dword %1, %2" ::"s"(lbase + L_FXL), "v"(o), "s"(pc & ~127ull) : "memory");
  }
  asm volatile("" : "+s"(x), "+s"(ws), "+s"(wqkv));
#define IDS()                                                                                         \
  int tid = threadIdx.x, w = w0_, bid = blockIdx.x;                                                   \
  asm volatile("" : "+v"(tid));                                                                       \
  asm volatile("" : "+s"(w));                                                                         \
  asm volatile("" : "+s"(bid));                                                                       \
  const int l = tid & 63, xcd = bid & 7, bi = bid >> 3;                                               \
  const int p = (xcd & 1) * 4 + (bi & 3), h = (xcd >> 1) * 8 + (bi >> 2);         \
  const int rb = 2 * p + ((h >> 3) & 1), ct = (h >> 4) * 8 + (h & 7);       \
  const int slice = xcd * 28 + bi;                                                   \
  const bool ffn_on = bi < 28;                                                                        \
  const int g = l >> 4;                                                                               \
  const int rt = w < 4 ? w : 11 - w;          \
  KARGS();                                                                                            \
  (void)p; (void)h; (void)rb; (void)ct; (void)slice; (void)ffn_on; (void)g; (void)rt;
  const rsrc_t rs = __builtin_amdgcn_make_buffer_rsrc((void*)ws, 0, WS_BYTES, 0x00020000);
  typedef const void* const __attribute__((address_space(4)))* karg_t;
#define KARGS() karg_t ka = (karg_t)__builtin_amdgcn_kernarg_segment_ptr(); asm volatile("" : "+s"(ka))
#define ARGP(name, i) const GAS bf16* name = (const GAS bf16*)ka[i]; asm volatile("" : "+s"(name))
#define RSRC(ptr, bytes) __builtin_amdgcn_make_buffer_rsrc((void*)(ptr), 0, bytes, 0x00020000)
#define rwq RSRC(pwq, 1024 * 3072 * 2)
#define rwo RSRC(pwo, 1024 * 1024 * 2)
#define rwg RSRC(pwg, 1024 * F_ * 2)
#define rwu RSRC(pwu, 1024 * F_ * 2)
#define rwd RSRC(pwd, 1024 * F_ * 2)
#define rout RSRC(pout, 2048 * 1024 * 2)

#define WQ_VOFF() ((l >> 2) * 6144 + (((l & 3) ^ ((((l >> 4) ^ (l >> 5)) & 1) << 1)) << 4))
#define WQ_PIECE(t, j, dst) dma16(rwq, wqv, ((((t) + rot) & 7) * 128 + ((j) & 7) * 16) * 6144 + ((j) >> 3) * 2048 + h * 64, lbase + (dst) + (j) * 1024)
#define WQ_BOFF(dt) ((8 * g + ((l & 15) >> 2)) * 64 + (l & 3) * 8 + (((dt) ^ (g & 1)) << 5))
#define WQ_POFF() ((4 * g + ((l & 15) >> 2)) * 64 + (l & 3) * 8 + (((g ^ (g >> 1)) & 1) << 5))
#define X_UNIT1(u, j) dma16(rx, (j) ? xv1 : xv_, (unsigned)(p * 256 + rt * 32 + ((u) & 1) * 16 + (j) * 8) * 2048 + ((((u) >> 2) + rot) & 7) * 256 + (((u) >> 1) & 1) * 128, lbase + rt * 2048 + unit_slot(u) + (j) * 1024)
#define X_VOFF() ((l >> 3) * 2048 + (((l & 7) ^ (l >> 4)) << 4))
#define X_ROFF(hs) (lbase + rt * 2048 + (l & 15) * 128 + (((4 * (hs) + g) ^ ((l >> 1) & 7)) << 4))
#define X_POFF() (lbase + rt * 2048 + (l & 15) * 128 + (((g >> 1) ^ ((l >> 1) & 7)) << 4) + (g & 1) * 8)
#define TOUCH_TO(ptr, dst) asm volatile("s_mov_b32 m0, %0\n\ts_nop 0\n\tglobal_load_lds_dword %1, off" ::"s"(lbase + (dst)), "v"(ptr) : "memory")
#define TOUCH(ptr) TOUCH_TO(ptr, L_FXL)
#define WO_PIECE_W(j, w_)                                                                                                                 \
  {                                                                                                                                       \
    dma16s<0>(rs, l * 16, OFF_WOI + ct * 131072 + (j) * 8192 + (w_) * 1024, lbase + 32768 + (j) * 8192 + (w_) * 1024);                    \
  }
#define WO_PIECE(j) WO_PIECE_W(j, w)
#define WO_VOFF() ((l >> 3) * 2048 + (((((l & 7) >> 1) ^ ((l >> 4) & 3)) * 2 + (l & 1)) << 4))
#define WD_DMA() _Pragma("unroll") for (int i = 0; i < 4; ++i) WD_DMA1(i)
#define WD_DMA1(i)                                                                                   \
      dma16n(rwd, (l >> 4) * 2048 + (((l & 15) * 16) ^ ((l >> 4) * 32) ^ (((i) & 1) * 128)), (unsigned)(slice * 16 + 4 * (i)) * 2048 + w * 256, lbase + L_DN + w * 4096 + (i) * 1024)
#define FFN_DMA(m) dma16((m) & 1 ? rwu : rwg, fnv, (unsigned)(((4 * w + ((m) >> 1)) * 32) * F_ + slice * 16) * 2, lbase + 65536 + (m) * 8192 + w * 1024)
  u32x4 gv[4], uv[4];
  s16x4 dfr[8];
  u32x4 keep[9];
  u32x4 pre[4][2];
#define PRE_LOAD(it_, rot_, s0, s1)                                                                  \
  static_for<(s1) - (s0)>([&](auto c_) __attribute__((always_inline)) {                              \
    constexpr int s = (s0) + decltype(c_)::value;                                                    \
    const unsigned ab_ = OFF_XN + (it_) * (4u << 20) + (((rot_) + (s >> 2)) & 7) * 8192 + ((s >> 1) & 1) * 4096; \
    ld16w<2 * s, (s & 1) * 2048>(pre[s][0], rs, l * 16, ab_ + (p * 8 + rt) * 65536);                         \
    ld16w<2 * s + 1, (s & 1) * 2048 + 1024>(pre[s][1], rs, l * 16, ab_ + (p * 8 + rt) * 65536);                 \
  })
  u32x2 T;
  u32x2 ep;
  u32x4 xl[2];
#define LN_LOAD()                                                                                                                                   \
  {                                                                                                                                                 \
    asm volatile("buffer_load_dwordx4 %0, %1, %2, %3 offen" : "=v"(xl[0]) : "v"(l * 16), "s"(rx), "s"((p * 256 + h * 8 + w) * 2048) : "memory");      \
    asm volatile("buffer_load_dwordx4 %0, %1, %2, %3 offen offset:1024" : "=v"(xl[1]) : "v"(l * 16), "s"(rx), "s"((p * 256 + h * 8 + w) * 2048) : "memory"); \
    asm volatile("buffer_load_dwordx2 %0, off, %1, 0 sc1" : "=v"(ep) : "s"(rs) : "memory");                                                         \
  }
  const GAS bf16* tsrc;
  {
    IDS();
    const int it = 0, rot = ROT0;
    (void)it;
    const bf16* pwq = wqkv;
    const rsrc_t rx = RSRC(x, 2048 * 1024 * 2);
    const unsigned wqv = WQ_VOFF(), xv_ = X_VOFF(), xv1 = xv_ ^ 64;
    if (tid == 0) *(LDS_AS u32x4*)(smem + L_QC) = u32x4{0u, 0u, 0u, 0u};
    if (w == 0 && l < 24) TOUCH((const GAS bf16*)wqkv + (bid * 24 + l) * 512 + ((3 - (unsigned)((size_t)wqkv >> 7)) & 7) * 64);
#pragma unroll
    for (int i = 0; i < 3; ++i) WQ_PIECE(0, 3 * w + i, L_RING);
    X_UNIT1(0, 0); X_UNIT1(0, 1); X_UNIT1(1, 0); X_UNIT1(1, 1);
    BAR();
    X_UNIT1(2, 0); X_UNIT1(2, 1);
#pragma unroll
    for (int i = 0; i < 3; ++i) WQ_PIECE(1, 3 * w + i, L_RING + STAGE);
  }
  unsigned sel0 = 0x7FFFFFFFu;
  f32x2 v[8];
  u32x4 x2p[2];
  float wsum;

  auto layer = [&](auto it_c) __attribute__((always_inline)) {
    constexpr int it = decltype(it_c)::value;
    f32x4 acc[2][6];
    f32x2 qs2[2];
    u32x4 k0, k1, v0, v1;
    {
      IDS();
      bool aff = false;
      unsigned aq[2];
      u32x4 av[2];
#define PIN_ACC() asm volatile("" : "+{v[176:179]}"(acc[0][0]), "+{v[180:183]}"(acc[0][1]), "+{v[184:187]}"(acc[0][2]), "+{v[188:191]}"(acc[0][3]), "+{v[192:195]}"(acc[0][4]), "+{v[196:199]}"(acc[0][5]), \
                               "+{v[200:203]}"(acc[1][0]), "+{v[204:207]}"(acc[1][1]), "+{v[208:211]}"(acc[1][2]), "+{v[212:215]}"(acc[1][3]), "+{v[216:219]}"(acc[1][4]), "+{v[220:223]}"(acc[1][5]))
#define PINV4(x) asm volatile("" : "+v"(x[0]), "+v"(x[1]), "+v"(x[2]), "+v"(x[3])::"memory")
      auto qkv_loop = [&](auto it_c) __attribute__((always_inline)) {
        constexpr int IT = decltype(it_c)::value;
        const unsigned abase = OFF_XN + IT * (4u << 20) + (p * 8 + rt) * 65536;
        const int rot = ROT0 + 5 * IT;
        unsigned fkey = 0, kv, bq[2];
        u32x4 fx[4], bv[2], af[WA][2];
        u32x2 cxn, cx1, hh;
        f32x4 sa, sb, cs4[2];
        f32x2 b2[2];
#define AB_OFF() (OFF_AB + IT * 32768 + (p * 8 + rt) * 512)
#define A_LOAD1(k, a) ld16w<(k) % WA * 2 + (a), ((k) & 1) * 2048 + (a) * 1024>(af[(k) % WA][a], rs, l * 16, abase + ((((k) >> 2) + rot) & 7) * 8192 + (((k) >> 1) & 1) * 4096)
        if constexpr (IT == 1) {
#pragma unroll
          for (int s = 0; s < NPRE; ++s) { af[s][0] = pre[s][0]; af[s][1] = pre[s][1]; }
        }
        const bf16* pwq = wqkv;
        ARGP(pwo, 3);
        const rsrc_t rx = RSRC(x, 2048 * 1024 * 2);
        unsigned xv_ = X_VOFF(), xv1 = xv_ ^ 64, xr[4] = {X_ROFF(0), X_ROFF(1), 0u, 0u}, bf_[2];
        if constexpr (IT == 0) asm volatile("" : "+v"(xv_), "+v"(xv1), "+v"(xr[0]), "+v"(xr[1]));
#define LDS8(a_) (*(const LDS_AS u32x2*)(size_t)(a_))
#define X_READ(u)                                                                                                              \
  {                                                                                                                            \
    if constexpr ((u) < 4 * IMT) {                                                                                             \
      af[((u) & ~1) % WA][(u) & 1] = *(const LDS_AS u32x4*)(size_t)(xr[0] + unit_slot(u));                                     \
      af[((u) | 1) % WA][(u) & 1] = *(const LDS_AS u32x4*)(size_t)(xr[1] + unit_slot(u));                                      \
    } else {                                                                                                                   \
      const u32x2 a_ = LDS8(xr[0] + unit_slot(u)), b_ = LDS8(xr[1] + unit_slot(u)), c_ = LDS8(xr[2] + unit_slot(u)), d_ = LDS8(xr[3] + unit_slot(u)); \
      af[((u) & ~1) % WA][(u) & 1] = u32x4{a_[0], a_[1], b_[0], b_[1]};                                                        \
      af[((u) | 1) % WA][(u) & 1] = u32x4{c_[0], c_[1], d_[0], d_[1]};                                                         \
    }                                                                                                                          \
  }
        const int tstep = TS0 + bid % TSN + TSD * (w >> TSG);
        const unsigned wqv = WQ_VOFF();
#define QKV_DMA1(t, i)                                                                            \
  {                                                                                               \
    if constexpr (IT == 0 && (t) < IMT) WQ_PIECE(t, 3 * w + (i), L_RING + SLOT(t) * STAGE);       \
    else dma16s<(i) * 1024>(rs, l * 16, OFF_WI + (h * 8 + (((t) + rot) & 7)) * STAGE + w * 3072, lbase + L_RING + SLOT(t) * STAGE + w * 3072); \
  }
        unsigned bo[2][2];
#pragma unroll
        for (int dt = 0; dt < 2 - IT; ++dt) {
          bo[dt][0] = lbase + L_RING + (IT ? l * 16 : WQ_BOFF(dt)); bo[dt][1] = bo[dt][0] + 2 * STAGE;
          asm volatile("" : "+v"(bo[dt][0]), "+v"(bo[dt][1]));
        }
        auto frag0 = [&](int sl, int f) __attribute__((always_inline)) {
          unsigned pb = lbase + L_RING + WQ_POFF();
          asm volatile("" : "+v"(pb));
          const unsigned o = (pb ^ ((f & 1) << 5)) + sl * STAGE + ((f % 6) >> 1) * 8192 + (f / 6) * 2048;
          frag_t t;
          t.hf.a = trabs(o); t.hf.b = trabs(o + 1024);
          return t.v;
        };
        u32x4 df[4];
        auto dot_mma = [&](const u32x4 (&xf)[4]) __attribute__((always_inline)) {
          sa = mfma16(df[0], xf[0], f32x4{0.f, 0.f, 0.f, 0.f}); sb = mfma16(df[1], xf[1], f32x4{0.f, 0.f, 0.f, 0.f});
          sa = mfma16(df[2], xf[2], sa); sb = mfma16(df[3], xf[3], sb);
        };
        auto dot_store = [&](unsigned dst) __attribute__((always_inline)) {
          if ((l & 15) == 0) st16(unpoison4(__builtin_bit_cast(u32x4, sa + sb)), rs, g * 16, dst + (h * 8 + p) * 512 + w * 64);
        };
        auto tile_sum = [&](u32x4 pt, int dst) __attribute__((always_inline)) {
          f32x4 c = __builtin_bit_cast(f32x4, pt);
#pragma unroll
          for (int e = 0; e < 4; ++e) { c[e] += dppf<0x124>(c[e]); c[e] += dppf<0x128>(c[e]); c[e] = add_x16(c[e]); }
          if ((l & 28) == 0) *(LDS_AS f32x4*)(smem + dst + (w * 2 + (l >> 5)) * 64 + (l & 3) * 16) = c;
        };
        frag_t bfr[6];
#define QKV_BO(s, n, x) (bo[((n) ^ (x)) & 1][SLOT((s) >> 2) >> 1] + (SLOT((s) >> 2) & 1) * STAGE + ((n) >> 1) * 8192 + ((s) & 3) * 2048 + (x) * 256)
#define QKV_RDA(s, n)                                                                             \
  {                                                                                               \
    if constexpr (IT == 0 && (s) < 4 * IMT) bfr[n].hf.a = trabs(QKV_BO(s, n, 0));                 \
    else if constexpr (IT == 0) bfr[n].v = *(const LDS_AS u32x4*)(size_t)(bf_[SLOT((s) >> 2) >> 1] + (SLOT((s) >> 2) & 1) * STAGE + (((s) & 3) * 6 + (n)) * 1024); \
    else bfr[n].v = *(const LDS_AS u32x4*)(size_t)(bo[0][SLOT((s) >> 2) >> 1] + (SLOT((s) >> 2) & 1) * STAGE + (((s) & 3) * 6 + (n)) * 1024); \
  }
#define QKV_RDB(s, n) { if constexpr (IT == 0 && (s) < 4 * IMT) bfr[n].hf.b = trabs(QKV_BO(s, n, 1)); }
#define XP_OFF() (OFF_XNEW + ((rot + 5) & 7) * 1024)
#define FP_VO() (w == 3 ? (l >> 2) * 256 + (l & 3) * 16 : ((l >> 2) & 7) * 512 + (l >> 5) * 64 + (l & 3) * 16)
#define FP_OFF(i) (w == 3 ? OFF_XNEW + (i) * 4096 : OFF_FX + h * 4096 + w * 128)
#define FX_OK(n) __all(((n) == 4 ? max(max(umax4(fx[0]), umax4(fx[1])), max(umax4(fx[2]), umax4(fx[3]))) : max(umax4(fx[0]), umax4(fx[1]))) != POISON)
#define SINCE(s0) (vm_at(IT, s, n) - vm_at(IT, s0, 5))
#define DEF4(x) asm volatile("" : "=v"(x[0]), "=v"(x[1]), "=v"(x[2]), "=v"(x[3]))
        LDS_AS unsigned* qcnt = (LDS_AS unsigned*)(smem + L_QC);
        unsigned qc;
        unsigned qadr = lbase + L_QC, qone = 1;
        asm volatile("" : "+v"(qadr), "+v"(qone));
#define count_in(i) asm volatile("s_mov_b64 exec, 1\n\tds_add_u32 %0, %1 offset:%2\n\ts_mov_b64 exec, -1" ::"v"(qadr), "v"(qone), "n"(4 * (i)) : "memory")
        auto count_wait = [&](int i, unsigned want) __attribute__((always_inline)) {
          if (__builtin_expect((unsigned)__builtin_amdgcn_readfirstlane(qc) < want, 0)) {
            while ((unsigned)__builtin_amdgcn_readfirstlane(__hip_atomic_load(qcnt + i, __ATOMIC_RELAXED, __HIP_MEMORY_SCOPE_WORKGROUP)) < want) {}
          }
          asm volatile("" ::: "memory");
        };
        if constexpr (IT == 0) {
          wait_vm<PRE0 - vm_unit(1)>();
          X_READ(0); X_READ(1);
          asm volatile("s_barrier" ::: "memory");
        }
#pragma unroll
        for (int n = 0; n < 6; ++n) { QKV_RDA(0, n); if (n < 5) QKV_RDB(0, n); }
        f32x2 lv[4], xs, xq;
        float lmean;
        f32x4 sc;
#define X1_OFF() (F_X1 + ((p * RX1 + (xcd >> 1) % RX1) * 32 + (rt >> 2) * 16 + (l & 15)) * SX1)
        unsigned long long xok = 0;
        RowFin fin;
        auto x1_need = [&](unsigned long long m) __attribute__((always_inline)) {
          if (__builtin_expect((xok & m) != m, 0)) {
            for (unsigned spins = 0;;) {
              const u32x2 f = ld8(rs, X1_OFF(), 0);
              xok = __builtin_amdgcn_ballot_w64(f[0] == T[0] && f[1] == T[1]);
              if ((xok & m) == m) break;
              if (spin_fail(spins, rs, T)) { xok = ~0ull; break; }
              __builtin_amdgcn_s_sleep(KSL);
            }
            asm volatile("" ::: "memory");
          }
        };
        auto gap = [&](auto sc_, auto nc_) __attribute__((always_inline)) {
          constexpr int s = decltype(sc_)::value, n = decltype(nc_)::value, ar = a_req(s, n);
          {
            constexpr int tc = (s + 2) / 4;
            if constexpr (tc >= 1 && tc < 8 && s == 4 * tc - 2 && n == 5) {
              if constexpr (!IT || tc >= 3) wait_vm<vm_at(IT, s, n) - vm_tile(IT, tc)>();
              if constexpr (tc == 7 && !IT) asm volatile("s_waitcnt lgkmcnt(4)" ::: "memory");
              asm volatile("s_barrier" ::: "memory");
            }
          }
          if constexpr (IT == 0 && s == 4 * IMT - 3 && n == 5) {
            xr[0] = X_POFF(); xr[1] = xr[0] ^ 32; xr[2] = xr[0] ^ 64; xr[3] = xr[0] ^ 96;
            bf_[0] = lbase + L_RING + l * 16; bf_[1] = bf_[0] + 2 * STAGE;
            asm volatile("" : "+v"(xr[0]), "+v"(xr[1]), "+v"(xr[2]), "+v"(xr[3]), "+v"(bf_[0]), "+v"(bf_[1]));
          }
          if constexpr (IT == 0 && n == 0 && s + 2 < 32) { wait_vm<vm_at(0, s, 0) - vm_unit(s + 2)>(); X_READ(s + 2); SB(); }
          if constexpr (n == 0 && tile_piece(IT, s)) { constexpr int t_ = piece_tile(IT, s), i_ = s - tile_at(IT, t_); QKV_DMA1(t_, i_); }
          if constexpr (IT == 0) {
            if constexpr ((n == 2 || n == 3) && s + 5 < 32) {
              if constexpr (n == 2) asm volatile("s_waitcnt lgkmcnt(%0)" ::"n"(s + 1 < QL ? (s + 1 < 4 * IMT ? 4 : 2) : 1) : "memory");
              X_UNIT1(s + 5, n - 2);
            }
            if constexpr (s >= W2S && s < W2S + 3 && n == 4) QKV_DMA1(2, s - W2S);
            if constexpr (s == LNL && n == 5) LN_LOAD();
            if constexpr (s == 0 && n < 2) { X_UNIT1(3 + n, 0); X_UNIT1(3 + n, 1); }
            if constexpr (s == LNS && n == 1) {
              wait_vm<vm_at(0, s, n) - vm_at(0, LNL + 1, 0)>();
              asm volatile("" : "+v"(ep), "+v"(xl[0]), "+v"(xl[1])::"memory");
              const unsigned long long e = (((unsigned long long)(unsigned)__builtin_amdgcn_readfirstlane(ep[1]) << 32) | (unsigned)__builtin_amdgcn_readfirstlane(ep[0])) + 1ull + dispatch_id() * 0x9E3779B97F4A7C16ull;
              T = u32x2{(unsigned)e, (unsigned)(e >> 32)};
              unpack8v(xl[0], lv);
              xs = (lv[0] + lv[1]) + (lv[2] + lv[3]);
              xq = __builtin_elementwise_fma(lv[0], lv[0], lv[1] * lv[1]) + __builtin_elementwise_fma(lv[2], lv[2], lv[3] * lv[3]);
              asm volatile("" : "+v"(xs), "+v"(xq));
            }
            if constexpr (s == LNS && n == 4) {
              unpack8v(xl[1], lv);
              xs += (lv[0] + lv[1]) + (lv[2] + lv[3]);
              xq += __builtin_elementwise_fma(lv[0], lv[0], lv[1] * lv[1]) + __builtin_elementwise_fma(lv[2], lv[2], lv[3] * lv[3]);
              auto t = __builtin_amdgcn_permlane32_swap(__builtin_bit_cast(unsigned, hadd(xs)), __builtin_bit_cast(unsigned, hadd(xq)), false, false);
              lmean = asf(t[0]) + asf(t[1]);
              asm volatile("" : "+v"(lmean));
            }
            if constexpr (s == LNS + 1 && n == 1) { lmean += dppf<0xB1>(lmean); lmean += dppf<0x4E>(lmean); lmean += dppf<0x124>(lmean); asm volatile("" : "+v"(lmean)); }
            if constexpr (s == LNS + 1 && n == 4) { lmean = add_x16(lmean + dppf<0x128>(lmean)) * (1.0f / 1024.0f); asm volatile("" : "+v"(lmean)); }
            if constexpr (s == LNS + 2 && n == 1) {
              const float m2 = asf(__builtin_amdgcn_readlane(__builtin_bit_cast(unsigned, lmean), 32));
              xs[0] = ln_rd(fmaxf(__builtin_fmaf(-lmean, lmean, m2), 0.f));
              asm volatile("" : "+v"(xs));
            }
            if constexpr (s == LNS + 2 && n == 5) {
              if (l == 0) st16(u32x4{__builtin_bit_cast(unsigned, (float)xs[0]), __builtin_bit_cast(unsigned, -rbf1(lmean)), T[0], T[1]}, rs, OFF_AB + (p * 256 + h * 8 + w) * 16, 0);
            }
            if constexpr (s == POI && n == 1) {
              if (w == 1) {
                const unsigned prb = 2 * p + (h >> 4), pct = h & 15;
                unsigned pz1 = POISON;
                asm volatile("" : "+v"(pz1));
                const u32x4 pz = {pz1, pz1, pz1, pz1};
                st16(pz, rs, OFF_STAT + (prb * 16 + pct) * 2048 + l * 16, 0);
                st16(pz, rs, OFF_STAT + (prb * 16 + pct) * 2048 + 1024 + l * 16, 0);
                if (l < 16) st16(pz, rs, OFF_CA + prb * 4096 + pct * 256 + l * 16, 0);
                else if (l == 16 && pct < RKEY) st4(pz1, rs, KEY_AT(0, pct) + prb * SKEY, 0);
              }
            }
            if constexpr (s == POI + 4 && n == 1) { if (w == 1 && l < RX) st8(T, rs, F_XN + (p * RX + l) * 256 + h * 8, 0); }
          } else {
            if constexpr ((s == 0 && n == 0) || (s == 2 && n == 2)) asm volatile("buffer_load_dwordx2 %0, %1, %2, 0 offen sc1" : "=v"(cxn) : "v"(X1_OFF()), "s"(rs) : "memory");
            if constexpr ((s == 1 && n == 3) || (s == 4 && n == 0)) {
              wait_vm<vm_at(1, s, n) - (s == 1 ? vm_at(1, 0, 1) : vm_at(1, 2, 3))>();
              asm volatile("" : "+v"(cxn)::"memory");
              xok |= __builtin_amdgcn_ballot_w64(cxn[0] == T[0] && cxn[1] == T[1]);
            }
            if constexpr (ar >= 0 && (ar & 7) == 0) x1_need(3ull << (((rot + ar / 8) & 7) * 2));
            const unsigned R = (rb * 4 + (w & 3)) * 32 + (w >> 2) * 16 + (l & 15), so = OFF_STAT + rb * 32768;
            if constexpr (s == (SPL < 0 ? 0 : SPL) && n == (SPL < 0 ? 1 : 4)) {
#pragma unroll
              for (int i = 0; i < 4; ++i) ld16h<0>(fx[i], rs, (4 * g + i) * 2048 + (R & 127) * 16, so);
            }
            if constexpr (s == SPL + 2 && n == 1) {
              wait_vm<vm_at(1, s, n) - (SPL < 0 ? vm_at(1, 0, 2) : vm_at(1, SPL, 5))>();
              PINV4(fx);
              fin = fin_key(rs, T, g * 8192 + (R & 127) * 16, so, smem + L_SKEY, l, w, R, wsum, fx);
              count_in(2);
            }
            if constexpr (s == SPL + 3 && n == 0) qc = __hip_atomic_load(qcnt + 2, __ATOMIC_RELAXED, __HIP_MEMORY_SCOPE_WORKGROUP);
            if constexpr (s == SPL + 3 && n == 1) { count_wait(2, 8); fin_cand(rs, smem + L_SKEY, 0, g, rb, ct, fin, v); }
            if constexpr (s == SPL + 3 && n == 3) {
              fin_x2(fin, v);
              x2p[0] = pack8v(v); x2p[1] = pack8v(v + 4);
              asm volatile("" : "+v"(x2p[0]), "+v"(x2p[1]));
            }
            if constexpr (s == SPL + 3 && n == 5) {
              if ((w & 3) * 2 + (w >> 2) == ct >> 1) {
                const float f = 1.0f + fin.rd, rd2 = ln_rd(fin.var * f * f), mb2 = rbf1(__builtin_fmaf(fin.mean - fin.mb, fin.rd, fin.mean));
                if (g == 0 && ((l >> 3) & 1) == (ct & 1))
                  st16(u32x4{__builtin_bit_cast(unsigned, f * rd2), __builtin_bit_cast(unsigned, -__builtin_fmaf(fin.mb, fin.rd, mb2) * __builtin_amdgcn_rcpf(f)), T[0], T[1]}, rs, OFF_AB + 32768 + R * 16, 0);
              }
            }
          }
          if constexpr (IT == 1 && ar >= 0) A_LOAD1(ar / 2, ar & 1);
          if constexpr (!IT && s == CSL && n == 4) { asm volatile("" : "=v"(fx[0])); if (w < 6) ld16h<0>(fx[0], rs, l * 16, OFF_CS + (h * 6 + w) * 1024); }
          if constexpr (!IT && s == CSS && n == 2) {
            if (w < 6) {
              wait_vm<SINCE(CSL)>();
              asm volatile("" : "+v"(fx[0])::"memory");
              tag_wait(rs, T, l * 16, OFF_CS + (h * 6 + w) * 1024, fx[0]);
              const f32x2 c = {sum_hi8(asf(fx[0][0])), sum_hi8(asf(fx[0][1]))};
              if (l < 8) *(LDS_AS f32x2*)(smem + L_CSL + w * 64 + l * 8) = c;
            }
          }
          constexpr int ABL = abl(IT), ABS = abs_(IT);
          if constexpr (s == ABL && n == 4) ld16h<0>(bv[0], rs, (l & 31) * 16, AB_OFF());
          if constexpr (s == ABS && n == 2) {
            wait_vm<SINCE(ABL)>();
            asm volatile("" : "+v"(bv[0])::"memory");
            tag_wait(rs, T, (l & 31) * 16, AB_OFF(), bv[0]);
            const unsigned t0 = bv[0][0], t1 = bv[0][1];
            ((LDS_AS unsigned*)(smem + L_ABW + rt * 2048))[l] = l < 32 ? t0 : t1;
          }
          if constexpr (s == ABS && n == 3) {
#pragma unroll
            for (int a = 0; a < 2; ++a) {
              const LDS_AS unsigned* aw = (const LDS_AS unsigned*)(smem + L_ABW + rt * 2048) + 16 * a;
              aq[a] = aw[l & 15]; bq[a] = aw[32 + (l & 15)];
              av[a] = *(const LDS_AS u32x4*)(aw + 4 * g); bv[a] = *(const LDS_AS u32x4*)(aw + 32 + 4 * g);
            }
          }
          if constexpr (s == ABS && n == 5) {
#pragma unroll
            for (int a = 0; a < 2; ++a) { b2[a] = f32x2{asf(bq[a]), asf(bq[a])}; asm volatile("" : "+v"(b2[a])); }
          }
          if constexpr (s == ABS + 1) {
            constexpr int k = (n + 4) % 6;
            if constexpr (k < 4) cs4[k & 1] = *(const LDS_AS f32x4*)(smem + L_CSL + k * 64 + g * 16);
            else cs4[k & 1][0] = *(const LDS_AS float*)(smem + L_CSL + k * 64 + (l & 15) * 4);
          }
          if constexpr ((s == ABS + 1 && n > 0) || (s == ABS + 2 && n == 0)) {
            constexpr int k = (n + 3) % 6;
            if constexpr (k < 4) {
#pragma unroll
              for (int a = 0; a < 2; ++a) acc[a][k] = __builtin_elementwise_fma(cs4[k & 1], f32x4{b2[a][0], b2[a][1], b2[a][0], b2[a][1]}, acc[a][k]);
            } else {
              f32x2 c2 = {cs4[k & 1][0], cs4[k & 1][0]};
              asm volatile("" : "+v"(c2));
#pragma unroll
              for (int a = 0; a < 2; ++a) acc[a][k] = __builtin_elementwise_fma(f32x4{c2[0], c2[1], c2[0], c2[1]}, __builtin_bit_cast(f32x4, bv[a]), acc[a][k]);
            }
          }
          if constexpr (IT == 0) {
            if constexpr (s == CSD && n < 3) fx[n] = frag0(SLOT(s >> 2), 3 * w + n);
            if constexpr (s == CSD + 1 && n == 1) {
              unsigned one2 = 0x3F803F80u;
              asm volatile("" : "+v"(one2));
              const u32x4 o4 = {one2, one2, one2, one2};
              const f32x4 z4 = {0.f, 0.f, 0.f, 0.f};
              sa = mfma16(fx[0], o4, z4); sb = mfma16(fx[1], o4, z4); sc = mfma16(fx[2], o4, z4);
            }
            constexpr int is_ = im_st(s, n);
            if constexpr (is_ >= 0) st16(fx[is_ < 0 ? 0 : is_], rs, l * 16 + is_ * 1024, OFF_WI + (h * 8 + ((rot + (CSD >> 2)) & 7)) * STAGE + w * 3072);
            if constexpr (s == IMF && n == 1) { if (tid == 0) st8(T, rs, (h * 8 + p) * 256, F_IM); }
            if constexpr (s == IML && n == 4) { asm volatile("" : "=v"(cxn)); if (w == 0) ld8h(cxn, rs, (l & 7) * 256, F_IM + h * 2048); }
            if constexpr (s == IMK && n == 3) {
              if (w == 0) {
                wait_vm<SINCE(IML)>();
                asm volatile("" : "+v"(cxn)::"memory");
                if (!__all(cxn[0] == T[0] && cxn[1] == T[1])) poll_flags(rs, (l & 7) * 256, F_IM + h * 2048, T);
              }
            }
            if constexpr (s == CSD + 2 && n == 1) {
              const int c = l & 15;
              const f32x4 t0 = sa, t1 = sb, t2 = sc, t = c == 0 ? +t0 : c == 1 ? +t1 : +t2;
              if (c < 3) *(LDS_AS f32x4*)(smem + L_CSP + (w * 3 + c) * 64 + g * 16) = t;
              count_in(2);
            }
            if constexpr (s == CSD + 4 && n == 1) {
              if (w == 2) {
                qc = 0;
                count_wait(2, 8);
                const LDS_AS f32x2* cp = (const LDS_AS f32x2*)(smem + L_CSP) + min(l, 47);
                const f32x2 t = (cp[0] + cp[48]) + (cp[96] + cp[144]);
                if (l < 48) st16(u32x4{__builtin_bit_cast(unsigned, (float)t[0]), __builtin_bit_cast(unsigned, (float)t[1]), T[0], T[1]}, rs, (l >> 3) * 1024 + (l & 7) * 16, OFF_CS + h * 6144 + p * 128);
              }
            }
            if constexpr (s == WOL && n == 5) dma16(rwo, WO_VOFF(), (rb * 8 + w) * 16384 + ct * 128, lbase + L_WOS + w * 1024);
            if constexpr (s == WOS && n == 1) {
              const unsigned o_ = lbase + L_WOS + (w >> 2) * 4096 + (4 * g + ((l & 15) >> 2)) * 128 + (l & 3) * 8 + (((w & 3) ^ (2 * (g & 1) + ((l & 15) >> 3))) << 5);
              frag_t f_;
              f_.hf.a = trabs(o_); f_.hf.b = trabs(o_ + 2048);
              fx[3] = f_.v;
            }
            if constexpr (s == WOS + 1 && n == 5) st16(fx[3], rs, (ct * 128 + rb * 8 + w) * 1024 + l * 16, OFF_WOI);
            if constexpr (s == WOF && n == 1) { if (tid == 128) st8(T, rs, (ct * 16 + rb) * 256, F_WOI); }
            if constexpr (s == WOC && n == 4) { asm volatile("" : "=v"(cx1)); if (w == 3) ld8h(cx1, rs, (l & 15) * 256, F_WOI + ct * 4096); }
            if constexpr (s == WOK && n == 3) {
              if (w == 3) {
                wait_vm<SINCE(WOC)>();
                asm volatile("" : "+v"(cx1)::"memory");
                if (!__all(cx1[0] == T[0] && cx1[1] == T[1])) poll_flags(rs, (l & 15) * 256, F_WOI + ct * 4096, T);
              }
            }
            if constexpr (s >= 29 && (n & 1)) {
              if constexpr (s - 24 < IMT) keep[3 * (s - 29) + (n >> 1)] = frag0(SLOT(s - 24), 3 * w + (n >> 1));
              else keep[3 * (s - 29) + (n >> 1)] = *(const LDS_AS u32x4*)(size_t)(bf_[0] + SLOT(s - 24) * STAGE + w * 3072 + (n >> 1) * 1024);
            }
            if constexpr (s == (TS0 < TEX ? TS0 : TEX) - 2 && n == 1) {
              ARGP(pwg, 5); ARGP(pwu, 6);
              tsrc = (w < 4 ? pwg : pwu) + (bi * 32 + (w & 3) * 8 + (l >> 3)) * F_ + xcd * 448 + min(l & 7, 6) * 64;
              asm volatile("" : "+v"(tsrc));
            }
            if constexpr (s >= TS0 && s < TS0 + TSN + TSD * (7 >> TSG) && n == 4) {
              if (tstep == s && ((TM0 >> w) & 1)) TOUCH(tsrc);
            }
            if constexpr (s == TEX && n == 5) {
              const GAS bf16* t2 = ((TM1 >> w) & 1) ? tsrc : (const GAS bf16*)x + p * 262144;
              TOUCH(t2);
            }
          } else {
            if constexpr (s >= 27 && s < 30 && (n == 0 || n == 3)) WO_PIECE(4 + 2 * (s - 27) + n / 3);
            if constexpr (s == FKL && n == 4) { asm volatile("" : "=v"(kv)); if (w == 0) ld4h<0>(kv, rs, (l & 15) * SKEY, KEY_AT(0, KREP())); }
            if constexpr (s == FSL && n == 1) {
              asm volatile("" : "=v"(fx[0]), "=v"(fx[1]));
              if (w == 0) {
                wait_vm<SINCE(FKL)>();
                asm volatile("" : "+v"(kv)::"memory");
                fkey = __builtin_amdgcn_readfirstlane(umax16(kv == POISON ? 0u : kv));
                ld4h<0>(kv, rs, (l & 15) * SKEY, KEY_AT(0, KREP()));
                ld16h<0>(fx[0], rs, (l >> 3) * 256 + (l & 7) * 16, cand_off(fkey, 0)); ld16h<2048>(fx[1], rs, (l >> 3) * 256 + (l & 7) * 16, cand_off(fkey, 0));
              }
            }
            if constexpr (s == FS0 - 1 && n == FSN) {
              if (w == 0) {
                wait_vm<vm_at(1, s, n) - vm_at(1, FSL, 2)>();
                asm volatile("" : "+v"(fx[0]), "+v"(fx[1]), "+v"(kv)::"memory");
                const unsigned k_ = key_wait(rs, T, l, 0, KREP(), kv);
                if (l == 0) *(LDS_AS unsigned*)(smem + L_SKEY + 32) = k_;
                if (k_ != fkey) fx[0][0] = POISON;
                word_wait<2>(rs, T, (l >> 3) * 256 + (l & 7) * 16, cand_off(k_, 0), fx);
                *(LDS_AS u32x4*)(smem + L_ROW + l * 16) = fx[0]; *(LDS_AS u32x4*)(smem + L_ROW + 1024 + l * 16) = fx[1];
                count_in(0);
              }
            }
            if constexpr (s == FS0 - 1 && n == 1) {
              asm volatile("" : "=v"(dfr[0]), "=v"(dfr[1]), "=v"(dfr[2]), "=v"(dfr[3]), "=v"(dfr[4]), "=v"(dfr[5]), "=v"(dfr[6]), "=v"(dfr[7]));
              if (ffn_on) {
#pragma unroll
                for (int i = 0; i < 8; ++i) dfr[i] = trread(smem, L_DN + w * 4096 + g * 1024 + ((l & 15) >> 2) * 256 + ((i ^ ((l >> 2) & 7)) * 32) + (l & 3) * 8);
              }
            }
            if constexpr (s == FS0) {
              if constexpr (n == 0) qc = __hip_atomic_load(qcnt, __ATOMIC_RELAXED, __HIP_MEMORY_SCOPE_WORKGROUP);
              if constexpr (n == 1) {
                count_wait(0, 1);
                kv = *(const LDS_AS unsigned*)(smem + L_SKEY + 32);
#pragma unroll
                for (int i = 0; i < 4; ++i) fx[i] = *(const LDS_AS u32x4*)(smem + L_ROW + w * 256 + i * 64 + g * 16);
              }
              if constexpr (n == 2) {
                fkey = __builtin_amdgcn_readfirstlane(kv);
                sel0 = key_row(fkey);
                aff = (int)(sel0 >> 8) == p;
                asm volatile("" : "=v"(sa), "=v"(sb));
                if (ffn_on) ffn_gu(gv, uv, fx, sa, sb);
              }
              if constexpr (n == 4) {
                if (ffn_on) {
                  ffn_gu_store(smem, l, w, sa, sb);
                  count_in(1);
                }
              }
            }
            if constexpr (s == FS0D) {
              if constexpr (n == 0) qc = __hip_atomic_load(qcnt + 1, __ATOMIC_RELAXED, __HIP_MEMORY_SCOPE_WORKGROUP);
              if constexpr (n == 1) { asm volatile("" : "=v"(sa)); if (ffn_on) { count_wait(1, 8); sa = ffn_d_read(smem, l); } }
              if constexpr (n == 2) { asm volatile("" : "=v"(hh)); if (ffn_on) hh = ffn_d_h(sa); }
              if constexpr (n == 4) { asm volatile("" : "=v"(sa)); if (ffn_on) sa = ffn_d_all(dfr, hh); }
              if constexpr (n == 5) { if (ffn_on) ffn_d_store(rs, l, w, 0, slice, sa); }
            }
            if constexpr (s == FS1 && n == 4) {
              DEF4(fx);
              asm volatile("" : "=v"(cxn), "=v"(cx1));
              if (!ffn_on) {
                const unsigned G = xcd * 4 + bi - 28;
#pragma unroll
                for (int k = 0; k < 4; ++k) ld16h<0>(fx[k], rs, RED_VO(k), RED_SO(0, G));
                ld8h(cxn, rs, RED_JJ(), cand_off(fkey, 0) + XO(G)); ld8h(cx1, rs, RED_JJ() + 128, cand_off(fkey, 0) + XO(G));
              }
            }
            if constexpr (s == FS1S && n == 2) {
              if (!ffn_on) {
                wait_vm<SINCE(FS1)>();
                PINV4(fx);
                asm volatile("" : "+v"(cxn), "+v"(cx1)::"memory");
                red_sum(rs, smem, T, tid, w, 0, xcd * 4 + bi - 28, fkey, fx, cxn, cx1);
                BAR();
                if (w == 0) {
                  const u32x2 ov = red_final(smem, l, red_gate(fkey), cxn, cx1);
                  if (l < 8) st8(unpoison2(ov), rs, OFF_XNEW + (xcd * 4 + bi - 28) * 256 + ((l & 3) * 2 + (l >> 2)) * 8, 0);
                }
              }
            }
            if constexpr (s == FXA && n == 4) { asm volatile("" : "=v"(fx[0])); if (w == 1) ld16h<0>(fx[0], rs, ((l >> 2) & 3) * 256 + (l & 3) * 16, XP_OFF()); }
            if constexpr (s == FXB - 1 && n == FSN) {
              if (w == 1) {
                wait_vm<SINCE(FXA)>();
                asm volatile("" : "+v"(fx[0])::"memory");
                word_wait<1>(rs, T, ((l >> 2) & 3) * 256 + (l & 3) * 16, XP_OFF(), fx);
                if (l < 16) *(LDS_AS u32x4*)(smem + L_ROW + l * 16) = fx[0];
                count_in(0);
              }
            }
            if constexpr (s == FXB) {
#define FX_FRAG(i) (*(const LDS_AS u32x4*)(size_t)(bo[0][0] + STAGE + (6 * (i) + w) * 1024))
              if constexpr (n == 0) { qc = __hip_atomic_load(qcnt, __ATOMIC_RELAXED, __HIP_MEMORY_SCOPE_WORKGROUP); }
              if constexpr (n == 1) {
                DEF4(fx); DEF4(df);
                if (w < 6) {
                  count_wait(0, 2);
#pragma unroll
                  for (int i = 0; i < 4; ++i) { fx[i] = *(const LDS_AS u32x4*)(smem + L_ROW + i * 64 + g * 16); df[i] = FX_FRAG(i); }
                }
              }
              if constexpr (n == 2) { asm volatile("" : "=v"(sa), "=v"(sb)); if (w < 6) dot_mma(fx); }
              if constexpr (n == 4) { if (w < 6) dot_store(OFF_FX); }
            }
            if constexpr (s == FXL && n == 4) {
              asm volatile("" : "=v"(fx[0]), "=v"(fx[1]));
              if (aff && w < 4) { ld16h<0>(fx[0], rs, FP_VO(), FP_OFF(0)); ld16h<0>(fx[1], rs, FP_VO(), FP_OFF(1)); }
            }
            if constexpr (s == FXS && n == 4) {
              if (aff && w < 4) {
                wait_vm<0>();
                asm volatile("" : "+v"(fx[0]), "+v"(fx[1])::"memory");
                if (!FX_OK(2)) {
                  unsigned spins = 0;
                  do {
                    if (spin_fail(spins, rs, T)) break;
                    fx[0] = ld16(rs, FP_VO(), FP_OFF(0)); fx[1] = ld16(rs, FP_VO(), FP_OFF(1));
                  } while (!FX_OK(2));
                }
                if (w == 3) {
                  f32x2 nx[8];
                  unpack8v(fx[0], nx); unpack8v(fx[1], nx + 4);
                  const f32x2 sm = ((nx[0] + nx[1]) + (nx[2] + nx[3])) + ((nx[4] + nx[5]) + (nx[6] + nx[7]));
                  const float mean = sum64(hadd(sm)) * (1.0f / 1024.0f);
                  const f32x2 m2 = {mean, mean};
                  f32x2 q = {0.f, 0.f};
#pragma unroll
                  for (int j = 0; j < 8; ++j) { const f32x2 d = nx[j] - m2; q = __builtin_elementwise_fma(d, d, q); }
                  const float rd = ln_rd(sum64(hadd(q)) * (1.0f / 1024.0f));
                  if (l == 0) *(LDS_AS f32x2*)(smem + L_FXL + 384) = f32x2{rd, -rbf1(mean)};
                } else {
                  tile_sum(fx[0], L_FXL);
                }
                count_in(3);
              }
            }
          }
        };
        static_for<32>([&](auto sc_) __attribute__((always_inline)) {
          constexpr int s = decltype(sc_)::value;
          if constexpr (s > 0) PIN_ACC();
          if constexpr (IT == 1) wait_vm<vm_at(IT, s, 0) - vm_a(IT, s)>();
          pin_w<s % WA>(af[s % WA][0], af[s % WA][1]);
          SB();
          static_for<6>([&](auto nc_) __attribute__((always_inline)) {
            constexpr int n = decltype(nc_)::value;
            if constexpr (s == 0) { acc[0][n] = f32x4{0.f, 0.f, 0.f, 0.f}; acc[1][n] = acc[0][n]; }
            constexpr bool on = s < QL || n >= 2;
            if constexpr (on) acc[0][n] = n < 4 ? mfma16(bfr[n].v, af[s % WA][0], acc[0][n]) : mfma16(af[s % WA][0], bfr[n].v, acc[0][n]);
            SB();
            if constexpr (n == 0) { QKV_RDB(s, 5); }
            else if constexpr (s + 1 < 32 && (s + 1 < QL || n > 2)) QKV_RDB(s + 1, n - 1);
            SB();
            if constexpr (on) acc[1][n] = n < 4 ? mfma16(bfr[n].v, af[s % WA][1], acc[1][n]) : mfma16(af[s % WA][1], bfr[n].v, acc[1][n]);
            SB();
            if constexpr (s + 1 < 32 && (s + 1 < QL || n >= 2)) QKV_RDA(s + 1, n);
            SB();
            gap(sc_, nc_);
            SB();
          });
        });
        PIN_ACC();
        frag_t qw[32 - QL][2];
        static_for<32 - QL>([&](auto c_) __attribute__((always_inline)) {
          constexpr int s = QL + decltype(c_)::value;
          QKV_RDA(s, 0); QKV_RDB(s, 0); QKV_RDA(s, 1); QKV_RDB(s, 1);
          qw[s - QL][0] = bfr[0]; qw[s - QL][1] = bfr[1];
        });
        SB();
        const bool own = aff && (int)((sel0 >> 5) & 7) == rt;
        const bool m0 = !(sel0 & 16), mq = (l & 15) == (int)(sel0 & 15), mv = g == (int)((sel0 & 15) >> 2);
        auto patch_qk = [&](int n) __attribute__((always_inline)) {
          f32x4 t = *(const LDS_AS f32x4*)(smem + L_FXL + n * 64 + g * 16);
          const f32x4 c = *(const LDS_AS f32x4*)(smem + L_CSL + n * 64 + g * 16);
          const float nb = *(const LDS_AS float*)(smem + L_FXL + 388);
#pragma unroll
          for (int e = 0; e < 4; ++e) { t[e] = __builtin_fmaf(c[e], nb, t[e]); asm volatile("" : "+v"(t[e])); }
          acc[0][n] = (mq && m0) ? t : acc[0][n];
          acc[1][n] = (mq && !m0) ? t : acc[1][n];
        };
        if (own) {
          while (__hip_atomic_load(qcnt + 3, __ATOMIC_RELAXED, __HIP_MEMORY_SCOPE_WORKGROUP) < 4u) {}
          asm volatile("" ::: "memory");
          patch_qk(2); patch_qk(3);
          const f32x2 nab = *(const LDS_AS f32x2*)(smem + L_FXL + 384);
          const LDS_AS float* fl_ = (const LDS_AS float*)(smem + L_FXL + 256) + (l & 15);
          const LDS_AS float* cl_ = (const LDS_AS float*)(smem + L_CSL + 256) + (l & 15);
          const float t4 = __builtin_fmaf(cl_[0], nab[1], fl_[0]), t5 = __builtin_fmaf(cl_[16], nab[1], fl_[16]);
          const unsigned na = __builtin_bit_cast(unsigned, (float)nab[0]);
#pragma unroll
          for (int a = 0; a < 2; ++a) {
            const bool ma = a ? !m0 : m0;
            aq[a] = (mq && ma) ? na : aq[a];
#pragma unroll
            for (int e = 0; e < 4; ++e) {
              const bool me = mv && ma && e == (int)(sel0 & 3);
              acc[a][4][e] = me ? t4 : acc[a][4][e]; acc[a][5][e] = me ? t5 : acc[a][5][e];
              av[a][e] = me ? na : av[a][e];
            }
          }
        }
        constexpr float C2 = 1.4426950408889634f * 0.17677669529663687f;
#pragma unroll
        for (int a = 0; a < 2; ++a) {
          f32x2 a2 = {asf(aq[a]), asf(aq[a])};
          qs2[a] = f32x2{asf(aq[a]) * C2, asf(aq[a]) * C2};
          asm volatile("" : "+v"(a2), "+v"(qs2[a]));
          acc[a][2] *= f32x4{a2[0], a2[1], a2[0], a2[1]}; acc[a][3] *= f32x4{a2[0], a2[1], a2[0], a2[1]};
          acc[a][4] *= __builtin_bit_cast(f32x4, av[a]); acc[a][5] *= __builtin_bit_cast(f32x4, av[a]);
        }
        k0 = pack8(acc[0][2], acc[0][3]); k1 = pack8(acc[1][2], acc[1][3]); v0 = pack8(acc[0][4], acc[1][4]); v1 = pack8(acc[0][5], acc[1][5]);
        const unsigned kvo = OFF_KV + IT * (8u << 20) + (p * 32 + h) * 32768 + rt * 4096;
        if (p < 7) { st16(k0, rs, l * 16, kvo); st16(k1, rs, l * 16 + 1024, kvo); st16(v0, rs, l * 16 + 2048, kvo); st16(v1, rs, l * 16 + 3072, kvo); }
        char* ownp = smem + (8 + rt) * 2048 + l * 16;
        *(u32x4*)(ownp + L_K) = k0;
        if (rt < 7) *(u32x4*)(ownp + L_K + 1024) = k1;
        *(u32x4*)(ownp + L_V) = v0; *(u32x4*)(ownp + L_V + 1024) = v1;
        SB();
        static_for<32 - QL>([&](auto c_) __attribute__((always_inline)) {
          constexpr int s = QL + decltype(c_)::value;
#pragma unroll
          for (int n = 0; n < 2; ++n) { acc[0][n] = mfma16(qw[s - QL][n].v, af[s % WA][0], acc[0][n]); acc[1][n] = mfma16(qw[s - QL][n].v, af[s % WA][1], acc[1][n]); }
        });
        if (own) { patch_qk(0); patch_qk(1); }
      };
      qkv_loop(it_c);
    }
#undef A_LOAD1
#undef QKV_RDA
#undef QKV_RDB
#undef QKV_BO
    {
      IDS();
      const GAS bf16 *pwg, *pwu;
      asm volatile("s_load_dwordx2 %0, %2, 40\n\ts_load_dwordx2 %1, %2, 48" : "=&s"(pwg), "=&s"(pwu) : "s"(ka));
      const int ql = l & 15;
      const f32x4 z4 = {0.f, 0.f, 0.f, 0.f}, ninf = {-INFINITY, -INFINITY, -INFINITY, -INFINITY};
      LDS_AS unsigned* rdy = (LDS_AS unsigned*)(smem + L_RDY);
      const unsigned rtag = T[0] + it;
      u32x4 qf[2];
      f32x4 cn[2], oa[2][2], sum[2], S[2][2];
      bool told = false;
      auto announce = [&](bool now) __attribute__((always_inline)) {
        if (!told) {
          unsigned st;
          asm volatile("s_getreg_b32 %0, hwreg(HW_REG_IB_STS)" : "=s"(st)::"memory");
          if (now || !(st & 0xC0000Fu)) {
            DRAIN();
            if (l == 0) st8(T, rs, rt * SKW, F_KV + (it * 256 + p * 32 + h) * SKV);
            told = true;
          }
        }
      };
      unsigned one2 = 0x3F803F80u;
      asm volatile("" : "+v"(one2));
      const u32x4 ones = {one2, one2, one2, one2};
      auto ex8 = [&](f32x4 a, f32x4 b) __attribute__((always_inline)) {
        return u32x4{pack2(__builtin_amdgcn_exp2f(a[0]), __builtin_amdgcn_exp2f(a[1])), pack2(__builtin_amdgcn_exp2f(a[2]), __builtin_amdgcn_exp2f(a[3])),
                     pack2(__builtin_amdgcn_exp2f(b[0]), __builtin_amdgcn_exp2f(b[1])), pack2(__builtin_amdgcn_exp2f(b[2]), __builtin_amdgcn_exp2f(b[3]))};
      };
      {
#pragma unroll
        for (int a = 0; a < 2; ++a) {
          const f32x4 c4 = {qs2[a][0], qs2[a][1], qs2[a][0], qs2[a][1]};
          qf[a] = pack8(acc[a][0] * c4, acc[a][1] * c4);
        }
        f32x4 mk;
#pragma unroll
        for (int e = 0; e < 4; ++e) mk[e] = (4 * g + e <= ql) ? 0.f : -INFINITY;
        const f32x4 d0 = mfma16(k0, qf[0], mk), d1 = mfma16(k1, qf[1], mk), d2 = mfma16(k0, qf[1], z4);
        const float m0 = -max_x32(max_x16(fmaxf(fmaxf(d0[0], d0[1]), fmaxf(d0[2], d0[3]))));
        const float m1 = -max_x32(max_x16(fmaxf(fmaxf(d1[0], d1[1]), fmaxf(d1[2], d1[3]))));
        cn[0] = f32x4{m0, m0, m0, m0}; cn[1] = f32x4{m1, m1, m1, m1};
        asm volatile("" : "+v"(cn[0]), "+v"(cn[1]));
        announce(false);
        const f32x4 e0 = d0 + cn[0];
        const u32x4 p0 = {pack2(__builtin_amdgcn_exp2f(e0[0]), __builtin_amdgcn_exp2f(e0[1])), pack2(__builtin_amdgcn_exp2f(e0[2]), __builtin_amdgcn_exp2f(e0[3])), 0u, 0u};
        const u32x4 p1 = ex8(d2 + cn[1], d1 + cn[1]);
        oa[0][0] = mfma16(v0, p0, z4); oa[0][1] = mfma16(v1, p0, z4); sum[0] = mfma16(ones, p0, z4);
        oa[1][0] = mfma16(v0, p1, z4); oa[1][1] = mfma16(v1, p1, z4); sum[1] = mfma16(ones, p1, z4);
      }
      const char* kb_ = smem + L_K + l * 16;
      const char* vb_ = smem + L_V + l * 16;
      const unsigned fnv = (l >> 1) * (F_ * 2) + (l & 1) * 16;
      asm volatile("" : "+v"(oa[0][0]), "+v"(oa[0][1]), "+v"(oa[1][0]), "+v"(oa[1][1]), "+v"(sum[0]), "+v"(sum[1]));
      const unsigned pw = __builtin_amdgcn_readfirstlane(it * 256 + p * 32 + h - 32), pfo = F_KV + pw * SKV + rt * SKW;
      const bool fetcher = rt < 4 && p > 0;
      announce(false);
      BAR();
      asm volatile("" : "+s"(pwg), "+s"(pwu));
#define KV_FETCH(gd)                                                                                                          \
  {                                                                                                                           \
    const unsigned pvo = OFF_KV + pw * 32768 + (gd) * 4096;                                                                   \
    dma16s<0>(rs, l * 16, pvo, lbase + L_K + (gd) * 2048); dma16s<1024>(rs, l * 16, pvo, lbase + L_K + (gd) * 2048);           \
    dma16s<0>(rs, l * 16, pvo + 2048, lbase + L_V + (gd) * 2048); dma16s<1024>(rs, l * 16, pvo + 2048, lbase + L_V + (gd) * 2048); \
  }
      auto fetch = [&]() __attribute__((always_inline)) {
        poll_flags(rs, (l & 1) * 4 * SKW, pfo, T);
        KV_FETCH(rt); KV_FETCH(rt + 4);
        if (l == 0) rdy[8 + rt] = rtag;
      };
      announce(false);
      const bool first = fetcher && told;
      if (first) fetch();
#define LDS16(a_) (*(const LDS_AS u32x4*)(size_t)(a_))
      u32x4 kf[2], vf[2], p0, p1;
#define AE(a, i) { S[a][(i) >> 2][(i) & 3] = __builtin_amdgcn_exp2f(S[a][(i) >> 2][(i) & 3]); asm volatile("" : "+v"(S[a][(i) >> 2][(i) & 3])); SB(); }
#define AC(a, P, j) { P[j] = pack2(S[a][(j) >> 1][2 * ((j) & 1)], S[a][(j) >> 1][2 * ((j) & 1) + 1]); asm volatile("" : "+v"(P[j])); SB(); }
#define PHASE(a, P, M1, M2, L2, M3, M4, L4, M5)                                                                               \
  AE(a, 0) AE(a, 1) M1; SB(); AE(a, 2) AE(a, 3) M2; SB(); L2; SB(); AC(a, P, 0) AC(a, P, 1) AE(a, 4) M3; SB(); AE(a, 5) AE(a, 6) M4; SB(); \
  L4; SB(); AE(a, 7) AC(a, P, 2) M5; SB(); AC(a, P, 3)
#define PV(a, d, P) oa[a][d] = mfma16(vf[d], P, oa[a][d])
#define RS(a, P) sum[a] = mfma16(ones, P, sum[a])
#define SC(a, t, c) S[a][t] = mfma16(kf[t], qf[a], c)
#define K_AT(ad) (kf[0] = LDS16(ad), kf[1] = LDS16((ad) + 1024))
#define V_AT(ad) (vf[0] = LDS16((ad) + L_V), vf[1] = LDS16((ad) + L_V + 1024))
#define A_FIRST(ad, c00, c01, c10, c11, KN) { K_AT(ad); V_AT(ad); SB(); SC(0, 0, c00); SC(0, 1, c01); SB(); PHASE(0, p0, SC(1, 0, c10), SC(1, 1, c11), KN, , , , ) }
#define A_NEXT(ad, c00, c01, c10, c11, KN)                                                                                        \
  { PHASE(1, p1, PV(0, 0, p0), PV(0, 1, p0), , SC(0, 0, c00), SC(0, 1, c01), , RS(0, p0))                                     \
    PHASE(0, p0, PV(1, 0, p1), PV(1, 1, p1), V_AT(ad), SC(1, 0, c10), SC(1, 1, c11), KN, RS(1, p1)) }
#define A_LAST() { PHASE(1, p1, PV(0, 0, p0), PV(0, 1, p0), , RS(0, p0), , , ) SB(); PV(1, 0, p1); PV(1, 1, p1); RS(1, p1); }
      int nw = 0;
      auto piece = [&]() __attribute__((always_inline)) {
        if constexpr (it == 0) { if (ffn_on && nw < 8) { FFN_DMA(nw); } ++nw; }
      };
#define XN_OFF() (F_XN + (((rt - 6) * 4 + g) * RX + bi % RX) * 256 + (l & 15) * 16)
      u32x4 fxn;
      asm volatile("" : "=v"(fxn));
      unsigned ad = lbase + l * 16 + (6 + rt) * 2048;
      asm volatile("" : "+v"(ad));
      if (rt > 0) {
        A_FIRST(ad + 2048, cn[0], cn[0], cn[1], cn[1], K_AT(ad));
#pragma unroll 1
        for (int i = rt - 1; i > 0; --i) {
          ad -= 2048;
          asm volatile("" : "+v"(ad));
          if (told && rt >= 4) piece();
          announce(false);
          A_NEXT(ad + 2048, cn[0], cn[0], cn[1], cn[1], K_AT(ad));
        }
        announce(false);
        A_LAST();
      }
      announce(true);
      {
        f32x4 mk;
#pragma unroll
        for (int e = 0; e < 4; ++e) mk[e] = (4 * g + e > ql) ? 0.f : -INFINITY;
        f32x4 cm0 = cn[0] + mk, cm1 = cn[1] + mk, cmi = ninf;
        asm volatile("" : "+v"(cm0), "+v"(cm1), "+v"(cmi));
        if (rt >= 4) {
          if (p > 0) {
            while (!__all(__hip_atomic_load(rdy + 8 + (l & 3), __ATOMIC_RELAXED, __HIP_MEMORY_SCOPE_WORKGROUP) == rtag)) __builtin_amdgcn_s_sleep(1);
            asm volatile("" ::: "memory");
          }
          if constexpr (it == 0) { for (; nw < 8;) piece(); }
          static_for<it ? 12 : 8>([&](auto c_) __attribute__((always_inline)) { constexpr int i = decltype(c_)::value; WO_PIECE_W(15 - i / 2, 2 * (rt & 3) + (i & 1)); });
          if (it == 0 && rt >= 6) ld16h<0>(fxn, rs, XN_OFF(), 0);
        } else if (p > 0) {
          if (!first) fetch();
          DRAIN();
          if (l < 2) rdy[rt + 4 * l] = rtag;
          if constexpr (it == 0) { for (; nw <= rt;) piece(); }
        }
        if (p > 0) {
          unsigned have = 0;
          auto need = [&](int gq) __attribute__((always_inline)) {
            if (__builtin_expect(gq < 8 && !((have >> gq) & 1), 0)) {
              do {
                const unsigned t = __hip_atomic_load(rdy + (l & 7), __ATOMIC_RELAXED, __HIP_MEMORY_SCOPE_WORKGROUP);
                have = (unsigned)__builtin_amdgcn_ballot_w64(t == rtag);
              } while (!((have >> gq) & 1));
              asm volatile("" ::: "memory");
            }
          };
          ad = lbase + l * 16 + rt * 2048;
          asm volatile("" : "+v"(ad));
          need(rt);
          K_AT(ad); V_AT(ad); SB(); SC(0, 0, cm0); SC(0, 1, cn[0]); SB();
          PHASE(0, p0, SC(1, 0, cmi), SC(1, 1, cm1), , , , , )
          need(rt + 1);
          K_AT(ad + 2048);
#pragma unroll 1
          for (int gq = rt + 1; gq < 8; ++gq) {
            ad += 2048;
            asm volatile("" : "+v"(ad));
            piece();
            need(gq + 1);
            A_NEXT(ad, cn[0], cn[0], cn[1], cn[1], K_AT(ad + 2048));
          }
          A_LAST();
        }
        if constexpr (it == 0) { for (; nw < 8;) piece(); }
      }
      auto o_store = [&]() __attribute__((always_inline)) {
        const unsigned oo = OFF_O + it * (4u << 20) + ((p * 8 + rt) * 64 + h * 2) * 1024;
#pragma unroll
        for (int a = 0; a < 2; ++a) {
          f32x2 inv = {__builtin_amdgcn_rcpf(sum[a][0]), 0.f};
          inv[1] = inv[0];
          asm volatile("" : "+v"(inv));
          const f32x4 i4 = {inv[0], inv[1], inv[0], inv[1]};
          st16(pack8(oa[a][0] * i4, oa[a][1] * i4), rs, l * 16 + a * 1024, oo);
        }
      };
      {
        float big = 0.f;
#pragma unroll
        for (int a = 0; a < 2; ++a)
#pragma unroll
          for (int d = 0; d < 2; ++d)
            big = __builtin_elementwise_maximum(__builtin_elementwise_maximum(big, __builtin_elementwise_maximum(fabsf(oa[a][d][0]), fabsf(oa[a][d][1]))),
                                                __builtin_elementwise_maximum(fabsf(oa[a][d][2]), fabsf(oa[a][d][3])));
        bool ok = sum[0][0] >= 0.5f && sum[0][0] < 0x1p64f && sum[1][0] >= 0.5f && sum[1][0] < 0x1p64f && big < INFINITY;
        if (!__all(ok)) {
#pragma unroll
          for (int a = 0; a < 2; ++a) {
            const int qs = 2 * rt + a, qq = 256 + 16 * qs + ql, klo = max(qq - 255, p > 0 ? 0 : 256);
            auto score = [&](int t) __attribute__((always_inline)) {
              f32x4 s = mfma16(t == 31 ? k1 : *(const u32x4*)(kb_ + t * 1024), qf[a], z4);
#pragma unroll
              for (int e = 0; e < 4; ++e) { const int kk = 16 * t + 4 * g + e; s[e] = (kk <= qq && kk >= klo) ? s[e] : -INFINITY; }
              return s;
            };
            float mx = -INFINITY;
#pragma unroll 1
            for (int t = p > 0 ? qs : 16; t <= qs + 16; ++t) { const f32x4 s = score(t); mx = fmaxf(fmaxf(mx, fmaxf(s[0], s[1])), fmaxf(s[2], s[3])); }
            mx = max_x32(max_x16(mx));
            oa[a][0] = z4; oa[a][1] = z4; sum[a] = z4;
#pragma unroll 1
            for (int G = p > 0 ? qs >> 1 : 8; G <= (qs + 16) >> 1; ++G) {
              const f32x4 s0 = score(2 * G), s1 = score(2 * G + 1);
              f32x4 e0, e1;
#pragma unroll
              for (int e = 0; e < 4; ++e) { e0[e] = __builtin_amdgcn_exp2f(s0[e] - mx); e1[e] = __builtin_amdgcn_exp2f(s1[e] - mx); }
              const u32x4 pf = pack8(e0, e1);
              oa[a][0] = mfma16(*(const u32x4*)(vb_ + G * 2048), pf, oa[a][0]);
              oa[a][1] = mfma16(*(const u32x4*)(vb_ + G * 2048 + 1024), pf, oa[a][1]);
              sum[a] = mfma16(ones, pf, sum[a]);
            }
          }
        }
      }
      o_store();
      if constexpr (it == 0) {
        wait_vm<2>();
        asm volatile("" : "+v"(fxn)::"memory");
        if (rt >= 6 && !__all(fxn[0] == T[0] && fxn[1] == T[1] && fxn[2] == T[0] && fxn[3] == T[1])) poll_flags2(rs, XN_OFF(), T);
        DEF4(gv); DEF4(uv);
        if (ffn_on) {
          const unsigned ro = 65536 + w * 1024 + (4 * g + ((l & 15) >> 2)) * 32 + (l & 3) * 8;
#pragma unroll
          for (int i = 0; i < 4; ++i) {
            frag_t f;
            f.hf.a = trread(smem, ro + i * 16384); f.hf.b = trread(smem, ro + i * 16384 + 512);
            gv[i] = f.v;
            f.hf.a = trread(smem, ro + i * 16384 + 8192); f.hf.b = trread(smem, ro + i * 16384 + 8704);
            uv[i] = f.v;
          }
        }
        PINV4(gv); PINV4(uv);
      }
      BAR();
      if constexpr (it == 1) { WO_PIECE(3); WO_PIECE(2); WO_PIECE(1); WO_PIECE(0); }
      wait_vm<it ? 4 : 0>();
      asm volatile("s_barrier" ::: "memory");
      if (tid == 0) st8(T, rs, h * SO, F_O + (it * 8 + p) * 32 * SO);
      if constexpr (it == 0) {
        WO_PIECE(11); WO_PIECE(10); WO_PIECE(9); WO_PIECE(8); WO_PIECE(7); WO_PIECE(6); WO_PIECE(5); WO_PIECE(4);
        WO_PIECE(3); WO_PIECE(2); WO_PIECE(1); WO_PIECE(0);
      }
    }
    IDS();
    ARGP(pwr, 4); ARGP(pwd, 7); ARGP(pout, 8); ARGP(px, 0);
    const int kh = w >> 2, rt2 = rb * 4 + (w & 3);
    const unsigned R = rt2 * 32 + kh * 16 + (l & 15);
    const unsigned to = (rt2 * 64 + ct * 4 + kh) * 1024 + l * 16;
    const unsigned rmo = ct * 64 + ((g & 1) ? 12 + 4 * g : 4 * g);
    f32x2 wv[8];
    {
      u32x4 wa[2], wrp[2], res[2];
#pragma unroll
      for (int j = 0; j < 2; ++j) {
        wrp[j] = *(const GAS u32x4*)(pwr + rmo + 32 * j);
        if constexpr (it == 0) wa[j] = *(const GAS u32x4*)(pwr + l * 8 + j * 512);
        if constexpr (it == 0) res[j] = *(const GAS u32x4*)(px + (R - (l & 15) + 8 * j + (l >> 3)) * 1024 + ct * 64 + (l & 7) * 8);
        else res[j] = ld16(rs, OFF_XNEW + (2 * ct + j) * 256 + g * 16, 0);
      }
      SB();
      if (w == 0) poll_flags(rs, (l & 31) * SO, F_O + (it * 8 + p) * 32 * SO, T);
      asm volatile("s_barrier" ::: "memory");
      auto prep = [&]() __attribute__((always_inline)) {
        if constexpr (it == 0) {
          f32x2 t[8];
          unpack8v(wa[0], t); unpack8v(wa[1], t + 4);
          const f32x2 s = ((t[0] + t[1]) + (t[2] + t[3])) + ((t[4] + t[5]) + (t[6] + t[7]));
          wsum = sum64(hadd(s));
        }
        unpack8v(trade16(wrp[0]), wv); unpack8v(trade16(wrp[1]), wv + 4);
        if constexpr (it == 0) {
          char* tp = smem + w * 2048;
          const unsigned c0 = (g & 1) * 2 + (g >> 1), r = l & 15;
#pragma unroll
          for (int j = 0; j < 2; ++j) *(u32x4*)(tp + j * 1024 + (l >> 3) * 128 + (((l ^ (l >> 3)) & 7) << 4)) = res[j];
          res[0] = trade16(*(const u32x4*)(tp + r * 128 + ((c0 ^ (r & 7)) << 4)));
          res[1] = trade16(*(const u32x4*)(tp + r * 128 + (((c0 + 4) ^ (r & 7)) << 4)));
        }
        else { res[0] = R == sel0 ? res[0] : x2p[0]; res[1] = R == sel0 ? res[1] : x2p[1]; }
        unpack8v(res[0], v + 0); unpack8v(res[1], v + 4);
        asm volatile("" : "+v"(v[0]), "+v"(v[1]), "+v"(v[2]), "+v"(v[3]), "+v"(v[4]), "+v"(v[5]), "+v"(v[6]), "+v"(v[7]));
        asm volatile("" : "+v"(wsum), "+v"(wv[0]), "+v"(wv[1]), "+v"(wv[2]), "+v"(wv[3]), "+v"(wv[4]), "+v"(wv[5]), "+v"(wv[6]), "+v"(wv[7]));
      };
      {
        const unsigned abase = OFF_O + it * (4u << 20) + (rt2 * 64 + kh) * 1024;
        u32x4 af[W2B], bfr[W2L + 1][4];
        unsigned b2[2] = {lbase + 32768 + l * 16, lbase + 98304 + l * 16};
        asm volatile("" : "+v"(b2[0]), "+v"(b2[1]));
#define O_LOAD(s_) af[(s_) % W2B] = ld16(rs, l * 16 + (~(s_) & 1) * 2048, abase + ((31 - (s_)) >> 1) * 4096)
#define WO_RD(s_, n) bfr[(s_) % (W2L + 1)][n] = *(const LDS_AS u32x4*)(size_t)(b2[(31 - (s_)) >> 4] + ((31 - (s_)) & 15) * 4096 + (n) * 1024)
#pragma unroll
        for (int s = 0; s < W2R; ++s) O_LOAD(s);
        SB();
        prep();
        SB();
        f32x4 ac[4];
#pragma unroll
        for (int n = 0; n < 4; ++n) ac[n] = f32x4{v[2 * n][0], v[2 * n][1], v[2 * n + 1][0], v[2 * n + 1][1]};
#pragma unroll
        for (int n = 0; n < 4 * W2L; ++n) WO_RD(n >> 2, n & 3);
#pragma unroll
        for (int s = 0; s < 32; ++s) {
          if (s == W2P && w >= 4) __builtin_amdgcn_s_setprio(1);
          if (s == (it ? 24 : 8) - W2L) asm volatile("s_barrier" ::: "memory");
        if (it == 0 && s == WPS) {
          unsigned pz1 = POISON;
          asm volatile("" : "+v"(pz1));
          const u32x4 pz = {pz1, pz1, pz1, pz1};
          st16(pz, rs, OFF_PART + (tid >> 8) * (1u << 20) + bid * 4096 + (tid & 255) * 16, 0);
          if (w < 2) st16(pz, rs, OFF_STAT + 524288 + (rb * 16 + ct) * 2048 + tid * 16, 0);
          if (w == 4 && l < 40) st16(pz, rs, l < 16 ? OFF_CA + 65536 + rb * 4096 + ct * 256 + l * 16 : OFF_FX + (h * 8 + p) * 512 + (l - 16) * 16, 0);
          if (w == 6 && l < (ct < (int)RKEY ? 3 : 2)) st4(pz1, rs, l < 2 ? OFF_XNEW + (bid >> 3) * 256 + (bid & 7) * 8 + l * 4 : KEY_AT(1, ct) + rb * SKEY, 0);
        }
          if (s < W2B - W2R) O_LOAD(W2R + 2 * s);
          SB();
#pragma unroll
          for (int n = 0; n < 4; ++n) {
            ac[n] = mfma16(bfr[s % (W2L + 1)][n], af[s % W2B], ac[n]);
            if (s + W2L < 32) WO_RD(s + W2L, n);
            SB();
          }
          if (s < W2B - W2R) O_LOAD(W2R + 2 * s + 1);
          else if (s + W2B < 32) O_LOAD(s + W2B);
          SB();
        }
        __builtin_amdgcn_s_setprio(0);
#undef WO_RD
#undef O_LOAD
#pragma unroll
        for (int n = 0; n < 4; ++n) { v[2 * n] = f32x2{ac[n][0], ac[n][1]}; v[2 * n + 1] = f32x2{ac[n][2], ac[n][3]}; }
      }
    }
    {
      if (it == 0) { st16(pack8v(v), rs, to, OFF_XN + (4u << 20)); st16(pack8v(v + 4), rs, to + 2048, OFF_XN + (4u << 20)); }
      SB();
      const f32x2 s = ((v[0] + v[1]) + (v[2] + v[3])) + ((v[4] + v[5]) + (v[6] + v[7]));
      f32x2 dp = v[0] * wv[0], q = v[0] * v[0];
#pragma unroll
      for (int j = 1; j < 8; ++j) { dp = __builtin_elementwise_fma(v[j], wv[j], dp); q = __builtin_elementwise_fma(v[j], v[j], q); }
      float mt = hadd(s), qq = hadd(q), dot = hadd(dp);
      add_g3(mt, qq, dot);
      const unsigned so = OFF_STAT + it * 524288 + rb * 32768, lrow = R & 127;
      if (it == 0) wait_vm<2>();
      if (g == 0) st16(unpoison4(__builtin_bit_cast(u32x4, f32x4{mt, qq, dot, 0.f})), rs, ct * 2048 + lrow * 16, so);
      SB();
      if (it == 0) wait_vm<1>();
      if constexpr (it == 0) {
        volatile LDS_AS u32x2* in = (volatile LDS_AS u32x2*)(smem + L_XC);
        if (l == 0) in[w] = T;
        const u32x2 t = in[l & 7];
        if (__all(t[0] == T[0] && t[1] == T[1]) && l < (int)RX1) st8(T, rs, (l * 32 + (rb & 1) * 16 + ct) * SX1, F_X1 + (rb >> 1) * RX1 * 32 * SX1);
      }
      if (it == 0) {
        asm volatile("s_barrier" ::: "memory");
        if (ffn_on) { WD_DMA(); }
#pragma unroll
        for (int j = 0; j < 9; ++j) *(u32x4*)(smem + L_RING + (j / 3) * STAGE + (w * 3 + j % 3) * 1024 + l * 16) = keep[j];
      }
      if constexpr (it == 0) {
        if (w == 0) poll_flags(rs, ((l & 2) * 8 + ((ROT0 + 5) & 7) * 2 + (l & 1)) * SX1, F_X1 + (p * RX1 + (xcd >> 1) % RX1) * 32 * SX1, T);
        if (tid == 192) *(LDS_AS u32x4*)(smem + L_QC) = u32x4{0u, 0u, 0u, 0u};
        BAR();
        PRE_LOAD(1, ROT0 + 5, 0, NPRE);
      } else {
        __builtin_amdgcn_s_sleep(SDIR);
        u32x4 sp[4];
#pragma unroll
        for (int i = 0; i < 4; ++i) sp[i] = ld16(rs, (4 * g + i) * 2048 + lrow * 16, so);
        const RowFin r = fin_key(rs, T, g * 8192 + lrow * 16, so, smem + L_XC, l, w, R, wsum, sp);
        BAR();
        fin_cand(rs, smem + L_XC, 1, g, rb, ct, r, v);
        fin_x2(r, v);
      }
    }
    if (it == 1) {
      u32x4 op[2];
      {
        char* tp = smem + L_OUT + w * 2048;
        const unsigned c0 = (g & 1) * 2 + (g >> 1), r = l & 15;
        *(u32x4*)(tp + r * 128 + ((c0 ^ (r & 7)) << 4)) = trade16(pack8v(v));
        *(u32x4*)(tp + r * 128 + (((c0 + 4) ^ (r & 7)) << 4)) = trade16(pack8v(v + 4));
#pragma unroll
        for (int j = 0; j < 2; ++j) op[j] = *(const u32x4*)(tp + j * 1024 + (l >> 3) * 128 + (((l ^ (l >> 3)) & 7) << 4));
      }
      const unsigned Ro = R - (l & 15) + (l >> 3), oo = (Ro * 1024 + ct * 64) * 2 + (l & 7) * 16;
#define OUT_STORE(c0, c1)                                                                           \
  {                                                                                                 \
    if (c0) __builtin_amdgcn_raw_buffer_store_b128(op[0], rout, oo, 0, SC1);                        \
    if (c1) __builtin_amdgcn_raw_buffer_store_b128(op[1], rout, oo + 16384, 0, SC1);                \
  }
      if (w == 0) {
        __builtin_amdgcn_s_sleep(GSL);
        u32x4 xr[4];
        unsigned cur = 0, best;
        asm volatile("" : "=v"(xr[0]), "=v"(xr[1]));
        for (unsigned spins = 0;;) {
          const unsigned k_ = key_load(rs, l, 1, KREP());
          const bool all = __all(k_ != POISON);
          best = __builtin_amdgcn_readfirstlane(umax16(k_ == POISON ? 0u : k_));
          if (best != cur && ffn_on) {
            cur = best;
            ld16h<0>(xr[0], rs, (l >> 3) * 256 + (l & 7) * 16, cand_off(cur, 1)); ld16h<2048>(xr[1], rs, (l >> 3) * 256 + (l & 7) * 16, cand_off(cur, 1));
          }
          if (all || spin_fail(spins, rs, T)) break;
        }
        if (l == 0) *(LDS_AS unsigned*)(smem + L_SKEY + 32) = best;
        asm volatile("s_waitcnt vmcnt(0)" : "+v"(xr[0]), "+v"(xr[1])::"memory");
        if (ffn_on) {
          word_wait<2>(rs, T, (l >> 3) * 256 + (l & 7) * 16, cand_off(best, 1), xr);
          *(LDS_AS u32x4*)(smem + L_ROW + l * 16) = xr[0]; *(LDS_AS u32x4*)(smem + L_ROW + 1024 + l * 16) = xr[1];
        }
      }
      BAR();
      const unsigned key = __builtin_amdgcn_readfirstlane(*(LDS_AS unsigned*)(smem + L_SKEY + 32)), krow = key_row(key);
      if (ffn_on) {
        u32x4 xv[4];
#pragma unroll
        for (int i = 0; i < 4; ++i) xv[i] = *(const LDS_AS u32x4*)(smem + L_ROW + w * 256 + i * 64 + g * 16);
        f32x4 ga, ua;
        ffn_gu(gv, uv, xv, ga, ua);
        ffn_gu_store(smem, l, w, ga, ua);
        BAR();
        ffn_d_store(rs, l, w, 1, slice, ffn_d_all(dfr, ffn_d_h(ffn_d_read(smem, l))));
        SB();
      }
      OUT_STORE(Ro != krow, Ro + 8 != krow);
      if (!ffn_on) {
        const unsigned G = xcd * 4 + bi - 28;
        if (G == 0 && tid == 0) st8(T, rs, F_EP, 0);
        u32x4 pv[4];
        u32x2 cxn, cx1;
        const float sig = red_gate(key);
        __builtin_amdgcn_s_sleep(RSL);
        red_load(rs, tid, 1, G, key, pv, cxn, cx1);
        red_sum(rs, smem, T, tid, w, 1, G, key, pv, cxn, cx1);
        BAR();
        if (w == 0) {
          const u32x2 ov = red_final(smem, l, sig, cxn, cx1);
          if (l < 8) __builtin_amdgcn_raw_buffer_store_b64(ov, rout, (krow * 1024 + G * 32 + l * 4) * 2, 0, 0);
        }
      }
#undef OUT_STORE
    }
  };
  layer(std::integral_constant<int, 0>{});
  layer(std::integral_constant<int, 1>{});
}

extern "C" void kernel_launch(void* const* d_in, const int* in_sizes, int n_in, void* d_out, int out_size,
                              void* d_ws, size_t ws_size, hipStream_t stream) {
  (void)hipFuncSetAttribute((const void*)mega_kernel, hipFuncAttributeMaxDynamicSharedMemorySize, LDS_BYTES);
  mega_kernel<<<256, 512, LDS_BYTES, stream>>>((const bf16*)d_in[0], (char*)d_ws, (const bf16*)d_in[1], (const bf16*)d_in[2], (const bf16*)d_in[4],
                                               (const bf16*)d_in[5], (const bf16*)d_in[6], (const bf16*)d_in[7], (bf16*)d_out);
}
```

```cpp
#include <hip/hip_runtime.h>
#include <stdint.h>
#include <utility>

typedef __bf16 bf16;
typedef __bf16 bf16x8 __attribute__((ext_vector_type(8)));
typedef __bf16 bf16x2 __attribute__((ext_vector_type(2)));
typedef float f32x4 __attribute__((ext_vector_type(4)));
typedef float f32x2 __attribute__((ext_vector_type(2)));
typedef short s16x4 __attribute__((ext_vector_type(4)));
typedef unsigned int u32x4 __attribute__((ext_vector_type(4)));
typedef unsigned int u32x2 __attribute__((ext_vector_type(2)));
#define LDS_AS __attribute__((address_space(3)))
#define GAS __attribute__((address_space(1)))
typedef __amdgpu_buffer_rsrc_t rsrc_t;

constexpr int F_ = 3584;
constexpr int SC1 = 16;
#define PAUX 16

constexpr int RX = 4;
constexpr unsigned F_EP = 0, F_TMO = 128;
constexpr unsigned F_XN = 4096;
#define FLG 256, 1024, 8, 64, 4, 256, 4
constexpr unsigned FLV[7] = {FLG};
constexpr unsigned SO = FLV[0], SKV = FLV[1], SKW = FLV[2], SX1 = FLV[3], RX1 = FLV[4], SKEY = FLV[5], RKEY = FLV[6];
constexpr unsigned F_O = 8u << 20;
constexpr unsigned F_KV = 11u << 20;
constexpr unsigned F_IM = F_O + 524288;
constexpr unsigned F_WOI = F_O + 262144;
constexpr unsigned F_X1 = 9u << 20;
constexpr unsigned OFF_KEY = 10u << 20;
#define KREP() (RKEY == 16 ? bid & 15 : (bid & 7) * RKEY / 8)
#define KEY_AT(L, rep) (OFF_KEY + ((L) * RKEY + (rep)) * (SKEY < 8 ? 128 : 16 * SKEY))
constexpr unsigned OFF_AB = 2u << 20;
constexpr unsigned OFF_XNEW = 262144;
constexpr unsigned OFF_CS = OFF_AB + 65536;
constexpr unsigned OFF_FX = 393216;
constexpr unsigned OFF_CA = 524288;
constexpr unsigned OFF_STAT = 1u << 20;
constexpr unsigned OFF_PART = 3u << 20;
constexpr unsigned OFF_XN = 8u << 20;
constexpr unsigned OFF_O = 16u << 20;
constexpr unsigned OFF_KV = 32u << 20;
constexpr unsigned OFF_WI = 52u << 20;
constexpr unsigned OFF_WOI = 58u << 20;
constexpr unsigned WS_BYTES = 60u << 20;
constexpr unsigned POISON = 0xFFFFFFFFu;

constexpr int L_WOS = 20480, L_ABW = 16640, L_CSP = 17408, L_CSL = 31872, L_FXL = 32256, L_K = 0, L_V = 32768, L_RING = 65536, L_DN = 32768, L_RDY = 31744, L_QC = 31808, LDS_BYTES = 163840;
constexpr int STAGE = 24576;
#define SLOT(t) ((t) & 3)

template <typename F, int... I> __device__ __forceinline__ void static_for_impl(F&& f, std::integer_sequence<int, I...>) {
  (f(std::integral_constant<int, I>{}), ...);
}
template <int N, typename F> __device__ __forceinline__ void static_for(F&& f) { static_for_impl(f, std::make_integer_sequence<int, N>{}); }
__device__ __forceinline__ unsigned pack2(float a, float b) {
  f32x2 v = {a, b};
  return __builtin_bit_cast(unsigned, __builtin_convertvector(v, bf16x2));
}
__device__ __forceinline__ float asf(unsigned u) { return __builtin_bit_cast(float, u); }
__device__ __forceinline__ float lo16(unsigned u) { return __builtin_bit_cast(float, u << 16); }
__device__ __forceinline__ float hi16(unsigned u) { return __builtin_bit_cast(float, u & 0xffff0000u); }
__device__ __forceinline__ u32x4 pack8(const f32x4 a, const f32x4 b) {
  u32x4 o;
  o[0] = pack2(a[0], a[1]); o[1] = pack2(a[2], a[3]); o[2] = pack2(b[0], b[1]); o[3] = pack2(b[2], b[3]);
  return o;
}
__device__ __forceinline__ f32x4 mfma16(u32x4 a, u32x4 b, f32x4 c) {
  return __builtin_amdgcn_mfma_f32_16x16x32_bf16(__builtin_bit_cast(bf16x8, a), __builtin_bit_cast(bf16x8, b), c, 0, 0, 0);
}
__device__ __forceinline__ f32x4 mfma16k16(s16x4 a, s16x4 b, f32x4 c) { return __builtin_amdgcn_mfma_f32_16x16x16bf16_1k(a, b, c, 0, 0, 0); }
template <int CTRL> __device__ __forceinline__ float dppf(float v) {
  return __builtin_bit_cast(float, __builtin_amdgcn_update_dpp(0, __builtin_bit_cast(int, v), CTRL, 0xf, 0xf, false));
}
template <int CTRL> __device__ __forceinline__ unsigned dppu(unsigned v) {
  return (unsigned)__builtin_amdgcn_update_dpp(0, (int)v, CTRL, 0xf, 0xf, false);
}
__device__ __forceinline__ float add_x16(float v) {
  auto r = __builtin_amdgcn_permlane16_swap(__builtin_bit_cast(unsigned, v), __builtin_bit_cast(unsigned, v), false, false);
  return __builtin_bit_cast(float, (unsigned)r[0]) + __builtin_bit_cast(float, (unsigned)r[1]);
}
__device__ __forceinline__ float add_x32(float v) {
  auto r = __builtin_amdgcn_permlane32_swap(__builtin_bit_cast(unsigned, v), __builtin_bit_cast(unsigned, v), false, false);
  return __builtin_bit_cast(float, (unsigned)r[0]) + __builtin_bit_cast(float, (unsigned)r[1]);
}
__device__ __forceinline__ float max_x16(float v) {
  auto r = __builtin_amdgcn_permlane16_swap(__builtin_bit_cast(unsigned, v), __builtin_bit_cast(unsigned, v), false, false);
  return fmaxf(__builtin_bit_cast(float, (unsigned)r[0]), __builtin_bit_cast(float, (unsigned)r[1]));
}
__device__ __forceinline__ float max_x32(float v) {
  auto r = __builtin_amdgcn_permlane32_swap(__builtin_bit_cast(unsigned, v), __builtin_bit_cast(unsigned, v), false, false);
  return fmaxf(__builtin_bit_cast(float, (unsigned)r[0]), __builtin_bit_cast(float, (unsigned)r[1]));
}
__device__ __forceinline__ float sum_hi8(float v) {
  v += dppf<0x128>(v);
  v = add_x16(v);
  return add_x32(v);
}
__device__ __forceinline__ float sum64(float v) {
  v += dppf<0xB1>(v);
  v += dppf<0x4E>(v);
  v += dppf<0x124>(v);
  v += dppf<0x128>(v);
  v = add_x16(v);
  return add_x32(v);
}

#define BAR() asm volatile("s_waitcnt lgkmcnt(0)\n\ts_barrier" ::: "memory")
#define DRAIN() asm volatile("s_waitcnt vmcnt(0)" ::: "memory")
#define SB() __builtin_amdgcn_sched_barrier(0)
template <int N> __device__ __forceinline__ void wait_vm() { asm volatile("s_waitcnt vmcnt(%0)" ::"n"(N) : "memory"); }

__device__ __forceinline__ void dma16(rsrc_t rs, unsigned voff, unsigned soff, unsigned lds) {
  asm volatile("s_mov_b32 m0, %0\n\ts_nop 0\n\tbuffer_load_dwordx4 %1, %2, %3 offen lds" ::"s"(lds), "v"(voff), "s"(rs), "s"(soff) : "memory");
}
__device__ __forceinline__ void dma16n(rsrc_t rs, unsigned voff, unsigned soff, unsigned lds) {
  asm volatile("s_mov_b32 m0, %0\n\ts_nop 0\n\tbuffer_load_dwordx4 %1, %2, %3 offen nt lds" ::"s"(lds), "v"(voff), "s"(rs), "s"(soff) : "memory");
}
template <int OFF> __device__ __forceinline__ void dma16s(rsrc_t rs, unsigned voff, unsigned soff, unsigned lds) {
  asm volatile("s_mov_b32 m0, %0\n\ts_nop 0\n\tbuffer_load_dwordx4 %1, %2, %3 offen offset:%4 sc1 lds" ::"s"(lds), "v"(voff), "s"(rs), "s"(soff), "n"(OFF) : "memory");
}
__device__ __forceinline__ float rbf1(float x) { return __builtin_bit_cast(float, pack2(0.f, x)); }
__device__ __forceinline__ u32x4 ld16(rsrc_t rs, unsigned voff, unsigned soff) { return __builtin_amdgcn_raw_buffer_load_b128(rs, voff, soff, SC1); }
template <int OFF> __device__ __forceinline__ void ld16h(u32x4& d, rsrc_t rs, unsigned voff, unsigned soff) {
  asm volatile("buffer_load_dwordx4 %0, %1, %2, %3 offen offset:%4 sc1" : "=v"(d) : "v"(voff), "s"(rs), "s"(soff), "n"(OFF) : "memory");
}
template <int I, int OFF> __device__ __forceinline__ void ld16w(u32x4& d, rsrc_t rs, unsigned voff, unsigned soff) {
  if constexpr (I == 0) asm volatile("buffer_load_dwordx4 %0, %1, %2, %3 offen offset:%4 sc1" : "={v[128:131]}"(d) : "v"(voff), "s"(rs), "s"(soff), "n"(OFF) : "memory");
  else if constexpr (I == 1) asm volatile("buffer_load_dwordx4 %0, %1, %2, %3 offen offset:%4 sc1" : "={v[132:135]}"(d) : "v"(voff), "s"(rs), "s"(soff), "n"(OFF) : "memory");
  else if constexpr (I == 2) asm volatile("buffer_load_dwordx4 %0, %1, %2, %3 offen offset:%4 sc1" : "={v[136:139]}"(d) : "v"(voff), "s"(rs), "s"(soff), "n"(OFF) : "memory");
  else if constexpr (I == 3) asm volatile("buffer_load_dwordx4 %0, %1, %2, %3 offen offset:%4 sc1" : "={v[140:143]}"(d) : "v"(voff), "s"(rs), "s"(soff), "n"(OFF) : "memory");
  else if constexpr (I == 4) asm volatile("buffer_load_dwordx4 %0, %1, %2, %3 offen offset:%4 sc1" : "={v[144:147]}"(d) : "v"(voff), "s"(rs), "s"(soff), "n"(OFF) : "memory");
  else if constexpr (I == 5) asm volatile("buffer_load_dwordx4 %0, %1, %2, %3 offen offset:%4 sc1" : "={v[148:151]}"(d) : "v"(voff), "s"(rs), "s"(soff), "n"(OFF) : "memory");
  else if constexpr (I == 6) asm volatile("buffer_load_dwordx4 %0, %1, %2, %3 offen offset:%4 sc1" : "={v[152:155]}"(d) : "v"(voff), "s"(rs), "s"(soff), "n"(OFF) : "memory");
  else if constexpr (I == 7) asm volatile("buffer_load_dwordx4 %0, %1, %2, %3 offen offset:%4 sc1" : "={v[156:159]}"(d) : "v"(voff), "s"(rs), "s"(soff), "n"(OFF) : "memory");
  else if constexpr (I == 8) asm volatile("buffer_load_dwordx4 %0, %1, %2, %3 offen offset:%4 sc1" : "={v[160:163]}"(d) : "v"(voff), "s"(rs), "s"(soff), "n"(OFF) : "memory");
  else if constexpr (I == 9) asm volatile("buffer_load_dwordx4 %0, %1, %2, %3 offen offset:%4 sc1" : "={v[164:167]}"(d) : "v"(voff), "s"(rs), "s"(soff), "n"(OFF) : "memory");
  else if constexpr (I == 10) asm volatile("buffer_load_dwordx4 %0, %1, %2, %3 offen offset:%4 sc1" : "={v[168:171]}"(d) : "v"(voff), "s"(rs), "s"(soff), "n"(OFF) : "memory");
  else if constexpr (I == 11) asm volatile("buffer_load_dwordx4 %0, %1, %2, %3 offen offset:%4 sc1" : "={v[172:175]}"(d) : "v"(voff), "s"(rs), "s"(soff), "n"(OFF) : "memory");
}
template <int K> __device__ __forceinline__ void pin_w(u32x4& a, u32x4& b) {
  if constexpr (K == 0) asm volatile("" : "+{v[128:131]}"(a), "+{v[132:135]}"(b)::"memory");
  else if constexpr (K == 1) asm volatile("" : "+{v[136:139]}"(a), "+{v[140:143]}"(b)::"memory");
  else if constexpr (K == 2) asm volatile("" : "+{v[144:147]}"(a), "+{v[148:151]}"(b)::"memory");
  else if constexpr (K == 3) asm volatile("" : "+{v[152:155]}"(a), "+{v[156:159]}"(b)::"memory");
  else if constexpr (K == 4) asm volatile("" : "+{v[160:163]}"(a), "+{v[164:167]}"(b)::"memory");
  else if constexpr (K == 5) asm volatile("" : "+{v[168:171]}"(a), "+{v[172:175]}"(b)::"memory");
}
__device__ __forceinline__ void ld8h(u32x2& d, rsrc_t rs, unsigned voff, unsigned soff) {
  asm volatile("buffer_load_dwordx2 %0, %1, %2, %3 offen sc1" : "=v"(d) : "v"(voff), "s"(rs), "s"(soff) : "memory");
}
template <int OFF> __device__ __forceinline__ void ld4h(unsigned& d, rsrc_t rs, unsigned voff, unsigned soff) {
  asm volatile("buffer_load_dword %0, %1, %2, %3 offen offset:%4 sc1" : "=v"(d) : "v"(voff), "s"(rs), "s"(soff), "n"(OFF) : "memory");
}
__device__ __forceinline__ u32x2 ld8(rsrc_t rs, unsigned voff, unsigned soff) { return __builtin_amdgcn_raw_buffer_load_b64(rs, voff, soff, SC1); }
__device__ __forceinline__ void st16(u32x4 v, rsrc_t rs, unsigned voff, unsigned soff) { __builtin_amdgcn_raw_buffer_store_b128(v, rs, voff + soff, 0, SC1); }
__device__ __forceinline__ void st8(u32x2 v, rsrc_t rs, unsigned voff, unsigned soff) { __builtin_amdgcn_raw_buffer_store_b64(v, rs, voff, soff, SC1); }

__device__ __forceinline__ unsigned ld4(rsrc_t rs, unsigned voff, unsigned soff) { return __builtin_amdgcn_raw_buffer_load_b32(rs, voff, soff, SC1); }
__device__ __forceinline__ void st4(unsigned v, rsrc_t rs, unsigned voff, unsigned soff) { __builtin_amdgcn_raw_buffer_store_b32(v, rs, voff, soff, SC1); }

constexpr unsigned kSpinCap = 1u << 16;
__device__ __forceinline__ bool spin_fail(unsigned& spins, rsrc_t rs, u32x2 T) {
  asm volatile("" ::: "memory");
  ++spins;
  if (spins > kSpinCap) { st8(T, rs, 0, F_TMO); return true; }
  if ((spins & 255u) == 0u) {
    const u32x2 v = ld8(rs, 0, F_TMO);
    if (v[0] == T[0] && v[1] == T[1]) return true;
  }
  __builtin_amdgcn_s_sleep(3);
  return false;
}
__device__ __forceinline__ void poll_flags2(rsrc_t rs, unsigned voff, u32x2 T) {
  for (unsigned spins = 0;;) {
    const u32x4 v = ld16(rs, voff, 0);
    if (__all(v[0] == T[0] && v[1] == T[1] && v[2] == T[0] && v[3] == T[1])) break;
    if (spin_fail(spins, rs, T)) break;
  }
  asm volatile("" ::: "memory");
  __builtin_amdgcn_fence(__ATOMIC_ACQUIRE, "wavefront");
}
__device__ __forceinline__ void tag_wait(rsrc_t rs, u32x2 T, unsigned voff, unsigned soff, u32x4& v) {
  if (__builtin_expect(!__all(v[2] == T[0] && v[3] == T[1]), 0)) {
    unsigned spins = 0;
    do {
      if (spin_fail(spins, rs, T)) break;
      v = ld16(rs, voff, soff);
    } while (!__all(v[2] == T[0] && v[3] == T[1]));
    asm volatile("s_waitcnt vmcnt(0)" : "+v"(v)::"memory");
  }
}
template <int N> __device__ __forceinline__ void word_wait(rsrc_t rs, u32x2 T, unsigned voff, unsigned soff, u32x4 (&v)[4]) {
#define WW_OK() __all(max(max(max(v[0][0], v[0][1]), max(v[0][2], v[0][3])), max(max(v[N - 1][0], v[N - 1][1]), max(v[N - 1][2], v[N - 1][3]))) != POISON)
  if (__builtin_expect(!WW_OK(), 0)) {
    unsigned spins = 0;
    do {
      if (spin_fail(spins, rs, T)) break;
#pragma unroll
      for (int i = 0; i < N; ++i) v[i] = ld16(rs, voff + i * 2048, soff);
    } while (!WW_OK());
    asm volatile("s_waitcnt vmcnt(0)" : "+v"(v[0]), "+v"(v[N - 1])::"memory");
  }
#undef WW_OK
}
__device__ __forceinline__ void poll_flags(rsrc_t rs, unsigned voff, unsigned soff, u32x2 T) {
  for (unsigned spins = 0;;) {
    const u32x2 v = __builtin_amdgcn_raw_buffer_load_b64(rs, voff, soff, PAUX);
    if (__all(v[0] == T[0] && v[1] == T[1])) break;
    if (spin_fail(spins, rs, T)) break;
  }
  asm volatile("" ::: "memory");
  __builtin_amdgcn_fence(__ATOMIC_ACQUIRE, "wavefront");
}

__device__ __forceinline__ void unpack8v(const u32x4 u, f32x2* v) {
#pragma unroll
  for (int j = 0; j < 4; ++j) v[j] = f32x2{lo16(u[j]), hi16(u[j])};
}
__device__ __forceinline__ u32x4 pack8v(const f32x2* v) {
  u32x4 o;
#pragma unroll
  for (int j = 0; j < 4; ++j) o[j] = pack2(v[j][0], v[j][1]);
  return o;
}
__device__ __forceinline__ float ln_rd(float var) { return __builtin_amdgcn_rcpf(rbf1(__builtin_amdgcn_sqrtf(rbf1(rbf1(var) + 1e-5f)))); }
__device__ __forceinline__ float hadd(f32x2 v) {
  float a = v[0];
  asm volatile("" : "+v"(a));
  return a + v[1];
}
__device__ __forceinline__ float add_g(float v) { return add_x32(add_x16(v)); }
__device__ __forceinline__ void add_g3(float& x, float& y, float& z) {
  auto a = __builtin_amdgcn_permlane16_swap(__builtin_bit_cast(unsigned, x), __builtin_bit_cast(unsigned, y), false, false);
  auto b = __builtin_amdgcn_permlane16_swap(__builtin_bit_cast(unsigned, z), __builtin_bit_cast(unsigned, z), false, false);
  const float r = asf(a[0]) + asf(a[1]), t = asf(b[0]) + asf(b[1]);
  auto c = __builtin_amdgcn_permlane32_swap(__builtin_bit_cast(unsigned, r), __builtin_bit_cast(unsigned, r), false, false);
  auto d = __builtin_amdgcn_permlane32_swap(__builtin_bit_cast(unsigned, t), __builtin_bit_cast(unsigned, t), false, false);
  const float u = asf(c[0]) + asf(c[1]);
  z = asf(d[0]) + asf(d[1]);
  auto e = __builtin_amdgcn_permlane16_swap(__builtin_bit_cast(unsigned, u), __builtin_bit_cast(unsigned, u), false, false);
  x = asf(e[0]); y = asf(e[1]);
}
__device__ __forceinline__ u32x4 trade16(u32x4 v) {
  auto a = __builtin_amdgcn_permlane16_swap(v[0], v[2], false, false);
  auto b = __builtin_amdgcn_permlane16_swap(v[1], v[3], false, false);
  return u32x4{(unsigned)a[0], (unsigned)b[0], (unsigned)a[1], (unsigned)b[1]};
}
__device__ __forceinline__ unsigned umax4(u32x4 v) { return max(max(v[0], v[1]), max(v[2], v[3])); }
__device__ __forceinline__ u32x2 unpoison2(u32x2 v) { return u32x2{min(v[0], POISON - 1), min(v[1], POISON - 1)}; }
__device__ __forceinline__ u32x4 unpoison4(u32x4 v) {
  return u32x4{min(v[0], POISON - 1), min(v[1], POISON - 1), min(v[2], POISON - 1), min(v[3], POISON - 1)};
}

typedef union { struct { s16x4 a, b; } hf; u32x4 v; } frag_t;
__device__ __forceinline__ s16x4 trread(const char* smem, unsigned off) {
  return __builtin_amdgcn_ds_read_tr16_b64_v4i16((LDS_AS s16x4*)(smem + off));
}
__device__ __forceinline__ s16x4 trabs(unsigned addr) { return __builtin_amdgcn_ds_read_tr16_b64_v4i16((LDS_AS s16x4*)(size_t)addr); }

constexpr int WA = 6;
#define WPS 16
#define CH 0, 6, 8, 9, 15, 18, 23, 25, 29, 31, 4, 4
constexpr int CHV[12] = {CH};
constexpr int FKL = CHV[10], FSN = CHV[11];
constexpr int SPL = CHV[0];
constexpr int FSL = CHV[1], FS0 = CHV[2], FS0D = CHV[3], FS1 = CHV[4], FS1S = CHV[5];
constexpr int FXA = CHV[6], FXB = CHV[7], FXL = CHV[8], FXS = CHV[9];
#define ROT0 p
#define L0S 22, 25, 1, 0, 24, 28, 1, 2, 4, 14
constexpr int L0V[10] = {L0S};
constexpr int CSL = L0V[0], CSS = L0V[1], CSD = L0V[2];
constexpr int abl(int it) { return it ? 25 : L0V[4]; }
constexpr int abs_(int it) { return it ? 27 : L0V[5]; }
constexpr int W2S = L0V[6], WSH = L0V[7];
constexpr int PRE0 = 12;
constexpr int LNL = L0V[3], LNS = L0V[8], POI = L0V[9];
#define TCH 22, 3, 0x07, 26, 0xF8, 0, 1
constexpr int TCV[7] = {TCH};
constexpr int TS0 = TCV[0], TSN = TCV[1], TM0 = TCV[2], TEX = TCV[3], TM1 = TCV[4], TSD = TCV[5], TSG = TCV[6];
constexpr int QL = 26;
#define SLP 18, 4, 28, 3, 0, 0
constexpr int SLV[6] = {SLP};
constexpr int SDIR = SLV[0], GSL = SLV[1], RSL = SLV[2], KSL = SLV[3];
#define IME 5, 7, 12, 14, 1, 2
constexpr int IMV[6] = {IME};
constexpr int IMB = IMV[4], IMD = IMV[5];
constexpr int im_st(int s, int n) { return IMB ? (s == CSD + 1 && (n & 1) ? n >> 1 : -1) : (s > CSD && s <= CSD + 3 && n == 5 ? s - CSD - 1 : -1); }
constexpr int IMT = IMV[0], IMF = IMV[1], IML = IMV[2], IMK = IMV[3];
constexpr int tp0(int it) { return it ? 3 : 3 + WSH; }
static_assert(((IMF + 1) & ~3) - 2 >= CSD + (IMB ? 5 : 7) && (IMK & 3) == 2 && IMK < 4 * IMT - 12 + 3 + WSH + IMD && WSH + IMD <= 4, "a wait for a younger request and a barrier in between");
constexpr int tile_at(int it, int t) { return 4 * t - 12 + tp0(it) + (!it && t == IMT ? IMD : !it && t == IMT + 1 && IMD > 1 ? IMD - 1 : 0); }
constexpr int piece_tile(int it, int s) { for (int t = 3; t < 8; ++t) if (s >= tile_at(it, t) && s < tile_at(it, t) + 3) return t; return -1; }
constexpr bool tile_piece(int it, int s) { return piece_tile(it, s) >= 0; }
#define PS 14, 10, 10, 0
constexpr int PSV[4] = {PS};
constexpr int PSX = PSV[0], PSO = PSV[1], PSO0 = PSV[2], PSK = PSV[3];
#define NPRE 3
#define QAD 4
#define WO2 5, 3, 2, 8
constexpr int W2V[4] = {WO2};
#define WOE 1, 7, 15, 19, 22
constexpr int WEV[5] = {WOE};
constexpr int WOL = WEV[0], WOS = WEV[1], WOF = WEV[2], WOC = WEV[3], WOK = WEV[4];
static_assert(((WOS + 1) & ~3) - 2 >= WOL + 4 && ((WOF + 1) & ~3) - 2 >= WOS + 5 && (WOK & 3) == 2, "a wait for a younger request and a barrier in between");
constexpr int W2B = W2V[0], W2R = W2V[1], W2L = W2V[2], W2P = W2V[3];
constexpr int a_req(int s, int n) {
  int r = 2 * NPRE;
  for (int u = 1; u < 192; u += 2) {
    const bool go = r < 64 && (r >> 1) <= u / 6 + QAD && (r < 8 || u >= 9);
    if (u == 6 * s + n) return go ? r : -1;
    r += go;
  }
  return -1;
}
constexpr int vm_n(int it, int s, int n) {
  int c = it ? (a_req(s, n) >= 0) + (s == 0 && n == 0) : (s + 5 < 32 && (n == 2 || n == 3));
  if (n == 0) c += tile_piece(it, s);
  if (n == 4) c += (s == abl(it));
  if (!it) c += (s >= W2S && s < W2S + 3 && n == 4) + (s == LNL && n == 5 ? 3 : 0) + (s == 0 && n < 2 ? 2 : 0) + (s == LNS + 2 && n == 5) + (s == TEX && n == 5) + (s == WOL && n == 5) + (s == WOS + 1 && n == 5) + (im_st(s, n) >= 0);
  else c += (s == 2 && n == 2) + (s == (SPL < 0 ? 0 : SPL) && n == (SPL < 0 ? 1 : 4) ? 4 : 0) + (s >= 27 && s < 30 && (n == 0 || n == 3));
  return c;
}
constexpr int vm_at(int it, int s, int n) {
  int c = it ? 2 * NPRE : PRE0;
  for (int u = 0; u < 6 * s + n; ++u) c += vm_n(it, u / 6, u % 6);
  return c;
}
constexpr int vm_a(int it, int k) {
  if (k < NPRE) return 2 * k + 2;
  for (int u = 0; u < 192; ++u)
    if (a_req(u / 6, u % 6) == 2 * k + 1) return vm_at(it, (u + 1) / 6, (u + 1) % 6);
  return 0;
}
constexpr int vm_tile(int it, int t) { return t >= 3 ? vm_at(it, tile_at(it, t) + 2, 1) : t == 1 ? 12 : vm_at(0, W2S + 2, 5); }
constexpr int vm_unit(int u) { return u < 3 ? 5 + 2 * u : u < 5 ? vm_at(0, 0, u - 2) : vm_at(0, u - 5, 4); }
constexpr int unit_slot(int u) { return u % 3 == 0 ? 0 : 16384 + (u % 3) * 16384; }

constexpr int L_XC = LDS_BYTES - 64, L_SKEY = 32704, L_ROW = 1024, L_OUT = 65536, L_SRED = 4096, L_SFIN = 20480;
__device__ __forceinline__ unsigned key_load(rsrc_t rs, int l, int L, int rep) { return ld4(rs, (l & 15) * SKEY, KEY_AT(L, rep)); }
__device__ __forceinline__ unsigned umax16(unsigned k) {
  k = max(k, dppu<0xB1>(k));
  k = max(k, dppu<0x4E>(k));
  k = max(k, dppu<0x141>(k));
  return max(k, dppu<0x140>(k));
}
__device__ __forceinline__ unsigned key_row(unsigned key) { return (0xffffu - (key & 0xffffu)) & 2047u; }
__device__ __forceinline__ unsigned key_wait(rsrc_t rs, u32x2 T, int l, int L, int rep, unsigned kv) {
  if (!__all(kv != POISON)) {
    unsigned spins = 0;
    do {
      if (spin_fail(spins, rs, T)) break;
      if (L == 0) __builtin_amdgcn_s_sleep(KSL);
      kv = key_load(rs, l, L, rep);
    } while (!__all(kv != POISON));
  }
  return __builtin_amdgcn_readfirstlane(umax16(kv));
}
__device__ __forceinline__ unsigned cand_off(unsigned key, int L) { return OFF_CA + L * 65536 + (key_row(key) >> 7) * 4096; }
#define XO(i) (((i) >> 1) * 256 + ((i) & 1) * 64)
__device__ __forceinline__ void ffn_gu(const u32x4 (&gv)[4], const u32x4 (&uv)[4], const u32x4 (&xv)[4], f32x4& ga, f32x4& ua) {
  ga = mfma16(gv[0], xv[0], f32x4{0.f, 0.f, 0.f, 0.f}); ua = mfma16(uv[0], xv[0], f32x4{0.f, 0.f, 0.f, 0.f});
#pragma unroll
  for (int i = 1; i < 4; ++i) { ga = mfma16(gv[i], xv[i], ga); ua = mfma16(uv[i], xv[i], ua); }
}
__device__ __forceinline__ void ffn_gu_store(char* smem, int l, int w, f32x4 ga, f32x4 ua) {
  LDS_AS f32x4* sred = (LDS_AS f32x4*)(smem + L_SRED);
  if ((l & 15) == 0) { sred[w * 8 + (l >> 4)] = ga; sred[w * 8 + 4 + (l >> 4)] = ua; }
}
__device__ __forceinline__ f32x4 ffn_d_read(char* smem, int l) {
  const LDS_AS float* t = (const LDS_AS float*)(smem + L_SRED) + (l & 8) * 16 + (l & 4) * 4 + (l >> 4) * 4 + (l & 3);
  return f32x4{t[0], t[32], t[64], t[96]};
}
__device__ __forceinline__ u32x2 ffn_d_h(f32x4 t) {
  float x = t[0] + t[1];
  asm volatile("" : "+v"(x));
  x += t[2] + t[3];
  x += dppf<0x128>(x);
  const float y = dppf<0x124>(x);
  const bool up = __builtin_amdgcn_inverse_ballot_w64(0xF0F0F0F0F0F0F0F0ull);
  const float gg = rbf1(up ? y : x), uu = rbf1(up ? x : y);
  const float h = rbf1(gg * __builtin_amdgcn_rcpf(1.0f + __expf(-gg))) * uu;
  const unsigned p = pack2(h, dppf<0xB1>(h));
  return u32x2{dppu<0x00>(p), dppu<0xAA>(p)};
}
template <int t> __device__ __forceinline__ f32x4 ffn_d_mma(s16x4 d, u32x2 hh, f32x4 y) {
  const bool me = __builtin_amdgcn_inverse_ballot_w64(0x0001000100010001ull << t);
  return mfma16k16(d, __builtin_bit_cast(s16x4, u32x2{me ? hh[0] : 0u, me ? hh[1] : 0u}), y);
}
__device__ __forceinline__ f32x4 ffn_d_all(const s16x4 (&dfr)[8], u32x2 hh) {
  f32x4 a = {0.f, 0.f, 0.f, 0.f}, b = a;
  a = ffn_d_mma<0>(dfr[0], hh, a); b = ffn_d_mma<1>(dfr[1], hh, b); a = ffn_d_mma<2>(dfr[2], hh, a); b = ffn_d_mma<3>(dfr[3], hh, b);
  a = ffn_d_mma<4>(dfr[4], hh, a); b = ffn_d_mma<5>(dfr[5], hh, b); a = ffn_d_mma<6>(dfr[6], hh, a); b = ffn_d_mma<7>(dfr[7], hh, b);
  return a + b;
}
__device__ __forceinline__ void ffn_d_store(rsrc_t rs, int l, int w, int L, int slice, f32x4 y) {
  const unsigned c = l & 15;
  if (c < 8) st16(unpoison4(__builtin_bit_cast(u32x4, y)), rs, (2 * w + (c >> 2)) * 65536 + slice * 256 + (c & 3) * 64 + (l >> 4) * 16, OFF_PART + L * (1u << 20));
}
#define RED_VO(k) ((min(tid + 512 * (k), 1791) >> 3) * 256 + (tid & 7) * 16)
#define RED_SO(L, G) (OFF_PART + (L) * (1u << 20) + ((G) >> 1) * 65536 + ((G) & 1) * 128)
#define RED_JJ() ((tid & 3) * 16 + (tid & 4) * 2)
__device__ __forceinline__ void red_load(rsrc_t rs, int tid, int L, unsigned G, unsigned key, u32x4 (&pv)[4], u32x2& cxn, u32x2& cx1) {
#pragma unroll
  for (int k = 0; k < 4; ++k) pv[k] = ld16(rs, RED_VO(k), RED_SO(L, G));
  cxn = ld8(rs, RED_JJ(), cand_off(key, L) + XO(G));
  cx1 = ld8(rs, RED_JJ() + 128, cand_off(key, L) + XO(G));
}
__device__ __forceinline__ void red_sum(rsrc_t rs, char* smem, u32x2 T, int tid, int w, int L, unsigned G, unsigned key,
                                        u32x4 (&pv)[4], u32x2& cxn, u32x2& cx1) {
#define PV_OK() __all(max(max(max(umax4(pv[0]), umax4(pv[1])), max(umax4(pv[2]), umax4(pv[3]))), max(max(cxn[0], cxn[1]), max(cx1[0], cx1[1]))) != POISON)
  if (!PV_OK()) {
    unsigned spins = 0;
    do {
      if (spin_fail(spins, rs, T)) break;
#pragma unroll
      for (int k = 0; k < 4; ++k)
        if (umax4(pv[k]) == POISON) pv[k] = ld16(rs, RED_VO(k), RED_SO(L, G));
      if (max(max(cxn[0], cxn[1]), max(cx1[0], cx1[1])) == POISON) { cxn = ld8(rs, RED_JJ(), cand_off(key, L) + XO(G)); cx1 = ld8(rs, RED_JJ() + 128, cand_off(key, L) + XO(G)); }
    } while (!PV_OK());
  }
#undef PV_OK
  f32x4 s = (__builtin_bit_cast(f32x4, pv[0]) + __builtin_bit_cast(f32x4, pv[1])) + __builtin_bit_cast(f32x4, pv[2]);
  if (w < 4) s += __builtin_bit_cast(f32x4, pv[3]);
#pragma unroll
  for (int j = 0; j < 4; ++j) s[j] = sum_hi8(s[j]);
  if ((tid & 63) < 8) ((LDS_AS f32x4*)(smem + L_SFIN))[w * 8 + (tid & 7)] = s;
}
__device__ __forceinline__ float red_gate(unsigned key) {
  const unsigned k16 = key >> 16;
  const float rmax = __builtin_bit_cast(float, ((k16 & 0x8000u) ? (k16 & 0x7fffu) : (~k16 & 0xffffu)) << 16);
  float sig = rbf1(__builtin_amdgcn_rcpf(1.0f + __expf(-rmax)));
  asm volatile("" : "+v"(sig));
  return sig;
}
__device__ __forceinline__ u32x2 red_final(char* smem, int l, float sig, u32x2 cxn, u32x2 cx1) {
  f32x4 s = ((const LDS_AS f32x4*)(smem + L_SFIN))[l];
#pragma unroll
  for (int j = 0; j < 4; ++j) s[j] = sum_hi8(s[j]);
  const float xn4[4] = {lo16(cxn[0]), hi16(cxn[0]), lo16(cxn[1]), hi16(cxn[1])};
  const float x14[4] = {lo16(cx1[0]), hi16(cx1[0]), lo16(cx1[1]), hi16(cx1[1])};
  float o[4];
#pragma unroll
  for (int j = 0; j < 4; ++j) o[j] = x14[j] + rbf1(xn4[j] + rbf1(rbf1(s[j]) * sig));
  return u32x2{pack2(o[0], o[1]), pack2(o[2], o[3])};
}

struct RowFin { float mean, var, mb, rd; unsigned key; };
__device__ __forceinline__ void fin_wait(rsrc_t rs, u32x2 T, unsigned voff, unsigned so, u32x4 (&sp)[4]) {
#define ST_OK() __all(max(max(umax4(sp[0]), umax4(sp[1])), max(umax4(sp[2]), umax4(sp[3]))) != POISON)
  if (!ST_OK()) {
    unsigned spins = 0;
    do {
      if (spin_fail(spins, rs, T)) break;
#pragma unroll
      for (int i = 0; i < 4; ++i) sp[i] = ld16(rs, voff + i * 2048, so);
    } while (!ST_OK());
  }
#undef ST_OK
}
__device__ __forceinline__ RowFin fin_key(rsrc_t rs, u32x2 T, unsigned voff, unsigned so, char* skey, int l, int w, unsigned R, float wsum, u32x4 (&sp)[4]) {
  RowFin r;
#define ST_SUM() ((__builtin_bit_cast(f32x4, sp[0]) + __builtin_bit_cast(f32x4, sp[1])) + (__builtin_bit_cast(f32x4, sp[2]) + __builtin_bit_cast(f32x4, sp[3])))
  f32x4 sf = ST_SUM();
  const float z = (sf[0] + sf[1]) + sf[2];
  if (__any(z != z)) {
    fin_wait(rs, T, voff, so, sp);
    sf = ST_SUM();
  }
#undef ST_SUM
  float s0 = sf[0], s1 = sf[1], s2 = sf[2];
  add_g3(s0, s1, s2);
  r.mean = s0 * (1.0f / 1024.0f);
  r.var = fmaxf(__builtin_fmaf(-r.mean, r.mean, s1 * (1.0f / 1024.0f)), 0.f);
  r.mb = rbf1(r.mean); r.rd = ln_rd(r.var);
  const unsigned rb16 = pack2((s2 - r.mb * wsum) * r.rd, 0.f) & 0xffffu;
  r.key = min((((rb16 & 0x8000u) ? (~rb16 & 0xffffu) : (rb16 | 0x8000u)) << 16) | (0xffffu - R), POISON - 1);
  const unsigned wk = umax16(r.key);
  if (l == 0) ((LDS_AS unsigned*)skey)[w] = wk;
  return r;
}
__device__ __forceinline__ void fin_cand(rsrc_t rs, char* skey, int L, int g, int rb, int ct, const RowFin& r, const f32x2 (&v)[8]) {
  const u32x4 k0 = *(const LDS_AS u32x4*)skey, k1 = *(const LDS_AS u32x4*)(skey + 16);
  const unsigned best = max(umax4(k0), umax4(k1));
  if (r.key == best) {
    const unsigned co = L * 65536 + rb * 4096 + ct * 256 + g * 16;
    f32x2 t[8], rd2 = {r.rd, r.rd}, of2 = {-r.mb * r.rd, -r.mb * r.rd};
    asm volatile("" : "+v"(rd2), "+v"(of2));
#pragma unroll
    for (int j = 0; j < 8; ++j) t[j] = __builtin_elementwise_fma(v[j], rd2, of2);
    st16(unpoison4(pack8v(t)), rs, co, OFF_CA); st16(unpoison4(pack8v(t + 4)), rs, co + 64, OFF_CA);
    if (g == 0 && ct < (int)RKEY) st4(best, rs, KEY_AT(L, ct) + rb * SKEY, 0);
    st16(unpoison4(pack8v(v)), rs, co + 128, OFF_CA); st16(unpoison4(pack8v(v + 4)), rs, co + 192, OFF_CA);
  }
}
__device__ __forceinline__ void fin_x2(const RowFin& r, f32x2 (&v)[8]) {
  f32x2 sc2 = {1.0f + r.rd, 1.0f + r.rd}, of2 = {-r.mb * r.rd, -r.mb * r.rd};
  asm volatile("" : "+v"(sc2), "+v"(of2));
#pragma unroll
  for (int j = 0; j < 8; ++j) v[j] = __builtin_elementwise_fma(v[j], sc2, of2);
}

extern "C" __device__ unsigned long long dispatch_id() __asm("llvm.amdgcn.dispatch.id");

extern "C" __global__ __launch_bounds__(512, 2) void mega_kernel(const bf16* x, char* ws, const bf16* wqkv, const bf16* wo, const bf16* wr,
                                                                 const bf16* wg, const bf16* wu, const bf16* wd, bf16* out) {
  extern __shared__ __attribute__((aligned(16))) char smem[];
  const int w0_ = __builtin_amdgcn_readfirstlane(threadIdx.x >> 6);
  const unsigned lbase = (unsigned)(size_t)(LDS_AS char*)smem;
  {
    unsigned long long pc;
    unsigned len;
    asm volatile("s_getpc_b64 %0\n\ts_mov_b32 %1, .Ltext_end-mega_kernel\n\t.subsection 1\n.Ltext_end:\n\t.subsection 0" : "=s"(pc), "=s"(len));
    const unsigned o = ((blockIdx.x >> 3) * 64 + (threadIdx.x & 63)) * 128;
    if (w0_ == 7 && o < len)
      asm volatile("s_mov_b32 m0, %0\n\ts_nop 0\n\tglobal_load_lds_dword %1, %2" ::"s"(lbase + L_FXL), "v"(o), "s"(pc & ~127ull) : "memory");
  }
  asm volatile("" : "+s"(x), "+s"(ws), "+s"(wqkv));
#define IDS()                                                                                         \
  int tid = threadIdx.x, w = w0_, bid = blockIdx.x;                                                   \
  asm volatile("" : "+v"(tid));                                                                       \
  asm volatile("" : "+s"(w));                                                                         \
  asm volatile("" : "+s"(bid));                                                                       \
  const int l = tid & 63, xcd = bid & 7, bi = bid >> 3;                                               \
  const int p = (xcd & 1) * 4 + (bi & 3), h = (xcd >> 1) * 8 + (bi >> 2);         \
  const int rb = 2 * p + ((h >> 3) & 1), ct = (h >> 4) * 8 + (h & 7);       \
  const int slice = xcd * 28 + bi;                                                   \
  const bool ffn_on = bi < 28;                                                                        \
  const int g = l >> 4;                                                                               \
  const int rt = w < 4 ? w : 11 - w;          \
  KARGS();                                                                                            \
  (void)p; (void)h; (void)rb; (void)ct; (void)slice; (void)ffn_on; (void)g; (void)rt;
  const rsrc_t rs = __builtin_amdgcn_make_buffer_rsrc((void*)ws, 0, WS_BYTES, 0x00020000);
  typedef const void* const __attribute__((address_space(4)))* karg_t;
#define KARGS() karg_t ka = (karg_t)__builtin_amdgcn_kernarg_segment_ptr(); asm volatile("" : "+s"(ka))
#define ARGP(name, i) const GAS bf16* name = (const GAS bf16*)ka[i]; asm volatile("" : "+s"(name))
#define RSRC(ptr, bytes) __builtin_amdgcn_make_buffer_rsrc((void*)(ptr), 0, bytes, 0x00020000)
#define rwq RSRC(pwq, 1024 * 3072 * 2)
#define rwo RSRC(pwo, 1024 * 1024 * 2)
#define rwg RSRC(pwg, 1024 * F_ * 2)
#define rwu RSRC(pwu, 1024 * F_ * 2)
#define rwd RSRC(pwd, 1024 * F_ * 2)
#define rout RSRC(pout, 2048 * 1024 * 2)

#define WQ_VOFF() ((l >> 2) * 6144 + (((l & 3) ^ ((((l >> 4) ^ (l >> 5)) & 1) << 1)) << 4))
#define WQ_PIECE(t, j, dst) dma16(rwq, wqv, ((((t) + rot) & 7) * 128 + ((j) & 7) * 16) * 6144 + ((j) >> 3) * 2048 + h * 64, lbase + (dst) + (j) * 1024)
#define WQ_BOFF(dt) ((8 * g + ((l & 15) >> 2)) * 64 + (l & 3) * 8 + (((dt) ^ (g & 1)) << 5))
#define WQ_POFF() ((4 * g + ((l & 15) >> 2)) * 64 + (l & 3) * 8 + (((g ^ (g >> 1)) & 1) << 5))
#define X_UNIT1(u, j) dma16(rx, (j) ? xv1 : xv_, (unsigned)(p * 256 + rt * 32 + ((u) & 1) * 16 + (j) * 8) * 2048 + ((((u) >> 2) + rot) & 7) * 256 + (((u) >> 1) & 1) * 128, lbase + rt * 2048 + unit_slot(u) + (j) * 1024)
#define X_VOFF() ((l >> 3) * 2048 + (((l & 7) ^ (l >> 4)) << 4))
#define X_ROFF(hs) (lbase + rt * 2048 + (l & 15) * 128 + (((4 * (hs) + g) ^ ((l >> 1) & 7)) << 4))
#define X_POFF() (lbase + rt * 2048 + (l & 15) * 128 + (((g >> 1) ^ ((l >> 1) & 7)) << 4) + (g & 1) * 8)
#define TOUCH_TO(ptr, dst) asm volatile("s_mov_b32 m0, %0\n\ts_nop 0\n\tglobal_load_lds_dword %1, off" ::"s"(lbase + (dst)), "v"(ptr) : "memory")
#define TOUCH(ptr) TOUCH_TO(ptr, L_FXL)
#define WO_PIECE_W(j, w_)                                                                                                                 \
  {                                                                                                                                       \
    dma16s<0>(rs, l * 16, OFF_WOI + ct * 131072 + (j) * 8192 + (w_) * 1024, lbase + 32768 + (j) * 8192 + (w_) * 1024);                    \
  }
#define WO_PIECE(j) WO_PIECE_W(j, w)
#define WO_VOFF() ((l >> 3) * 2048 + (((((l & 7) >> 1) ^ ((l >> 4) & 3)) * 2 + (l & 1)) << 4))
#define WD_DMA() _Pragma("unroll") for (int i = 0; i < 4; ++i) WD_DMA1(i)
#define WD_DMA1(i)                                                                                   \
      dma16n(rwd, (l >> 4) * 2048 + (((l & 15) * 16) ^ ((l >> 4) * 32) ^ (((i) & 1) * 128)), (unsigned)(slice * 16 + 4 * (i)) * 2048 + w * 256, lbase + L_DN + w * 4096 + (i) * 1024)
#define FFN_DMA(m) dma16((m) & 1 ? rwu : rwg, fnv, (unsigned)(((4 * w + ((m) >> 1)) * 32) * F_ + slice * 16) * 2, lbase + 65536 + (m) * 8192 + w * 1024)
  u32x4 gv[4], uv[4];
  s16x4 dfr[8];
  u32x4 keep[9];
  u32x4 pre[4][2];
#define PRE_LOAD(it_, rot_, s0, s1)                                                                  \
  static_for<(s1) - (s0)>([&](auto c_) __attribute__((always_inline)) {                              \
    constexpr int s = (s0) + decltype(c_)::value;                                                    \
    const unsigned ab_ = OFF_XN + (it_) * (4u << 20) + (((rot_) + (s >> 2)) & 7) * 8192 + ((s >> 1) & 1) * 4096; \
    ld16w<2 * s, (s & 1) * 2048>(pre[s][0], rs, l * 16, ab_ + (p * 8 + rt) * 65536);                         \
    ld16w<2 * s + 1, (s & 1) * 2048 + 1024>(pre[s][1], rs, l * 16, ab_ + (p * 8 + rt) * 65536);                 \
  })
  u32x2 T;
  u32x2 ep;
  u32x4 xl[2];
#define LN_LOAD()                                                                                                                                   \
  {                                                                                                                                                 \
    asm volatile("buffer_load_dwordx4 %0, %1, %2, %3 offen" : "=v"(xl[0]) : "v"(l * 16), "s"(rx), "s"((p * 256 + h * 8 + w) * 2048) : "memory");      \
    asm volatile("buffer_load_dwordx4 %0, %1, %2, %3 offen offset:1024" : "=v"(xl[1]) : "v"(l * 16), "s"(rx), "s"((p * 256 + h * 8 + w) * 2048) : "memory"); \
    asm volatile("buffer_load_dwordx2 %0, off, %1, 0 sc1" : "=v"(ep) : "s"(rs) : "memory");                                                         \
  }
  const GAS bf16* tsrc;
  {
    IDS();
    const int it = 0, rot = ROT0;
    (void)it;
    const bf16* pwq = wqkv;
    const rsrc_t rx = RSRC(x, 2048 * 1024 * 2);
    const unsigned wqv = WQ_VOFF(), xv_ = X_VOFF(), xv1 = xv_ ^ 64;
    if (tid == 0) *(LDS_AS u32x4*)(smem + L_QC) = u32x4{0u, 0u, 0u, 0u};
    if (w == 0 && l < 24) TOUCH((const GAS bf16*)wqkv + (bid * 24 + l) * 512 + ((3 - (unsigned)((size_t)wqkv >> 7)) & 7) * 64);
#pragma unroll
    for (int i = 0; i < 3; ++i) WQ_PIECE(0, 3 * w + i, L_RING);
    X_UNIT1(0, 0); X_UNIT1(0, 1); X_UNIT1(1, 0); X_UNIT1(1, 1);
    BAR();
    X_UNIT1(2, 0); X_UNIT1(2, 1);
#pragma unroll
    for (int i = 0; i < 3; ++i) WQ_PIECE(1, 3 * w + i, L_RING + STAGE);
  }
  unsigned sel0 = 0x7FFFFFFFu;
  f32x2 v[8];
  u32x4 x2p[2];
  float wsum;

  auto layer = [&](auto it_c) __attribute__((always_inline)) {
    constexpr int it = decltype(it_c)::value;
    f32x4 acc[2][6];
    f32x2 qs2[2];
    u32x4 k0, k1, v0, v1;
    {
      IDS();
      bool aff = false;
      unsigned aq[2];
      u32x4 av[2];
#define PIN_ACC() asm volatile("" : "+{v[176:179]}"(acc[0][0]), "+{v[180:183]}"(acc[0][1]), "+{v[184:187]}"(acc[0][2]), "+{v[188:191]}"(acc[0][3]), "+{v[192:195]}"(acc[0][4]), "+{v[196:199]}"(acc[0][5]), \
                               "+{v[200:203]}"(acc[1][0]), "+{v[204:207]}"(acc[1][1]), "+{v[208:211]}"(acc[1][2]), "+{v[212:215]}"(acc[1][3]), "+{v[216:219]}"(acc[1][4]), "+{v[220:223]}"(acc[1][5]))
#define PINV4(x) asm volatile("" : "+v"(x[0]), "+v"(x[1]), "+v"(x[2]), "+v"(x[3])::"memory")
      auto qkv_loop = [&](auto it_c) __attribute__((always_inline)) {
        constexpr int IT = decltype(it_c)::value;
        const unsigned abase = OFF_XN + IT * (4u << 20) + (p * 8 + rt) * 65536;
        const int rot = ROT0 + 5 * IT;
        unsigned fkey = 0, kv, bq[2];
        u32x4 fx[4], bv[2], af[WA][2];
        u32x2 cxn, cx1, hh;
        f32x4 sa, sb, cs4[2];
        f32x2 b2[2];
#define AB_OFF() (OFF_AB + IT * 32768 + (p * 8 + rt) * 512)
#define A_LOAD1(k, a) ld16w<(k) % WA * 2 + (a), ((k) & 1) * 2048 + (a) * 1024>(af[(k) % WA][a], rs, l * 16, abase + ((((k) >> 2) + rot) & 7) * 8192 + (((k) >> 1) & 1) * 4096)
        if constexpr (IT == 1) {
#pragma unroll
          for (int s = 0; s < NPRE; ++s) { af[s][0] = pre[s][0]; af[s][1] = pre[s][1]; }
        }
        const bf16* pwq = wqkv;
        ARGP(pwo, 3);
        const rsrc_t rx = RSRC(x, 2048 * 1024 * 2);
        unsigned xv_ = X_VOFF(), xv1 = xv_ ^ 64, xr[4] = {X_ROFF(0), X_ROFF(1), 0u, 0u}, bf_[2];
        if constexpr (IT == 0) asm volatile("" : "+v"(xv_), "+v"(xv1), "+v"(xr[0]), "+v"(xr[1]));
#define LDS8(a_) (*(const LDS_AS u32x2*)(size_t)(a_))
#define X_READ(u)                                                                                                              \
  {                                                                                                                            \
    if constexpr ((u) < 4 * IMT) {                                                                                             \
      af[((u) & ~1) % WA][(u) & 1] = *(const LDS_AS u32x4*)(size_t)(xr[0] + unit_slot(u));                                     \
      af[((u) | 1) % WA][(u) & 1] = *(const LDS_AS u32x4*)(size_t)(xr[1] + unit_slot(u));                                      \
    } else {                                                                                                                   \
      const u32x2 a_ = LDS8(xr[0] + unit_slot(u)), b_ = LDS8(xr[1] + unit_slot(u)), c_ = LDS8(xr[2] + unit_slot(u)), d_ = LDS8(xr[3] + unit_slot(u)); \
      af[((u) & ~1) % WA][(u) & 1] = u32x4{a_[0], a_[1], b_[0], b_[1]};                                                        \
      af[((u) | 1) % WA][(u) & 1] = u32x4{c_[0], c_[1], d_[0], d_[1]};                                                         \
    }                                                                                                                          \
  }
        const int tstep = TS0 + bid % TSN + TSD * (w >> TSG);
        const unsigned wqv = WQ_VOFF();
#define QKV_DMA1(t, i)                                                                            \
  {                                                                                               \
    if constexpr (IT == 0 && (t) < IMT) WQ_PIECE(t, 3 * w + (i), L_RING + SLOT(t) * STAGE);       \
    else dma16s<(i) * 1024>(rs, l * 16, OFF_WI + (h * 8 + (((t) + rot) & 7)) * STAGE + w * 3072, lbase + L_RING + SLOT(t) * STAGE + w * 3072); \
  }
        unsigned bo[2][2];
#pragma unroll
        for (int dt = 0; dt < 2 - IT; ++dt) {
          bo[dt][0] = lbase + L_RING + (IT ? l * 16 : WQ_BOFF(dt)); bo[dt][1] = bo[dt][0] + 2 * STAGE;
          asm volatile("" : "+v"(bo[dt][0]), "+v"(bo[dt][1]));
        }
        auto frag0 = [&](int sl, int f) __attribute__((always_inline)) {
          unsigned pb = lbase + L_RING + WQ_POFF();
          asm volatile("" : "+v"(pb));
          const unsigned o = (pb ^ ((f & 1) << 5)) + sl * STAGE + ((f % 6) >> 1) * 8192 + (f / 6) * 2048;
          frag_t t;
          t.hf.a = trabs(o); t.hf.b = trabs(o + 1024);
          return t.v;
        };
        u32x4 df[4];
        auto dot_mma = [&](const u32x4 (&xf)[4]) __attribute__((always_inline)) {
          sa = mfma16(df[0], xf[0], f32x4{0.f, 0.f, 0.f, 0.f}); sb = mfma16(df[1], xf[1], f32x4{0.f, 0.f, 0.f, 0.f});
          sa = mfma16(df[2], xf[2], sa); sb = mfma16(df[3], xf[3], sb);
        };
        auto dot_store = [&](unsigned dst) __attribute__((always_inline)) {
          if ((l & 15) == 0) st16(unpoison4(__builtin_bit_cast(u32x4, sa + sb)), rs, g * 16, dst + (h * 8 + p) * 512 + w * 64);
        };
        auto tile_sum = [&](u32x4 pt, int dst) __attribute__((always_inline)) {
          f32x4 c = __builtin_bit_cast(f32x4, pt);
#pragma unroll
          for (int e = 0; e < 4; ++e) { c[e] += dppf<0x124>(c[e]); c[e] += dppf<0x128>(c[e]); c[e] = add_x16(c[e]); }
          if ((l & 28) == 0) *(LDS_AS f32x4*)(smem + dst + (w * 2 + (l >> 5)) * 64 + (l & 3) * 16) = c;
        };
        frag_t bfr[6];
#define QKV_BO(s, n, x) (bo[((n) ^ (x)) & 1][SLOT((s) >> 2) >> 1] + (SLOT((s) >> 2) & 1) * STAGE + ((n) >> 1) * 8192 + ((s) & 3) * 2048 + (x) * 256)
#define QKV_RDA(s, n)                                                                             \
  {                                                                                               \
    if constexpr (IT == 0 && (s) < 4 * IMT) bfr[n].hf.a = trabs(QKV_BO(s, n, 0));                 \
    else if constexpr (IT == 0) bfr[n].v = *(const LDS_AS u32x4*)(size_t)(bf_[SLOT((s) >> 2) >> 1] + (SLOT((s) >> 2) & 1) * STAGE + (((s) & 3) * 6 + (n)) * 1024); \
    else bfr[n].v = *(const LDS_AS u32x4*)(size_t)(bo[0][SLOT((s) >> 2) >> 1] + (SLOT((s) >> 2) & 1) * STAGE + (((s) & 3) * 6 + (n)) * 1024); \
  }
#define QKV_RDB(s, n) { if constexpr (IT == 0 && (s) < 4 * IMT) bfr[n].hf.b = trabs(QKV_BO(s, n, 1)); }
#define XP_OFF() (OFF_XNEW + ((rot + 5) & 7) * 1024)
#define FP_VO() (w == 3 ? (l >> 2) * 256 + (l & 3) * 16 : ((l >> 2) & 7) * 512 + (l >> 5) * 64 + (l & 3) * 16)
#define FP_OFF(i) (w == 3 ? OFF_XNEW + (i) * 4096 : OFF_FX + h * 4096 + w * 128)
#define FX_OK(n) __all(((n) == 4 ? max(max(umax4(fx[0]), umax4(fx[1])), max(umax4(fx[2]), umax4(fx[3]))) : max(umax4(fx[0]), umax4(fx[1]))) != POISON)
#define SINCE(s0) (vm_at(IT, s, n) - vm_at(IT, s0, 5))
#define DEF4(x) asm volatile("" : "=v"(x[0]), "=v"(x[1]), "=v"(x[2]), "=v"(x[3]))
        LDS_AS unsigned* qcnt = (LDS_AS unsigned*)(smem + L_QC);
        unsigned qc;
        unsigned qadr = lbase + L_QC, qone = 1;
        asm volatile("" : "+v"(qadr), "+v"(qone));
#define count_in(i) asm volatile("s_mov_b64 exec, 1\n\tds_add_u32 %0, %1 offset:%2\n\ts_mov_b64 exec, -1" ::"v"(qadr), "v"(qone), "n"(4 * (i)) : "memory")
        auto count_wait = [&](int i, unsigned want) __attribute__((always_inline)) {
          if (__builtin_expect((unsigned)__builtin_amdgcn_readfirstlane(qc) < want, 0)) {
            while ((unsigned)__builtin_amdgcn_readfirstlane(__hip_atomic_load(qcnt + i, __ATOMIC_RELAXED, __HIP_MEMORY_SCOPE_WORKGROUP)) < want) {}
          }
          asm volatile("" ::: "memory");
        };
        if constexpr (IT == 0) {
          wait_vm<PRE0 - vm_unit(1)>();
          X_READ(0); X_READ(1);
          asm volatile("s_barrier" ::: "memory");
        }
#pragma unroll
        for (int n = 0; n < 6; ++n) { QKV_RDA(0, n); if (n < 5) QKV_RDB(0, n); }
        f32x2 lv[4], xs, xq;
        float lmean;
        f32x4 sc;
#define X1_OFF() (F_X1 + ((p * RX1 + (xcd >> 1) % RX1) * 32 + (rt >> 2) * 16 + (l & 15)) * SX1)
        unsigned long long xok = 0;
        RowFin fin;
        auto x1_need = [&](unsigned long long m) __attribute__((always_inline)) {
          if (__builtin_expect((xok & m) != m, 0)) {
            for (unsigned spins = 0;;) {
              const u32x2 f = ld8(rs, X1_OFF(), 0);
              xok = __builtin_amdgcn_ballot_w64(f[0] == T[0] && f[1] == T[1]);
              if ((xok & m) == m) break;
              if (spin_fail(spins, rs, T)) { xok = ~0ull; break; }
              __builtin_amdgcn_s_sleep(KSL);
            }
            asm volatile("" ::: "memory");
          }
        };
        auto gap = [&](auto sc_, auto nc_) __attribute__((always_inline)) {
          constexpr int s = decltype(sc_)::value, n = decltype(nc_)::value, ar = a_req(s, n);
          {
            constexpr int tc = (s + 2) / 4;
            if constexpr (tc >= 1 && tc < 8 && s == 4 * tc - 2 && n == 5) {
              if constexpr (!IT || tc >= 3) wait_vm<vm_at(IT, s, n) - vm_tile(IT, tc)>();
              if constexpr (tc == 7 && !IT) asm volatile("s_waitcnt lgkmcnt(4)" ::: "memory");
              asm volatile("s_barrier" ::: "memory");
            }
          }
          if constexpr (IT == 0 && s == 4 * IMT - 3 && n == 5) {
            xr[0] = X_POFF(); xr[1] = xr[0] ^ 32; xr[2] = xr[0] ^ 64; xr[3] = xr[0] ^ 96;
            bf_[0] = lbase + L_RING + l * 16; bf_[1] = bf_[0] + 2 * STAGE;
            asm volatile("" : "+v"(xr[0]), "+v"(xr[1]), "+v"(xr[2]), "+v"(xr[3]), "+v"(bf_[0]), "+v"(bf_[1]));
          }
          if constexpr (IT == 0 && n == 0 && s + 2 < 32) { wait_vm<vm_at(0, s, 0) - vm_unit(s + 2)>(); X_READ(s + 2); SB(); }
          if constexpr (n == 0 && tile_piece(IT, s)) { constexpr int t_ = piece_tile(IT, s), i_ = s - tile_at(IT, t_); QKV_DMA1(t_, i_); }
          if constexpr (IT == 0) {
            if constexpr ((n == 2 || n == 3) && s + 5 < 32) {
              if constexpr (n == 2) asm volatile("s_waitcnt lgkmcnt(%0)" ::"n"(s + 1 < QL ? (s + 1 < 4 * IMT ? 4 : 2) : 1) : "memory");
              X_UNIT1(s + 5, n - 2);
            }
            if constexpr (s >= W2S && s < W2S + 3 && n == 4) QKV_DMA1(2, s - W2S);
            if constexpr (s == LNL && n == 5) LN_LOAD();
            if constexpr (s == 0 && n < 2) { X_UNIT1(3 + n, 0); X_UNIT1(3 + n, 1); }
            if constexpr (s == LNS && n == 1) {
              wait_vm<vm_at(0, s, n) - vm_at(0, LNL + 1, 0)>();
              asm volatile("" : "+v"(ep), "+v"(xl[0]), "+v"(xl[1])::"memory");
              const unsigned long long e = (((unsigned long long)(unsigned)__builtin_amdgcn_readfirstlane(ep[1]) << 32) | (unsigned)__builtin_amdgcn_readfirstlane(ep[0])) + 1ull + dispatch_id() * 0x9E3779B97F4A7C16ull;
              T = u32x2{(unsigned)e, (unsigned)(e >> 32)};
              unpack8v(xl[0], lv);
              xs = (lv[0] + lv[1]) + (lv[2] + lv[3]);
              xq = __builtin_elementwise_fma(lv[0], lv[0], lv[1] * lv[1]) + __builtin_elementwise_fma(lv[2], lv[2], lv[3] * lv[3]);
              asm volatile("" : "+v"(xs), "+v"(xq));
            }
            if constexpr (s == LNS && n == 4) {
              unpack8v(xl[1], lv);
              xs += (lv[0] + lv[1]) + (lv[2] + lv[3]);
              xq += __builtin_elementwise_fma(lv[0], lv[0], lv[1] * lv[1]) + __builtin_elementwise_fma(lv[2], lv[2], lv[3] * lv[3]);
              auto t = __builtin_amdgcn_permlane32_swap(__builtin_bit_cast(unsigned, hadd(xs)), __builtin_bit_cast(unsigned, hadd(xq)), false, false);
              lmean = asf(t[0]) + asf(t[1]);
              asm volatile("" : "+v"(lmean));
            }
            if constexpr (s == LNS + 1 && n == 1) { lmean += dppf<0xB1>(lmean); lmean += dppf<0x4E>(lmean); lmean += dppf<0x124>(lmean); asm volatile("" : "+v"(lmean)); }
            if constexpr (s == LNS + 1 && n == 4) { lmean = add_x16(lmean + dppf<0x128>(lmean)) * (1.0f / 1024.0f); asm volatile("" : "+v"(lmean)); }
            if constexpr (s == LNS + 2 && n == 1) {
              const float m2 = asf(__builtin_amdgcn_readlane(__builtin_bit_cast(unsigned, lmean), 32));
              xs[0] = ln_rd(fmaxf(__builtin_fmaf(-lmean, lmean, m2), 0.f));
              asm volatile("" : "+v"(xs));
            }
            if constexpr (s == LNS + 2 && n == 5) {
              if (l == 0) st16(u32x4{__builtin_bit_cast(unsigned, (float)xs[0]), __builtin_bit_cast(unsigned, -rbf1(lmean)), T[0], T[1]}, rs, OFF_AB + (p * 256 + h * 8 + w) * 16, 0);
            }
            if constexpr (s == POI && n == 1) {
              if (w == 1) {
                const unsigned prb = 2 * p + (h >> 4), pct = h & 15;
                unsigned pz1 = POISON;
                asm volatile("" : "+v"(pz1));
                const u32x4 pz = {pz1, pz1, pz1, pz1};
                st16(pz, rs, OFF_STAT + (prb * 16 + pct) * 2048 + l * 16, 0);
                st16(pz, rs, OFF_STAT + (prb * 16 + pct) * 2048 + 1024 + l * 16, 0);
                if (l < 16) st16(pz, rs, OFF_CA + prb * 4096 + pct * 256 + l * 16, 0);
                else if (l == 16 && pct < RKEY) st4(pz1, rs, KEY_AT(0, pct) + prb * SKEY, 0);
              }
            }
            if constexpr (s == POI + 4 && n == 1) { if (w == 1 && l < RX) st8(T, rs, F_XN + (p * RX + l) * 256 + h * 8, 0); }
          } else {
            if constexpr ((s == 0 && n == 0) || (s == 2 && n == 2)) asm volatile("buffer_load_dwordx2 %0, %1, %2, 0 offen sc1" : "=v"(cxn) : "v"(X1_OFF()), "s"(rs) : "memory");
            if constexpr ((s == 1 && n == 3) || (s == 4 && n == 0)) {
              wait_vm<vm_at(1, s, n) - (s == 1 ? vm_at(1, 0, 1) : vm_at(1, 2, 3))>();
              asm volatile("" : "+v"(cxn)::"memory");
              xok |= __builtin_amdgcn_ballot_w64(cxn[0] == T[0] && cxn[1] == T[1]);
            }
            if constexpr (ar >= 0 && (ar & 7) == 0) x1_need(3ull << (((rot + ar / 8) & 7) * 2));
            const unsigned R = (rb * 4 + (w & 3)) * 32 + (w >> 2) * 16 + (l & 15), so = OFF_STAT + rb * 32768;
            if constexpr (s == (SPL < 0 ? 0 : SPL) && n == (SPL < 0 ? 1 : 4)) {
#pragma unroll
              for (int i = 0; i < 4; ++i) ld16h<0>(fx[i], rs, (4 * g + i) * 2048 + (R & 127) * 16, so);
            }
            if constexpr (s == SPL + 2 && n == 1) {
              wait_vm<vm_at(1, s, n) - (SPL < 0 ? vm_at(1, 0, 2) : vm_at(1, SPL, 5))>();
              PINV4(fx);
              fin = fin_key(rs, T, g * 8192 + (R & 127) * 16, so, smem + L_SKEY, l, w, R, wsum, fx);
              count_in(2);
            }
            if constexpr (s == SPL + 3 && n == 0) qc = __hip_atomic_load(qcnt + 2, __ATOMIC_RELAXED, __HIP_MEMORY_SCOPE_WORKGROUP);
            if constexpr (s == SPL + 3 && n == 1) { count_wait(2, 8); fin_cand(rs, smem + L_SKEY, 0, g, rb, ct, fin, v); }
            if constexpr (s == SPL + 3 && n == 3) {
              fin_x2(fin, v);
              x2p[0] = pack8v(v); x2p[1] = pack8v(v + 4);
              asm volatile("" : "+v"(x2p[0]), "+v"(x2p[1]));
            }
            if constexpr (s == SPL + 3 && n == 5) {
              if ((w & 3) * 2 + (w >> 2) == ct >> 1) {
                const float f = 1.0f + fin.rd, rd2 = ln_rd(fin.var * f * f), mb2 = rbf1(__builtin_fmaf(fin.mean - fin.mb, fin.rd, fin.mean));
                if (g == 0 && ((l >> 3) & 1) == (ct & 1))
                  st16(u32x4{__builtin_bit_cast(unsigned, f * rd2), __builtin_bit_cast(unsigned, -__builtin_fmaf(fin.mb, fin.rd, mb2) * __builtin_amdgcn_rcpf(f)), T[0], T[1]}, rs, OFF_AB + 32768 + R * 16, 0);
              }
            }
          }
          if constexpr (IT == 1 && ar >= 0) A_LOAD1(ar / 2, ar & 1);
          if constexpr (!IT && s == CSL && n == 4) { asm volatile("" : "=v"(fx[0])); if (w < 6) ld16h<0>(fx[0], rs, l * 16, OFF_CS + (h * 6 + w) * 1024); }
          if constexpr (!IT && s == CSS && n == 2) {
            if (w < 6) {
              wait_vm<SINCE(CSL)>();
              asm volatile("" : "+v"(fx[0])::"memory");
              tag_wait(rs, T, l * 16, OFF_CS + (h * 6 + w) * 1024, fx[0]);
              const f32x2 c = {sum_hi8(asf(fx[0][0])), sum_hi8(asf(fx[0][1]))};
              if (l < 8) *(LDS_AS f32x2*)(smem + L_CSL + w * 64 + l * 8) = c;
            }
          }
          constexpr int ABL = abl(IT), ABS = abs_(IT);
          if constexpr (s == ABL && n == 4) ld16h<0>(bv[0], rs, (l & 31) * 16, AB_OFF());
          if constexpr (s == ABS && n == 2) {
            wait_vm<SINCE(ABL)>();
            asm volatile("" : "+v"(bv[0])::"memory");
            tag_wait(rs, T, (l & 31) * 16, AB_OFF(), bv[0]);
            const unsigned t0 = bv[0][0], t1 = bv[0][1];
            ((LDS_AS unsigned*)(smem + L_ABW + rt * 2048))[l] = l < 32 ? t0 : t1;
          }
          if constexpr (s == ABS && n == 3) {
#pragma unroll
            for (int a = 0; a < 2; ++a) {
              const LDS_AS unsigned* aw = (const LDS_AS unsigned*)(smem + L_ABW + rt * 2048) + 16 * a;
              aq[a] = aw[l & 15]; bq[a] = aw[32 + (l & 15)];
              av[a] = *(const LDS_AS u32x4*)(aw + 4 * g); bv[a] = *(const LDS_AS u32x4*)(aw + 32 + 4 * g);
            }
          }
          if constexpr (s == ABS && n == 5) {
#pragma unroll
            for (int a = 0; a < 2; ++a) { b2[a] = f32x2{asf(bq[a]), asf(bq[a])}; asm volatile("" : "+v"(b2[a])); }
          }
          if constexpr (s == ABS + 1) {
            constexpr int k = (n + 4) % 6;
            if constexpr (k < 4) cs4[k & 1] = *(const LDS_AS f32x4*)(smem + L_CSL + k * 64 + g * 16);
            else cs4[k & 1][0] = *(const LDS_AS float*)(smem + L_CSL + k * 64 + (l & 15) * 4);
          }
          if constexpr ((s == ABS + 1 && n > 0) || (s == ABS + 2 && n == 0)) {
            constexpr int k = (n + 3) % 6;
            if constexpr (k < 4) {
#pragma unroll
              for (int a = 0; a < 2; ++a) acc[a][k] = __builtin_elementwise_fma(cs4[k & 1], f32x4{b2[a][0], b2[a][1], b2[a][0], b2[a][1]}, acc[a][k]);
            } else {
              f32x2 c2 = {cs4[k & 1][0], cs4[k & 1][0]};
              asm volatile("" : "+v"(c2));
#pragma unroll
              for (int a = 0; a < 2; ++a) acc[a][k] = __builtin_elementwise_fma(f32x4{c2[0], c2[1], c2[0], c2[1]}, __builtin_bit_cast(f32x4, bv[a]), acc[a][k]);
            }
          }
          if constexpr (IT == 0) {
            if constexpr (s == CSD && n < 3) fx[n] = frag0(SLOT(s >> 2), 3 * w + n);
            if constexpr (s == CSD + 1 && n == 1) {
              unsigned one2 = 0x3F803F80u;
              asm volatile("" : "+v"(one2));
              const u32x4 o4 = {one2, one2, one2, one2};
              const f32x4 z4 = {0.f, 0.f, 0.f, 0.f};
              sa = mfma16(fx[0], o4, z4); sb = mfma16(fx[1], o4, z4); sc = mfma16(fx[2], o4, z4);
            }
            constexpr int is_ = im_st(s, n);
            if constexpr (is_ >= 0) st16(fx[is_ < 0 ? 0 : is_], rs, l * 16 + is_ * 1024, OFF_WI + (h * 8 + ((rot + (CSD >> 2)) & 7)) * STAGE + w * 3072);
            if constexpr (s == IMF && n == 1) { if (tid == 0) st8(T, rs, (h * 8 + p) * 256, F_IM); }
            if constexpr (s == IML && n == 4) { asm volatile("" : "=v"(cxn)); if (w == 0) ld8h(cxn, rs, (l & 7) * 256, F_IM + h * 2048); }
            if constexpr (s == IMK && n == 3) {
              if (w == 0) {
                wait_vm<SINCE(IML)>();
                asm volatile("" : "+v"(cxn)::"memory");
                if (!__all(cxn[0] == T[0] && cxn[1] == T[1])) poll_flags(rs, (l & 7) * 256, F_IM + h * 2048, T);
              }
            }
            if constexpr (s == CSD + 2 && n == 1) {
              const int c = l & 15;
              const f32x4 t0 = sa, t1 = sb, t2 = sc, t = c == 0 ? +t0 : c == 1 ? +t1 : +t2;
              if (c < 3) *(LDS_AS f32x4*)(smem + L_CSP + (w * 3 + c) * 64 + g * 16) = t;
              count_in(2);
            }
            if constexpr (s == CSD + 4 && n == 1) {
              if (w == 2) {
                qc = 0;
                count_wait(2, 8);
                const LDS_AS f32x2* cp = (const LDS_AS f32x2*)(smem + L_CSP) + min(l, 47);
                const f32x2 t = (cp[0] + cp[48]) + (cp[96] + cp[144]);
                if (l < 48) st16(u32x4{__builtin_bit_cast(unsigned, (float)t[0]), __builtin_bit_cast(unsigned, (float)t[1]), T[0], T[1]}, rs, (l >> 3) * 1024 + (l & 7) * 16, OFF_CS + h * 6144 + p * 128);
              }
            }
            if constexpr (s == WOL && n == 5) dma16(rwo, WO_VOFF(), (rb * 8 + w) * 16384 + ct * 128, lbase + L_WOS + w * 1024);
            if constexpr (s == WOS && n == 1) {
              const unsigned o_ = lbase + L_WOS + (w >> 2) * 4096 + (4 * g + ((l & 15) >> 2)) * 128 + (l & 3) * 8 + (((w & 3) ^ (2 * (g & 1) + ((l & 15) >> 3))) << 5);
              frag_t f_;
              f_.hf.a = trabs(o_); f_.hf.b = trabs(o_ + 2048);
              fx[3] = f_.v;
            }
            if constexpr (s == WOS + 1 && n == 5) st16(fx[3], rs, (ct * 128 + rb * 8 + w) * 1024 + l * 16, OFF_WOI);
            if constexpr (s == WOF && n == 1) { if (tid == 128) st8(T, rs, (ct * 16 + rb) * 256, F_WOI); }
            if constexpr (s == WOC && n == 4) { asm volatile("" : "=v"(cx1)); if (w == 3) ld8h(cx1, rs, (l & 15) * 256, F_WOI + ct * 4096); }
            if constexpr (s == WOK && n == 3) {
              if (w == 3) {
                wait_vm<SINCE(WOC)>();
                asm volatile("" : "+v"(cx1)::"memory");
                if (!__all(cx1[0] == T[0] && cx1[1] == T[1])) poll_flags(rs, (l & 15) * 256, F_WOI + ct * 4096, T);
              }
            }
            if constexpr (s >= 29 && (n & 1)) {
              if constexpr (s - 24 < IMT) keep[3 * (s - 29) + (n >> 1)] = frag0(SLOT(s - 24), 3 * w + (n >> 1));
              else keep[3 * (s - 29) + (n >> 1)] = *(const LDS_AS u32x4*)(size_t)(bf_[0] + SLOT(s - 24) * STAGE + w * 3072 + (n >> 1) * 1024);
            }
            if constexpr (s == (TS0 < TEX ? TS0 : TEX) - 2 && n == 1) {
              ARGP(pwg, 5); ARGP(pwu, 6);
              tsrc = (w < 4 ? pwg : pwu) + (bi * 32 + (w & 3) * 8 + (l >> 3)) * F_ + xcd * 448 + min(l & 7, 6) * 64;
              asm volatile("" : "+v"(tsrc));
            }
            if constexpr (s >= TS0 && s < TS0 + TSN + TSD * (7 >> TSG) && n == 4) {
              if (tstep == s && ((TM0 >> w) & 1)) TOUCH(tsrc);
            }
            if constexpr (s == TEX && n == 5) {
              const GAS bf16* t2 = ((TM1 >> w) & 1) ? tsrc : (const GAS bf16*)x + p * 262144;
              TOUCH(t2);
            }
          } else {
            if constexpr (s >= 27 && s < 30 && (n == 0 || n == 3)) WO_PIECE(4 + 2 * (s - 27) + n / 3);
            if constexpr (s == FKL && n == 4) { asm volatile("" : "=v"(kv)); if (w == 0) ld4h<0>(kv, rs, (l & 15) * SKEY, KEY_AT(0, KREP())); }
            if constexpr (s == FSL && n == 1) {
              asm volatile("" : "=v"(fx[0]), "=v"(fx[1]));
              if (w == 0) {
                wait_vm<SINCE(FKL)>();
                asm volatile("" : "+v"(kv)::"memory");
                fkey = __builtin_amdgcn_readfirstlane(umax16(kv == POISON ? 0u : kv));
                ld4h<0>(kv, rs, (l & 15) * SKEY, KEY_AT(0, KREP()));
                ld16h<0>(fx[0], rs, (l >> 3) * 256 + (l & 7) * 16, cand_off(fkey, 0)); ld16h<2048>(fx[1], rs, (l >> 3) * 256 + (l & 7) * 16, cand_off(fkey, 0));
              }
            }
            if constexpr (s == FS0 - 1 && n == FSN) {
              if (w == 0) {
                wait_vm<vm_at(1, s, n) - vm_at(1, FSL, 2)>();
                asm volatile("" : "+v"(fx[0]), "+v"(fx[1]), "+v"(kv)::"memory");
                const unsigned k_ = key_wait(rs, T, l, 0, KREP(), kv);
                if (l == 0) *(LDS_AS unsigned*)(smem + L_SKEY + 32) = k_;
                if (k_ != fkey) fx[0][0] = POISON;
                word_wait<2>(rs, T, (l >> 3) * 256 + (l & 7) * 16, cand_off(k_, 0), fx);
                *(LDS_AS u32x4*)(smem + L_ROW + l * 16) = fx[0]; *(LDS_AS u32x4*)(smem + L_ROW + 1024 + l * 16) = fx[1];
                count_in(0);
              }
            }
            if constexpr (s == FS0 - 1 && n == 1) {
              asm volatile("" : "=v"(dfr[0]), "=v"(dfr[1]), "=v"(dfr[2]), "=v"(dfr[3]), "=v"(dfr[4]), "=v"(dfr[5]), "=v"(dfr[6]), "=v"(dfr[7]));
              if (ffn_on) {
#pragma unroll
                for (int i = 0; i < 8; ++i) dfr[i] = trread(smem, L_DN + w * 4096 + g * 1024 + ((l & 15) >> 2) * 256 + ((i ^ ((l >> 2) & 7)) * 32) + (l & 3) * 8);
              }
            }
            if constexpr (s == FS0) {
              if constexpr (n == 0) qc = __hip_atomic_load(qcnt, __ATOMIC_RELAXED, __HIP_MEMORY_SCOPE_WORKGROUP);
              if constexpr (n == 1) {
                count_wait(0, 1);
                kv = *(const LDS_AS unsigned*)(smem + L_SKEY + 32);
#pragma unroll
                for (int i = 0; i < 4; ++i) fx[i] = *(const LDS_AS u32x4*)(smem + L_ROW + w * 256 + i * 64 + g * 16);
              }
              if constexpr (n == 2) {
                fkey = __builtin_amdgcn_readfirstlane(kv);
                sel0 = key_row(fkey);
                aff = (int)(sel0 >> 8) == p;
                asm volatile("" : "=v"(sa), "=v"(sb));
                if (ffn_on) ffn_gu(gv, uv, fx, sa, sb);
              }
              if constexpr (n == 4) {
                if (ffn_on) {
                  ffn_gu_store(smem, l, w, sa, sb);
                  count_in(1);
                }
              }
            }
            if constexpr (s == FS0D) {
              if constexpr (n == 0) qc = __hip_atomic_load(qcnt + 1, __ATOMIC_RELAXED, __HIP_MEMORY_SCOPE_WORKGROUP);
              if constexpr (n == 1) { asm volatile("" : "=v"(sa)); if (ffn_on) { count_wait(1, 8); sa = ffn_d_read(smem, l); } }
              if constexpr (n == 2) { asm volatile("" : "=v"(hh)); if (ffn_on) hh = ffn_d_h(sa); }
              if constexpr (n == 4) { asm volatile("" : "=v"(sa)); if (ffn_on) sa = ffn_d_all(dfr, hh); }
              if constexpr (n == 5) { if (ffn_on) ffn_d_store(rs, l, w, 0, slice, sa); }
            }
            if constexpr (s == FS1 && n == 4) {
              DEF4(fx);
              asm volatile("" : "=v"(cxn), "=v"(cx1));
              if (!ffn_on) {
                const unsigned G = xcd * 4 + bi - 28;
#pragma unroll
                for (int k = 0; k < 4; ++k) ld16h<0>(fx[k], rs, RED_VO(k), RED_SO(0, G));
                ld8h(cxn, rs, RED_JJ(), cand_off(fkey, 0) + XO(G)); ld8h(cx1, rs, RED_JJ() + 128, cand_off(fkey, 0) + XO(G));
              }
            }
            if constexpr (s == FS1S && n == 2) {
              if (!ffn_on) {
                wait_vm<SINCE(FS1)>();
                PINV4(fx);
                asm volatile("" : "+v"(cxn), "+v"(cx1)::"memory");
                red_sum(rs, smem, T, tid, w, 0, xcd * 4 + bi - 28, fkey, fx, cxn, cx1);
                BAR();
                if (w == 0) {
                  const u32x2 ov = red_final(smem, l, red_gate(fkey), cxn, cx1);
                  if (l < 8) st8(unpoison2(ov), rs, OFF_XNEW + (xcd * 4 + bi - 28) * 256 + ((l & 3) * 2 + (l >> 2)) * 8, 0);
                }
              }
            }
            if constexpr (s == FXA && n == 4) { asm volatile("" : "=v"(fx[0])); if (w == 1) ld16h<0>(fx[0], rs, ((l >> 2) & 3) * 256 + (l & 3) * 16, XP_OFF()); }
            if constexpr (s == FXB - 1 && n == FSN) {
              if (w == 1) {
                wait_vm<SINCE(FXA)>();
                asm volatile("" : "+v"(fx[0])::"memory");
                word_wait<1>(rs, T, ((l >> 2) & 3) * 256 + (l & 3) * 16, XP_OFF(), fx);
                if (l < 16) *(LDS_AS u32x4*)(smem + L_ROW + l * 16) = fx[0];
                count_in(0);
              }
            }
            if constexpr (s == FXB) {
#define FX_FRAG(i) (*(const LDS_AS u32x4*)(size_t)(bo[0][0] + STAGE + (6 * (i) + w) * 1024))
              if constexpr (n == 0) { qc = __hip_atomic_load(qcnt, __ATOMIC_RELAXED, __HIP_MEMORY_SCOPE_WORKGROUP); }
              if constexpr (n == 1) {
                DEF4(fx); DEF4(df);
                if (w < 6) {
                  count_wait(0, 2);
#pragma unroll
                  for (int i = 0; i < 4; ++i) { fx[i] = *(const LDS_AS u32x4*)(smem + L_ROW + i * 64 + g * 16); df[i] = FX_FRAG(i); }
                }
              }
              if constexpr (n == 2) { asm volatile("" : "=v"(sa), "=v"(sb)); if (w < 6) dot_mma(fx); }
              if constexpr (n == 4) { if (w < 6) dot_store(OFF_FX); }
            }
            if constexpr (s == FXL && n == 4) {
              asm volatile("" : "=v"(fx[0]), "=v"(fx[1]));
              if (aff && w < 4) { ld16h<0>(fx[0], rs, FP_VO(), FP_OFF(0)); ld16h<0>(fx[1], rs, FP_VO(), FP_OFF(1)); }
            }
            if constexpr (s == FXS && n == 4) {
              if (aff && w < 4) {
                wait_vm<0>();
                asm volatile("" : "+v"(fx[0]), "+v"(fx[1])::"memory");
                if (!FX_OK(2)) {
                  unsigned spins = 0;
                  do {
                    if (spin_fail(spins, rs, T)) break;
                    fx[0] = ld16(rs, FP_VO(), FP_OFF(0)); fx[1] = ld16(rs, FP_VO(), FP_OFF(1));
                  } while (!FX_OK(2));
                }
                if (w == 3) {
                  f32x2 nx[8];
                  unpack8v(fx[0], nx); unpack8v(fx[1], nx + 4);
                  const f32x2 sm = ((nx[0] + nx[1]) + (nx[2] + nx[3])) + ((nx[4] + nx[5]) + (nx[6] + nx[7]));
                  const float mean = sum64(hadd(sm)) * (1.0f / 1024.0f);
                  const f32x2 m2 = {mean, mean};
                  f32x2 q = {0.f, 0.f};
#pragma unroll
                  for (int j = 0; j < 8; ++j) { const f32x2 d = nx[j] - m2; q = __builtin_elementwise_fma(d, d, q); }
                  const float rd = ln_rd(sum64(hadd(q)) * (1.0f / 1024.0f));
                  if (l == 0) *(LDS_AS f32x2*)(smem + L_FXL + 384) = f32x2{rd, -rbf1(mean)};
                } else {
                  tile_sum(fx[0], L_FXL);
                }
                count_in(3);
              }
            }
          }
        };
        static_for<32>([&](auto sc_) __attribute__((always_inline)) {
          constexpr int s = decltype(sc_)::value;
          if constexpr (s > 0) PIN_ACC();
          if constexpr (IT == 1) wait_vm<vm_at(IT, s, 0) - vm_a(IT, s)>();
          pin_w<s % WA>(af[s % WA][0], af[s % WA][1]);
          SB();
          static_for<6>([&](auto nc_) __attribute__((always_inline)) {
            constexpr int n = decltype(nc_)::value;
            if constexpr (s == 0) { acc[0][n] = f32x4{0.f, 0.f, 0.f, 0.f}; acc[1][n] = acc[0][n]; }
            constexpr bool on = s < QL || n >= 2;
            if constexpr (on) acc[0][n] = n < 4 ? mfma16(bfr[n].v, af[s % WA][0], acc[0][n]) : mfma16(af[s % WA][0], bfr[n].v, acc[0][n]);
            SB();
            if constexpr (n == 0) { QKV_RDB(s, 5); }
            else if constexpr (s + 1 < 32 && (s + 1 < QL || n > 2)) QKV_RDB(s + 1, n - 1);
            SB();
            if constexpr (on) acc[1][n] = n < 4 ? mfma16(bfr[n].v, af[s % WA][1], acc[1][n]) : mfma16(af[s % WA][1], bfr[n].v, acc[1][n]);
            SB();
            if constexpr (s + 1 < 32 && (s + 1 < QL || n >= 2)) QKV_RDA(s + 1, n);
            SB();
            gap(sc_, nc_);
            SB();
          });
        });
        PIN_ACC();
        frag_t qw[32 - QL][2];
        static_for<32 - QL>([&](auto c_) __attribute__((always_inline)) {
          constexpr int s = QL + decltype(c_)::value;
          QKV_RDA(s, 0); QKV_RDB(s, 0); QKV_RDA(s, 1); QKV_RDB(s, 1);
          qw[s - QL][0] = bfr[0]; qw[s - QL][1] = bfr[1];
        });
        SB();
        const bool own = aff && (int)((sel0 >> 5) & 7) == rt;
        const bool m0 = !(sel0 & 16), mq = (l & 15) == (int)(sel0 & 15), mv = g == (int)((sel0 & 15) >> 2);
        auto patch_qk = [&](int n) __attribute__((always_inline)) {
          f32x4 t = *(const LDS_AS f32x4*)(smem + L_FXL + n * 64 + g * 16);
          const f32x4 c = *(const LDS_AS f32x4*)(smem + L_CSL + n * 64 + g * 16);
          const float nb = *(const LDS_AS float*)(smem + L_FXL + 388);
#pragma unroll
          for (int e = 0; e < 4; ++e) { t[e] = __builtin_fmaf(c[e], nb, t[e]); asm volatile("" : "+v"(t[e])); }
          acc[0][n] = (mq && m0) ? t : acc[0][n];
          acc[1][n] = (mq && !m0) ? t : acc[1][n];
        };
        if (own) {
          while (__hip_atomic_load(qcnt + 3, __ATOMIC_RELAXED, __HIP_MEMORY_SCOPE_WORKGROUP) < 4u) {}
          asm volatile("" ::: "memory");
          patch_qk(2); patch_qk(3);
          const f32x2 nab = *(const LDS_AS f32x2*)(smem + L_FXL + 384);
          const LDS_AS float* fl_ = (const LDS_AS float*)(smem + L_FXL + 256) + (l & 15);
          const LDS_AS float* cl_ = (const LDS_AS float*)(smem + L_CSL + 256) + (l & 15);
          const float t4 = __builtin_fmaf(cl_[0], nab[1], fl_[0]), t5 = __builtin_fmaf(cl_[16], nab[1], fl_[16]);
          const unsigned na = __builtin_bit_cast(unsigned, (float)nab[0]);
#pragma unroll
          for (int a = 0; a < 2; ++a) {
            const bool ma = a ? !m0 : m0;
            aq[a] = (mq && ma) ? na : aq[a];
#pragma unroll
            for (int e = 0; e < 4; ++e) {
              const bool me = mv && ma && e == (int)(sel0 & 3);
              acc[a][4][e] = me ? t4 : acc[a][4][e]; acc[a][5][e] = me ? t5 : acc[a][5][e];
              av[a][e] = me ? na : av[a][e];
            }
          }
        }
        constexpr float C2 = 1.4426950408889634f * 0.17677669529663687f;
#pragma unroll
        for (int a = 0; a < 2; ++a) {
          f32x2 a2 = {asf(aq[a]), asf(aq[a])};
          qs2[a] = f32x2{asf(aq[a]) * C2, asf(aq[a]) * C2};
          asm volatile("" : "+v"(a2), "+v"(qs2[a]));
          acc[a][2] *= f32x4{a2[0], a2[1], a2[0], a2[1]}; acc[a][3] *= f32x4{a2[0], a2[1], a2[0], a2[1]};
          acc[a][4] *= __builtin_bit_cast(f32x4, av[a]); acc[a][5] *= __builtin_bit_cast(f32x4, av[a]);
        }
        k0 = pack8(acc[0][2], acc[0][3]); k1 = pack8(acc[1][2], acc[1][3]); v0 = pack8(acc[0][4], acc[1][4]); v1 = pack8(acc[0][5], acc[1][5]);
        const unsigned kvo = OFF_KV + IT * (8u << 20) + (p * 32 + h) * 32768 + rt * 4096;
        if (p < 7) { st16(k0, rs, l * 16, kvo); st16(k1, rs, l * 16 + 1024, kvo); st16(v0, rs, l * 16 + 2048, kvo); st16(v1, rs, l * 16 + 3072, kvo); }
        char* ownp = smem + (8 + rt) * 2048 + l * 16;
        *(u32x4*)(ownp + L_K) = k0;
        if (rt < 7) *(u32x4*)(ownp + L_K + 1024) = k1;
        *(u32x4*)(ownp + L_V) = v0; *(u32x4*)(ownp + L_V + 1024) = v1;
        SB();
        static_for<32 - QL>([&](auto c_) __attribute__((always_inline)) {
          constexpr int s = QL + decltype(c_)::value;
#pragma unroll
          for (int n = 0; n < 2; ++n) { acc[0][n] = mfma16(qw[s - QL][n].v, af[s % WA][0], acc[0][n]); acc[1][n] = mfma16(qw[s - QL][n].v, af[s % WA][1], acc[1][n]); }
        });
        if (own) { patch_qk(0); patch_qk(1); }
      };
      qkv_loop(it_c);
    }
#undef A_LOAD1
#undef QKV_RDA
#undef QKV_RDB
#undef QKV_BO
    {
      IDS();
      const GAS bf16 *pwg, *pwu;
      asm volatile("s_load_dwordx2 %0, %2, 40\n\ts_load_dwordx2 %1, %2, 48" : "=&s"(pwg), "=&s"(pwu) : "s"(ka));
      const int ql = l & 15;
      const f32x4 z4 = {0.f, 0.f, 0.f, 0.f}, ninf = {-INFINITY, -INFINITY, -INFINITY, -INFINITY};
      LDS_AS unsigned* rdy = (LDS_AS unsigned*)(smem + L_RDY);
      const unsigned rtag = T[0] + it;
      u32x4 qf[2];
      f32x4 cn[2], oa[2][2], sum[2], S[2][2];
      bool told = false;
      auto announce = [&](bool now) __attribute__((always_inline)) {
        if (!told) {
          unsigned st;
          asm volatile("s_getreg_b32 %0, hwreg(HW_REG_IB_STS)" : "=s"(st)::"memory");
          if (now || !(st & 0xC0000Fu)) {
            DRAIN();
            if (l == 0) st8(T, rs, rt * SKW, F_KV + (it * 256 + p * 32 + h) * SKV);
            told = true;
          }
        }
      };
      unsigned one2 = 0x3F803F80u;
      asm volatile("" : "+v"(one2));
      const u32x4 ones = {one2, one2, one2, one2};
      auto ex8 = [&](f32x4 a, f32x4 b) __attribute__((always_inline)) {
        return u32x4{pack2(__builtin_amdgcn_exp2f(a[0]), __builtin_amdgcn_exp2f(a[1])), pack2(__builtin_amdgcn_exp2f(a[2]), __builtin_amdgcn_exp2f(a[3])),
                     pack2(__builtin_amdgcn_exp2f(b[0]), __builtin_amdgcn_exp2f(b[1])), pack2(__builtin_amdgcn_exp2f(b[2]), __builtin_amdgcn_exp2f(b[3]))};
      };
      {
#pragma unroll
        for (int a = 0; a < 2; ++a) {
          const f32x4 c4 = {qs2[a][0], qs2[a][1], qs2[a][0], qs2[a][1]};
          qf[a] = pack8(acc[a][0] * c4, acc[a][1] * c4);
        }
        f32x4 mk;
#pragma unroll
        for (int e = 0; e < 4; ++e) mk[e] = (4 * g + e <= ql) ? 0.f : -INFINITY;
        const f32x4 d0 = mfma16(k0, qf[0], mk), d1 = mfma16(k1, qf[1], mk), d2 = mfma16(k0, qf[1], z4);
        const float m0 = -max_x32(max_x16(fmaxf(fmaxf(d0[0], d0[1]), fmaxf(d0[2], d0[3]))));
        const float m1 = -max_x32(max_x16(fmaxf(fmaxf(d1[0], d1[1]), fmaxf(d1[2], d1[3]))));
        cn[0] = f32x4{m0, m0, m0, m0}; cn[1] = f32x4{m1, m1, m1, m1};
        asm volatile("" : "+v"(cn[0]), "+v"(cn[1]));
        announce(false);
        const f32x4 e0 = d0 + cn[0];
        const u32x4 p0 = {pack2(__builtin_amdgcn_exp2f(e0[0]), __builtin_amdgcn_exp2f(e0[1])), pack2(__builtin_amdgcn_exp2f(e0[2]), __builtin_amdgcn_exp2f(e0[3])), 0u, 0u};
        const u32x4 p1 = ex8(d2 + cn[1], d1 + cn[1]);
        oa[0][0] = mfma16(v0, p0, z4); oa[0][1] = mfma16(v1, p0, z4); sum[0] = mfma16(ones, p0, z4);
        oa[1][0] = mfma16(v0, p1, z4); oa[1][1] = mfma16(v1, p1, z4); sum[1] = mfma16(ones, p1, z4);
      }
      const char* kb_ = smem + L_K + l * 16;
      const char* vb_ = smem + L_V + l * 16;
      const unsigned fnv = (l >> 1) * (F_ * 2) + (l & 1) * 16;
      asm volatile("" : "+v"(oa[0][0]), "+v"(oa[0][1]), "+v"(oa[1][0]), "+v"(oa[1][1]), "+v"(sum[0]), "+v"(sum[1]));
      const unsigned pw = __builtin_amdgcn_readfirstlane(it * 256 + p * 32 + h - 32), pfo = F_KV + pw * SKV + rt * SKW;
      const bool fetcher = rt < 4 && p > 0;
      announce(false);
      BAR();
      asm volatile("" : "+s"(pwg), "+s"(pwu));
#define KV_FETCH(gd)                                                                                                          \
  {                                                                                                                           \
    const unsigned pvo = OFF_KV + pw * 32768 + (gd) * 4096;                                                                   \
    dma16s<0>(rs, l * 16, pvo, lbase + L_K + (gd) * 2048); dma16s<1024>(rs, l * 16, pvo, lbase + L_K + (gd) * 2048);           \
    dma16s<0>(rs, l * 16, pvo + 2048, lbase + L_V + (gd) * 2048); dma16s<1024>(rs, l * 16, pvo + 2048, lbase + L_V + (gd) * 2048); \
  }
      auto fetch = [&]() __attribute__((always_inline)) {
        if (PSK) __builtin_amdgcn_s_sleep(PSK);
        poll_flags(rs, (l & 1) * 4 * SKW, pfo, T);
        KV_FETCH(rt); KV_FETCH(rt + 4);
        if (l == 0) rdy[8 + rt] = rtag;
      };
      announce(false);
      const bool first = fetcher && told;
      if (first) fetch();
#define LDS16(a_) (*(const LDS_AS u32x4*)(size_t)(a_))
      u32x4 kf[2], vf[2], p0, p1;
#define AE(a, i) { S[a][(i) >> 2][(i) & 3] = __builtin_amdgcn_exp2f(S[a][(i) >> 2][(i) & 3]); asm volatile("" : "+v"(S[a][(i) >> 2][(i) & 3])); SB(); }
#define AC(a, P, j) { P[j] = pack2(S[a][(j) >> 1][2 * ((j) & 1)], S[a][(j) >> 1][2 * ((j) & 1) + 1]); asm volatile("" : "+v"(P[j])); SB(); }
#define PHASE(a, P, M1, M2, L2, M3, M4, L4, M5)                                                                               \
  AE(a, 0) AE(a, 1) M1; SB(); AE(a, 2) AE(a, 3) M2; SB(); L2; SB(); AC(a, P, 0) AC(a, P, 1) AE(a, 4) M3; SB(); AE(a, 5) AE(a, 6) M4; SB(); \
  L4; SB(); AE(a, 7) AC(a, P, 2) M5; SB(); AC(a, P, 3)
#define PV(a, d, P) oa[a][d] = mfma16(vf[d], P, oa[a][d])
#define RS(a, P) sum[a] = mfma16(ones, P, sum[a])
#define SC(a, t, c) S[a][t] = mfma16(kf[t], qf[a], c)
#define K_AT(ad) (kf[0] = LDS16(ad), kf[1] = LDS16((ad) + 1024))
#define V_AT(ad) (vf[0] = LDS16((ad) + L_V), vf[1] = LDS16((ad) + L_V + 1024))
#define A_FIRST(ad, c00, c01, c10, c11, KN) { K_AT(ad); V_AT(ad); SB(); SC(0, 0, c00); SC(0, 1, c01); SB(); PHASE(0, p0, SC(1, 0, c10), SC(1, 1, c11), KN, , , , ) }
#define A_NEXT(ad, c00, c01, c10, c11, KN)                                                                                        \
  { PHASE(1, p1, PV(0, 0, p0), PV(0, 1, p0), , SC(0, 0, c00), SC(0, 1, c01), , RS(0, p0))                                     \
    PHASE(0, p0, PV(1, 0, p1), PV(1, 1, p1), V_AT(ad), SC(1, 0, c10), SC(1, 1, c11), KN, RS(1, p1)) }
#define A_LAST() { PHASE(1, p1, PV(0, 0, p0), PV(0, 1, p0), , RS(0, p0), , , ) SB(); PV(1, 0, p1); PV(1, 1, p1); RS(1, p1); }
      int nw = 0;
      auto piece = [&]() __attribute__((always_inline)) {
        if constexpr (it == 0) { if (ffn_on && nw < 8) { FFN_DMA(nw); } ++nw; }
      };
#define XN_OFF() (F_XN + (((rt - 6) * 4 + g) * RX + bi % RX) * 256 + (l & 15) * 16)
      u32x4 fxn;
      asm volatile("" : "=v"(fxn));
      unsigned ad = lbase + l * 16 + (6 + rt) * 2048;
      asm volatile("" : "+v"(ad));
      if (rt > 0) {
        A_FIRST(ad + 2048, cn[0], cn[0], cn[1], cn[1], K_AT(ad));
#pragma unroll 1
        for (int i = rt - 1; i > 0; --i) {
          ad -= 2048;
          asm volatile("" : "+v"(ad));
          if (told && rt >= 4) piece();
          announce(false);
          A_NEXT(ad + 2048, cn[0], cn[0], cn[1], cn[1], K_AT(ad));
        }
        announce(false);
        A_LAST();
      }
      announce(true);
      {
        f32x4 mk;
#pragma unroll
        for (int e = 0; e < 4; ++e) mk[e] = (4 * g + e > ql) ? 0.f : -INFINITY;
        f32x4 cm0 = cn[0] + mk, cm1 = cn[1] + mk, cmi = ninf;
        asm volatile("" : "+v"(cm0), "+v"(cm1), "+v"(cmi));
        if (rt >= 4) {
          if (p > 0) {
            while (!__all(__hip_atomic_load(rdy + 8 + (l & 3), __ATOMIC_RELAXED, __HIP_MEMORY_SCOPE_WORKGROUP) == rtag)) __builtin_amdgcn_s_sleep(1);
            asm volatile("" ::: "memory");
          }
          if constexpr (it == 0) { for (; nw < 8;) piece(); }
          static_for<it ? 12 : 8>([&](auto c_) __attribute__((always_inline)) { constexpr int i = decltype(c_)::value; WO_PIECE_W(15 - i / 2, 2 * (rt & 3) + (i & 1)); });
          if (it == 0 && rt >= 6) ld16h<0>(fxn, rs, XN_OFF(), 0);
        } else if (p > 0) {
          if (!first) fetch();
          DRAIN();
          if (l < 2) rdy[rt + 4 * l] = rtag;
          if constexpr (it == 0) { for (; nw <= rt;) piece(); }
        }
        if (p > 0) {
          unsigned have = 0;
          auto need = [&](int gq) __attribute__((always_inline)) {
            if (__builtin_expect(gq < 8 && !((have >> gq) & 1), 0)) {
              do {
                const unsigned t = __hip_atomic_load(rdy + (l & 7), __ATOMIC_RELAXED, __HIP_MEMORY_SCOPE_WORKGROUP);
                have = (unsigned)__builtin_amdgcn_ballot_w64(t == rtag);
              } while (!((have >> gq) & 1));
              asm volatile("" ::: "memory");
            }
          };
          ad = lbase + l * 16 + rt * 2048;
          asm volatile("" : "+v"(ad));
          need(rt);
          K_AT(ad); V_AT(ad); SB(); SC(0, 0, cm0); SC(0, 1, cn[0]); SB();
          PHASE(0, p0, SC(1, 0, cmi), SC(1, 1, cm1), , , , , )
          need(rt + 1);
          K_AT(ad + 2048);
#pragma unroll 1
          for (int gq = rt + 1; gq < 8; ++gq) {
            ad += 2048;
            asm volatile("" : "+v"(ad));
            piece();
            need(gq + 1);
            A_NEXT(ad, cn[0], cn[0], cn[1], cn[1], K_AT(ad + 2048));
          }
          A_LAST();
        }
        if constexpr (it == 0) { for (; nw < 8;) piece(); }
      }
      auto o_store = [&]() __attribute__((always_inline)) {
        const unsigned oo = OFF_O + it * (4u << 20) + ((p * 8 + rt) * 64 + h * 2) * 1024;
#pragma unroll
        for (int a = 0; a < 2; ++a) {
          f32x2 inv = {__builtin_amdgcn_rcpf(sum[a][0]), 0.f};
          inv[1] = inv[0];
          asm volatile("" : "+v"(inv));
          const f32x4 i4 = {inv[0], inv[1], inv[0], inv[1]};
          st16(pack8(oa[a][0] * i4, oa[a][1] * i4), rs, l * 16 + a * 1024, oo);
        }
      };
      {
        float big = 0.f;
#pragma unroll
        for (int a = 0; a < 2; ++a)
#pragma unroll
          for (int d = 0; d < 2; ++d)
            big = __builtin_elementwise_maximum(__builtin_elementwise_maximum(big, __builtin_elementwise_maximum(fabsf(oa[a][d][0]), fabsf(oa[a][d][1]))),
                                                __builtin_elementwise_maximum(fabsf(oa[a][d][2]), fabsf(oa[a][d][3])));
        bool ok = sum[0][0] >= 0.5f && sum[0][0] < 0x1p64f && sum[1][0] >= 0.5f && sum[1][0] < 0x1p64f && big < INFINITY;
        if (!__all(ok)) {
#pragma unroll
          for (int a = 0; a < 2; ++a) {
            const int qs = 2 * rt + a, qq = 256 + 16 * qs + ql, klo = max(qq - 255, p > 0 ? 0 : 256);
            auto score = [&](int t) __attribute__((always_inline)) {
              f32x4 s = mfma16(t == 31 ? k1 : *(const u32x4*)(kb_ + t * 1024), qf[a], z4);
#pragma unroll
              for (int e = 0; e < 4; ++e) { const int kk = 16 * t + 4 * g + e; s[e] = (kk <= qq && kk >= klo) ? s[e] : -INFINITY; }
              return s;
            };
            float mx = -INFINITY;
#pragma unroll 1
            for (int t = p > 0 ? qs : 16; t <= qs + 16; ++t) { const f32x4 s = score(t); mx = fmaxf(fmaxf(mx, fmaxf(s[0], s[1])), fmaxf(s[2], s[3])); }
            mx = max_x32(max_x16(mx));
            oa[a][0] = z4; oa[a][1] = z4; sum[a] = z4;
#pragma unroll 1
            for (int G = p > 0 ? qs >> 1 : 8; G <= (qs + 16) >> 1; ++G) {
              const f32x4 s0 = score(2 * G), s1 = score(2 * G + 1);
              f32x4 e0, e1;
#pragma unroll
              for (int e = 0; e < 4; ++e) { e0[e] = __builtin_amdgcn_exp2f(s0[e] - mx); e1[e] = __builtin_amdgcn_exp2f(s1[e] - mx); }
              const u32x4 pf = pack8(e0, e1);
              oa[a][0] = mfma16(*(const u32x4*)(vb_ + G * 2048), pf, oa[a][0]);
              oa[a][1] = mfma16(*(const u32x4*)(vb_ + G * 2048 + 1024), pf, oa[a][1]);
              sum[a] = mfma16(ones, pf, sum[a]);
            }
          }
        }
      }
      o_store();
      if constexpr (it == 0) {
        wait_vm<2>();
        asm volatile("" : "+v"(fxn)::"memory");
        if (rt >= 6 && !__all(fxn[0] == T[0] && fxn[1] == T[1] && fxn[2] == T[0] && fxn[3] == T[1])) poll_flags2(rs, XN_OFF(), T);
        DEF4(gv); DEF4(uv);
        if (ffn_on) {
          const unsigned ro = 65536 + w * 1024 + (4 * g + ((l & 15) >> 2)) * 32 + (l & 3) * 8;
#pragma unroll
          for (int i = 0; i < 4; ++i) {
            frag_t f;
            f.hf.a = trread(smem, ro + i * 16384); f.hf.b = trread(smem, ro + i * 16384 + 512);
            gv[i] = f.v;
            f.hf.a = trread(smem, ro + i * 16384 + 8192); f.hf.b = trread(smem, ro + i * 16384 + 8704);
            uv[i] = f.v;
          }
        }
        PINV4(gv); PINV4(uv);
      }
      BAR();
      if constexpr (it == 1) { WO_PIECE(3); WO_PIECE(2); WO_PIECE(1); WO_PIECE(0); }
      wait_vm<it ? 4 : 0>();
      asm volatile("s_barrier" ::: "memory");
      if (tid == 0) st8(T, rs, h * SO, F_O + (it * 8 + p) * 32 * SO);
      if constexpr (it == 0) {
        WO_PIECE(11); WO_PIECE(10); WO_PIECE(9); WO_PIECE(8); WO_PIECE(7); WO_PIECE(6); WO_PIECE(5); WO_PIECE(4);
        WO_PIECE(3); WO_PIECE(2); WO_PIECE(1); WO_PIECE(0);
      }
    }
    IDS();
    ARGP(pwr, 4); ARGP(pwd, 7); ARGP(pout, 8); ARGP(px, 0);
    const int kh = w >> 2, rt2 = rb * 4 + (w & 3);
    const unsigned R = rt2 * 32 + kh * 16 + (l & 15);
    const unsigned to = (rt2 * 64 + ct * 4 + kh) * 1024 + l * 16;
    const unsigned rmo = ct * 64 + ((g & 1) ? 12 + 4 * g : 4 * g);
    f32x2 wv[8];
    {
      u32x4 wa[2], wrp[2], res[2];
#pragma unroll
      for (int j = 0; j < 2; ++j) {
        wrp[j] = *(const GAS u32x4*)(pwr + rmo + 32 * j);
        if constexpr (it == 0) wa[j] = *(const GAS u32x4*)(pwr + l * 8 + j * 512);
        if constexpr (it == 0) res[j] = *(const GAS u32x4*)(px + (R - (l & 15) + 8 * j + (l >> 3)) * 1024 + ct * 64 + (l & 7) * 8);
        else res[j] = ld16(rs, OFF_XNEW + (2 * ct + j) * 256 + g * 16, 0);
      }
      SB();
      if ((it ? PSO : PSO0) && w == 0) __builtin_amdgcn_s_sleep(it ? PSO : PSO0);
      if (w == 0) poll_flags(rs, (l & 31) * SO, F_O + (it * 8 + p) * 32 * SO, T);
      asm volatile("s_barrier" ::: "memory");
      auto prep = [&]() __attribute__((always_inline)) {
        if constexpr (it == 0) {
          f32x2 t[8];
          unpack8v(wa[0], t); unpack8v(wa[1], t + 4);
          const f32x2 s = ((t[0] + t[1]) + (t[2] + t[3])) + ((t[4] + t[5]) + (t[6] + t[7]));
          wsum = sum64(hadd(s));
        }
        unpack8v(trade16(wrp[0]), wv); unpack8v(trade16(wrp[1]), wv + 4);
        if constexpr (it == 0) {
          char* tp = smem + w * 2048;
          const unsigned c0 = (g & 1) * 2 + (g >> 1), r = l & 15;
#pragma unroll
          for (int j = 0; j < 2; ++j) *(u32x4*)(tp + j * 1024 + (l >> 3) * 128 + (((l ^ (l >> 3)) & 7) << 4)) = res[j];
          res[0] = trade16(*(const u32x4*)(tp + r * 128 + ((c0 ^ (r & 7)) << 4)));
          res[1] = trade16(*(const u32x4*)(tp + r * 128 + (((c0 + 4) ^ (r & 7)) << 4)));
        }
        else { res[0] = R == sel0 ? res[0] : x2p[0]; res[1] = R == sel0 ? res[1] : x2p[1]; }
        unpack8v(res[0], v + 0); unpack8v(res[1], v + 4);
        asm volatile("" : "+v"(v[0]), "+v"(v[1]), "+v"(v[2]), "+v"(v[3]), "+v"(v[4]), "+v"(v[5]), "+v"(v[6]), "+v"(v[7]));
        asm volatile("" : "+v"(wsum), "+v"(wv[0]), "+v"(wv[1]), "+v"(wv[2]), "+v"(wv[3]), "+v"(wv[4]), "+v"(wv[5]), "+v"(wv[6]), "+v"(wv[7]));
      };
      {
        const unsigned abase = OFF_O + it * (4u << 20) + (rt2 * 64 + kh) * 1024;
        u32x4 af[W2B], bfr[W2L + 1][4];
        unsigned b2[2] = {lbase + 32768 + l * 16, lbase + 98304 + l * 16};
        asm volatile("" : "+v"(b2[0]), "+v"(b2[1]));
#define O_LOAD(s_) af[(s_) % W2B] = ld16(rs, l * 16 + (~(s_) & 1) * 2048, abase + ((31 - (s_)) >> 1) * 4096)
#define WO_RD(s_, n) bfr[(s_) % (W2L + 1)][n] = *(const LDS_AS u32x4*)(size_t)(b2[(31 - (s_)) >> 4] + ((31 - (s_)) & 15) * 4096 + (n) * 1024)
#pragma unroll
        for (int s = 0; s < W2R; ++s) O_LOAD(s);
        SB();
        prep();
        SB();
        f32x4 ac[4];
#pragma unroll
        for (int n = 0; n < 4; ++n) ac[n] = f32x4{v[2 * n][0], v[2 * n][1], v[2 * n + 1][0], v[2 * n + 1][1]};
#pragma unroll
        for (int n = 0; n < 4 * W2L; ++n) WO_RD(n >> 2, n & 3);
#pragma unroll
        for (int s = 0; s < 32; ++s) {
          if (s == W2P && w >= 4) __builtin_amdgcn_s_setprio(1);
          if (s == (it ? 24 : 8) - W2L) asm volatile("s_barrier" ::: "memory");
        if (it == 0 && s == WPS) {
          unsigned pz1 = POISON;
          asm volatile("" : "+v"(pz1));
          const u32x4 pz = {pz1, pz1, pz1, pz1};
          st16(pz, rs, OFF_PART + (tid >> 8) * (1u << 20) + bid * 4096 + (tid & 255) * 16, 0);
          if (w < 2) st16(pz, rs, OFF_STAT + 524288 + (rb * 16 + ct) * 2048 + tid * 16, 0);
          if (w == 4 && l < 40) st16(pz, rs, l < 16 ? OFF_CA + 65536 + rb * 4096 + ct * 256 + l * 16 : OFF_FX + (h * 8 + p) * 512 + (l - 16) * 16, 0);
          if (w == 6 && l < (ct < (int)RKEY ? 3 : 2)) st4(pz1, rs, l < 2 ? OFF_XNEW + (bid >> 3) * 256 + (bid & 7) * 8 + l * 4 : KEY_AT(1, ct) + rb * SKEY, 0);
        }
          if (s < W2B - W2R) O_LOAD(W2R + 2 * s);
          SB();
#pragma unroll
          for (int n = 0; n < 4; ++n) {
            ac[n] = mfma16(bfr[s % (W2L + 1)][n], af[s % W2B], ac[n]);
            if (s + W2L < 32) WO_RD(s + W2L, n);
            SB();
          }
          if (s < W2B - W2R) O_LOAD(W2R + 2 * s + 1);
          else if (s + W2B < 32) O_LOAD(s + W2B);
          SB();
        }
        __builtin_amdgcn_s_setprio(0);
#undef WO_RD
#undef O_LOAD
#pragma unroll
        for (int n = 0; n < 4; ++n) { v[2 * n] = f32x2{ac[n][0], ac[n][1]}; v[2 * n + 1] = f32x2{ac[n][2], ac[n][3]}; }
      }
    }
    {
      if (it == 0) { st16(pack8v(v), rs, to, OFF_XN + (4u << 20)); st16(pack8v(v + 4), rs, to + 2048, OFF_XN + (4u << 20)); }
      SB();
      const f32x2 s = ((v[0] + v[1]) + (v[2] + v[3])) + ((v[4] + v[5]) + (v[6] + v[7]));
      f32x2 dp = v[0] * wv[0], q = v[0] * v[0];
#pragma unroll
      for (int j = 1; j < 8; ++j) { dp = __builtin_elementwise_fma(v[j], wv[j], dp); q = __builtin_elementwise_fma(v[j], v[j], q); }
      float mt = hadd(s), qq = hadd(q), dot = hadd(dp);
      add_g3(mt, qq, dot);
      const unsigned so = OFF_STAT + it * 524288 + rb * 32768, lrow = R & 127;
      if (it == 0) wait_vm<2>();
      if (g == 0) st16(unpoison4(__builtin_bit_cast(u32x4, f32x4{mt, qq, dot, 0.f})), rs, ct * 2048 + lrow * 16, so);
      SB();
      if (it == 0) wait_vm<1>();
      if constexpr (it == 0) {
        volatile LDS_AS u32x2* in = (volatile LDS_AS u32x2*)(smem + L_XC);
        if (l == 0) in[w] = T;
        const u32x2 t = in[l & 7];
        if (__all(t[0] == T[0] && t[1] == T[1]) && l < (int)RX1) st8(T, rs, (l * 32 + (rb & 1) * 16 + ct) * SX1, F_X1 + (rb >> 1) * RX1 * 32 * SX1);
      }
      if (it == 0) {
        asm volatile("s_barrier" ::: "memory");
        if (ffn_on) { WD_DMA(); }
#pragma unroll
        for (int j = 0; j < 9; ++j) *(u32x4*)(smem + L_RING + (j / 3) * STAGE + (w * 3 + j % 3) * 1024 + l * 16) = keep[j];
      }
      if constexpr (it == 0) {
        if (PSX && w == 0) __builtin_amdgcn_s_sleep(PSX);
        if (w == 0) poll_flags(rs, ((l & 2) * 8 + ((ROT0 + 5) & 7) * 2 + (l & 1)) * SX1, F_X1 + (p * RX1 + (xcd >> 1) % RX1) * 32 * SX1, T);
        if (tid == 192) *(LDS_AS u32x4*)(smem + L_QC) = u32x4{0u, 0u, 0u, 0u};
        BAR();
        PRE_LOAD(1, ROT0 + 5, 0, NPRE);
      } else {
        __builtin_amdgcn_s_sleep(SDIR);
        u32x4 sp[4];
#pragma unroll
        for (int i = 0; i < 4; ++i) sp[i] = ld16(rs, (4 * g + i) * 2048 + lrow * 16, so);
        const RowFin r = fin_key(rs, T, g * 8192 + lrow * 16, so, smem + L_XC, l, w, R, wsum, sp);
        BAR();
        fin_cand(rs, smem + L_XC, 1, g, rb, ct, r, v);
        fin_x2(r, v);
      }
    }
    if (it == 1) {
      u32x4 op[2];
      {
        char* tp = smem + L_OUT + w * 2048;
        const unsigned c0 = (g & 1) * 2 + (g >> 1), r = l & 15;
        *(u32x4*)(tp + r * 128 + ((c0 ^ (r & 7)) << 4)) = trade16(pack8v(v));
        *(u32x4*)(tp + r * 128 + (((c0 + 4) ^ (r & 7)) << 4)) = trade16(pack8v(v + 4));
#pragma unroll
        for (int j = 0; j < 2; ++j) op[j] = *(const u32x4*)(tp + j * 1024 + (l >> 3) * 128 + (((l ^ (l >> 3)) & 7) << 4));
      }
      const unsigned Ro = R - (l & 15) + (l >> 3), oo = (Ro * 1024 + ct * 64) * 2 + (l & 7) * 16;
#define OUT_STORE(c0, c1)                                                                           \
  {                                                                                                 \
    if (c0) __builtin_amdgcn_raw_buffer_store_b128(op[0], rout, oo, 0, SC1);                        \
    if (c1) __builtin_amdgcn_raw_buffer_store_b128(op[1], rout, oo + 16384, 0, SC1);                \
  }
      if (w == 0) {
        __builtin_amdgcn_s_sleep(GSL);
        u32x4 xr[4];
        unsigned cur = 0, best;
        asm volatile("" : "=v"(xr[0]), "=v"(xr[1]));
        for (unsigned spins = 0;;) {
          const unsigned k_ = key_load(rs, l, 1, KREP());
          const bool all = __all(k_ != POISON);
          best = __builtin_amdgcn_readfirstlane(umax16(k_ == POISON ? 0u : k_));
          if (best != cur && ffn_on) {
            cur = best;
            ld16h<0>(xr[0], rs, (l >> 3) * 256 + (l & 7) * 16, cand_off(cur, 1)); ld16h<2048>(xr[1], rs, (l >> 3) * 256 + (l & 7) * 16, cand_off(cur, 1));
          }
          if (all || spin_fail(spins, rs, T)) break;
        }
        if (l == 0) *(LDS_AS unsigned*)(smem + L_SKEY + 32) = best;
        asm volatile("s_waitcnt vmcnt(0)" : "+v"(xr[0]), "+v"(xr[1])::"memory");
        if (ffn_on) {
          word_wait<2>(rs, T, (l >> 3) * 256 + (l & 7) * 16, cand_off(best, 1), xr);
          *(LDS_AS u32x4*)(smem + L_ROW + l * 16) = xr[0]; *(LDS_AS u32x4*)(smem + L_ROW + 1024 + l * 16) = xr[1];
        }
      }
      BAR();
      const unsigned key = __builtin_amdgcn_readfirstlane(*(LDS_AS unsigned*)(smem + L_SKEY + 32)), krow = key_row(key);
      if (ffn_on) {
        u32x4 xv[4];
#pragma unroll
        for (int i = 0; i < 4; ++i) xv[i] = *(const LDS_AS u32x4*)(smem + L_ROW + w * 256 + i * 64 + g * 16);
        f32x4 ga, ua;
        ffn_gu(gv, uv, xv, ga, ua);
        ffn_gu_store(smem, l, w, ga, ua);
        BAR();
        ffn_d_store(rs, l, w, 1, slice, ffn_d_all(dfr, ffn_d_h(ffn_d_read(smem, l))));
        SB();
      }
      OUT_STORE(Ro != krow, Ro + 8 != krow);
      if (!ffn_on) {
        const unsigned G = xcd * 4 + bi - 28;
        if (G == 0 && tid == 0) st8(T, rs, F_EP, 0);
        u32x4 pv[4];
        u32x2 cxn, cx1;
        const float sig = red_gate(key);
        __builtin_amdgcn_s_sleep(RSL);
        red_load(rs, tid, 1, G, key, pv, cxn, cx1);
        red_sum(rs, smem, T, tid, w, 1, G, key, pv, cxn, cx1);
        BAR();
        if (w == 0) {
          const u32x2 ov = red_final(smem, l, sig, cxn, cx1);
          if (l < 8) __builtin_amdgcn_raw_buffer_store_b64(ov, rout, (krow * 1024 + G * 32 + l * 4) * 2, 0, 0);
        }
      }
#undef OUT_STORE
    }
  };
  layer(std::integral_constant<int, 0>{});
  layer(std::integral_constant<int, 1>{});
}

extern "C" void kernel_launch(void* const* d_in, const int* in_sizes, int n_in, void* d_out, int out_size,
                              void* d_ws, size_t ws_size, hipStream_t stream) {
  (void)hipFuncSetAttribute((const void*)mega_kernel, hipFuncAttributeMaxDynamicSharedMemorySize, LDS_BYTES);
  mega_kernel<<<256, 512, LDS_BYTES, stream>>>((const bf16*)d_in[0], (char*)d_ws, (const bf16*)d_in[1], (const bf16*)d_in[2], (const bf16*)d_in[4],
                                               (const bf16*)d_in[5], (const bf16*)d_in[6], (const bf16*)d_in[7], (bf16*)d_out);
}
```

```cpp
#include <hip/hip_runtime.h>
#include <stdint.h>
#include <utility>

typedef __bf16 bf16;
typedef __bf16 bf16x8 __attribute__((ext_vector_type(8)));
typedef __bf16 bf16x2 __attribute__((ext_vector_type(2)));
typedef float f32x4 __attribute__((ext_vector_type(4)));
typedef float f32x2 __attribute__((ext_vector_type(2)));
typedef short s16x4 __attribute__((ext_vector_type(4)));
typedef unsigned int u32x4 __attribute__((ext_vector_type(4)));
typedef unsigned int u32x2 __attribute__((ext_vector_type(2)));
#define LDS_AS __attribute__((address_space(3)))
#define GAS __attribute__((address_space(1)))
typedef __amdgpu_buffer_rsrc_t rsrc_t;

constexpr int F_ = 3584;
constexpr int SC1 = 16;
#define PAUX 16

constexpr int RX = 4;
constexpr unsigned F_EP = 0, F_TMO = 128;
constexpr unsigned F_XN = 4096;
#define FLG 256, 1024, 8, 64, 4, 256, 4
constexpr unsigned FLV[7] = {FLG};
constexpr unsigned SO = FLV[0], SKV = FLV[1], SKW = FLV[2], SX1 = FLV[3], RX1 = FLV[4], SKEY = FLV[5], RKEY = FLV[6];
constexpr unsigned F_O = 8u << 20;
constexpr unsigned F_KV = 11u << 20;
constexpr unsigned F_IM = F_O + 524288;
constexpr unsigned F_WOI = F_O + 262144;
constexpr unsigned F_X1 = 9u << 20;
constexpr unsigned OFF_KEY = 10u << 20;
#define KREP() (RKEY == 16 ? bid & 15 : (bid & 7) * RKEY / 8)
#define KEY_AT(L, rep) (OFF_KEY + ((L) * RKEY + (rep)) * (SKEY < 8 ? 128 : 16 * SKEY))
constexpr unsigned OFF_AB = 2u << 20;
constexpr unsigned OFF_XNEW = 262144;
constexpr unsigned OFF_CS = OFF_AB + 65536;
constexpr unsigned OFF_FX = 393216;
constexpr unsigned OFF_CA = 524288;
constexpr unsigned OFF_STAT = 1u << 20;
constexpr unsigned OFF_PART = 3u << 20;
constexpr unsigned OFF_XN = 8u << 20;
constexpr unsigned OFF_O = 16u << 20;
constexpr unsigned OFF_KV = 32u << 20;
constexpr unsigned OFF_WI = 52u << 20;
constexpr unsigned OFF_WOI = 58u << 20;
constexpr unsigned WS_BYTES = 60u << 20;
constexpr unsigned POISON = 0xFFFFFFFFu;

constexpr int L_WOS = 20480, L_ABW = 16640, L_CSP = 17408, L_CSL = 31872, L_FXL = 32256, L_K = 0, L_V = 32768, L_RING = 65536, L_DN = 32768, L_RDY = 31744, L_QC = 31808, LDS_BYTES = 163840;
constexpr int STAGE = 24576;
#define SLOT(t) ((t) & 3)

template <typename F, int... I> __device__ __forceinline__ void static_for_impl(F&& f, std::integer_sequence<int, I...>) {
  (f(std::integral_constant<int, I>{}), ...);
}
template <int N, typename F> __device__ __forceinline__ void static_for(F&& f) { static_for_impl(f, std::make_integer_sequence<int, N>{}); }
__device__ __forceinline__ unsigned pack2(float a, float b) {
  f32x2 v = {a, b};
  return __builtin_bit_cast(unsigned, __builtin_convertvector(v, bf16x2));
}
__device__ __forceinline__ float asf(unsigned u) { return __builtin_bit_cast(float, u); }
__device__ __forceinline__ float lo16(unsigned u) { return __builtin_bit_cast(float, u << 16); }
__device__ __forceinline__ float hi16(unsigned u) { return __builtin_bit_cast(float, u & 0xffff0000u); }
__device__ __forceinline__ u32x4 pack8(const f32x4 a, const f32x4 b) {
  u32x4 o;
  o[0] = pack2(a[0], a[1]); o[1] = pack2(a[2], a[3]); o[2] = pack2(b[0], b[1]); o[3] = pack2(b[2], b[3]);
  return o;
}
__device__ __forceinline__ f32x4 mfma16(u32x4 a, u32x4 b, f32x4 c) {
  return __builtin_amdgcn_mfma_f32_16x16x32_bf16(__builtin_bit_cast(bf16x8, a), __builtin_bit_cast(bf16x8, b), c, 0, 0, 0);
}
__device__ __forceinline__ f32x4 mfma16k16(s16x4 a, s16x4 b, f32x4 c) { return __builtin_amdgcn_mfma_f32_16x16x16bf16_1k(a, b, c, 0, 0, 0); }
template <int CTRL> __device__ __forceinline__ float dppf(float v) {
  return __builtin_bit_cast(float, __builtin_amdgcn_update_dpp(0, __builtin_bit_cast(int, v), CTRL, 0xf, 0xf, false));
}
template <int CTRL> __device__ __forceinline__ unsigned dppu(unsigned v) {
  return (unsigned)__builtin_amdgcn_update_dpp(0, (int)v, CTRL, 0xf, 0xf, false);
}
__device__ __forceinline__ float add_x16(float v) {
  auto r = __builtin_amdgcn_permlane16_swap(__builtin_bit_cast(unsigned, v), __builtin_bit_cast(unsigned, v), false, false);
  return __builtin_bit_cast(float, (unsigned)r[0]) + __builtin_bit_cast(float, (unsigned)r[1]);
}
__device__ __forceinline__ float add_x32(float v) {
  auto r = __builtin_amdgcn_permlane32_swap(__builtin_bit_cast(unsigned, v), __builtin_bit_cast(unsigned, v), false, false);
  return __builtin_bit_cast(float, (unsigned)r[0]) + __builtin_bit_cast(float, (unsigned)r[1]);
}
__device__ __forceinline__ float max_x16(float v) {
  auto r = __builtin_amdgcn_permlane16_swap(__builtin_bit_cast(unsigned, v), __builtin_bit_cast(unsigned, v), false, false);
  return fmaxf(__builtin_bit_cast(float, (unsigned)r[0]), __builtin_bit_cast(float, (unsigned)r[1]));
}
__device__ __forceinline__ float max_x32(float v) {
  auto r = __builtin_amdgcn_permlane32_swap(__builtin_bit_cast(unsigned, v), __builtin_bit_cast(unsigned, v), false, false);
  return fmaxf(__builtin_bit_cast(float, (unsigned)r[0]), __builtin_bit_cast(float, (unsigned)r[1]));
}
__device__ __forceinline__ float sum_hi8(float v) {
  v += dppf<0x128>(v);
  v = add_x16(v);
  return add_x32(v);
}
__device__ __forceinline__ float sum64(float v) {
  v += dppf<0xB1>(v);
  v += dppf<0x4E>(v);
  v += dppf<0x124>(v);
  v += dppf<0x128>(v);
  v = add_x16(v);
  return add_x32(v);
}

#define BAR() asm volatile("s_waitcnt lgkmcnt(0)\n\ts_barrier" ::: "memory")
#define DRAIN() asm volatile("s_waitcnt vmcnt(0)" ::: "memory")
#define SB() __builtin_amdgcn_sched_barrier(0)
template <int N> __device__ __forceinline__ void wait_vm() { asm volatile("s_waitcnt vmcnt(%0)" ::"n"(N) : "memory"); }

__device__ __forceinline__ void dma16(rsrc_t rs, unsigned voff, unsigned soff, unsigned lds) {
  asm volatile("s_mov_b32 m0, %0\n\ts_nop 0\n\tbuffer_load_dwordx4 %1, %2, %3 offen lds" ::"s"(lds), "v"(voff), "s"(rs), "s"(soff) : "memory");
}
__device__ __forceinline__ void dma16n(rsrc_t rs, unsigned voff, unsigned soff, unsigned lds) {
  asm volatile("s_mov_b32 m0, %0\n\ts_nop 0\n\tbuffer_load_dwordx4 %1, %2, %3 offen nt lds" ::"s"(lds), "v"(voff), "s"(rs), "s"(soff) : "memory");
}
template <int OFF> __device__ __forceinline__ void dma16s(rsrc_t rs, unsigned voff, unsigned soff, unsigned lds) {
  asm volatile("s_mov_b32 m0, %0\n\ts_nop 0\n\tbuffer_load_dwordx4 %1, %2, %3 offen offset:%4 sc1 lds" ::"s"(lds), "v"(voff), "s"(rs), "s"(soff), "n"(OFF) : "memory");
}
__device__ __forceinline__ float rbf1(float x) { return __builtin_bit_cast(float, pack2(0.f, x)); }
__device__ __forceinline__ u32x4 ld16(rsrc_t rs, unsigned voff, unsigned soff) { return __builtin_amdgcn_raw_buffer_load_b128(rs, voff, soff, SC1); }
template <int OFF> __device__ __forceinline__ void ld16h(u32x4& d, rsrc_t rs, unsigned voff, unsigned soff) {
  asm volatile("buffer_load_dwordx4 %0, %1, %2, %3 offen offset:%4 sc1" : "=v"(d) : "v"(voff), "s"(rs), "s"(soff), "n"(OFF) : "memory");
}
template <int I, int OFF> __device__ __forceinline__ void ld16w(u32x4& d, rsrc_t rs, unsigned voff, unsigned soff) {
  if constexpr (I == 0) asm volatile("buffer_load_dwordx4 %0, %1, %2, %3 offen offset:%4 sc1" : "={v[128:131]}"(d) : "v"(voff), "s"(rs), "s"(soff), "n"(OFF) : "memory");
  else if constexpr (I == 1) asm volatile("buffer_load_dwordx4 %0, %1, %2, %3 offen offset:%4 sc1" : "={v[132:135]}"(d) : "v"(voff), "s"(rs), "s"(soff), "n"(OFF) : "memory");
  else if constexpr (I == 2) asm volatile("buffer_load_dwordx4 %0, %1, %2, %3 offen offset:%4 sc1" : "={v[136:139]}"(d) : "v"(voff), "s"(rs), "s"(soff), "n"(OFF) : "memory");
  else if constexpr (I == 3) asm volatile("buffer_load_dwordx4 %0, %1, %2, %3 offen offset:%4 sc1" : "={v[140:143]}"(d) : "v"(voff), "s"(rs), "s"(soff), "n"(OFF) : "memory");
  else if constexpr (I == 4) asm volatile("buffer_load_dwordx4 %0, %1, %2, %3 offen offset:%4 sc1" : "={v[144:147]}"(d) : "v"(voff), "s"(rs), "s"(soff), "n"(OFF) : "memory");
  else if constexpr (I == 5) asm volatile("buffer_load_dwordx4 %0, %1, %2, %3 offen offset:%4 sc1" : "={v[148:151]}"(d) : "v"(voff), "s"(rs), "s"(soff), "n"(OFF) : "memory");
  else if constexpr (I == 6) asm volatile("buffer_load_dwordx4 %0, %1, %2, %3 offen offset:%4 sc1" : "={v[152:155]}"(d) : "v"(voff), "s"(rs), "s"(soff), "n"(OFF) : "memory");
  else if constexpr (I == 7) asm volatile("buffer_load_dwordx4 %0, %1, %2, %3 offen offset:%4 sc1" : "={v[156:159]}"(d) : "v"(voff), "s"(rs), "s"(soff), "n"(OFF) : "memory");
  else if constexpr (I == 8) asm volatile("buffer_load_dwordx4 %0, %1, %2, %3 offen offset:%4 sc1" : "={v[160:163]}"(d) : "v"(voff), "s"(rs), "s"(soff), "n"(OFF) : "memory");
  else if constexpr (I == 9) asm volatile("buffer_load_dwordx4 %0, %1, %2, %3 offen offset:%4 sc1" : "={v[164:167]}"(d) : "v"(voff), "s"(rs), "s"(soff), "n"(OFF) : "memory");
  else if constexpr (I == 10) asm volatile("buffer_load_dwordx4 %0, %1, %2, %3 offen offset:%4 sc1" : "={v[168:171]}"(d) : "v"(voff), "s"(rs), "s"(soff), "n"(OFF) : "memory");
  else if constexpr (I == 11) asm volatile("buffer_load_dwordx4 %0, %1, %2, %3 offen offset:%4 sc1" : "={v[172:175]}"(d) : "v"(voff), "s"(rs), "s"(soff), "n"(OFF) : "memory");
}
template <int K> __device__ __forceinline__ void pin_w(u32x4& a, u32x4& b) {
  if constexpr (K == 0) asm volatile("" : "+{v[128:131]}"(a), "+{v[132:135]}"(b)::"memory");
  else if constexpr (K == 1) asm volatile("" : "+{v[136:139]}"(a), "+{v[140:143]}"(b)::"memory");
  else if constexpr (K == 2) asm volatile("" : "+{v[144:147]}"(a), "+{v[148:151]}"(b)::"memory");
  else if constexpr (K == 3) asm volatile("" : "+{v[152:155]}"(a), "+{v[156:159]}"(b)::"memory");
  else if constexpr (K == 4) asm volatile("" : "+{v[160:163]}"(a), "+{v[164:167]}"(b)::"memory");
  else if constexpr (K == 5) asm volatile("" : "+{v[168:171]}"(a), "+{v[172:175]}"(b)::"memory");
}
__device__ __forceinline__ void ld8h(u32x2& d, rsrc_t rs, unsigned voff, unsigned soff) {
  asm volatile("buffer_load_dwordx2 %0, %1, %2, %3 offen sc1" : "=v"(d) : "v"(voff), "s"(rs), "s"(soff) : "memory");
}
template <int OFF> __device__ __forceinline__ void ld4h(unsigned& d, rsrc_t rs, unsigned voff, unsigned soff) {
  asm volatile("buffer_load_dword %0, %1, %2, %3 offen offset:%4 sc1" : "=v"(d) : "v"(voff), "s"(rs), "s"(soff), "n"(OFF) : "memory");
}
__device__ __forceinline__ u32x2 ld8(rsrc_t rs, unsigned voff, unsigned soff) { return __builtin_amdgcn_raw_buffer_load_b64(rs, voff, soff, SC1); }
__device__ __forceinline__ void st16(u32x4 v, rsrc_t rs, unsigned voff, unsigned soff) { __builtin_amdgcn_raw_buffer_store_b128(v, rs, voff + soff, 0, SC1); }
__device__ __forceinline__ void st8(u32x2 v, rsrc_t rs, unsigned voff, unsigned soff) { __builtin_amdgcn_raw_buffer_store_b64(v, rs, voff, soff, SC1); }

__device__ __forceinline__ unsigned ld4(rsrc_t rs, unsigned voff, unsigned soff) { return __builtin_amdgcn_raw_buffer_load_b32(rs, voff, soff, SC1); }
__device__ __forceinline__ void st4(unsigned v, rsrc_t rs, unsigned voff, unsigned soff) { __builtin_amdgcn_raw_buffer_store_b32(v, rs, voff, soff, SC1); }

constexpr unsigned kSpinCap = 1u << 16;
__device__ __forceinline__ bool spin_fail(unsigned& spins, rsrc_t rs, u32x2 T) {
  asm volatile("" ::: "memory");
  ++spins;
  if (spins > kSpinCap) { st8(T, rs, 0, F_TMO); return true; }
  if ((spins & 255u) == 0u) {
    const u32x2 v = ld8(rs, 0, F_TMO);
    if (v[0] == T[0] && v[1] == T[1]) return true;
  }
  __builtin_amdgcn_s_sleep(3);
  return false;
}
__device__ __forceinline__ void poll_flags2(rsrc_t rs, unsigned voff, u32x2 T) {
  for (unsigned spins = 0;;) {
    const u32x4 v = ld16(rs, voff, 0);
    if (__all(v[0] == T[0] && v[1] == T[1] && v[2] == T[0] && v[3] == T[1])) break;
    if (spin_fail(spins, rs, T)) break;
  }
  asm volatile("" ::: "memory");
  __builtin_amdgcn_fence(__ATOMIC_ACQUIRE, "wavefront");
}
__device__ __forceinline__ void tag_wait(rsrc_t rs, u32x2 T, unsigned voff, unsigned soff, u32x4& v) {
  if (__builtin_expect(!__all(v[2] == T[0] && v[3] == T[1]), 0)) {
    unsigned spins = 0;
    do {
      if (spin_fail(spins, rs, T)) break;
      v = ld16(rs, voff, soff);
    } while (!__all(v[2] == T[0] && v[3] == T[1]));
    asm volatile("s_waitcnt vmcnt(0)" : "+v"(v)::"memory");
  }
}
template <int N> __device__ __forceinline__ void word_wait(rsrc_t rs, u32x2 T, unsigned voff, unsigned soff, u32x4 (&v)[4]) {
#define WW_OK() __all(max(max(max(v[0][0], v[0][1]), max(v[0][2], v[0][3])), max(max(v[N - 1][0], v[N - 1][1]), max(v[N - 1][2], v[N - 1][3]))) != POISON)
  if (__builtin_expect(!WW_OK(), 0)) {
    unsigned spins = 0;
    do {
      if (spin_fail(spins, rs, T)) break;
#pragma unroll
      for (int i = 0; i < N; ++i) v[i] = ld16(rs, voff + i * 2048, soff);
    } while (!WW_OK());
    asm volatile("s_waitcnt vmcnt(0)" : "+v"(v[0]), "+v"(v[N - 1])::"memory");
  }
#undef WW_OK
}
__device__ __forceinline__ void poll_flags(rsrc_t rs, unsigned voff, unsigned soff, u32x2 T) {
  for (unsigned spins = 0;;) {
    const u32x2 v = __builtin_amdgcn_raw_buffer_load_b64(rs, voff, soff, PAUX);
    if (__all(v[0] == T[0] && v[1] == T[1])) break;
    if (spin_fail(spins, rs, T)) break;
  }
  asm volatile("" ::: "memory");
  __builtin_amdgcn_fence(__ATOMIC_ACQUIRE, "wavefront");
}

__device__ __forceinline__ void unpack8v(const u32x4 u, f32x2* v) {
#pragma unroll
  for (int j = 0; j < 4; ++j) v[j] = f32x2{lo16(u[j]), hi16(u[j])};
}
__device__ __forceinline__ u32x4 pack8v(const f32x2* v) {
  u32x4 o;
#pragma unroll
  for (int j = 0; j < 4; ++j) o[j] = pack2(v[j][0], v[j][1]);
  return o;
}
__device__ __forceinline__ float ln_rd(float var) { return __builtin_amdgcn_rcpf(rbf1(__builtin_amdgcn_sqrtf(rbf1(rbf1(var) + 1e-5f)))); }
__device__ __forceinline__ float hadd(f32x2 v) {
  float a = v[0];
  asm volatile("" : "+v"(a));
  return a + v[1];
}
__device__ __forceinline__ float add_g(float v) { return add_x32(add_x16(v)); }
__device__ __forceinline__ void add_g3(float& x, float& y, float& z) {
  auto a = __builtin_amdgcn_permlane16_swap(__builtin_bit_cast(unsigned, x), __builtin_bit_cast(unsigned, y), false, false);
  auto b = __builtin_amdgcn_permlane16_swap(__builtin_bit_cast(unsigned, z), __builtin_bit_cast(unsigned, z), false, false);
  const float r = asf(a[0]) + asf(a[1]), t = asf(b[0]) + asf(b[1]);
  auto c = __builtin_amdgcn_permlane32_swap(__builtin_bit_cast(unsigned, r), __builtin_bit_cast(unsigned, r), false, false);
  auto d = __builtin_amdgcn_permlane32_swap(__builtin_bit_cast(unsigned, t), __builtin_bit_cast(unsigned, t), false, false);
  const float u = asf(c[0]) + asf(c[1]);
  z = asf(d[0]) + asf(d[1]);
  auto e = __builtin_amdgcn_permlane16_swap(__builtin_bit_cast(unsigned, u), __builtin_bit_cast(unsigned, u), false, false);
  x = asf(e[0]); y = asf(e[1]);
}
__device__ __forceinline__ u32x4 trade16(u32x4 v) {
  auto a = __builtin_amdgcn_permlane16_swap(v[0], v[2], false, false);
  auto b = __builtin_amdgcn_permlane16_swap(v[1], v[3], false, false);
  return u32x4{(unsigned)a[0], (unsigned)b[0], (unsigned)a[1], (unsigned)b[1]};
}
__device__ __forceinline__ unsigned umax4(u32x4 v) { return max(max(v[0], v[1]), max(v[2], v[3])); }
__device__ __forceinline__ u32x2 unpoison2(u32x2 v) { return u32x2{min(v[0], POISON - 1), min(v[1], POISON - 1)}; }
__device__ __forceinline__ u32x4 unpoison4(u32x4 v) {
  return u32x4{min(v[0], POISON - 1), min(v[1], POISON - 1), min(v[2], POISON - 1), min(v[3], POISON - 1)};
}

typedef union { struct { s16x4 a, b; } hf; u32x4 v; } frag_t;
__device__ __forceinline__ s16x4 trread(const char* smem, unsigned off) {
  return __builtin_amdgcn_ds_read_tr16_b64_v4i16((LDS_AS s16x4*)(smem + off));
}
__device__ __forceinline__ s16x4 trabs(unsigned addr) { return __builtin_amdgcn_ds_read_tr16_b64_v4i16((LDS_AS s16x4*)(size_t)addr); }

constexpr int WA = 6;
#define WPS 16
#define CH 0, 6, 8, 9, 15, 18, 23, 25, 29, 31, 4, 4
constexpr int CHV[12] = {CH};
constexpr int FKL = CHV[10], FSN = CHV[11];
constexpr int SPL = CHV[0];
constexpr int FSL = CHV[1], FS0 = CHV[2], FS0D = CHV[3], FS1 = CHV[4], FS1S = CHV[5];
constexpr int FXA = CHV[6], FXB = CHV[7], FXL = CHV[8], FXS = CHV[9];
#define ROT0 p
#define L0S 22, 25, 0, 0, 24, 28, 2, 2, 4, 14
constexpr int L0V[10] = {L0S};
constexpr int CSL = L0V[0], CSS = L0V[1], CSD = L0V[2];
constexpr int abl(int it) { return it ? 25 : L0V[4]; }
constexpr int abs_(int it) { return it ? 27 : L0V[5]; }
constexpr int W2S = L0V[6], WSH = L0V[7];
constexpr int PRE0 = 13;
constexpr int LNL = L0V[3], LNS = L0V[8], POI = L0V[9];
#define TCH 22, 3, 0x07, 26, 0xF8, 0, 1
constexpr int TCV[7] = {TCH};
constexpr int TS0 = TCV[0], TSN = TCV[1], TM0 = TCV[2], TEX = TCV[3], TM1 = TCV[4], TSD = TCV[5], TSG = TCV[6];
constexpr int QL = 26;
#define SLP 18, 4, 28, 3, 0, 0
constexpr int SLV[6] = {SLP};
constexpr int SDIR = SLV[0], GSL = SLV[1], RSL = SLV[2], KSL = SLV[3];
#define IME 4, 3, 8, 10, 2, 2
constexpr int IMV[6] = {IME};
constexpr int IMB = IMV[4], IMD = IMV[5];
constexpr int im_st(int s, int n) { return IMB == 2 ? (s == CSD && n >= 4 ? n - 4 : s == CSD + 1 && n == 0 ? 2 : -1) : IMB ? (s == CSD + 1 && (n & 1) ? n >> 1 : -1) : (s > CSD && s <= CSD + 3 && n == 5 ? s - CSD - 1 : -1); }
constexpr int IMT = IMV[0], IMF = IMV[1], IML = IMV[2], IMK = IMV[3];
constexpr int tp0(int it) { return it ? 3 : 3 + WSH; }
static_assert(((IMF + 1) & ~3) - 2 >= CSD + (IMB == 2 ? 2 : IMB ? 5 : 7) && (IMK & 3) == 2 && IMK < 4 * IMT - 12 + 3 + WSH + IMD && WSH + IMD <= 4, "a wait for a younger request and a barrier in between");
constexpr int tile_at(int it, int t) { return 4 * t - 12 + tp0(it) + (!it && t == IMT ? IMD : !it && t == IMT + 1 && IMD > 1 ? IMD - 1 : 0); }
constexpr int piece_tile(int it, int s) { for (int t = 3; t < 8; ++t) if (s >= tile_at(it, t) && s < tile_at(it, t) + 3) return t; return -1; }
constexpr bool tile_piece(int it, int s) { return piece_tile(it, s) >= 0; }
#define PS 14, 10, 10, 0
constexpr int PSV[4] = {PS};
constexpr int PSX = PSV[0], PSO = PSV[1], PSO0 = PSV[2], PSK = PSV[3];
#define NPRE 3
#define QAD 4
#define WO2 5, 3, 2, 8
constexpr int W2V[4] = {WO2};
#define WOE 1, 7, 15, 19, 22
constexpr int WEV[5] = {WOE};
constexpr int WOL = WEV[0], WOS = WEV[1], WOF = WEV[2], WOC = WEV[3], WOK = WEV[4];
static_assert(((WOS + 1) & ~3) - 2 >= WOL + 4 && ((WOF + 1) & ~3) - 2 >= WOS + 5 && (WOK & 3) == 2, "a wait for a younger request and a barrier in between");
constexpr int W2B = W2V[0], W2R = W2V[1], W2L = W2V[2], W2P = W2V[3];
constexpr int a_req(int s, int n) {
  int r = 2 * NPRE;
  for (int u = 1; u < 192; u += 2) {
    const bool go = r < 64 && (r >> 1) <= u / 6 + QAD && (r < 8 || u >= 9);
    if (u == 6 * s + n) return go ? r : -1;
    r += go;
  }
  return -1;
}
constexpr int vm_n(int it, int s, int n) {
  int c = it ? (a_req(s, n) >= 0) + (s == 0 && n == 0) : (s + 5 < 32 && (n == 2 || n == 3));
  if (n == 0) c += tile_piece(it, s);
  if (n == 4) c += (s == abl(it));
  if (!it) c += (s >= W2S && s < W2S + 3 && n == 4) + (s == LNL && n == 5 ? 2 : 0) + (s == 0 && n < 2 ? 2 : 0) + (s == LNS + 2 && n == 5) + (s == TEX && n == 5) + (s == WOL && n == 5) + (s == WOS + 1 && n == 5) + (im_st(s, n) >= 0);
  else c += (s == 2 && n == 2) + (s == (SPL < 0 ? 0 : SPL) && n == (SPL < 0 ? 1 : 4) ? 4 : 0) + (s >= 27 && s < 30 && (n == 0 || n == 3));
  return c;
}
constexpr int vm_at(int it, int s, int n) {
  int c = it ? 2 * NPRE : PRE0;
  for (int u = 0; u < 6 * s + n; ++u) c += vm_n(it, u / 6, u % 6);
  return c;
}
constexpr int vm_a(int it, int k) {
  if (k < NPRE) return 2 * k + 2;
  for (int u = 0; u < 192; ++u)
    if (a_req(u / 6, u % 6) == 2 * k + 1) return vm_at(it, (u + 1) / 6, (u + 1) % 6);
  return 0;
}
constexpr int vm_tile(int it, int t) { return t >= 3 ? vm_at(it, tile_at(it, t) + 2, 1) : t == 1 ? 12 : vm_at(0, W2S + 2, 5); }
constexpr int vm_unit(int u) { return u < 3 ? 5 + 2 * u : u < 5 ? vm_at(0, 0, u - 2) : vm_at(0, u - 5, 4); }
constexpr int unit_slot(int u) { return u % 3 == 0 ? 0 : 16384 + (u % 3) * 16384; }

constexpr int L_XC = LDS_BYTES - 64, L_SKEY = 32704, L_ROW = 1024, L_OUT = 65536, L_SRED = 4096, L_SFIN = 20480;
__device__ __forceinline__ unsigned key_load(rsrc_t rs, int l, int L, int rep) { return ld4(rs, (l & 15) * SKEY, KEY_AT(L, rep)); }
__device__ __forceinline__ unsigned umax16(unsigned k) {
  k = max(k, dppu<0xB1>(k));
  k = max(k, dppu<0x4E>(k));
  k = max(k, dppu<0x141>(k));
  return max(k, dppu<0x140>(k));
}
__device__ __forceinline__ unsigned key_row(unsigned key) { return (0xffffu - (key & 0xffffu)) & 2047u; }
__device__ __forceinline__ unsigned key_wait(rsrc_t rs, u32x2 T, int l, int L, int rep, unsigned kv) {
  if (!__all(kv != POISON)) {
    unsigned spins = 0;
    do {
      if (spin_fail(spins, rs, T)) break;
      if (L == 0) __builtin_amdgcn_s_sleep(KSL);
      kv = key_load(rs, l, L, rep);
    } while (!__all(kv != POISON));
  }
  return __builtin_amdgcn_readfirstlane(umax16(kv));
}
__device__ __forceinline__ unsigned cand_off(unsigned key, int L) { return OFF_CA + L * 65536 + (key_row(key) >> 7) * 4096; }
#define XO(i) (((i) >> 1) * 256 + ((i) & 1) * 64)
__device__ __forceinline__ void ffn_gu(const u32x4 (&gv)[4], const u32x4 (&uv)[4], const u32x4 (&xv)[4], f32x4& ga, f32x4& ua) {
  ga = mfma16(gv[0], xv[0], f32x4{0.f, 0.f, 0.f, 0.f}); ua = mfma16(uv[0], xv[0], f32x4{0.f, 0.f, 0.f, 0.f});
#pragma unroll
  for (int i = 1; i < 4; ++i) { ga = mfma16(gv[i], xv[i], ga); ua = mfma16(uv[i], xv[i], ua); }
}
__device__ __forceinline__ void ffn_gu_store(char* smem, int l, int w, f32x4 ga, f32x4 ua) {
  LDS_AS f32x4* sred = (LDS_AS f32x4*)(smem + L_SRED);
  if ((l & 15) == 0) { sred[w * 8 + (l >> 4)] = ga; sred[w * 8 + 4 + (l >> 4)] = ua; }
}
__device__ __forceinline__ f32x4 ffn_d_read(char* smem, int l) {
  const LDS_AS float* t = (const LDS_AS float*)(smem + L_SRED) + (l & 8) * 16 + (l & 4) * 4 + (l >> 4) * 4 + (l & 3);
  return f32x4{t[0], t[32], t[64], t[96]};
}
__device__ __forceinline__ u32x2 ffn_d_h(f32x4 t) {
  float x = t[0] + t[1];
  asm volatile("" : "+v"(x));
  x += t[2] + t[3];
  x += dppf<0x128>(x);
  const float y = dppf<0x124>(x);
  const bool up = __builtin_amdgcn_inverse_ballot_w64(0xF0F0F0F0F0F0F0F0ull);
  const float gg = rbf1(up ? y : x), uu = rbf1(up ? x : y);
  const float h = rbf1(gg * __builtin_amdgcn_rcpf(1.0f + __expf(-gg))) * uu;
  const unsigned p = pack2(h, dppf<0xB1>(h));
  return u32x2{dppu<0x00>(p), dppu<0xAA>(p)};
}
template <int t> __device__ __forceinline__ f32x4 ffn_d_mma(s16x4 d, u32x2 hh, f32x4 y) {
  const bool me = __builtin_amdgcn_inverse_ballot_w64(0x0001000100010001ull << t);
  return mfma16k16(d, __builtin_bit_cast(s16x4, u32x2{me ? hh[0] : 0u, me ? hh[1] : 0u}), y);
}
__device__ __forceinline__ f32x4 ffn_d_all(const s16x4 (&dfr)[8], u32x2 hh) {
  f32x4 a = {0.f, 0.f, 0.f, 0.f}, b = a;
  a = ffn_d_mma<0>(dfr[0], hh, a); b = ffn_d_mma<1>(dfr[1], hh, b); a = ffn_d_mma<2>(dfr[2], hh, a); b = ffn_d_mma<3>(dfr[3], hh, b);
  a = ffn_d_mma<4>(dfr[4], hh, a); b = ffn_d_mma<5>(dfr[5], hh, b); a = ffn_d_mma<6>(dfr[6], hh, a); b = ffn_d_mma<7>(dfr[7], hh, b);
  return a + b;
}
__device__ __forceinline__ void ffn_d_store(rsrc_t rs, int l, int w, int L, int slice, f32x4 y) {
  const unsigned c = l & 15;
  if (c < 8) st16(unpoison4(__builtin_bit_cast(u32x4, y)), rs, (2 * w + (c >> 2)) * 65536 + slice * 256 + (c & 3) * 64 + (l >> 4) * 16, OFF_PART + L * (1u << 20));
}
#define RED_VO(k) ((min(tid + 512 * (k), 1791) >> 3) * 256 + (tid & 7) * 16)
#define RED_SO(L, G) (OFF_PART + (L) * (1u << 20) + ((G) >> 1) * 65536 + ((G) & 1) * 128)
#define RED_JJ() ((tid & 3) * 16 + (tid & 4) * 2)
__device__ __forceinline__ void red_load(rsrc_t rs, int tid, int L, unsigned G, unsigned key, u32x4 (&pv)[4], u32x2& cxn, u32x2& cx1) {
#pragma unroll
  for (int k = 0; k < 4; ++k) pv[k] = ld16(rs, RED_VO(k), RED_SO(L, G));
  cxn = ld8(rs, RED_JJ(), cand_off(key, L) + XO(G));
  cx1 = ld8(rs, RED_JJ() + 128, cand_off(key, L) + XO(G));
}
__device__ __forceinline__ void red_sum(rsrc_t rs, char* smem, u32x2 T, int tid, int w, int L, unsigned G, unsigned key,
                                        u32x4 (&pv)[4], u32x2& cxn, u32x2& cx1) {
#define PV_OK() __all(max(max(max(umax4(pv[0]), umax4(pv[1])), max(umax4(pv[2]), umax4(pv[3]))), max(max(cxn[0], cxn[1]), max(cx1[0], cx1[1]))) != POISON)
  if (!PV_OK()) {
    unsigned spins = 0;
    do {
      if (spin_fail(spins, rs, T)) break;
#pragma unroll
      for (int k = 0; k < 4; ++k)
        if (umax4(pv[k]) == POISON) pv[k] = ld16(rs, RED_VO(k), RED_SO(L, G));
      if (max(max(cxn[0], cxn[1]), max(cx1[0], cx1[1])) == POISON) { cxn = ld8(rs, RED_JJ(), cand_off(key, L) + XO(G)); cx1 = ld8(rs, RED_JJ() + 128, cand_off(key, L) + XO(G)); }
    } while (!PV_OK());
  }
#undef PV_OK
  f32x4 s = (__builtin_bit_cast(f32x4, pv[0]) + __builtin_bit_cast(f32x4, pv[1])) + __builtin_bit_cast(f32x4, pv[2]);
  if (w < 4) s += __builtin_bit_cast(f32x4, pv[3]);
#pragma unroll
  for (int j = 0; j < 4; ++j) s[j] = sum_hi8(s[j]);
  if ((tid & 63) < 8) ((LDS_AS f32x4*)(smem + L_SFIN))[w * 8 + (tid & 7)] = s;
}
__device__ __forceinline__ float red_gate(unsigned key) {
  const unsigned k16 = key >> 16;
  const float rmax = __builtin_bit_cast(float, ((k16 & 0x8000u) ? (k16 & 0x7fffu) : (~k16 & 0xffffu)) << 16);
  float sig = rbf1(__builtin_amdgcn_rcpf(1.0f + __expf(-rmax)));
  asm volatile("" : "+v"(sig));
  return sig;
}
__device__ __forceinline__ u32x2 red_final(char* smem, int l, float sig, u32x2 cxn, u32x2 cx1) {
  f32x4 s = ((const LDS_AS f32x4*)(smem + L_SFIN))[l];
#pragma unroll
  for (int j = 0; j < 4; ++j) s[j] = sum_hi8(s[j]);
  const float xn4[4] = {lo16(cxn[0]), hi16(cxn[0]), lo16(cxn[1]), hi16(cxn[1])};
  const float x14[4] = {lo16(cx1[0]), hi16(cx1[0]), lo16(cx1[1]), hi16(cx1[1])};
  float o[4];
#pragma unroll
  for (int j = 0; j < 4; ++j) o[j] = x14[j] + rbf1(xn4[j] + rbf1(rbf1(s[j]) * sig));
  return u32x2{pack2(o[0], o[1]), pack2(o[2], o[3])};
}

struct RowFin { float mean, var, mb, rd; unsigned key; };
__device__ __forceinline__ void fin_wait(rsrc_t rs, u32x2 T, unsigned voff, unsigned so, u32x4 (&sp)[4]) {
#define ST_OK() __all(max(max(umax4(sp[0]), umax4(sp[1])), max(umax4(sp[2]), umax4(sp[3]))) != POISON)
  if (!ST_OK()) {
    unsigned spins = 0;
    do {
      if (spin_fail(spins, rs, T)) break;
#pragma unroll
      for (int i = 0; i < 4; ++i) sp[i] = ld16(rs, voff + i * 2048, so);
    } while (!ST_OK());
  }
#undef ST_OK
}
__device__ __forceinline__ RowFin fin_key(rsrc_t rs, u32x2 T, unsigned voff, unsigned so, char* skey, int l, int w, unsigned R, float wsum, u32x4 (&sp)[4]) {
  RowFin r;
#define ST_SUM() ((__builtin_bit_cast(f32x4, sp[0]) + __builtin_bit_cast(f32x4, sp[1])) + (__builtin_bit_cast(f32x4, sp[2]) + __builtin_bit_cast(f32x4, sp[3])))
  f32x4 sf = ST_SUM();
  const float z = (sf[0] + sf[1]) + sf[2];
  if (__any(z != z)) {
    fin_wait(rs, T, voff, so, sp);
    sf = ST_SUM();
  }
#undef ST_SUM
  float s0 = sf[0], s1 = sf[1], s2 = sf[2];
  add_g3(s0, s1, s2);
  r.mean = s0 * (1.0f / 1024.0f);
  r.var = fmaxf(__builtin_fmaf(-r.mean, r.mean, s1 * (1.0f / 1024.0f)), 0.f);
  r.mb = rbf1(r.mean); r.rd = ln_rd(r.var);
  const unsigned rb16 = pack2((s2 - r.mb * wsum) * r.rd, 0.f) & 0xffffu;
  r.key = min((((rb16 & 0x8000u) ? (~rb16 & 0xffffu) : (rb16 | 0x8000u)) << 16) | (0xffffu - R), POISON - 1);
  const unsigned wk = umax16(r.key);
  if (l == 0) ((LDS_AS unsigned*)skey)[w] = wk;
  return r;
}
__device__ __forceinline__ void fin_cand(rsrc_t rs, char* skey, int L, int g, int rb, int ct, const RowFin& r, const f32x2 (&v)[8]) {
  const u32x4 k0 = *(const LDS_AS u32x4*)skey, k1 = *(const LDS_AS u32x4*)(skey + 16);
  const unsigned best = max(umax4(k0), umax4(k1));
  if (r.key == best) {
    const unsigned co = L * 65536 + rb * 4096 + ct * 256 + g * 16;
    f32x2 t[8], rd2 = {r.rd, r.rd}, of2 = {-r.mb * r.rd, -r.mb * r.rd};
    asm volatile("" : "+v"(rd2), "+v"(of2));
#pragma unroll
    for (int j = 0; j < 8; ++j) t[j] = __builtin_elementwise_fma(v[j], rd2, of2);
    st16(unpoison4(pack8v(t)), rs, co, OFF_CA); st16(unpoison4(pack8v(t + 4)), rs, co + 64, OFF_CA);
    if (g == 0 && ct < (int)RKEY) st4(best, rs, KEY_AT(L, ct) + rb * SKEY, 0);
    st16(unpoison4(pack8v(v)), rs, co + 128, OFF_CA); st16(unpoison4(pack8v(v + 4)), rs, co + 192, OFF_CA);
  }
}
__device__ __forceinline__ void fin_x2(const RowFin& r, f32x2 (&v)[8]) {
  f32x2 sc2 = {1.0f + r.rd, 1.0f + r.rd}, of2 = {-r.mb * r.rd, -r.mb * r.rd};
  asm volatile("" : "+v"(sc2), "+v"(of2));
#pragma unroll
  for (int j = 0; j < 8; ++j) v[j] = __builtin_elementwise_fma(v[j], sc2, of2);
}

extern "C" __device__ unsigned long long dispatch_id() __asm("llvm.amdgcn.dispatch.id");

extern "C" __global__ __launch_bounds__(512, 2) void mega_kernel(const bf16* x, char* ws, const bf16* wqkv, const bf16* wo, const bf16* wr,
                                                                 const bf16* wg, const bf16* wu, const bf16* wd, bf16* out) {
  extern __shared__ __attribute__((aligned(16))) char smem[];
  const int w0_ = __builtin_amdgcn_readfirstlane(threadIdx.x >> 6);
  const unsigned lbase = (unsigned)(size_t)(LDS_AS char*)smem;
  {
    unsigned long long pc;
    unsigned len;
    asm volatile("s_getpc_b64 %0\n\ts_mov_b32 %1, .Ltext_end-mega_kernel\n\t.subsection 1\n.Ltext_end:\n\t.subsection 0" : "=s"(pc), "=s"(len));
    const unsigned o = ((blockIdx.x >> 3) * 64 + (threadIdx.x & 63)) * 128;
    if (w0_ == 7 && o < len)
      asm volatile("s_mov_b32 m0, %0\n\ts_nop 0\n\tglobal_load_lds_dword %1, %2" ::"s"(lbase + L_FXL), "v"(o), "s"(pc & ~127ull) : "memory");
  }
  asm volatile("" : "+s"(x), "+s"(ws), "+s"(wqkv));
#define IDS()                                                                                         \
  int tid = threadIdx.x, w = w0_, bid = blockIdx.x;                                                   \
  asm volatile("" : "+v"(tid));                                                                       \
  asm volatile("" : "+s"(w));                                                                         \
  asm volatile("" : "+s"(bid));                                                                       \
  const int l = tid & 63, xcd = bid & 7, bi = bid >> 3;                                               \
  const int p = (xcd & 1) * 4 + (bi & 3), h = (xcd >> 1) * 8 + (bi >> 2);         \
  const int rb = 2 * p + ((h >> 3) & 1), ct = (h >> 4) * 8 + (h & 7);       \
  const int slice = xcd * 28 + bi;                                                   \
  const bool ffn_on = bi < 28;                                                                        \
  const int g = l >> 4;                                                                               \
  const int rt = w < 4 ? w : 11 - w;          \
  KARGS();                                                                                            \
  (void)p; (void)h; (void)rb; (void)ct; (void)slice; (void)ffn_on; (void)g; (void)rt;
  const rsrc_t rs = __builtin_amdgcn_make_buffer_rsrc((void*)ws, 0, WS_BYTES, 0x00020000);
  typedef const void* const __attribute__((address_space(4)))* karg_t;
#define KARGS() karg_t ka = (karg_t)__builtin_amdgcn_kernarg_segment_ptr(); asm volatile("" : "+s"(ka))
#define ARGP(name, i) const GAS bf16* name = (const GAS bf16*)ka[i]; asm volatile("" : "+s"(name))
#define RSRC(ptr, bytes) __builtin_amdgcn_make_buffer_rsrc((void*)(ptr), 0, bytes, 0x00020000)
#define rwq RSRC(pwq, 1024 * 3072 * 2)
#define rwo RSRC(pwo, 1024 * 1024 * 2)
#define rwg RSRC(pwg, 1024 * F_ * 2)
#define rwu RSRC(pwu, 1024 * F_ * 2)
#define rwd RSRC(pwd, 1024 * F_ * 2)
#define rout RSRC(pout, 2048 * 1024 * 2)

#define WQ_VOFF() ((l >> 2) * 6144 + (((l & 3) ^ ((((l >> 4) ^ (l >> 5)) & 1) << 1)) << 4))
#define WQ_PIECE(t, j, dst) dma16(rwq, wqv, ((((t) + rot) & 7) * 128 + ((j) & 7) * 16) * 6144 + ((j) >> 3) * 2048 + h * 64, lbase + (dst) + (j) * 1024)
#define WQ_BOFF(dt) ((8 * g + ((l & 15) >> 2)) * 64 + (l & 3) * 8 + (((dt) ^ (g & 1)) << 5))
#define WQ_POFF() ((4 * g + ((l & 15) >> 2)) * 64 + (l & 3) * 8 + (((g ^ (g >> 1)) & 1) << 5))
#define X_UNIT1(u, j) dma16(rx, (j) ? xv1 : xv_, (unsigned)(p * 256 + rt * 32 + ((u) & 1) * 16 + (j) * 8) * 2048 + ((((u) >> 2) + rot) & 7) * 256 + (((u) >> 1) & 1) * 128, lbase + rt * 2048 + unit_slot(u) + (j) * 1024)
#define X_VOFF() ((l >> 3) * 2048 + (((l & 7) ^ (l >> 4)) << 4))
#define X_ROFF(hs) (lbase + rt * 2048 + (l & 15) * 128 + (((4 * (hs) + g) ^ ((l >> 1) & 7)) << 4))
#define X_POFF() (lbase + rt * 2048 + (l & 15) * 128 + (((g >> 1) ^ ((l >> 1) & 7)) << 4) + (g & 1) * 8)
#define TOUCH_TO(ptr, dst) asm volatile("s_mov_b32 m0, %0\n\ts_nop 0\n\tglobal_load_lds_dword %1, off" ::"s"(lbase + (dst)), "v"(ptr) : "memory")
#define TOUCH(ptr) TOUCH_TO(ptr, L_FXL)
#define WO_PIECE_W(j, w_)                                                                                                                 \
  {                                                                                                                                       \
    dma16s<0>(rs, l * 16, OFF_WOI + ct * 131072 + (j) * 8192 + (w_) * 1024, lbase + 32768 + (j) * 8192 + (w_) * 1024);                    \
  }
#define WO_PIECE(j) WO_PIECE_W(j, w)
#define WO_VOFF() ((l >> 3) * 2048 + (((((l & 7) >> 1) ^ ((l >> 4) & 3)) * 2 + (l & 1)) << 4))
#define WD_DMA() _Pragma("unroll") for (int i = 0; i < 4; ++i) WD_DMA1(i)
#define WD_DMA1(i)                                                                                   \
      dma16n(rwd, (l >> 4) * 2048 + (((l & 15) * 16) ^ ((l >> 4) * 32) ^ (((i) & 1) * 128)), (unsigned)(slice * 16 + 4 * (i)) * 2048 + w * 256, lbase + L_DN + w * 4096 + (i) * 1024)
#define FFN_DMA(m) dma16((m) & 1 ? rwu : rwg, fnv, (unsigned)(((4 * w + ((m) >> 1)) * 32) * F_ + slice * 16) * 2, lbase + 65536 + (m) * 8192 + w * 1024)
  u32x4 gv[4], uv[4];
  s16x4 dfr[8];
  u32x4 keep[9];
  u32x4 pre[4][2];
#define PRE_LOAD(it_, rot_, s0, s1)                                                                  \
  static_for<(s1) - (s0)>([&](auto c_) __attribute__((always_inline)) {                              \
    constexpr int s = (s0) + decltype(c_)::value;                                                    \
    const unsigned ab_ = OFF_XN + (it_) * (4u << 20) + (((rot_) + (s >> 2)) & 7) * 8192 + ((s >> 1) & 1) * 4096; \
    ld16w<2 * s, (s & 1) * 2048>(pre[s][0], rs, l * 16, ab_ + (p * 8 + rt) * 65536);                         \
    ld16w<2 * s + 1, (s & 1) * 2048 + 1024>(pre[s][1], rs, l * 16, ab_ + (p * 8 + rt) * 65536);                 \
  })
  u32x2 T;
  u32x2 ep;
  u32x4 xl[2];
#define LN_LOAD()                                                                                                                                   \
  {                                                                                                                                                 \
    asm volatile("buffer_load_dwordx4 %0, %1, %2, %3 offen" : "=v"(xl[0]) : "v"(l * 16), "s"(rx), "s"((p * 256 + h * 8 + w) * 2048) : "memory");      \
    asm volatile("buffer_load_dwordx4 %0, %1, %2, %3 offen offset:1024" : "=v"(xl[1]) : "v"(l * 16), "s"(rx), "s"((p * 256 + h * 8 + w) * 2048) : "memory"); \
  }
  const GAS bf16* tsrc;
  {
    IDS();
    const int it = 0, rot = ROT0;
    (void)it;
    const bf16* pwq = wqkv;
    const rsrc_t rx = RSRC(x, 2048 * 1024 * 2);
    const unsigned wqv = WQ_VOFF(), xv_ = X_VOFF(), xv1 = xv_ ^ 64;
    if (tid == 0) *(LDS_AS u32x4*)(smem + L_QC) = u32x4{0u, 0u, 0u, 0u};
    if (w == 0 && l < 24) TOUCH((const GAS bf16*)wqkv + (bid * 24 + l) * 512 + ((3 - (unsigned)((size_t)wqkv >> 7)) & 7) * 64);
#pragma unroll
    for (int i = 0; i < 3; ++i) WQ_PIECE(0, 3 * w + i, L_RING);
    X_UNIT1(0, 0); X_UNIT1(0, 1); X_UNIT1(1, 0); X_UNIT1(1, 1);
    BAR();
    X_UNIT1(2, 0); X_UNIT1(2, 1);
#pragma unroll
    for (int i = 0; i < 3; ++i) WQ_PIECE(1, 3 * w + i, L_RING + STAGE);
    asm volatile("buffer_load_dwordx2 %0, off, %1, 0 sc1" : "=v"(ep) : "s"(rs) : "memory");
  }
  unsigned sel0 = 0x7FFFFFFFu;
  f32x2 v[8];
  u32x4 x2p[2];
  float wsum;

  auto layer = [&](auto it_c) __attribute__((always_inline)) {
    constexpr int it = decltype(it_c)::value;
    f32x4 acc[2][6];
    f32x2 qs2[2];
    u32x4 k0, k1, v0, v1;
    {
      IDS();
      bool aff = false;
      unsigned aq[2];
      u32x4 av[2];
#define PIN_ACC() asm volatile("" : "+{v[176:179]}"(acc[0][0]), "+{v[180:183]}"(acc[0][1]), "+{v[184:187]}"(acc[0][2]), "+{v[188:191]}"(acc[0][3]), "+{v[192:195]}"(acc[0][4]), "+{v[196:199]}"(acc[0][5]), \
                               "+{v[200:203]}"(acc[1][0]), "+{v[204:207]}"(acc[1][1]), "+{v[208:211]}"(acc[1][2]), "+{v[212:215]}"(acc[1][3]), "+{v[216:219]}"(acc[1][4]), "+{v[220:223]}"(acc[1][5]))
#define PINV4(x) asm volatile("" : "+v"(x[0]), "+v"(x[1]), "+v"(x[2]), "+v"(x[3])::"memory")
      auto qkv_loop = [&](auto it_c) __attribute__((always_inline)) {
        constexpr int IT = decltype(it_c)::value;
        const unsigned abase = OFF_XN + IT * (4u << 20) + (p * 8 + rt) * 65536;
        const int rot = ROT0 + 5 * IT;
        unsigned fkey = 0, kv, bq[2];
        u32x4 fx[4], bv[2], af[WA][2];
        u32x2 cxn, cx1, hh;
        f32x4 sa, sb, cs4[2];
        f32x2 b2[2];
#define AB_OFF() (OFF_AB + IT * 32768 + (p * 8 + rt) * 512)
#define A_LOAD1(k, a) ld16w<(k) % WA * 2 + (a), ((k) & 1) * 2048 + (a) * 1024>(af[(k) % WA][a], rs, l * 16, abase + ((((k) >> 2) + rot) & 7) * 8192 + (((k) >> 1) & 1) * 4096)
        if constexpr (IT == 1) {
#pragma unroll
          for (int s = 0; s < NPRE; ++s) { af[s][0] = pre[s][0]; af[s][1] = pre[s][1]; }
        }
        const bf16* pwq = wqkv;
        ARGP(pwo, 3);
        const rsrc_t rx = RSRC(x, 2048 * 1024 * 2);
        unsigned xv_ = X_VOFF(), xv1 = xv_ ^ 64, xr[4] = {X_ROFF(0), X_ROFF(1), 0u, 0u}, bf_[2];
        if constexpr (IT == 0) asm volatile("" : "+v"(xv_), "+v"(xv1), "+v"(xr[0]), "+v"(xr[1]));
#define LDS8(a_) (*(const LDS_AS u32x2*)(size_t)(a_))
#define X_READ(u)                                                                                                              \
  {                                                                                                                            \
    if constexpr ((u) < 4 * IMT) {                                                                                             \
      af[((u) & ~1) % WA][(u) & 1] = *(const LDS_AS u32x4*)(size_t)(xr[0] + unit_slot(u));                                     \
      af[((u) | 1) % WA][(u) & 1] = *(const LDS_AS u32x4*)(size_t)(xr[1] + unit_slot(u));                                      \
    } else {                                                                                                                   \
      const u32x2 a_ = LDS8(xr[0] + unit_slot(u)), b_ = LDS8(xr[1] + unit_slot(u)), c_ = LDS8(xr[2] + unit_slot(u)), d_ = LDS8(xr[3] + unit_slot(u)); \
      af[((u) & ~1) % WA][(u) & 1] = u32x4{a_[0], a_[1], b_[0], b_[1]};                                                        \
      af[((u) | 1) % WA][(u) & 1] = u32x4{c_[0], c_[1], d_[0], d_[1]};                                                         \
    }                                                                                                                          \
  }
        const int tstep = TS0 + bid % TSN + TSD * (w >> TSG);
        const unsigned wqv = WQ_VOFF();
#define QKV_DMA1(t, i)                                                                            \
  {                                                                                               \
    if constexpr (IT == 0 && (t) < IMT) WQ_PIECE(t, 3 * w + (i), L_RING + SLOT(t) * STAGE);       \
    else dma16s<(i) * 1024>(rs, l * 16, OFF_WI + (h * 8 + (((t) + rot) & 7)) * STAGE + w * 3072, lbase + L_RING + SLOT(t) * STAGE + w * 3072); \
  }
        unsigned bo[2][2];
#pragma unroll
        for (int dt = 0; dt < 2 - IT; ++dt) {
          bo[dt][0] = lbase + L_RING + (IT ? l * 16 : WQ_BOFF(dt)); bo[dt][1] = bo[dt][0] + 2 * STAGE;
          asm volatile("" : "+v"(bo[dt][0]), "+v"(bo[dt][1]));
        }
        auto frag0 = [&](int sl, int f) __attribute__((always_inline)) {
          unsigned pb = lbase + L_RING + WQ_POFF();
          asm volatile("" : "+v"(pb));
          const unsigned o = (pb ^ ((f & 1) << 5)) + sl * STAGE + ((f % 6) >> 1) * 8192 + (f / 6) * 2048;
          frag_t t;
          t.hf.a = trabs(o); t.hf.b = trabs(o + 1024);
          return t.v;
        };
        u32x4 df[4];
        auto dot_mma = [&](const u32x4 (&xf)[4]) __attribute__((always_inline)) {
          sa = mfma16(df[0], xf[0], f32x4{0.f, 0.f, 0.f, 0.f}); sb = mfma16(df[1], xf[1], f32x4{0.f, 0.f, 0.f, 0.f});
          sa = mfma16(df[2], xf[2], sa); sb = mfma16(df[3], xf[3], sb);
        };
        auto dot_store = [&](unsigned dst) __attribute__((always_inline)) {
          if ((l & 15) == 0) st16(unpoison4(__builtin_bit_cast(u32x4, sa + sb)), rs, g * 16, dst + (h * 8 + p) * 512 + w * 64);
        };
        auto tile_sum = [&](u32x4 pt, int dst) __attribute__((always_inline)) {
          f32x4 c = __builtin_bit_cast(f32x4, pt);
#pragma unroll
          for (int e = 0; e < 4; ++e) { c[e] += dppf<0x124>(c[e]); c[e] += dppf<0x128>(c[e]); c[e] = add_x16(c[e]); }
          if ((l & 28) == 0) *(LDS_AS f32x4*)(smem + dst + (w * 2 + (l >> 5)) * 64 + (l & 3) * 16) = c;
        };
        frag_t bfr[6];
#define QKV_BO(s, n, x) (bo[((n) ^ (x)) & 1][SLOT((s) >> 2) >> 1] + (SLOT((s) >> 2) & 1) * STAGE + ((n) >> 1) * 8192 + ((s) & 3) * 2048 + (x) * 256)
#define QKV_RDA(s, n)                                                                             \
  {                                                                                               \
    if constexpr (IT == 0 && (s) < 4 * IMT) bfr[n].hf.a = trabs(QKV_BO(s, n, 0));                 \
    else if constexpr (IT == 0) bfr[n].v = *(const LDS_AS u32x4*)(size_t)(bf_[SLOT((s) >> 2) >> 1] + (SLOT((s) >> 2) & 1) * STAGE + (((s) & 3) * 6 + (n)) * 1024); \
    else bfr[n].v = *(const LDS_AS u32x4*)(size_t)(bo[0][SLOT((s) >> 2) >> 1] + (SLOT((s) >> 2) & 1) * STAGE + (((s) & 3) * 6 + (n)) * 1024); \
  }
#define QKV_RDB(s, n) { if constexpr (IT == 0 && (s) < 4 * IMT) bfr[n].hf.b = trabs(QKV_BO(s, n, 1)); }
#define XP_OFF() (OFF_XNEW + ((rot + 5) & 7) * 1024)
#define FP_VO() (w == 3 ? (l >> 2) * 256 + (l & 3) * 16 : ((l >> 2) & 7) * 512 + (l >> 5) * 64 + (l & 3) * 16)
#define FP_OFF(i) (w == 3 ? OFF_XNEW + (i) * 4096 : OFF_FX + h * 4096 + w * 128)
#define FX_OK(n) __all(((n) == 4 ? max(max(umax4(fx[0]), umax4(fx[1])), max(umax4(fx[2]), umax4(fx[3]))) : max(umax4(fx[0]), umax4(fx[1]))) != POISON)
#define SINCE(s0) (vm_at(IT, s, n) - vm_at(IT, s0, 5))
#define DEF4(x) asm volatile("" : "=v"(x[0]), "=v"(x[1]), "=v"(x[2]), "=v"(x[3]))
        LDS_AS unsigned* qcnt = (LDS_AS unsigned*)(smem + L_QC);
        unsigned qc;
        unsigned qadr = lbase + L_QC, qone = 1;
        asm volatile("" : "+v"(qadr), "+v"(qone));
#define count_in(i) asm volatile("s_mov_b64 exec, 1\n\tds_add_u32 %0, %1 offset:%2\n\ts_mov_b64 exec, -1" ::"v"(qadr), "v"(qone), "n"(4 * (i)) : "memory")
        auto count_wait = [&](int i, unsigned want) __attribute__((always_inline)) {
          if (__builtin_expect((unsigned)__builtin_amdgcn_readfirstlane(qc) < want, 0)) {
            while ((unsigned)__builtin_amdgcn_readfirstlane(__hip_atomic_load(qcnt + i, __ATOMIC_RELAXED, __HIP_MEMORY_SCOPE_WORKGROUP)) < want) {}
          }
          asm volatile("" ::: "memory");
        };
        if constexpr (IT == 0) {
          wait_vm<PRE0 - vm_unit(1)>();
          X_READ(0); X_READ(1);
          asm volatile("s_barrier" ::: "memory");
        }
#pragma unroll
        for (int n = 0; n < 6; ++n) { QKV_RDA(0, n); if (n < 5) QKV_RDB(0, n); }
        f32x2 lv[4], xs, xq;
        float lmean;
        f32x4 sc;
#define X1_OFF() (F_X1 + ((p * RX1 + (xcd >> 1) % RX1) * 32 + (rt >> 2) * 16 + (l & 15)) * SX1)
        unsigned long long xok = 0;
        RowFin fin;
        auto x1_need = [&](unsigned long long m) __attribute__((always_inline)) {
          if (__builtin_expect((xok & m) != m, 0)) {
            for (unsigned spins = 0;;) {
              const u32x2 f = ld8(rs, X1_OFF(), 0);
              xok = __builtin_amdgcn_ballot_w64(f[0] == T[0] && f[1] == T[1]);
              if ((xok & m) == m) break;
              if (spin_fail(spins, rs, T)) { xok = ~0ull; break; }
              __builtin_amdgcn_s_sleep(KSL);
            }
            asm volatile("" ::: "memory");
          }
        };
        auto gap = [&](auto sc_, auto nc_) __attribute__((always_inline)) {
          constexpr int s = decltype(sc_)::value, n = decltype(nc_)::value, ar = a_req(s, n);
          {
            constexpr int tc = (s + 2) / 4;
            if constexpr (tc >= 1 && tc < 8 && s == 4 * tc - 2 && n == 5) {
              if constexpr (!IT || tc >= 3) wait_vm<vm_at(IT, s, n) - vm_tile(IT, tc)>();
              if constexpr (!IT && IMB == 2 && s == ((IMF + 1) & ~3) - 2) wait_vm<vm_at(0, s, n) - vm_at(0, CSD + 1, 1)>();
              if constexpr (tc == 7 && !IT) asm volatile("s_waitcnt lgkmcnt(4)" ::: "memory");
              asm volatile("s_barrier" ::: "memory");
            }
          }
          if constexpr (IT == 0 && s == 4 * IMT - 3 && n == 5) {
            xr[0] = X_POFF(); xr[1] = xr[0] ^ 32; xr[2] = xr[0] ^ 64; xr[3] = xr[0] ^ 96;
            bf_[0] = lbase + L_RING + l * 16; bf_[1] = bf_[0] + 2 * STAGE;
            asm volatile("" : "+v"(xr[0]), "+v"(xr[1]), "+v"(xr[2]), "+v"(xr[3]), "+v"(bf_[0]), "+v"(bf_[1]));
          }
          if constexpr (IT == 0 && n == 0 && s + 2 < 32) { wait_vm<vm_at(0, s, 0) - vm_unit(s + 2)>(); X_READ(s + 2); SB(); }
          if constexpr (n == 0 && tile_piece(IT, s)) { constexpr int t_ = piece_tile(IT, s), i_ = s - tile_at(IT, t_); QKV_DMA1(t_, i_); }
          if constexpr (IT == 0) {
            if constexpr ((n == 2 || n == 3) && s + 5 < 32) {
              if constexpr (n == 2) asm volatile("s_waitcnt lgkmcnt(%0)" ::"n"(s + 1 < QL ? (s + 1 < 4 * IMT ? 4 : 2) : 1) : "memory");
              X_UNIT1(s + 5, n - 2);
            }
            if constexpr (s >= W2S && s < W2S + 3 && n == 4) QKV_DMA1(2, s - W2S);
            if constexpr (s == LNL && n == 5) LN_LOAD();
            if constexpr (s == 1 && n == 4) {
              asm volatile("" : "+v"(ep)::"memory");
              const unsigned long long e = (((unsigned long long)(unsigned)__builtin_amdgcn_readfirstlane(ep[1]) << 32) | (unsigned)__builtin_amdgcn_readfirstlane(ep[0])) + 1ull + dispatch_id() * 0x9E3779B97F4A7C16ull;
              T = u32x2{(unsigned)e, (unsigned)(e >> 32)};
            }
            if constexpr (s == 0 && n < 2) { X_UNIT1(3 + n, 0); X_UNIT1(3 + n, 1); }
            if constexpr (s == LNS && n == 1) {
              wait_vm<vm_at(0, s, n) - vm_at(0, LNL + 1, 0)>();
              asm volatile("" : "+v"(xl[0]), "+v"(xl[1])::"memory");
              unpack8v(xl[0], lv);
              xs = (lv[0] + lv[1]) + (lv[2] + lv[3]);
              xq = __builtin_elementwise_fma(lv[0], lv[0], lv[1] * lv[1]) + __builtin_elementwise_fma(lv[2], lv[2], lv[3] * lv[3]);
              asm volatile("" : "+v"(xs), "+v"(xq));
            }
            if constexpr (s == LNS && n == 4) {
              unpack8v(xl[1], lv);
              xs += (lv[0] + lv[1]) + (lv[2] + lv[3]);
              xq += __builtin_elementwise_fma(lv[0], lv[0], lv[1] * lv[1]) + __builtin_elementwise_fma(lv[2], lv[2], lv[3] * lv[3]);
              auto t = __builtin_amdgcn_permlane32_swap(__builtin_bit_cast(unsigned, hadd(xs)), __builtin_bit_cast(unsigned, hadd(xq)), false, false);
              lmean = asf(t[0]) + asf(t[1]);
              asm volatile("" : "+v"(lmean));
            }
            if constexpr (s == LNS + 1 && n == 1) { lmean += dppf<0xB1>(lmean); lmean += dppf<0x4E>(lmean); lmean += dppf<0x124>(lmean); asm volatile("" : "+v"(lmean)); }
            if constexpr (s == LNS + 1 && n == 4) { lmean = add_x16(lmean + dppf<0x128>(lmean)) * (1.0f / 1024.0f); asm volatile("" : "+v"(lmean)); }
            if constexpr (s == LNS + 2 && n == 1) {
              const float m2 = asf(__builtin_amdgcn_readlane(__builtin_bit_cast(unsigned, lmean), 32));
              xs[0] = ln_rd(fmaxf(__builtin_fmaf(-lmean, lmean, m2), 0.f));
              asm volatile("" : "+v"(xs));
            }
            if constexpr (s == LNS + 2 && n == 5) {
              if (l == 0) st16(u32x4{__builtin_bit_cast(unsigned, (float)xs[0]), __builtin_bit_cast(unsigned, -rbf1(lmean)), T[0], T[1]}, rs, OFF_AB + (p * 256 + h * 8 + w) * 16, 0);
            }
            if constexpr (s == POI && n == 1) {
              if (w == 1) {
                const unsigned prb = 2 * p + (h >> 4), pct = h & 15;
                unsigned pz1 = POISON;
                asm volatile("" : "+v"(pz1));
                const u32x4 pz = {pz1, pz1, pz1, pz1};
                st16(pz, rs, OFF_STAT + (prb * 16 + pct) * 2048 + l * 16, 0);
                st16(pz, rs, OFF_STAT + (prb * 16 + pct) * 2048 + 1024 + l * 16, 0);
                if (l < 16) st16(pz, rs, OFF_CA + prb * 4096 + pct * 256 + l * 16, 0);
                else if (l == 16 && pct < RKEY) st4(pz1, rs, KEY_AT(0, pct) + prb * SKEY, 0);
              }
            }
            if constexpr (s == POI + 4 && n == 1) { if (w == 1 && l < RX) st8(T, rs, F_XN + (p * RX + l) * 256 + h * 8, 0); }
          } else {
            if constexpr ((s == 0 && n == 0) || (s == 2 && n == 2)) asm volatile("buffer_load_dwordx2 %0, %1, %2, 0 offen sc1" : "=v"(cxn) : "v"(X1_OFF()), "s"(rs) : "memory");
            if constexpr ((s == 1 && n == 3) || (s == 4 && n == 0)) {
              wait_vm<vm_at(1, s, n) - (s == 1 ? vm_at(1, 0, 1) : vm_at(1, 2, 3))>();
              asm volatile("" : "+v"(cxn)::"memory");
              xok |= __builtin_amdgcn_ballot_w64(cxn[0] == T[0] && cxn[1] == T[1]);
            }
            if constexpr (ar >= 0 && (ar & 7) == 0) x1_need(3ull << (((rot + ar / 8) & 7) * 2));
            const unsigned R = (rb * 4 + (w & 3)) * 32 + (w >> 2) * 16 + (l & 15), so = OFF_STAT + rb * 32768;
            if constexpr (s == (SPL < 0 ? 0 : SPL) && n == (SPL < 0 ? 1 : 4)) {
#pragma unroll
              for (int i = 0; i < 4; ++i) ld16h<0>(fx[i], rs, (4 * g + i) * 2048 + (R & 127) * 16, so);
            }
            if constexpr (s == SPL + 2 && n == 1) {
              wait_vm<vm_at(1, s, n) - (SPL < 0 ? vm_at(1, 0, 2) : vm_at(1, SPL, 5))>();
              PINV4(fx);
              fin = fin_key(rs, T, g * 8192 + (R & 127) * 16, so, smem + L_SKEY, l, w, R, wsum, fx);
              count_in(2);
            }
            if constexpr (s == SPL + 3 && n == 0) qc = __hip_atomic_load(qcnt + 2, __ATOMIC_RELAXED, __HIP_MEMORY_SCOPE_WORKGROUP);
            if constexpr (s == SPL + 3 && n == 1) { count_wait(2, 8); fin_cand(rs, smem + L_SKEY, 0, g, rb, ct, fin, v); }
            if constexpr (s == SPL + 3 && n == 3) {
              fin_x2(fin, v);
              x2p[0] = pack8v(v); x2p[1] = pack8v(v + 4);
              asm volatile("" : "+v"(x2p[0]), "+v"(x2p[1]));
            }
            if constexpr (s == SPL + 3 && n == 5) {
              if ((w & 3) * 2 + (w >> 2) == ct >> 1) {
                const float f = 1.0f + fin.rd, rd2 = ln_rd(fin.var * f * f), mb2 = rbf1(__builtin_fmaf(fin.mean - fin.mb, fin.rd, fin.mean));
                if (g == 0 && ((l >> 3) & 1) == (ct & 1))
                  st16(u32x4{__builtin_bit_cast(unsigned, f * rd2), __builtin_bit_cast(unsigned, -__builtin_fmaf(fin.mb, fin.rd, mb2) * __builtin_amdgcn_rcpf(f)), T[0], T[1]}, rs, OFF_AB + 32768 + R * 16, 0);
              }
            }
          }
          if constexpr (IT == 1 && ar >= 0) A_LOAD1(ar / 2, ar & 1);
          if constexpr (!IT && s == CSL && n == 4) { asm volatile("" : "=v"(fx[0])); if (w < 6) ld16h<0>(fx[0], rs, l * 16, OFF_CS + (h * 6 + w) * 1024); }
          if constexpr (!IT && s == CSS && n == 2) {
            if (w < 6) {
              wait_vm<SINCE(CSL)>();
              asm volatile("" : "+v"(fx[0])::"memory");
              tag_wait(rs, T, l * 16, OFF_CS + (h * 6 + w) * 1024, fx[0]);
              const f32x2 c = {sum_hi8(asf(fx[0][0])), sum_hi8(asf(fx[0][1]))};
              if (l < 8) *(LDS_AS f32x2*)(smem + L_CSL + w * 64 + l * 8) = c;
            }
          }
          constexpr int ABL = abl(IT), ABS = abs_(IT);
          if constexpr (s == ABL && n == 4) ld16h<0>(bv[0], rs, (l & 31) * 16, AB_OFF());
          if constexpr (s == ABS && n == 2) {
            wait_vm<SINCE(ABL)>();
            asm volatile("" : "+v"(bv[0])::"memory");
            tag_wait(rs, T, (l & 31) * 16, AB_OFF(), bv[0]);
            const unsigned t0 = bv[0][0], t1 = bv[0][1];
            ((LDS_AS unsigned*)(smem + L_ABW + rt * 2048))[l] = l < 32 ? t0 : t1;
          }
          if constexpr (s == ABS && n == 3) {
#pragma unroll
            for (int a = 0; a < 2; ++a) {
              const LDS_AS unsigned* aw = (const LDS_AS unsigned*)(smem + L_ABW + rt * 2048) + 16 * a;
              aq[a] = aw[l & 15]; bq[a] = aw[32 + (l & 15)];
              av[a] = *(const LDS_AS u32x4*)(aw + 4 * g); bv[a] = *(const LDS_AS u32x4*)(aw + 32 + 4 * g);
            }
          }
          if constexpr (s == ABS && n == 5) {
#pragma unroll
            for (int a = 0; a < 2; ++a) { b2[a] = f32x2{asf(bq[a]), asf(bq[a])}; asm volatile("" : "+v"(b2[a])); }
          }
          if constexpr (s == ABS + 1) {
            constexpr int k = (n + 4) % 6;
            if constexpr (k < 4) cs4[k & 1] = *(const LDS_AS f32x4*)(smem + L_CSL + k * 64 + g * 16);
            else cs4[k & 1][0] = *(const LDS_AS float*)(smem + L_CSL + k * 64 + (l & 15) * 4);
          }
          if constexpr ((s == ABS + 1 && n > 0) || (s == ABS + 2 && n == 0)) {
            constexpr int k = (n + 3) % 6;
            if constexpr (k < 4) {
#pragma unroll
              for (int a = 0; a < 2; ++a) acc[a][k] = __builtin_elementwise_fma(cs4[k & 1], f32x4{b2[a][0], b2[a][1], b2[a][0], b2[a][1]}, acc[a][k]);
            } else {
              f32x2 c2 = {cs4[k & 1][0], cs4[k & 1][0]};
              asm volatile("" : "+v"(c2));
#pragma unroll
              for (int a = 0; a < 2; ++a) acc[a][k] = __builtin_elementwise_fma(f32x4{c2[0], c2[1], c2[0], c2[1]}, __builtin_bit_cast(f32x4, bv[a]), acc[a][k]);
            }
          }
          if constexpr (IT == 0) {
            if constexpr (s == CSD && n < 3) fx[n] = frag0(SLOT(s >> 2), 3 * w + n);
            if constexpr (s == CSD + 1 && n == 1) {
              unsigned one2 = 0x3F803F80u;
              asm volatile("" : "+v"(one2));
              const u32x4 o4 = {one2, one2, one2, one2};
              const f32x4 z4 = {0.f, 0.f, 0.f, 0.f};
              sa = mfma16(fx[0], o4, z4); sb = mfma16(fx[1], o4, z4); sc = mfma16(fx[2], o4, z4);
            }
            constexpr int is_ = im_st(s, n);
            if constexpr (is_ >= 0) st16(fx[is_ < 0 ? 0 : is_], rs, l * 16 + is_ * 1024, OFF_WI + (h * 8 + ((rot + (CSD >> 2)) & 7)) * STAGE + w * 3072);
            if constexpr (s == IMF && n == 1) { if (tid == 0) st8(T, rs, (h * 8 + p) * 256, F_IM); }
            if constexpr (s == IML && n == 4) { asm volatile("" : "=v"(cxn)); if (w == 0) ld8h(cxn, rs, (l & 7) * 256, F_IM + h * 2048); }
            if constexpr (s == IMK && n == 3) {
              if (w == 0) {
                wait_vm<SINCE(IML)>();
                asm volatile("" : "+v"(cxn)::"memory");
                if (!__all(cxn[0] == T[0] && cxn[1] == T[1])) poll_flags(rs, (l & 7) * 256, F_IM + h * 2048, T);
              }
            }
            if constexpr (s == CSD + 2 && n == 1) {
              const int c = l & 15;
              const f32x4 t0 = sa, t1 = sb, t2 = sc, t = c == 0 ? +t0 : c == 1 ? +t1 : +t2;
              if (c < 3) *(LDS_AS f32x4*)(smem + L_CSP + (w * 3 + c) * 64 + g * 16) = t;
              count_in(2);
            }
            if constexpr (s == CSD + 4 && n == 1) {
              if (w == 2) {
                qc = 0;
                count_wait(2, 8);
                const LDS_AS f32x2* cp = (const LDS_AS f32x2*)(smem + L_CSP) + min(l, 47);
                const f32x2 t = (cp[0] + cp[48]) + (cp[96] + cp[144]);
                if (l < 48) st16(u32x4{__builtin_bit_cast(unsigned, (float)t[0]), __builtin_bit_cast(unsigned, (float)t[1]), T[0], T[1]}, rs, (l >> 3) * 1024 + (l & 7) * 16, OFF_CS + h * 6144 + p * 128);
              }
            }
            if constexpr (s == WOL && n == 5) dma16(rwo, WO_VOFF(), (rb * 8 + w) * 16384 + ct * 128, lbase + L_WOS + w * 1024);
            if constexpr (s == WOS && n == 1) {
              const unsigned o_ = lbase + L_WOS + (w >> 2) * 4096 + (4 * g + ((l & 15) >> 2)) * 128 + (l & 3) * 8 + (((w & 3) ^ (2 * (g & 1) + ((l & 15) >> 3))) << 5);
              frag_t f_;
              f_.hf.a = trabs(o_); f_.hf.b = trabs(o_ + 2048);
              fx[3] = f_.v;
            }
            if constexpr (s == WOS + 1 && n == 5) st16(fx[3], rs, (ct * 128 + rb * 8 + w) * 1024 + l * 16, OFF_WOI);
            if constexpr (s == WOF && n == 1) { if (tid == 128) st8(T, rs, (ct * 16 + rb) * 256, F_WOI); }
            if constexpr (s == WOC && n == 4) { asm volatile("" : "=v"(cx1)); if (w == 3) ld8h(cx1, rs, (l & 15) * 256, F_WOI + ct * 4096); }
            if constexpr (s == WOK && n == 3) {
              if (w == 3) {
                wait_vm<SINCE(WOC)>();
                asm volatile("" : "+v"(cx1)::"memory");
                if (!__all(cx1[0] == T[0] && cx1[1] == T[1])) poll_flags(rs, (l & 15) * 256, F_WOI + ct * 4096, T);
              }
            }
            if constexpr (s >= 29 && (n & 1)) {
              if constexpr (s - 24 < IMT) keep[3 * (s - 29) + (n >> 1)] = frag0(SLOT(s - 24), 3 * w + (n >> 1));
              else keep[3 * (s - 29) + (n >> 1)] = *(const LDS_AS u32x4*)(size_t)(bf_[0] + SLOT(s - 24) * STAGE + w * 3072 + (n >> 1) * 1024);
            }
            if constexpr (s == (TS0 < TEX ? TS0 : TEX) - 2 && n == 1) {
              ARGP(pwg, 5); ARGP(pwu, 6);
              tsrc = (w < 4 ? pwg : pwu) + (bi * 32 + (w & 3) * 8 + (l >> 3)) * F_ + xcd * 448 + min(l & 7, 6) * 64;
              asm volatile("" : "+v"(tsrc));
            }
            if constexpr (s >= TS0 && s < TS0 + TSN + TSD * (7 >> TSG) && n == 4) {
              if (tstep == s && ((TM0 >> w) & 1)) TOUCH(tsrc);
            }
            if constexpr (s == TEX && n == 5) {
              const GAS bf16* t2 = ((TM1 >> w) & 1) ? tsrc : (const GAS bf16*)x + p * 262144;
              TOUCH(t2);
            }
          } else {
            if constexpr (s >= 27 && s < 30 && (n == 0 || n == 3)) WO_PIECE(4 + 2 * (s - 27) + n / 3);
            if constexpr (s == FKL && n == 4) { asm volatile("" : "=v"(kv)); if (w == 0) ld4h<0>(kv, rs, (l & 15) * SKEY, KEY_AT(0, KREP())); }
            if constexpr (s == FSL && n == 1) {
              asm volatile("" : "=v"(fx[0]), "=v"(fx[1]));
              if (w == 0) {
                wait_vm<SINCE(FKL)>();
                asm volatile("" : "+v"(kv)::"memory");
                fkey = __builtin_amdgcn_readfirstlane(umax16(kv == POISON ? 0u : kv));
                ld4h<0>(kv, rs, (l & 15) * SKEY, KEY_AT(0, KREP()));
                ld16h<0>(fx[0], rs, (l >> 3) * 256 + (l & 7) * 16, cand_off(fkey, 0)); ld16h<2048>(fx[1], rs, (l >> 3) * 256 + (l & 7) * 16, cand_off(fkey, 0));
              }
            }
            if constexpr (s == FS0 - 1 && n == FSN) {
              if (w == 0) {
                wait_vm<vm_at(1, s, n) - vm_at(1, FSL, 2)>();
                asm volatile("" : "+v"(fx[0]), "+v"(fx[1]), "+v"(kv)::"memory");
                const unsigned k_ = key_wait(rs, T, l, 0, KREP(), kv);
                if (l == 0) *(LDS_AS unsigned*)(smem + L_SKEY + 32) = k_;
                if (k_ != fkey) fx[0][0] = POISON;
                word_wait<2>(rs, T, (l >> 3) * 256 + (l & 7) * 16, cand_off(k_, 0), fx);
                *(LDS_AS u32x4*)(smem + L_ROW + l * 16) = fx[0]; *(LDS_AS u32x4*)(smem + L_ROW + 1024 + l * 16) = fx[1];
                count_in(0);
              }
            }
            if constexpr (s == FS0 - 1 && n == 1) {
              asm volatile("" : "=v"(dfr[0]), "=v"(dfr[1]), "=v"(dfr[2]), "=v"(dfr[3]), "=v"(dfr[4]), "=v"(dfr[5]), "=v"(dfr[6]), "=v"(dfr[7]));
              if (ffn_on) {
#pragma unroll
                for (int i = 0; i < 8; ++i) dfr[i] = trread(smem, L_DN + w * 4096 + g * 1024 + ((l & 15) >> 2) * 256 + ((i ^ ((l >> 2) & 7)) * 32) + (l & 3) * 8);
              }
            }
            if constexpr (s == FS0) {
              if constexpr (n == 0) qc = __hip_atomic_load(qcnt, __ATOMIC_RELAXED, __HIP_MEMORY_SCOPE_WORKGROUP);
              if constexpr (n == 1) {
                count_wait(0, 1);
                kv = *(const LDS_AS unsigned*)(smem + L_SKEY + 32);
#pragma unroll
                for (int i = 0; i < 4; ++i) fx[i] = *(const LDS_AS u32x4*)(smem + L_ROW + w * 256 + i * 64 + g * 16);
              }
              if constexpr (n == 2) {
                fkey = __builtin_amdgcn_readfirstlane(kv);
                sel0 = key_row(fkey);
                aff = (int)(sel0 >> 8) == p;
                asm volatile("" : "=v"(sa), "=v"(sb));
                if (ffn_on) ffn_gu(gv, uv, fx, sa, sb);
              }
              if constexpr (n == 4) {
                if (ffn_on) {
                  ffn_gu_store(smem, l, w, sa, sb);
                  count_in(1);
                }
              }
            }
            if constexpr (s == FS0D) {
              if constexpr (n == 0) qc = __hip_atomic_load(qcnt + 1, __ATOMIC_RELAXED, __HIP_MEMORY_SCOPE_WORKGROUP);
              if constexpr (n == 1) { asm volatile("" : "=v"(sa)); if (ffn_on) { count_wait(1, 8); sa = ffn_d_read(smem, l); } }
              if constexpr (n == 2) { asm volatile("" : "=v"(hh)); if (ffn_on) hh = ffn_d_h(sa); }
              if constexpr (n == 4) { asm volatile("" : "=v"(sa)); if (ffn_on) sa = ffn_d_all(dfr, hh); }
              if constexpr (n == 5) { if (ffn_on) ffn_d_store(rs, l, w, 0, slice, sa); }
            }
            if constexpr (s == FS1 && n == 4) {
              DEF4(fx);
              asm volatile("" : "=v"(cxn), "=v"(cx1));
              if (!ffn_on) {
                const unsigned G = xcd * 4 + bi - 28;
#pragma unroll
                for (int k = 0; k < 4; ++k) ld16h<0>(fx[k], rs, RED_VO(k), RED_SO(0, G));
                ld8h(cxn, rs, RED_JJ(), cand_off(fkey, 0) + XO(G)); ld8h(cx1, rs, RED_JJ() + 128, cand_off(fkey, 0) + XO(G));
              }
            }
            if constexpr (s == FS1S && n == 2) {
              if (!ffn_on) {
                wait_vm<SINCE(FS1)>();
                PINV4(fx);
                asm volatile("" : "+v"(cxn), "+v"(cx1)::"memory");
                red_sum(rs, smem, T, tid, w, 0, xcd * 4 + bi - 28, fkey, fx, cxn, cx1);
                BAR();
                if (w == 0) {
                  const u32x2 ov = red_final(smem, l, red_gate(fkey), cxn, cx1);
                  if (l < 8) st8(unpoison2(ov), rs, OFF_XNEW + (xcd * 4 + bi - 28) * 256 + ((l & 3) * 2 + (l >> 2)) * 8, 0);
                }
              }
            }
            if constexpr (s == FXA && n == 4) { asm volatile("" : "=v"(fx[0])); if (w == 1) ld16h<0>(fx[0], rs, ((l >> 2) & 3) * 256 + (l & 3) * 16, XP_OFF()); }
            if constexpr (s == FXB - 1 && n == FSN) {
              if (w == 1) {
                wait_vm<SINCE(FXA)>();
                asm volatile("" : "+v"(fx[0])::"memory");
                word_wait<1>(rs, T, ((l >> 2) & 3) * 256 + (l & 3) * 16, XP_OFF(), fx);
                if (l < 16) *(LDS_AS u32x4*)(smem + L_ROW + l * 16) = fx[0];
                count_in(0);
              }
            }
            if constexpr (s == FXB) {
#define FX_FRAG(i) (*(const LDS_AS u32x4*)(size_t)(bo[0][0] + STAGE + (6 * (i) + w) * 1024))
              if constexpr (n == 0) { qc = __hip_atomic_load(qcnt, __ATOMIC_RELAXED, __HIP_MEMORY_SCOPE_WORKGROUP); }
              if constexpr (n == 1) {
                DEF4(fx); DEF4(df);
                if (w < 6) {
                  count_wait(0, 2);
#pragma unroll
                  for (int i = 0; i < 4; ++i) { fx[i] = *(const LDS_AS u32x4*)(smem + L_ROW + i * 64 + g * 16); df[i] = FX_FRAG(i); }
                }
              }
              if constexpr (n == 2) { asm volatile("" : "=v"(sa), "=v"(sb)); if (w < 6) dot_mma(fx); }
              if constexpr (n == 4) { if (w < 6) dot_store(OFF_FX); }
            }
            if constexpr (s == FXL && n == 4) {
              asm volatile("" : "=v"(fx[0]), "=v"(fx[1]));
              if (aff && w < 4) { ld16h<0>(fx[0], rs, FP_VO(), FP_OFF(0)); ld16h<0>(fx[1], rs, FP_VO(), FP_OFF(1)); }
            }
            if constexpr (s == FXS && n == 4) {
              if (aff && w < 4) {
                wait_vm<0>();
                asm volatile("" : "+v"(fx[0]), "+v"(fx[1])::"memory");
                if (!FX_OK(2)) {
                  unsigned spins = 0;
                  do {
                    if (spin_fail(spins, rs, T)) break;
                    fx[0] = ld16(rs, FP_VO(), FP_OFF(0)); fx[1] = ld16(rs, FP_VO(), FP_OFF(1));
                  } while (!FX_OK(2));
                }
                if (w == 3) {
                  f32x2 nx[8];
                  unpack8v(fx[0], nx); unpack8v(fx[1], nx + 4);
                  const f32x2 sm = ((nx[0] + nx[1]) + (nx[2] + nx[3])) + ((nx[4] + nx[5]) + (nx[6] + nx[7]));
                  const float mean = sum64(hadd(sm)) * (1.0f / 1024.0f);
                  const f32x2 m2 = {mean, mean};
                  f32x2 q = {0.f, 0.f};
#pragma unroll
                  for (int j = 0; j < 8; ++j) { const f32x2 d = nx[j] - m2; q = __builtin_elementwise_fma(d, d, q); }
                  const float rd = ln_rd(sum64(hadd(q)) * (1.0f / 1024.0f));
                  if (l == 0) *(LDS_AS f32x2*)(smem + L_FXL + 384) = f32x2{rd, -rbf1(mean)};
                } else {
                  tile_sum(fx[0], L_FXL);
                }
                count_in(3);
              }
            }
          }
        };
        static_for<32>([&](auto sc_) __attribute__((always_inline)) {
          constexpr int s = decltype(sc_)::value;
          if constexpr (s > 0) PIN_ACC();
          if constexpr (IT == 1) wait_vm<vm_at(IT, s, 0) - vm_a(IT, s)>();
          pin_w<s % WA>(af[s % WA][0], af[s % WA][1]);
          SB();
          static_for<6>([&](auto nc_) __attribute__((always_inline)) {
            constexpr int n = decltype(nc_)::value;
            if constexpr (s == 0) { acc[0][n] = f32x4{0.f, 0.f, 0.f, 0.f}; acc[1][n] = acc[0][n]; }
            constexpr bool on = s < QL || n >= 2;
            if constexpr (on) acc[0][n] = n < 4 ? mfma16(bfr[n].v, af[s % WA][0], acc[0][n]) : mfma16(af[s % WA][0], bfr[n].v, acc[0][n]);
            SB();
            if constexpr (n == 0) { QKV_RDB(s, 5); }
            else if constexpr (s + 1 < 32 && (s + 1 < QL || n > 2)) QKV_RDB(s + 1, n - 1);
            SB();
            if constexpr (on) acc[1][n] = n < 4 ? mfma16(bfr[n].v, af[s % WA][1], acc[1][n]) : mfma16(af[s % WA][1], bfr[n].v, acc[1][n]);
            SB();
            if constexpr (s + 1 < 32 && (s + 1 < QL || n >= 2)) QKV_RDA(s + 1, n);
            SB();
            gap(sc_, nc_);
            SB();
          });
        });
        PIN_ACC();
        frag_t qw[32 - QL][2];
        static_for<32 - QL>([&](auto c_) __attribute__((always_inline)) {
          constexpr int s = QL + decltype(c_)::value;
          QKV_RDA(s, 0); QKV_RDB(s, 0); QKV_RDA(s, 1); QKV_RDB(s, 1);
          qw[s - QL][0] = bfr[0]; qw[s - QL][1] = bfr[1];
        });
        SB();
        const bool own = aff && (int)((sel0 >> 5) & 7) == rt;
        const bool m0 = !(sel0 & 16), mq = (l & 15) == (int)(sel0 & 15), mv = g == (int)((sel0 & 15) >> 2);
        auto patch_qk = [&](int n) __attribute__((always_inline)) {
          f32x4 t = *(const LDS_AS f32x4*)(smem + L_FXL + n * 64 + g * 16);
          const f32x4 c = *(const LDS_AS f32x4*)(smem + L_CSL + n * 64 + g * 16);
          const float nb = *(const LDS_AS float*)(smem + L_FXL + 388);
#pragma unroll
          for (int e = 0; e < 4; ++e) { t[e] = __builtin_fmaf(c[e], nb, t[e]); asm volatile("" : "+v"(t[e])); }
          acc[0][n] = (mq && m0) ? t : acc[0][n];
          acc[1][n] = (mq && !m0) ? t : acc[1][n];
        };
        if (own) {
          while (__hip_atomic_load(qcnt + 3, __ATOMIC_RELAXED, __HIP_MEMORY_SCOPE_WORKGROUP) < 4u) {}
          asm volatile("" ::: "memory");
          patch_qk(2); patch_qk(3);
          const f32x2 nab = *(const LDS_AS f32x2*)(smem + L_FXL + 384);
          const LDS_AS float* fl_ = (const LDS_AS float*)(smem + L_FXL + 256) + (l & 15);
          const LDS_AS float* cl_ = (const LDS_AS float*)(smem + L_CSL + 256) + (l & 15);
          const float t4 = __builtin_fmaf(cl_[0], nab[1], fl_[0]), t5 = __builtin_fmaf(cl_[16], nab[1], fl_[16]);
          const unsigned na = __builtin_bit_cast(unsigned, (float)nab[0]);
#pragma unroll
          for (int a = 0; a < 2; ++a) {
            const bool ma = a ? !m0 : m0;
            aq[a] = (mq && ma) ? na : aq[a];
#pragma unroll
            for (int e = 0; e < 4; ++e) {
              const bool me = mv && ma && e == (int)(sel0 & 3);
              acc[a][4][e] = me ? t4 : acc[a][4][e]; acc[a][5][e] = me ? t5 : acc[a][5][e];
              av[a][e] = me ? na : av[a][e];
            }
          }
        }
        constexpr float C2 = 1.4426950408889634f * 0.17677669529663687f;
#pragma unroll
        for (int a = 0; a < 2; ++a) {
          f32x2 a2 = {asf(aq[a]), asf(aq[a])};
          qs2[a] = f32x2{asf(aq[a]) * C2, asf(aq[a]) * C2};
          asm volatile("" : "+v"(a2), "+v"(qs2[a]));
          acc[a][2] *= f32x4{a2[0], a2[1], a2[0], a2[1]}; acc[a][3] *= f32x4{a2[0], a2[1], a2[0], a2[1]};
          acc[a][4] *= __builtin_bit_cast(f32x4, av[a]); acc[a][5] *= __builtin_bit_cast(f32x4, av[a]);
        }
        k0 = pack8(acc[0][2], acc[0][3]); k1 = pack8(acc[1][2], acc[1][3]); v0 = pack8(acc[0][4], acc[1][4]); v1 = pack8(acc[0][5], acc[1][5]);
        const unsigned kvo = OFF_KV + IT * (8u << 20) + (p * 32 + h) * 32768 + rt * 4096;
        if (p < 7) { st16(k0, rs, l * 16, kvo); st16(k1, rs, l * 16 + 1024, kvo); st16(v0, rs, l * 16 + 2048, kvo); st16(v1, rs, l * 16 + 3072, kvo); }
        char* ownp = smem + (8 + rt) * 2048 + l * 16;
        *(u32x4*)(ownp + L_K) = k0;
        if (rt < 7) *(u32x4*)(ownp + L_K + 1024) = k1;
        *(u32x4*)(ownp + L_V) = v0; *(u32x4*)(ownp + L_V + 1024) = v1;
        SB();
        static_for<32 - QL>([&](auto c_) __attribute__((always_inline)) {
          constexpr int s = QL + decltype(c_)::value;
#pragma unroll
          for (int n = 0; n < 2; ++n) { acc[0][n] = mfma16(qw[s - QL][n].v, af[s % WA][0], acc[0][n]); acc[1][n] = mfma16(qw[s - QL][n].v, af[s % WA][1], acc[1][n]); }
        });
        if (own) { patch_qk(0); patch_qk(1); }
      };
      qkv_loop(it_c);
    }
#undef A_LOAD1
#undef QKV_RDA
#undef QKV_RDB
#undef QKV_BO
    {
      IDS();
      const GAS bf16 *pwg, *pwu;
      asm volatile("s_load_dwordx2 %0, %2, 40\n\ts_load_dwordx2 %1, %2, 48" : "=&s"(pwg), "=&s"(pwu) : "s"(ka));
      const int ql = l & 15;
      const f32x4 z4 = {0.f, 0.f, 0.f, 0.f}, ninf = {-INFINITY, -INFINITY, -INFINITY, -INFINITY};
      LDS_AS unsigned* rdy = (LDS_AS unsigned*)(smem + L_RDY);
      const unsigned rtag = T[0] + it;
      u32x4 qf[2];
      f32x4 cn[2], oa[2][2], sum[2], S[2][2];
      bool told = false;
      auto announce = [&](bool now) __attribute__((always_inline)) {
        if (!told) {
          unsigned st;
          asm volatile("s_getreg_b32 %0, hwreg(HW_REG_IB_STS)" : "=s"(st)::"memory");
          if (now || !(st & 0xC0000Fu)) {
            DRAIN();
            if (l == 0) st8(T, rs, rt * SKW, F_KV + (it * 256 + p * 32 + h) * SKV);
            told = true;
          }
        }
      };
      unsigned one2 = 0x3F803F80u;
      asm volatile("" : "+v"(one2));
      const u32x4 ones = {one2, one2, one2, one2};
      auto ex8 = [&](f32x4 a, f32x4 b) __attribute__((always_inline)) {
        return u32x4{pack2(__builtin_amdgcn_exp2f(a[0]), __builtin_amdgcn_exp2f(a[1])), pack2(__builtin_amdgcn_exp2f(a[2]), __builtin_amdgcn_exp2f(a[3])),
                     pack2(__builtin_amdgcn_exp2f(b[0]), __builtin_amdgcn_exp2f(b[1])), pack2(__builtin_amdgcn_exp2f(b[2]), __builtin_amdgcn_exp2f(b[3]))};
      };
      {
#pragma unroll
        for (int a = 0; a < 2; ++a) {
          const f32x4 c4 = {qs2[a][0], qs2[a][1], qs2[a][0], qs2[a][1]};
          qf[a] = pack8(acc[a][0] * c4, acc[a][1] * c4);
        }
        f32x4 mk;
#pragma unroll
        for (int e = 0; e < 4; ++e) mk[e] = (4 * g + e <= ql) ? 0.f : -INFINITY;
        const f32x4 d0 = mfma16(k0, qf[0], mk), d1 = mfma16(k1, qf[1], mk), d2 = mfma16(k0, qf[1], z4);
        const float m0 = -max_x32(max_x16(fmaxf(fmaxf(d0[0], d0[1]), fmaxf(d0[2], d0[3]))));
        const float m1 = -max_x32(max_x16(fmaxf(fmaxf(d1[0], d1[1]), fmaxf(d1[2], d1[3]))));
        cn[0] = f32x4{m0, m0, m0, m0}; cn[1] = f32x4{m1, m1, m1, m1};
        asm volatile("" : "+v"(cn[0]), "+v"(cn[1]));
        announce(false);
        const f32x4 e0 = d0 + cn[0];
        const u32x4 p0 = {pack2(__builtin_amdgcn_exp2f(e0[0]), __builtin_amdgcn_exp2f(e0[1])), pack2(__builtin_amdgcn_exp2f(e0[2]), __builtin_amdgcn_exp2f(e0[3])), 0u, 0u};
        const u32x4 p1 = ex8(d2 + cn[1], d1 + cn[1]);
        oa[0][0] = mfma16(v0, p0, z4); oa[0][1] = mfma16(v1, p0, z4); sum[0] = mfma16(ones, p0, z4);
        oa[1][0] = mfma16(v0, p1, z4); oa[1][1] = mfma16(v1, p1, z4); sum[1] = mfma16(ones, p1, z4);
      }
      const char* kb_ = smem + L_K + l * 16;
      const char* vb_ = smem + L_V + l * 16;
      const unsigned fnv = (l >> 1) * (F_ * 2) + (l & 1) * 16;
      asm volatile("" : "+v"(oa[0][0]), "+v"(oa[0][1]), "+v"(oa[1][0]), "+v"(oa[1][1]), "+v"(sum[0]), "+v"(sum[1]));
      const unsigned pw = __builtin_amdgcn_readfirstlane(it * 256 + p * 32 + h - 32), pfo = F_KV + pw * SKV + rt * SKW;
      const bool fetcher = rt < 4 && p > 0;
      announce(false);
      BAR();
      asm volatile("" : "+s"(pwg), "+s"(pwu));
#define KV_FETCH(gd)                                                                                                          \
  {                                                                                                                           \
    const unsigned pvo = OFF_KV + pw * 32768 + (gd) * 4096;                                                                   \
    dma16s<0>(rs, l * 16, pvo, lbase + L_K + (gd) * 2048); dma16s<1024>(rs, l * 16, pvo, lbase + L_K + (gd) * 2048);           \
    dma16s<0>(rs, l * 16, pvo + 2048, lbase + L_V + (gd) * 2048); dma16s<1024>(rs, l * 16, pvo + 2048, lbase + L_V + (gd) * 2048); \
  }
      auto fetch = [&]() __attribute__((always_inline)) {
        if (PSK) __builtin_amdgcn_s_sleep(PSK);
        poll_flags(rs, (l & 1) * 4 * SKW, pfo, T);
        KV_FETCH(rt); KV_FETCH(rt + 4);
        if (l == 0) rdy[8 + rt] = rtag;
      };
      announce(false);
      const bool first = fetcher && told;
      if (first) fetch();
#define LDS16(a_) (*(const LDS_AS u32x4*)(size_t)(a_))
      u32x4 kf[2], vf[2], p0, p1;
#define AE(a, i) { S[a][(i) >> 2][(i) & 3] = __builtin_amdgcn_exp2f(S[a][(i) >> 2][(i) & 3]); asm volatile("" : "+v"(S[a][(i) >> 2][(i) & 3])); SB(); }
#define AC(a, P, j) { P[j] = pack2(S[a][(j) >> 1][2 * ((j) & 1)], S[a][(j) >> 1][2 * ((j) & 1) + 1]); asm volatile("" : "+v"(P[j])); SB(); }
#define PHASE(a, P, M1, M2, L2, M3, M4, L4, M5)                                                                               \
  AE(a, 0) AE(a, 1) M1; SB(); AE(a, 2) AE(a, 3) M2; SB(); L2; SB(); AC(a, P, 0) AC(a, P, 1) AE(a, 4) M3; SB(); AE(a, 5) AE(a, 6) M4; SB(); \
  L4; SB(); AE(a, 7) AC(a, P, 2) M5; SB(); AC(a, P, 3)
#define PV(a, d, P) oa[a][d] = mfma16(vf[d], P, oa[a][d])
#define RS(a, P) sum[a] = mfma16(ones, P, sum[a])
#define SC(a, t, c) S[a][t] = mfma16(kf[t], qf[a], c)
#define K_AT(ad) (kf[0] = LDS16(ad), kf[1] = LDS16((ad) + 1024))
#define V_AT(ad) (vf[0] = LDS16((ad) + L_V), vf[1] = LDS16((ad) + L_V + 1024))
#define A_FIRST(ad, c00, c01, c10, c11, KN) { K_AT(ad); V_AT(ad); SB(); SC(0, 0, c00); SC(0, 1, c01); SB(); PHASE(0, p0, SC(1, 0, c10), SC(1, 1, c11), KN, , , , ) }
#define A_NEXT(ad, c00, c01, c10, c11, KN)                                                                                        \
  { PHASE(1, p1, PV(0, 0, p0), PV(0, 1, p0), , SC(0, 0, c00), SC(0, 1, c01), , RS(0, p0))                                     \
    PHASE(0, p0, PV(1, 0, p1), PV(1, 1, p1), V_AT(ad), SC(1, 0, c10), SC(1, 1, c11), KN, RS(1, p1)) }
#define A_LAST() { PHASE(1, p1, PV(0, 0, p0), PV(0, 1, p0), , RS(0, p0), , , ) SB(); PV(1, 0, p1); PV(1, 1, p1); RS(1, p1); }
      int nw = 0;
      auto piece = [&]() __attribute__((always_inline)) {
        if constexpr (it == 0) { if (ffn_on && nw < 8) { FFN_DMA(nw); } ++nw; }
      };
#define XN_OFF() (F_XN + (((rt - 6) * 4 + g) * RX + bi % RX) * 256 + (l & 15) * 16)
      u32x4 fxn;
      asm volatile("" : "=v"(fxn));
      unsigned ad = lbase + l * 16 + (6 + rt) * 2048;
      asm volatile("" : "+v"(ad));
      if (rt > 0) {
        A_FIRST(ad + 2048, cn[0], cn[0], cn[1], cn[1], K_AT(ad));
#pragma unroll 1
        for (int i = rt - 1; i > 0; --i) {
          ad -= 2048;
          asm volatile("" : "+v"(ad));
          if (told && rt >= 4) piece();
          announce(false);
          A_NEXT(ad + 2048, cn[0], cn[0], cn[1], cn[1], K_AT(ad));
        }
        announce(false);
        A_LAST();
      }
      announce(true);
      {
        f32x4 mk;
#pragma unroll
        for (int e = 0; e < 4; ++e) mk[e] = (4 * g + e > ql) ? 0.f : -INFINITY;
        f32x4 cm0 = cn[0] + mk, cm1 = cn[1] + mk, cmi = ninf;
        asm volatile("" : "+v"(cm0), "+v"(cm1), "+v"(cmi));
        if (rt >= 4) {
          if (p > 0) {
            while (!__all(__hip_atomic_load(rdy + 8 + (l & 3), __ATOMIC_RELAXED, __HIP_MEMORY_SCOPE_WORKGROUP) == rtag)) __builtin_amdgcn_s_sleep(1);
            asm volatile("" ::: "memory");
          }
          if constexpr (it == 0) { for (; nw < 8;) piece(); }
          static_for<it ? 12 : 8>([&](auto c_) __attribute__((always_inline)) { constexpr int i = decltype(c_)::value; WO_PIECE_W(15 - i / 2, 2 * (rt & 3) + (i & 1)); });
          if (it == 0 && rt >= 6) ld16h<0>(fxn, rs, XN_OFF(), 0);
        } else if (p > 0) {
          if (!first) fetch();
          DRAIN();
          if (l < 2) rdy[rt + 4 * l] = rtag;
          if constexpr (it == 0) { for (; nw <= rt;) piece(); }
        }
        if (p > 0) {
          unsigned have = 0;
          auto need = [&](int gq) __attribute__((always_inline)) {
            if (__builtin_expect(gq < 8 && !((have >> gq) & 1), 0)) {
              do {
                const unsigned t = __hip_atomic_load(rdy + (l & 7), __ATOMIC_RELAXED, __HIP_MEMORY_SCOPE_WORKGROUP);
                have = (unsigned)__builtin_amdgcn_ballot_w64(t == rtag);
              } while (!((have >> gq) & 1));
              asm volatile("" ::: "memory");
            }
          };
          ad = lbase + l * 16 + rt * 2048;
          asm volatile("" : "+v"(ad));
          need(rt);
          K_AT(ad); V_AT(ad); SB(); SC(0, 0, cm0); SC(0, 1, cn[0]); SB();
          PHASE(0, p0, SC(1, 0, cmi), SC(1, 1, cm1), , , , , )
          need(rt + 1);
          K_AT(ad + 2048);
#pragma unroll 1
          for (int gq = rt + 1; gq < 8; ++gq) {
            ad += 2048;
            asm volatile("" : "+v"(ad));
            piece();
            need(gq + 1);
            A_NEXT(ad, cn[0], cn[0], cn[1], cn[1], K_AT(ad + 2048));
          }
          A_LAST();
        }
        if constexpr (it == 0) { for (; nw < 8;) piece(); }
      }
      auto o_store = [&]() __attribute__((always_inline)) {
        const unsigned oo = OFF_O + it * (4u << 20) + ((p * 8 + rt) * 64 + h * 2) * 1024;
#pragma unroll
        for (int a = 0; a < 2; ++a) {
          f32x2 inv = {__builtin_amdgcn_rcpf(sum[a][0]), 0.f};
          inv[1] = inv[0];
          asm volatile("" : "+v"(inv));
          const f32x4 i4 = {inv[0], inv[1], inv[0], inv[1]};
          st16(pack8(oa[a][0] * i4, oa[a][1] * i4), rs, l * 16 + a * 1024, oo);
        }
      };
      {
        float big = 0.f;
#pragma unroll
        for (int a = 0; a < 2; ++a)
#pragma unroll
          for (int d = 0; d < 2; ++d)
            big = __builtin_elementwise_maximum(__builtin_elementwise_maximum(big, __builtin_elementwise_maximum(fabsf(oa[a][d][0]), fabsf(oa[a][d][1]))),
                                                __builtin_elementwise_maximum(fabsf(oa[a][d][2]), fabsf(oa[a][d][3])));
        bool ok = sum[0][0] >= 0.5f && sum[0][0] < 0x1p64f && sum[1][0] >= 0.5f && sum[1][0] < 0x1p64f && big < INFINITY;
        if (!__all(ok)) {
#pragma unroll
          for (int a = 0; a < 2; ++a) {
            const int qs = 2 * rt + a, qq = 256 + 16 * qs + ql, klo = max(qq - 255, p > 0 ? 0 : 256);
            auto score = [&](int t) __attribute__((always_inline)) {
              f32x4 s = mfma16(t == 31 ? k1 : *(const u32x4*)(kb_ + t * 1024), qf[a], z4);
#pragma unroll
              for (int e = 0; e < 4; ++e) { const int kk = 16 * t + 4 * g + e; s[e] = (kk <= qq && kk >= klo) ? s[e] : -INFINITY; }
              return s;
            };
            float mx = -INFINITY;
#pragma unroll 1
            for (int t = p > 0 ? qs : 16; t <= qs + 16; ++t) { const f32x4 s = score(t); mx = fmaxf(fmaxf(mx, fmaxf(s[0], s[1])), fmaxf(s[2], s[3])); }
            mx = max_x32(max_x16(mx));
            oa[a][0] = z4; oa[a][1] = z4; sum[a] = z4;
#pragma unroll 1
            for (int G = p > 0 ? qs >> 1 : 8; G <= (qs + 16) >> 1; ++G) {
              const f32x4 s0 = score(2 * G), s1 = score(2 * G + 1);
              f32x4 e0, e1;
#pragma unroll
              for (int e = 0; e < 4; ++e) { e0[e] = __builtin_amdgcn_exp2f(s0[e] - mx); e1[e] = __builtin_amdgcn_exp2f(s1[e] - mx); }
              const u32x4 pf = pack8(e0, e1);
              oa[a][0] = mfma16(*(const u32x4*)(vb_ + G * 2048), pf, oa[a][0]);
              oa[a][1] = mfma16(*(const u32x4*)(vb_ + G * 2048 + 1024), pf, oa[a][1]);
              sum[a] = mfma16(ones, pf, sum[a]);
            }
          }
        }
      }
      o_store();
      if constexpr (it == 0) {
        wait_vm<2>();
        asm volatile("" : "+v"(fxn)::"memory");
        if (rt >= 6 && !__all(fxn[0] == T[0] && fxn[1] == T[1] && fxn[2] == T[0] && fxn[3] == T[1])) poll_flags2(rs, XN_OFF(), T);
        DEF4(gv); DEF4(uv);
        if (ffn_on) {
          const unsigned ro = 65536 + w * 1024 + (4 * g + ((l & 15) >> 2)) * 32 + (l & 3) * 8;
#pragma unroll
          for (int i = 0; i < 4; ++i) {
            frag_t f;
            f.hf.a = trread(smem, ro + i * 16384); f.hf.b = trread(smem, ro + i * 16384 + 512);
            gv[i] = f.v;
            f.hf.a = trread(smem, ro + i * 16384 + 8192); f.hf.b = trread(smem, ro + i * 16384 + 8704);
            uv[i] = f.v;
          }
        }
        PINV4(gv); PINV4(uv);
      }
      BAR();
      if constexpr (it == 1) { WO_PIECE(3); WO_PIECE(2); WO_PIECE(1); WO_PIECE(0); }
      wait_vm<it ? 4 : 0>();
      asm volatile("s_barrier" ::: "memory");
      if (tid == 0) st8(T, rs, h * SO, F_O + (it * 8 + p) * 32 * SO);
      if constexpr (it == 0) {
        WO_PIECE(11); WO_PIECE(10); WO_PIECE(9); WO_PIECE(8); WO_PIECE(7); WO_PIECE(6); WO_PIECE(5); WO_PIECE(4);
        WO_PIECE(3); WO_PIECE(2); WO_PIECE(1); WO_PIECE(0);
      }
    }
    IDS();
    ARGP(pwr, 4); ARGP(pwd, 7); ARGP(pout, 8); ARGP(px, 0);
    const int kh = w >> 2, rt2 = rb * 4 + (w & 3);
    const unsigned R = rt2 * 32 + kh * 16 + (l & 15);
    const unsigned to = (rt2 * 64 + ct * 4 + kh) * 1024 + l * 16;
    const unsigned rmo = ct * 64 + ((g & 1) ? 12 + 4 * g : 4 * g);
    f32x2 wv[8];
    {
      u32x4 wa[2], wrp[2], res[2];
#pragma unroll
      for (int j = 0; j < 2; ++j) {
        wrp[j] = *(const GAS u32x4*)(pwr + rmo + 32 * j);
        if constexpr (it == 0) wa[j] = *(const GAS u32x4*)(pwr + l * 8 + j * 512);
        if constexpr (it == 0) res[j] = *(const GAS u32x4*)(px + (R - (l & 15) + 8 * j + (l >> 3)) * 1024 + ct * 64 + (l & 7) * 8);
        else res[j] = ld16(rs, OFF_XNEW + (2 * ct + j) * 256 + g * 16, 0);
      }
      SB();
      if ((it ? PSO : PSO0) && w == 0) __builtin_amdgcn_s_sleep(it ? PSO : PSO0);
      if (w == 0) poll_flags(rs, (l & 31) * SO, F_O + (it * 8 + p) * 32 * SO, T);
      asm volatile("s_barrier" ::: "memory");
      auto prep = [&]() __attribute__((always_inline)) {
        if constexpr (it == 0) {
          f32x2 t[8];
          unpack8v(wa[0], t); unpack8v(wa[1], t + 4);
          const f32x2 s = ((t[0] + t[1]) + (t[2] + t[3])) + ((t[4] + t[5]) + (t[6] + t[7]));
          wsum = sum64(hadd(s));
        }
        unpack8v(trade16(wrp[0]), wv); unpack8v(trade16(wrp[1]), wv + 4);
        if constexpr (it == 0) {
          char* tp = smem + w * 2048;
          const unsigned c0 = (g & 1) * 2 + (g >> 1), r = l & 15;
#pragma unroll
          for (int j = 0; j < 2; ++j) *(u32x4*)(tp + j * 1024 + (l >> 3) * 128 + (((l ^ (l >> 3)) & 7) << 4)) = res[j];
          res[0] = trade16(*(const u32x4*)(tp + r * 128 + ((c0 ^ (r & 7)) << 4)));
          res[1] = trade16(*(const u32x4*)(tp + r * 128 + (((c0 + 4) ^ (r & 7)) << 4)));
        }
        else { res[0] = R == sel0 ? res[0] : x2p[0]; res[1] = R == sel0 ? res[1] : x2p[1]; }
        unpack8v(res[0], v + 0); unpack8v(res[1], v + 4);
        asm volatile("" : "+v"(v[0]), "+v"(v[1]), "+v"(v[2]), "+v"(v[3]), "+v"(v[4]), "+v"(v[5]), "+v"(v[6]), "+v"(v[7]));
        asm volatile("" : "+v"(wsum), "+v"(wv[0]), "+v"(wv[1]), "+v"(wv[2]), "+v"(wv[3]), "+v"(wv[4]), "+v"(wv[5]), "+v"(wv[6]), "+v"(wv[7]));
      };
      {
        const unsigned abase = OFF_O + it * (4u << 20) + (rt2 * 64 + kh) * 1024;
        u32x4 af[W2B], bfr[W2L + 1][4];
        unsigned b2[2] = {lbase + 32768 + l * 16, lbase + 98304 + l * 16};
        asm volatile("" : "+v"(b2[0]), "+v"(b2[1]));
#define O_LOAD(s_) af[(s_) % W2B] = ld16(rs, l * 16 + (~(s_) & 1) * 2048, abase + ((31 - (s_)) >> 1) * 4096)
#define WO_RD(s_, n) bfr[(s_) % (W2L + 1)][n] = *(const LDS_AS u32x4*)(size_t)(b2[(31 - (s_)) >> 4] + ((31 - (s_)) & 15) * 4096 + (n) * 1024)
#pragma unroll
        for (int s = 0; s < W2R; ++s) O_LOAD(s);
        SB();
        prep();
        SB();
        f32x4 ac[4];
#pragma unroll
        for (int n = 0; n < 4; ++n) ac[n] = f32x4{v[2 * n][0], v[2 * n][1], v[2 * n + 1][0], v[2 * n + 1][1]};
#pragma unroll
        for (int n = 0; n < 4 * W2L; ++n) WO_RD(n >> 2, n & 3);
#pragma unroll
        for (int s = 0; s < 32; ++s) {
          if (s == W2P && w >= 4) __builtin_amdgcn_s_setprio(1);
          if (s == (it ? 24 : 8) - W2L) asm volatile("s_barrier" ::: "memory");
        if (it == 0 && s == WPS) {
          unsigned pz1 = POISON;
          asm volatile("" : "+v"(pz1));
          const u32x4 pz = {pz1, pz1, pz1, pz1};
          st16(pz, rs, OFF_PART + (tid >> 8) * (1u << 20) + bid * 4096 + (tid & 255) * 16, 0);
          if (w < 2) st16(pz, rs, OFF_STAT + 524288 + (rb * 16 + ct) * 2048 + tid * 16, 0);
          if (w == 4 && l < 40) st16(pz, rs, l < 16 ? OFF_CA + 65536 + rb * 4096 + ct * 256 + l * 16 : OFF_FX + (h * 8 + p) * 512 + (l - 16) * 16, 0);
          if (w == 6 && l < (ct < (int)RKEY ? 3 : 2)) st4(pz1, rs, l < 2 ? OFF_XNEW + (bid >> 3) * 256 + (bid & 7) * 8 + l * 4 : KEY_AT(1, ct) + rb * SKEY, 0);
        }
          if (s < W2B - W2R) O_LOAD(W2R + 2 * s);
          SB();
#pragma unroll
          for (int n = 0; n < 4; ++n) {
            ac[n] = mfma16(bfr[s % (W2L + 1)][n], af[s % W2B], ac[n]);
            if (s + W2L < 32) WO_RD(s + W2L, n);
            SB();
          }
          if (s < W2B - W2R) O_LOAD(W2R + 2 * s + 1);
          else if (s + W2B < 32) O_LOAD(s + W2B);
          SB();
        }
        __builtin_amdgcn_s_setprio(0);
#undef WO_RD
#undef O_LOAD
#pragma unroll
        for (int n = 0; n < 4; ++n) { v[2 * n] = f32x2{ac[n][0], ac[n][1]}; v[2 * n + 1] = f32x2{ac[n][2], ac[n][3]}; }
      }
    }
    {
      if (it == 0) { st16(pack8v(v), rs, to, OFF_XN + (4u << 20)); st16(pack8v(v + 4), rs, to + 2048, OFF_XN + (4u << 20)); }
      SB();
      const f32x2 s = ((v[0] + v[1]) + (v[2] + v[3])) + ((v[4] + v[5]) + (v[6] + v[7]));
      f32x2 dp = v[0] * wv[0], q = v[0] * v[0];
#pragma unroll
      for (int j = 1; j < 8; ++j) { dp = __builtin_elementwise_fma(v[j], wv[j], dp); q = __builtin_elementwise_fma(v[j], v[j], q); }
      float mt = hadd(s), qq = hadd(q), dot = hadd(dp);
      add_g3(mt, qq, dot);
      const unsigned so = OFF_STAT + it * 524288 + rb * 32768, lrow = R & 127;
      if (it == 0) wait_vm<2>();
      if (g == 0) st16(unpoison4(__builtin_bit_cast(u32x4, f32x4{mt, qq, dot, 0.f})), rs, ct * 2048 + lrow * 16, so);
      SB();
      if (it == 0) wait_vm<1>();
      if constexpr (it == 0) {
        volatile LDS_AS u32x2* in = (volatile LDS_AS u32x2*)(smem + L_XC);
        if (l == 0) in[w] = T;
        const u32x2 t = in[l & 7];
        if (__all(t[0] == T[0] && t[1] == T[1]) && l < (int)RX1) st8(T, rs, (l * 32 + (rb & 1) * 16 + ct) * SX1, F_X1 + (rb >> 1) * RX1 * 32 * SX1);
      }
      if (it == 0) {
        asm volatile("s_barrier" ::: "memory");
        if (ffn_on) { WD_DMA(); }
#pragma unroll
        for (int j = 0; j < 9; ++j) *(u32x4*)(smem + L_RING + (j / 3) * STAGE + (w * 3 + j % 3) * 1024 + l * 16) = keep[j];
      }
      if constexpr (it == 0) {
        if (PSX && w == 0) __builtin_amdgcn_s_sleep(PSX);
        if (w == 0) poll_flags(rs, ((l & 2) * 8 + ((ROT0 + 5) & 7) * 2 + (l & 1)) * SX1, F_X1 + (p * RX1 + (xcd >> 1) % RX1) * 32 * SX1, T);
        if (tid == 192) *(LDS_AS u32x4*)(smem + L_QC) = u32x4{0u, 0u, 0u, 0u};
        BAR();
        PRE_LOAD(1, ROT0 + 5, 0, NPRE);
      } else {
        __builtin_amdgcn_s_sleep(SDIR);
        u32x4 sp[4];
#pragma unroll
        for (int i = 0; i < 4; ++i) sp[i] = ld16(rs, (4 * g + i) * 2048 + lrow * 16, so);
        const RowFin r = fin_key(rs, T, g * 8192 + lrow * 16, so, smem + L_XC, l, w, R, wsum, sp);
        BAR();
        fin_cand(rs, smem + L_XC, 1, g, rb, ct, r, v);
        fin_x2(r, v);
      }
    }
    if (it == 1) {
      u32x4 op[2];
      {
        char* tp = smem + L_OUT + w * 2048;
        const unsigned c0 = (g & 1) * 2 + (g >> 1), r = l & 15;
        *(u32x4*)(tp + r * 128 + ((c0 ^ (r & 7)) << 4)) = trade16(pack8v(v));
        *(u32x4*)(tp + r * 128 + (((c0 + 4) ^ (r & 7)) << 4)) = trade16(pack8v(v + 4));
#pragma unroll
        for (int j = 0; j < 2; ++j) op[j] = *(const u32x4*)(tp + j * 1024 + (l >> 3) * 128 + (((l ^ (l >> 3)) & 7) << 4));
      }
      const unsigned Ro = R - (l & 15) + (l >> 3), oo = (Ro * 1024 + ct * 64) * 2 + (l & 7) * 16;
#define OUT_STORE(c0, c1)                                                                           \
  {                                                                                                 \
    if (c0) __builtin_amdgcn_raw_buffer_store_b128(op[0], rout, oo, 0, SC1);                        \
    if (c1) __builtin_amdgcn_raw_buffer_store_b128(op[1], rout, oo + 16384, 0, SC1);                \
  }
      if (w == 0) {
        __builtin_amdgcn_s_sleep(GSL);
        u32x4 xr[4];
        unsigned cur = 0, best;
        asm volatile("" : "=v"(xr[0]), "=v"(xr[1]));
        for (unsigned spins = 0;;) {
          const unsigned k_ = key_load(rs, l, 1, KREP());
          const bool all = __all(k_ != POISON);
          best = __builtin_amdgcn_readfirstlane(umax16(k_ == POISON ? 0u : k_));
          if (best != cur && ffn_on) {
            cur = best;
            ld16h<0>(xr[0], rs, (l >> 3) * 256 + (l & 7) * 16, cand_off(cur, 1)); ld16h<2048>(xr[1], rs, (l >> 3) * 256 + (l & 7) * 16, cand_off(cur, 1));
          }
          if (all || spin_fail(spins, rs, T)) break;
        }
        if (l == 0) *(LDS_AS unsigned*)(smem + L_SKEY + 32) = best;
        asm volatile("s_waitcnt vmcnt(0)" : "+v"(xr[0]), "+v"(xr[1])::"memory");
        if (ffn_on) {
          word_wait<2>(rs, T, (l >> 3) * 256 + (l & 7) * 16, cand_off(best, 1), xr);
          *(LDS_AS u32x4*)(smem + L_ROW + l * 16) = xr[0]; *(LDS_AS u32x4*)(smem + L_ROW + 1024 + l * 16) = xr[1];
        }
      }
      BAR();
      const unsigned key = __builtin_amdgcn_readfirstlane(*(LDS_AS unsigned*)(smem + L_SKEY + 32)), krow = key_row(key);
      if (ffn_on) {
        u32x4 xv[4];
#pragma unroll
        for (int i = 0; i < 4; ++i) xv[i] = *(const LDS_AS u32x4*)(smem + L_ROW + w * 256 + i * 64 + g * 16);
        f32x4 ga, ua;
        ffn_gu(gv, uv, xv, ga, ua);
        ffn_gu_store(smem, l, w, ga, ua);
        BAR();
        ffn_d_store(rs, l, w, 1, slice, ffn_d_all(dfr, ffn_d_h(ffn_d_read(smem, l))));
        SB();
      }
      OUT_STORE(Ro != krow, Ro + 8 != krow);
      if (!ffn_on) {
        const unsigned G = xcd * 4 + bi - 28;
        if (G == 0 && tid == 0) st8(T, rs, F_EP, 0);
        u32x4 pv[4];
        u32x2 cxn, cx1;
        const float sig = red_gate(key);
        __builtin_amdgcn_s_sleep(RSL);
        red_load(rs, tid, 1, G, key, pv, cxn, cx1);
        red_sum(rs, smem, T, tid, w, 1, G, key, pv, cxn, cx1);
        BAR();
        if (w == 0) {
          const u32x2 ov = red_final(smem, l, sig, cxn, cx1);
          if (l < 8) __builtin_amdgcn_raw_buffer_store_b64(ov, rout, (krow * 1024 + G * 32 + l * 4) * 2, 0, 0);
        }
      }
#undef OUT_STORE
    }
  };
  layer(std::integral_constant<int, 0>{});
  layer(std::integral_constant<int, 1>{});
}

extern "C" void kernel_launch(void* const* d_in, const int* in_sizes, int n_in, void* d_out, int out_size,
                              void* d_ws, size_t ws_size, hipStream_t stream) {
  (void)hipFuncSetAttribute((const void*)mega_kernel, hipFuncAttributeMaxDynamicSharedMemorySize, LDS_BYTES);
  mega_kernel<<<256, 512, LDS_BYTES, stream>>>((const bf16*)d_in[0], (char*)d_ws, (const bf16*)d_in[1], (const bf16*)d_in[2], (const bf16*)d_in[4],
                                               (const bf16*)d_in[5], (const bf16*)d_in[6], (const bf16*)d_in[7], (bf16*)d_out);
}
```

```cpp
#include <hip/hip_runtime.h>
#include <stdint.h>
#include <utility>

typedef __bf16 bf16;
typedef __bf16 bf16x8 __attribute__((ext_vector_type(8)));
typedef __bf16 bf16x2 __attribute__((ext_vector_type(2)));
typedef float f32x4 __attribute__((ext_vector_type(4)));
typedef float f32x2 __attribute__((ext_vector_type(2)));
typedef short s16x4 __attribute__((ext_vector_type(4)));
typedef unsigned int u32x4 __attribute__((ext_vector_type(4)));
typedef unsigned int u32x2 __attribute__((ext_vector_type(2)));
#define LDS_AS __attribute__((address_space(3)))
#define GAS __attribute__((address_space(1)))
typedef __amdgpu_buffer_rsrc_t rsrc_t;

constexpr int F_ = 3584;
constexpr int SC1 = 16;
#define PAUX 16

constexpr int RX = 4;
constexpr unsigned F_EP = 0, F_TMO = 128;
constexpr unsigned F_XN = 4096;
#define FLG 256, 1024, 8, 64, 4, 256, 4
constexpr unsigned FLV[7] = {FLG};
constexpr unsigned SO = FLV[0], SKV = FLV[1], SKW = FLV[2], SX1 = FLV[3], RX1 = FLV[4], SKEY = FLV[5], RKEY = FLV[6];
constexpr unsigned F_O = 8u << 20;
constexpr unsigned F_KV = 11u << 20;
constexpr unsigned F_IM = F_O + 524288;
constexpr unsigned F_WOI = F_O + 262144;
constexpr unsigned F_X1 = 9u << 20;
constexpr unsigned OFF_KEY = 10u << 20;
#define KREP() (RKEY == 16 ? bid & 15 : (bid & 7) * RKEY / 8)
#define KEY_AT(L, rep) (OFF_KEY + ((L) * RKEY + (rep)) * (SKEY < 8 ? 128 : 16 * SKEY))
constexpr unsigned OFF_AB = 2u << 20;
constexpr unsigned OFF_XNEW = 262144;
constexpr unsigned OFF_CS = OFF_AB + 65536;
constexpr unsigned OFF_FX = 393216;
constexpr unsigned OFF_CA = 524288;
constexpr unsigned OFF_STAT = 1u << 20;
constexpr unsigned OFF_PART = 3u << 20;
constexpr unsigned OFF_STAMP = 6u << 20;
constexpr unsigned OFF_XN = 8u << 20;
constexpr unsigned OFF_O = 16u << 20;
constexpr unsigned OFF_KV = 32u << 20;
constexpr unsigned OFF_WI = 52u << 20;
constexpr unsigned OFF_WOI = 58u << 20;
constexpr unsigned WS_BYTES = 60u << 20;
constexpr unsigned POISON = 0xFFFFFFFFu;

constexpr int L_WOS = 20480, L_ABW = 16640, L_CSP = 17408, L_CSL = 31872, L_FXL = 32256, L_K = 0, L_V = 32768, L_RING = 65536, L_DN = 32768, L_RDY = 31744, L_QC = 31808, LDS_BYTES = 163840;
constexpr int STAGE = 24576;
#define SLOT(t) ((t) & 3)

template <typename F, int... I> __device__ __forceinline__ void static_for_impl(F&& f, std::integer_sequence<int, I...>) {
  (f(std::integral_constant<int, I>{}), ...);
}
template <int N, typename F> __device__ __forceinline__ void static_for(F&& f) { static_for_impl(f, std::make_integer_sequence<int, N>{}); }
__device__ __forceinline__ unsigned pack2(float a, float b) {
  f32x2 v = {a, b};
  return __builtin_bit_cast(unsigned, __builtin_convertvector(v, bf16x2));
}
__device__ __forceinline__ float asf(unsigned u) { return __builtin_bit_cast(float, u); }
__device__ __forceinline__ float lo16(unsigned u) { return __builtin_bit_cast(float, u << 16); }
__device__ __forceinline__ float hi16(unsigned u) { return __builtin_bit_cast(float, u & 0xffff0000u); }
__device__ __forceinline__ u32x4 pack8(const f32x4 a, const f32x4 b) {
  u32x4 o;
  o[0] = pack2(a[0], a[1]); o[1] = pack2(a[2], a[3]); o[2] = pack2(b[0], b[1]); o[3] = pack2(b[2], b[3]);
  return o;
}
__device__ __forceinline__ f32x4 mfma16(u32x4 a, u32x4 b, f32x4 c) {
  return __builtin_amdgcn_mfma_f32_16x16x32_bf16(__builtin_bit_cast(bf16x8, a), __builtin_bit_cast(bf16x8, b), c, 0, 0, 0);
}
__device__ __forceinline__ f32x4 mfma16k16(s16x4 a, s16x4 b, f32x4 c) { return __builtin_amdgcn_mfma_f32_16x16x16bf16_1k(a, b, c, 0, 0, 0); }
template <int CTRL> __device__ __forceinline__ float dppf(float v) {
  return __builtin_bit_cast(float, __builtin_amdgcn_update_dpp(0, __builtin_bit_cast(int, v), CTRL, 0xf, 0xf, false));
}
template <int CTRL> __device__ __forceinline__ unsigned dppu(unsigned v) {
  return (unsigned)__builtin_amdgcn_update_dpp(0, (int)v, CTRL, 0xf, 0xf, false);
}
__device__ __forceinline__ float add_x16(float v) {
  auto r = __builtin_amdgcn_permlane16_swap(__builtin_bit_cast(unsigned, v), __builtin_bit_cast(unsigned, v), false, false);
  return __builtin_bit_cast(float, (unsigned)r[0]) + __builtin_bit_cast(float, (unsigned)r[1]);
}
__device__ __forceinline__ float add_x32(float v) {
  auto r = __builtin_amdgcn_permlane32_swap(__builtin_bit_cast(unsigned, v), __builtin_bit_cast(unsigned, v), false, false);
  return __builtin_bit_cast(float, (unsigned)r[0]) + __builtin_bit_cast(float, (unsigned)r[1]);
}
__device__ __forceinline__ float max_x16(float v) {
  auto r = __builtin_amdgcn_permlane16_swap(__builtin_bit_cast(unsigned, v), __builtin_bit_cast(unsigned, v), false, false);
  return fmaxf(__builtin_bit_cast(float, (unsigned)r[0]), __builtin_bit_cast(float, (unsigned)r[1]));
}
__device__ __forceinline__ float max_x32(float v) {
  auto r = __builtin_amdgcn_permlane32_swap(__builtin_bit_cast(unsigned, v), __builtin_bit_cast(unsigned, v), false, false);
  return fmaxf(__builtin_bit_cast(float, (unsigned)r[0]), __builtin_bit_cast(float, (unsigned)r[1]));
}
__device__ __forceinline__ float sum_hi8(float v) {
  v += dppf<0x128>(v);
  v = add_x16(v);
  return add_x32(v);
}
__device__ __forceinline__ float sum64(float v) {
  v += dppf<0xB1>(v);
  v += dppf<0x4E>(v);
  v += dppf<0x124>(v);
  v += dppf<0x128>(v);
  v = add_x16(v);
  return add_x32(v);
}

#define BAR() asm volatile("s_waitcnt lgkmcnt(0)\n\ts_barrier" ::: "memory")
#define DRAIN() asm volatile("s_waitcnt vmcnt(0)" ::: "memory")
#define SB() __builtin_amdgcn_sched_barrier(0)
template <int N> __device__ __forceinline__ void wait_vm() { asm volatile("s_waitcnt vmcnt(%0)" ::"n"(N) : "memory"); }

__device__ __forceinline__ void dma16(rsrc_t rs, unsigned voff, unsigned soff, unsigned lds) {
  asm volatile("s_mov_b32 m0, %0\n\ts_nop 0\n\tbuffer_load_dwordx4 %1, %2, %3 offen lds" ::"s"(lds), "v"(voff), "s"(rs), "s"(soff) : "memory");
}
__device__ __forceinline__ void dma16n(rsrc_t rs, unsigned voff, unsigned soff, unsigned lds) {
  asm volatile("s_mov_b32 m0, %0\n\ts_nop 0\n\tbuffer_load_dwordx4 %1, %2, %3 offen nt lds" ::"s"(lds), "v"(voff), "s"(rs), "s"(soff) : "memory");
}
template <int OFF> __device__ __forceinline__ void dma16s(rsrc_t rs, unsigned voff, unsigned soff, unsigned lds) {
  asm volatile("s_mov_b32 m0, %0\n\ts_nop 0\n\tbuffer_load_dwordx4 %1, %2, %3 offen offset:%4 sc1 lds" ::"s"(lds), "v"(voff), "s"(rs), "s"(soff), "n"(OFF) : "memory");
}
__device__ __forceinline__ float rbf1(float x) { return __builtin_bit_cast(float, pack2(0.f, x)); }
__device__ __forceinline__ u32x4 ld16(rsrc_t rs, unsigned voff, unsigned soff) { return __builtin_amdgcn_raw_buffer_load_b128(rs, voff, soff, SC1); }
template <int OFF> __device__ __forceinline__ void ld16h(u32x4& d, rsrc_t rs, unsigned voff, unsigned soff) {
  asm volatile("buffer_load_dwordx4 %0, %1, %2, %3 offen offset:%4 sc1" : "=v"(d) : "v"(voff), "s"(rs), "s"(soff), "n"(OFF) : "memory");
}
template <int I, int OFF> __device__ __forceinline__ void ld16w(u32x4& d, rsrc_t rs, unsigned voff, unsigned soff) {
  if constexpr (I == 0) asm volatile("buffer_load_dwordx4 %0, %1, %2, %3 offen offset:%4 sc1" : "={v[128:131]}"(d) : "v"(voff), "s"(rs), "s"(soff), "n"(OFF) : "memory");
  else if constexpr (I == 1) asm volatile("buffer_load_dwordx4 %0, %1, %2, %3 offen offset:%4 sc1" : "={v[132:135]}"(d) : "v"(voff), "s"(rs), "s"(soff), "n"(OFF) : "memory");
  else if constexpr (I == 2) asm volatile("buffer_load_dwordx4 %0, %1, %2, %3 offen offset:%4 sc1" : "={v[136:139]}"(d) : "v"(voff), "s"(rs), "s"(soff), "n"(OFF) : "memory");
  else if constexpr (I == 3) asm volatile("buffer_load_dwordx4 %0, %1, %2, %3 offen offset:%4 sc1" : "={v[140:143]}"(d) : "v"(voff), "s"(rs), "s"(soff), "n"(OFF) : "memory");
  else if constexpr (I == 4) asm volatile("buffer_load_dwordx4 %0, %1, %2, %3 offen offset:%4 sc1" : "={v[144:147]}"(d) : "v"(voff), "s"(rs), "s"(soff), "n"(OFF) : "memory");
  else if constexpr (I == 5) asm volatile("buffer_load_dwordx4 %0, %1, %2, %3 offen offset:%4 sc1" : "={v[148:151]}"(d) : "v"(voff), "s"(rs), "s"(soff), "n"(OFF) : "memory");
  else if constexpr (I == 6) asm volatile("buffer_load_dwordx4 %0, %1, %2, %3 offen offset:%4 sc1" : "={v[152:155]}"(d) : "v"(voff), "s"(rs), "s"(soff), "n"(OFF) : "memory");
  else if constexpr (I == 7) asm volatile("buffer_load_dwordx4 %0, %1, %2, %3 offen offset:%4 sc1" : "={v[156:159]}"(d) : "v"(voff), "s"(rs), "s"(soff), "n"(OFF) : "memory");
  else if constexpr (I == 8) asm volatile("buffer_load_dwordx4 %0, %1, %2, %3 offen offset:%4 sc1" : "={v[160:163]}"(d) : "v"(voff), "s"(rs), "s"(soff), "n"(OFF) : "memory");
  else if constexpr (I == 9) asm volatile("buffer_load_dwordx4 %0, %1, %2, %3 offen offset:%4 sc1" : "={v[164:167]}"(d) : "v"(voff), "s"(rs), "s"(soff), "n"(OFF) : "memory");
  else if constexpr (I == 10) asm volatile("buffer_load_dwordx4 %0, %1, %2, %3 offen offset:%4 sc1" : "={v[168:171]}"(d) : "v"(voff), "s"(rs), "s"(soff), "n"(OFF) : "memory");
  else if constexpr (I == 11) asm volatile("buffer_load_dwordx4 %0, %1, %2, %3 offen offset:%4 sc1" : "={v[172:175]}"(d) : "v"(voff), "s"(rs), "s"(soff), "n"(OFF) : "memory");
}
template <int K> __device__ __forceinline__ void pin_w(u32x4& a, u32x4& b) {
  if constexpr (K == 0) asm volatile("" : "+{v[128:131]}"(a), "+{v[132:135]}"(b)::"memory");
  else if constexpr (K == 1) asm volatile("" : "+{v[136:139]}"(a), "+{v[140:143]}"(b)::"memory");
  else if constexpr (K == 2) asm volatile("" : "+{v[144:147]}"(a), "+{v[148:151]}"(b)::"memory");
  else if constexpr (K == 3) asm volatile("" : "+{v[152:155]}"(a), "+{v[156:159]}"(b)::"memory");
  else if constexpr (K == 4) asm volatile("" : "+{v[160:163]}"(a), "+{v[164:167]}"(b)::"memory");
  else if constexpr (K == 5) asm volatile("" : "+{v[168:171]}"(a), "+{v[172:175]}"(b)::"memory");
}
__device__ __forceinline__ void ld8h(u32x2& d, rsrc_t rs, unsigned voff, unsigned soff) {
  asm volatile("buffer_load_dwordx2 %0, %1, %2, %3 offen sc1" : "=v"(d) : "v"(voff), "s"(rs), "s"(soff) : "memory");
}
template <int OFF> __device__ __forceinline__ void ld4h(unsigned& d, rsrc_t rs, unsigned voff, unsigned soff) {
  asm volatile("buffer_load_dword %0, %1, %2, %3 offen offset:%4 sc1" : "=v"(d) : "v"(voff), "s"(rs), "s"(soff), "n"(OFF) : "memory");
}
__device__ __forceinline__ u32x2 ld8(rsrc_t rs, unsigned voff, unsigned soff) { return __builtin_amdgcn_raw_buffer_load_b64(rs, voff, soff, SC1); }
__device__ __forceinline__ void st16(u32x4 v, rsrc_t rs, unsigned voff, unsigned soff) { __builtin_amdgcn_raw_buffer_store_b128(v, rs, voff + soff, 0, SC1); }
__device__ __forceinline__ void st8(u32x2 v, rsrc_t rs, unsigned voff, unsigned soff) { __builtin_amdgcn_raw_buffer_store_b64(v, rs, voff, soff, SC1); }

__device__ __forceinline__ unsigned ld4(rsrc_t rs, unsigned voff, unsigned soff) { return __builtin_amdgcn_raw_buffer_load_b32(rs, voff, soff, SC1); }
__device__ __forceinline__ void st4(unsigned v, rsrc_t rs, unsigned voff, unsigned soff) { __builtin_amdgcn_raw_buffer_store_b32(v, rs, voff, soff, SC1); }

constexpr unsigned kSpinCap = 1u << 16;
__device__ __forceinline__ bool spin_fail(unsigned& spins, rsrc_t rs, u32x2 T) {
  asm volatile("" ::: "memory");
  ++spins;
  if (spins > kSpinCap) { st8(T, rs, 0, F_TMO); return true; }
  if ((spins & 255u) == 0u) {
    const u32x2 v = ld8(rs, 0, F_TMO);
    if (v[0] == T[0] && v[1] == T[1]) return true;
  }
  __builtin_amdgcn_s_sleep(3);
  return false;
}
__device__ __forceinline__ void poll_flags2(rsrc_t rs, unsigned voff, u32x2 T) {
  for (unsigned spins = 0;;) {
    const u32x4 v = ld16(rs, voff, 0);
    if (__all(v[0] == T[0] && v[1] == T[1] && v[2] == T[0] && v[3] == T[1])) break;
    if (spin_fail(spins, rs, T)) break;
  }
  asm volatile("" ::: "memory");
  __builtin_amdgcn_fence(__ATOMIC_ACQUIRE, "wavefront");
}
__device__ __forceinline__ void tag_wait(rsrc_t rs, u32x2 T, unsigned voff, unsigned soff, u32x4& v) {
  if (__builtin_expect(!__all(v[2] == T[0] && v[3] == T[1]), 0)) {
    unsigned spins = 0;
    do {
      if (spin_fail(spins, rs, T)) break;
      v = ld16(rs, voff, soff);
    } while (!__all(v[2] == T[0] && v[3] == T[1]));
    asm volatile("s_waitcnt vmcnt(0)" : "+v"(v)::"memory");
  }
}
template <int N> __device__ __forceinline__ void word_wait(rsrc_t rs, u32x2 T, unsigned voff, unsigned soff, u32x4 (&v)[4]) {
#define WW_OK() __all(max(max(max(v[0][0], v[0][1]), max(v[0][2], v[0][3])), max(max(v[N - 1][0], v[N - 1][1]), max(v[N - 1][2], v[N - 1][3]))) != POISON)
  if (__builtin_expect(!WW_OK(), 0)) {
    unsigned spins = 0;
    do {
      if (spin_fail(spins, rs, T)) break;
#pragma unroll
      for (int i = 0; i < N; ++i) v[i] = ld16(rs, voff + i * 2048, soff);
    } while (!WW_OK());
    asm volatile("s_waitcnt vmcnt(0)" : "+v"(v[0]), "+v"(v[N - 1])::"memory");
  }
#undef WW_OK
}
__device__ __forceinline__ void poll_flags(rsrc_t rs, unsigned voff, unsigned soff, u32x2 T) {
  for (unsigned spins = 0;;) {
    const u32x2 v = __builtin_amdgcn_raw_buffer_load_b64(rs, voff, soff, PAUX);
    if (__all(v[0] == T[0] && v[1] == T[1])) break;
    if (spin_fail(spins, rs, T)) break;
  }
  asm volatile("" ::: "memory");
  __builtin_amdgcn_fence(__ATOMIC_ACQUIRE, "wavefront");
}

#define STAMP_ON(i) true
#define STAMP_VAL(i) (((i) == 23 || (i) == 22 || (i) == 21) ? __builtin_amdgcn_s_memtime() : __builtin_amdgcn_s_memrealtime())
#define STAMPAT(i)

#define GSTEP(s, n)
#define VSTEP(i)

__device__ __forceinline__ void unpack8v(const u32x4 u, f32x2* v) {
#pragma unroll
  for (int j = 0; j < 4; ++j) v[j] = f32x2{lo16(u[j]), hi16(u[j])};
}
__device__ __forceinline__ u32x4 pack8v(const f32x2* v) {
  u32x4 o;
#pragma unroll
  for (int j = 0; j < 4; ++j) o[j] = pack2(v[j][0], v[j][1]);
  return o;
}
__device__ __forceinline__ float ln_rd(float var) { return __builtin_amdgcn_rcpf(rbf1(__builtin_amdgcn_sqrtf(rbf1(rbf1(var) + 1e-5f)))); }
__device__ __forceinline__ float hadd(f32x2 v) {
  float a = v[0];
  asm volatile("" : "+v"(a));
  return a + v[1];
}
__device__ __forceinline__ float add_g(float v) { return add_x32(add_x16(v)); }
__device__ __forceinline__ void add_g3(float& x, float& y, float& z) {
  auto a = __builtin_amdgcn_permlane16_swap(__builtin_bit_cast(unsigned, x), __builtin_bit_cast(unsigned, y), false, false);
  auto b = __builtin_amdgcn_permlane16_swap(__builtin_bit_cast(unsigned, z), __builtin_bit_cast(unsigned, z), false, false);
  const float r = asf(a[0]) + asf(a[1]), t = asf(b[0]) + asf(b[1]);
  auto c = __builtin_amdgcn_permlane32_swap(__builtin_bit_cast(unsigned, r), __builtin_bit_cast(unsigned, r), false, false);
  auto d = __builtin_amdgcn_permlane32_swap(__builtin_bit_cast(unsigned, t), __builtin_bit_cast(unsigned, t), false, false);
  const float u = asf(c[0]) + asf(c[1]);
  z = asf(d[0]) + asf(d[1]);
  auto e = __builtin_amdgcn_permlane16_swap(__builtin_bit_cast(unsigned, u), __builtin_bit_cast(unsigned, u), false, false);
  x = asf(e[0]); y = asf(e[1]);
}
__device__ __forceinline__ u32x4 trade16(u32x4 v) {
  auto a = __builtin_amdgcn_permlane16_swap(v[0], v[2], false, false);
  auto b = __builtin_amdgcn_permlane16_swap(v[1], v[3], false, false);
  return u32x4{(unsigned)a[0], (unsigned)b[0], (unsigned)a[1], (unsigned)b[1]};
}
__device__ __forceinline__ unsigned umax4(u32x4 v) { return max(max(v[0], v[1]), max(v[2], v[3])); }
__device__ __forceinline__ u32x2 unpoison2(u32x2 v) { return u32x2{min(v[0], POISON - 1), min(v[1], POISON - 1)}; }
__device__ __forceinline__ u32x4 unpoison4(u32x4 v) {
  return u32x4{min(v[0], POISON - 1), min(v[1], POISON - 1), min(v[2], POISON - 1), min(v[3], POISON - 1)};
}

typedef union { struct { s16x4 a, b; } hf; u32x4 v; } frag_t;
__device__ __forceinline__ s16x4 trread(const char* smem, unsigned off) {
  return __builtin_amdgcn_ds_read_tr16_b64_v4i16((LDS_AS s16x4*)(smem + off));
}
__device__ __forceinline__ s16x4 trabs(unsigned addr) { return __builtin_amdgcn_ds_read_tr16_b64_v4i16((LDS_AS s16x4*)(size_t)addr); }

constexpr int WA = 6;
#define WPS 16
#define CH 0, 6, 8, 9, 15, 18, 23, 25, 29, 31, 4, 4
constexpr int CHV[12] = {CH};
constexpr int FKL = CHV[10], FSN = CHV[11];
constexpr int SPL = CHV[0];
constexpr int FSL = CHV[1], FS0 = CHV[2], FS0D = CHV[3], FS1 = CHV[4], FS1S = CHV[5];
constexpr int FXA = CHV[6], FXB = CHV[7], FXL = CHV[8], FXS = CHV[9];
#define ROT0 p
#define L0S 22, 25, 0, 0, 24, 28, 2, 2, 4, 14
constexpr int L0V[10] = {L0S};
constexpr int CSL = L0V[0], CSS = L0V[1], CSD = L0V[2];
constexpr int abl(int it) { return it ? 25 : L0V[4]; }
constexpr int abs_(int it) { return it ? 27 : L0V[5]; }
constexpr int W2S = L0V[6], WSH = L0V[7];
constexpr int PRE0 = 13;
constexpr int LNL = L0V[3], LNS = L0V[8], POI = L0V[9];
#define TCH 22, 3, 0x07, 26, 0xF8, 0, 1
constexpr int TCV[7] = {TCH};
constexpr int TS0 = TCV[0], TSN = TCV[1], TM0 = TCV[2], TEX = TCV[3], TM1 = TCV[4], TSD = TCV[5], TSG = TCV[6];
constexpr int QL = 26;
#define SLP 18, 4, 28, 3, 0, 0
constexpr int SLV[6] = {SLP};
constexpr int SDIR = SLV[0], GSL = SLV[1], RSL = SLV[2], KSL = SLV[3];
#define IME 4, 4, 8, 10, 2, 2
constexpr int IMV[6] = {IME};
constexpr int IMB = IMV[4], IMD = IMV[5];
constexpr int im_st(int s, int n) { return IMB == 2 ? (s == CSD && n >= 4 ? n - 4 : s == CSD + 1 && n == 0 ? 2 : -1) : IMB ? (s == CSD + 1 && (n & 1) ? n >> 1 : -1) : (s > CSD && s <= CSD + 3 && n == 5 ? s - CSD - 1 : -1); }
constexpr int IMT = IMV[0], IMF = IMV[1], IML = IMV[2], IMK = IMV[3];
constexpr int tp0(int it) { return it ? 3 : 3 + WSH; }
#define IMN 1
static_assert(IMB == 2 && IMF >= 2 && (IMK & 3) == 2 && IMK < 4 * IMT - 12 + 3 + WSH + IMD && WSH + IMD <= 4, "a wait for a younger request and a barrier in between");
constexpr int tile_at(int it, int t) { return 4 * t - 12 + tp0(it) + (!it && t == IMT ? IMD : !it && t == IMT + 1 && IMD > 1 ? IMD - 1 : 0); }
constexpr int piece_tile(int it, int s) { for (int t = 3; t < 8; ++t) if (s >= tile_at(it, t) && s < tile_at(it, t) + 3) return t; return -1; }
constexpr bool tile_piece(int it, int s) { return piece_tile(it, s) >= 0; }
#define PS 14, 10, 10, 0
constexpr int PSV[4] = {PS};
constexpr int PSX = PSV[0], PSO = PSV[1], PSO0 = PSV[2], PSK = PSV[3];
#define NPRE 3
#define QAD 4
#define WO2 5, 3, 2, 8
constexpr int W2V[4] = {WO2};
#define WOE 1, 7, 15, 19, 22
constexpr int WEV[5] = {WOE};
constexpr int WOL = WEV[0], WOS = WEV[1], WOF = WEV[2], WOC = WEV[3], WOK = WEV[4];
static_assert(((WOS + 1) & ~3) - 2 >= WOL + 4 && ((WOF + 1) & ~3) - 2 >= WOS + 5 && (WOK & 3) == 2, "a wait for a younger request and a barrier in between");
constexpr int W2B = W2V[0], W2R = W2V[1], W2L = W2V[2], W2P = W2V[3];
constexpr int a_req(int s, int n) {
  int r = 2 * NPRE;
  for (int u = 1; u < 192; u += 2) {
    const bool go = r < 64 && (r >> 1) <= u / 6 + QAD && (r < 8 || u >= 9);
    if (u == 6 * s + n) return go ? r : -1;
    r += go;
  }
  return -1;
}
constexpr int vm_n(int it, int s, int n) {
  int c = it ? (a_req(s, n) >= 0) + (s == 0 && n == 0) : (s + 5 < 32 && (n == 2 || n == 3));
  if (n == 0) c += tile_piece(it, s);
  if (n == 4) c += (s == abl(it));
  if (!it) c += (s >= W2S && s < W2S + 3 && n == 4) + (s == LNL && n == 5 ? 2 : 0) + (s == 0 && n < 2 ? 2 : 0) + (s == LNS + 2 && n == 5) + (s == TEX && n == 5) + (s == WOL && n == 5) + (s == WOS + 1 && n == 5) + (im_st(s, n) >= 0);
  else c += (s == 2 && n == 2) + (s == (SPL < 0 ? 0 : SPL) && n == (SPL < 0 ? 1 : 4) ? 4 : 0) + (s >= 27 && s < 30 && (n == 0 || n == 3));
  return c;
}
constexpr int vm_at(int it, int s, int n) {
  int c = it ? 2 * NPRE : PRE0;
  for (int u = 0; u < 6 * s + n; ++u) c += vm_n(it, u / 6, u % 6);
  return c;
}
constexpr int vm_a(int it, int k) {
  if (k < NPRE) return 2 * k + 2;
  for (int u = 0; u < 192; ++u)
    if (a_req(u / 6, u % 6) == 2 * k + 1) return vm_at(it, (u + 1) / 6, (u + 1) % 6);
  return 0;
}
constexpr int vm_tile(int it, int t) { return t >= 3 ? vm_at(it, tile_at(it, t) + 2, 1) : t == 1 ? 12 : vm_at(0, W2S + 2, 5); }
constexpr int vm_unit(int u) { return u < 3 ? 5 + 2 * u : u < 5 ? vm_at(0, 0, u - 2) : vm_at(0, u - 5, 4); }
constexpr int unit_slot(int u) { return u % 3 == 0 ? 0 : 16384 + (u % 3) * 16384; }

constexpr int L_XC = LDS_BYTES - 64, L_SKEY = 32704, L_ROW = 1024, L_OUT = 65536, L_SRED = 4096, L_SFIN = 20480;
__device__ __forceinline__ unsigned key_load(rsrc_t rs, int l, int L, int rep) { return ld4(rs, (l & 15) * SKEY, KEY_AT(L, rep)); }
__device__ __forceinline__ unsigned umax16(unsigned k) {
  k = max(k, dppu<0xB1>(k));
  k = max(k, dppu<0x4E>(k));
  k = max(k, dppu<0x141>(k));
  return max(k, dppu<0x140>(k));
}
__device__ __forceinline__ unsigned key_row(unsigned key) { return (0xffffu - (key & 0xffffu)) & 2047u; }
__device__ __forceinline__ unsigned key_wait(rsrc_t rs, u32x2 T, int l, int L, int rep, unsigned kv) {
  if (!__all(kv != POISON)) {
    unsigned spins = 0;
    do {
      if (spin_fail(spins, rs, T)) break;
      if (L == 0) __builtin_amdgcn_s_sleep(KSL);
      kv = key_load(rs, l, L, rep);
    } while (!__all(kv != POISON));
  }
  return __builtin_amdgcn_readfirstlane(umax16(kv));
}
__device__ __forceinline__ unsigned cand_off(unsigned key, int L) { return OFF_CA + L * 65536 + (key_row(key) >> 7) * 4096; }
#define XO(i) (((i) >> 1) * 256 + ((i) & 1) * 64)
__device__ __forceinline__ void ffn_gu(const u32x4 (&gv)[4], const u32x4 (&uv)[4], const u32x4 (&xv)[4], f32x4& ga, f32x4& ua) {
  ga = mfma16(gv[0], xv[0], f32x4{0.f, 0.f, 0.f, 0.f}); ua = mfma16(uv[0], xv[0], f32x4{0.f, 0.f, 0.f, 0.f});
#pragma unroll
  for (int i = 1; i < 4; ++i) { ga = mfma16(gv[i], xv[i], ga); ua = mfma16(uv[i], xv[i], ua); }
}
__device__ __forceinline__ void ffn_gu_store(char* smem, int l, int w, f32x4 ga, f32x4 ua) {
  LDS_AS f32x4* sred = (LDS_AS f32x4*)(smem + L_SRED);
  if ((l & 15) == 0) { sred[w * 8 + (l >> 4)] = ga; sred[w * 8 + 4 + (l >> 4)] = ua; }
}
__device__ __forceinline__ f32x4 ffn_d_read(char* smem, int l) {
  const LDS_AS float* t = (const LDS_AS float*)(smem + L_SRED) + (l & 8) * 16 + (l & 4) * 4 + (l >> 4) * 4 + (l & 3);
  return f32x4{t[0], t[32], t[64], t[96]};
}
__device__ __forceinline__ u32x2 ffn_d_h(f32x4 t) {
  float x = t[0] + t[1];
  asm volatile("" : "+v"(x));
  x += t[2] + t[3];
  x += dppf<0x128>(x);
  const float y = dppf<0x124>(x);
  const bool up = __builtin_amdgcn_inverse_ballot_w64(0xF0F0F0F0F0F0F0F0ull);
  const float gg = rbf1(up ? y : x), uu = rbf1(up ? x : y);
  const float h = rbf1(gg * __builtin_amdgcn_rcpf(1.0f + __expf(-gg))) * uu;
  const unsigned p = pack2(h, dppf<0xB1>(h));
  return u32x2{dppu<0x00>(p), dppu<0xAA>(p)};
}
template <int t> __device__ __forceinline__ f32x4 ffn_d_mma(s16x4 d, u32x2 hh, f32x4 y) {
  const bool me = __builtin_amdgcn_inverse_ballot_w64(0x0001000100010001ull << t);
  return mfma16k16(d, __builtin_bit_cast(s16x4, u32x2{me ? hh[0] : 0u, me ? hh[1] : 0u}), y);
}
__device__ __forceinline__ f32x4 ffn_d_all(const s16x4 (&dfr)[8], u32x2 hh) {
  f32x4 a = {0.f, 0.f, 0.f, 0.f}, b = a;
  a = ffn_d_mma<0>(dfr[0], hh, a); b = ffn_d_mma<1>(dfr[1], hh, b); a = ffn_d_mma<2>(dfr[2], hh, a); b = ffn_d_mma<3>(dfr[3], hh, b);
  a = ffn_d_mma<4>(dfr[4], hh, a); b = ffn_d_mma<5>(dfr[5], hh, b); a = ffn_d_mma<6>(dfr[6], hh, a); b = ffn_d_mma<7>(dfr[7], hh, b);
  return a + b;
}
__device__ __forceinline__ void ffn_d_store(rsrc_t rs, int l, int w, int L, int slice, f32x4 y) {
  const unsigned c = l & 15;
  if (c < 8) st16(unpoison4(__builtin_bit_cast(u32x4, y)), rs, (2 * w + (c >> 2)) * 65536 + slice * 256 + (c & 3) * 64 + (l >> 4) * 16, OFF_PART + L * (1u << 20));
}
#define RED_VO(k) ((min(tid + 512 * (k), 1791) >> 3) * 256 + (tid & 7) * 16)
#define RED_SO(L, G) (OFF_PART + (L) * (1u << 20) + ((G) >> 1) * 65536 + ((G) & 1) * 128)
#define RED_JJ() ((tid & 3) * 16 + (tid & 4) * 2)
__device__ __forceinline__ void red_load(rsrc_t rs, int tid, int L, unsigned G, unsigned key, u32x4 (&pv)[4], u32x2& cxn, u32x2& cx1) {
#pragma unroll
  for (int k = 0; k < 4; ++k) pv[k] = ld16(rs, RED_VO(k), RED_SO(L, G));
  cxn = ld8(rs, RED_JJ(), cand_off(key, L) + XO(G));
  cx1 = ld8(rs, RED_JJ() + 128, cand_off(key, L) + XO(G));
}
__device__ __forceinline__ void red_sum(rsrc_t rs, char* smem, u32x2 T, int tid, int w, int L, unsigned G, unsigned key,
                                        u32x4 (&pv)[4], u32x2& cxn, u32x2& cx1) {
#define PV_OK() __all(max(max(max(umax4(pv[0]), umax4(pv[1])), max(umax4(pv[2]), umax4(pv[3]))), max(max(cxn[0], cxn[1]), max(cx1[0], cx1[1]))) != POISON)
  if (!PV_OK()) {
    unsigned spins = 0;
    do {
      if (spin_fail(spins, rs, T)) break;
#pragma unroll
      for (int k = 0; k < 4; ++k)
        if (umax4(pv[k]) == POISON) pv[k] = ld16(rs, RED_VO(k), RED_SO(L, G));
      if (max(max(cxn[0], cxn[1]), max(cx1[0], cx1[1])) == POISON) { cxn = ld8(rs, RED_JJ(), cand_off(key, L) + XO(G)); cx1 = ld8(rs, RED_JJ() + 128, cand_off(key, L) + XO(G)); }
    } while (!PV_OK());
  }
#undef PV_OK
  f32x4 s = (__builtin_bit_cast(f32x4, pv[0]) + __builtin_bit_cast(f32x4, pv[1])) + __builtin_bit_cast(f32x4, pv[2]);
  if (w < 4) s += __builtin_bit_cast(f32x4, pv[3]);
#pragma unroll
  for (int j = 0; j < 4; ++j) s[j] = sum_hi8(s[j]);
  if ((tid & 63) < 8) ((LDS_AS f32x4*)(smem + L_SFIN))[w * 8 + (tid & 7)] = s;
}
__device__ __forceinline__ float red_gate(unsigned key) {
  const unsigned k16 = key >> 16;
  const float rmax = __builtin_bit_cast(float, ((k16 & 0x8000u) ? (k16 & 0x7fffu) : (~k16 & 0xffffu)) << 16);
  float sig = rbf1(__builtin_amdgcn_rcpf(1.0f + __expf(-rmax)));
  asm volatile("" : "+v"(sig));
  return sig;
}
__device__ __forceinline__ u32x2 red_final(char* smem, int l, float sig, u32x2 cxn, u32x2 cx1) {
  f32x4 s = ((const LDS_AS f32x4*)(smem + L_SFIN))[l];
#pragma unroll
  for (int j = 0; j < 4; ++j) s[j] = sum_hi8(s[j]);
  const float xn4[4] = {lo16(cxn[0]), hi16(cxn[0]), lo16(cxn[1]), hi16(cxn[1])};
  const float x14[4] = {lo16(cx1[0]), hi16(cx1[0]), lo16(cx1[1]), hi16(cx1[1])};
  float o[4];
#pragma unroll
  for (int j = 0; j < 4; ++j) o[j] = x14[j] + rbf1(xn4[j] + rbf1(rbf1(s[j]) * sig));
  return u32x2{pack2(o[0], o[1]), pack2(o[2], o[3])};
}

struct RowFin { float mean, var, mb, rd; unsigned key; };
__device__ __forceinline__ void fin_wait(rsrc_t rs, u32x2 T, unsigned voff, unsigned so, u32x4 (&sp)[4]) {
#define ST_OK() __all(max(max(umax4(sp[0]), umax4(sp[1])), max(umax4(sp[2]), umax4(sp[3]))) != POISON)
  if (!ST_OK()) {
    unsigned spins = 0;
    do {
      if (spin_fail(spins, rs, T)) break;
#pragma unroll
      for (int i = 0; i < 4; ++i) sp[i] = ld16(rs, voff + i * 2048, so);
    } while (!ST_OK());
  }
#undef ST_OK
}
__device__ __forceinline__ RowFin fin_key(rsrc_t rs, u32x2 T, unsigned voff, unsigned so, char* skey, int l, int w, unsigned R, float wsum, u32x4 (&sp)[4]) {
  RowFin r;
#define ST_SUM() ((__builtin_bit_cast(f32x4, sp[0]) + __builtin_bit_cast(f32x4, sp[1])) + (__builtin_bit_cast(f32x4, sp[2]) + __builtin_bit_cast(f32x4, sp[3])))
  f32x4 sf = ST_SUM();
  const float z = (sf[0] + sf[1]) + sf[2];
  if (__any(z != z)) {
    fin_wait(rs, T, voff, so, sp);
    sf = ST_SUM();
  }
#undef ST_SUM
  float s0 = sf[0], s1 = sf[1], s2 = sf[2];
  add_g3(s0, s1, s2);
  r.mean = s0 * (1.0f / 1024.0f);
  r.var = fmaxf(__builtin_fmaf(-r.mean, r.mean, s1 * (1.0f / 1024.0f)), 0.f);
  r.mb = rbf1(r.mean); r.rd = ln_rd(r.var);
  const unsigned rb16 = pack2((s2 - r.mb * wsum) * r.rd, 0.f) & 0xffffu;
  r.key = min((((rb16 & 0x8000u) ? (~rb16 & 0xffffu) : (rb16 | 0x8000u)) << 16) | (0xffffu - R), POISON - 1);
  const unsigned wk = umax16(r.key);
  if (l == 0) ((LDS_AS unsigned*)skey)[w] = wk;
  return r;
}
__device__ __forceinline__ void fin_cand(rsrc_t rs, char* skey, int L, int g, int rb, int ct, const RowFin& r, const f32x2 (&v)[8]) {
  const u32x4 k0 = *(const LDS_AS u32x4*)skey, k1 = *(const LDS_AS u32x4*)(skey + 16);
  const unsigned best = max(umax4(k0), umax4(k1));
  if (r.key == best) {
    const unsigned co = L * 65536 + rb * 4096 + ct * 256 + g * 16;
    f32x2 t[8], rd2 = {r.rd, r.rd}, of2 = {-r.mb * r.rd, -r.mb * r.rd};
    asm volatile("" : "+v"(rd2), "+v"(of2));
#pragma unroll
    for (int j = 0; j < 8; ++j) t[j] = __builtin_elementwise_fma(v[j], rd2, of2);
    st16(unpoison4(pack8v(t)), rs, co, OFF_CA); st16(unpoison4(pack8v(t + 4)), rs, co + 64, OFF_CA);
    if (g == 0 && ct < (int)RKEY) st4(best, rs, KEY_AT(L, ct) + rb * SKEY, 0);
    st16(unpoison4(pack8v(v)), rs, co + 128, OFF_CA); st16(unpoison4(pack8v(v + 4)), rs, co + 192, OFF_CA);
  }
}
__device__ __forceinline__ void fin_x2(const RowFin& r, f32x2 (&v)[8]) {
  f32x2 sc2 = {1.0f + r.rd, 1.0f + r.rd}, of2 = {-r.mb * r.rd, -r.mb * r.rd};
  asm volatile("" : "+v"(sc2), "+v"(of2));
#pragma unroll
  for (int j = 0; j < 8; ++j) v[j] = __builtin_elementwise_fma(v[j], sc2, of2);
}

extern "C" __device__ unsigned long long dispatch_id() __asm("llvm.amdgcn.dispatch.id");

extern "C" __global__ __launch_bounds__(512, 2) void mega_kernel(const bf16* x, char* ws, const bf16* wqkv, const bf16* wo, const bf16* wr,
                                                                 const bf16* wg, const bf16* wu, const bf16* wd, bf16* out) {
  extern __shared__ __attribute__((aligned(16))) char smem[];
  const int w0_ = __builtin_amdgcn_readfirstlane(threadIdx.x >> 6);
  const unsigned lbase = (unsigned)(size_t)(LDS_AS char*)smem;
  {
    unsigned long long pc;
    unsigned len;
    asm volatile("s_getpc_b64 %0\n\ts_mov_b32 %1, .Ltext_end-mega_kernel\n\t.subsection 1\n.Ltext_end:\n\t.subsection 0" : "=s"(pc), "=s"(len));
    const unsigned o = ((blockIdx.x >> 3) * 64 + (threadIdx.x & 63)) * 128;
    if (w0_ == 7 && o < len)
      asm volatile("s_mov_b32 m0, %0\n\ts_nop 0\n\tglobal_load_lds_dword %1, %2" ::"s"(lbase + L_FXL), "v"(o), "s"(pc & ~127ull) : "memory");
  }
  asm volatile("" : "+s"(x), "+s"(ws), "+s"(wqkv));
#define IDS()                                                                                         \
  int tid = threadIdx.x, w = w0_, bid = blockIdx.x;                                                   \
  asm volatile("" : "+v"(tid));                                                                       \
  asm volatile("" : "+s"(w));                                                                         \
  asm volatile("" : "+s"(bid));                                                                       \
  const int l = tid & 63, xcd = bid & 7, bi = bid >> 3;                                               \
  const int p = (xcd & 1) * 4 + (bi & 3), h = (xcd >> 1) * 8 + (bi >> 2);         \
  const int rb = 2 * p + ((h >> 3) & 1), ct = (h >> 4) * 8 + (h & 7);       \
  const int slice = xcd * 28 + bi;                                                   \
  const bool ffn_on = bi < 28;                                                                        \
  const int g = l >> 4;                                                                               \
  const int rt = w < 4 ? w : 11 - w;          \
  KARGS();                                                                                            \
  (void)p; (void)h; (void)rb; (void)ct; (void)slice; (void)ffn_on; (void)g; (void)rt;
  const rsrc_t rs = __builtin_amdgcn_make_buffer_rsrc((void*)ws, 0, WS_BYTES, 0x00020000);
  typedef const void* const __attribute__((address_space(4)))* karg_t;
#define KARGS() karg_t ka = (karg_t)__builtin_amdgcn_kernarg_segment_ptr(); asm volatile("" : "+s"(ka))
#define ARGP(name, i) const GAS bf16* name = (const GAS bf16*)ka[i]; asm volatile("" : "+s"(name))
#define RSRC(ptr, bytes) __builtin_amdgcn_make_buffer_rsrc((void*)(ptr), 0, bytes, 0x00020000)
#define rwq RSRC(pwq, 1024 * 3072 * 2)
#define rwo RSRC(pwo, 1024 * 1024 * 2)
#define rwg RSRC(pwg, 1024 * F_ * 2)
#define rwu RSRC(pwu, 1024 * F_ * 2)
#define rwd RSRC(pwd, 1024 * F_ * 2)
#define rout RSRC(pout, 2048 * 1024 * 2)

#define WQ_VOFF() ((l >> 2) * 6144 + (((l & 3) ^ ((((l >> 4) ^ (l >> 5)) & 1) << 1)) << 4))
#define WQ_PIECE(t, j, dst) dma16(rwq, wqv, ((((t) + rot) & 7) * 128 + ((j) & 7) * 16) * 6144 + ((j) >> 3) * 2048 + h * 64, lbase + (dst) + (j) * 1024)
#define WQ_BOFF(dt) ((8 * g + ((l & 15) >> 2)) * 64 + (l & 3) * 8 + (((dt) ^ (g & 1)) << 5))
#define WQ_POFF() ((4 * g + ((l & 15) >> 2)) * 64 + (l & 3) * 8 + (((g ^ (g >> 1)) & 1) << 5))
#define X_UNIT1(u, j) dma16(rx, (j) ? xv1 : xv_, (unsigned)(p * 256 + rt * 32 + ((u) & 1) * 16 + (j) * 8) * 2048 + ((((u) >> 2) + rot) & 7) * 256 + (((u) >> 1) & 1) * 128, lbase + rt * 2048 + unit_slot(u) + (j) * 1024)
#define X_VOFF() ((l >> 3) * 2048 + (((l & 7) ^ (l >> 4)) << 4))
#define X_ROFF(hs) (lbase + rt * 2048 + (l & 15) * 128 + (((4 * (hs) + g) ^ ((l >> 1) & 7)) << 4))
#define X_POFF() (lbase + rt * 2048 + (l & 15) * 128 + (((g >> 1) ^ ((l >> 1) & 7)) << 4) + (g & 1) * 8)
#define TOUCH_TO(ptr, dst) asm volatile("s_mov_b32 m0, %0\n\ts_nop 0\n\tglobal_load_lds_dword %1, off" ::"s"(lbase + (dst)), "v"(ptr) : "memory")
#define TOUCH(ptr) TOUCH_TO(ptr, L_FXL)
#define WO_PIECE_W(j, w_)                                                                                                                 \
  {                                                                                                                                       \
    dma16s<0>(rs, l * 16, OFF_WOI + ct * 131072 + (j) * 8192 + (w_) * 1024, lbase + 32768 + (j) * 8192 + (w_) * 1024);                    \
  }
#define WO_PIECE(j) WO_PIECE_W(j, w)
#define WO_VOFF() ((l >> 3) * 2048 + (((((l & 7) >> 1) ^ ((l >> 4) & 3)) * 2 + (l & 1)) << 4))
#define WD_DMA() _Pragma("unroll") for (int i = 0; i < 4; ++i) WD_DMA1(i)
#define WD_DMA1(i)                                                                                   \
      dma16n(rwd, (l >> 4) * 2048 + (((l & 15) * 16) ^ ((l >> 4) * 32) ^ (((i) & 1) * 128)), (unsigned)(slice * 16 + 4 * (i)) * 2048 + w * 256, lbase + L_DN + w * 4096 + (i) * 1024)
#define FFN_DMA(m) dma16((m) & 1 ? rwu : rwg, fnv, (unsigned)(((4 * w + ((m) >> 1)) * 32) * F_ + slice * 16) * 2, lbase + 65536 + (m) * 8192 + w * 1024)
  u32x4 gv[4], uv[4];
  s16x4 dfr[8];
  u32x4 keep[9];
  u32x4 pre[4][2];
#define PRE_LOAD(it_, rot_, s0, s1)                                                                  \
  static_for<(s1) - (s0)>([&](auto c_) __attribute__((always_inline)) {                              \
    constexpr int s = (s0) + decltype(c_)::value;                                                    \
    const unsigned ab_ = OFF_XN + (it_) * (4u << 20) + (((rot_) + (s >> 2)) & 7) * 8192 + ((s >> 1) & 1) * 4096; \
    ld16w<2 * s, (s & 1) * 2048>(pre[s][0], rs, l * 16, ab_ + (p * 8 + rt) * 65536);                         \
    ld16w<2 * s + 1, (s & 1) * 2048 + 1024>(pre[s][1], rs, l * 16, ab_ + (p * 8 + rt) * 65536);                 \
  })
  u32x2 T;
  u32x2 ep;
  u32x4 xl[2];
#define LN_LOAD()                                                                                                                                   \
  {                                                                                                                                                 \
    asm volatile("buffer_load_dwordx4 %0, %1, %2, %3 offen" : "=v"(xl[0]) : "v"(l * 16), "s"(rx), "s"((p * 256 + h * 8 + w) * 2048) : "memory");      \
    asm volatile("buffer_load_dwordx4 %0, %1, %2, %3 offen offset:1024" : "=v"(xl[1]) : "v"(l * 16), "s"(rx), "s"((p * 256 + h * 8 + w) * 2048) : "memory"); \
  }
  const GAS bf16* tsrc;
  {
    IDS();
    const int it = 0, rot = ROT0;
    (void)it;
    STAMPAT(0);
    const bf16* pwq = wqkv;
    const rsrc_t rx = RSRC(x, 2048 * 1024 * 2);
    const unsigned wqv = WQ_VOFF(), xv_ = X_VOFF(), xv1 = xv_ ^ 64;
    if (tid == 0) *(LDS_AS u32x4*)(smem + L_QC) = u32x4{0u, 0u, 0u, 0u};
    if (w == 0 && l < 24) TOUCH((const GAS bf16*)wqkv + (bid * 24 + l) * 512 + ((3 - (unsigned)((size_t)wqkv >> 7)) & 7) * 64);
#pragma unroll
    for (int i = 0; i < 3; ++i) WQ_PIECE(0, 3 * w + i, L_RING);
    STAMPAT(41);
    X_UNIT1(0, 0); X_UNIT1(0, 1); X_UNIT1(1, 0); X_UNIT1(1, 1);
    STAMPAT(42);
    BAR();
    STAMPAT(43);
    X_UNIT1(2, 0); X_UNIT1(2, 1);
#pragma unroll
    for (int i = 0; i < 3; ++i) WQ_PIECE(1, 3 * w + i, L_RING + STAGE);
    asm volatile("buffer_load_dwordx2 %0, off, %1, 0 sc1" : "=v"(ep) : "s"(rs) : "memory");
    STAMPAT(44);
    STAMPAT(31);
  }
  unsigned sel0 = 0x7FFFFFFFu;
  f32x2 v[8];
  u32x4 x2p[2];
  float wsum;

  auto layer = [&](auto it_c) __attribute__((always_inline)) {
    constexpr int it = decltype(it_c)::value;
    STAMPAT(1); STAMPAT(22);
    f32x4 acc[2][6];
    f32x2 qs2[2];
    u32x4 k0, k1, v0, v1;
    {
      IDS();
      bool aff = false;
      unsigned aq[2];
      u32x4 av[2];
#define PIN_ACC() asm volatile("" : "+{v[176:179]}"(acc[0][0]), "+{v[180:183]}"(acc[0][1]), "+{v[184:187]}"(acc[0][2]), "+{v[188:191]}"(acc[0][3]), "+{v[192:195]}"(acc[0][4]), "+{v[196:199]}"(acc[0][5]), \
                               "+{v[200:203]}"(acc[1][0]), "+{v[204:207]}"(acc[1][1]), "+{v[208:211]}"(acc[1][2]), "+{v[212:215]}"(acc[1][3]), "+{v[216:219]}"(acc[1][4]), "+{v[220:223]}"(acc[1][5]))
#define PINV4(x) asm volatile("" : "+v"(x[0]), "+v"(x[1]), "+v"(x[2]), "+v"(x[3])::"memory")
      auto qkv_loop = [&](auto it_c) __attribute__((always_inline)) {
        constexpr int IT = decltype(it_c)::value;
        const unsigned abase = OFF_XN + IT * (4u << 20) + (p * 8 + rt) * 65536;
        const int rot = ROT0 + 5 * IT;
        unsigned fkey = 0, kv, bq[2];
        u32x4 fx[4], bv[2], af[WA][2];
        u32x2 cxn, cx1, hh;
        f32x4 sa, sb, cs4[2];
        f32x2 b2[2];
#define AB_OFF() (OFF_AB + IT * 32768 + (p * 8 + rt) * 512)
#define A_LOAD1(k, a) ld16w<(k) % WA * 2 + (a), ((k) & 1) * 2048 + (a) * 1024>(af[(k) % WA][a], rs, l * 16, abase + ((((k) >> 2) + rot) & 7) * 8192 + (((k) >> 1) & 1) * 4096)
        if constexpr (IT == 1) {
#pragma unroll
          for (int s = 0; s < NPRE; ++s) { af[s][0] = pre[s][0]; af[s][1] = pre[s][1]; }
        }
        const bf16* pwq = wqkv;
        ARGP(pwo, 3);
        const rsrc_t rx = RSRC(x, 2048 * 1024 * 2);
        unsigned xv_ = X_VOFF(), xv1 = xv_ ^ 64, xr[4] = {X_ROFF(0), X_ROFF(1), 0u, 0u}, bf_[2];
        if constexpr (IT == 0) asm volatile("" : "+v"(xv_), "+v"(xv1), "+v"(xr[0]), "+v"(xr[1]));
#define LDS8(a_) (*(const LDS_AS u32x2*)(size_t)(a_))
#define X_READ(u)                                                                                                              \
  {                                                                                                                            \
    if constexpr ((u) < 4 * IMT) {                                                                                             \
      af[((u) & ~1) % WA][(u) & 1] = *(const LDS_AS u32x4*)(size_t)(xr[0] + unit_slot(u));                                     \
      af[((u) | 1) % WA][(u) & 1] = *(const LDS_AS u32x4*)(size_t)(xr[1] + unit_slot(u));                                      \
    } else {                                                                                                                   \
      const u32x2 a_ = LDS8(xr[0] + unit_slot(u)), b_ = LDS8(xr[1] + unit_slot(u)), c_ = LDS8(xr[2] + unit_slot(u)), d_ = LDS8(xr[3] + unit_slot(u)); \
      af[((u) & ~1) % WA][(u) & 1] = u32x4{a_[0], a_[1], b_[0], b_[1]};                                                        \
      af[((u) | 1) % WA][(u) & 1] = u32x4{c_[0], c_[1], d_[0], d_[1]};                                                         \
    }                                                                                                                          \
  }
        const int tstep = TS0 + bid % TSN + TSD * (w >> TSG);
        const unsigned wqv = WQ_VOFF();
#define QKV_DMA1(t, i)                                                                            \
  {                                                                                               \
    if constexpr (IT == 0 && (t) < IMT) WQ_PIECE(t, 3 * w + (i), L_RING + SLOT(t) * STAGE);       \
    else dma16s<(i) * 1024>(rs, l * 16, OFF_WI + (h * 8 + (((t) + rot) & 7)) * STAGE + w * 3072, lbase + L_RING + SLOT(t) * STAGE + w * 3072); \
  }
        unsigned bo[2][2];
#pragma unroll
        for (int dt = 0; dt < 2 - IT; ++dt) {
          bo[dt][0] = lbase + L_RING + (IT ? l * 16 : WQ_BOFF(dt)); bo[dt][1] = bo[dt][0] + 2 * STAGE;
          asm volatile("" : "+v"(bo[dt][0]), "+v"(bo[dt][1]));
        }
        auto frag0 = [&](int sl, int f) __attribute__((always_inline)) {
          unsigned pb = lbase + L_RING + WQ_POFF();
          asm volatile("" : "+v"(pb));
          const unsigned o = (pb ^ ((f & 1) << 5)) + sl * STAGE + ((f % 6) >> 1) * 8192 + (f / 6) * 2048;
          frag_t t;
          t.hf.a = trabs(o); t.hf.b = trabs(o + 1024);
          return t.v;
        };
        u32x4 df[4];
        auto dot_mma = [&](const u32x4 (&xf)[4]) __attribute__((always_inline)) {
          sa = mfma16(df[0], xf[0], f32x4{0.f, 0.f, 0.f, 0.f}); sb = mfma16(df[1], xf[1], f32x4{0.f, 0.f, 0.f, 0.f});
          sa = mfma16(df[2], xf[2], sa); sb = mfma16(df[3], xf[3], sb);
        };
        auto dot_store = [&](unsigned dst) __attribute__((always_inline)) {
          if ((l & 15) == 0) st16(unpoison4(__builtin_bit_cast(u32x4, sa + sb)), rs, g * 16, dst + (h * 8 + p) * 512 + w * 64);
        };
        auto tile_sum = [&](u32x4 pt, int dst) __attribute__((always_inline)) {
          f32x4 c = __builtin_bit_cast(f32x4, pt);
#pragma unroll
          for (int e = 0; e < 4; ++e) { c[e] += dppf<0x124>(c[e]); c[e] += dppf<0x128>(c[e]); c[e] = add_x16(c[e]); }
          if ((l & 28) == 0) *(LDS_AS f32x4*)(smem + dst + (w * 2 + (l >> 5)) * 64 + (l & 3) * 16) = c;
        };
        frag_t bfr[6];
#define QKV_BO(s, n, x) (bo[((n) ^ (x)) & 1][SLOT((s) >> 2) >> 1] + (SLOT((s) >> 2) & 1) * STAGE + ((n) >> 1) * 8192 + ((s) & 3) * 2048 + (x) * 256)
#define QKV_RDA(s, n)                                                                             \
  {                                                                                               \
    if constexpr (IT == 0 && (s) < 4 * IMT) bfr[n].hf.a = trabs(QKV_BO(s, n, 0));                 \
    else if constexpr (IT == 0) bfr[n].v = *(const LDS_AS u32x4*)(size_t)(bf_[SLOT((s) >> 2) >> 1] + (SLOT((s) >> 2) & 1) * STAGE + (((s) & 3) * 6 + (n)) * 1024); \
    else bfr[n].v = *(const LDS_AS u32x4*)(size_t)(bo[0][SLOT((s) >> 2) >> 1] + (SLOT((s) >> 2) & 1) * STAGE + (((s) & 3) * 6 + (n)) * 1024); \
  }
#define QKV_RDB(s, n) { if constexpr (IT == 0 && (s) < 4 * IMT) bfr[n].hf.b = trabs(QKV_BO(s, n, 1)); }
#define XP_OFF() (OFF_XNEW + ((rot + 5) & 7) * 1024)
#define FP_VO() (w == 3 ? (l >> 2) * 256 + (l & 3) * 16 : ((l >> 2) & 7) * 512 + (l >> 5) * 64 + (l & 3) * 16)
#define FP_OFF(i) (w == 3 ? OFF_XNEW + (i) * 4096 : OFF_FX + h * 4096 + w * 128)
#define FX_OK(n) __all(((n) == 4 ? max(max(umax4(fx[0]), umax4(fx[1])), max(umax4(fx[2]), umax4(fx[3]))) : max(umax4(fx[0]), umax4(fx[1]))) != POISON)
#define SINCE(s0) (vm_at(IT, s, n) - vm_at(IT, s0, 5))
#define DEF4(x) asm volatile("" : "=v"(x[0]), "=v"(x[1]), "=v"(x[2]), "=v"(x[3]))
        LDS_AS unsigned* qcnt = (LDS_AS unsigned*)(smem + L_QC);
        unsigned qc;
        unsigned qadr = lbase + L_QC, qone = 1;
        asm volatile("" : "+v"(qadr), "+v"(qone));
#define count_in(i) asm volatile("s_mov_b64 exec, 1\n\tds_add_u32 %0, %1 offset:%2\n\ts_mov_b64 exec, -1" ::"v"(qadr), "v"(qone), "n"(4 * (i)) : "memory")
        auto count_wait = [&](int i, unsigned want) __attribute__((always_inline)) {
          if (__builtin_expect((unsigned)__builtin_amdgcn_readfirstlane(qc) < want, 0)) {
            while ((unsigned)__builtin_amdgcn_readfirstlane(__hip_atomic_load(qcnt + i, __ATOMIC_RELAXED, __HIP_MEMORY_SCOPE_WORKGROUP)) < want) {}
          }
          asm volatile("" ::: "memory");
        };
        if constexpr (IT == 0) {
          wait_vm<PRE0 - vm_unit(1)>();
          STAMPAT(46);
          X_READ(0); X_READ(1);
          asm volatile("s_barrier" ::: "memory");
        }
        STAMPAT(39);
#pragma unroll
        for (int n = 0; n < 6; ++n) { QKV_RDA(0, n); if (n < 5) QKV_RDB(0, n); }
        f32x2 lv[4], xs, xq;
        float lmean;
        f32x4 sc;
#define X1_OFF() (F_X1 + ((p * RX1 + (xcd >> 1) % RX1) * 32 + (rt >> 2) * 16 + (l & 15)) * SX1)
        unsigned long long xok = 0;
        RowFin fin;
        auto x1_need = [&](unsigned long long m) __attribute__((always_inline)) {
          if (__builtin_expect((xok & m) != m, 0)) {
            for (unsigned spins = 0;;) {
              const u32x2 f = ld8(rs, X1_OFF(), 0);
              xok = __builtin_amdgcn_ballot_w64(f[0] == T[0] && f[1] == T[1]);
              if ((xok & m) == m) break;
              if (spin_fail(spins, rs, T)) { xok = ~0ull; break; }
              __builtin_amdgcn_s_sleep(KSL);
            }
            asm volatile("" ::: "memory");
          }
        };
        auto gap = [&](auto sc_, auto nc_) __attribute__((always_inline)) {
          constexpr int s = decltype(sc_)::value, n = decltype(nc_)::value, ar = a_req(s, n);
          {
            constexpr int tc = (s + 2) / 4;
            if constexpr (tc >= 1 && tc < 8 && s == 4 * tc - 2 && n == 5) {
              if constexpr (!IT || tc >= 3) wait_vm<vm_at(IT, s, n) - vm_tile(IT, tc)>();
              if constexpr (tc == 7 && !IT) asm volatile("s_waitcnt lgkmcnt(4)" ::: "memory");
              asm volatile("s_barrier" ::: "memory");
            }
          }
          if constexpr (IT == 0 && s == 4 * IMT - 3 && n == 5) {
            xr[0] = X_POFF(); xr[1] = xr[0] ^ 32; xr[2] = xr[0] ^ 64; xr[3] = xr[0] ^ 96;
            bf_[0] = lbase + L_RING + l * 16; bf_[1] = bf_[0] + 2 * STAGE;
            asm volatile("" : "+v"(xr[0]), "+v"(xr[1]), "+v"(xr[2]), "+v"(xr[3]), "+v"(bf_[0]), "+v"(bf_[1]));
          }
          if constexpr (IT == 0 && n == 0 && s + 2 < 32) { wait_vm<vm_at(0, s, 0) - vm_unit(s + 2)>(); X_READ(s + 2); SB(); }
          if constexpr (n == 0 && tile_piece(IT, s)) { constexpr int t_ = piece_tile(IT, s), i_ = s - tile_at(IT, t_); QKV_DMA1(t_, i_); }
          if constexpr (IT == 0) {
            if constexpr ((n == 2 || n == 3) && s + 5 < 32) {
              if constexpr (n == 2) asm volatile("s_waitcnt lgkmcnt(%0)" ::"n"(s + 1 < QL ? (s + 1 < 4 * IMT ? 4 : 2) : 1) : "memory");
              X_UNIT1(s + 5, n - 2);
            }
            if constexpr (s >= W2S && s < W2S + 3 && n == 4) QKV_DMA1(2, s - W2S);
            if constexpr (s == LNL && n == 5) LN_LOAD();
            if constexpr (s == 1 && n == 4) {
              asm volatile("" : "+v"(ep)::"memory");
              const unsigned long long e = (((unsigned long long)(unsigned)__builtin_amdgcn_readfirstlane(ep[1]) << 32) | (unsigned)__builtin_amdgcn_readfirstlane(ep[0])) + 1ull + dispatch_id() * 0x9E3779B97F4A7C16ull;
              T = u32x2{(unsigned)e, (unsigned)(e >> 32)};
            }
            if constexpr (s == 0 && n < 2) { X_UNIT1(3 + n, 0); X_UNIT1(3 + n, 1); }
            if constexpr (s == LNS && n == 1) {
              wait_vm<vm_at(0, s, n) - vm_at(0, LNL + 1, 0)>();
              asm volatile("" : "+v"(xl[0]), "+v"(xl[1])::"memory");
              unpack8v(xl[0], lv);
              xs = (lv[0] + lv[1]) + (lv[2] + lv[3]);
              xq = __builtin_elementwise_fma(lv[0], lv[0], lv[1] * lv[1]) + __builtin_elementwise_fma(lv[2], lv[2], lv[3] * lv[3]);
              asm volatile("" : "+v"(xs), "+v"(xq));
            }
            if constexpr (s == LNS && n == 4) {
              unpack8v(xl[1], lv);
              xs += (lv[0] + lv[1]) + (lv[2] + lv[3]);
              xq += __builtin_elementwise_fma(lv[0], lv[0], lv[1] * lv[1]) + __builtin_elementwise_fma(lv[2], lv[2], lv[3] * lv[3]);
              auto t = __builtin_amdgcn_permlane32_swap(__builtin_bit_cast(unsigned, hadd(xs)), __builtin_bit_cast(unsigned, hadd(xq)), false, false);
              lmean = asf(t[0]) + asf(t[1]);
              asm volatile("" : "+v"(lmean));
            }
            if constexpr (s == LNS + 1 && n == 1) { lmean += dppf<0xB1>(lmean); lmean += dppf<0x4E>(lmean); lmean += dppf<0x124>(lmean); asm volatile("" : "+v"(lmean)); }
            if constexpr (s == LNS + 1 && n == 4) { lmean = add_x16(lmean + dppf<0x128>(lmean)) * (1.0f / 1024.0f); asm volatile("" : "+v"(lmean)); }
            if constexpr (s == LNS + 2 && n == 1) {
              const float m2 = asf(__builtin_amdgcn_readlane(__builtin_bit_cast(unsigned, lmean), 32));
              xs[0] = ln_rd(fmaxf(__builtin_fmaf(-lmean, lmean, m2), 0.f));
              asm volatile("" : "+v"(xs));
            }
            if constexpr (s == LNS + 2 && n == 5) {
              if (l == 0) st16(u32x4{__builtin_bit_cast(unsigned, (float)xs[0]), __builtin_bit_cast(unsigned, -rbf1(lmean)), T[0], T[1]}, rs, OFF_AB + (p * 256 + h * 8 + w) * 16, 0);
            }
            if constexpr (s == POI && n == 1) {
              if (w == 1) {
                const unsigned prb = 2 * p + (h >> 4), pct = h & 15;
                unsigned pz1 = POISON;
                asm volatile("" : "+v"(pz1));
                const u32x4 pz = {pz1, pz1, pz1, pz1};
                st16(pz, rs, OFF_STAT + (prb * 16 + pct) * 2048 + l * 16, 0);
                st16(pz, rs, OFF_STAT + (prb * 16 + pct) * 2048 + 1024 + l * 16, 0);
                if (l < 16) st16(pz, rs, OFF_CA + prb * 4096 + pct * 256 + l * 16, 0);
                else if (l == 16 && pct < RKEY) st4(pz1, rs, KEY_AT(0, pct) + prb * SKEY, 0);
              }
            }
            if constexpr (s == POI + 4 && n == 1) { if (w == 1 && l < RX) st8(T, rs, F_XN + (p * RX + l) * 256 + h * 8, 0); }
          } else {
            if constexpr ((s == 0 && n == 0) || (s == 2 && n == 2)) asm volatile("buffer_load_dwordx2 %0, %1, %2, 0 offen sc1" : "=v"(cxn) : "v"(X1_OFF()), "s"(rs) : "memory");
            if constexpr ((s == 1 && n == 3) || (s == 4 && n == 0)) {
              wait_vm<vm_at(1, s, n) - (s == 1 ? vm_at(1, 0, 1) : vm_at(1, 2, 3))>();
              asm volatile("" : "+v"(cxn)::"memory");
              xok |= __builtin_amdgcn_ballot_w64(cxn[0] == T[0] && cxn[1] == T[1]);
            }
            if constexpr (ar >= 0 && (ar & 7) == 0) x1_need(3ull << (((rot + ar / 8) & 7) * 2));
            const unsigned R = (rb * 4 + (w & 3)) * 32 + (w >> 2) * 16 + (l & 15), so = OFF_STAT + rb * 32768;
            if constexpr (s == (SPL < 0 ? 0 : SPL) && n == (SPL < 0 ? 1 : 4)) {
#pragma unroll
              for (int i = 0; i < 4; ++i) ld16h<0>(fx[i], rs, (4 * g + i) * 2048 + (R & 127) * 16, so);
            }
            if constexpr (s == SPL + 2 && n == 1) {
              wait_vm<vm_at(1, s, n) - (SPL < 0 ? vm_at(1, 0, 2) : vm_at(1, SPL, 5))>();
              PINV4(fx);
              fin = fin_key(rs, T, g * 8192 + (R & 127) * 16, so, smem + L_SKEY, l, w, R, wsum, fx);
              count_in(2);
            }
            if constexpr (s == SPL + 3 && n == 0) qc = __hip_atomic_load(qcnt + 2, __ATOMIC_RELAXED, __HIP_MEMORY_SCOPE_WORKGROUP);
            if constexpr (s == SPL + 3 && n == 1) { count_wait(2, 8); fin_cand(rs, smem + L_SKEY, 0, g, rb, ct, fin, v); STAMPAT(51); }
            if constexpr (s == SPL + 3 && n == 3) {
              fin_x2(fin, v);
              x2p[0] = pack8v(v); x2p[1] = pack8v(v + 4);
              asm volatile("" : "+v"(x2p[0]), "+v"(x2p[1]));
            }
            if constexpr (s == SPL + 3 && n == 5) {
              if ((w & 3) * 2 + (w >> 2) == ct >> 1) {
                const float f = 1.0f + fin.rd, rd2 = ln_rd(fin.var * f * f), mb2 = rbf1(__builtin_fmaf(fin.mean - fin.mb, fin.rd, fin.mean));
                if (g == 0 && ((l >> 3) & 1) == (ct & 1))
                  st16(u32x4{__builtin_bit_cast(unsigned, f * rd2), __builtin_bit_cast(unsigned, -__builtin_fmaf(fin.mb, fin.rd, mb2) * __builtin_amdgcn_rcpf(f)), T[0], T[1]}, rs, OFF_AB + 32768 + R * 16, 0);
              }
            }
          }
          if constexpr (IT == 1 && ar >= 0) A_LOAD1(ar / 2, ar & 1);
          if constexpr (!IT && s == CSL && n == 4) { asm volatile("" : "=v"(fx[0])); if (w < 6) ld16h<0>(fx[0], rs, l * 16, OFF_CS + (h * 6 + w) * 1024); }
          if constexpr (!IT && s == CSS && n == 2) {
            if (w < 6) {
              wait_vm<SINCE(CSL)>();
              asm volatile("" : "+v"(fx[0])::"memory");
              tag_wait(rs, T, l * 16, OFF_CS + (h * 6 + w) * 1024, fx[0]);
              const f32x2 c = {sum_hi8(asf(fx[0][0])), sum_hi8(asf(fx[0][1]))};
              if (l < 8) *(LDS_AS f32x2*)(smem + L_CSL + w * 64 + l * 8) = c;
            }
          }
          constexpr int ABL = abl(IT), ABS = abs_(IT);
          if constexpr (s == ABL && n == 4) ld16h<0>(bv[0], rs, (l & 31) * 16, AB_OFF());
          if constexpr (s == ABS && n == 2) {
            wait_vm<SINCE(ABL)>();
            asm volatile("" : "+v"(bv[0])::"memory");
            tag_wait(rs, T, (l & 31) * 16, AB_OFF(), bv[0]);
            const unsigned t0 = bv[0][0], t1 = bv[0][1];
            ((LDS_AS unsigned*)(smem + L_ABW + rt * 2048))[l] = l < 32 ? t0 : t1;
          }
          if constexpr (s == ABS && n == 3) {
#pragma unroll
            for (int a = 0; a < 2; ++a) {
              const LDS_AS unsigned* aw = (const LDS_AS unsigned*)(smem + L_ABW + rt * 2048) + 16 * a;
              aq[a] = aw[l & 15]; bq[a] = aw[32 + (l & 15)];
              av[a] = *(const LDS_AS u32x4*)(aw + 4 * g); bv[a] = *(const LDS_AS u32x4*)(aw + 32 + 4 * g);
            }
          }
          if constexpr (s == ABS && n == 5) {
#pragma unroll
            for (int a = 0; a < 2; ++a) { b2[a] = f32x2{asf(bq[a]), asf(bq[a])}; asm volatile("" : "+v"(b2[a])); }
          }
          if constexpr (s == ABS + 1) {
            constexpr int k = (n + 4) % 6;
            if constexpr (k < 4) cs4[k & 1] = *(const LDS_AS f32x4*)(smem + L_CSL + k * 64 + g * 16);
            else cs4[k & 1][0] = *(const LDS_AS float*)(smem + L_CSL + k * 64 + (l & 15) * 4);
          }
          if constexpr ((s == ABS + 1 && n > 0) || (s == ABS + 2 && n == 0)) {
            constexpr int k = (n + 3) % 6;
            if constexpr (k < 4) {
#pragma unroll
              for (int a = 0; a < 2; ++a) acc[a][k] = __builtin_elementwise_fma(cs4[k & 1], f32x4{b2[a][0], b2[a][1], b2[a][0], b2[a][1]}, acc[a][k]);
            } else {
              f32x2 c2 = {cs4[k & 1][0], cs4[k & 1][0]};
              asm volatile("" : "+v"(c2));
#pragma unroll
              for (int a = 0; a < 2; ++a) acc[a][k] = __builtin_elementwise_fma(f32x4{c2[0], c2[1], c2[0], c2[1]}, __builtin_bit_cast(f32x4, bv[a]), acc[a][k]);
            }
          }
          if constexpr (IT == 0) {
            if constexpr (s == CSD && n < 3) fx[n] = frag0(SLOT(s >> 2), 3 * w + n);
            if constexpr (s == CSD + 1 && n == 1) {
              unsigned one2 = 0x3F803F80u;
              asm volatile("" : "+v"(one2));
              const u32x4 o4 = {one2, one2, one2, one2};
              const f32x4 z4 = {0.f, 0.f, 0.f, 0.f};
              sa = mfma16(fx[0], o4, z4); sb = mfma16(fx[1], o4, z4); sc = mfma16(fx[2], o4, z4);
            }
            constexpr int is_ = im_st(s, n);
            if constexpr (is_ >= 0) st16(fx[is_ < 0 ? 0 : is_], rs, l * 16 + is_ * 1024, OFF_WI + (h * 8 + ((rot + (CSD >> 2)) & 7)) * STAGE + w * 3072);
            if constexpr (s == IMF && n == IMN) {
              wait_vm<vm_at(0, s, n) - vm_at(0, CSD + 1, 1)>();
              kv = 0;
              if (l == 0) kv = __hip_atomic_fetch_add(qcnt + 1, 1u, __ATOMIC_RELAXED, __HIP_MEMORY_SCOPE_WORKGROUP);
            }
            if constexpr (6 * s + n == 6 * IMF + IMN + 2) { if (__builtin_amdgcn_readfirstlane(kv) == 7u && l == 0) st8(T, rs, (h * 8 + p) * 256, F_IM); }
            if constexpr (s == IML && n == 4) { asm volatile("" : "=v"(cxn)); if (w == 0) ld8h(cxn, rs, (l & 7) * 256, F_IM + h * 2048); }
            if constexpr (s == IMK && n == 3) {
              if (w == 0) {
                wait_vm<SINCE(IML)>();
                asm volatile("" : "+v"(cxn)::"memory");
                if (!__all(cxn[0] == T[0] && cxn[1] == T[1])) poll_flags(rs, (l & 7) * 256, F_IM + h * 2048, T);
              }
            }
            if constexpr (s == CSD + 2 && n == 1) {
              const int c = l & 15;
              const f32x4 t0 = sa, t1 = sb, t2 = sc, t = c == 0 ? +t0 : c == 1 ? +t1 : +t2;
              if (c < 3) *(LDS_AS f32x4*)(smem + L_CSP + (w * 3 + c) * 64 + g * 16) = t;
              count_in(2);
            }
            if constexpr (s == CSD + 4 && n == 1) {
              if (w == 2) {
                qc = 0;
                count_wait(2, 8);
                const LDS_AS f32x2* cp = (const LDS_AS f32x2*)(smem + L_CSP) + min(l, 47);
                const f32x2 t = (cp[0] + cp[48]) + (cp[96] + cp[144]);
                if (l < 48) st16(u32x4{__builtin_bit_cast(unsigned, (float)t[0]), __builtin_bit_cast(unsigned, (float)t[1]), T[0], T[1]}, rs, (l >> 3) * 1024 + (l & 7) * 16, OFF_CS + h * 6144 + p * 128);
              }
            }
            if constexpr (s == WOL && n == 5) dma16(rwo, WO_VOFF(), (rb * 8 + w) * 16384 + ct * 128, lbase + L_WOS + w * 1024);
            if constexpr (s == WOS && n == 1) {
              const unsigned o_ = lbase + L_WOS + (w >> 2) * 4096 + (4 * g + ((l & 15) >> 2)) * 128 + (l & 3) * 8 + (((w & 3) ^ (2 * (g & 1) + ((l & 15) >> 3))) << 5);
              frag_t f_;
              f_.hf.a = trabs(o_); f_.hf.b = trabs(o_ + 2048);
              fx[3] = f_.v;
            }
            if constexpr (s == WOS + 1 && n == 5) st16(fx[3], rs, (ct * 128 + rb * 8 + w) * 1024 + l * 16, OFF_WOI);
            if constexpr (s == WOF && n == 1) { if (tid == 128) st8(T, rs, (ct * 16 + rb) * 256, F_WOI); }
            if constexpr (s == WOC && n == 4) { asm volatile("" : "=v"(cx1)); if (w == 3) ld8h(cx1, rs, (l & 15) * 256, F_WOI + ct * 4096); }
            if constexpr (s == WOK && n == 3) {
              if (w == 3) {
                wait_vm<SINCE(WOC)>();
                asm volatile("" : "+v"(cx1)::"memory");
                if (!__all(cx1[0] == T[0] && cx1[1] == T[1])) poll_flags(rs, (l & 15) * 256, F_WOI + ct * 4096, T);
              }
            }
            if constexpr (s >= 29 && (n & 1)) {
              if constexpr (s - 24 < IMT) keep[3 * (s - 29) + (n >> 1)] = frag0(SLOT(s - 24), 3 * w + (n >> 1));
              else keep[3 * (s - 29) + (n >> 1)] = *(const LDS_AS u32x4*)(size_t)(bf_[0] + SLOT(s - 24) * STAGE + w * 3072 + (n >> 1) * 1024);
            }
            if constexpr (s == (TS0 < TEX ? TS0 : TEX) - 2 && n == 1) {
              ARGP(pwg, 5); ARGP(pwu, 6);
              tsrc = (w < 4 ? pwg : pwu) + (bi * 32 + (w & 3) * 8 + (l >> 3)) * F_ + xcd * 448 + min(l & 7, 6) * 64;
              asm volatile("" : "+v"(tsrc));
            }
            if constexpr (s >= TS0 && s < TS0 + TSN + TSD * (7 >> TSG) && n == 4) {
              if (tstep == s && ((TM0 >> w) & 1)) TOUCH(tsrc);
            }
            if constexpr (s == TEX && n == 5) {
              const GAS bf16* t2 = ((TM1 >> w) & 1) ? tsrc : (const GAS bf16*)x + p * 262144;
              TOUCH(t2);
            }
          } else {
            if constexpr (s >= 27 && s < 30 && (n == 0 || n == 3)) WO_PIECE(4 + 2 * (s - 27) + n / 3);
            if constexpr (s == FKL && n == 4) { asm volatile("" : "=v"(kv)); if (w == 0) ld4h<0>(kv, rs, (l & 15) * SKEY, KEY_AT(0, KREP())); }
            if constexpr (s == FSL && n == 1) {
              asm volatile("" : "=v"(fx[0]), "=v"(fx[1]));
              if (w == 0) {
                wait_vm<SINCE(FKL)>();
                asm volatile("" : "+v"(kv)::"memory");
                fkey = __builtin_amdgcn_readfirstlane(umax16(kv == POISON ? 0u : kv));
                ld4h<0>(kv, rs, (l & 15) * SKEY, KEY_AT(0, KREP()));
                ld16h<0>(fx[0], rs, (l >> 3) * 256 + (l & 7) * 16, cand_off(fkey, 0)); ld16h<2048>(fx[1], rs, (l >> 3) * 256 + (l & 7) * 16, cand_off(fkey, 0));
              }
            }
            if constexpr (s == FS0 - 1 && n == FSN) {
              if (w == 0) {
                wait_vm<vm_at(1, s, n) - vm_at(1, FSL, 2)>();
                asm volatile("" : "+v"(fx[0]), "+v"(fx[1]), "+v"(kv)::"memory");
                STAMPAT(52);
                const unsigned k_ = key_wait(rs, T, l, 0, KREP(), kv);
                STAMPAT(53);
                if (l == 0) *(LDS_AS unsigned*)(smem + L_SKEY + 32) = k_;
                if (k_ != fkey) fx[0][0] = POISON;
                word_wait<2>(rs, T, (l >> 3) * 256 + (l & 7) * 16, cand_off(k_, 0), fx);
                *(LDS_AS u32x4*)(smem + L_ROW + l * 16) = fx[0]; *(LDS_AS u32x4*)(smem + L_ROW + 1024 + l * 16) = fx[1];
                count_in(0);
              }
            }
            if constexpr (s == FS0 - 1 && n == 1) {
              asm volatile("" : "=v"(dfr[0]), "=v"(dfr[1]), "=v"(dfr[2]), "=v"(dfr[3]), "=v"(dfr[4]), "=v"(dfr[5]), "=v"(dfr[6]), "=v"(dfr[7]));
              if (ffn_on) {
#pragma unroll
                for (int i = 0; i < 8; ++i) dfr[i] = trread(smem, L_DN + w * 4096 + g * 1024 + ((l & 15) >> 2) * 256 + ((i ^ ((l >> 2) & 7)) * 32) + (l & 3) * 8);
              }
            }
            if constexpr (s == FS0) {
              if constexpr (n == 0) qc = __hip_atomic_load(qcnt, __ATOMIC_RELAXED, __HIP_MEMORY_SCOPE_WORKGROUP);
              if constexpr (n == 1) {
                count_wait(0, 1);
                kv = *(const LDS_AS unsigned*)(smem + L_SKEY + 32);
#pragma unroll
                for (int i = 0; i < 4; ++i) fx[i] = *(const LDS_AS u32x4*)(smem + L_ROW + w * 256 + i * 64 + g * 16);
              }
              if constexpr (n == 2) {
                fkey = __builtin_amdgcn_readfirstlane(kv);
                sel0 = key_row(fkey);
                aff = (int)(sel0 >> 8) == p;
                asm volatile("" : "=v"(sa), "=v"(sb));
                if (ffn_on) ffn_gu(gv, uv, fx, sa, sb);
              }
              if constexpr (n == 4) {
                if (ffn_on) {
                  ffn_gu_store(smem, l, w, sa, sb);
                  count_in(1);
                }
              }
            }
            if constexpr (s == FS0D) {
              if constexpr (n == 0) qc = __hip_atomic_load(qcnt + 1, __ATOMIC_RELAXED, __HIP_MEMORY_SCOPE_WORKGROUP);
              if constexpr (n == 1) { asm volatile("" : "=v"(sa)); if (ffn_on) { count_wait(1, 8); sa = ffn_d_read(smem, l); } }
              if constexpr (n == 2) { asm volatile("" : "=v"(hh)); if (ffn_on) hh = ffn_d_h(sa); }
              if constexpr (n == 4) { asm volatile("" : "=v"(sa)); if (ffn_on) sa = ffn_d_all(dfr, hh); }
              if constexpr (n == 5) { if (ffn_on) ffn_d_store(rs, l, w, 0, slice, sa); STAMPAT(54); }
            }
            if constexpr (s == FS1 && n == 4) {
              DEF4(fx);
              asm volatile("" : "=v"(cxn), "=v"(cx1));
              if (!ffn_on) {
                const unsigned G = xcd * 4 + bi - 28;
#pragma unroll
                for (int k = 0; k < 4; ++k) ld16h<0>(fx[k], rs, RED_VO(k), RED_SO(0, G));
                ld8h(cxn, rs, RED_JJ(), cand_off(fkey, 0) + XO(G)); ld8h(cx1, rs, RED_JJ() + 128, cand_off(fkey, 0) + XO(G));
              }
            }
            if constexpr (s == FS1S && n == 2) {
              if (!ffn_on) {
                wait_vm<SINCE(FS1)>();
                PINV4(fx);
                asm volatile("" : "+v"(cxn), "+v"(cx1)::"memory");
                STAMPAT(55);
                red_sum(rs, smem, T, tid, w, 0, xcd * 4 + bi - 28, fkey, fx, cxn, cx1);
                STAMPAT(56);
                BAR();
                if (w == 0) {
                  const u32x2 ov = red_final(smem, l, red_gate(fkey), cxn, cx1);
                  if (l < 8) st8(unpoison2(ov), rs, OFF_XNEW + (xcd * 4 + bi - 28) * 256 + ((l & 3) * 2 + (l >> 2)) * 8, 0);
                }
              }
            }
            if constexpr (s == FXA && n == 4) { asm volatile("" : "=v"(fx[0])); if (w == 1) ld16h<0>(fx[0], rs, ((l >> 2) & 3) * 256 + (l & 3) * 16, XP_OFF()); }
            if constexpr (s == FXB - 1 && n == FSN) {
              if (w == 1) {
                wait_vm<SINCE(FXA)>();
                asm volatile("" : "+v"(fx[0])::"memory");
                word_wait<1>(rs, T, ((l >> 2) & 3) * 256 + (l & 3) * 16, XP_OFF(), fx);
                if (l < 16) *(LDS_AS u32x4*)(smem + L_ROW + l * 16) = fx[0];
                count_in(0);
              }
            }
            if constexpr (s == FXB) {
#define FX_FRAG(i) (*(const LDS_AS u32x4*)(size_t)(bo[0][0] + STAGE + (6 * (i) + w) * 1024))
              if constexpr (n == 0) { STAMPAT(58); qc = __hip_atomic_load(qcnt, __ATOMIC_RELAXED, __HIP_MEMORY_SCOPE_WORKGROUP); }
              if constexpr (n == 1) {
                DEF4(fx); DEF4(df);
                if (w < 6) {
                  count_wait(0, 2);
#pragma unroll
                  for (int i = 0; i < 4; ++i) { fx[i] = *(const LDS_AS u32x4*)(smem + L_ROW + i * 64 + g * 16); df[i] = FX_FRAG(i); }
                }
              }
              if constexpr (n == 2) { asm volatile("" : "=v"(sa), "=v"(sb)); if (w < 6) dot_mma(fx); }
              if constexpr (n == 4) { if (w < 6) dot_store(OFF_FX); STAMPAT(57); }
            }
            if constexpr (s == FXL && n == 4) {
              asm volatile("" : "=v"(fx[0]), "=v"(fx[1]));
              if (aff && w < 4) { ld16h<0>(fx[0], rs, FP_VO(), FP_OFF(0)); ld16h<0>(fx[1], rs, FP_VO(), FP_OFF(1)); }
            }
            if constexpr (s == FXS && n == 4) {
              if (aff && w < 4) {
                STAMPAT(59);
                wait_vm<0>();
                asm volatile("" : "+v"(fx[0]), "+v"(fx[1])::"memory");
                if (!FX_OK(2)) {
                  unsigned spins = 0;
                  do {
                    if (spin_fail(spins, rs, T)) break;
                    fx[0] = ld16(rs, FP_VO(), FP_OFF(0)); fx[1] = ld16(rs, FP_VO(), FP_OFF(1));
                  } while (!FX_OK(2));
                }
                STAMPAT(60);
                if (w == 3) {
                  f32x2 nx[8];
                  unpack8v(fx[0], nx); unpack8v(fx[1], nx + 4);
                  const f32x2 sm = ((nx[0] + nx[1]) + (nx[2] + nx[3])) + ((nx[4] + nx[5]) + (nx[6] + nx[7]));
                  const float mean = sum64(hadd(sm)) * (1.0f / 1024.0f);
                  const f32x2 m2 = {mean, mean};
                  f32x2 q = {0.f, 0.f};
#pragma unroll
                  for (int j = 0; j < 8; ++j) { const f32x2 d = nx[j] - m2; q = __builtin_elementwise_fma(d, d, q); }
                  const float rd = ln_rd(sum64(hadd(q)) * (1.0f / 1024.0f));
                  if (l == 0) *(LDS_AS f32x2*)(smem + L_FXL + 384) = f32x2{rd, -rbf1(mean)};
                } else {
                  tile_sum(fx[0], L_FXL);
                }
                count_in(3);
              }
            }
          }
        };
        static_for<32>([&](auto sc_) __attribute__((always_inline)) {
          constexpr int s = decltype(sc_)::value;
          if constexpr (s > 0) PIN_ACC();
          if constexpr (IT == 1) wait_vm<vm_at(IT, s, 0) - vm_a(IT, s)>();
          pin_w<s % WA>(af[s % WA][0], af[s % WA][1]);
          VSTEP(s);
          SB();
          static_for<6>([&](auto nc_) __attribute__((always_inline)) {
            constexpr int n = decltype(nc_)::value;
            if constexpr (s == 0) { acc[0][n] = f32x4{0.f, 0.f, 0.f, 0.f}; acc[1][n] = acc[0][n]; }
            constexpr bool on = s < QL || n >= 2;
            if constexpr (on) acc[0][n] = n < 4 ? mfma16(bfr[n].v, af[s % WA][0], acc[0][n]) : mfma16(af[s % WA][0], bfr[n].v, acc[0][n]);
            SB();
            if constexpr (n == 0) { QKV_RDB(s, 5); }
            else if constexpr (s + 1 < 32 && (s + 1 < QL || n > 2)) QKV_RDB(s + 1, n - 1);
            SB();
            if constexpr (on) acc[1][n] = n < 4 ? mfma16(bfr[n].v, af[s % WA][1], acc[1][n]) : mfma16(af[s % WA][1], bfr[n].v, acc[1][n]);
            SB();
            if constexpr (s + 1 < 32 && (s + 1 < QL || n >= 2)) QKV_RDA(s + 1, n);
            SB();
            gap(sc_, nc_);
            GSTEP(s, n);
            SB();
          });
        });
        PIN_ACC();
        VSTEP(32);
        STAMPAT(18);
        VSTEP(33);
        frag_t qw[32 - QL][2];
        static_for<32 - QL>([&](auto c_) __attribute__((always_inline)) {
          constexpr int s = QL + decltype(c_)::value;
          QKV_RDA(s, 0); QKV_RDB(s, 0); QKV_RDA(s, 1); QKV_RDB(s, 1);
          qw[s - QL][0] = bfr[0]; qw[s - QL][1] = bfr[1];
        });
        SB();
        const bool own = aff && (int)((sel0 >> 5) & 7) == rt;
        const bool m0 = !(sel0 & 16), mq = (l & 15) == (int)(sel0 & 15), mv = g == (int)((sel0 & 15) >> 2);
        auto patch_qk = [&](int n) __attribute__((always_inline)) {
          f32x4 t = *(const LDS_AS f32x4*)(smem + L_FXL + n * 64 + g * 16);
          const f32x4 c = *(const LDS_AS f32x4*)(smem + L_CSL + n * 64 + g * 16);
          const float nb = *(const LDS_AS float*)(smem + L_FXL + 388);
#pragma unroll
          for (int e = 0; e < 4; ++e) { t[e] = __builtin_fmaf(c[e], nb, t[e]); asm volatile("" : "+v"(t[e])); }
          acc[0][n] = (mq && m0) ? t : acc[0][n];
          acc[1][n] = (mq && !m0) ? t : acc[1][n];
        };
        if (own) {
          while (__hip_atomic_load(qcnt + 3, __ATOMIC_RELAXED, __HIP_MEMORY_SCOPE_WORKGROUP) < 4u) {}
          asm volatile("" ::: "memory");
          patch_qk(2); patch_qk(3);
          const f32x2 nab = *(const LDS_AS f32x2*)(smem + L_FXL + 384);
          const LDS_AS float* fl_ = (const LDS_AS float*)(smem + L_FXL + 256) + (l & 15);
          const LDS_AS float* cl_ = (const LDS_AS float*)(smem + L_CSL + 256) + (l & 15);
          const float t4 = __builtin_fmaf(cl_[0], nab[1], fl_[0]), t5 = __builtin_fmaf(cl_[16], nab[1], fl_[16]);
          const unsigned na = __builtin_bit_cast(unsigned, (float)nab[0]);
#pragma unroll
          for (int a = 0; a < 2; ++a) {
            const bool ma = a ? !m0 : m0;
            aq[a] = (mq && ma) ? na : aq[a];
#pragma unroll
            for (int e = 0; e < 4; ++e) {
              const bool me = mv && ma && e == (int)(sel0 & 3);
              acc[a][4][e] = me ? t4 : acc[a][4][e]; acc[a][5][e] = me ? t5 : acc[a][5][e];
              av[a][e] = me ? na : av[a][e];
            }
          }
        }
        constexpr float C2 = 1.4426950408889634f * 0.17677669529663687f;
#pragma unroll
        for (int a = 0; a < 2; ++a) {
          f32x2 a2 = {asf(aq[a]), asf(aq[a])};
          qs2[a] = f32x2{asf(aq[a]) * C2, asf(aq[a]) * C2};
          asm volatile("" : "+v"(a2), "+v"(qs2[a]));
          acc[a][2] *= f32x4{a2[0], a2[1], a2[0], a2[1]}; acc[a][3] *= f32x4{a2[0], a2[1], a2[0], a2[1]};
          acc[a][4] *= __builtin_bit_cast(f32x4, av[a]); acc[a][5] *= __builtin_bit_cast(f32x4, av[a]);
        }
        k0 = pack8(acc[0][2], acc[0][3]); k1 = pack8(acc[1][2], acc[1][3]); v0 = pack8(acc[0][4], acc[1][4]); v1 = pack8(acc[0][5], acc[1][5]);
        const unsigned kvo = OFF_KV + IT * (8u << 20) + (p * 32 + h) * 32768 + rt * 4096;
        if (p < 7) { st16(k0, rs, l * 16, kvo); st16(k1, rs, l * 16 + 1024, kvo); st16(v0, rs, l * 16 + 2048, kvo); st16(v1, rs, l * 16 + 3072, kvo); }
        char* ownp = smem + (8 + rt) * 2048 + l * 16;
        *(u32x4*)(ownp + L_K) = k0;
        if (rt < 7) *(u32x4*)(ownp + L_K + 1024) = k1;
        *(u32x4*)(ownp + L_V) = v0; *(u32x4*)(ownp + L_V + 1024) = v1;
        SB();
        static_for<32 - QL>([&](auto c_) __attribute__((always_inline)) {
          constexpr int s = QL + decltype(c_)::value;
#pragma unroll
          for (int n = 0; n < 2; ++n) { acc[0][n] = mfma16(qw[s - QL][n].v, af[s % WA][0], acc[0][n]); acc[1][n] = mfma16(qw[s - QL][n].v, af[s % WA][1], acc[1][n]); }
        });
        if (own) { patch_qk(0); patch_qk(1); }
      };
      qkv_loop(it_c);
    }
#undef A_LOAD1
#undef QKV_RDA
#undef QKV_RDB
#undef QKV_BO
    STAMPAT(2); STAMPAT(21);
    {
      IDS();
      const GAS bf16 *pwg, *pwu;
      asm volatile("s_load_dwordx2 %0, %2, 40\n\ts_load_dwordx2 %1, %2, 48" : "=&s"(pwg), "=&s"(pwu) : "s"(ka));
      const int ql = l & 15;
      const f32x4 z4 = {0.f, 0.f, 0.f, 0.f}, ninf = {-INFINITY, -INFINITY, -INFINITY, -INFINITY};
      LDS_AS unsigned* rdy = (LDS_AS unsigned*)(smem + L_RDY);
      const unsigned rtag = T[0] + it;
      u32x4 qf[2];
      f32x4 cn[2], oa[2][2], sum[2], S[2][2];
      bool told = false;
      auto announce = [&](bool now) __attribute__((always_inline)) {
        if (!told) {
          unsigned st;
          asm volatile("s_getreg_b32 %0, hwreg(HW_REG_IB_STS)" : "=s"(st)::"memory");
          if (now || !(st & 0xC0000Fu)) {
            DRAIN();
            if (l == 0) st8(T, rs, rt * SKW, F_KV + (it * 256 + p * 32 + h) * SKV);
            told = true;
            STAMPAT(34);
          }
        }
      };
      unsigned one2 = 0x3F803F80u;
      asm volatile("" : "+v"(one2));
      const u32x4 ones = {one2, one2, one2, one2};
      auto ex8 = [&](f32x4 a, f32x4 b) __attribute__((always_inline)) {
        return u32x4{pack2(__builtin_amdgcn_exp2f(a[0]), __builtin_amdgcn_exp2f(a[1])), pack2(__builtin_amdgcn_exp2f(a[2]), __builtin_amdgcn_exp2f(a[3])),
                     pack2(__builtin_amdgcn_exp2f(b[0]), __builtin_amdgcn_exp2f(b[1])), pack2(__builtin_amdgcn_exp2f(b[2]), __builtin_amdgcn_exp2f(b[3]))};
      };
      {
#pragma unroll
        for (int a = 0; a < 2; ++a) {
          const f32x4 c4 = {qs2[a][0], qs2[a][1], qs2[a][0], qs2[a][1]};
          qf[a] = pack8(acc[a][0] * c4, acc[a][1] * c4);
        }
        f32x4 mk;
#pragma unroll
        for (int e = 0; e < 4; ++e) mk[e] = (4 * g + e <= ql) ? 0.f : -INFINITY;
        const f32x4 d0 = mfma16(k0, qf[0], mk), d1 = mfma16(k1, qf[1], mk), d2 = mfma16(k0, qf[1], z4);
        const float m0 = -max_x32(max_x16(fmaxf(fmaxf(d0[0], d0[1]), fmaxf(d0[2], d0[3]))));
        const float m1 = -max_x32(max_x16(fmaxf(fmaxf(d1[0], d1[1]), fmaxf(d1[2], d1[3]))));
        cn[0] = f32x4{m0, m0, m0, m0}; cn[1] = f32x4{m1, m1, m1, m1};
        asm volatile("" : "+v"(cn[0]), "+v"(cn[1]));
        announce(false);
        const f32x4 e0 = d0 + cn[0];
        const u32x4 p0 = {pack2(__builtin_amdgcn_exp2f(e0[0]), __builtin_amdgcn_exp2f(e0[1])), pack2(__builtin_amdgcn_exp2f(e0[2]), __builtin_amdgcn_exp2f(e0[3])), 0u, 0u};
        const u32x4 p1 = ex8(d2 + cn[1], d1 + cn[1]);
        oa[0][0] = mfma16(v0, p0, z4); oa[0][1] = mfma16(v1, p0, z4); sum[0] = mfma16(ones, p0, z4);
        oa[1][0] = mfma16(v0, p1, z4); oa[1][1] = mfma16(v1, p1, z4); sum[1] = mfma16(ones, p1, z4);
      }
      const char* kb_ = smem + L_K + l * 16;
      const char* vb_ = smem + L_V + l * 16;
      const unsigned fnv = (l >> 1) * (F_ * 2) + (l & 1) * 16;
      asm volatile("" : "+v"(oa[0][0]), "+v"(oa[0][1]), "+v"(oa[1][0]), "+v"(oa[1][1]), "+v"(sum[0]), "+v"(sum[1]));
      const unsigned pw = __builtin_amdgcn_readfirstlane(it * 256 + p * 32 + h - 32), pfo = F_KV + pw * SKV + rt * SKW;
      const bool fetcher = rt < 4 && p > 0;
      announce(false);
      STAMPAT(32);
      BAR();
      asm volatile("" : "+s"(pwg), "+s"(pwu));
      STAMPAT(33);
#define KV_FETCH(gd)                                                                                                          \
  {                                                                                                                           \
    const unsigned pvo = OFF_KV + pw * 32768 + (gd) * 4096;                                                                   \
    dma16s<0>(rs, l * 16, pvo, lbase + L_K + (gd) * 2048); dma16s<1024>(rs, l * 16, pvo, lbase + L_K + (gd) * 2048);           \
    dma16s<0>(rs, l * 16, pvo + 2048, lbase + L_V + (gd) * 2048); dma16s<1024>(rs, l * 16, pvo + 2048, lbase + L_V + (gd) * 2048); \
  }
      auto fetch = [&]() __attribute__((always_inline)) {
        if (PSK) __builtin_amdgcn_s_sleep(PSK);
        poll_flags(rs, (l & 1) * 4 * SKW, pfo, T);
        KV_FETCH(rt); KV_FETCH(rt + 4);
        if (l == 0) rdy[8 + rt] = rtag;
        STAMPAT(35);
      };
      announce(false);
      const bool first = fetcher && told;
      if (first) fetch();
#define LDS16(a_) (*(const LDS_AS u32x4*)(size_t)(a_))
      u32x4 kf[2], vf[2], p0, p1;
#define AE(a, i) { S[a][(i) >> 2][(i) & 3] = __builtin_amdgcn_exp2f(S[a][(i) >> 2][(i) & 3]); asm volatile("" : "+v"(S[a][(i) >> 2][(i) & 3])); SB(); }
#define AC(a, P, j) { P[j] = pack2(S[a][(j) >> 1][2 * ((j) & 1)], S[a][(j) >> 1][2 * ((j) & 1) + 1]); asm volatile("" : "+v"(P[j])); SB(); }
#define PHASE(a, P, M1, M2, L2, M3, M4, L4, M5)                                                                               \
  AE(a, 0) AE(a, 1) M1; SB(); AE(a, 2) AE(a, 3) M2; SB(); L2; SB(); AC(a, P, 0) AC(a, P, 1) AE(a, 4) M3; SB(); AE(a, 5) AE(a, 6) M4; SB(); \
  L4; SB(); AE(a, 7) AC(a, P, 2) M5; SB(); AC(a, P, 3)
#define PV(a, d, P) oa[a][d] = mfma16(vf[d], P, oa[a][d])
#define RS(a, P) sum[a] = mfma16(ones, P, sum[a])
#define SC(a, t, c) S[a][t] = mfma16(kf[t], qf[a], c)
#define K_AT(ad) (kf[0] = LDS16(ad), kf[1] = LDS16((ad) + 1024))
#define V_AT(ad) (vf[0] = LDS16((ad) + L_V), vf[1] = LDS16((ad) + L_V + 1024))
#define A_FIRST(ad, c00, c01, c10, c11, KN) { K_AT(ad); V_AT(ad); SB(); SC(0, 0, c00); SC(0, 1, c01); SB(); PHASE(0, p0, SC(1, 0, c10), SC(1, 1, c11), KN, , , , ) }
#define A_NEXT(ad, c00, c01, c10, c11, KN)                                                                                        \
  { PHASE(1, p1, PV(0, 0, p0), PV(0, 1, p0), , SC(0, 0, c00), SC(0, 1, c01), , RS(0, p0))                                     \
    PHASE(0, p0, PV(1, 0, p1), PV(1, 1, p1), V_AT(ad), SC(1, 0, c10), SC(1, 1, c11), KN, RS(1, p1)) }
#define A_LAST() { PHASE(1, p1, PV(0, 0, p0), PV(0, 1, p0), , RS(0, p0), , , ) SB(); PV(1, 0, p1); PV(1, 1, p1); RS(1, p1); }
      int nw = 0;
      auto piece = [&]() __attribute__((always_inline)) {
        if constexpr (it == 0) { if (ffn_on && nw < 8) { FFN_DMA(nw); } ++nw; }
      };
#define XN_OFF() (F_XN + (((rt - 6) * 4 + g) * RX + bi % RX) * 256 + (l & 15) * 16)
      u32x4 fxn;
      asm volatile("" : "=v"(fxn));
      unsigned ad = lbase + l * 16 + (6 + rt) * 2048;
      asm volatile("" : "+v"(ad));
      if (rt > 0) {
        A_FIRST(ad + 2048, cn[0], cn[0], cn[1], cn[1], K_AT(ad));
#pragma unroll 1
        for (int i = rt - 1; i > 0; --i) {
          ad -= 2048;
          asm volatile("" : "+v"(ad));
          if (told && rt >= 4) piece();
          announce(false);
          A_NEXT(ad + 2048, cn[0], cn[0], cn[1], cn[1], K_AT(ad));
        }
        announce(false);
        A_LAST();
      }
      announce(true);
      STAMPAT(26);
      {
        f32x4 mk;
#pragma unroll
        for (int e = 0; e < 4; ++e) mk[e] = (4 * g + e > ql) ? 0.f : -INFINITY;
        f32x4 cm0 = cn[0] + mk, cm1 = cn[1] + mk, cmi = ninf;
        asm volatile("" : "+v"(cm0), "+v"(cm1), "+v"(cmi));
        if (rt >= 4) {
          if (p > 0) {
            while (!__all(__hip_atomic_load(rdy + 8 + (l & 3), __ATOMIC_RELAXED, __HIP_MEMORY_SCOPE_WORKGROUP) == rtag)) __builtin_amdgcn_s_sleep(1);
            asm volatile("" ::: "memory");
          }
          if constexpr (it == 0) { for (; nw < 8;) piece(); }
          static_for<it ? 12 : 8>([&](auto c_) __attribute__((always_inline)) { constexpr int i = decltype(c_)::value; WO_PIECE_W(15 - i / 2, 2 * (rt & 3) + (i & 1)); });
          if (it == 0 && rt >= 6) ld16h<0>(fxn, rs, XN_OFF(), 0);
        } else if (p > 0) {
          if (!first) fetch();
          DRAIN();
          if (l < 2) rdy[rt + 4 * l] = rtag;
          if constexpr (it == 0) { for (; nw <= rt;) piece(); }
        }
        STAMPAT(3);
        if (p > 0) {
          unsigned have = 0;
          auto need = [&](int gq) __attribute__((always_inline)) {
            if (__builtin_expect(gq < 8 && !((have >> gq) & 1), 0)) {
              do {
                const unsigned t = __hip_atomic_load(rdy + (l & 7), __ATOMIC_RELAXED, __HIP_MEMORY_SCOPE_WORKGROUP);
                have = (unsigned)__builtin_amdgcn_ballot_w64(t == rtag);
              } while (!((have >> gq) & 1));
              asm volatile("" ::: "memory");
            }
          };
          ad = lbase + l * 16 + rt * 2048;
          asm volatile("" : "+v"(ad));
          need(rt);
          K_AT(ad); V_AT(ad); SB(); SC(0, 0, cm0); SC(0, 1, cn[0]); SB();
          PHASE(0, p0, SC(1, 0, cmi), SC(1, 1, cm1), , , , , )
          need(rt + 1);
          K_AT(ad + 2048);
#pragma unroll 1
          for (int gq = rt + 1; gq < 8; ++gq) {
            ad += 2048;
            asm volatile("" : "+v"(ad));
            piece();
            need(gq + 1);
            A_NEXT(ad, cn[0], cn[0], cn[1], cn[1], K_AT(ad + 2048));
          }
          A_LAST();
        }
        if constexpr (it == 0) { for (; nw < 8;) piece(); }
      }
      STAMPAT(25);
      auto o_store = [&]() __attribute__((always_inline)) {
        const unsigned oo = OFF_O + it * (4u << 20) + ((p * 8 + rt) * 64 + h * 2) * 1024;
#pragma unroll
        for (int a = 0; a < 2; ++a) {
          f32x2 inv = {__builtin_amdgcn_rcpf(sum[a][0]), 0.f};
          inv[1] = inv[0];
          asm volatile("" : "+v"(inv));
          const f32x4 i4 = {inv[0], inv[1], inv[0], inv[1]};
          st16(pack8(oa[a][0] * i4, oa[a][1] * i4), rs, l * 16 + a * 1024, oo);
        }
      };
      {
        float big = 0.f;
#pragma unroll
        for (int a = 0; a < 2; ++a)
#pragma unroll
          for (int d = 0; d < 2; ++d)
            big = __builtin_elementwise_maximum(__builtin_elementwise_maximum(big, __builtin_elementwise_maximum(fabsf(oa[a][d][0]), fabsf(oa[a][d][1]))),
                                                __builtin_elementwise_maximum(fabsf(oa[a][d][2]), fabsf(oa[a][d][3])));
        bool ok = sum[0][0] >= 0.5f && sum[0][0] < 0x1p64f && sum[1][0] >= 0.5f && sum[1][0] < 0x1p64f && big < INFINITY;
        if (!__all(ok)) {
#pragma unroll
          for (int a = 0; a < 2; ++a) {
            const int qs = 2 * rt + a, qq = 256 + 16 * qs + ql, klo = max(qq - 255, p > 0 ? 0 : 256);
            auto score = [&](int t) __attribute__((always_inline)) {
              f32x4 s = mfma16(t == 31 ? k1 : *(const u32x4*)(kb_ + t * 1024), qf[a], z4);
#pragma unroll
              for (int e = 0; e < 4; ++e) { const int kk = 16 * t + 4 * g + e; s[e] = (kk <= qq && kk >= klo) ? s[e] : -INFINITY; }
              return s;
            };
            float mx = -INFINITY;
#pragma unroll 1
            for (int t = p > 0 ? qs : 16; t <= qs + 16; ++t) { const f32x4 s = score(t); mx = fmaxf(fmaxf(mx, fmaxf(s[0], s[1])), fmaxf(s[2], s[3])); }
            mx = max_x32(max_x16(mx));
            oa[a][0] = z4; oa[a][1] = z4; sum[a] = z4;
#pragma unroll 1
            for (int G = p > 0 ? qs >> 1 : 8; G <= (qs + 16) >> 1; ++G) {
              const f32x4 s0 = score(2 * G), s1 = score(2 * G + 1);
              f32x4 e0, e1;
#pragma unroll
              for (int e = 0; e < 4; ++e) { e0[e] = __builtin_amdgcn_exp2f(s0[e] - mx); e1[e] = __builtin_amdgcn_exp2f(s1[e] - mx); }
              const u32x4 pf = pack8(e0, e1);
              oa[a][0] = mfma16(*(const u32x4*)(vb_ + G * 2048), pf, oa[a][0]);
              oa[a][1] = mfma16(*(const u32x4*)(vb_ + G * 2048 + 1024), pf, oa[a][1]);
              sum[a] = mfma16(ones, pf, sum[a]);
            }
          }
        }
      }
      o_store();
      STAMPAT(4);
      if constexpr (it == 0) {
        wait_vm<2>();
        asm volatile("" : "+v"(fxn)::"memory");
        if (rt >= 6 && !__all(fxn[0] == T[0] && fxn[1] == T[1] && fxn[2] == T[0] && fxn[3] == T[1])) poll_flags2(rs, XN_OFF(), T);
        DEF4(gv); DEF4(uv);
        if (ffn_on) {
          const unsigned ro = 65536 + w * 1024 + (4 * g + ((l & 15) >> 2)) * 32 + (l & 3) * 8;
#pragma unroll
          for (int i = 0; i < 4; ++i) {
            frag_t f;
            f.hf.a = trread(smem, ro + i * 16384); f.hf.b = trread(smem, ro + i * 16384 + 512);
            gv[i] = f.v;
            f.hf.a = trread(smem, ro + i * 16384 + 8192); f.hf.b = trread(smem, ro + i * 16384 + 8704);
            uv[i] = f.v;
          }
        }
        PINV4(gv); PINV4(uv);
      }
      BAR();
      if constexpr (it == 1) { WO_PIECE(3); WO_PIECE(2); WO_PIECE(1); WO_PIECE(0); }
      wait_vm<it ? 4 : 0>();
      asm volatile("s_barrier" ::: "memory");
      if (tid == 0) st8(T, rs, h * SO, F_O + (it * 8 + p) * 32 * SO);
      if constexpr (it == 0) {
        WO_PIECE(11); WO_PIECE(10); WO_PIECE(9); WO_PIECE(8); WO_PIECE(7); WO_PIECE(6); WO_PIECE(5); WO_PIECE(4);
        WO_PIECE(3); WO_PIECE(2); WO_PIECE(1); WO_PIECE(0);
      }
    }
    STAMPAT(5);
    IDS();
    ARGP(pwr, 4); ARGP(pwd, 7); ARGP(pout, 8); ARGP(px, 0);
    const int kh = w >> 2, rt2 = rb * 4 + (w & 3);
    const unsigned R = rt2 * 32 + kh * 16 + (l & 15);
    const unsigned to = (rt2 * 64 + ct * 4 + kh) * 1024 + l * 16;
    const unsigned rmo = ct * 64 + ((g & 1) ? 12 + 4 * g : 4 * g);
    f32x2 wv[8];
    {
      u32x4 wa[2], wrp[2], res[2];
#pragma unroll
      for (int j = 0; j < 2; ++j) {
        wrp[j] = *(const GAS u32x4*)(pwr + rmo + 32 * j);
        if constexpr (it == 0) wa[j] = *(const GAS u32x4*)(pwr + l * 8 + j * 512);
        if constexpr (it == 0) res[j] = *(const GAS u32x4*)(px + (R - (l & 15) + 8 * j + (l >> 3)) * 1024 + ct * 64 + (l & 7) * 8);
        else res[j] = ld16(rs, OFF_XNEW + (2 * ct + j) * 256 + g * 16, 0);
      }
      SB();
      if ((it ? PSO : PSO0) && w == 0) __builtin_amdgcn_s_sleep(it ? PSO : PSO0);
      if (w == 0) poll_flags(rs, (l & 31) * SO, F_O + (it * 8 + p) * 32 * SO, T);
      asm volatile("s_barrier" ::: "memory");
      STAMPAT(6);
      auto prep = [&]() __attribute__((always_inline)) {
        if constexpr (it == 0) {
          f32x2 t[8];
          unpack8v(wa[0], t); unpack8v(wa[1], t + 4);
          const f32x2 s = ((t[0] + t[1]) + (t[2] + t[3])) + ((t[4] + t[5]) + (t[6] + t[7]));
          wsum = sum64(hadd(s));
        }
        unpack8v(trade16(wrp[0]), wv); unpack8v(trade16(wrp[1]), wv + 4);
        if constexpr (it == 0) {
          char* tp = smem + w * 2048;
          const unsigned c0 = (g & 1) * 2 + (g >> 1), r = l & 15;
#pragma unroll
          for (int j = 0; j < 2; ++j) *(u32x4*)(tp + j * 1024 + (l >> 3) * 128 + (((l ^ (l >> 3)) & 7) << 4)) = res[j];
          res[0] = trade16(*(const u32x4*)(tp + r * 128 + ((c0 ^ (r & 7)) << 4)));
          res[1] = trade16(*(const u32x4*)(tp + r * 128 + (((c0 + 4) ^ (r & 7)) << 4)));
        }
        else { res[0] = R == sel0 ? res[0] : x2p[0]; res[1] = R == sel0 ? res[1] : x2p[1]; }
        unpack8v(res[0], v + 0); unpack8v(res[1], v + 4);
        asm volatile("" : "+v"(v[0]), "+v"(v[1]), "+v"(v[2]), "+v"(v[3]), "+v"(v[4]), "+v"(v[5]), "+v"(v[6]), "+v"(v[7]));
        asm volatile("" : "+v"(wsum), "+v"(wv[0]), "+v"(wv[1]), "+v"(wv[2]), "+v"(wv[3]), "+v"(wv[4]), "+v"(wv[5]), "+v"(wv[6]), "+v"(wv[7]));
      };
      {
        const unsigned abase = OFF_O + it * (4u << 20) + (rt2 * 64 + kh) * 1024;
        u32x4 af[W2B], bfr[W2L + 1][4];
        unsigned b2[2] = {lbase + 32768 + l * 16, lbase + 98304 + l * 16};
        asm volatile("" : "+v"(b2[0]), "+v"(b2[1]));
#define O_LOAD(s_) af[(s_) % W2B] = ld16(rs, l * 16 + (~(s_) & 1) * 2048, abase + ((31 - (s_)) >> 1) * 4096)
#define WO_RD(s_, n) bfr[(s_) % (W2L + 1)][n] = *(const LDS_AS u32x4*)(size_t)(b2[(31 - (s_)) >> 4] + ((31 - (s_)) & 15) * 4096 + (n) * 1024)
#pragma unroll
        for (int s = 0; s < W2R; ++s) O_LOAD(s);
        SB();
        prep();
        SB();
        f32x4 ac[4];
#pragma unroll
        for (int n = 0; n < 4; ++n) ac[n] = f32x4{v[2 * n][0], v[2 * n][1], v[2 * n + 1][0], v[2 * n + 1][1]};
#pragma unroll
        for (int n = 0; n < 4 * W2L; ++n) WO_RD(n >> 2, n & 3);
#pragma unroll
        for (int s = 0; s < 32; ++s) {
          if (s == W2P && w >= 4) __builtin_amdgcn_s_setprio(1);
          if (s == (it ? 24 : 8) - W2L) asm volatile("s_barrier" ::: "memory");
        if (it == 0 && s == WPS) {
          unsigned pz1 = POISON;
          asm volatile("" : "+v"(pz1));
          const u32x4 pz = {pz1, pz1, pz1, pz1};
          st16(pz, rs, OFF_PART + (tid >> 8) * (1u << 20) + bid * 4096 + (tid & 255) * 16, 0);
          if (w < 2) st16(pz, rs, OFF_STAT + 524288 + (rb * 16 + ct) * 2048 + tid * 16, 0);
          if (w == 4 && l < 40) st16(pz, rs, l < 16 ? OFF_CA + 65536 + rb * 4096 + ct * 256 + l * 16 : OFF_FX + (h * 8 + p) * 512 + (l - 16) * 16, 0);
          if (w == 6 && l < (ct < (int)RKEY ? 3 : 2)) st4(pz1, rs, l < 2 ? OFF_XNEW + (bid >> 3) * 256 + (bid & 7) * 8 + l * 4 : KEY_AT(1, ct) + rb * SKEY, 0);
        }
          if (s < W2B - W2R) O_LOAD(W2R + 2 * s);
          SB();
#pragma unroll
          for (int n = 0; n < 4; ++n) {
            ac[n] = mfma16(bfr[s % (W2L + 1)][n], af[s % W2B], ac[n]);
            if (s + W2L < 32) WO_RD(s + W2L, n);
            SB();
          }
          if (s < W2B - W2R) O_LOAD(W2R + 2 * s + 1);
          else if (s + W2B < 32) O_LOAD(s + W2B);
          SB();
        }
        __builtin_amdgcn_s_setprio(0);
#undef WO_RD
#undef O_LOAD
        STAMPAT(7);
#pragma unroll
        for (int n = 0; n < 4; ++n) { v[2 * n] = f32x2{ac[n][0], ac[n][1]}; v[2 * n + 1] = f32x2{ac[n][2], ac[n][3]}; }
      }
    }
    {
      if (it == 0) { st16(pack8v(v), rs, to, OFF_XN + (4u << 20)); st16(pack8v(v + 4), rs, to + 2048, OFF_XN + (4u << 20)); }
      SB();
      const f32x2 s = ((v[0] + v[1]) + (v[2] + v[3])) + ((v[4] + v[5]) + (v[6] + v[7]));
      f32x2 dp = v[0] * wv[0], q = v[0] * v[0];
#pragma unroll
      for (int j = 1; j < 8; ++j) { dp = __builtin_elementwise_fma(v[j], wv[j], dp); q = __builtin_elementwise_fma(v[j], v[j], q); }
      float mt = hadd(s), qq = hadd(q), dot = hadd(dp);
      add_g3(mt, qq, dot);
      const unsigned so = OFF_STAT + it * 524288 + rb * 32768, lrow = R & 127;
      if (it == 0) wait_vm<2>();
      if (g == 0) st16(unpoison4(__builtin_bit_cast(u32x4, f32x4{mt, qq, dot, 0.f})), rs, ct * 2048 + lrow * 16, so);
      SB();
      if (it == 0) wait_vm<1>();
      if constexpr (it == 0) {
        volatile LDS_AS u32x2* in = (volatile LDS_AS u32x2*)(smem + L_XC);
        if (l == 0) in[w] = T;
        const u32x2 t = in[l & 7];
        if (__all(t[0] == T[0] && t[1] == T[1]) && l < (int)RX1) st8(T, rs, (l * 32 + (rb & 1) * 16 + ct) * SX1, F_X1 + (rb >> 1) * RX1 * 32 * SX1);
      }
      STAMPAT(8);
      if (it == 0) {
        asm volatile("s_barrier" ::: "memory");
        if (ffn_on) { WD_DMA(); }
#pragma unroll
        for (int j = 0; j < 9; ++j) *(u32x4*)(smem + L_RING + (j / 3) * STAGE + (w * 3 + j % 3) * 1024 + l * 16) = keep[j];
      }
      STAMPAT(16);
      if constexpr (it == 0) {
        if (PSX && w == 0) __builtin_amdgcn_s_sleep(PSX);
        if (w == 0) poll_flags(rs, ((l & 2) * 8 + ((ROT0 + 5) & 7) * 2 + (l & 1)) * SX1, F_X1 + (p * RX1 + (xcd >> 1) % RX1) * 32 * SX1, T);
        if (tid == 192) *(LDS_AS u32x4*)(smem + L_QC) = u32x4{0u, 0u, 0u, 0u};
        BAR();
        PRE_LOAD(1, ROT0 + 5, 0, NPRE);
        STAMPAT(9);
      } else {
        __builtin_amdgcn_s_sleep(SDIR);
        u32x4 sp[4];
#pragma unroll
        for (int i = 0; i < 4; ++i) sp[i] = ld16(rs, (4 * g + i) * 2048 + lrow * 16, so);
        const RowFin r = fin_key(rs, T, g * 8192 + lrow * 16, so, smem + L_XC, l, w, R, wsum, sp);
        STAMPAT(9);
        BAR();
        fin_cand(rs, smem + L_XC, 1, g, rb, ct, r, v);
        STAMPAT(10);
        fin_x2(r, v);
      }
    }
    STAMPAT(11);
    if (it == 1) {
      u32x4 op[2];
      {
        char* tp = smem + L_OUT + w * 2048;
        const unsigned c0 = (g & 1) * 2 + (g >> 1), r = l & 15;
        *(u32x4*)(tp + r * 128 + ((c0 ^ (r & 7)) << 4)) = trade16(pack8v(v));
        *(u32x4*)(tp + r * 128 + (((c0 + 4) ^ (r & 7)) << 4)) = trade16(pack8v(v + 4));
#pragma unroll
        for (int j = 0; j < 2; ++j) op[j] = *(const u32x4*)(tp + j * 1024 + (l >> 3) * 128 + (((l ^ (l >> 3)) & 7) << 4));
      }
      const unsigned Ro = R - (l & 15) + (l >> 3), oo = (Ro * 1024 + ct * 64) * 2 + (l & 7) * 16;
#define OUT_STORE(c0, c1)                                                                           \
  {                                                                                                 \
    if (c0) __builtin_amdgcn_raw_buffer_store_b128(op[0], rout, oo, 0, SC1);                        \
    if (c1) __builtin_amdgcn_raw_buffer_store_b128(op[1], rout, oo + 16384, 0, SC1);                \
  }
      if (w == 0) {
        __builtin_amdgcn_s_sleep(GSL);
        u32x4 xr[4];
        unsigned cur = 0, best;
        asm volatile("" : "=v"(xr[0]), "=v"(xr[1]));
        for (unsigned spins = 0;;) {
          const unsigned k_ = key_load(rs, l, 1, KREP());
          const bool all = __all(k_ != POISON);
          best = __builtin_amdgcn_readfirstlane(umax16(k_ == POISON ? 0u : k_));
          if (best != cur && ffn_on) {
            cur = best;
            ld16h<0>(xr[0], rs, (l >> 3) * 256 + (l & 7) * 16, cand_off(cur, 1)); ld16h<2048>(xr[1], rs, (l >> 3) * 256 + (l & 7) * 16, cand_off(cur, 1));
          }
          if (all || spin_fail(spins, rs, T)) break;
        }
        STAMPAT(17);
        if (l == 0) *(LDS_AS unsigned*)(smem + L_SKEY + 32) = best;
        asm volatile("s_waitcnt vmcnt(0)" : "+v"(xr[0]), "+v"(xr[1])::"memory");
        if (ffn_on) {
          word_wait<2>(rs, T, (l >> 3) * 256 + (l & 7) * 16, cand_off(best, 1), xr);
          *(LDS_AS u32x4*)(smem + L_ROW + l * 16) = xr[0]; *(LDS_AS u32x4*)(smem + L_ROW + 1024 + l * 16) = xr[1];
        }
      }
      BAR();
      const unsigned key = __builtin_amdgcn_readfirstlane(*(LDS_AS unsigned*)(smem + L_SKEY + 32)), krow = key_row(key);
      STAMPAT(12);
      if (ffn_on) {
        u32x4 xv[4];
#pragma unroll
        for (int i = 0; i < 4; ++i) xv[i] = *(const LDS_AS u32x4*)(smem + L_ROW + w * 256 + i * 64 + g * 16);
        f32x4 ga, ua;
        ffn_gu(gv, uv, xv, ga, ua);
        ffn_gu_store(smem, l, w, ga, ua);
        BAR();
        ffn_d_store(rs, l, w, 1, slice, ffn_d_all(dfr, ffn_d_h(ffn_d_read(smem, l))));
        STAMPAT(13);
        SB();
      }
      OUT_STORE(Ro != krow, Ro + 8 != krow);
      if (!ffn_on) {
        const unsigned G = xcd * 4 + bi - 28;
        if (G == 0 && tid == 0) st8(T, rs, F_EP, 0);
        u32x4 pv[4];
        u32x2 cxn, cx1;
        const float sig = red_gate(key);
        __builtin_amdgcn_s_sleep(RSL);
        red_load(rs, tid, 1, G, key, pv, cxn, cx1);
        red_sum(rs, smem, T, tid, w, 1, G, key, pv, cxn, cx1);
        STAMPAT(14);
        BAR();
        if (w == 0) {
          const u32x2 ov = red_final(smem, l, sig, cxn, cx1);
          if (l < 8) __builtin_amdgcn_raw_buffer_store_b64(ov, rout, (krow * 1024 + G * 32 + l * 4) * 2, 0, 0);
        }
      }
#undef OUT_STORE
    }
    STAMPAT(15); STAMPAT(23);
  };
  layer(std::integral_constant<int, 0>{});
  layer(std::integral_constant<int, 1>{});
}

extern "C" void kernel_launch(void* const* d_in, const int* in_sizes, int n_in, void* d_out, int out_size,
                              void* d_ws, size_t ws_size, hipStream_t stream) {
  (void)hipFuncSetAttribute((const void*)mega_kernel, hipFuncAttributeMaxDynamicSharedMemorySize, LDS_BYTES);
  mega_kernel<<<256, 512, LDS_BYTES, stream>>>((const bf16*)d_in[0], (char*)d_ws, (const bf16*)d_in[1], (const bf16*)d_in[2], (const bf16*)d_in[4],
                                               (const bf16*)d_in[5], (const bf16*)d_in[6], (const bf16*)d_in[7], (bf16*)d_out);
}
```

```cpp
#include <hip/hip_runtime.h>
#include <stdint.h>
#include <utility>

typedef __bf16 bf16;
typedef __bf16 bf16x8 __attribute__((ext_vector_type(8)));
typedef __bf16 bf16x2 __attribute__((ext_vector_type(2)));
typedef float f32x4 __attribute__((ext_vector_type(4)));
typedef float f32x2 __attribute__((ext_vector_type(2)));
typedef short s16x4 __attribute__((ext_vector_type(4)));
typedef unsigned int u32x4 __attribute__((ext_vector_type(4)));
typedef unsigned int u32x2 __attribute__((ext_vector_type(2)));
#define LDS_AS __attribute__((address_space(3)))
#define GAS __attribute__((address_space(1)))
typedef __amdgpu_buffer_rsrc_t rsrc_t;

constexpr int F_ = 3584;
constexpr int SC1 = 16;
#define PAUX 16

constexpr int RX = 4;
constexpr unsigned F_EP = 0, F_TMO = 128;
constexpr unsigned F_XN = 4096;
#define FLG 256, 1024, 8, 64, 4, 256, 4
constexpr unsigned FLV[7] = {FLG};
constexpr unsigned SO = FLV[0], SKV = FLV[1], SKW = FLV[2], SX1 = FLV[3], RX1 = FLV[4], SKEY = FLV[5], RKEY = FLV[6];
constexpr unsigned F_O = 8u << 20;
constexpr unsigned F_KV = 11u << 20;
constexpr unsigned F_IM = F_O + 524288;
constexpr unsigned F_WOI = F_O + 262144;
constexpr unsigned F_X1 = 9u << 20;
constexpr unsigned OFF_KEY = 10u << 20;
#define KREP() (RKEY == 16 ? bid & 15 : (bid & 7) * RKEY / 8)
#define KEY_AT(L, rep) (OFF_KEY + ((L) * RKEY + (rep)) * (SKEY < 8 ? 128 : 16 * SKEY))
constexpr unsigned OFF_AB = 2u << 20;
constexpr unsigned OFF_XNEW = 262144;
constexpr unsigned OFF_CS = OFF_AB + 65536;
constexpr unsigned OFF_FX = 393216;
constexpr unsigned OFF_CA = 524288;
constexpr unsigned OFF_STAT = 1u << 20;
constexpr unsigned OFF_PART = 3u << 20;
constexpr unsigned OFF_XN = 8u << 20;
constexpr unsigned OFF_O = 16u << 20;
constexpr unsigned OFF_KV = 32u << 20;
constexpr unsigned OFF_WI = 52u << 20;
constexpr unsigned OFF_WOI = 58u << 20;
constexpr unsigned WS_BYTES = 60u << 20;
constexpr unsigned POISON = 0xFFFFFFFFu;

constexpr int L_WOS = 20480, L_ABW = 16640, L_CSP = 17408, L_CSL = 31872, L_FXL = 32256, L_K = 0, L_V = 32768, L_RING = 65536, L_DN = 32768, L_RDY = 31744, L_QC = 31808, LDS_BYTES = 163840;
constexpr int STAGE = 24576;
#define SLOT(t) ((t) & 3)

template <typename F, int... I> __device__ __forceinline__ void static_for_impl(F&& f, std::integer_sequence<int, I...>) {
  (f(std::integral_constant<int, I>{}), ...);
}
template <int N, typename F> __device__ __forceinline__ void static_for(F&& f) { static_for_impl(f, std::make_integer_sequence<int, N>{}); }
__device__ __forceinline__ unsigned pack2(float a, float b) {
  f32x2 v = {a, b};
  return __builtin_bit_cast(unsigned, __builtin_convertvector(v, bf16x2));
}
__device__ __forceinline__ float asf(unsigned u) { return __builtin_bit_cast(float, u); }
__device__ __forceinline__ float lo16(unsigned u) { return __builtin_bit_cast(float, u << 16); }
__device__ __forceinline__ float hi16(unsigned u) { return __builtin_bit_cast(float, u & 0xffff0000u); }
__device__ __forceinline__ u32x4 pack8(const f32x4 a, const f32x4 b) {
  u32x4 o;
  o[0] = pack2(a[0], a[1]); o[1] = pack2(a[2], a[3]); o[2] = pack2(b[0], b[1]); o[3] = pack2(b[2], b[3]);
  return o;
}
__device__ __forceinline__ f32x4 mfma16(u32x4 a, u32x4 b, f32x4 c) {
  return __builtin_amdgcn_mfma_f32_16x16x32_bf16(__builtin_bit_cast(bf16x8, a), __builtin_bit_cast(bf16x8, b), c, 0, 0, 0);
}
__device__ __forceinline__ f32x4 mfma16k16(s16x4 a, s16x4 b, f32x4 c) { return __builtin_amdgcn_mfma_f32_16x16x16bf16_1k(a, b, c, 0, 0, 0); }
template <int CTRL> __device__ __forceinline__ float dppf(float v) {
  return __builtin_bit_cast(float, __builtin_amdgcn_update_dpp(0, __builtin_bit_cast(int, v), CTRL, 0xf, 0xf, false));
}
template <int CTRL> __device__ __forceinline__ unsigned dppu(unsigned v) {
  return (unsigned)__builtin_amdgcn_update_dpp(0, (int)v, CTRL, 0xf, 0xf, false);
}
__device__ __forceinline__ float add_x16(float v) {
  auto r = __builtin_amdgcn_permlane16_swap(__builtin_bit_cast(unsigned, v), __builtin_bit_cast(unsigned, v), false, false);
  return __builtin_bit_cast(float, (unsigned)r[0]) + __builtin_bit_cast(float, (unsigned)r[1]);
}
__device__ __forceinline__ float add_x32(float v) {
  auto r = __builtin_amdgcn_permlane32_swap(__builtin_bit_cast(unsigned, v), __builtin_bit_cast(unsigned, v), false, false);
  return __builtin_bit_cast(float, (unsigned)r[0]) + __builtin_bit_cast(float, (unsigned)r[1]);
}
__device__ __forceinline__ float max_x16(float v) {
  auto r = __builtin_amdgcn_permlane16_swap(__builtin_bit_cast(unsigned, v), __builtin_bit_cast(unsigned, v), false, false);
  return fmaxf(__builtin_bit_cast(float, (unsigned)r[0]), __builtin_bit_cast(float, (unsigned)r[1]));
}
__device__ __forceinline__ float max_x32(float v) {
  auto r = __builtin_amdgcn_permlane32_swap(__builtin_bit_cast(unsigned, v), __builtin_bit_cast(unsigned, v), false, false);
  return fmaxf(__builtin_bit_cast(float, (unsigned)r[0]), __builtin_bit_cast(float, (unsigned)r[1]));
}
__device__ __forceinline__ float sum_hi8(float v) {
  v += dppf<0x128>(v);
  v = add_x16(v);
  return add_x32(v);
}
__device__ __forceinline__ float sum64(float v) {
  v += dppf<0xB1>(v);
  v += dppf<0x4E>(v);
  v += dppf<0x124>(v);
  v += dppf<0x128>(v);
  v = add_x16(v);
  return add_x32(v);
}

#define BAR() asm volatile("s_waitcnt lgkmcnt(0)\n\ts_barrier" ::: "memory")
#define DRAIN() asm volatile("s_waitcnt vmcnt(0)" ::: "memory")
#define SB() __builtin_amdgcn_sched_barrier(0)
template <int N> __device__ __forceinline__ void wait_vm() { asm volatile("s_waitcnt vmcnt(%0)" ::"n"(N) : "memory"); }

__device__ __forceinline__ void dma16(rsrc_t rs, unsigned voff, unsigned soff, unsigned lds) {
  asm volatile("s_mov_b32 m0, %0\n\ts_nop 0\n\tbuffer_load_dwordx4 %1, %2, %3 offen lds" ::"s"(lds), "v"(voff), "s"(rs), "s"(soff) : "memory");
}
__device__ __forceinline__ void dma16n(rsrc_t rs, unsigned voff, unsigned soff, unsigned lds) {
  asm volatile("s_mov_b32 m0, %0\n\ts_nop 0\n\tbuffer_load_dwordx4 %1, %2, %3 offen nt lds" ::"s"(lds), "v"(voff), "s"(rs), "s"(soff) : "memory");
}
template <int OFF> __device__ __forceinline__ void dma16s(rsrc_t rs, unsigned voff, unsigned soff, unsigned lds) {
  asm volatile("s_mov_b32 m0, %0\n\ts_nop 0\n\tbuffer_load_dwordx4 %1, %2, %3 offen offset:%4 sc1 lds" ::"s"(lds), "v"(voff), "s"(rs), "s"(soff), "n"(OFF) : "memory");
}
__device__ __forceinline__ float rbf1(float x) { return __builtin_bit_cast(float, pack2(0.f, x)); }
__device__ __forceinline__ u32x4 ld16(rsrc_t rs, unsigned voff, unsigned soff) { return __builtin_amdgcn_raw_buffer_load_b128(rs, voff, soff, SC1); }
template <int OFF> __device__ __forceinline__ void ld16h(u32x4& d, rsrc_t rs, unsigned voff, unsigned soff) {
  asm volatile("buffer_load_dwordx4 %0, %1, %2, %3 offen offset:%4 sc1" : "=v"(d) : "v"(voff), "s"(rs), "s"(soff), "n"(OFF) : "memory");
}
template <int I, int OFF> __device__ __forceinline__ void ld16w(u32x4& d, rsrc_t rs, unsigned voff, unsigned soff) {
  if constexpr (I == 0) asm volatile("buffer_load_dwordx4 %0, %1, %2, %3 offen offset:%4 sc1" : "={v[128:131]}"(d) : "v"(voff), "s"(rs), "s"(soff), "n"(OFF) : "memory");
  else if constexpr (I == 1) asm volatile("buffer_load_dwordx4 %0, %1, %2, %3 offen offset:%4 sc1" : "={v[132:135]}"(d) : "v"(voff), "s"(rs), "s"(soff), "n"(OFF) : "memory");
  else if constexpr (I == 2) asm volatile("buffer_load_dwordx4 %0, %1, %2, %3 offen offset:%4 sc1" : "={v[136:139]}"(d) : "v"(voff), "s"(rs), "s"(soff), "n"(OFF) : "memory");
  else if constexpr (I == 3) asm volatile("buffer_load_dwordx4 %0, %1, %2, %3 offen offset:%4 sc1" : "={v[140:143]}"(d) : "v"(voff), "s"(rs), "s"(soff), "n"(OFF) : "memory");
  else if constexpr (I == 4) asm volatile("buffer_load_dwordx4 %0, %1, %2, %3 offen offset:%4 sc1" : "={v[144:147]}"(d) : "v"(voff), "s"(rs), "s"(soff), "n"(OFF) : "memory");
  else if constexpr (I == 5) asm volatile("buffer_load_dwordx4 %0, %1, %2, %3 offen offset:%4 sc1" : "={v[148:151]}"(d) : "v"(voff), "s"(rs), "s"(soff), "n"(OFF) : "memory");
  else if constexpr (I == 6) asm volatile("buffer_load_dwordx4 %0, %1, %2, %3 offen offset:%4 sc1" : "={v[152:155]}"(d) : "v"(voff), "s"(rs), "s"(soff), "n"(OFF) : "memory");
  else if constexpr (I == 7) asm volatile("buffer_load_dwordx4 %0, %1, %2, %3 offen offset:%4 sc1" : "={v[156:159]}"(d) : "v"(voff), "s"(rs), "s"(soff), "n"(OFF) : "memory");
  else if constexpr (I == 8) asm volatile("buffer_load_dwordx4 %0, %1, %2, %3 offen offset:%4 sc1" : "={v[160:163]}"(d) : "v"(voff), "s"(rs), "s"(soff), "n"(OFF) : "memory");
  else if constexpr (I == 9) asm volatile("buffer_load_dwordx4 %0, %1, %2, %3 offen offset:%4 sc1" : "={v[164:167]}"(d) : "v"(voff), "s"(rs), "s"(soff), "n"(OFF) : "memory");
  else if constexpr (I == 10) asm volatile("buffer_load_dwordx4 %0, %1, %2, %3 offen offset:%4 sc1" : "={v[168:171]}"(d) : "v"(voff), "s"(rs), "s"(soff), "n"(OFF) : "memory");
  else if constexpr (I == 11) asm volatile("buffer_load_dwordx4 %0, %1, %2, %3 offen offset:%4 sc1" : "={v[172:175]}"(d) : "v"(voff), "s"(rs), "s"(soff), "n"(OFF) : "memory");
}
template <int K> __device__ __forceinline__ void pin_w(u32x4& a, u32x4& b) {
  if constexpr (K == 0) asm volatile("" : "+{v[128:131]}"(a), "+{v[132:135]}"(b)::"memory");
  else if constexpr (K == 1) asm volatile("" : "+{v[136:139]}"(a), "+{v[140:143]}"(b)::"memory");
  else if constexpr (K == 2) asm volatile("" : "+{v[144:147]}"(a), "+{v[148:151]}"(b)::"memory");
  else if constexpr (K == 3) asm volatile("" : "+{v[152:155]}"(a), "+{v[156:159]}"(b)::"memory");
  else if constexpr (K == 4) asm volatile("" : "+{v[160:163]}"(a), "+{v[164:167]}"(b)::"memory");
  else if constexpr (K == 5) asm volatile("" : "+{v[168:171]}"(a), "+{v[172:175]}"(b)::"memory");
}
__device__ __forceinline__ void ld8h(u32x2& d, rsrc_t rs, unsigned voff, unsigned soff) {
  asm volatile("buffer_load_dwordx2 %0, %1, %2, %3 offen sc1" : "=v"(d) : "v"(voff), "s"(rs), "s"(soff) : "memory");
}
template <int OFF> __device__ __forceinline__ void ld4h(unsigned& d, rsrc_t rs, unsigned voff, unsigned soff) {
  asm volatile("buffer_load_dword %0, %1, %2, %3 offen offset:%4 sc1" : "=v"(d) : "v"(voff), "s"(rs), "s"(soff), "n"(OFF) : "memory");
}
__device__ __forceinline__ u32x2 ld8(rsrc_t rs, unsigned voff, unsigned soff) { return __builtin_amdgcn_raw_buffer_load_b64(rs, voff, soff, SC1); }
__device__ __forceinline__ void st16(u32x4 v, rsrc_t rs, unsigned voff, unsigned soff) { __builtin_amdgcn_raw_buffer_store_b128(v, rs, voff + soff, 0, SC1); }
__device__ __forceinline__ void st8(u32x2 v, rsrc_t rs, unsigned voff, unsigned soff) { __builtin_amdgcn_raw_buffer_store_b64(v, rs, voff, soff, SC1); }

__device__ __forceinline__ unsigned ld4(rsrc_t rs, unsigned voff, unsigned soff) { return __builtin_amdgcn_raw_buffer_load_b32(rs, voff, soff, SC1); }
__device__ __forceinline__ void st4(unsigned v, rsrc_t rs, unsigned voff, unsigned soff) { __builtin_amdgcn_raw_buffer_store_b32(v, rs, voff, soff, SC1); }

constexpr unsigned kSpinCap = 1u << 16;
__device__ __forceinline__ bool spin_fail(unsigned& spins, rsrc_t rs, u32x2 T) {
  asm volatile("" ::: "memory");
  ++spins;
  if (spins > kSpinCap) { st8(T, rs, 0, F_TMO); return true; }
  if ((spins & 255u) == 0u) {
    const u32x2 v = ld8(rs, 0, F_TMO);
    if (v[0] == T[0] && v[1] == T[1]) return true;
  }
  __builtin_amdgcn_s_sleep(3);
  return false;
}
__device__ __forceinline__ void poll_flags2(rsrc_t rs, unsigned voff, u32x2 T) {
  for (unsigned spins = 0;;) {
    const u32x4 v = ld16(rs, voff, 0);
    if (__all(v[0] == T[0] && v[1] == T[1] && v[2] == T[0] && v[3] == T[1])) break;
    if (spin_fail(spins, rs, T)) break;
  }
  asm volatile("" ::: "memory");
  __builtin_amdgcn_fence(__ATOMIC_ACQUIRE, "wavefront");
}
__device__ __forceinline__ void tag_wait(rsrc_t rs, u32x2 T, unsigned voff, unsigned soff, u32x4& v) {
  if (__builtin_expect(!__all(v[2] == T[0] && v[3] == T[1]), 0)) {
    unsigned spins = 0;
    do {
      if (spin_fail(spins, rs, T)) break;
      v = ld16(rs, voff, soff);
    } while (!__all(v[2] == T[0] && v[3] == T[1]));
    asm volatile("s_waitcnt vmcnt(0)" : "+v"(v)::"memory");
  }
}
template <int N> __device__ __forceinline__ void word_wait(rsrc_t rs, u32x2 T, unsigned voff, unsigned soff, u32x4 (&v)[4]) {
#define WW_OK() __all(max(max(max(v[0][0], v[0][1]), max(v[0][2], v[0][3])), max(max(v[N - 1][0], v[N - 1][1]), max(v[N - 1][2], v[N - 1][3]))) != POISON)
  if (__builtin_expect(!WW_OK(), 0)) {
    unsigned spins = 0;
    do {
      if (spin_fail(spins, rs, T)) break;
#pragma unroll
      for (int i = 0; i < N; ++i) v[i] = ld16(rs, voff + i * 2048, soff);
    } while (!WW_OK());
    asm volatile("s_waitcnt vmcnt(0)" : "+v"(v[0]), "+v"(v[N - 1])::"memory");
  }
#undef WW_OK
}
__device__ __forceinline__ void poll_flags(rsrc_t rs, unsigned voff, unsigned soff, u32x2 T) {
  for (unsigned spins = 0;;) {
    const u32x2 v = __builtin_amdgcn_raw_buffer_load_b64(rs, voff, soff, PAUX);
    if (__all(v[0] == T[0] && v[1] == T[1])) break;
    if (spin_fail(spins, rs, T)) break;
  }
  asm volatile("" ::: "memory");
  __builtin_amdgcn_fence(__ATOMIC_ACQUIRE, "wavefront");
}

__device__ __forceinline__ void unpack8v(const u32x4 u, f32x2* v) {
#pragma unroll
  for (int j = 0; j < 4; ++j) v[j] = f32x2{lo16(u[j]), hi16(u[j])};
}
__device__ __forceinline__ u32x4 pack8v(const f32x2* v) {
  u32x4 o;
#pragma unroll
  for (int j = 0; j < 4; ++j) o[j] = pack2(v[j][0], v[j][1]);
  return o;
}
__device__ __forceinline__ float ln_rd(float var) { return __builtin_amdgcn_rcpf(rbf1(__builtin_amdgcn_sqrtf(rbf1(rbf1(var) + 1e-5f)))); }
__device__ __forceinline__ float hadd(f32x2 v) {
  float a = v[0];
  asm volatile("" : "+v"(a));
  return a + v[1];
}
__device__ __forceinline__ float add_g(float v) { return add_x32(add_x16(v)); }
__device__ __forceinline__ void add_g3(float& x, float& y, float& z) {
  auto a = __builtin_amdgcn_permlane16_swap(__builtin_bit_cast(unsigned, x), __builtin_bit_cast(unsigned, y), false, false);
  auto b = __builtin_amdgcn_permlane16_swap(__builtin_bit_cast(unsigned, z), __builtin_bit_cast(unsigned, z), false, false);
  const float r = asf(a[0]) + asf(a[1]), t = asf(b[0]) + asf(b[1]);
  auto c = __builtin_amdgcn_permlane32_swap(__builtin_bit_cast(unsigned, r), __builtin_bit_cast(unsigned, r), false, false);
  auto d = __builtin_amdgcn_permlane32_swap(__builtin_bit_cast(unsigned, t), __builtin_bit_cast(unsigned, t), false, false);
  const float u = asf(c[0]) + asf(c[1]);
  z = asf(d[0]) + asf(d[1]);
  auto e = __builtin_amdgcn_permlane16_swap(__builtin_bit_cast(unsigned, u), __builtin_bit_cast(unsigned, u), false, false);
  x = asf(e[0]); y = asf(e[1]);
}
__device__ __forceinline__ u32x4 trade16(u32x4 v) {
  auto a = __builtin_amdgcn_permlane16_swap(v[0], v[2], false, false);
  auto b = __builtin_amdgcn_permlane16_swap(v[1], v[3], false, false);
  return u32x4{(unsigned)a[0], (unsigned)b[0], (unsigned)a[1], (unsigned)b[1]};
}
__device__ __forceinline__ unsigned umax4(u32x4 v) { return max(max(v[0], v[1]), max(v[2], v[3])); }
__device__ __forceinline__ u32x2 unpoison2(u32x2 v) { return u32x2{min(v[0], POISON - 1), min(v[1], POISON - 1)}; }
__device__ __forceinline__ u32x4 unpoison4(u32x4 v) {
  return u32x4{min(v[0], POISON - 1), min(v[1], POISON - 1), min(v[2], POISON - 1), min(v[3], POISON - 1)};
}

typedef union { struct { s16x4 a, b; } hf; u32x4 v; } frag_t;
__device__ __forceinline__ s16x4 trread(const char* smem, unsigned off) {
  return __builtin_amdgcn_ds_read_tr16_b64_v4i16((LDS_AS s16x4*)(smem + off));
}
__device__ __forceinline__ s16x4 trabs(unsigned addr) { return __builtin_amdgcn_ds_read_tr16_b64_v4i16((LDS_AS s16x4*)(size_t)addr); }

constexpr int WA = 6;
#define WPS 16
#define CH 0, 6, 8, 9, 15, 18, 23, 25, 29, 31, 4, 4
constexpr int CHV[12] = {CH};
constexpr int FKL = CHV[10], FSN = CHV[11];
constexpr int SPL = CHV[0];
constexpr int FSL = CHV[1], FS0 = CHV[2], FS0D = CHV[3], FS1 = CHV[4], FS1S = CHV[5];
constexpr int FXA = CHV[6], FXB = CHV[7], FXL = CHV[8], FXS = CHV[9];
#define ROT0 p
#define L0S 22, 25, 0, 0, 24, 28, 2, 2, 4, 14
constexpr int L0V[10] = {L0S};
constexpr int CSL = L0V[0], CSS = L0V[1], CSD = L0V[2];
constexpr int abl(int it) { return it ? 25 : L0V[4]; }
constexpr int abs_(int it) { return it ? 27 : L0V[5]; }
constexpr int W2S = L0V[6], WSH = L0V[7];
constexpr int PRE0 = 13;
constexpr int LNL = L0V[3], LNS = L0V[8], POI = L0V[9];
#define TCH 22, 3, 0x07, 26, 0xF8, 0, 1
constexpr int TCV[7] = {TCH};
constexpr int TS0 = TCV[0], TSN = TCV[1], TM0 = TCV[2], TEX = TCV[3], TM1 = TCV[4], TSD = TCV[5], TSG = TCV[6];
constexpr int QL = 26;
#define SLP 18, 4, 28, 3, 0, 0
constexpr int SLV[6] = {SLP};
constexpr int SDIR = SLV[0], GSL = SLV[1], RSL = SLV[2], KSL = SLV[3];
#define IME 4, 4, 8, 10, 2, 2
constexpr int IMV[6] = {IME};
constexpr int IMB = IMV[4], IMD = IMV[5];
constexpr int im_st(int s, int n) { return IMB == 2 ? (s == CSD && n >= 4 ? n - 4 : s == CSD + 1 && n == 0 ? 2 : -1) : IMB ? (s == CSD + 1 && (n & 1) ? n >> 1 : -1) : (s > CSD && s <= CSD + 3 && n == 5 ? s - CSD - 1 : -1); }
constexpr int IMT = IMV[0], IMF = IMV[1], IML = IMV[2], IMK = IMV[3];
constexpr int tp0(int it) { return it ? 3 : 3 + WSH; }
#define IMN 1
static_assert(IMB == 2 && IMF >= 2 && (IMK & 3) == 2 && IMK < 4 * IMT - 12 + 3 + WSH + IMD && WSH + IMD <= 4, "a wait for a younger request and a barrier in between");
constexpr int tile_at(int it, int t) { return 4 * t - 12 + tp0(it) + (!it && t == IMT ? IMD : !it && t == IMT + 1 && IMD > 1 ? IMD - 1 : 0); }
constexpr int piece_tile(int it, int s) { for (int t = 3; t < 8; ++t) if (s >= tile_at(it, t) && s < tile_at(it, t) + 3) return t; return -1; }
constexpr bool tile_piece(int it, int s) { return piece_tile(it, s) >= 0; }
#define PS 14, 10, 10, 0
constexpr int PSV[4] = {PS};
constexpr int PSX = PSV[0], PSO = PSV[1], PSO0 = PSV[2], PSK = PSV[3];
#define NPRE 3
#define QAD 4
#define WO2 5, 3, 2, 8
constexpr int W2V[4] = {WO2};
#define WOE 1, 7, 15, 19, 22
constexpr int WEV[5] = {WOE};
constexpr int WOL = WEV[0], WOS = WEV[1], WOF = WEV[2], WOC = WEV[3], WOK = WEV[4];
static_assert(((WOS + 1) & ~3) - 2 >= WOL + 4 && ((WOF + 1) & ~3) - 2 >= WOS + 5 && (WOK & 3) == 2, "a wait for a younger request and a barrier in between");
constexpr int W2B = W2V[0], W2R = W2V[1], W2L = W2V[2], W2P = W2V[3];
constexpr int a_req(int s, int n) {
  int r = 2 * NPRE;
  for (int u = 1; u < 192; u += 2) {
    const bool go = r < 64 && (r >> 1) <= u / 6 + QAD && (r < 8 || u >= 9);
    if (u == 6 * s + n) return go ? r : -1;
    r += go;
  }
  return -1;
}
constexpr int vm_n(int it, int s, int n) {
  int c = it ? (a_req(s, n) >= 0) + (s == 0 && n == 0) : (s + 5 < 32 && (n == 2 || n == 3));
  if (n == 0) c += tile_piece(it, s);
  if (n == 4) c += (s == abl(it));
  if (!it) c += (s >= W2S && s < W2S + 3 && n == 4) + (s == LNL && n == 5 ? 2 : 0) + (s == 0 && n < 2 ? 2 : 0) + (s == LNS + 2 && n == 5) + (s == TEX && n == 5) + (s == WOL && n == 5) + (s == WOS + 1 && n == 5) + (im_st(s, n) >= 0);
  else c += (s == 2 && n == 2) + (s == (SPL < 0 ? 0 : SPL) && n == (SPL < 0 ? 1 : 4) ? 4 : 0) + (s >= 27 && s < 30 && (n == 0 || n == 3));
  return c;
}
constexpr int vm_at(int it, int s, int n) {
  int c = it ? 2 * NPRE : PRE0;
  for (int u = 0; u < 6 * s + n; ++u) c += vm_n(it, u / 6, u % 6);
  return c;
}
constexpr int vm_a(int it, int k) {
  if (k < NPRE) return 2 * k + 2;
  for (int u = 0; u < 192; ++u)
    if (a_req(u / 6, u % 6) == 2 * k + 1) return vm_at(it, (u + 1) / 6, (u + 1) % 6);
  return 0;
}
constexpr int vm_tile(int it, int t) { return t >= 3 ? vm_at(it, tile_at(it, t) + 2, 1) : t == 1 ? 12 : vm_at(0, W2S + 2, 5); }
constexpr int vm_unit(int u) { return u < 3 ? 5 + 2 * u : u < 5 ? vm_at(0, 0, u - 2) : vm_at(0, u - 5, 4); }
constexpr int unit_slot(int u) { return u % 3 == 0 ? 0 : 16384 + (u % 3) * 16384; }

constexpr int L_XC = LDS_BYTES - 64, L_SKEY = 32704, L_ROW = 1024, L_OUT = 65536, L_SRED = 4096, L_SFIN = 20480;
__device__ __forceinline__ unsigned key_load(rsrc_t rs, int l, int L, int rep) { return ld4(rs, (l & 15) * SKEY, KEY_AT(L, rep)); }
__device__ __forceinline__ unsigned umax16(unsigned k) {
  k = max(k, dppu<0xB1>(k));
  k = max(k, dppu<0x4E>(k));
  k = max(k, dppu<0x141>(k));
  return max(k, dppu<0x140>(k));
}
__device__ __forceinline__ unsigned key_row(unsigned key) { return (0xffffu - (key & 0xffffu)) & 2047u; }
__device__ __forceinline__ unsigned key_wait(rsrc_t rs, u32x2 T, int l, int L, int rep, unsigned kv) {
  if (!__all(kv != POISON)) {
    unsigned spins = 0;
    do {
      if (spin_fail(spins, rs, T)) break;
      if (L == 0) __builtin_amdgcn_s_sleep(KSL);
      kv = key_load(rs, l, L, rep);
    } while (!__all(kv != POISON));
  }
  return __builtin_amdgcn_readfirstlane(umax16(kv));
}
__device__ __forceinline__ unsigned cand_off(unsigned key, int L) { return OFF_CA + L * 65536 + (key_row(key) >> 7) * 4096; }
#define XO(i) (((i) >> 1) * 256 + ((i) & 1) * 64)
__device__ __forceinline__ void ffn_gu(const u32x4 (&gv)[4], const u32x4 (&uv)[4], const u32x4 (&xv)[4], f32x4& ga, f32x4& ua) {
  ga = mfma16(gv[0], xv[0], f32x4{0.f, 0.f, 0.f, 0.f}); ua = mfma16(uv[0], xv[0], f32x4{0.f, 0.f, 0.f, 0.f});
#pragma unroll
  for (int i = 1; i < 4; ++i) { ga = mfma16(gv[i], xv[i], ga); ua = mfma16(uv[i], xv[i], ua); }
}
__device__ __forceinline__ void ffn_gu_store(char* smem, int l, int w, f32x4 ga, f32x4 ua) {
  LDS_AS f32x4* sred = (LDS_AS f32x4*)(smem + L_SRED);
  if ((l & 15) == 0) { sred[w * 8 + (l >> 4)] = ga; sred[w * 8 + 4 + (l >> 4)] = ua; }
}
__device__ __forceinline__ f32x4 ffn_d_read(char* smem, int l) {
  const LDS_AS float* t = (const LDS_AS float*)(smem + L_SRED) + (l & 8) * 16 + (l & 4) * 4 + (l >> 4) * 4 + (l & 3);
  return f32x4{t[0], t[32], t[64], t[96]};
}
__device__ __forceinline__ u32x2 ffn_d_h(f32x4 t) {
  float x = t[0] + t[1];
  asm volatile("" : "+v"(x));
  x += t[2] + t[3];
  x += dppf<0x128>(x);
  const float y = dppf<0x124>(x);
  const bool up = __builtin_amdgcn_inverse_ballot_w64(0xF0F0F0F0F0F0F0F0ull);
  const float gg = rbf1(up ? y : x), uu = rbf1(up ? x : y);
  const float h = rbf1(gg * __builtin_amdgcn_rcpf(1.0f + __expf(-gg))) * uu;
  const unsigned p = pack2(h, dppf<0xB1>(h));
  return u32x2{dppu<0x00>(p), dppu<0xAA>(p)};
}
template <int t> __device__ __forceinline__ f32x4 ffn_d_mma(s16x4 d, u32x2 hh, f32x4 y) {
  const bool me = __builtin_amdgcn_inverse_ballot_w64(0x0001000100010001ull << t);
  return mfma16k16(d, __builtin_bit_cast(s16x4, u32x2{me ? hh[0] : 0u, me ? hh[1] : 0u}), y);
}
__device__ __forceinline__ f32x4 ffn_d_all(const s16x4 (&dfr)[8], u32x2 hh) {
  f32x4 a = {0.f, 0.f, 0.f, 0.f}, b = a;
  a = ffn_d_mma<0>(dfr[0], hh, a); b = ffn_d_mma<1>(dfr[1], hh, b); a = ffn_d_mma<2>(dfr[2], hh, a); b = ffn_d_mma<3>(dfr[3], hh, b);
  a = ffn_d_mma<4>(dfr[4], hh, a); b = ffn_d_mma<5>(dfr[5], hh, b); a = ffn_d_mma<6>(dfr[6], hh, a); b = ffn_d_mma<7>(dfr[7], hh, b);
  return a + b;
}
__device__ __forceinline__ void ffn_d_store(rsrc_t rs, int l, int w, int L, int slice, f32x4 y) {
  const unsigned c = l & 15;
  if (c < 8) st16(unpoison4(__builtin_bit_cast(u32x4, y)), rs, (2 * w + (c >> 2)) * 65536 + slice * 256 + (c & 3) * 64 + (l >> 4) * 16, OFF_PART + L * (1u << 20));
}
#define RED_VO(k) ((min(tid + 512 * (k), 1791) >> 3) * 256 + (tid & 7) * 16)
#define RED_SO(L, G) (OFF_PART + (L) * (1u << 20) + ((G) >> 1) * 65536 + ((G) & 1) * 128)
#define RED_JJ() ((tid & 3) * 16 + (tid & 4) * 2)
__device__ __forceinline__ void red_load(rsrc_t rs, int tid, int L, unsigned G, unsigned key, u32x4 (&pv)[4], u32x2& cxn, u32x2& cx1) {
#pragma unroll
  for (int k = 0; k < 4; ++k) pv[k] = ld16(rs, RED_VO(k), RED_SO(L, G));
  cxn = ld8(rs, RED_JJ(), cand_off(key, L) + XO(G));
  cx1 = ld8(rs, RED_JJ() + 128, cand_off(key, L) + XO(G));
}
__device__ __forceinline__ void red_sum(rsrc_t rs, char* smem, u32x2 T, int tid, int w, int L, unsigned G, unsigned key,
                                        u32x4 (&pv)[4], u32x2& cxn, u32x2& cx1) {
#define PV_OK() __all(max(max(max(umax4(pv[0]), umax4(pv[1])), max(umax4(pv[2]), umax4(pv[3]))), max(max(cxn[0], cxn[1]), max(cx1[0], cx1[1]))) != POISON)
  if (!PV_OK()) {
    unsigned spins = 0;
    do {
      if (spin_fail(spins, rs, T)) break;
#pragma unroll
      for (int k = 0; k < 4; ++k)
        if (umax4(pv[k]) == POISON) pv[k] = ld16(rs, RED_VO(k), RED_SO(L, G));
      if (max(max(cxn[0], cxn[1]), max(cx1[0], cx1[1])) == POISON) { cxn = ld8(rs, RED_JJ(), cand_off(key, L) + XO(G)); cx1 = ld8(rs, RED_JJ() + 128, cand_off(key, L) + XO(G)); }
    } while (!PV_OK());
  }
#undef PV_OK
  f32x4 s = (__builtin_bit_cast(f32x4, pv[0]) + __builtin_bit_cast(f32x4, pv[1])) + __builtin_bit_cast(f32x4, pv[2]);
  if (w < 4) s += __builtin_bit_cast(f32x4, pv[3]);
#pragma unroll
  for (int j = 0; j < 4; ++j) s[j] = sum_hi8(s[j]);
  if ((tid & 63) < 8) ((LDS_AS f32x4*)(smem + L_SFIN))[w * 8 + (tid & 7)] = s;
}
__device__ __forceinline__ float red_gate(unsigned key) {
  const unsigned k16 = key >> 16;
  const float rmax = __builtin_bit_cast(float, ((k16 & 0x8000u) ? (k16 & 0x7fffu) : (~k16 & 0xffffu)) << 16);
  float sig = rbf1(__builtin_amdgcn_rcpf(1.0f + __expf(-rmax)));
  asm volatile("" : "+v"(sig));
  return sig;
}
__device__ __forceinline__ u32x2 red_final(char* smem, int l, float sig, u32x2 cxn, u32x2 cx1) {
  f32x4 s = ((const LDS_AS f32x4*)(smem + L_SFIN))[l];
#pragma unroll
  for (int j = 0; j < 4; ++j) s[j] = sum_hi8(s[j]);
  const float xn4[4] = {lo16(cxn[0]), hi16(cxn[0]), lo16(cxn[1]), hi16(cxn[1])};
  const float x14[4] = {lo16(cx1[0]), hi16(cx1[0]), lo16(cx1[1]), hi16(cx1[1])};
  float o[4];
#pragma unroll
  for (int j = 0; j < 4; ++j) o[j] = x14[j] + rbf1(xn4[j] + rbf1(rbf1(s[j]) * sig));
  return u32x2{pack2(o[0], o[1]), pack2(o[2], o[3])};
}

struct RowFin { float mean, var, mb, rd; unsigned key; };
__device__ __forceinline__ void fin_wait(rsrc_t rs, u32x2 T, unsigned voff, unsigned so, u32x4 (&sp)[4]) {
#define ST_OK() __all(max(max(umax4(sp[0]), umax4(sp[1])), max(umax4(sp[2]), umax4(sp[3]))) != POISON)
  if (!ST_OK()) {
    unsigned spins = 0;
    do {
      if (spin_fail(spins, rs, T)) break;
#pragma unroll
      for (int i = 0; i < 4; ++i) sp[i] = ld16(rs, voff + i * 2048, so);
    } while (!ST_OK());
  }
#undef ST_OK
}
__device__ __forceinline__ RowFin fin_key(rsrc_t rs, u32x2 T, unsigned voff, unsigned so, char* skey, int l, int w, unsigned R, float wsum, u32x4 (&sp)[4]) {
  RowFin r;
#define ST_SUM() ((__builtin_bit_cast(f32x4, sp[0]) + __builtin_bit_cast(f32x4, sp[1])) + (__builtin_bit_cast(f32x4, sp[2]) + __builtin_bit_cast(f32x4, sp[3])))
  f32x4 sf = ST_SUM();
  const float z = (sf[0] + sf[1]) + sf[2];
  if (__any(z != z)) {
    fin_wait(rs, T, voff, so, sp);
    sf = ST_SUM();
  }
#undef ST_SUM
  float s0 = sf[0], s1 = sf[1], s2 = sf[2];
  add_g3(s0, s1, s2);
  r.mean = s0 * (1.0f / 1024.0f);
  r.var = fmaxf(__builtin_fmaf(-r.mean, r.mean, s1 * (1.0f / 1024.0f)), 0.f);
  r.mb = rbf1(r.mean); r.rd = ln_rd(r.var);
  const unsigned rb16 = pack2((s2 - r.mb * wsum) * r.rd, 0.f) & 0xffffu;
  r.key = min((((rb16 & 0x8000u) ? (~rb16 & 0xffffu) : (rb16 | 0x8000u)) << 16) | (0xffffu - R), POISON - 1);
  const unsigned wk = umax16(r.key);
  if (l == 0) ((LDS_AS unsigned*)skey)[w] = wk;
  return r;
}
__device__ __forceinline__ void fin_cand(rsrc_t rs, char* skey, int L, int g, int rb, int ct, const RowFin& r, const f32x2 (&v)[8]) {
  const u32x4 k0 = *(const LDS_AS u32x4*)skey, k1 = *(const LDS_AS u32x4*)(skey + 16);
  const unsigned best = max(umax4(k0), umax4(k1));
  if (r.key == best) {
    const unsigned co = L * 65536 + rb * 4096 + ct * 256 + g * 16;
    f32x2 t[8], rd2 = {r.rd, r.rd}, of2 = {-r.mb * r.rd, -r.mb * r.rd};
    asm volatile("" : "+v"(rd2), "+v"(of2));
#pragma unroll
    for (int j = 0; j < 8; ++j) t[j] = __builtin_elementwise_fma(v[j], rd2, of2);
    st16(unpoison4(pack8v(t)), rs, co, OFF_CA); st16(unpoison4(pack8v(t + 4)), rs, co + 64, OFF_CA);
    if (g == 0 && ct < (int)RKEY) st4(best, rs, KEY_AT(L, ct) + rb * SKEY, 0);
    st16(unpoison4(pack8v(v)), rs, co + 128, OFF_CA); st16(unpoison4(pack8v(v + 4)), rs, co + 192, OFF_CA);
  }
}
__device__ __forceinline__ void fin_x2(const RowFin& r, f32x2 (&v)[8]) {
  f32x2 sc2 = {1.0f + r.rd, 1.0f + r.rd}, of2 = {-r.mb * r.rd, -r.mb * r.rd};
  asm volatile("" : "+v"(sc2), "+v"(of2));
#pragma unroll
  for (int j = 0; j < 8; ++j) v[j] = __builtin_elementwise_fma(v[j], sc2, of2);
}

extern "C" __device__ unsigned long long dispatch_id() __asm("llvm.amdgcn.dispatch.id");

extern "C" __global__ __launch_bounds__(512, 2) void mega_kernel(const bf16* x, char* ws, const bf16* wqkv, const bf16* wo, const bf16* wr,
                                                                 const bf16* wg, const bf16* wu, const bf16* wd, bf16* out) {
  extern __shared__ __attribute__((aligned(16))) char smem[];
  const int w0_ = __builtin_amdgcn_readfirstlane(threadIdx.x >> 6);
  const unsigned lbase = (unsigned)(size_t)(LDS_AS char*)smem;
  {
    unsigned long long pc;
    unsigned len;
    asm volatile("s_getpc_b64 %0\n\ts_mov_b32 %1, .Ltext_end-mega_kernel\n\t.subsection 1\n.Ltext_end:\n\t.subsection 0" : "=s"(pc), "=s"(len));
    const unsigned o = ((blockIdx.x >> 3) * 64 + (threadIdx.x & 63)) * 128;
    if (w0_ == 7 && o < len)
      asm volatile("s_mov_b32 m0, %0\n\ts_nop 0\n\tglobal_load_lds_dword %1, %2" ::"s"(lbase + L_FXL), "v"(o), "s"(pc & ~127ull) : "memory");
  }
  asm volatile("" : "+s"(x), "+s"(ws), "+s"(wqkv));
#define IDS()                                                                                         \
  int tid = threadIdx.x, w = w0_, bid = blockIdx.x;                                                   \
  asm volatile("" : "+v"(tid));                                                                       \
  asm volatile("" : "+s"(w));                                                                         \
  asm volatile("" : "+s"(bid));                                                                       \
  const int l = tid & 63, xcd = bid & 7, bi = bid >> 3;                                               \
  const int p = (xcd & 1) * 4 + (bi & 3), h = (xcd >> 1) * 8 + (bi >> 2);         \
  const int rb = 2 * p + ((h >> 3) & 1), ct = (h >> 4) * 8 + (h & 7);       \
  const int slice = xcd * 28 + bi;                                                   \
  const bool ffn_on = bi < 28;                                                                        \
  const int g = l >> 4;                                                                               \
  const int rt = w < 4 ? w : 11 - w;          \
  KARGS();                                                                                            \
  (void)p; (void)h; (void)rb; (void)ct; (void)slice; (void)ffn_on; (void)g; (void)rt;
  const rsrc_t rs = __builtin_amdgcn_make_buffer_rsrc((void*)ws, 0, WS_BYTES, 0x00020000);
  typedef const void* const __attribute__((address_space(4)))* karg_t;
#define KARGS() karg_t ka = (karg_t)__builtin_amdgcn_kernarg_segment_ptr(); asm volatile("" : "+s"(ka))
#define ARGP(name, i) const GAS bf16* name = (const GAS bf16*)ka[i]; asm volatile("" : "+s"(name))
#define RSRC(ptr, bytes) __builtin_amdgcn_make_buffer_rsrc((void*)(ptr), 0, bytes, 0x00020000)
#define rwq RSRC(pwq, 1024 * 3072 * 2)
#define rwo RSRC(pwo, 1024 * 1024 * 2)
#define rwg RSRC(pwg, 1024 * F_ * 2)
#define rwu RSRC(pwu, 1024 * F_ * 2)
#define rwd RSRC(pwd, 1024 * F_ * 2)
#define rout RSRC(pout, 2048 * 1024 * 2)

#define WQ_VOFF() ((l >> 2) * 6144 + (((l & 3) ^ ((((l >> 4) ^ (l >> 5)) & 1) << 1)) << 4))
#define WQ_PIECE(t, j, dst) dma16(rwq, wqv, ((((t) + rot) & 7) * 128 + ((j) & 7) * 16) * 6144 + ((j) >> 3) * 2048 + h * 64, lbase + (dst) + (j) * 1024)
#define WQ_BOFF(dt) ((8 * g + ((l & 15) >> 2)) * 64 + (l & 3) * 8 + (((dt) ^ (g & 1)) << 5))
#define WQ_POFF() ((4 * g + ((l & 15) >> 2)) * 64 + (l & 3) * 8 + (((g ^ (g >> 1)) & 1) << 5))
#define X_UNIT1(u, j) dma16(rx, (j) ? xv1 : xv_, (unsigned)(p * 256 + rt * 32 + ((u) & 1) * 16 + (j) * 8) * 2048 + ((((u) >> 2) + rot) & 7) * 256 + (((u) >> 1) & 1) * 128, lbase + rt * 2048 + unit_slot(u) + (j) * 1024)
#define X_VOFF() ((l >> 3) * 2048 + (((l & 7) ^ (l >> 4)) << 4))
#define X_ROFF(hs) (lbase + rt * 2048 + (l & 15) * 128 + (((4 * (hs) + g) ^ ((l >> 1) & 7)) << 4))
#define X_POFF() (lbase + rt * 2048 + (l & 15) * 128 + (((g >> 1) ^ ((l >> 1) & 7)) << 4) + (g & 1) * 8)
#define TOUCH_TO(ptr, dst) asm volatile("s_mov_b32 m0, %0\n\ts_nop 0\n\tglobal_load_lds_dword %1, off" ::"s"(lbase + (dst)), "v"(ptr) : "memory")
#define TOUCH(ptr) TOUCH_TO(ptr, L_FXL)
#define WO_PIECE_W(j, w_)                                                                                                                 \
  {                                                                                                                                       \
    dma16s<0>(rs, l * 16, OFF_WOI + ct * 131072 + (j) * 8192 + (w_) * 1024, lbase + 32768 + (j) * 8192 + (w_) * 1024);                    \
  }
#define WO_PIECE(j) WO_PIECE_W(j, w)
#define WO_VOFF() ((l >> 3) * 2048 + (((((l & 7) >> 1) ^ ((l >> 4) & 3)) * 2 + (l & 1)) << 4))
#define WD_DMA() _Pragma("unroll") for (int i = 0; i < 4; ++i) WD_DMA1(i)
#define WD_DMA1(i)                                                                                   \
      dma16n(rwd, (l >> 4) * 2048 + (((l & 15) * 16) ^ ((l >> 4) * 32) ^ (((i) & 1) * 128)), (unsigned)(slice * 16 + 4 * (i)) * 2048 + w * 256, lbase + L_DN + w * 4096 + (i) * 1024)
#define FFN_DMA(m) dma16((m) & 1 ? rwu : rwg, fnv, (unsigned)(((4 * w + ((m) >> 1)) * 32) * F_ + slice * 16) * 2, lbase + 65536 + (m) * 8192 + w * 1024)
  u32x4 gv[4], uv[4];
  s16x4 dfr[8];
  u32x4 keep[9];
  u32x4 pre[4][2];
#define PRE_LOAD(it_, rot_, s0, s1)                                                                  \
  static_for<(s1) - (s0)>([&](auto c_) __attribute__((always_inline)) {                              \
    constexpr int s = (s0) + decltype(c_)::value;                                                    \
    const unsigned ab_ = OFF_XN + (it_) * (4u << 20) + (((rot_) + (s >> 2)) & 7) * 8192 + ((s >> 1) & 1) * 4096; \
    ld16w<2 * s, (s & 1) * 2048>(pre[s][0], rs, l * 16, ab_ + (p * 8 + rt) * 65536);                         \
    ld16w<2 * s + 1, (s & 1) * 2048 + 1024>(pre[s][1], rs, l * 16, ab_ + (p * 8 + rt) * 65536);                 \
  })
  u32x2 T;
  u32x2 ep;
  u32x4 xl[2];
#define LN_LOAD()                                                                                                                                   \
  {                                                                                                                                                 \
    asm volatile("buffer_load_dwordx4 %0, %1, %2, %3 offen" : "=v"(xl[0]) : "v"(l * 16), "s"(rx), "s"((p * 256 + h * 8 + w) * 2048) : "memory");      \
    asm volatile("buffer_load_dwordx4 %0, %1, %2, %3 offen offset:1024" : "=v"(xl[1]) : "v"(l * 16), "s"(rx), "s"((p * 256 + h * 8 + w) * 2048) : "memory"); \
  }
  const GAS bf16* tsrc;
  {
    IDS();
    const int it = 0, rot = ROT0;
    (void)it;
    const bf16* pwq = wqkv;
    const rsrc_t rx = RSRC(x, 2048 * 1024 * 2);
    const unsigned wqv = WQ_VOFF(), xv_ = X_VOFF(), xv1 = xv_ ^ 64;
    if (tid == 0) *(LDS_AS u32x4*)(smem + L_QC) = u32x4{0u, 0u, 0u, 0u};
    if (w == 0 && l < 24) TOUCH((const GAS bf16*)wqkv + (bid * 24 + l) * 512 + ((3 - (unsigned)((size_t)wqkv >> 7)) & 7) * 64);
#pragma unroll
    for (int i = 0; i < 3; ++i) WQ_PIECE(0, 3 * w + i, L_RING);
    X_UNIT1(0, 0); X_UNIT1(0, 1); X_UNIT1(1, 0); X_UNIT1(1, 1);
    BAR();
    X_UNIT1(2, 0); X_UNIT1(2, 1);
#pragma unroll
    for (int i = 0; i < 3; ++i) WQ_PIECE(1, 3 * w + i, L_RING + STAGE);
    asm volatile("buffer_load_dwordx2 %0, off, %1, 0 sc1" : "=v"(ep) : "s"(rs) : "memory");
  }
  unsigned sel0 = 0x7FFFFFFFu;
  f32x2 v[8];
  u32x4 x2p[2];
  float wsum;

  auto layer = [&](auto it_c) __attribute__((always_inline)) {
    constexpr int it = decltype(it_c)::value;
    f32x4 acc[2][6];
    f32x2 qs2[2];
    u32x4 k0, k1, v0, v1;
    {
      IDS();
      bool aff = false;
      unsigned aq[2];
      u32x4 av[2];
#define PIN_ACC() asm volatile("" : "+{v[176:179]}"(acc[0][0]), "+{v[180:183]}"(acc[0][1]), "+{v[184:187]}"(acc[0][2]), "+{v[188:191]}"(acc[0][3]), "+{v[192:195]}"(acc[0][4]), "+{v[196:199]}"(acc[0][5]), \
                               "+{v[200:203]}"(acc[1][0]), "+{v[204:207]}"(acc[1][1]), "+{v[208:211]}"(acc[1][2]), "+{v[212:215]}"(acc[1][3]), "+{v[216:219]}"(acc[1][4]), "+{v[220:223]}"(acc[1][5]))
#define PINV4(x) asm volatile("" : "+v"(x[0]), "+v"(x[1]), "+v"(x[2]), "+v"(x[3])::"memory")
      auto qkv_loop = [&](auto it_c) __attribute__((always_inline)) {
        constexpr int IT = decltype(it_c)::value;
        const unsigned abase = OFF_XN + IT * (4u << 20) + (p * 8 + rt) * 65536;
        const int rot = ROT0 + 5 * IT;
        unsigned fkey = 0, kv, bq[2];
        u32x4 fx[4], bv[2], af[WA][2];
        u32x2 cxn, cx1, hh;
        f32x4 sa, sb, cs4[2];
        f32x2 b2[2];
#define AB_OFF() (OFF_AB + IT * 32768 + (p * 8 + rt) * 512)
#define A_LOAD1(k, a) ld16w<(k) % WA * 2 + (a), ((k) & 1) * 2048 + (a) * 1024>(af[(k) % WA][a], rs, l * 16, abase + ((((k) >> 2) + rot) & 7) * 8192 + (((k) >> 1) & 1) * 4096)
        if constexpr (IT == 1) {
#pragma unroll
          for (int s = 0; s < NPRE; ++s) { af[s][0] = pre[s][0]; af[s][1] = pre[s][1]; }
        }
        const bf16* pwq = wqkv;
        ARGP(pwo, 3);
        const rsrc_t rx = RSRC(x, 2048 * 1024 * 2);
        unsigned xv_ = X_VOFF(), xv1 = xv_ ^ 64, xr[4] = {X_ROFF(0), X_ROFF(1), 0u, 0u}, bf_[2];
        if constexpr (IT == 0) asm volatile("" : "+v"(xv_), "+v"(xv1), "+v"(xr[0]), "+v"(xr[1]));
#define LDS8(a_) (*(const LDS_AS u32x2*)(size_t)(a_))
#define X_READ(u)                                                                                                              \
  {                                                                                                                            \
    if constexpr ((u) < 4 * IMT) {                                                                                             \
      af[((u) & ~1) % WA][(u) & 1] = *(const LDS_AS u32x4*)(size_t)(xr[0] + unit_slot(u));                                     \
      af[((u) | 1) % WA][(u) & 1] = *(const LDS_AS u32x4*)(size_t)(xr[1] + unit_slot(u));                                      \
    } else {                                                                                                                   \
      const u32x2 a_ = LDS8(xr[0] + unit_slot(u)), b_ = LDS8(xr[1] + unit_slot(u)), c_ = LDS8(xr[2] + unit_slot(u)), d_ = LDS8(xr[3] + unit_slot(u)); \
      af[((u) & ~1) % WA][(u) & 1] = u32x4{a_[0], a_[1], b_[0], b_[1]};                                                        \
      af[((u) | 1) % WA][(u) & 1] = u32x4{c_[0], c_[1], d_[0], d_[1]};                                                         \
    }                                                                                                                          \
  }
        const int tstep = TS0 + bid % TSN + TSD * (w >> TSG);
        const unsigned wqv = WQ_VOFF();
#define QKV_DMA1(t, i)                                                                            \
  {                                                                                               \
    if constexpr (IT == 0 && (t) < IMT) WQ_PIECE(t, 3 * w + (i), L_RING + SLOT(t) * STAGE);       \
    else dma16s<(i) * 1024>(rs, l * 16, OFF_WI + (h * 8 + (((t) + rot) & 7)) * STAGE + w * 3072, lbase + L_RING + SLOT(t) * STAGE + w * 3072); \
  }
        unsigned bo[2][2];
#pragma unroll
        for (int dt = 0; dt < 2 - IT; ++dt) {
          bo[dt][0] = lbase + L_RING + (IT ? l * 16 : WQ_BOFF(dt)); bo[dt][1] = bo[dt][0] + 2 * STAGE;
          asm volatile("" : "+v"(bo[dt][0]), "+v"(bo[dt][1]));
        }
        auto frag0 = [&](int sl, int f) __attribute__((always_inline)) {
          unsigned pb = lbase + L_RING + WQ_POFF();
          asm volatile("" : "+v"(pb));
          const unsigned o = (pb ^ ((f & 1) << 5)) + sl * STAGE + ((f % 6) >> 1) * 8192 + (f / 6) * 2048;
          frag_t t;
          t.hf.a = trabs(o); t.hf.b = trabs(o + 1024);
          return t.v;
        };
        u32x4 df[4];
        auto dot_mma = [&](const u32x4 (&xf)[4]) __attribute__((always_inline)) {
          sa = mfma16(df[0], xf[0], f32x4{0.f, 0.f, 0.f, 0.f}); sb = mfma16(df[1], xf[1], f32x4{0.f, 0.f, 0.f, 0.f});
          sa = mfma16(df[2], xf[2], sa); sb = mfma16(df[3], xf[3], sb);
        };
        auto dot_store = [&](unsigned dst) __attribute__((always_inline)) {
          if ((l & 15) == 0) st16(unpoison4(__builtin_bit_cast(u32x4, sa + sb)), rs, g * 16, dst + (h * 8 + p) * 512 + w * 64);
        };
        auto tile_sum = [&](u32x4 pt, int dst) __attribute__((always_inline)) {
          f32x4 c = __builtin_bit_cast(f32x4, pt);
#pragma unroll
          for (int e = 0; e < 4; ++e) { c[e] += dppf<0x124>(c[e]); c[e] += dppf<0x128>(c[e]); c[e] = add_x16(c[e]); }
          if ((l & 28) == 0) *(LDS_AS f32x4*)(smem + dst + (w * 2 + (l >> 5)) * 64 + (l & 3) * 16) = c;
        };
        frag_t bfr[6];
#define QKV_BO(s, n, x) (bo[((n) ^ (x)) & 1][SLOT((s) >> 2) >> 1] + (SLOT((s) >> 2) & 1) * STAGE + ((n) >> 1) * 8192 + ((s) & 3) * 2048 + (x) * 256)
#define QKV_RDA(s, n)                                                                             \
  {                                                                                               \
    if constexpr (IT == 0 && (s) < 4 * IMT) bfr[n].hf.a = trabs(QKV_BO(s, n, 0));                 \
    else if constexpr (IT == 0) bfr[n].v = *(const LDS_AS u32x4*)(size_t)(bf_[SLOT((s) >> 2) >> 1] + (SLOT((s) >> 2) & 1) * STAGE + (((s) & 3) * 6 + (n)) * 1024); \
    else bfr[n].v = *(const LDS_AS u32x4*)(size_t)(bo[0][SLOT((s) >> 2) >> 1] + (SLOT((s) >> 2) & 1) * STAGE + (((s) & 3) * 6 + (n)) * 1024); \
  }
#define QKV_RDB(s, n) { if constexpr (IT == 0 && (s) < 4 * IMT) bfr[n].hf.b = trabs(QKV_BO(s, n, 1)); }
#define XP_OFF() (OFF_XNEW + ((rot + 5) & 7) * 1024)
#define FP_VO() (w == 3 ? (l >> 2) * 256 + (l & 3) * 16 : ((l >> 2) & 7) * 512 + (l >> 5) * 64 + (l & 3) * 16)
#define FP_OFF(i) (w == 3 ? OFF_XNEW + (i) * 4096 : OFF_FX + h * 4096 + w * 128)
#define FX_OK(n) __all(((n) == 4 ? max(max(umax4(fx[0]), umax4(fx[1])), max(umax4(fx[2]), umax4(fx[3]))) : max(umax4(fx[0]), umax4(fx[1]))) != POISON)
#define SINCE(s0) (vm_at(IT, s, n) - vm_at(IT, s0, 5))
#define DEF4(x) asm volatile("" : "=v"(x[0]), "=v"(x[1]), "=v"(x[2]), "=v"(x[3]))
        LDS_AS unsigned* qcnt = (LDS_AS unsigned*)(smem + L_QC);
        unsigned qc;
        unsigned qadr = lbase + L_QC, qone = 1;
        asm volatile("" : "+v"(qadr), "+v"(qone));
#define count_in(i) asm volatile("s_mov_b64 exec, 1\n\tds_add_u32 %0, %1 offset:%2\n\ts_mov_b64 exec, -1" ::"v"(qadr), "v"(qone), "n"(4 * (i)) : "memory")
        auto count_wait = [&](int i, unsigned want) __attribute__((always_inline)) {
          if (__builtin_expect((unsigned)__builtin_amdgcn_readfirstlane(qc) < want, 0)) {
            while ((unsigned)__builtin_amdgcn_readfirstlane(__hip_atomic_load(qcnt + i, __ATOMIC_RELAXED, __HIP_MEMORY_SCOPE_WORKGROUP)) < want) {}
          }
          asm volatile("" ::: "memory");
        };
        if constexpr (IT == 0) {
          wait_vm<PRE0 - vm_unit(1)>();
          X_READ(0); X_READ(1);
          asm volatile("s_barrier" ::: "memory");
        }
#pragma unroll
        for (int n = 0; n < 6; ++n) { QKV_RDA(0, n); if (n < 5) QKV_RDB(0, n); }
        f32x2 lv[4], xs, xq;
        float lmean;
        f32x4 sc;
#define X1_OFF() (F_X1 + ((p * RX1 + (xcd >> 1) % RX1) * 32 + (rt >> 2) * 16 + (l & 15)) * SX1)
        unsigned long long xok = 0;
        RowFin fin;
        auto x1_need = [&](unsigned long long m) __attribute__((always_inline)) {
          if (__builtin_expect((xok & m) != m, 0)) {
            for (unsigned spins = 0;;) {
              const u32x2 f = ld8(rs, X1_OFF(), 0);
              xok = __builtin_amdgcn_ballot_w64(f[0] == T[0] && f[1] == T[1]);
              if ((xok & m) == m) break;
              if (spin_fail(spins, rs, T)) { xok = ~0ull; break; }
              __builtin_amdgcn_s_sleep(KSL);
            }
            asm volatile("" ::: "memory");
          }
        };
        auto gap = [&](auto sc_, auto nc_) __attribute__((always_inline)) {
          constexpr int s = decltype(sc_)::value, n = decltype(nc_)::value, ar = a_req(s, n);
          {
            constexpr int tc = (s + 2) / 4;
            if constexpr (tc >= 1 && tc < 8 && s == 4 * tc - 2 && n == 5) {
              if constexpr (!IT || tc >= 3) wait_vm<vm_at(IT, s, n) - vm_tile(IT, tc)>();
              if constexpr (tc == 7 && !IT) asm volatile("s_waitcnt lgkmcnt(4)" ::: "memory");
              asm volatile("s_barrier" ::: "memory");
            }
          }
          if constexpr (IT == 0 && s == 4 * IMT - 3 && n == 5) {
            xr[0] = X_POFF(); xr[1] = xr[0] ^ 32; xr[2] = xr[0] ^ 64; xr[3] = xr[0] ^ 96;
            bf_[0] = lbase + L_RING + l * 16; bf_[1] = bf_[0] + 2 * STAGE;
            asm volatile("" : "+v"(xr[0]), "+v"(xr[1]), "+v"(xr[2]), "+v"(xr[3]), "+v"(bf_[0]), "+v"(bf_[1]));
          }
          if constexpr (IT == 0 && n == 0 && s + 2 < 32) { wait_vm<vm_at(0, s, 0) - vm_unit(s + 2)>(); X_READ(s + 2); SB(); }
          if constexpr (n == 0 && tile_piece(IT, s)) { constexpr int t_ = piece_tile(IT, s), i_ = s - tile_at(IT, t_); QKV_DMA1(t_, i_); }
          if constexpr (IT == 0) {
            if constexpr ((n == 2 || n == 3) && s + 5 < 32) {
              if constexpr (n == 2) asm volatile("s_waitcnt lgkmcnt(%0)" ::"n"(s + 1 < QL ? (s + 1 < 4 * IMT ? 4 : 2) : 1) : "memory");
              X_UNIT1(s + 5, n - 2);
            }
            if constexpr (s >= W2S && s < W2S + 3 && n == 4) QKV_DMA1(2, s - W2S);
            if constexpr (s == LNL && n == 5) LN_LOAD();
            if constexpr (s == 1 && n == 4) {
              asm volatile("" : "+v"(ep)::"memory");
              const unsigned long long e = (((unsigned long long)(unsigned)__builtin_amdgcn_readfirstlane(ep[1]) << 32) | (unsigned)__builtin_amdgcn_readfirstlane(ep[0])) + 1ull + dispatch_id() * 0x9E3779B97F4A7C16ull;
              T = u32x2{(unsigned)e, (unsigned)(e >> 32)};
            }
            if constexpr (s == 0 && n < 2) { X_UNIT1(3 + n, 0); X_UNIT1(3 + n, 1); }
            if constexpr (s == LNS && n == 1) {
              wait_vm<vm_at(0, s, n) - vm_at(0, LNL + 1, 0)>();
              asm volatile("" : "+v"(xl[0]), "+v"(xl[1])::"memory");
              unpack8v(xl[0], lv);
              xs = (lv[0] + lv[1]) + (lv[2] + lv[3]);
              xq = __builtin_elementwise_fma(lv[0], lv[0], lv[1] * lv[1]) + __builtin_elementwise_fma(lv[2], lv[2], lv[3] * lv[3]);
              asm volatile("" : "+v"(xs), "+v"(xq));
            }
            if constexpr (s == LNS && n == 4) {
              unpack8v(xl[1], lv);
              xs += (lv[0] + lv[1]) + (lv[2] + lv[3]);
              xq += __builtin_elementwise_fma(lv[0], lv[0], lv[1] * lv[1]) + __builtin_elementwise_fma(lv[2], lv[2], lv[3] * lv[3]);
              auto t = __builtin_amdgcn_permlane32_swap(__builtin_bit_cast(unsigned, hadd(xs)), __builtin_bit_cast(unsigned, hadd(xq)), false, false);
              lmean = asf(t[0]) + asf(t[1]);
              asm volatile("" : "+v"(lmean));
            }
            if constexpr (s == LNS + 1 && n == 1) { lmean += dppf<0xB1>(lmean); lmean += dppf<0x4E>(lmean); lmean += dppf<0x124>(lmean); asm volatile("" : "+v"(lmean)); }
            if constexpr (s == LNS + 1 && n == 4) { lmean = add_x16(lmean + dppf<0x128>(lmean)) * (1.0f / 1024.0f); asm volatile("" : "+v"(lmean)); }
            if constexpr (s == LNS + 2 && n == 1) {
              const float m2 = asf(__builtin_amdgcn_readlane(__builtin_bit_cast(unsigned, lmean), 32));
              xs[0] = ln_rd(fmaxf(__builtin_fmaf(-lmean, lmean, m2), 0.f));
              asm volatile("" : "+v"(xs));
            }
            if constexpr (s == LNS + 2 && n == 5) {
              if (l == 0) st16(u32x4{__builtin_bit_cast(unsigned, (float)xs[0]), __builtin_bit_cast(unsigned, -rbf1(lmean)), T[0], T[1]}, rs, OFF_AB + (p * 256 + h * 8 + w) * 16, 0);
            }
            if constexpr (s == POI && n == 1) {
              if (w == 1) {
                const unsigned prb = 2 * p + (h >> 4), pct = h & 15;
                unsigned pz1 = POISON;
                asm volatile("" : "+v"(pz1));
                const u32x4 pz = {pz1, pz1, pz1, pz1};
                st16(pz, rs, OFF_STAT + (prb * 16 + pct) * 2048 + l * 16, 0);
                st16(pz, rs, OFF_STAT + (prb * 16 + pct) * 2048 + 1024 + l * 16, 0);
                if (l < 16) st16(pz, rs, OFF_CA + prb * 4096 + pct * 256 + l * 16, 0);
                else if (l == 16 && pct < RKEY) st4(pz1, rs, KEY_AT(0, pct) + prb * SKEY, 0);
              }
            }
            if constexpr (s == POI + 4 && n == 1) { if (w == 1 && l < RX) st8(T, rs, F_XN + (p * RX + l) * 256 + h * 8, 0); }
          } else {
            if constexpr ((s == 0 && n == 0) || (s == 2 && n == 2)) asm volatile("buffer_load_dwordx2 %0, %1, %2, 0 offen sc1" : "=v"(cxn) : "v"(X1_OFF()), "s"(rs) : "memory");
            if constexpr ((s == 1 && n == 3) || (s == 4 && n == 0)) {
              wait_vm<vm_at(1, s, n) - (s == 1 ? vm_at(1, 0, 1) : vm_at(1, 2, 3))>();
              asm volatile("" : "+v"(cxn)::"memory");
              xok |= __builtin_amdgcn_ballot_w64(cxn[0] == T[0] && cxn[1] == T[1]);
            }
            if constexpr (ar >= 0 && (ar & 7) == 0) x1_need(3ull << (((rot + ar / 8) & 7) * 2));
            const unsigned R = (rb * 4 + (w & 3)) * 32 + (w >> 2) * 16 + (l & 15), so = OFF_STAT + rb * 32768;
            if constexpr (s == (SPL < 0 ? 0 : SPL) && n == (SPL < 0 ? 1 : 4)) {
#pragma unroll
              for (int i = 0; i < 4; ++i) ld16h<0>(fx[i], rs, (4 * g + i) * 2048 + (R & 127) * 16, so);
            }
            if constexpr (s == SPL + 2 && n == 1) {
              wait_vm<vm_at(1, s, n) - (SPL < 0 ? vm_at(1, 0, 2) : vm_at(1, SPL, 5))>();
              PINV4(fx);
              fin = fin_key(rs, T, g * 8192 + (R & 127) * 16, so, smem + L_SKEY, l, w, R, wsum, fx);
              count_in(2);
            }
            if constexpr (s == SPL + 3 && n == 0) qc = __hip_atomic_load(qcnt + 2, __ATOMIC_RELAXED, __HIP_MEMORY_SCOPE_WORKGROUP);
            if constexpr (s == SPL + 3 && n == 1) { count_wait(2, 8); fin_cand(rs, smem + L_SKEY, 0, g, rb, ct, fin, v); }
            if constexpr (s == SPL + 3 && n == 3) {
              fin_x2(fin, v);
              x2p[0] = pack8v(v); x2p[1] = pack8v(v + 4);
              asm volatile("" : "+v"(x2p[0]), "+v"(x2p[1]));
            }
            if constexpr (s == SPL + 3 && n == 5) {
              if ((w & 3) * 2 + (w >> 2) == ct >> 1) {
                const float f = 1.0f + fin.rd, rd2 = ln_rd(fin.var * f * f), mb2 = rbf1(__builtin_fmaf(fin.mean - fin.mb, fin.rd, fin.mean));
                if (g == 0 && ((l >> 3) & 1) == (ct & 1))
                  st16(u32x4{__builtin_bit_cast(unsigned, f * rd2), __builtin_bit_cast(unsigned, -__builtin_fmaf(fin.mb, fin.rd, mb2) * __builtin_amdgcn_rcpf(f)), T[0], T[1]}, rs, OFF_AB + 32768 + R * 16, 0);
              }
            }
          }
          if constexpr (IT == 1 && ar >= 0) A_LOAD1(ar / 2, ar & 1);
          if constexpr (!IT && s == CSL && n == 4) { asm volatile("" : "=v"(fx[0])); if (w < 6) ld16h<0>(fx[0], rs, l * 16, OFF_CS + (h * 6 + w) * 1024); }
          if constexpr (!IT && s == CSS && n == 2) {
            if (w < 6) {
              wait_vm<SINCE(CSL)>();
              asm volatile("" : "+v"(fx[0])::"memory");
              tag_wait(rs, T, l * 16, OFF_CS + (h * 6 + w) * 1024, fx[0]);
              const f32x2 c = {sum_hi8(asf(fx[0][0])), sum_hi8(asf(fx[0][1]))};
              if (l < 8) *(LDS_AS f32x2*)(smem + L_CSL + w * 64 + l * 8) = c;
            }
          }
          constexpr int ABL = abl(IT), ABS = abs_(IT);
          if constexpr (s == ABL && n == 4) ld16h<0>(bv[0], rs, (l & 31) * 16, AB_OFF());
          if constexpr (s == ABS && n == 2) {
            wait_vm<SINCE(ABL)>();
            asm volatile("" : "+v"(bv[0])::"memory");
            tag_wait(rs, T, (l & 31) * 16, AB_OFF(), bv[0]);
            const unsigned t0 = bv[0][0], t1 = bv[0][1];
            ((LDS_AS unsigned*)(smem + L_ABW + rt * 2048))[l] = l < 32 ? t0 : t1;
          }
          if constexpr (s == ABS && n == 3) {
#pragma unroll
            for (int a = 0; a < 2; ++a) {
              const LDS_AS unsigned* aw = (const LDS_AS unsigned*)(smem + L_ABW + rt * 2048) + 16 * a;
              aq[a] = aw[l & 15]; bq[a] = aw[32 + (l & 15)];
              av[a] = *(const LDS_AS u32x4*)(aw + 4 * g); bv[a] = *(const LDS_AS u32x4*)(aw + 32 + 4 * g);
            }
          }
          if constexpr (s == ABS && n == 5) {
#pragma unroll
            for (int a = 0; a < 2; ++a) { b2[a] = f32x2{asf(bq[a]), asf(bq[a])}; asm volatile("" : "+v"(b2[a])); }
          }
          if constexpr (s == ABS + 1) {
            constexpr int k = (n + 4) % 6;
            if constexpr (k < 4) cs4[k & 1] = *(const LDS_AS f32x4*)(smem + L_CSL + k * 64 + g * 16);
            else cs4[k & 1][0] = *(const LDS_AS float*)(smem + L_CSL + k * 64 + (l & 15) * 4);
          }
          if constexpr ((s == ABS + 1 && n > 0) || (s == ABS + 2 && n == 0)) {
            constexpr int k = (n + 3) % 6;
            if constexpr (k < 4) {
#pragma unroll
              for (int a = 0; a < 2; ++a) acc[a][k] = __builtin_elementwise_fma(cs4[k & 1], f32x4{b2[a][0], b2[a][1], b2[a][0], b2[a][1]}, acc[a][k]);
            } else {
              f32x2 c2 = {cs4[k & 1][0], cs4[k & 1][0]};
              asm volatile("" : "+v"(c2));
#pragma unroll
              for (int a = 0; a < 2; ++a) acc[a][k] = __builtin_elementwise_fma(f32x4{c2[0], c2[1], c2[0], c2[1]}, __builtin_bit_cast(f32x4, bv[a]), acc[a][k]);
            }
          }
          if constexpr (IT == 0) {
            if constexpr (s == CSD && n < 3) fx[n] = frag0(SLOT(s >> 2), 3 * w + n);
            if constexpr (s == CSD + 1 && n == 1) {
              unsigned one2 = 0x3F803F80u;
              asm volatile("" : "+v"(one2));
              const u32x4 o4 = {one2, one2, one2, one2};
              const f32x4 z4 = {0.f, 0.f, 0.f, 0.f};
              sa = mfma16(fx[0], o4, z4); sb = mfma16(fx[1], o4, z4); sc = mfma16(fx[2], o4, z4);
            }
            constexpr int is_ = im_st(s, n);
            if constexpr (is_ >= 0) st16(fx[is_ < 0 ? 0 : is_], rs, l * 16 + is_ * 1024, OFF_WI + (h * 8 + ((rot + (CSD >> 2)) & 7)) * STAGE + w * 3072);
            if constexpr (s == IMF && n == IMN) {
              wait_vm<vm_at(0, s, n) - vm_at(0, CSD + 1, 1)>();
              kv = 0;
              if (l == 0) kv = __hip_atomic_fetch_add(qcnt + 1, 1u, __ATOMIC_RELAXED, __HIP_MEMORY_SCOPE_WORKGROUP);
            }
            if constexpr (6 * s + n == 6 * IMF + IMN + 2) { if (__builtin_amdgcn_readfirstlane(kv) == 7u && l == 0) st8(T, rs, (h * 8 + p) * 256, F_IM); }
            if constexpr (s == IML && n == 4) { asm volatile("" : "=v"(cxn)); if (w == 0) ld8h(cxn, rs, (l & 7) * 256, F_IM + h * 2048); }
            if constexpr (s == IMK && n == 3) {
              if (w == 0) {
                wait_vm<SINCE(IML)>();
                asm volatile("" : "+v"(cxn)::"memory");
                if (!__all(cxn[0] == T[0] && cxn[1] == T[1])) poll_flags(rs, (l & 7) * 256, F_IM + h * 2048, T);
              }
            }
            if constexpr (s == CSD + 2 && n == 1) {
              const int c = l & 15;
              const f32x4 t0 = sa, t1 = sb, t2 = sc, t = c == 0 ? +t0 : c == 1 ? +t1 : +t2;
              if (c < 3) *(LDS_AS f32x4*)(smem + L_CSP + (w * 3 + c) * 64 + g * 16) = t;
              count_in(2);
            }
            if constexpr (s == CSD + 4 && n == 1) {
              if (w == 2) {
                qc = 0;
                count_wait(2, 8);
                const LDS_AS f32x2* cp = (const LDS_AS f32x2*)(smem + L_CSP) + min(l, 47);
                const f32x2 t = (cp[0] + cp[48]) + (cp[96] + cp[144]);
                if (l < 48) st16(u32x4{__builtin_bit_cast(unsigned, (float)t[0]), __builtin_bit_cast(unsigned, (float)t[1]), T[0], T[1]}, rs, (l >> 3) * 1024 + (l & 7) * 16, OFF_CS + h * 6144 + p * 128);
              }
            }
            if constexpr (s == WOL && n == 5) dma16(rwo, WO_VOFF(), (rb * 8 + w) * 16384 + ct * 128, lbase + L_WOS + w * 1024);
            if constexpr (s == WOS && n == 1) {
              const unsigned o_ = lbase + L_WOS + (w >> 2) * 4096 + (4 * g + ((l & 15) >> 2)) * 128 + (l & 3) * 8 + (((w & 3) ^ (2 * (g & 1) + ((l & 15) >> 3))) << 5);
              frag_t f_;
              f_.hf.a = trabs(o_); f_.hf.b = trabs(o_ + 2048);
              fx[3] = f_.v;
            }
            if constexpr (s == WOS + 1 && n == 5) st16(fx[3], rs, (ct * 128 + rb * 8 + w) * 1024 + l * 16, OFF_WOI);
            if constexpr (s == WOF && n == 1) { if (tid == 128) st8(T, rs, (ct * 16 + rb) * 256, F_WOI); }
            if constexpr (s == WOC && n == 4) { asm volatile("" : "=v"(cx1)); if (w == 3) ld8h(cx1, rs, (l & 15) * 256, F_WOI + ct * 4096); }
            if constexpr (s == WOK && n == 3) {
              if (w == 3) {
                wait_vm<SINCE(WOC)>();
                asm volatile("" : "+v"(cx1)::"memory");
                if (!__all(cx1[0] == T[0] && cx1[1] == T[1])) poll_flags(rs, (l & 15) * 256, F_WOI + ct * 4096, T);
              }
            }
            if constexpr (s >= 29 && (n & 1)) {
              if constexpr (s - 24 < IMT) keep[3 * (s - 29) + (n >> 1)] = frag0(SLOT(s - 24), 3 * w + (n >> 1));
              else keep[3 * (s - 29) + (n >> 1)] = *(const LDS_AS u32x4*)(size_t)(bf_[0] + SLOT(s - 24) * STAGE + w * 3072 + (n >> 1) * 1024);
            }
            if constexpr (s == (TS0 < TEX ? TS0 : TEX) - 2 && n == 1) {
              ARGP(pwg, 5); ARGP(pwu, 6);
              tsrc = (w < 4 ? pwg : pwu) + (bi * 32 + (w & 3) * 8 + (l >> 3)) * F_ + xcd * 448 + min(l & 7, 6) * 64;
              asm volatile("" : "+v"(tsrc));
            }
            if constexpr (s >= TS0 && s < TS0 + TSN + TSD * (7 >> TSG) && n == 4) {
              if (tstep == s && ((TM0 >> w) & 1)) TOUCH(tsrc);
            }
            if constexpr (s == TEX && n == 5) {
              const GAS bf16* t2 = ((TM1 >> w) & 1) ? tsrc : (const GAS bf16*)x + p * 262144;
              TOUCH(t2);
            }
          } else {
            if constexpr (s >= 27 && s < 30 && (n == 0 || n == 3)) WO_PIECE(4 + 2 * (s - 27) + n / 3);
            if constexpr (s == FKL && n == 4) { asm volatile("" : "=v"(kv)); if (w == 0) ld4h<0>(kv, rs, (l & 15) * SKEY, KEY_AT(0, KREP())); }
            if constexpr (s == FSL && n == 1) {
              asm volatile("" : "=v"(fx[0]), "=v"(fx[1]));
              if (w == 0) {
                wait_vm<SINCE(FKL)>();
                asm volatile("" : "+v"(kv)::"memory");
                fkey = __builtin_amdgcn_readfirstlane(umax16(kv == POISON ? 0u : kv));
                ld4h<0>(kv, rs, (l & 15) * SKEY, KEY_AT(0, KREP()));
                ld16h<0>(fx[0], rs, (l >> 3) * 256 + (l & 7) * 16, cand_off(fkey, 0)); ld16h<2048>(fx[1], rs, (l >> 3) * 256 + (l & 7) * 16, cand_off(fkey, 0));
              }
            }
            if constexpr (s == FS0 - 1 && n == FSN) {
              if (w == 0) {
                wait_vm<vm_at(1, s, n) - vm_at(1, FSL, 2)>();
                asm volatile("" : "+v"(fx[0]), "+v"(fx[1]), "+v"(kv)::"memory");
                const unsigned k_ = key_wait(rs, T, l, 0, KREP(), kv);
                if (l == 0) *(LDS_AS unsigned*)(smem + L_SKEY + 32) = k_;
                if (k_ != fkey) fx[0][0] = POISON;
                word_wait<2>(rs, T, (l >> 3) * 256 + (l & 7) * 16, cand_off(k_, 0), fx);
                *(LDS_AS u32x4*)(smem + L_ROW + l * 16) = fx[0]; *(LDS_AS u32x4*)(smem + L_ROW + 1024 + l * 16) = fx[1];
                count_in(0);
              }
            }
            if constexpr (s == FS0 - 1 && n == 1) {
              asm volatile("" : "=v"(dfr[0]), "=v"(dfr[1]), "=v"(dfr[2]), "=v"(dfr[3]), "=v"(dfr[4]), "=v"(dfr[5]), "=v"(dfr[6]), "=v"(dfr[7]));
              if (ffn_on) {
#pragma unroll
                for (int i = 0; i < 8; ++i) dfr[i] = trread(smem, L_DN + w * 4096 + g * 1024 + ((l & 15) >> 2) * 256 + ((i ^ ((l >> 2) & 7)) * 32) + (l & 3) * 8);
              }
            }
            if constexpr (s == FS0) {
              if constexpr (n == 0) qc = __hip_atomic_load(qcnt, __ATOMIC_RELAXED, __HIP_MEMORY_SCOPE_WORKGROUP);
              if constexpr (n == 1) {
                count_wait(0, 1);
                kv = *(const LDS_AS unsigned*)(smem + L_SKEY + 32);
#pragma unroll
                for (int i = 0; i < 4; ++i) fx[i] = *(const LDS_AS u32x4*)(smem + L_ROW + w * 256 + i * 64 + g * 16);
              }
              if constexpr (n == 2) {
                fkey = __builtin_amdgcn_readfirstlane(kv);
                sel0 = key_row(fkey);
                aff = (int)(sel0 >> 8) == p;
                asm volatile("" : "=v"(sa), "=v"(sb));
                if (ffn_on) ffn_gu(gv, uv, fx, sa, sb);
              }
              if constexpr (n == 4) {
                if (ffn_on) {
                  ffn_gu_store(smem, l, w, sa, sb);
                  count_in(1);
                }
              }
            }
            if constexpr (s == FS0D) {
              if constexpr (n == 0) qc = __hip_atomic_load(qcnt + 1, __ATOMIC_RELAXED, __HIP_MEMORY_SCOPE_WORKGROUP);
              if constexpr (n == 1) { asm volatile("" : "=v"(sa)); if (ffn_on) { count_wait(1, 8); sa = ffn_d_read(smem, l); } }
              if constexpr (n == 2) { asm volatile("" : "=v"(hh)); if (ffn_on) hh = ffn_d_h(sa); }
              if constexpr (n == 4) { asm volatile("" : "=v"(sa)); if (ffn_on) sa = ffn_d_all(dfr, hh); }
              if constexpr (n == 5) { if (ffn_on) ffn_d_store(rs, l, w, 0, slice, sa); }
            }
            if constexpr (s == FS1 && n == 4) {
              DEF4(fx);
              asm volatile("" : "=v"(cxn), "=v"(cx1));
              if (!ffn_on) {
                const unsigned G = xcd * 4 + bi - 28;
#pragma unroll
                for (int k = 0; k < 4; ++k) ld16h<0>(fx[k], rs, RED_VO(k), RED_SO(0, G));
                ld8h(cxn, rs, RED_JJ(), cand_off(fkey, 0) + XO(G)); ld8h(cx1, rs, RED_JJ() + 128, cand_off(fkey, 0) + XO(G));
              }
            }
            if constexpr (s == FS1S && n == 2) {
              if (!ffn_on) {
                wait_vm<SINCE(FS1)>();
                PINV4(fx);
                asm volatile("" : "+v"(cxn), "+v"(cx1)::"memory");
                red_sum(rs, smem, T, tid, w, 0, xcd * 4 + bi - 28, fkey, fx, cxn, cx1);
                BAR();
                if (w == 0) {
                  const u32x2 ov = red_final(smem, l, red_gate(fkey), cxn, cx1);
                  if (l < 8) st8(unpoison2(ov), rs, OFF_XNEW + (xcd * 4 + bi - 28) * 256 + ((l & 3) * 2 + (l >> 2)) * 8, 0);
                }
              }
            }
            if constexpr (s == FXA && n == 4) { asm volatile("" : "=v"(fx[0])); if (w == 1) ld16h<0>(fx[0], rs, ((l >> 2) & 3) * 256 + (l & 3) * 16, XP_OFF()); }
            if constexpr (s == FXB - 1 && n == FSN) {
              if (w == 1) {
                wait_vm<SINCE(FXA)>();
                asm volatile("" : "+v"(fx[0])::"memory");
                word_wait<1>(rs, T, ((l >> 2) & 3) * 256 + (l & 3) * 16, XP_OFF(), fx);
                if (l < 16) *(LDS_AS u32x4*)(smem + L_ROW + l * 16) = fx[0];
                count_in(0);
              }
            }
            if constexpr (s == FXB) {
#define FX_FRAG(i) (*(const LDS_AS u32x4*)(size_t)(bo[0][0] + STAGE + (6 * (i) + w) * 1024))
              if constexpr (n == 0) { qc = __hip_atomic_load(qcnt, __ATOMIC_RELAXED, __HIP_MEMORY_SCOPE_WORKGROUP); }
              if constexpr (n == 1) {
                DEF4(fx); DEF4(df);
                if (w < 6) {
                  count_wait(0, 2);
#pragma unroll
                  for (int i = 0; i < 4; ++i) { fx[i] = *(const LDS_AS u32x4*)(smem + L_ROW + i * 64 + g * 16); df[i] = FX_FRAG(i); }
                }
              }
              if constexpr (n == 2) { asm volatile("" : "=v"(sa), "=v"(sb)); if (w < 6) dot_mma(fx); }
              if constexpr (n == 4) { if (w < 6) dot_store(OFF_FX); }
            }
            if constexpr (s == FXL && n == 4) {
              asm volatile("" : "=v"(fx[0]), "=v"(fx[1]));
              if (aff && w < 4) { ld16h<0>(fx[0], rs, FP_VO(), FP_OFF(0)); ld16h<0>(fx[1], rs, FP_VO(), FP_OFF(1)); }
            }
            if constexpr (s == FXS && n == 4) {
              if (aff && w < 4) {
                wait_vm<0>();
                asm volatile("" : "+v"(fx[0]), "+v"(fx[1])::"memory");
                if (!FX_OK(2)) {
                  unsigned spins = 0;
                  do {
                    if (spin_fail(spins, rs, T)) break;
                    fx[0] = ld16(rs, FP_VO(), FP_OFF(0)); fx[1] = ld16(rs, FP_VO(), FP_OFF(1));
                  } while (!FX_OK(2));
                }
                if (w == 3) {
                  f32x2 nx[8];
                  unpack8v(fx[0], nx); unpack8v(fx[1], nx + 4);
                  const f32x2 sm = ((nx[0] + nx[1]) + (nx[2] + nx[3])) + ((nx[4] + nx[5]) + (nx[6] + nx[7]));
                  const float mean = sum64(hadd(sm)) * (1.0f / 1024.0f);
                  const f32x2 m2 = {mean, mean};
                  f32x2 q = {0.f, 0.f};
#pragma unroll
                  for (int j = 0; j < 8; ++j) { const f32x2 d = nx[j] - m2; q = __builtin_elementwise_fma(d, d, q); }
                  const float rd = ln_rd(sum64(hadd(q)) * (1.0f / 1024.0f));
                  if (l == 0) *(LDS_AS f32x2*)(smem + L_FXL + 384) = f32x2{rd, -rbf1(mean)};
                } else {
                  tile_sum(fx[0], L_FXL);
                }
                count_in(3);
              }
            }
          }
        };
        static_for<32>([&](auto sc_) __attribute__((always_inline)) {
          constexpr int s = decltype(sc_)::value;
          if constexpr (s > 0) PIN_ACC();
          if constexpr (IT == 1) wait_vm<vm_at(IT, s, 0) - vm_a(IT, s)>();
          pin_w<s % WA>(af[s % WA][0], af[s % WA][1]);
          SB();
          static_for<6>([&](auto nc_) __attribute__((always_inline)) {
            constexpr int n = decltype(nc_)::value;
            if constexpr (s == 0) { acc[0][n] = f32x4{0.f, 0.f, 0.f, 0.f}; acc[1][n] = acc[0][n]; }
            constexpr bool on = s < QL || n >= 2;
            if constexpr (on) acc[0][n] = n < 4 ? mfma16(bfr[n].v, af[s % WA][0], acc[0][n]) : mfma16(af[s % WA][0], bfr[n].v, acc[0][n]);
            SB();
            if constexpr (n == 0) { QKV_RDB(s, 5); }
            else if constexpr (s + 1 < 32 && (s + 1 < QL || n > 2)) QKV_RDB(s + 1, n - 1);
            SB();
            if constexpr (on) acc[1][n] = n < 4 ? mfma16(bfr[n].v, af[s % WA][1], acc[1][n]) : mfma16(af[s % WA][1], bfr[n].v, acc[1][n]);
            SB();
            if constexpr (s + 1 < 32 && (s + 1 < QL || n >= 2)) QKV_RDA(s + 1, n);
            SB();
            gap(sc_, nc_);
            SB();
          });
        });
        PIN_ACC();
        frag_t qw[32 - QL][2];
        static_for<32 - QL>([&](auto c_) __attribute__((always_inline)) {
          constexpr int s = QL + decltype(c_)::value;
          QKV_RDA(s, 0); QKV_RDB(s, 0); QKV_RDA(s, 1); QKV_RDB(s, 1);
          qw[s - QL][0] = bfr[0]; qw[s - QL][1] = bfr[1];
        });
        SB();
        const bool own = aff && (int)((sel0 >> 5) & 7) == rt;
        const bool m0 = !(sel0 & 16), mq = (l & 15) == (int)(sel0 & 15), mv = g == (int)((sel0 & 15) >> 2);
        auto patch_qk = [&](int n) __attribute__((always_inline)) {
          f32x4 t = *(const LDS_AS f32x4*)(smem + L_FXL + n * 64 + g * 16);
          const f32x4 c = *(const LDS_AS f32x4*)(smem + L_CSL + n * 64 + g * 16);
          const float nb = *(const LDS_AS float*)(smem + L_FXL + 388);
#pragma unroll
          for (int e = 0; e < 4; ++e) { t[e] = __builtin_fmaf(c[e], nb, t[e]); asm volatile("" : "+v"(t[e])); }
          acc[0][n] = (mq && m0) ? t : acc[0][n];
          acc[1][n] = (mq && !m0) ? t : acc[1][n];
        };
        if (own) {
          while (__hip_atomic_load(qcnt + 3, __ATOMIC_RELAXED, __HIP_MEMORY_SCOPE_WORKGROUP) < 4u) {}
          asm volatile("" ::: "memory");
          patch_qk(2); patch_qk(3);
          const f32x2 nab = *(const LDS_AS f32x2*)(smem + L_FXL + 384);
          const LDS_AS float* fl_ = (const LDS_AS float*)(smem + L_FXL + 256) + (l & 15);
          const LDS_AS float* cl_ = (const LDS_AS float*)(smem + L_CSL + 256) + (l & 15);
          const float t4 = __builtin_fmaf(cl_[0], nab[1], fl_[0]), t5 = __builtin_fmaf(cl_[16], nab[1], fl_[16]);
          const unsigned na = __builtin_bit_cast(unsigned, (float)nab[0]);
#pragma unroll
          for (int a = 0; a < 2; ++a) {
            const bool ma = a ? !m0 : m0;
            aq[a] = (mq && ma) ? na : aq[a];
#pragma unroll
            for (int e = 0; e < 4; ++e) {
              const bool me = mv && ma && e == (int)(sel0 & 3);
              acc[a][4][e] = me ? t4 : acc[a][4][e]; acc[a][5][e] = me ? t5 : acc[a][5][e];
              av[a][e] = me ? na : av[a][e];
            }
          }
        }
        constexpr float C2 = 1.4426950408889634f * 0.17677669529663687f;
#pragma unroll
        for (int a = 0; a < 2; ++a) {
          f32x2 a2 = {asf(aq[a]), asf(aq[a])};
          qs2[a] = f32x2{asf(aq[a]) * C2, asf(aq[a]) * C2};
          asm volatile("" : "+v"(a2), "+v"(qs2[a]));
          acc[a][2] *= f32x4{a2[0], a2[1], a2[0], a2[1]}; acc[a][3] *= f32x4{a2[0], a2[1], a2[0], a2[1]};
          acc[a][4] *= __builtin_bit_cast(f32x4, av[a]); acc[a][5] *= __builtin_bit_cast(f32x4, av[a]);
        }
        k0 = pack8(acc[0][2], acc[0][3]); k1 = pack8(acc[1][2], acc[1][3]); v0 = pack8(acc[0][4], acc[1][4]); v1 = pack8(acc[0][5], acc[1][5]);
        const unsigned kvo = OFF_KV + IT * (8u << 20) + (p * 32 + h) * 32768 + rt * 4096;
        if (p < 7) { st16(k0, rs, l * 16, kvo); st16(k1, rs, l * 16 + 1024, kvo); st16(v0, rs, l * 16 + 2048, kvo); st16(v1, rs, l * 16 + 3072, kvo); }
        char* ownp = smem + (8 + rt) * 2048 + l * 16;
        *(u32x4*)(ownp + L_K) = k0;
        if (rt < 7) *(u32x4*)(ownp + L_K + 1024) = k1;
        *(u32x4*)(ownp + L_V) = v0; *(u32x4*)(ownp + L_V + 1024) = v1;
        SB();
        static_for<32 - QL>([&](auto c_) __attribute__((always_inline)) {
          constexpr int s = QL + decltype(c_)::value;
#pragma unroll
          for (int n = 0; n < 2; ++n) { acc[0][n] = mfma16(qw[s - QL][n].v, af[s % WA][0], acc[0][n]); acc[1][n] = mfma16(qw[s - QL][n].v, af[s % WA][1], acc[1][n]); }
        });
        if (own) { patch_qk(0); patch_qk(1); }
      };
      qkv_loop(it_c);
    }
#undef A_LOAD1
#undef QKV_RDA
#undef QKV_RDB
#undef QKV_BO
    {
      IDS();
      const GAS bf16 *pwg, *pwu;
      asm volatile("s_load_dwordx2 %0, %2, 40\n\ts_load_dwordx2 %1, %2, 48" : "=&s"(pwg), "=&s"(pwu) : "s"(ka));
      const int ql = l & 15;
      const f32x4 z4 = {0.f, 0.f, 0.f, 0.f}, ninf = {-INFINITY, -INFINITY, -INFINITY, -INFINITY};
      LDS_AS unsigned* rdy = (LDS_AS unsigned*)(smem + L_RDY);
      const unsigned rtag = T[0] + it;
      u32x4 qf[2];
      f32x4 cn[2], oa[2][2], sum[2], S[2][2];
      bool told = false;
      auto announce = [&](bool now) __attribute__((always_inline)) {
        if (!told) {
          unsigned st;
          asm volatile("s_getreg_b32 %0, hwreg(HW_REG_IB_STS)" : "=s"(st)::"memory");
          if (now || !(st & 0xC0000Fu)) {
            DRAIN();
            if (l == 0) st8(T, rs, rt * SKW, F_KV + (it * 256 + p * 32 + h) * SKV);
            told = true;
          }
        }
      };
      unsigned one2 = 0x3F803F80u;
      asm volatile("" : "+v"(one2));
      const u32x4 ones = {one2, one2, one2, one2};
      auto ex8 = [&](f32x4 a, f32x4 b) __attribute__((always_inline)) {
        return u32x4{pack2(__builtin_amdgcn_exp2f(a[0]), __builtin_amdgcn_exp2f(a[1])), pack2(__builtin_amdgcn_exp2f(a[2]), __builtin_amdgcn_exp2f(a[3])),
                     pack2(__builtin_amdgcn_exp2f(b[0]), __builtin_amdgcn_exp2f(b[1])), pack2(__builtin_amdgcn_exp2f(b[2]), __builtin_amdgcn_exp2f(b[3]))};
      };
      {
#pragma unroll
        for (int a = 0; a < 2; ++a) {
          const f32x4 c4 = {qs2[a][0], qs2[a][1], qs2[a][0], qs2[a][1]};
          qf[a] = pack8(acc[a][0] * c4, acc[a][1] * c4);
        }
        f32x4 mk;
#pragma unroll
        for (int e = 0; e < 4; ++e) mk[e] = (4 * g + e <= ql) ? 0.f : -INFINITY;
        const f32x4 d0 = mfma16(k0, qf[0], mk), d1 = mfma16(k1, qf[1], mk), d2 = mfma16(k0, qf[1], z4);
        const float m0 = -max_x32(max_x16(fmaxf(fmaxf(d0[0], d0[1]), fmaxf(d0[2], d0[3]))));
        const float m1 = -max_x32(max_x16(fmaxf(fmaxf(d1[0], d1[1]), fmaxf(d1[2], d1[3]))));
        cn[0] = f32x4{m0, m0, m0, m0}; cn[1] = f32x4{m1, m1, m1, m1};
        asm volatile("" : "+v"(cn[0]), "+v"(cn[1]));
        announce(false);
        const f32x4 e0 = d0 + cn[0];
        const u32x4 p0 = {pack2(__builtin_amdgcn_exp2f(e0[0]), __builtin_amdgcn_exp2f(e0[1])), pack2(__builtin_amdgcn_exp2f(e0[2]), __builtin_amdgcn_exp2f(e0[3])), 0u, 0u};
        const u32x4 p1 = ex8(d2 + cn[1], d1 + cn[1]);
        oa[0][0] = mfma16(v0, p0, z4); oa[0][1] = mfma16(v1, p0, z4); sum[0] = mfma16(ones, p0, z4);
        oa[1][0] = mfma16(v0, p1, z4); oa[1][1] = mfma16(v1, p1, z4); sum[1] = mfma16(ones, p1, z4);
      }
      const char* kb_ = smem + L_K + l * 16;
      const char* vb_ = smem + L_V + l * 16;
      const unsigned fnv = (l >> 1) * (F_ * 2) + (l & 1) * 16;
      asm volatile("" : "+v"(oa[0][0]), "+v"(oa[0][1]), "+v"(oa[1][0]), "+v"(oa[1][1]), "+v"(sum[0]), "+v"(sum[1]));
      const unsigned pw = __builtin_amdgcn_readfirstlane(it * 256 + p * 32 + h - 32), pfo = F_KV + pw * SKV + rt * SKW;
      const bool fetcher = rt < 4 && p > 0;
      announce(false);
      BAR();
      asm volatile("" : "+s"(pwg), "+s"(pwu));
#define KV_FETCH(gd)                                                                                                          \
  {                                                                                                                           \
    const unsigned pvo = OFF_KV + pw * 32768 + (gd) * 4096;                                                                   \
    dma16s<0>(rs, l * 16, pvo, lbase + L_K + (gd) * 2048); dma16s<1024>(rs, l * 16, pvo, lbase + L_K + (gd) * 2048);           \
    dma16s<0>(rs, l * 16, pvo + 2048, lbase + L_V + (gd) * 2048); dma16s<1024>(rs, l * 16, pvo + 2048, lbase + L_V + (gd) * 2048); \
  }
      auto fetch = [&]() __attribute__((always_inline)) {
        if (PSK) __builtin_amdgcn_s_sleep(PSK);
        poll_flags(rs, (l & 1) * 4 * SKW, pfo, T);
        KV_FETCH(rt); KV_FETCH(rt + 4);
        if (l == 0) rdy[8 + rt] = rtag;
      };
      announce(false);
      const bool first = fetcher && told;
      if (first) fetch();
#define LDS16(a_) (*(const LDS_AS u32x4*)(size_t)(a_))
      u32x4 kf[2], vf[2], p0, p1;
#define AE(a, i) { S[a][(i) >> 2][(i) & 3] = __builtin_amdgcn_exp2f(S[a][(i) >> 2][(i) & 3]); asm volatile("" : "+v"(S[a][(i) >> 2][(i) & 3])); SB(); }
#define AC(a, P, j) { P[j] = pack2(S[a][(j) >> 1][2 * ((j) & 1)], S[a][(j) >> 1][2 * ((j) & 1) + 1]); asm volatile("" : "+v"(P[j])); SB(); }
#define PHASE(a, P, M1, M2, L2, M3, M4, L4, M5)                                                                               \
  AE(a, 0) AE(a, 1) M1; SB(); AE(a, 2) AE(a, 3) M2; SB(); L2; SB(); AC(a, P, 0) AC(a, P, 1) AE(a, 4) M3; SB(); AE(a, 5) AE(a, 6) M4; SB(); \
  L4; SB(); AE(a, 7) AC(a, P, 2) M5; SB(); AC(a, P, 3)
#define PV(a, d, P) oa[a][d] = mfma16(vf[d], P, oa[a][d])
#define RS(a, P) sum[a] = mfma16(ones, P, sum[a])
#define SC(a, t, c) S[a][t] = mfma16(kf[t], qf[a], c)
#define K_AT(ad) (kf[0] = LDS16(ad), kf[1] = LDS16((ad) + 1024))
#define V_AT(ad) (vf[0] = LDS16((ad) + L_V), vf[1] = LDS16((ad) + L_V + 1024))
#define A_FIRST(ad, c00, c01, c10, c11, KN) { K_AT(ad); V_AT(ad); SB(); SC(0, 0, c00); SC(0, 1, c01); SB(); PHASE(0, p0, SC(1, 0, c10), SC(1, 1, c11), KN, , , , ) }
#define A_NEXT(ad, c00, c01, c10, c11, KN)                                                                                        \
  { PHASE(1, p1, PV(0, 0, p0), PV(0, 1, p0), , SC(0, 0, c00), SC(0, 1, c01), , RS(0, p0))                                     \
    PHASE(0, p0, PV(1, 0, p1), PV(1, 1, p1), V_AT(ad), SC(1, 0, c10), SC(1, 1, c11), KN, RS(1, p1)) }
#define A_LAST() { PHASE(1, p1, PV(0, 0, p0), PV(0, 1, p0), , RS(0, p0), , , ) SB(); PV(1, 0, p1); PV(1, 1, p1); RS(1, p1); }
      int nw = 0;
      auto piece = [&]() __attribute__((always_inline)) {
        if constexpr (it == 0) { if (ffn_on && nw < 8) { FFN_DMA(nw); } ++nw; }
      };
#define XN_OFF() (F_XN + (((rt - 6) * 4 + g) * RX + bi % RX) * 256 + (l & 15) * 16)
      u32x4 fxn;
      asm volatile("" : "=v"(fxn));
      unsigned ad = lbase + l * 16 + (6 + rt) * 2048;
      asm volatile("" : "+v"(ad));
      if (rt > 0) {
        A_FIRST(ad + 2048, cn[0], cn[0], cn[1], cn[1], K_AT(ad));
#pragma unroll 1
        for (int i = rt - 1; i > 0; --i) {
          ad -= 2048;
          asm volatile("" : "+v"(ad));
          if (told && rt >= 4) piece();
          announce(false);
          A_NEXT(ad + 2048, cn[0], cn[0], cn[1], cn[1], K_AT(ad));
        }
        announce(false);
        A_LAST();
      }
      announce(true);
      {
        f32x4 mk;
#pragma unroll
        for (int e = 0; e < 4; ++e) mk[e] = (4 * g + e > ql) ? 0.f : -INFINITY;
        f32x4 cm0 = cn[0] + mk, cm1 = cn[1] + mk, cmi = ninf;
        asm volatile("" : "+v"(cm0), "+v"(cm1), "+v"(cmi));
        if (rt >= 4) {
          if (p > 0) {
            while (!__all(__hip_atomic_load(rdy + 8 + (l & 3), __ATOMIC_RELAXED, __HIP_MEMORY_SCOPE_WORKGROUP) == rtag)) __builtin_amdgcn_s_sleep(1);
            asm volatile("" ::: "memory");
          }
          if constexpr (it == 0) { for (; nw < 8;) piece(); }
          static_for<it ? 12 : 8>([&](auto c_) __attribute__((always_inline)) { constexpr int i = decltype(c_)::value; WO_PIECE_W(15 - i / 2, 2 * (rt & 3) + (i & 1)); });
          if (it == 0 && rt >= 6) ld16h<0>(fxn, rs, XN_OFF(), 0);
        } else if (p > 0) {
          if (!first) fetch();
          DRAIN();
          if (l < 2) rdy[rt + 4 * l] = rtag;
          if constexpr (it == 0) { for (; nw <= rt;) piece(); }
        }
        if (p > 0) {
          unsigned have = 0;
          auto need = [&](int gq) __attribute__((always_inline)) {
            if (__builtin_expect(gq < 8 && !((have >> gq) & 1), 0)) {
              do {
                const unsigned t = __hip_atomic_load(rdy + (l & 7), __ATOMIC_RELAXED, __HIP_MEMORY_SCOPE_WORKGROUP);
                have = (unsigned)__builtin_amdgcn_ballot_w64(t == rtag);
              } while (!((have >> gq) & 1));
              asm volatile("" ::: "memory");
            }
          };
          ad = lbase + l * 16 + rt * 2048;
          asm volatile("" : "+v"(ad));
          need(rt);
          K_AT(ad); V_AT(ad); SB(); SC(0, 0, cm0); SC(0, 1, cn[0]); SB();
          PHASE(0, p0, SC(1, 0, cmi), SC(1, 1, cm1), , , , , )
          need(rt + 1);
          K_AT(ad + 2048);
#pragma unroll 1
          for (int gq = rt + 1; gq < 8; ++gq) {
            ad += 2048;
            asm volatile("" : "+v"(ad));
            piece();
            need(gq + 1);
            A_NEXT(ad, cn[0], cn[0], cn[1], cn[1], K_AT(ad + 2048));
          }
          A_LAST();
        }
        if constexpr (it == 0) { for (; nw < 8;) piece(); }
      }
      auto o_store = [&]() __attribute__((always_inline)) {
        const unsigned oo = OFF_O + it * (4u << 20) + ((p * 8 + rt) * 64 + h * 2) * 1024;
#pragma unroll
        for (int a = 0; a < 2; ++a) {
          f32x2 inv = {__builtin_amdgcn_rcpf(sum[a][0]), 0.f};
          inv[1] = inv[0];
          asm volatile("" : "+v"(inv));
          const f32x4 i4 = {inv[0], inv[1], inv[0], inv[1]};
          st16(pack8(oa[a][0] * i4, oa[a][1] * i4), rs, l * 16 + a * 1024, oo);
        }
      };
      {
        float big = 0.f;
#pragma unroll
        for (int a = 0; a < 2; ++a)
#pragma unroll
          for (int d = 0; d < 2; ++d)
            big = __builtin_elementwise_maximum(__builtin_elementwise_maximum(big, __builtin_elementwise_maximum(fabsf(oa[a][d][0]), fabsf(oa[a][d][1]))),
                                                __builtin_elementwise_maximum(fabsf(oa[a][d][2]), fabsf(oa[a][d][3])));
        bool ok = sum[0][0] >= 0.5f && sum[0][0] < 0x1p64f && sum[1][0] >= 0.5f && sum[1][0] < 0x1p64f && big < INFINITY;
        if (!__all(ok)) {
#pragma unroll
          for (int a = 0; a < 2; ++a) {
            const int qs = 2 * rt + a, qq = 256 + 16 * qs + ql, klo = max(qq - 255, p > 0 ? 0 : 256);
            auto score = [&](int t) __attribute__((always_inline)) {
              f32x4 s = mfma16(t == 31 ? k1 : *(const u32x4*)(kb_ + t * 1024), qf[a], z4);
#pragma unroll
              for (int e = 0; e < 4; ++e) { const int kk = 16 * t + 4 * g + e; s[e] = (kk <= qq && kk >= klo) ? s[e] : -INFINITY; }
              return s;
            };
            float mx = -INFINITY;
#pragma unroll 1
            for (int t = p > 0 ? qs : 16; t <= qs + 16; ++t) { const f32x4 s = score(t); mx = fmaxf(fmaxf(mx, fmaxf(s[0], s[1])), fmaxf(s[2], s[3])); }
            mx = max_x32(max_x16(mx));
            oa[a][0] = z4; oa[a][1] = z4; sum[a] = z4;
#pragma unroll 1
            for (int G = p > 0 ? qs >> 1 : 8; G <= (qs + 16) >> 1; ++G) {
              const f32x4 s0 = score(2 * G), s1 = score(2 * G + 1);
              f32x4 e0, e1;
#pragma unroll
              for (int e = 0; e < 4; ++e) { e0[e] = __builtin_amdgcn_exp2f(s0[e] - mx); e1[e] = __builtin_amdgcn_exp2f(s1[e] - mx); }
              const u32x4 pf = pack8(e0, e1);
              oa[a][0] = mfma16(*(const u32x4*)(vb_ + G * 2048), pf, oa[a][0]);
              oa[a][1] = mfma16(*(const u32x4*)(vb_ + G * 2048 + 1024), pf, oa[a][1]);
              sum[a] = mfma16(ones, pf, sum[a]);
            }
          }
        }
      }
      o_store();
      if constexpr (it == 0) {
        wait_vm<2>();
        asm volatile("" : "+v"(fxn)::"memory");
        if (rt >= 6 && !__all(fxn[0] == T[0] && fxn[1] == T[1] && fxn[2] == T[0] && fxn[3] == T[1])) poll_flags2(rs, XN_OFF(), T);
        DEF4(gv); DEF4(uv);
        if (ffn_on) {
          const unsigned ro = 65536 + w * 1024 + (4 * g + ((l & 15) >> 2)) * 32 + (l & 3) * 8;
#pragma unroll
          for (int i = 0; i < 4; ++i) {
            frag_t f;
            f.hf.a = trread(smem, ro + i * 16384); f.hf.b = trread(smem, ro + i * 16384 + 512);
            gv[i] = f.v;
            f.hf.a = trread(smem, ro + i * 16384 + 8192); f.hf.b = trread(smem, ro + i * 16384 + 8704);
            uv[i] = f.v;
          }
        }
        PINV4(gv); PINV4(uv);
      }
      BAR();
      if constexpr (it == 1) { WO_PIECE(3); WO_PIECE(2); WO_PIECE(1); WO_PIECE(0); }
      wait_vm<it ? 4 : 0>();
      asm volatile("s_barrier" ::: "memory");
      if (tid == 0) st8(T, rs, h * SO, F_O + (it * 8 + p) * 32 * SO);
      if constexpr (it == 0) {
        WO_PIECE(11); WO_PIECE(10); WO_PIECE(9); WO_PIECE(8); WO_PIECE(7); WO_PIECE(6); WO_PIECE(5); WO_PIECE(4);
        WO_PIECE(3); WO_PIECE(2); WO_PIECE(1); WO_PIECE(0);
      }
    }
    IDS();
    ARGP(pwr, 4); ARGP(pwd, 7); ARGP(pout, 8); ARGP(px, 0);
    const int kh = w >> 2, rt2 = rb * 4 + (w & 3);
    const unsigned R = rt2 * 32 + kh * 16 + (l & 15);
    const unsigned to = (rt2 * 64 + ct * 4 + kh) * 1024 + l * 16;
    const unsigned rmo = ct * 64 + ((g & 1) ? 12 + 4 * g : 4 * g);
    f32x2 wv[8];
    {
      u32x4 wa[2], wrp[2], res[2];
#pragma unroll
      for (int j = 0; j < 2; ++j) {
        wrp[j] = *(const GAS u32x4*)(pwr + rmo + 32 * j);
        if constexpr (it == 0) wa[j] = *(const GAS u32x4*)(pwr + l * 8 + j * 512);
        if constexpr (it == 0) res[j] = *(const GAS u32x4*)(px + (R - (l & 15) + 8 * j + (l >> 3)) * 1024 + ct * 64 + (l & 7) * 8);
        else res[j] = ld16(rs, OFF_XNEW + (2 * ct + j) * 256 + g * 16, 0);
      }
      SB();
      if ((it ? PSO : PSO0) && w == 0) __builtin_amdgcn_s_sleep(it ? PSO : PSO0);
      if (w == 0) poll_flags(rs, (l & 31) * SO, F_O + (it * 8 + p) * 32 * SO, T);
      asm volatile("s_barrier" ::: "memory");
      auto prep = [&]() __attribute__((always_inline)) {
        if constexpr (it == 0) {
          f32x2 t[8];
          unpack8v(wa[0], t); unpack8v(wa[1], t + 4);
          const f32x2 s = ((t[0] + t[1]) + (t[2] + t[3])) + ((t[4] + t[5]) + (t[6] + t[7]));
          wsum = sum64(hadd(s));
        }
        unpack8v(trade16(wrp[0]), wv); unpack8v(trade16(wrp[1]), wv + 4);
        if constexpr (it == 0) {
          char* tp = smem + w * 2048;
          const unsigned c0 = (g & 1) * 2 + (g >> 1), r = l & 15;
#pragma unroll
          for (int j = 0; j < 2; ++j) *(u32x4*)(tp + j * 1024 + (l >> 3) * 128 + (((l ^ (l >> 3)) & 7) << 4)) = res[j];
          res[0] = trade16(*(const u32x4*)(tp + r * 128 + ((c0 ^ (r & 7)) << 4)));
          res[1] = trade16(*(const u32x4*)(tp + r * 128 + (((c0 + 4) ^ (r & 7)) << 4)));
        }
        else { res[0] = R == sel0 ? res[0] : x2p[0]; res[1] = R == sel0 ? res[1] : x2p[1]; }
        unpack8v(res[0], v + 0); unpack8v(res[1], v + 4);
        asm volatile("" : "+v"(v[0]), "+v"(v[1]), "+v"(v[2]), "+v"(v[3]), "+v"(v[4]), "+v"(v[5]), "+v"(v[6]), "+v"(v[7]));
        asm volatile("" : "+v"(wsum), "+v"(wv[0]), "+v"(wv[1]), "+v"(wv[2]), "+v"(wv[3]), "+v"(wv[4]), "+v"(wv[5]), "+v"(wv[6]), "+v"(wv[7]));
      };
      {
        const unsigned abase = OFF_O + it * (4u << 20) + (rt2 * 64 + kh) * 1024;
        u32x4 af[W2B], bfr[W2L + 1][4];
        unsigned b2[2] = {lbase + 32768 + l * 16, lbase + 98304 + l * 16};
        asm volatile("" : "+v"(b2[0]), "+v"(b2[1]));
#define O_LOAD(s_) af[(s_) % W2B] = ld16(rs, l * 16 + (~(s_) & 1) * 2048, abase + ((31 - (s_)) >> 1) * 4096)
#define WO_RD(s_, n) bfr[(s_) % (W2L + 1)][n] = *(const LDS_AS u32x4*)(size_t)(b2[(31 - (s_)) >> 4] + ((31 - (s_)) & 15) * 4096 + (n) * 1024)
#pragma unroll
        for (int s = 0; s < W2R; ++s) O_LOAD(s);
        SB();
        prep();
        SB();
        f32x4 ac[4];
#pragma unroll
        for (int n = 0; n < 4; ++n) ac[n] = f32x4{v[2 * n][0], v[2 * n][1], v[2 * n + 1][0], v[2 * n + 1][1]};
#pragma unroll
        for (int n = 0; n < 4 * W2L; ++n) WO_RD(n >> 2, n & 3);
#pragma unroll
        for (int s = 0; s < 32; ++s) {
          if (s == W2P && w >= 4) __builtin_amdgcn_s_setprio(1);
          if (s == (it ? 24 : 8) - W2L) asm volatile("s_barrier" ::: "memory");
        if (it == 0 && s == WPS) {
          unsigned pz1 = POISON;
          asm volatile("" : "+v"(pz1));
          const u32x4 pz = {pz1, pz1, pz1, pz1};
          st16(pz, rs, OFF_PART + (tid >> 8) * (1u << 20) + bid * 4096 + (tid & 255) * 16, 0);
          if (w < 2) st16(pz, rs, OFF_STAT + 524288 + (rb * 16 + ct) * 2048 + tid * 16, 0);
          if (w == 4 && l < 40) st16(pz, rs, l < 16 ? OFF_CA + 65536 + rb * 4096 + ct * 256 + l * 16 : OFF_FX + (h * 8 + p) * 512 + (l - 16) * 16, 0);
          if (w == 6 && l < (ct < (int)RKEY ? 3 : 2)) st4(pz1, rs, l < 2 ? OFF_XNEW + (bid >> 3) * 256 + (bid & 7) * 8 + l * 4 : KEY_AT(1, ct) + rb * SKEY, 0);
        }
          if (s < W2B - W2R) O_LOAD(W2R + 2 * s);
          SB();
#pragma unroll
          for (int n = 0; n < 4; ++n) {
            ac[n] = mfma16(bfr[s % (W2L + 1)][n], af[s % W2B], ac[n]);
            if (s + W2L < 32) WO_RD(s + W2L, n);
            SB();
          }
          if (s < W2B - W2R) O_LOAD(W2R + 2 * s + 1);
          else if (s + W2B < 32) O_LOAD(s + W2B);
          SB();
        }
        __builtin_amdgcn_s_setprio(0);
#undef WO_RD
#undef O_LOAD
#pragma unroll
        for (int n = 0; n < 4; ++n) { v[2 * n] = f32x2{ac[n][0], ac[n][1]}; v[2 * n + 1] = f32x2{ac[n][2], ac[n][3]}; }
      }
    }
    {
      if (it == 0) { st16(pack8v(v), rs, to, OFF_XN + (4u << 20)); st16(pack8v(v + 4), rs, to + 2048, OFF_XN + (4u << 20)); }
      SB();
      const f32x2 s = ((v[0] + v[1]) + (v[2] + v[3])) + ((v[4] + v[5]) + (v[6] + v[7]));
      f32x2 dp = v[0] * wv[0], q = v[0] * v[0];
#pragma unroll
      for (int j = 1; j < 8; ++j) { dp = __builtin_elementwise_fma(v[j], wv[j], dp); q = __builtin_elementwise_fma(v[j], v[j], q); }
      float mt = hadd(s), qq = hadd(q), dot = hadd(dp);
      add_g3(mt, qq, dot);
      const unsigned so = OFF_STAT + it * 524288 + rb * 32768, lrow = R & 127;
      if (it == 0) wait_vm<2>();
      if (g == 0) st16(unpoison4(__builtin_bit_cast(u32x4, f32x4{mt, qq, dot, 0.f})), rs, ct * 2048 + lrow * 16, so);
      SB();
      if (it == 0) wait_vm<1>();
      if constexpr (it == 0) {
        volatile LDS_AS u32x2* in = (volatile LDS_AS u32x2*)(smem + L_XC);
        if (l == 0) in[w] = T;
        const u32x2 t = in[l & 7];
        if (__all(t[0] == T[0] && t[1] == T[1]) && l < (int)RX1) st8(T, rs, (l * 32 + (rb & 1) * 16 + ct) * SX1, F_X1 + (rb >> 1) * RX1 * 32 * SX1);
      }
      if (it == 0) {
        asm volatile("s_barrier" ::: "memory");
        if (ffn_on) { WD_DMA(); }
#pragma unroll
        for (int j = 0; j < 9; ++j) *(u32x4*)(smem + L_RING + (j / 3) * STAGE + (w * 3 + j % 3) * 1024 + l * 16) = keep[j];
      }
      if constexpr (it == 0) {
        if (PSX && w == 0) __builtin_amdgcn_s_sleep(PSX);
        if (w == 0) poll_flags(rs, ((l & 2) * 8 + ((ROT0 + 5) & 7) * 2 + (l & 1)) * SX1, F_X1 + (p * RX1 + (xcd >> 1) % RX1) * 32 * SX1, T);
        if (tid == 192) *(LDS_AS u32x4*)(smem + L_QC) = u32x4{0u, 0u, 0u, 0u};
        BAR();
        PRE_LOAD(1, ROT0 + 5, 0, NPRE);
      } else {
        __builtin_amdgcn_s_sleep(SDIR);
        u32x4 sp[4];
#pragma unroll
        for (int i = 0; i < 4; ++i) sp[i] = ld16(rs, (4 * g + i) * 2048 + lrow * 16, so);
        const RowFin r = fin_key(rs, T, g * 8192 + lrow * 16, so, smem + L_XC, l, w, R, wsum, sp);
        BAR();
        fin_cand(rs, smem + L_XC, 1, g, rb, ct, r, v);
        fin_x2(r, v);
      }
    }
    if (it == 1) {
      u32x4 op[2];
      {
        char* tp = smem + L_OUT + w * 2048;
        const unsigned c0 = (g & 1) * 2 + (g >> 1), r = l & 15;
        *(u32x4*)(tp + r * 128 + ((c0 ^ (r & 7)) << 4)) = trade16(pack8v(v));
        *(u32x4*)(tp + r * 128 + (((c0 + 4) ^ (r & 7)) << 4)) = trade16(pack8v(v + 4));
#pragma unroll
        for (int j = 0; j < 2; ++j) op[j] = *(const u32x4*)(tp + j * 1024 + (l >> 3) * 128 + (((l ^ (l >> 3)) & 7) << 4));
      }
      const unsigned Ro = R - (l & 15) + (l >> 3), oo = (Ro * 1024 + ct * 64) * 2 + (l & 7) * 16;
#define OUT_STORE(c0, c1)                                                                           \
  {                                                                                                 \
    if (c0) __builtin_amdgcn_raw_buffer_store_b128(op[0], rout, oo, 0, SC1);                        \
    if (c1) __builtin_amdgcn_raw_buffer_store_b128(op[1], rout, oo + 16384, 0, SC1);                \
  }
      if (w == 0) {
        __builtin_amdgcn_s_sleep(GSL);
        u32x4 xr[4];
        unsigned cur = 0, best;
        asm volatile("" : "=v"(xr[0]), "=v"(xr[1]));
        for (unsigned spins = 0;;) {
          const unsigned k_ = key_load(rs, l, 1, KREP());
          const bool all = __all(k_ != POISON);
          best = __builtin_amdgcn_readfirstlane(umax16(k_ == POISON ? 0u : k_));
          if (best != cur && ffn_on) {
            cur = best;
            ld16h<0>(xr[0], rs, (l >> 3) * 256 + (l & 7) * 16, cand_off(cur, 1)); ld16h<2048>(xr[1], rs, (l >> 3) * 256 + (l & 7) * 16, cand_off(cur, 1));
          }
          if (all || spin_fail(spins, rs, T)) break;
        }
        if (l == 0) *(LDS_AS unsigned*)(smem + L_SKEY + 32) = best;
        asm volatile("s_waitcnt vmcnt(0)" : "+v"(xr[0]), "+v"(xr[1])::"memory");
        if (ffn_on) {
          word_wait<2>(rs, T, (l >> 3) * 256 + (l & 7) * 16, cand_off(best, 1), xr);
          *(LDS_AS u32x4*)(smem + L_ROW + l * 16) = xr[0]; *(LDS_AS u32x4*)(smem + L_ROW + 1024 + l * 16) = xr[1];
        }
      }
      BAR();
      const unsigned key = __builtin_amdgcn_readfirstlane(*(LDS_AS unsigned*)(smem + L_SKEY + 32)), krow = key_row(key);
      if (ffn_on) {
        u32x4 xv[4];
#pragma unroll
        for (int i = 0; i < 4; ++i) xv[i] = *(const LDS_AS u32x4*)(smem + L_ROW + w * 256 + i * 64 + g * 16);
        f32x4 ga, ua;
        ffn_gu(gv, uv, xv, ga, ua);
        ffn_gu_store(smem, l, w, ga, ua);
        BAR();
        ffn_d_store(rs, l, w, 1, slice, ffn_d_all(dfr, ffn_d_h(ffn_d_read(smem, l))));
        SB();
      }
      OUT_STORE(Ro != krow, Ro + 8 != krow);
      if (!ffn_on) {
        const unsigned G = xcd * 4 + bi - 28;
        if (G == 0 && tid == 0) st8(T, rs, F_EP, 0);
        u32x4 pv[4];
        u32x2 cxn, cx1;
        const float sig = red_gate(key);
        __builtin_amdgcn_s_sleep(RSL);
        red_load(rs, tid, 1, G, key, pv, cxn, cx1);
        red_sum(rs, smem, T, tid, w, 1, G, key, pv, cxn, cx1);
        BAR();
        if (w == 0) {
          const u32x2 ov = red_final(smem, l, sig, cxn, cx1);
          if (l < 8) __builtin_amdgcn_raw_buffer_store_b64(ov, rout, (krow * 1024 + G * 32 + l * 4) * 2, 0, 0);
        }
      }
#undef OUT_STORE
    }
  };
  layer(std::integral_constant<int, 0>{});
  layer(std::integral_constant<int, 1>{});
}

extern "C" void kernel_launch(void* const* d_in, const int* in_sizes, int n_in, void* d_out, int out_size,
                              void* d_ws, size_t ws_size, hipStream_t stream) {
  (void)hipFuncSetAttribute((const void*)mega_kernel, hipFuncAttributeMaxDynamicSharedMemorySize, LDS_BYTES);
  mega_kernel<<<256, 512, LDS_BYTES, stream>>>((const bf16*)d_in[0], (char*)d_ws, (const bf16*)d_in[1], (const bf16*)d_in[2], (const bf16*)d_in[4],
                                               (const bf16*)d_in[5], (const bf16*)d_in[6], (const bf16*)d_in[7], (bf16*)d_out);
}
```
